```python
import torch

def _softmax(x, axis=-1):
    return torch.softmax(x, dim=axis)
def _gelu(x):
    return torch.nn.functional.gelu(x, approximate="tanh")
def _silu(x):
    return torch.nn.functional.silu(x)
def _sigmoid(x):
    return torch.sigmoid(x)
def _layernorm(x, axis=-1, eps=1e-5):
    m = x.mean(dim=axis, keepdim=True)
    v = x.var(dim=axis, keepdim=True, unbiased=False)
    return (x - m) / torch.sqrt(v + eps)
def _scatter_add(tgt, idx, src, axis=0):
    return tgt.clone().index_add_(axis, idx, src)
def _segment_sum(src, seg, n):
    out = torch.zeros((n,) + src.shape[1:], dtype=src.dtype)
    return out.index_add_(0, seg, src)
def _pad(x, pw):
    flat = tuple(v for pair in reversed(pw) for v in pair)
    return torch.nn.functional.pad(x, flat)

kernel_name = 'concat_heads__sliding_local_dilated__compressed_kv_cla__prompt'

def setup_inputs():

    _w = (lambda a: (lambda t: t.to(torch.bfloat16) if t.is_floating_point() else t)(torch.as_tensor(a)))
    def _A_setup():
        B, S, D = 1, 2048, 1024
        return {
            "x": _w((lambda *s: torch.randn(*s, dtype=torch.bfloat16) * 0.2)(B, S, D)),
            "wqkv": _w((lambda *s: torch.randn(*s, dtype=torch.bfloat16) * 0.2)(D, 3 * D)),
            "wo": _w((lambda *s: torch.randn(*s, dtype=torch.bfloat16) * 0.05)(D, D)),
        }
    _A_in = _A_setup()

    def _B_setup():
        B, H, Sq, Sk, D, M = 1, 32, 2048, 2048, 128, 1024
        Dc = 4 * D
        return {
            "x": _w((lambda *s: torch.randn(*s, dtype=torch.bfloat16) * 0.2)(B, Sq, M)),
            "c_kv": _w((lambda *s: torch.randn(*s, dtype=torch.bfloat16) * 1.6)(B, Sk, Dc)),
            "w_q": _w((lambda *s: torch.randn(*s, dtype=torch.bfloat16) * 3.2)(M, H, D) * (M ** -0.5)),
            "w_uk": _w((lambda *s: torch.randn(*s, dtype=torch.bfloat16) * 3.2)(Dc, H, D) * (Dc ** -0.5)),
            "w_uv": _w((lambda *s: torch.randn(*s, dtype=torch.bfloat16) * 1.6)(Dc, H, D) * (Dc ** -0.5)),
            "w_o": _w((lambda *s: torch.randn(*s, dtype=torch.bfloat16) * 1.6)(H, D, M) * ((H * D) ** -0.5)),
        }
    _B_in = _B_setup()

    return {
        "A_x": _A_in["x"],
        "A_wqkv": _A_in["wqkv"],
        "A_wo": _A_in["wo"],
        "B_x": _B_in["x"],
        "B_c_kv": _B_in["c_kv"],
        "B_w_q": _B_in["w_q"],
        "B_w_uk": _B_in["w_uk"],
        "B_w_uv": _B_in["w_uv"],
        "B_w_o": _B_in["w_o"],
    }

def reference(A_x, A_wqkv, A_wo, B_x, B_c_kv, B_w_q, B_w_uk, B_w_uv, B_w_o):
    def _A_ref(x, wqkv, wo):
        B, S, D = x.shape
        H = 32; Dh = D // H
        W, dil = 8, 2
        qkv = (lambda x, *d: x.permute(*d))((x @ wqkv).reshape(B, S, 3, H, Dh), 2, 0, 3, 1, 4)
        q, k, v = qkv[0], qkv[1], qkv[2]
        scale = Dh ** -0.5
        pos = torch.arange(S)
        off = torch.arange(W) * dil
        idx = pos[:, None] - off[None, :]
        mask = idx >= 0
        idx_c = torch.where(mask, idx, 0).reshape(-1)
        kg = k[:, :, idx_c, :].reshape(B, H, S, W, Dh)
        vg = v[:, :, idx_c, :].reshape(B, H, S, W, Dh)
        logits = torch.einsum("bhsd,bhswd->bhsw", q, kg) * scale
        logits = torch.where(mask[None, None, :, :], logits, -1e9)
        attn = _softmax(logits, axis=-1)
        o = torch.einsum("bhsw,bhswd->bhsd", attn, vg)
        return (lambda x, *d: x.permute(*d))(o, 0, 2, 1, 3).reshape(B, S, D) @ wo

    def _B_ref(x, c_kv, w_q, w_uk, w_uv, w_o):
        B, Sq, M = x.shape[0], x.shape[1], x.shape[2]
        Sk = c_kv.shape[1]
        H, D = w_q.shape[1], w_q.shape[2]
        q = torch.einsum("bqm,mhd->bhqd", x, w_q)
        k = torch.einsum("bkc,chd->bhkd", c_kv, w_uk)
        v = torch.einsum("bkc,chd->bhkd", c_kv, w_uv)
        scores = torch.einsum("bhqd,bhkd->bhqk", q, k) * (D ** -0.5)
        off = Sk - Sq
        qi = torch.arange(Sq)[:, None]
        kj = torch.arange(Sk)[None, :]
        cmask = kj <= (qi + off)
        scores = torch.where(cmask[None, None], scores, -1e9)
        p = _softmax(scores, axis=-1)
        ctx = torch.einsum("bhqk,bhkd->bhqd", p, v)
        return torch.einsum("bhqd,hdm->bqm", ctx, w_o)

    a = _A_ref(A_x, A_wqkv, A_wo)
    b = _B_ref(B_x, B_c_kv, B_w_q, B_w_uk, B_w_uv, B_w_o)
    return torch.concatenate([a, b], axis=-1)
```

```cpp
#include <hip/hip_runtime.h>
#include <hip/hip_bf16.h>
#include <stdint.h>
#include <utility>

typedef unsigned short bf16_t;
typedef short bf16x4 __attribute__((ext_vector_type(4)));
typedef float f32x4 __attribute__((ext_vector_type(4)));
typedef unsigned int u32x2 __attribute__((ext_vector_type(2)));

__device__ __forceinline__ unsigned pack_bf16(float a, float b) {
  typedef __bf16 bf2 __attribute__((ext_vector_type(2)));
  typedef float f2 __attribute__((ext_vector_type(2)));
  bf2 r = __builtin_convertvector((f2){a, b}, bf2);
  return __builtin_bit_cast(unsigned, r);
}
#define Z_PRO1 \
"s_nop 4\nbuffer_load_dwordx2 a[252:253], off, s[60:63], 0 sc1\ns_lshl_b32 s88, s32, 1\nv_xor_b32 v28, 128, v6\ns_mov_b32 s73, 7168\ns_mov_b32 s74, 14336\ns_mov_b32 s75, 21504\ns_mov_b32 s76, 32768\n" \
"s_mov_b32 s77, 39936\ns_mov_b32 s78, 47104\ns_mov_b32 s79, 54272\ns_mov_b32 s89, 31744\ns_mov_b32 s90, 63488\ns_mov_b32 s91, 95232\ns_mov_b32 s92, 256\ns_mov_b32 s93, 32000\n" \
"s_mov_b32 s94, 63744\ns_mov_b32 s95, 95488\ns_mov_b32 s72, 524288\ns_add_u32 m0, s88, 0\ns_nop 0\nbuffer_load_dwordx4 v5, s[64:67], 0 offen offset:0 lds\nbuffer_load_dwordx4 v5, s[64:67], s73 offen offset:1024 lds\nbuffer_load_dwordx4 v5, s[64:67], s74 offen offset:2048 lds\n" \
"buffer_load_dwordx4 v5, s[64:67], s75 offen offset:3072 lds\ns_add_u32 m0, s88, 4096\ns_nop 0\nbuffer_load_dwordx4 v5, s[64:67], s76 offen offset:0 lds\nbuffer_load_dwordx4 v5, s[64:67], s77 offen offset:1024 lds\nbuffer_load_dwordx4 v5, s[64:67], s78 offen offset:2048 lds\nbuffer_load_dwordx4 v5, s[64:67], s79 offen offset:3072 lds\ns_add_u32 m0, s32, 65536\n" \
"s_nop 0\nbuffer_load_dwordx4 v6, s[68:71], 0 offen offset:0 lds\nbuffer_load_dwordx4 v6, s[68:71], s89 offen offset:1024 lds\nbuffer_load_dwordx4 v28, s[68:71], s90 offen offset:2048 lds\nbuffer_load_dwordx4 v28, s[68:71], s91 offen offset:3072 lds\ns_add_u32 m0, s32, 81920\ns_nop 0\nbuffer_load_dwordx4 v6, s[68:71], s92 offen offset:0 lds\n" \
"buffer_load_dwordx4 v6, s[68:71], s93 offen offset:1024 lds\nbuffer_load_dwordx4 v28, s[68:71], s94 offen offset:2048 lds\nbuffer_load_dwordx4 v28, s[68:71], s95 offen offset:3072 lds\nv_add_u32 v5, 128, v5\nv_add_u32 v6, s72, v6\nv_add_u32 v28, s72, v28\nbuffer_load_dword a251, v29, s[64:67], 0 offen sc1\n" \
""
#define Z_MAIN \
".macro D v,s,o,i\nbuffer_load_dwordx4 v\\v, s[\\s:\\s+3], \\o offen offset:\\i lds\n.endm\n.macro Ta d,v,o\nds_read_b64_tr_b16 a[\\d:\\d+1], v\\v offset:\\o\n.endm\n.macro Ra d,v,o\nds_read_b128 a[\\d:\\d+3], v\\v offset:\\o\n.endm\n.macro Zvaa d,a,b\nv_mfma_f32_16x16x32_bf16 v[\\d:\\d+3], a[\\a:\\a+3], a[\\b:\\b+3], 0\n.endm\n.macro AR d,a\nv_accvgpr_read_b32 v\\d, a\\a\n.endm\n.macro Zaaa d,a,b\nv_mfma_f32_16x16x32_bf16 a[\\d:\\d+3], a[\\a:\\a+3], a[\\b:\\b+3], 0\n.endm\n.macro Mvaa d,a,b\nv_mfma_f32_16x16x32_bf16 v[\\d:\\d+3], a[\\a:\\a+3], a[\\b:\\b+3], v[\\d:\\d+3]\n.endm\n.macro Maaa d,a,b\nv_mfma_f32_16x16x32_bf16 a[\\d:\\d+3], a[\\a:\\a+3], a[\\b:\\b+3], a[\\d:\\d+3]\n.endm\n" \
".macro C d,a,b\nv_cvt_pk_bf16_f32 v\\d, v\\a, v\\b\n.endm\n.macro AW d,a\nv_accvgpr_write_b32 a\\d, v\\a\n.endm\n.macro DS v,s,o,i\nbuffer_load_dwordx4 v\\v, s[\\s:\\s+3], \\o offen offset:\\i sc1 lds\n.endm\n.macro Qvav d,a,b,c\nv_mfma_f32_16x16x32_bf16 v[\\d:\\d+3], a[\\a:\\a+3], v[\\b:\\b+3], v[\\c:\\c+3]\n.endm\n.macro Mvav d,a,b\nv_mfma_f32_16x16x32_bf16 v[\\d:\\d+3], a[\\a:\\a+3], v[\\b:\\b+3], v[\\d:\\d+3]\n.endm\n.macro E d,a\nv_exp_f32 v\\d, v\\a\n.endm\n.macro Zaav d,a,b\nv_mfma_f32_16x16x32_bf16 a[\\d:\\d+3], a[\\a:\\a+3], v[\\b:\\b+3], 0\n.endm\n.macro Maav d,a,b\nv_mfma_f32_16x16x32_bf16 a[\\d:\\d+3], a[\\a:\\a+3], v[\\b:\\b+3], a[\\d:\\d+3]\n.endm\n" \
".macro Mvvv d,a,b\nv_mfma_f32_16x16x32_bf16 v[\\d:\\d+3], v[\\a:\\a+3], v[\\b:\\b+3], v[\\d:\\d+3]\n.endm\n.macro Qvaa d,a,b,c\nv_mfma_f32_16x16x32_bf16 v[\\d:\\d+3], a[\\a:\\a+3], a[\\b:\\b+3], v[\\c:\\c+3]\n.endm\n.macro Zvvv d,a,b\nv_mfma_f32_16x16x32_bf16 v[\\d:\\d+3], v[\\a:\\a+3], v[\\b:\\b+3], 0\n.endm\n.macro Qvva d,a,b,c\nv_mfma_f32_16x16x32_bf16 v[\\d:\\d+3], v[\\a:\\a+3], a[\\b:\\b+3], v[\\c:\\c+3]\n.endm\ns_nop 1\ns_lshl_b32 s88, s32, 1\nv_xor_b32 v28, 128, v6\ns_mov_b32 s73, 7168\n" \
"s_mov_b32 s74, 14336\ns_mov_b32 s75, 21504\ns_mov_b32 s76, 32768\ns_mov_b32 s77, 39936\ns_mov_b32 s78, 47104\ns_mov_b32 s79, 54272\ns_mov_b32 s89, 31744\ns_mov_b32 s90, 63488\n" \
"s_mov_b32 s91, 95232\ns_mov_b32 s92, 256\ns_mov_b32 s93, 32000\ns_mov_b32 s94, 63744\ns_mov_b32 s95, 95488\ns_mov_b32 s72, 524288\nv_xor_b32 v20, 32, v8\nv_xor_b32 v21, 64, v8\n" \
"v_xor_b32 v22, 96, v8\nv_xor_b32 v23, 128, v8\nv_xor_b32 v24, 160, v8\nv_xor_b32 v25, 192, v8\nv_xor_b32 v26, 224, v8\nv_xor_b32 v27, 64, v7\ns_add_u32 m0, s88, 32768\ns_nop 0\n" \
"D 5,64,0,0\nD 5,64,s73,1024\nD 5,64,s74,2048\nD 5,64,s75,3072\ns_add_u32 m0, s88, 36864\ns_nop 0\nD 5,64,s76,0\nD 5,64,s77,1024\n" \
"D 5,64,s78,2048\nD 5,64,s79,3072\ns_and_b32 s100, s33, 3\ns_cmp_eq_u32 s100, 1\ns_cbranch_scc1 zu1\ns_cmp_eq_u32 s100, 2\ns_cbranch_scc1 zu2\ns_cmp_eq_u32 s100, 3\n" \
"s_cbranch_scc1 zu3\ns_getpc_b64 s[96:97]\n1:\ns_cmp_eq_u32 s32, 4096\ns_cbranch_scc1 2f\ns_cmp_eq_u32 s32, 8192\ns_cbranch_scc1 3f\ns_cmp_eq_u32 s32, 12288\n" \
"s_cbranch_scc1 4f\ns_add_u32 s100, s96, zp0-1b\ns_addc_u32 s101, s97, 0\ns_swappc_b64 s[98:99], s[100:101]\ns_add_u32 s100, s96, zp4-1b\ns_addc_u32 s101, s97, 0\ns_swappc_b64 s[98:99], s[100:101]\ns_add_u32 s100, s96, zp8-1b\n" \
"s_addc_u32 s101, s97, 0\ns_swappc_b64 s[98:99], s[100:101]\ns_branch 9f\n2:\ns_add_u32 s100, s96, zp1-1b\ns_addc_u32 s101, s97, 0\ns_swappc_b64 s[98:99], s[100:101]\ns_add_u32 s100, s96, zp5-1b\n" \
"s_addc_u32 s101, s97, 0\ns_swappc_b64 s[98:99], s[100:101]\ns_add_u32 s100, s96, zp9-1b\ns_addc_u32 s101, s97, 0\ns_swappc_b64 s[98:99], s[100:101]\ns_branch 9f\n3:\ns_add_u32 s100, s96, zp2-1b\n" \
"s_addc_u32 s101, s97, 0\ns_swappc_b64 s[98:99], s[100:101]\ns_add_u32 s100, s96, zp6-1b\ns_addc_u32 s101, s97, 0\ns_swappc_b64 s[98:99], s[100:101]\ns_add_u32 s100, s96, zp10-1b\ns_addc_u32 s101, s97, 0\ns_swappc_b64 s[98:99], s[100:101]\n" \
"s_branch 9f\n4:\ns_add_u32 s100, s96, zp3-1b\ns_addc_u32 s101, s97, 0\ns_swappc_b64 s[98:99], s[100:101]\ns_add_u32 s100, s96, zp7-1b\ns_addc_u32 s101, s97, 0\ns_swappc_b64 s[98:99], s[100:101]\n" \
"s_add_u32 s100, s96, zp11-1b\ns_addc_u32 s101, s97, 0\ns_swappc_b64 s[98:99], s[100:101]\n9:\ns_branch zu4\nzu1:\ns_getpc_b64 s[96:97]\n1:\n" \
"s_cmp_eq_u32 s32, 4096\ns_cbranch_scc1 2f\ns_cmp_eq_u32 s32, 8192\ns_cbranch_scc1 3f\ns_cmp_eq_u32 s32, 12288\ns_cbranch_scc1 4f\ns_add_u32 s100, s96, zp0-1b\ns_addc_u32 s101, s97, 0\n" \
"s_swappc_b64 s[98:99], s[100:101]\ns_add_u32 s100, s96, zp3-1b\ns_addc_u32 s101, s97, 0\ns_swappc_b64 s[98:99], s[100:101]\ns_add_u32 s100, s96, zp7-1b\ns_addc_u32 s101, s97, 0\ns_swappc_b64 s[98:99], s[100:101]\ns_add_u32 s100, s96, zp11-1b\n" \
"s_addc_u32 s101, s97, 0\ns_swappc_b64 s[98:99], s[100:101]\ns_branch 9f\n2:\ns_add_u32 s100, s96, zp12-1b\ns_addc_u32 s101, s97, 0\ns_swappc_b64 s[98:99], s[100:101]\ns_add_u32 s100, s96, zp4-1b\n" \
"s_addc_u32 s101, s97, 0\ns_swappc_b64 s[98:99], s[100:101]\ns_add_u32 s100, s96, zp8-1b\ns_addc_u32 s101, s97, 0\ns_swappc_b64 s[98:99], s[100:101]\ns_branch 9f\n3:\ns_add_u32 s100, s96, zp13-1b\n" \
"s_addc_u32 s101, s97, 0\ns_swappc_b64 s[98:99], s[100:101]\ns_add_u32 s100, s96, zp5-1b\ns_addc_u32 s101, s97, 0\ns_swappc_b64 s[98:99], s[100:101]\ns_add_u32 s100, s96, zp9-1b\ns_addc_u32 s101, s97, 0\ns_swappc_b64 s[98:99], s[100:101]\n" \
"s_branch 9f\n4:\ns_add_u32 s100, s96, zp14-1b\ns_addc_u32 s101, s97, 0\ns_swappc_b64 s[98:99], s[100:101]\ns_add_u32 s100, s96, zp6-1b\ns_addc_u32 s101, s97, 0\ns_swappc_b64 s[98:99], s[100:101]\n" \
"s_add_u32 s100, s96, zp10-1b\ns_addc_u32 s101, s97, 0\ns_swappc_b64 s[98:99], s[100:101]\n9:\ns_branch zu4\nzu2:\ns_getpc_b64 s[96:97]\n1:\n" \
"s_cmp_eq_u32 s32, 4096\ns_cbranch_scc1 2f\ns_cmp_eq_u32 s32, 8192\ns_cbranch_scc1 3f\ns_cmp_eq_u32 s32, 12288\ns_cbranch_scc1 4f\ns_add_u32 s100, s96, zp0-1b\ns_addc_u32 s101, s97, 0\n" \
"s_swappc_b64 s[98:99], s[100:101]\ns_add_u32 s100, s96, zp18-1b\ns_addc_u32 s101, s97, 0\ns_swappc_b64 s[98:99], s[100:101]\ns_add_u32 s100, s96, zp22-1b\ns_addc_u32 s101, s97, 0\ns_swappc_b64 s[98:99], s[100:101]\ns_add_u32 s100, s96, zp26-1b\n" \
"s_addc_u32 s101, s97, 0\ns_swappc_b64 s[98:99], s[100:101]\ns_add_u32 s100, s96, zp27-1b\ns_addc_u32 s101, s97, 0\ns_swappc_b64 s[98:99], s[100:101]\ns_branch 9f\n2:\ns_add_u32 s100, s96, zp1-1b\n" \
"s_addc_u32 s101, s97, 0\ns_swappc_b64 s[98:99], s[100:101]\ns_add_u32 s100, s96, zp19-1b\ns_addc_u32 s101, s97, 0\ns_swappc_b64 s[98:99], s[100:101]\ns_add_u32 s100, s96, zp23-1b\ns_addc_u32 s101, s97, 0\ns_swappc_b64 s[98:99], s[100:101]\n" \
"s_add_u32 s100, s96, zp9-1b\ns_addc_u32 s101, s97, 0\ns_swappc_b64 s[98:99], s[100:101]\ns_add_u32 s100, s96, zp28-1b\ns_addc_u32 s101, s97, 0\ns_swappc_b64 s[98:99], s[100:101]\ns_branch 9f\n3:\n" \
"s_add_u32 s100, s96, zp16-1b\ns_addc_u32 s101, s97, 0\ns_swappc_b64 s[98:99], s[100:101]\ns_add_u32 s100, s96, zp20-1b\ns_addc_u32 s101, s97, 0\ns_swappc_b64 s[98:99], s[100:101]\ns_add_u32 s100, s96, zp24-1b\ns_addc_u32 s101, s97, 0\n" \
"s_swappc_b64 s[98:99], s[100:101]\ns_add_u32 s100, s96, zp10-1b\ns_addc_u32 s101, s97, 0\ns_swappc_b64 s[98:99], s[100:101]\ns_branch 9f\n4:\ns_add_u32 s100, s96, zp17-1b\ns_addc_u32 s101, s97, 0\n" \
"s_swappc_b64 s[98:99], s[100:101]\ns_add_u32 s100, s96, zp21-1b\ns_addc_u32 s101, s97, 0\ns_swappc_b64 s[98:99], s[100:101]\ns_add_u32 s100, s96, zp25-1b\ns_addc_u32 s101, s97, 0\ns_swappc_b64 s[98:99], s[100:101]\ns_add_u32 s100, s96, zp11-1b\n" \
"s_addc_u32 s101, s97, 0\ns_swappc_b64 s[98:99], s[100:101]\n9:\ns_branch zu4\nzu3:\ns_getpc_b64 s[96:97]\n1:\ns_cmp_eq_u32 s32, 4096\n" \
"s_cbranch_scc1 2f\ns_cmp_eq_u32 s32, 8192\ns_cbranch_scc1 3f\ns_cmp_eq_u32 s32, 12288\ns_cbranch_scc1 4f\ns_add_u32 s100, s96, zp0-1b\ns_addc_u32 s101, s97, 0\ns_swappc_b64 s[98:99], s[100:101]\n" \
"s_add_u32 s100, s96, zp17-1b\ns_addc_u32 s101, s97, 0\ns_swappc_b64 s[98:99], s[100:101]\ns_add_u32 s100, s96, zp21-1b\ns_addc_u32 s101, s97, 0\ns_swappc_b64 s[98:99], s[100:101]\ns_add_u32 s100, s96, zp25-1b\ns_addc_u32 s101, s97, 0\n" \
"s_swappc_b64 s[98:99], s[100:101]\ns_add_u32 s100, s96, zp11-1b\ns_addc_u32 s101, s97, 0\ns_swappc_b64 s[98:99], s[100:101]\ns_branch 9f\n2:\ns_add_u32 s100, s96, zp12-1b\ns_addc_u32 s101, s97, 0\n" \
"s_swappc_b64 s[98:99], s[100:101]\ns_add_u32 s100, s96, zp18-1b\ns_addc_u32 s101, s97, 0\ns_swappc_b64 s[98:99], s[100:101]\ns_add_u32 s100, s96, zp22-1b\ns_addc_u32 s101, s97, 0\ns_swappc_b64 s[98:99], s[100:101]\ns_add_u32 s100, s96, zp26-1b\n" \
"s_addc_u32 s101, s97, 0\ns_swappc_b64 s[98:99], s[100:101]\ns_add_u32 s100, s96, zp27-1b\ns_addc_u32 s101, s97, 0\ns_swappc_b64 s[98:99], s[100:101]\ns_branch 9f\n3:\ns_add_u32 s100, s96, zp13-1b\n" \
"s_addc_u32 s101, s97, 0\ns_swappc_b64 s[98:99], s[100:101]\ns_add_u32 s100, s96, zp19-1b\ns_addc_u32 s101, s97, 0\ns_swappc_b64 s[98:99], s[100:101]\ns_add_u32 s100, s96, zp23-1b\ns_addc_u32 s101, s97, 0\ns_swappc_b64 s[98:99], s[100:101]\n" \
"s_add_u32 s100, s96, zp9-1b\ns_addc_u32 s101, s97, 0\ns_swappc_b64 s[98:99], s[100:101]\ns_add_u32 s100, s96, zp28-1b\ns_addc_u32 s101, s97, 0\ns_swappc_b64 s[98:99], s[100:101]\ns_branch 9f\n4:\n" \
"s_add_u32 s100, s96, zp15-1b\ns_addc_u32 s101, s97, 0\ns_swappc_b64 s[98:99], s[100:101]\ns_add_u32 s100, s96, zp20-1b\ns_addc_u32 s101, s97, 0\ns_swappc_b64 s[98:99], s[100:101]\ns_add_u32 s100, s96, zp24-1b\ns_addc_u32 s101, s97, 0\n" \
"s_swappc_b64 s[98:99], s[100:101]\ns_add_u32 s100, s96, zp10-1b\ns_addc_u32 s101, s97, 0\ns_swappc_b64 s[98:99], s[100:101]\n9:\nzu4:\ns_branch 99f\n.p2align 12\n" \
"zp0:\ns_setpc_b64 s[98:99]\n99:\ns_waitcnt vmcnt(9)\ns_barrier\ns_add_u32 m0, s32, 98304\nTa 64,8,0\nTa 66,8,1024\n" \
"D 6,68,0,0\nRa 0,7,0\nTa 68,20,0\nD 6,68,s89,1024\nTa 70,20,1024\nRa 4,7,2048\nD 28,68,s90,2048\nTa 72,21,0\n" \
"Ta 74,21,1024\nD 28,68,s91,3072\ns_add_u32 m0, s32, 114688\nRa 8,7,4096\nTa 76,22,0\nD 6,68,s92,0\nTa 78,22,1024\nRa 12,7,6144\n" \
"D 6,68,s93,1024\nTa 80,23,0\nTa 82,23,1024\nD 28,68,s94,2048\nRa 16,7,8192\nTa 84,24,0\nD 28,68,s95,3072\nTa 86,24,1024\n" \
"Ra 20,7,10240\nTa 88,25,0\nTa 90,25,1024\nRa 24,7,12288\nTa 92,26,0\nTa 94,26,1024\nRa 28,7,14336\nv_add_u32 v5, 128, v5\n" \
"v_add_u32 v6, s72, v6\nv_add_u32 v28, s72, v28\nbuffer_load_dword v30, v4, s[52:55], 0 offen sc1\nv_add_u32 v4, s44, v4\ns_bitcmp1_b32 s33, 0\ns_waitcnt lgkmcnt(0)\ns_cbranch_scc1 zV\nZvaa 32,64,0\n" \
"Ta 96,8,8192\nZvaa 64,64,4\nTa 98,8,9216\nZvaa 68,68,4\nRa 32,27,0\nZvaa 36,68,0\nTa 100,20,8192\nZvaa 40,72,0\n" \
"Ta 102,20,9216\nZvaa 72,72,4\nRa 36,27,2048\nZvaa 76,76,4\nTa 104,21,8192\nZvaa 44,76,0\nTa 106,21,9216\nZvaa 48,80,0\n" \
"Ra 40,27,4096\nZvaa 80,80,4\nTa 108,22,8192\nZvaa 84,84,4\nTa 110,22,9216\nZvaa 52,84,0\nRa 44,27,6144\nZvaa 56,88,0\n" \
"Ta 112,23,8192\nZvaa 88,88,4\nTa 114,23,9216\nZvaa 92,92,4\nRa 48,27,8192\nZvaa 60,92,0\nTa 116,24,8192\nZvaa 124,92,8\n" \
"Ta 118,24,9216\nZvaa 156,92,12\nRa 52,27,10240\nZvaa 152,88,12\nTa 120,25,8192\nZvaa 120,88,8\nTa 122,25,9216\nZvaa 116,84,8\n" \
"Ra 56,27,12288\nZvaa 148,84,12\nTa 124,26,8192\nZvaa 144,80,12\nTa 126,26,9216\nZvaa 112,80,8\nRa 60,27,14336\nZvaa 108,76,8\n" \
"AR 2,252\nAR 3,253\nZvaa 140,76,12\nv_readfirstlane_b32 s96, v2\nv_readfirstlane_b32 s97, v3\nZvaa 136,72,12\ns_mul_i32 s97, s97, 0x7f4a7c15\ns_mul_hi_u32 s98, s96, 0x7f4a7c15\n" \
"Zvaa 104,72,8\ns_add_u32 s97, s97, s98\ns_mul_i32 s98, s96, 0x9e3779b9\nZvaa 100,68,8\ns_add_u32 s97, s97, s98\ns_mul_i32 s96, s96, 0x7f4a7c15\nZvaa 132,68,12\ns_add_u32 s96, s96, 0xd9b4e019\n" \
"s_addc_u32 s97, s97, 0x632be59b\nZvaa 128,64,12\nv_mov_b32 v2, s96\nv_mov_b32 v3, s97\nZvaa 96,64,8\ns_mov_b32 s96, 3\nZvaa 160,64,16\ns_waitcnt lgkmcnt(0)\n" \
"s_barrier\nZvaa 192,64,20\ns_add_u32 m0, s88, 0\nZvaa 196,68,20\nD 5,64,0,0\nZvaa 164,68,16\nZvaa 168,72,16\nZvaa 200,72,20\n" \
"Zvaa 204,76,20\nD 5,64,s73,1024\nZvaa 172,76,16\nZvaa 176,80,16\nZvaa 208,80,20\nZvaa 212,84,20\nD 5,64,s74,2048\nZvaa 180,84,16\n" \
"Zvaa 184,88,16\nZvaa 216,88,20\nZvaa 220,92,20\nD 5,64,s75,3072\nZvaa 188,92,16\nZvaa 252,92,24\nZaaa 156,92,28\ns_add_u32 m0, s88, 4096\n" \
"Zaaa 152,88,28\nD 5,64,s76,0\nZvaa 248,88,24\nZvaa 244,84,24\nZaaa 148,84,28\nZaaa 144,80,28\nD 5,64,s77,1024\nZvaa 240,80,24\n" \
"Zvaa 236,76,24\nZaaa 140,76,28\nZaaa 136,72,28\nD 5,64,s78,2048\nZvaa 232,72,24\nZvaa 228,68,24\nZaaa 132,68,28\nZaaa 128,64,28\n" \
"D 5,64,s79,3072\nZvaa 224,64,24\ns_branch 99f\n.p2align 12\nzp1:\ns_setpc_b64 s[98:99]\n99:\ns_nop 0\n" \
"zgK:\nMvaa 32,96,32\nMvaa 64,96,36\ns_add_u32 m0, s32, 65536\nMvaa 68,100,36\nD 6,68,0,0\nMvaa 36,100,32\nMvaa 40,104,32\n" \
"Mvaa 72,104,36\nMvaa 76,108,36\nD 6,68,s89,1024\nMvaa 44,108,32\nMvaa 48,112,32\nMvaa 80,112,36\nMvaa 84,116,36\nD 28,68,s90,2048\n" \
"Mvaa 52,116,32\nMvaa 56,120,32\nMvaa 88,120,36\nMvaa 92,124,36\nD 28,68,s91,3072\nMvaa 60,124,32\nMvaa 124,124,40\nMvaa 156,124,44\n" \
"s_add_u32 m0, s32, 81920\nMvaa 152,120,44\nD 6,68,s92,0\nMvaa 120,120,40\nMvaa 116,116,40\nMvaa 148,116,44\nMvaa 144,112,44\nD 6,68,s93,1024\n" \
"Mvaa 112,112,40\nMvaa 108,108,40\nMvaa 140,108,44\nMvaa 136,104,44\nD 28,68,s94,2048\nMvaa 104,104,40\nMvaa 100,100,40\nMvaa 132,100,44\n" \
"Mvaa 128,96,44\nD 28,68,s95,3072\nMvaa 96,96,40\ns_waitcnt vmcnt(17)\ns_barrier\nMvaa 160,96,48\nbuffer_load_dword v30, v4, s[52:55], 0 offen sc1\nTa 64,8,32768\n" \
"v_add_u32 v5, 128, v5\nMvaa 192,96,52\nTa 66,8,33792\nv_add_u32 v6, s72, v6\nMvaa 196,100,52\nRa 0,7,32768\nv_add_u32 v28, s72, v28\nMvaa 164,100,48\n" \
"Ta 68,20,32768\nMvaa 168,104,48\nTa 70,20,33792\nMvaa 200,104,52\nRa 4,7,34816\nMvaa 204,108,52\nv_add_u32 v4, s44, v4\nTa 72,21,32768\n" \
"Mvaa 172,108,48\nTa 74,21,33792\nMvaa 176,112,48\nRa 8,7,36864\nMvaa 208,112,52\nTa 76,22,32768\nMvaa 212,116,52\nTa 78,22,33792\n" \
"Mvaa 180,116,48\nRa 12,7,38912\nMvaa 184,120,48\nTa 80,23,32768\nMvaa 216,120,52\nTa 82,23,33792\nMvaa 220,124,52\nRa 16,7,40960\n" \
"Mvaa 188,124,48\nTa 84,24,32768\nMvaa 252,124,56\nTa 86,24,33792\nMaaa 156,124,60\nRa 20,7,43008\nMaaa 152,120,60\nTa 88,25,32768\n" \
"Mvaa 248,120,56\nTa 90,25,33792\nMvaa 244,116,56\nRa 24,7,45056\nMaaa 148,116,60\nTa 92,26,32768\nMaaa 144,112,60\nTa 94,26,33792\n" \
"Mvaa 240,112,56\nRa 28,7,47104\nMvaa 236,108,56\nMaaa 140,108,60\nMaaa 136,104,60\nMvaa 232,104,56\nMvaa 228,100,56\nMaaa 132,100,60\n" \
"Maaa 128,96,60\nMvaa 224,96,56\ns_waitcnt lgkmcnt(0)\nMvaa 32,64,0\nTa 96,8,40960\nMvaa 64,64,4\nTa 98,8,41984\nMvaa 68,68,4\n" \
"Ra 32,27,32768\nMvaa 36,68,0\nTa 100,20,40960\nMvaa 40,72,0\nTa 102,20,41984\nMvaa 72,72,4\nRa 36,27,34816\nMvaa 76,76,4\n" \
"Ta 104,21,40960\nMvaa 44,76,0\nTa 106,21,41984\nMvaa 48,80,0\nRa 40,27,36864\nMvaa 80,80,4\nTa 108,22,40960\nMvaa 84,84,4\n" \
"Ta 110,22,41984\nMvaa 52,84,0\nRa 44,27,38912\nMvaa 56,88,0\nTa 112,23,40960\nMvaa 88,88,4\nTa 114,23,41984\nMvaa 92,92,4\n" \
"Ra 48,27,40960\nMvaa 60,92,0\nTa 116,24,40960\nMvaa 124,92,8\nTa 118,24,41984\nMvaa 156,92,12\nRa 52,27,43008\nMvaa 152,88,12\n" \
"Ta 120,25,40960\nMvaa 120,88,8\nTa 122,25,41984\nMvaa 116,84,8\nRa 56,27,45056\nMvaa 148,84,12\nTa 124,26,40960\nMvaa 144,80,12\n" \
"Ta 126,26,41984\nMvaa 112,80,8\nRa 60,27,47104\nMvaa 108,76,8\nMvaa 140,76,12\nMvaa 136,72,12\nMvaa 104,72,8\nMvaa 100,68,8\n" \
"Mvaa 132,68,12\nMvaa 128,64,12\nMvaa 96,64,8\nMvaa 160,64,16\ns_waitcnt lgkmcnt(0)\ns_barrier\nMvaa 192,64,20\ns_add_u32 m0, s88, 32768\n" \
"Mvaa 196,68,20\nD 5,64,0,0\nMvaa 164,68,16\nMvaa 168,72,16\nMvaa 200,72,20\nMvaa 204,76,20\nD 5,64,s73,1024\nMvaa 172,76,16\n" \
"Mvaa 176,80,16\nMvaa 208,80,20\nMvaa 212,84,20\nD 5,64,s74,2048\nMvaa 180,84,16\nMvaa 184,88,16\nMvaa 216,88,20\nMvaa 220,92,20\n" \
"D 5,64,s75,3072\nMvaa 188,92,16\nMvaa 252,92,24\nMaaa 156,92,28\ns_add_u32 m0, s88, 36864\nMaaa 152,88,28\nD 5,64,s76,0\nMvaa 248,88,24\n" \
"Mvaa 244,84,24\nMaaa 148,84,28\nMaaa 144,80,28\nD 5,64,s77,1024\nMvaa 240,80,24\nMvaa 236,76,24\nMaaa 140,76,28\nMaaa 136,72,28\n" \
"D 5,64,s78,2048\nMvaa 232,72,24\nMvaa 228,68,24\nMaaa 132,68,28\nMaaa 128,64,28\nD 5,64,s79,3072\nMvaa 224,64,24\nMvaa 32,96,32\n" \
"Mvaa 64,96,36\ns_add_u32 m0, s32, 98304\nMvaa 68,100,36\nD 6,68,0,0\nMvaa 36,100,32\nMvaa 40,104,32\nMvaa 72,104,36\nMvaa 76,108,36\n" \
"D 6,68,s89,1024\nMvaa 44,108,32\nMvaa 48,112,32\nMvaa 80,112,36\nMvaa 84,116,36\nD 28,68,s90,2048\nMvaa 52,116,32\nMvaa 56,120,32\n" \
"Mvaa 88,120,36\nMvaa 92,124,36\nD 28,68,s91,3072\nMvaa 60,124,32\nMvaa 124,124,40\nMvaa 156,124,44\ns_add_u32 m0, s32, 114688\nMvaa 152,120,44\n" \
"D 6,68,s92,0\nMvaa 120,120,40\nMvaa 116,116,40\nMvaa 148,116,44\nMvaa 144,112,44\nD 6,68,s93,1024\nMvaa 112,112,40\nMvaa 108,108,40\n" \
"Mvaa 140,108,44\nMvaa 136,104,44\nD 28,68,s94,2048\nMvaa 104,104,40\nMvaa 100,100,40\nMvaa 132,100,44\nMvaa 128,96,44\nD 28,68,s95,3072\n" \
"Mvaa 96,96,40\ns_waitcnt vmcnt(17)\ns_barrier\nMvaa 160,96,48\nbuffer_load_dword v30, v4, s[52:55], 0 offen sc1\nTa 64,8,0\nv_add_u32 v5, 128, v5\nMvaa 192,96,52\n" \
"Ta 66,8,1024\nv_add_u32 v6, s72, v6\nMvaa 196,100,52\nRa 0,7,0\nv_add_u32 v28, s72, v28\nMvaa 164,100,48\nTa 68,20,0\nMvaa 168,104,48\n" \
"Ta 70,20,1024\nMvaa 200,104,52\nRa 4,7,2048\nMvaa 204,108,52\nv_add_u32 v4, s44, v4\nTa 72,21,0\nMvaa 172,108,48\nTa 74,21,1024\n" \
"Mvaa 176,112,48\nRa 8,7,4096\nMvaa 208,112,52\nTa 76,22,0\nMvaa 212,116,52\nTa 78,22,1024\nMvaa 180,116,48\nRa 12,7,6144\n" \
"Mvaa 184,120,48\nTa 80,23,0\nMvaa 216,120,52\nTa 82,23,1024\nMvaa 220,124,52\nRa 16,7,8192\nMvaa 188,124,48\nTa 84,24,0\n" \
"Mvaa 252,124,56\nTa 86,24,1024\nMaaa 156,124,60\nRa 20,7,10240\nMaaa 152,120,60\nTa 88,25,0\nMvaa 248,120,56\nTa 90,25,1024\n" \
"Mvaa 244,116,56\nRa 24,7,12288\nMaaa 148,116,60\nTa 92,26,0\nMaaa 144,112,60\nTa 94,26,1024\nMvaa 240,112,56\nRa 28,7,14336\n" \
"Mvaa 236,108,56\nMaaa 140,108,60\nMaaa 136,104,60\nMvaa 232,104,56\ns_sub_u32 s96, s96, 1\nMvaa 228,100,56\ns_cmp_lg_u32 s96, 0\nMaaa 132,100,60\n" \
"Maaa 128,96,60\nMvaa 224,96,56\ns_waitcnt lgkmcnt(0)\ns_cbranch_scc0 zhK\nMvaa 32,64,0\nTa 96,8,8192\nMvaa 64,64,4\nTa 98,8,9216\n" \
"Mvaa 68,68,4\nRa 32,27,0\nMvaa 36,68,0\nTa 100,20,8192\nMvaa 40,72,0\nTa 102,20,9216\nMvaa 72,72,4\nRa 36,27,2048\n" \
"Mvaa 76,76,4\nTa 104,21,8192\nMvaa 44,76,0\nTa 106,21,9216\nMvaa 48,80,0\nRa 40,27,4096\nMvaa 80,80,4\nTa 108,22,8192\n" \
"Mvaa 84,84,4\nTa 110,22,9216\nMvaa 52,84,0\nRa 44,27,6144\nMvaa 56,88,0\nTa 112,23,8192\nMvaa 88,88,4\nTa 114,23,9216\n" \
"Mvaa 92,92,4\nRa 48,27,8192\nMvaa 60,92,0\nTa 116,24,8192\nMvaa 124,92,8\nTa 118,24,9216\nMvaa 156,92,12\nRa 52,27,10240\n" \
"Mvaa 152,88,12\nTa 120,25,8192\nMvaa 120,88,8\nTa 122,25,9216\nMvaa 116,84,8\nRa 56,27,12288\nMvaa 148,84,12\nTa 124,26,8192\n" \
"Mvaa 144,80,12\nTa 126,26,9216\nMvaa 112,80,8\nRa 60,27,14336\nMvaa 108,76,8\nMvaa 140,76,12\nMvaa 136,72,12\nMvaa 104,72,8\n" \
"Mvaa 100,68,8\nMvaa 132,68,12\nMvaa 128,64,12\nMvaa 96,64,8\nMvaa 160,64,16\ns_waitcnt lgkmcnt(0)\ns_barrier\nMvaa 192,64,20\n" \
"s_add_u32 m0, s88, 0\nMvaa 196,68,20\nD 5,64,0,0\nMvaa 164,68,16\nMvaa 168,72,16\nMvaa 200,72,20\nMvaa 204,76,20\nD 5,64,s73,1024\n" \
"Mvaa 172,76,16\nMvaa 176,80,16\nMvaa 208,80,20\nMvaa 212,84,20\nD 5,64,s74,2048\nMvaa 180,84,16\nMvaa 184,88,16\nMvaa 216,88,20\n" \
"Mvaa 220,92,20\nD 5,64,s75,3072\nMvaa 188,92,16\nMvaa 252,92,24\nMaaa 156,92,28\ns_add_u32 m0, s88, 4096\nMaaa 152,88,28\nD 5,64,s76,0\n" \
"Mvaa 248,88,24\nMvaa 244,84,24\nMaaa 148,84,28\nMaaa 144,80,28\nD 5,64,s77,1024\nMvaa 240,80,24\nMvaa 236,76,24\nMaaa 140,76,28\n" \
"Maaa 136,72,28\nD 5,64,s78,2048\nMvaa 232,72,24\nMvaa 228,68,24\nMaaa 132,68,28\nMaaa 128,64,28\nD 5,64,s79,3072\nMvaa 224,64,24\n" \
"s_branch zgK\nzhK:\ns_bitcmp1_b32 s33, 1\ns_cbranch_scc1 zxK\nMvaa 32,64,0\nTa 96,8,8192\ns_mov_b64 s[64:65], s[84:85]\nMvaa 64,64,4\n" \
"Ta 98,8,9216\ns_mov_b32 s66, 4194304\nMvaa 68,68,4\nRa 32,27,0\ns_mov_b64 s[68:69], s[86:87]\nMvaa 36,68,0\nTa 100,20,8192\ns_mov_b32 s70, 8388608\n" \
"Mvaa 40,72,0\nTa 102,20,9216\ns_mov_b32 s73, 15360\nMvaa 72,72,4\nRa 36,27,2048\ns_mov_b32 s74, 129024\nMvaa 76,76,4\nTa 104,21,8192\n" \
"s_mov_b32 s75, 144384\nMvaa 44,76,0\nTa 106,21,9216\ns_add_u32 s76, s37, 0\nMvaa 48,80,0\nRa 40,27,4096\ns_add_u32 s77, s37, 15360\nMvaa 80,80,4\n" \
"Ta 108,22,8192\ns_add_u32 s78, s37, 129024\nMvaa 84,84,4\nTa 110,22,9216\ns_add_u32 s79, s37, 144384\nMvaa 52,84,0\nRa 44,27,6144\ns_mov_b32 s89, 31744\n" \
"Mvaa 56,88,0\nTa 112,23,8192\ns_mov_b32 s90, 63488\nMvaa 88,88,4\nTa 114,23,9216\ns_mov_b32 s91, 95232\nMvaa 92,92,4\nRa 48,27,8192\n" \
"s_mov_b32 s72, 524288\nMvaa 60,92,0\nTa 116,24,8192\nv_xor_b32 v28, 128, v14\nMvaa 124,92,8\nTa 118,24,9216\nv_mov_b32 v4, v15\nMvaa 156,92,12\n" \
"Ra 52,27,10240\nMvaa 152,88,12\nTa 120,25,8192\nMvaa 120,88,8\nTa 122,25,9216\nMvaa 116,84,8\nRa 56,27,12288\nMvaa 148,84,12\n" \
"Ta 124,26,8192\nMvaa 144,80,12\nTa 126,26,9216\nMvaa 112,80,8\nRa 60,27,14336\nMvaa 108,76,8\nMvaa 140,76,12\nMvaa 136,72,12\n" \
"Mvaa 104,72,8\nMvaa 100,68,8\nMvaa 132,68,12\nMvaa 128,64,12\nMvaa 96,64,8\nMvaa 160,64,16\ns_waitcnt lgkmcnt(0)\ns_barrier\n" \
"Mvaa 192,64,20\ns_add_u32 m0, s88, 131072\nMvaa 196,68,20\nD 13,64,0,0\nMvaa 164,68,16\nMvaa 168,72,16\nMvaa 200,72,20\nMvaa 204,76,20\n" \
"D 13,64,s73,1024\nMvaa 172,76,16\nMvaa 176,80,16\nMvaa 208,80,20\nMvaa 212,84,20\nD 13,64,s74,2048\nMvaa 180,84,16\nMvaa 184,88,16\n" \
"Mvaa 216,88,20\nMvaa 220,92,20\nD 13,64,s75,3072\nMvaa 188,92,16\nMvaa 252,92,24\nMaaa 156,92,28\ns_add_u32 m0, s88, 135168\nMaaa 152,88,28\n" \
"D 13,64,s76,0\nMvaa 248,88,24\nMvaa 244,84,24\nMaaa 148,84,28\nMaaa 144,80,28\nD 13,64,s77,1024\nMvaa 240,80,24\ns_branch 99f\n" \
".p2align 12\nzp2:\ns_setpc_b64 s[98:99]\n99:\nMvaa 236,76,24\nMaaa 140,76,28\nMaaa 136,72,28\nD 13,64,s78,2048\n" \
"Mvaa 232,72,24\nMvaa 228,68,24\nMaaa 132,68,28\nMaaa 128,64,28\nD 13,64,s79,3072\nMvaa 224,64,24\nMvaa 32,96,32\nMvaa 64,96,36\n" \
"s_add_u32 m0, s32, 81920\nMvaa 68,100,36\nD 14,68,0,0\nMvaa 36,100,32\nMvaa 40,104,32\nMvaa 72,104,36\nMvaa 76,108,36\nD 14,68,s89,1024\n" \
"Mvaa 44,108,32\nMvaa 48,112,32\nMvaa 80,112,36\nMvaa 84,116,36\nD 28,68,s90,2048\nMvaa 52,116,32\nMvaa 56,120,32\nMvaa 88,120,36\n" \
"Mvaa 92,124,36\nD 28,68,s91,3072\nMvaa 60,124,32\nMvaa 124,124,40\nMvaa 156,124,44\ns_add_u32 m0, s88, 0\nv_add_u32 v13, 128, v13\nMvaa 152,120,44\n" \
"D 13,64,0,0\nMvaa 120,120,40\nMvaa 116,116,40\nMvaa 148,116,44\nMvaa 144,112,44\nD 13,64,s73,1024\nMvaa 112,112,40\nMvaa 108,108,40\n" \
"Mvaa 140,108,44\nMvaa 136,104,44\nD 13,64,s74,2048\nMvaa 104,104,40\nMvaa 100,100,40\nMvaa 132,100,44\nMvaa 128,96,44\nD 13,64,s75,3072\n" \
"Mvaa 96,96,40\ns_waitcnt vmcnt(17)\ns_barrier\nMvaa 160,96,48\nTa 64,8,32768\nMvaa 192,96,52\nTa 66,8,33792\nMvaa 196,100,52\n" \
"Ra 0,7,32768\nMvaa 164,100,48\nTa 68,20,32768\nMvaa 168,104,48\nTa 70,20,33792\nMvaa 200,104,52\nRa 4,7,34816\nMvaa 204,108,52\n" \
"Ta 72,21,32768\nMvaa 172,108,48\nTa 74,21,33792\nMvaa 176,112,48\nRa 8,7,36864\nMvaa 208,112,52\nTa 76,22,32768\nMvaa 212,116,52\n" \
"Ta 78,22,33792\nMvaa 180,116,48\nRa 12,7,38912\nMvaa 184,120,48\nTa 80,23,32768\nMvaa 216,120,52\nTa 82,23,33792\nMvaa 220,124,52\n" \
"Ra 16,7,40960\nMvaa 188,124,48\nTa 84,24,32768\nMvaa 252,124,56\nTa 86,24,33792\nMaaa 156,124,60\nRa 20,7,43008\nMaaa 152,120,60\n" \
"Ta 88,25,32768\nMvaa 248,120,56\nTa 90,25,33792\nMvaa 244,116,56\nRa 24,7,45056\nMaaa 148,116,60\nTa 92,26,32768\nMaaa 144,112,60\n" \
"Ta 94,26,33792\nMvaa 240,112,56\nRa 28,7,47104\nMvaa 236,108,56\nMaaa 140,108,60\nMaaa 136,104,60\nMvaa 232,104,56\nMvaa 228,100,56\n" \
"Maaa 132,100,60\nMaaa 128,96,60\nMvaa 224,96,56\ns_waitcnt lgkmcnt(0)\nMvaa 32,64,0\nTa 96,8,40960\nMvaa 64,64,4\nTa 98,8,41984\n" \
"Mvaa 68,68,4\nRa 32,27,32768\nMvaa 36,68,0\nTa 100,20,40960\nMvaa 40,72,0\nTa 102,20,41984\nMvaa 72,72,4\nRa 36,27,34816\n" \
"Mvaa 76,76,4\nTa 104,21,40960\nMvaa 44,76,0\nTa 106,21,41984\nMvaa 48,80,0\nRa 40,27,36864\nMvaa 80,80,4\nTa 108,22,40960\n" \
"Mvaa 84,84,4\nTa 110,22,41984\nMvaa 52,84,0\nRa 44,27,38912\nMvaa 56,88,0\nTa 112,23,40960\nMvaa 88,88,4\nTa 114,23,41984\n" \
"Mvaa 92,92,4\nRa 48,27,40960\nMvaa 60,92,0\nTa 116,24,40960\nMvaa 124,92,8\nTa 118,24,41984\nMvaa 156,92,12\nRa 52,27,43008\n" \
"Mvaa 152,88,12\nTa 120,25,40960\nMvaa 120,88,8\nTa 122,25,41984\nMvaa 116,84,8\nRa 56,27,45056\nMvaa 148,84,12\nTa 124,26,40960\n" \
"Mvaa 144,80,12\nTa 126,26,41984\nMvaa 112,80,8\nRa 60,27,47104\nMvaa 108,76,8\ns_and_b32 s96, s32, 0x1000\ns_lshl_b32 s97, s96, 1\ns_lshl_b32 s96, s96, 2\n" \
"Mvaa 140,76,12\nv_add_u32 v16, s97, v7\nv_subrev_u32 v20, s96, v8\nv_xor_b32 v17, 64, v16\nMvaa 136,72,12\nv_add_u32 v18, 0x20000, v16\nv_add_u32 v19, 0x20000, v17\nv_xor_b32 v21, 32, v20\n" \
"Mvaa 104,72,8\nv_xor_b32 v22, 64, v20\nv_xor_b32 v23, 96, v20\nv_xor_b32 v24, 128, v20\nMvaa 100,68,8\nv_xor_b32 v25, 160, v20\nv_xor_b32 v26, 192, v20\nv_xor_b32 v27, 224, v20\n" \
"Mvaa 132,68,12\nMvaa 128,64,12\nMvaa 96,64,8\nMvaa 160,64,16\ns_waitcnt vmcnt(4) lgkmcnt(0)\ns_barrier\nMvaa 192,64,20\ns_add_u32 m0, s88, 4096\n" \
"Mvaa 196,68,20\nD 13,64,s76,0\nMvaa 164,68,16\nTa 160,20,24576\nMvaa 168,72,16\nTa 162,20,25600\nMvaa 200,72,20\nD 13,64,s77,1024\n" \
"Mvaa 204,76,20\nTa 164,21,24576\nMvaa 172,76,16\nTa 166,21,25600\nMvaa 176,80,16\nD 13,64,s78,2048\nMvaa 208,80,20\nTa 168,22,24576\n" \
"Mvaa 212,84,20\nTa 170,22,25600\nMvaa 180,84,16\nD 13,64,s79,3072\nMvaa 184,88,16\nTa 172,23,24576\nMvaa 216,88,20\ns_add_u32 m0, s32, 65536\n" \
"v_add_u32 v14, s72, v14\nv_add_u32 v28, s72, v28\nMvaa 220,92,20\nD 14,68,0,0\nMvaa 188,92,16\nTa 174,23,25600\nMvaa 252,92,24\nTa 176,24,24576\n" \
"Maaa 156,92,28\nD 14,68,s89,1024\nMaaa 152,88,28\nTa 178,24,25600\nMvaa 248,88,24\nTa 180,25,24576\nMvaa 244,84,24\nD 28,68,s90,2048\n" \
"Maaa 148,84,28\nTa 182,25,25600\nMaaa 144,80,28\nTa 184,26,24576\nMvaa 240,80,24\nD 28,68,s91,3072\nMvaa 236,76,24\nTa 186,26,25600\n" \
"Maaa 140,76,28\ns_add_u32 m0, s88, 32768\nv_add_u32 v13, 128, v13\nMaaa 136,72,28\nD 13,64,0,0\nMvaa 232,72,24\nTa 188,27,24576\nMvaa 228,68,24\n" \
"Ta 190,27,25600\nMaaa 132,68,28\nD 13,64,s73,1024\nMaaa 128,64,28\nMvaa 224,64,24\nMvaa 32,96,32\nD 13,64,s74,2048\nMvaa 36,100,32\n" \
"Ta 64,20,16384\nMvaa 40,104,32\nTa 66,20,17408\nMvaa 44,108,32\nD 13,64,s75,3072\nMvaa 48,112,32\nMvaa 52,116,32\ns_add_u32 m0, s88, 36864\n" \
"Mvaa 56,120,32\nD 13,64,s76,0\nMvaa 60,124,32\nRa 0,18,0\nMvaa 64,96,36\nMvaa 68,100,36\nD 13,64,s77,1024\nMvaa 72,104,36\n" \
"Ra 16,19,0\nC 32,32,33\nC 33,34,35\nMvaa 76,108,36\nC 34,36,37\nC 35,38,39\nC 36,40,41\nMvaa 80,112,36\n" \
"D 13,64,s78,2048\nC 37,42,43\nMvaa 84,116,36\nTa 68,21,16384\nC 38,44,45\nC 39,46,47\nMvaa 88,120,36\nC 40,48,49\n" \
"C 41,50,51\nC 42,52,53\nMvaa 92,124,36\nD 13,64,s79,3072\nC 43,54,55\nMvaa 96,96,40\nTa 70,21,17408\nC 44,56,57\n" \
"C 45,58,59\nMvaa 100,100,40\ns_add_u32 m0, s32, 98304\nv_add_u32 v14, s72, v14\nv_add_u32 v28, s72, v28\nMvaa 104,104,40\nD 14,68,0,0\nC 46,60,61\n" \
"Mvaa 108,108,40\nC 47,62,63\nC 48,64,65\nC 49,66,67\nMvaa 112,112,40\nTa 72,22,16384\nC 50,68,69\nC 51,70,71\n" \
"Mvaa 116,116,40\nD 14,68,s89,1024\nC 52,72,73\nMvaa 120,120,40\nC 53,74,75\nC 54,76,77\nC 55,78,79\nMvaa 124,124,40\n" \
"Ta 74,22,17408\nC 56,80,81\nC 57,82,83\nMvaa 128,96,44\nD 28,68,s90,2048\nC 58,84,85\nMvaa 132,100,44\nC 59,86,87\n" \
"C 60,88,89\nC 61,90,91\nMvaa 136,104,44\nTa 76,23,16384\nC 62,92,93\nC 63,94,95\nMvaa 140,108,44\nD 28,68,s91,3072\n" \
"C 64,96,97\nMvaa 144,112,44\nC 65,98,99\nC 66,100,101\nC 67,102,103\nMvaa 148,116,44\nTa 78,23,17408\nC 68,104,105\n" \
"C 69,106,107\nMvaa 152,120,44\nC 70,108,109\nC 71,110,111\nC 72,112,113\nMvaa 156,124,44\nTa 80,24,16384\nC 73,114,115\n" \
"C 74,116,117\nMaaa 128,96,60\nC 75,118,119\nC 76,120,121\nC 77,122,123\nMaaa 132,100,60\nTa 82,24,17408\nC 78,124,125\n" \
"C 79,126,127\nMaaa 136,104,60\nC 80,128,129\nC 81,130,131\nC 82,132,133\nMaaa 140,108,60\nTa 84,25,16384\nC 83,134,135\n" \
"C 84,136,137\nMaaa 144,112,60\nC 85,138,139\nC 86,140,141\nC 87,142,143\nMaaa 148,116,60\nTa 86,25,17408\nC 88,144,145\n" \
"C 89,146,147\nMaaa 152,120,60\nC 90,148,149\nC 91,150,151\nC 92,152,153\nMaaa 156,124,60\nTa 88,26,16384\nC 93,154,155\n" \
"C 94,156,157\nMvaa 160,96,48\nC 95,158,159\nMvaa 164,100,48\nTa 90,26,17408\nMvaa 168,104,48\nAR 96,128\nAR 97,129\n" \
"AR 98,130\nMvaa 172,108,48\nTa 92,27,16384\nAR 99,131\nAR 100,132\nMvaa 176,112,48\nAR 101,133\nAR 102,134\n" \
"AR 103,135\nMvaa 180,116,48\nTa 94,27,17408\nC 96,96,97\nC 97,98,99\nMvaa 184,120,48\nC 98,100,101\nC 99,102,103\n" \
"AW 240,96\nMvaa 188,124,48\nRa 4,18,2048\nAW 241,97\nAW 242,98\nMvaa 192,96,52\nAW 243,99\nAR 104,136\n" \
"AR 105,137\nMvaa 196,100,52\nRa 20,19,2048\nAR 106,138\nAR 107,139\nMvaa 200,104,52\nAR 108,140\nAR 109,141\n" \
"AR 110,142\nMvaa 204,108,52\nRa 8,18,4096\nAR 111,143\nC 104,104,105\nMvaa 208,112,52\nC 105,106,107\nC 106,108,109\n" \
"C 107,110,111\nMvaa 212,116,52\nRa 24,19,4096\nAW 244,104\nAW 245,105\nMvaa 216,120,52\nAW 246,106\nAW 247,107\n" \
"AR 112,144\nMvaa 220,124,52\nRa 12,18,6144\nAR 113,145\nAR 114,146\nMvaa 224,96,56\nAR 115,147\nAR 116,148\n" \
"AR 117,149\nMvaa 228,100,56\nRa 28,19,6144\nAR 118,150\nAR 119,151\nMvaa 232,104,56\nC 112,112,113\nC 113,114,115\n" \
"C 114,116,117\nMvaa 236,108,56\nC 115,118,119\nAW 248,112\nAW 249,113\nMvaa 240,112,56\nAW 250,114\nAW 251,115\n" \
"AR 120,152\nMvaa 244,116,56\nAR 121,153\nAR 122,154\nAR 123,155\nMvaa 248,120,56\nAR 124,156\nAR 125,157\n" \
"AR 126,158\nMvaa 252,124,56\nAR 127,159\nC 120,120,121\nC 121,122,123\ns_waitcnt lgkmcnt(6)\nzY:\nZvaa 128,64,0\n" \
"v_add_u32 v13, 128, v13\nC 122,124,125\nC 123,126,127\nMvaa 128,160,16\ns_add_u32 m0, s88, 131072\nAW 252,120\nAW 253,121\nZvaa 132,164,16\n" \
"D 13,64,0,0\nAW 254,122\nMvaa 132,68,0\nv_add_u32 v14, s72, v14\nAW 255,123\nC 160,160,161\nZvaa 136,72,0\nv_add_u32 v28, s72, v28\n" \
"C 161,162,163\nC 162,164,165\nMvaa 136,168,16\nC 163,166,167\nAW 192,160\nAW 193,161\nZvaa 140,172,16\nAW 194,162\n" \
"AW 195,163\nC 168,168,169\nMvaa 140,76,0\ns_waitcnt lgkmcnt(0)\nD 13,64,s73,1024\nZvaa 144,80,0\nC 169,170,171\nC 170,172,173\n" \
"C 171,174,175\nMvaa 144,176,16\nAW 196,168\nAW 197,169\nAW 198,170\nZvaa 148,180,16\nAW 199,171\nC 176,176,177\n" \
"C 177,178,179\nMvaa 148,84,0\nC 178,180,181\nC 179,182,183\nAW 200,176\nZvaa 152,88,0\ns_branch 99f\n.p2align 12\n" \
"zp3:\ns_setpc_b64 s[98:99]\n99:\nD 13,64,s74,2048\nAW 201,177\nMvaa 152,184,16\nAW 202,178\nAW 203,179\n" \
"C 184,184,185\nZvaa 156,188,16\nC 185,186,187\nC 186,188,189\nC 187,190,191\nAW 204,184\nAW 205,185\nAW 206,186\n" \
"AW 207,187\nMvaa 156,92,0\nC 192,192,193\nC 193,194,195\nC 194,196,197\nZvaa 188,92,4\ns_waitcnt vmcnt(15)\ns_barrier\n" \
"C 195,198,199\nMvaa 188,188,20\nTa 96,20,0\nTa 98,20,1024\nAW 208,192\nZvaa 184,184,20\nD 13,64,s75,3072\nAW 209,193\n" \
"Mvaa 184,88,4\nTa 128,20,8192\nAW 210,194\nAW 211,195\nZvaa 180,84,4\nTa 130,20,9216\nC 200,200,201\nC 201,202,203\n" \
"Mvaa 180,180,20\nRa 32,16,0\nRa 48,17,0\nC 202,204,205\nZvaa 176,176,20\ns_add_u32 m0, s88, 135168\nC 203,206,207\nAW 212,200\n" \
"Mvaa 176,80,4\nD 13,64,s76,0\nAW 213,201\nZvaa 172,76,4\nTa 100,21,0\nAW 214,202\nAW 215,203\nMvaa 172,172,20\n" \
"Ta 102,21,1024\nTa 132,21,8192\nC 208,208,209\nZvaa 168,168,20\nTa 134,21,9216\nC 209,210,211\nC 210,212,213\nMvaa 168,72,4\n" \
"Ta 104,22,0\nC 211,214,215\nAW 216,208\nZvaa 164,68,4\nD 13,64,s77,1024\nAW 217,209\nMvaa 164,164,20\nTa 106,22,1024\n" \
"Ta 136,22,8192\nAW 218,210\nZvaa 160,160,20\nTa 138,22,9216\nAW 219,211\nC 216,216,217\nC 217,218,219\nC 218,220,221\n" \
"C 219,222,223\nAW 220,216\nAW 221,217\nAW 222,218\nAW 223,219\nMvaa 160,64,4\nTa 108,23,0\nC 224,224,225\n" \
"C 225,226,227\nZvaa 192,64,8\nTa 110,23,1024\nC 226,228,229\nC 227,230,231\nMvaa 192,160,24\nD 13,64,s78,2048\nAW 224,224\n" \
"Zvaa 196,164,24\nTa 140,23,8192\nTa 142,23,9216\nAW 225,225\nMvaa 196,68,8\nTa 112,24,0\nAW 226,226\nAW 227,227\n" \
"Zvaa 200,72,8\nTa 114,24,1024\nC 232,232,233\nC 233,234,235\nMvaa 200,168,24\nTa 144,24,8192\nC 234,236,237\nC 235,238,239\n" \
"Zvaa 204,172,24\nD 13,64,s79,3072\nAW 228,232\nMvaa 204,76,8\nTa 146,24,9216\nTa 116,25,0\nAW 229,233\nZvaa 208,80,8\n" \
"Ta 118,25,1024\nAW 230,234\nAW 231,235\nMvaa 208,176,24\nv_add_u32 v13, 128, v13\nTa 148,25,8192\nC 240,240,241\nZvaa 212,180,24\n" \
"s_add_u32 m0, s32, 81920\nC 241,242,243\nC 242,244,245\nMvaa 212,84,8\nD 14,68,0,0\nC 243,246,247\nZvaa 216,88,8\nTa 150,25,9216\n" \
"Ta 120,26,0\nAW 232,240\nMvaa 216,184,24\nTa 122,26,1024\nAW 233,241\nAW 234,242\nZvaa 220,188,24\nTa 152,26,8192\n" \
"AW 235,243\nC 248,248,249\nC 249,250,251\nC 250,252,253\nC 251,254,255\nAW 236,248\nAW 237,249\nAW 238,250\n" \
"AW 239,251\nMvaa 220,92,8\nTa 154,26,9216\nZvaa 252,92,12\nD 14,68,s89,1024\nMvaa 252,188,28\nTa 124,27,0\nTa 126,27,1024\n" \
"Zvaa 248,184,28\nTa 156,27,8192\nMvaa 248,88,12\nTa 158,27,9216\nZvaa 244,84,12\nRa 36,16,2048\nMvaa 244,180,28\nD 28,68,s90,2048\n" \
"Zvaa 240,176,28\nRa 52,17,2048\nRa 40,16,4096\nMvaa 240,80,12\nRa 56,17,4096\nZvaa 236,76,12\nRa 44,16,6144\nMvaa 236,172,28\n" \
"Ra 60,17,6144\nZvaa 232,168,28\nD 28,68,s91,3072\nMvaa 232,72,12\ns_add_u32 s95, s34, 0\nZvaa 228,68,12\nbuffer_store_dwordx4 v[32:35], v0, s[60:63], s95 offen offset:0 sc1\nMvaa 228,164,28\n" \
"v_add_u32 v14, s72, v14\nZvaa 224,160,28\nv_add_u32 v28, s72, v28\nMvaa 224,64,12\ns_waitcnt lgkmcnt(6)\nMvaa 128,96,32\nMvaa 128,128,48\ns_add_u32 m0, s88, 0\n" \
"Mvaa 132,132,48\nD 13,64,0,0\nMvaa 132,100,32\nMvaa 136,104,32\nMvaa 136,136,48\nMvaa 140,140,48\nMvaa 140,108,32\ns_waitcnt lgkmcnt(0)\n" \
"D 13,64,s73,1024\nMvaa 144,112,32\nMvaa 144,144,48\nMvaa 148,148,48\nbuffer_store_dwordx4 v[36:39], v0, s[60:63], s95 offen offset:1024 sc1\nMvaa 148,116,32\nMvaa 152,120,32\nD 13,64,s74,2048\n" \
"Mvaa 152,152,48\nMvaa 156,156,48\nMvaa 156,124,32\nMvaa 188,124,36\ns_waitcnt vmcnt(17)\ns_barrier\nMvaa 188,156,52\nTa 64,20,32768\n" \
"Ta 66,20,33792\nMvaa 184,152,52\nD 13,64,s75,3072\nMvaa 184,120,36\nTa 160,20,40960\nMvaa 180,116,36\nTa 162,20,41984\nMvaa 180,148,52\n" \
"Ra 0,16,32768\nRa 16,17,32768\nMvaa 176,144,52\ns_add_u32 m0, s88, 4096\nMvaa 176,112,36\nD 13,64,s76,0\nMvaa 172,108,36\nTa 68,21,32768\n" \
"Mvaa 172,140,52\nTa 70,21,33792\nTa 164,21,40960\nMvaa 168,136,52\nTa 166,21,41984\nMvaa 168,104,36\nTa 72,22,32768\nMvaa 164,100,36\n" \
"D 13,64,s77,1024\nMvaa 164,132,52\nTa 74,22,33792\nTa 168,22,40960\nMvaa 160,128,52\nTa 170,22,41984\nMvaa 160,96,36\nTa 76,23,32768\n" \
"Mvaa 192,96,40\nTa 78,23,33792\nMvaa 192,128,56\nD 13,64,s78,2048\nMvaa 196,132,56\nTa 172,23,40960\nTa 174,23,41984\nMvaa 196,100,40\n" \
"Ta 80,24,32768\nMvaa 200,104,40\nTa 82,24,33792\nMvaa 200,136,56\nTa 176,24,40960\nMvaa 204,140,56\nD 13,64,s79,3072\nMvaa 204,108,40\n" \
"Ta 178,24,41984\nTa 84,25,32768\nMvaa 208,112,40\nTa 86,25,33792\nMvaa 208,144,56\nv_add_u32 v13, 128, v13\nTa 180,25,40960\nMvaa 212,148,56\n" \
"s_add_u32 m0, s32, 65536\nMvaa 212,116,40\nD 14,68,0,0\nMvaa 216,120,40\nTa 182,25,41984\nTa 88,26,32768\nMvaa 216,152,56\nTa 90,26,33792\n" \
"Mvaa 220,156,56\nTa 184,26,40960\nMvaa 220,124,40\nTa 186,26,41984\nMvaa 252,124,44\nD 14,68,s89,1024\nMvaa 252,156,60\nTa 92,27,32768\n" \
"Ta 94,27,33792\nMvaa 248,152,60\nTa 188,27,40960\nMvaa 248,120,44\nTa 190,27,41984\nMvaa 244,116,44\nRa 4,16,34816\nMvaa 244,148,60\n" \
"D 28,68,s90,2048\nMvaa 240,144,60\nRa 20,17,34816\nRa 8,16,36864\nMvaa 240,112,44\nRa 24,17,36864\nMvaa 236,108,44\nRa 12,16,38912\n" \
"Mvaa 236,140,60\nRa 28,17,38912\nMvaa 232,136,60\nD 28,68,s91,3072\nMvaa 232,104,44\nMvaa 228,100,44\nMvaa 228,132,60\nv_add_u32 v14, s72, v14\n" \
"Mvaa 224,128,60\nv_add_u32 v28, s72, v28\nMvaa 224,96,44\ns_waitcnt lgkmcnt(6)\nMvaa 128,64,0\nMvaa 128,160,16\ns_add_u32 m0, s88, 32768\nMvaa 132,164,16\n" \
"D 13,64,0,0\nMvaa 132,68,0\nMvaa 136,72,0\nMvaa 136,168,16\nMvaa 140,172,16\nMvaa 140,76,0\ns_waitcnt lgkmcnt(0)\nD 13,64,s73,1024\n" \
"Mvaa 144,80,0\nMvaa 144,176,16\nMvaa 148,180,16\nbuffer_store_dwordx4 v[40:43], v0, s[60:63], s95 offen offset:2048 sc1\nMvaa 148,84,0\nMvaa 152,88,0\nD 13,64,s74,2048\nMvaa 152,184,16\n" \
"Mvaa 156,188,16\nMvaa 156,92,0\nMvaa 188,92,4\ns_waitcnt vmcnt(18)\ns_barrier\nMvaa 188,188,20\nTa 96,20,16384\nTa 98,20,17408\n" \
"Mvaa 184,184,20\nD 13,64,s75,3072\nMvaa 184,88,4\nTa 128,20,24576\nMvaa 180,84,4\nTa 130,20,25600\nMvaa 180,180,20\nRa 32,18,0\n" \
"Ra 48,19,0\nMvaa 176,176,20\ns_add_u32 m0, s88, 36864\nMvaa 176,80,4\nD 13,64,s76,0\nMvaa 172,76,4\nTa 100,21,16384\nMvaa 172,172,20\n" \
"Ta 102,21,17408\nTa 132,21,24576\nMvaa 168,168,20\nTa 134,21,25600\nMvaa 168,72,4\nTa 104,22,16384\nMvaa 164,68,4\nD 13,64,s77,1024\n" \
"Mvaa 164,164,20\nTa 106,22,17408\nTa 136,22,24576\nMvaa 160,160,20\nTa 138,22,25600\nMvaa 160,64,4\nTa 108,23,16384\nMvaa 192,64,8\n" \
"Ta 110,23,17408\nMvaa 192,160,24\nD 13,64,s78,2048\nMvaa 196,164,24\nTa 140,23,24576\nTa 142,23,25600\nMvaa 196,68,8\nTa 112,24,16384\n" \
"Mvaa 200,72,8\nTa 114,24,17408\nMvaa 200,168,24\nTa 144,24,24576\nMvaa 204,172,24\nD 13,64,s79,3072\nMvaa 204,76,8\nTa 146,24,25600\n" \
"Ta 116,25,16384\nMvaa 208,80,8\nTa 118,25,17408\nMvaa 208,176,24\nv_add_u32 v13, 128, v13\nTa 148,25,24576\nMvaa 212,180,24\ns_add_u32 m0, s32, 98304\n" \
"Mvaa 212,84,8\nD 14,68,0,0\nMvaa 216,88,8\nTa 150,25,25600\nTa 120,26,16384\nMvaa 216,184,24\nTa 122,26,17408\nMvaa 220,188,24\n" \
"Ta 152,26,24576\nMvaa 220,92,8\nTa 154,26,25600\nMvaa 252,92,12\nD 14,68,s89,1024\nMvaa 252,188,28\nTa 124,27,16384\nTa 126,27,17408\n" \
"Mvaa 248,184,28\nTa 156,27,24576\nMvaa 248,88,12\nTa 158,27,25600\nMvaa 244,84,12\nRa 36,18,2048\nMvaa 244,180,28\nD 28,68,s90,2048\n" \
"Mvaa 240,176,28\nRa 52,19,2048\nRa 40,18,4096\nMvaa 240,80,12\nRa 56,19,4096\nMvaa 236,76,12\nRa 44,18,6144\nMvaa 236,172,28\n" \
"Ra 60,19,6144\nMvaa 232,168,28\nD 28,68,s91,3072\nMvaa 232,72,12\nMvaa 228,68,12\nMvaa 228,164,28\nv_add_u32 v14, s72, v14\nMvaa 224,160,28\n" \
"v_add_u32 v28, s72, v28\nMvaa 224,64,12\ns_waitcnt lgkmcnt(6)\nMvaa 128,96,32\nMvaa 128,128,48\ns_add_u32 m0, s88, 131072\nMvaa 132,132,48\nD 13,64,0,0\n" \
"Mvaa 132,100,32\nMvaa 136,104,32\nMvaa 136,136,48\nMvaa 140,140,48\nMvaa 140,108,32\ns_waitcnt lgkmcnt(0)\nD 13,64,s73,1024\nMvaa 144,112,32\n" \
"Mvaa 144,144,48\nMvaa 148,148,48\nbuffer_store_dwordx4 v[44:47], v0, s[60:63], s95 offen offset:3072 sc1\nMvaa 148,116,32\nMvaa 152,120,32\nD 13,64,s74,2048\nMvaa 152,152,48\nMvaa 156,156,48\n" \
"Mvaa 156,124,32\nMvaa 188,124,36\ns_waitcnt vmcnt(17)\ns_barrier\nMvaa 188,156,52\nTa 64,20,0\nTa 66,20,1024\nMvaa 184,152,52\n" \
"D 13,64,s75,3072\nMvaa 184,120,36\nTa 160,20,8192\nMvaa 180,116,36\nTa 162,20,9216\nMvaa 180,148,52\nRa 0,16,0\nRa 16,17,0\n" \
"Mvaa 176,144,52\ns_add_u32 m0, s88, 135168\nMvaa 176,112,36\nD 13,64,s76,0\nMvaa 172,108,36\nTa 68,21,0\nMvaa 172,140,52\nTa 70,21,1024\n" \
"Ta 164,21,8192\nMvaa 168,136,52\nTa 166,21,9216\nMvaa 168,104,36\nTa 72,22,0\nMvaa 164,100,36\nD 13,64,s77,1024\nMvaa 164,132,52\n" \
"Ta 74,22,1024\nTa 168,22,8192\nMvaa 160,128,52\nTa 170,22,9216\nMvaa 160,96,36\nTa 76,23,0\nMvaa 192,96,40\nTa 78,23,1024\n" \
"Mvaa 192,128,56\nD 13,64,s78,2048\nMvaa 196,132,56\nTa 172,23,8192\nTa 174,23,9216\nMvaa 196,100,40\nTa 80,24,0\nMvaa 200,104,40\n" \
"Ta 82,24,1024\nMvaa 200,136,56\nTa 176,24,8192\nMvaa 204,140,56\nD 13,64,s79,3072\nMvaa 204,108,40\nTa 178,24,9216\nTa 84,25,0\n" \
"Mvaa 208,112,40\nTa 86,25,1024\nMvaa 208,144,56\nv_add_u32 v13, 128, v13\nTa 180,25,8192\ns_branch 99f\n.p2align 12\nzp4:\n" \
"s_setpc_b64 s[98:99]\n99:\nMvaa 212,148,56\ns_add_u32 m0, s32, 81920\nMvaa 212,116,40\nD 14,68,0,0\nMvaa 216,120,40\nTa 182,25,9216\n" \
"Ta 88,26,0\nMvaa 216,152,56\nTa 90,26,1024\nMvaa 220,156,56\nTa 184,26,8192\nMvaa 220,124,40\nTa 186,26,9216\nMvaa 252,124,44\n" \
"D 14,68,s89,1024\nMvaa 252,156,60\nTa 92,27,0\nTa 94,27,1024\nMvaa 248,152,60\nTa 188,27,8192\nMvaa 248,120,44\nTa 190,27,9216\n" \
"Mvaa 244,116,44\nRa 4,16,2048\nMvaa 244,148,60\nD 28,68,s90,2048\nMvaa 240,144,60\nRa 20,17,2048\nRa 8,16,4096\nMvaa 240,112,44\n" \
"Ra 24,17,4096\nMvaa 236,108,44\nRa 12,16,6144\nMvaa 236,140,60\nRa 28,17,6144\nMvaa 232,136,60\nD 28,68,s91,3072\nMvaa 232,104,44\n" \
"Mvaa 228,100,44\nMvaa 228,132,60\nv_add_u32 v14, s72, v14\nMvaa 224,128,60\nv_add_u32 v28, s72, v28\nMvaa 224,96,44\ns_waitcnt lgkmcnt(6)\nMvaa 128,64,0\n" \
"Mvaa 128,160,16\ns_add_u32 m0, s88, 0\nMvaa 132,164,16\nD 13,64,0,0\nMvaa 132,68,0\nMvaa 136,72,0\nMvaa 136,168,16\nMvaa 140,172,16\n" \
"Mvaa 140,76,0\ns_waitcnt lgkmcnt(0)\nD 13,64,s73,1024\nMvaa 144,80,0\nMvaa 144,176,16\ns_add_u32 s95, s34, 4096\nMvaa 148,180,16\nbuffer_store_dwordx4 v[48:51], v0, s[60:63], s95 offen offset:0 sc1\n" \
"Mvaa 148,84,0\nMvaa 152,88,0\nD 13,64,s74,2048\nMvaa 152,184,16\nMvaa 156,188,16\nMvaa 156,92,0\nMvaa 188,92,4\ns_waitcnt vmcnt(17)\n" \
"s_barrier\nMvaa 188,188,20\nTa 96,20,32768\nTa 98,20,33792\nMvaa 184,184,20\nD 13,64,s75,3072\nMvaa 184,88,4\nTa 128,20,40960\n" \
"Mvaa 180,84,4\nTa 130,20,41984\nMvaa 180,180,20\nRa 32,16,32768\nRa 48,17,32768\nMvaa 176,176,20\ns_add_u32 m0, s88, 4096\nMvaa 176,80,4\n" \
"D 13,64,s76,0\nMvaa 172,76,4\nTa 100,21,32768\nMvaa 172,172,20\nTa 102,21,33792\nTa 132,21,40960\nMvaa 168,168,20\nTa 134,21,41984\n" \
"Mvaa 168,72,4\nTa 104,22,32768\nMvaa 164,68,4\nD 13,64,s77,1024\nMvaa 164,164,20\nTa 106,22,33792\nTa 136,22,40960\nMvaa 160,160,20\n" \
"Ta 138,22,41984\nMvaa 160,64,4\nTa 108,23,32768\nMvaa 192,64,8\nTa 110,23,33792\nMvaa 192,160,24\nD 13,64,s78,2048\nMvaa 196,164,24\n" \
"Ta 140,23,40960\nTa 142,23,41984\nMvaa 196,68,8\nTa 112,24,32768\nMvaa 200,72,8\nTa 114,24,33792\nMvaa 200,168,24\nTa 144,24,40960\n" \
"Mvaa 204,172,24\nD 13,64,s79,3072\nMvaa 204,76,8\nTa 146,24,41984\nTa 116,25,32768\nMvaa 208,80,8\nTa 118,25,33792\nMvaa 208,176,24\n" \
"v_add_u32 v13, 128, v13\nTa 148,25,40960\nMvaa 212,180,24\ns_add_u32 m0, s32, 65536\nMvaa 212,84,8\nD 14,68,0,0\nMvaa 216,88,8\nTa 150,25,41984\n" \
"Ta 120,26,32768\nMvaa 216,184,24\nTa 122,26,33792\nMvaa 220,188,24\nTa 152,26,40960\nMvaa 220,92,8\nTa 154,26,41984\nMvaa 252,92,12\n" \
"D 14,68,s89,1024\nMvaa 252,188,28\nTa 124,27,32768\nTa 126,27,33792\nMvaa 248,184,28\nTa 156,27,40960\nMvaa 248,88,12\nTa 158,27,41984\n" \
"Mvaa 244,84,12\nRa 36,16,34816\nMvaa 244,180,28\nD 28,68,s90,2048\nMvaa 240,176,28\nRa 52,17,34816\nRa 40,16,36864\nMvaa 240,80,12\n" \
"Ra 56,17,36864\nMvaa 236,76,12\nRa 44,16,38912\nMvaa 236,172,28\nRa 60,17,38912\nMvaa 232,168,28\nD 28,68,s91,3072\nMvaa 232,72,12\n" \
"Mvaa 228,68,12\nMvaa 228,164,28\nv_add_u32 v14, s72, v14\nMvaa 224,160,28\nv_add_u32 v28, s72, v28\nMvaa 224,64,12\ns_waitcnt lgkmcnt(6)\nMvaa 128,96,32\n" \
"Mvaa 128,128,48\ns_add_u32 m0, s88, 32768\nMvaa 132,132,48\nD 13,64,0,0\nMvaa 132,100,32\nMvaa 136,104,32\nMvaa 136,136,48\nMvaa 140,140,48\n" \
"Mvaa 140,108,32\ns_waitcnt lgkmcnt(0)\nD 13,64,s73,1024\nMvaa 144,112,32\nMvaa 144,144,48\nMvaa 148,148,48\nbuffer_store_dwordx4 v[52:55], v0, s[60:63], s95 offen offset:1024 sc1\nMvaa 148,116,32\n" \
"Mvaa 152,120,32\nD 13,64,s74,2048\nMvaa 152,152,48\nMvaa 156,156,48\nMvaa 156,124,32\nMvaa 188,124,36\ns_waitcnt vmcnt(17)\ns_barrier\n" \
"Mvaa 188,156,52\nTa 64,20,16384\nTa 66,20,17408\nMvaa 184,152,52\nD 13,64,s75,3072\nMvaa 184,120,36\nTa 160,20,24576\nMvaa 180,116,36\n" \
"Ta 162,20,25600\nMvaa 180,148,52\nRa 0,18,0\nRa 16,19,0\nMvaa 176,144,52\ns_add_u32 m0, s88, 36864\nMvaa 176,112,36\nD 13,64,s76,0\n" \
"Mvaa 172,108,36\nTa 68,21,16384\nMvaa 172,140,52\nTa 70,21,17408\nTa 164,21,24576\nMvaa 168,136,52\nTa 166,21,25600\nMvaa 168,104,36\n" \
"Ta 72,22,16384\nMvaa 164,100,36\nD 13,64,s77,1024\nMvaa 164,132,52\nTa 74,22,17408\nTa 168,22,24576\nMvaa 160,128,52\nTa 170,22,25600\n" \
"Mvaa 160,96,36\nTa 76,23,16384\nMvaa 192,96,40\nTa 78,23,17408\nMvaa 192,128,56\nD 13,64,s78,2048\nMvaa 196,132,56\nTa 172,23,24576\n" \
"Ta 174,23,25600\nMvaa 196,100,40\nTa 80,24,16384\nMvaa 200,104,40\nTa 82,24,17408\nMvaa 200,136,56\nTa 176,24,24576\nMvaa 204,140,56\n" \
"D 13,64,s79,3072\nMvaa 204,108,40\nTa 178,24,25600\nTa 84,25,16384\nMvaa 208,112,40\nTa 86,25,17408\nMvaa 208,144,56\nv_add_u32 v13, 128, v13\n" \
"Ta 180,25,24576\nMvaa 212,148,56\ns_add_u32 m0, s32, 98304\nMvaa 212,116,40\nD 14,68,0,0\nMvaa 216,120,40\nTa 182,25,25600\nTa 88,26,16384\n" \
"Mvaa 216,152,56\nTa 90,26,17408\nMvaa 220,156,56\nTa 184,26,24576\nMvaa 220,124,40\nTa 186,26,25600\nMvaa 252,124,44\nD 14,68,s89,1024\n" \
"Mvaa 252,156,60\nTa 92,27,16384\nTa 94,27,17408\nMvaa 248,152,60\nTa 188,27,24576\nMvaa 248,120,44\nTa 190,27,25600\nMvaa 244,116,44\n" \
"Ra 4,18,2048\nMvaa 244,148,60\nD 28,68,s90,2048\nMvaa 240,144,60\nRa 20,19,2048\nRa 8,18,4096\nMvaa 240,112,44\nRa 24,19,4096\n" \
"Mvaa 236,108,44\nRa 12,18,6144\nMvaa 236,140,60\nRa 28,19,6144\nMvaa 232,136,60\nD 28,68,s91,3072\nMvaa 232,104,44\nMvaa 228,100,44\n" \
"Mvaa 228,132,60\nv_add_u32 v14, s72, v14\nMvaa 224,128,60\nv_add_u32 v28, s72, v28\nMvaa 224,96,44\ns_waitcnt lgkmcnt(6)\nMvaa 128,64,0\nMvaa 128,160,16\n" \
"s_add_u32 m0, s88, 131072\nMvaa 132,164,16\nD 13,64,0,0\nMvaa 132,68,0\nMvaa 136,72,0\nMvaa 136,168,16\nMvaa 140,172,16\nMvaa 140,76,0\n" \
"s_waitcnt lgkmcnt(0)\nD 13,64,s73,1024\nMvaa 144,80,0\nMvaa 144,176,16\nMvaa 148,180,16\nbuffer_store_dwordx4 v[56:59], v0, s[60:63], s95 offen offset:2048 sc1\nMvaa 148,84,0\nMvaa 152,88,0\n" \
"D 13,64,s74,2048\nMvaa 152,184,16\nMvaa 156,188,16\nMvaa 156,92,0\nMvaa 188,92,4\ns_waitcnt vmcnt(17)\ns_barrier\nMvaa 188,188,20\n" \
"Ta 96,20,0\nTa 98,20,1024\nMvaa 184,184,20\nD 13,64,s75,3072\nMvaa 184,88,4\nTa 128,20,8192\nMvaa 180,84,4\nTa 130,20,9216\n" \
"Mvaa 180,180,20\nRa 32,16,0\nRa 48,17,0\nMvaa 176,176,20\ns_add_u32 m0, s88, 135168\nMvaa 176,80,4\nD 13,64,s76,0\nMvaa 172,76,4\n" \
"Ta 100,21,0\nMvaa 172,172,20\nTa 102,21,1024\nTa 132,21,8192\nMvaa 168,168,20\nTa 134,21,9216\nMvaa 168,72,4\nTa 104,22,0\n" \
"Mvaa 164,68,4\nD 13,64,s77,1024\nMvaa 164,164,20\nTa 106,22,1024\nTa 136,22,8192\nMvaa 160,160,20\nTa 138,22,9216\nMvaa 160,64,4\n" \
"Ta 108,23,0\nMvaa 192,64,8\nTa 110,23,1024\nMvaa 192,160,24\nD 13,64,s78,2048\nMvaa 196,164,24\nTa 140,23,8192\nTa 142,23,9216\n" \
"Mvaa 196,68,8\nTa 112,24,0\nMvaa 200,72,8\nTa 114,24,1024\nMvaa 200,168,24\nTa 144,24,8192\nMvaa 204,172,24\nD 13,64,s79,3072\n" \
"Mvaa 204,76,8\nTa 146,24,9216\nTa 116,25,0\nMvaa 208,80,8\nTa 118,25,1024\nMvaa 208,176,24\nv_add_u32 v13, 128, v13\nTa 148,25,8192\n" \
"Mvaa 212,180,24\ns_add_u32 m0, s32, 81920\nMvaa 212,84,8\nD 14,68,0,0\nMvaa 216,88,8\nTa 150,25,9216\nTa 120,26,0\nMvaa 216,184,24\n" \
"Ta 122,26,1024\nMvaa 220,188,24\nTa 152,26,8192\nMvaa 220,92,8\nTa 154,26,9216\nMvaa 252,92,12\nD 14,68,s89,1024\nMvaa 252,188,28\n" \
"Ta 124,27,0\nTa 126,27,1024\nMvaa 248,184,28\nTa 156,27,8192\nMvaa 248,88,12\nTa 158,27,9216\nMvaa 244,84,12\nRa 36,16,2048\n" \
"Mvaa 244,180,28\nD 28,68,s90,2048\nMvaa 240,176,28\nRa 52,17,2048\nRa 40,16,4096\nMvaa 240,80,12\nRa 56,17,4096\nMvaa 236,76,12\n" \
"Ra 44,16,6144\nMvaa 236,172,28\nRa 60,17,6144\nMvaa 232,168,28\nD 28,68,s91,3072\nMvaa 232,72,12\nMvaa 228,68,12\nMvaa 228,164,28\n" \
"v_add_u32 v14, s72, v14\nMvaa 224,160,28\nv_add_u32 v28, s72, v28\nMvaa 224,64,12\ns_waitcnt lgkmcnt(6)\nMvaa 128,96,32\nMvaa 128,128,48\ns_add_u32 m0, s88, 0\n" \
"Mvaa 132,132,48\nD 13,64,0,0\nMvaa 132,100,32\nMvaa 136,104,32\nMvaa 136,136,48\nMvaa 140,140,48\nMvaa 140,108,32\ns_waitcnt lgkmcnt(0)\n" \
"D 13,64,s73,1024\nMvaa 144,112,32\nMvaa 144,144,48\nMvaa 148,148,48\nbuffer_store_dwordx4 v[60:63], v0, s[60:63], s95 offen offset:3072 sc1\nMvaa 148,116,32\nMvaa 152,120,32\nD 13,64,s74,2048\n" \
"Mvaa 152,152,48\nMvaa 156,156,48\nMvaa 156,124,32\nMvaa 188,124,36\ns_waitcnt vmcnt(17)\ns_barrier\nMvaa 188,156,52\nTa 64,20,32768\n" \
"Ta 66,20,33792\nMvaa 184,152,52\nD 13,64,s75,3072\nMvaa 184,120,36\nTa 160,20,40960\nMvaa 180,116,36\nTa 162,20,41984\nMvaa 180,148,52\n" \
"Ra 0,16,32768\nRa 16,17,32768\nMvaa 176,144,52\ns_add_u32 m0, s88, 4096\nMvaa 176,112,36\nD 13,64,s76,0\nMvaa 172,108,36\nTa 68,21,32768\n" \
"Mvaa 172,140,52\nTa 70,21,33792\nTa 164,21,40960\nMvaa 168,136,52\nTa 166,21,41984\nMvaa 168,104,36\nTa 72,22,32768\nMvaa 164,100,36\n" \
"D 13,64,s77,1024\nMvaa 164,132,52\nTa 74,22,33792\nTa 168,22,40960\nMvaa 160,128,52\nTa 170,22,41984\nMvaa 160,96,36\nTa 76,23,32768\n" \
"Mvaa 192,96,40\nTa 78,23,33792\nMvaa 192,128,56\nD 13,64,s78,2048\nMvaa 196,132,56\nTa 172,23,40960\nTa 174,23,41984\nMvaa 196,100,40\n" \
"Ta 80,24,32768\nMvaa 200,104,40\nTa 82,24,33792\nMvaa 200,136,56\nTa 176,24,40960\nMvaa 204,140,56\nD 13,64,s79,3072\nMvaa 204,108,40\n" \
"Ta 178,24,41984\nTa 84,25,32768\nMvaa 208,112,40\nTa 86,25,33792\nMvaa 208,144,56\nv_add_u32 v13, 128, v13\nTa 180,25,40960\nMvaa 212,148,56\n" \
"s_add_u32 m0, s32, 65536\nMvaa 212,116,40\nD 14,68,0,0\nMvaa 216,120,40\nTa 182,25,41984\nTa 88,26,32768\nMvaa 216,152,56\nTa 90,26,33792\n" \
"Mvaa 220,156,56\nTa 184,26,40960\nMvaa 220,124,40\nTa 186,26,41984\nMvaa 252,124,44\nD 14,68,s89,1024\ns_branch 99f\n.p2align 12\n" \
"zp5:\ns_setpc_b64 s[98:99]\n99:\nMvaa 252,156,60\nTa 92,27,32768\nTa 94,27,33792\nMvaa 248,152,60\nTa 188,27,40960\n" \
"Mvaa 248,120,44\nTa 190,27,41984\nMvaa 244,116,44\nRa 4,16,34816\nMvaa 244,148,60\nD 28,68,s90,2048\nMvaa 240,144,60\nRa 20,17,34816\n" \
"Ra 8,16,36864\nMvaa 240,112,44\nRa 24,17,36864\nMvaa 236,108,44\nRa 12,16,38912\nMvaa 236,140,60\nRa 28,17,38912\nMvaa 232,136,60\n" \
"D 28,68,s91,3072\nMvaa 232,104,44\ns_add_u32 s95, s34, 8192\nMvaa 228,100,44\nbuffer_store_dwordx4 v[64:67], v0, s[60:63], s95 offen offset:0 sc1\nMvaa 228,132,60\nv_add_u32 v14, s72, v14\nMvaa 224,128,60\n" \
"v_add_u32 v28, s72, v28\nMvaa 224,96,44\ns_waitcnt lgkmcnt(6)\nMvaa 128,64,0\nMvaa 128,160,16\ns_add_u32 m0, s88, 32768\nMvaa 132,164,16\nD 13,64,0,0\n" \
"Mvaa 132,68,0\nMvaa 136,72,0\nMvaa 136,168,16\nMvaa 140,172,16\nMvaa 140,76,0\ns_waitcnt lgkmcnt(0)\nD 13,64,s73,1024\nMvaa 144,80,0\n" \
"Mvaa 144,176,16\nMvaa 148,180,16\nbuffer_store_dwordx4 v[68:71], v0, s[60:63], s95 offen offset:1024 sc1\nMvaa 148,84,0\nMvaa 152,88,0\nD 13,64,s74,2048\nMvaa 152,184,16\nMvaa 156,188,16\n" \
"Mvaa 156,92,0\nMvaa 188,92,4\ns_waitcnt vmcnt(18)\ns_barrier\nMvaa 188,188,20\nTa 96,20,16384\nTa 98,20,17408\nMvaa 184,184,20\n" \
"D 13,64,s75,3072\nMvaa 184,88,4\nTa 128,20,24576\nMvaa 180,84,4\nTa 130,20,25600\nMvaa 180,180,20\nRa 32,18,0\nRa 48,19,0\n" \
"Mvaa 176,176,20\ns_add_u32 m0, s88, 36864\nMvaa 176,80,4\nD 13,64,s76,0\nMvaa 172,76,4\nTa 100,21,16384\nMvaa 172,172,20\nTa 102,21,17408\n" \
"Ta 132,21,24576\nMvaa 168,168,20\nTa 134,21,25600\nMvaa 168,72,4\nTa 104,22,16384\nMvaa 164,68,4\nD 13,64,s77,1024\nMvaa 164,164,20\n" \
"Ta 106,22,17408\nTa 136,22,24576\nMvaa 160,160,20\nTa 138,22,25600\nMvaa 160,64,4\nTa 108,23,16384\nMvaa 192,64,8\nTa 110,23,17408\n" \
"Mvaa 192,160,24\nD 13,64,s78,2048\nMvaa 196,164,24\nTa 140,23,24576\nTa 142,23,25600\nMvaa 196,68,8\nTa 112,24,16384\nMvaa 200,72,8\n" \
"Ta 114,24,17408\nMvaa 200,168,24\nTa 144,24,24576\nMvaa 204,172,24\nD 13,64,s79,3072\nMvaa 204,76,8\nTa 146,24,25600\nTa 116,25,16384\n" \
"Mvaa 208,80,8\nTa 118,25,17408\nMvaa 208,176,24\nv_add_u32 v13, 128, v13\nTa 148,25,24576\nMvaa 212,180,24\ns_add_u32 m0, s32, 98304\nMvaa 212,84,8\n" \
"D 14,68,0,0\nMvaa 216,88,8\nTa 150,25,25600\nTa 120,26,16384\nMvaa 216,184,24\nTa 122,26,17408\nMvaa 220,188,24\nTa 152,26,24576\n" \
"Mvaa 220,92,8\nTa 154,26,25600\nMvaa 252,92,12\nD 14,68,s89,1024\nMvaa 252,188,28\nTa 124,27,16384\nTa 126,27,17408\nMvaa 248,184,28\n" \
"Ta 156,27,24576\nMvaa 248,88,12\nTa 158,27,25600\nMvaa 244,84,12\nRa 36,18,2048\nMvaa 244,180,28\nD 28,68,s90,2048\nMvaa 240,176,28\n" \
"Ra 52,19,2048\nRa 40,18,4096\nMvaa 240,80,12\nRa 56,19,4096\nMvaa 236,76,12\nRa 44,18,6144\nMvaa 236,172,28\nRa 60,19,6144\n" \
"Mvaa 232,168,28\nD 28,68,s91,3072\nMvaa 232,72,12\nMvaa 228,68,12\nMvaa 228,164,28\nv_add_u32 v14, s72, v14\nMvaa 224,160,28\nv_add_u32 v28, s72, v28\n" \
"Mvaa 224,64,12\ns_waitcnt lgkmcnt(6)\nMvaa 128,96,32\nMvaa 128,128,48\ns_add_u32 m0, s88, 131072\nMvaa 132,132,48\nD 13,64,0,0\nMvaa 132,100,32\n" \
"Mvaa 136,104,32\nMvaa 136,136,48\nMvaa 140,140,48\nMvaa 140,108,32\ns_waitcnt lgkmcnt(0)\nD 13,64,s73,1024\nMvaa 144,112,32\nMvaa 144,144,48\n" \
"Mvaa 148,148,48\nbuffer_store_dwordx4 v[72:75], v0, s[60:63], s95 offen offset:2048 sc1\nMvaa 148,116,32\nMvaa 152,120,32\nD 13,64,s74,2048\nMvaa 152,152,48\nMvaa 156,156,48\nMvaa 156,124,32\n" \
"Mvaa 188,124,36\ns_waitcnt vmcnt(18)\ns_barrier\nMvaa 188,156,52\nTa 64,20,0\nTa 66,20,1024\nMvaa 184,152,52\nD 13,64,s75,3072\n" \
"Mvaa 184,120,36\nTa 160,20,8192\nMvaa 180,116,36\nTa 162,20,9216\nMvaa 180,148,52\nRa 0,16,0\nRa 16,17,0\nMvaa 176,144,52\n" \
"s_add_u32 m0, s88, 135168\nMvaa 176,112,36\nD 13,64,s76,0\nMvaa 172,108,36\nTa 68,21,0\nMvaa 172,140,52\nTa 70,21,1024\nTa 164,21,8192\n" \
"Mvaa 168,136,52\nTa 166,21,9216\nMvaa 168,104,36\nTa 72,22,0\nMvaa 164,100,36\nD 13,64,s77,1024\nMvaa 164,132,52\nTa 74,22,1024\n" \
"Ta 168,22,8192\nMvaa 160,128,52\nTa 170,22,9216\nMvaa 160,96,36\nTa 76,23,0\nMvaa 192,96,40\nTa 78,23,1024\nMvaa 192,128,56\n" \
"D 13,64,s78,2048\nMvaa 196,132,56\nTa 172,23,8192\nTa 174,23,9216\nMvaa 196,100,40\nTa 80,24,0\nMvaa 200,104,40\nTa 82,24,1024\n" \
"Mvaa 200,136,56\nTa 176,24,8192\nMvaa 204,140,56\nD 13,64,s79,3072\nMvaa 204,108,40\nTa 178,24,9216\nTa 84,25,0\nMvaa 208,112,40\n" \
"Ta 86,25,1024\nMvaa 208,144,56\nv_add_u32 v13, 128, v13\nTa 180,25,8192\nMvaa 212,148,56\ns_add_u32 m0, s32, 81920\nMvaa 212,116,40\nD 14,68,0,0\n" \
"Mvaa 216,120,40\nTa 182,25,9216\nTa 88,26,0\nMvaa 216,152,56\nTa 90,26,1024\nMvaa 220,156,56\nTa 184,26,8192\nMvaa 220,124,40\n" \
"Ta 186,26,9216\nMvaa 252,124,44\nD 14,68,s89,1024\nMvaa 252,156,60\nTa 92,27,0\nTa 94,27,1024\nMvaa 248,152,60\nTa 188,27,8192\n" \
"Mvaa 248,120,44\nTa 190,27,9216\nMvaa 244,116,44\nRa 4,16,2048\nMvaa 244,148,60\nD 28,68,s90,2048\nMvaa 240,144,60\nRa 20,17,2048\n" \
"Ra 8,16,4096\nMvaa 240,112,44\nRa 24,17,4096\nMvaa 236,108,44\nRa 12,16,6144\nMvaa 236,140,60\nRa 28,17,6144\nMvaa 232,136,60\n" \
"D 28,68,s91,3072\nMvaa 232,104,44\nMvaa 228,100,44\nMvaa 228,132,60\nv_add_u32 v14, s72, v14\nMvaa 224,128,60\nv_add_u32 v28, s72, v28\nMvaa 224,96,44\n" \
"s_waitcnt lgkmcnt(6)\nMvaa 128,64,0\nMvaa 128,160,16\ns_add_u32 m0, s88, 0\nMvaa 132,164,16\nD 13,64,0,0\nMvaa 132,68,0\nMvaa 136,72,0\n" \
"Mvaa 136,168,16\nMvaa 140,172,16\nMvaa 140,76,0\ns_waitcnt lgkmcnt(0)\nD 13,64,s73,1024\nMvaa 144,80,0\nMvaa 144,176,16\nMvaa 148,180,16\n" \
"buffer_store_dwordx4 v[76:79], v0, s[60:63], s95 offen offset:3072 sc1\nMvaa 148,84,0\nMvaa 152,88,0\nD 13,64,s74,2048\nMvaa 152,184,16\nMvaa 156,188,16\nMvaa 156,92,0\nMvaa 188,92,4\n" \
"s_waitcnt vmcnt(17)\ns_barrier\nMvaa 188,188,20\nTa 96,20,32768\nTa 98,20,33792\nMvaa 184,184,20\nD 13,64,s75,3072\nMvaa 184,88,4\n" \
"Ta 128,20,40960\nMvaa 180,84,4\nTa 130,20,41984\nMvaa 180,180,20\nRa 32,16,32768\nRa 48,17,32768\nMvaa 176,176,20\ns_add_u32 m0, s88, 4096\n" \
"Mvaa 176,80,4\nD 13,64,s76,0\nMvaa 172,76,4\nTa 100,21,32768\nMvaa 172,172,20\nTa 102,21,33792\nTa 132,21,40960\nMvaa 168,168,20\n" \
"Ta 134,21,41984\nMvaa 168,72,4\nTa 104,22,32768\nMvaa 164,68,4\nD 13,64,s77,1024\nMvaa 164,164,20\nTa 106,22,33792\nTa 136,22,40960\n" \
"Mvaa 160,160,20\nTa 138,22,41984\nMvaa 160,64,4\nTa 108,23,32768\nMvaa 192,64,8\nTa 110,23,33792\nMvaa 192,160,24\nD 13,64,s78,2048\n" \
"Mvaa 196,164,24\nTa 140,23,40960\nTa 142,23,41984\nMvaa 196,68,8\nTa 112,24,32768\nMvaa 200,72,8\nTa 114,24,33792\nMvaa 200,168,24\n" \
"Ta 144,24,40960\nMvaa 204,172,24\nD 13,64,s79,3072\nMvaa 204,76,8\nTa 146,24,41984\nTa 116,25,32768\nMvaa 208,80,8\nTa 118,25,33792\n" \
"Mvaa 208,176,24\nv_add_u32 v13, 128, v13\nTa 148,25,40960\nMvaa 212,180,24\ns_add_u32 m0, s32, 65536\nMvaa 212,84,8\nD 14,68,0,0\nMvaa 216,88,8\n" \
"Ta 150,25,41984\nTa 120,26,32768\nMvaa 216,184,24\nTa 122,26,33792\nMvaa 220,188,24\nTa 152,26,40960\nMvaa 220,92,8\nTa 154,26,41984\n" \
"Mvaa 252,92,12\nD 14,68,s89,1024\nMvaa 252,188,28\nTa 124,27,32768\nTa 126,27,33792\nMvaa 248,184,28\nTa 156,27,40960\nMvaa 248,88,12\n" \
"Ta 158,27,41984\nMvaa 244,84,12\nRa 36,16,34816\nMvaa 244,180,28\nD 28,68,s90,2048\nMvaa 240,176,28\nRa 52,17,34816\nRa 40,16,36864\n" \
"Mvaa 240,80,12\nRa 56,17,36864\nMvaa 236,76,12\nRa 44,16,38912\nMvaa 236,172,28\nRa 60,17,38912\nMvaa 232,168,28\nD 28,68,s91,3072\n" \
"Mvaa 232,72,12\nMvaa 228,68,12\nMvaa 228,164,28\nv_add_u32 v14, s72, v14\nMvaa 224,160,28\nv_add_u32 v28, s72, v28\nMvaa 224,64,12\ns_waitcnt lgkmcnt(6)\n" \
"Mvaa 128,96,32\nMvaa 128,128,48\ns_add_u32 m0, s88, 32768\nMvaa 132,132,48\nD 13,64,0,0\nMvaa 132,100,32\nMvaa 136,104,32\nMvaa 136,136,48\n" \
"Mvaa 140,140,48\nMvaa 140,108,32\ns_waitcnt lgkmcnt(0)\nD 13,64,s73,1024\nMvaa 144,112,32\nMvaa 144,144,48\ns_add_u32 s95, s34, 12288\nMvaa 148,148,48\n" \
"buffer_store_dwordx4 v[80:83], v0, s[60:63], s95 offen offset:0 sc1\nMvaa 148,116,32\nMvaa 152,120,32\nD 13,64,s74,2048\nMvaa 152,152,48\nMvaa 156,156,48\nMvaa 156,124,32\nMvaa 188,124,36\n" \
"s_waitcnt vmcnt(17)\ns_barrier\nMvaa 188,156,52\nTa 64,20,16384\nTa 66,20,17408\nMvaa 184,152,52\nD 13,64,s75,3072\nMvaa 184,120,36\n" \
"Ta 160,20,24576\nMvaa 180,116,36\nTa 162,20,25600\nMvaa 180,148,52\nRa 0,18,0\nRa 16,19,0\nMvaa 176,144,52\ns_add_u32 m0, s88, 36864\n" \
"Mvaa 176,112,36\nD 13,64,s76,0\nMvaa 172,108,36\nTa 68,21,16384\nMvaa 172,140,52\nTa 70,21,17408\nTa 164,21,24576\nMvaa 168,136,52\n" \
"Ta 166,21,25600\nMvaa 168,104,36\nTa 72,22,16384\nMvaa 164,100,36\nD 13,64,s77,1024\nMvaa 164,132,52\nTa 74,22,17408\nTa 168,22,24576\n" \
"Mvaa 160,128,52\nTa 170,22,25600\nMvaa 160,96,36\nTa 76,23,16384\nMvaa 192,96,40\nTa 78,23,17408\nMvaa 192,128,56\nD 13,64,s78,2048\n" \
"Mvaa 196,132,56\nTa 172,23,24576\nTa 174,23,25600\nMvaa 196,100,40\nTa 80,24,16384\nMvaa 200,104,40\nTa 82,24,17408\nMvaa 200,136,56\n" \
"Ta 176,24,24576\nMvaa 204,140,56\nD 13,64,s79,3072\nMvaa 204,108,40\nTa 178,24,25600\nTa 84,25,16384\nMvaa 208,112,40\nTa 86,25,17408\n" \
"Mvaa 208,144,56\nv_add_u32 v13, 128, v13\nTa 180,25,24576\nMvaa 212,148,56\ns_add_u32 m0, s32, 98304\nMvaa 212,116,40\nD 14,68,0,0\nMvaa 216,120,40\n" \
"Ta 182,25,25600\nTa 88,26,16384\nMvaa 216,152,56\nTa 90,26,17408\nMvaa 220,156,56\nTa 184,26,24576\nMvaa 220,124,40\nTa 186,26,25600\n" \
"Mvaa 252,124,44\nD 14,68,s89,1024\nMvaa 252,156,60\nTa 92,27,16384\nTa 94,27,17408\nMvaa 248,152,60\nTa 188,27,24576\nMvaa 248,120,44\n" \
"Ta 190,27,25600\nMvaa 244,116,44\nRa 4,18,2048\nMvaa 244,148,60\nD 28,68,s90,2048\nMvaa 240,144,60\ns_branch 99f\n.p2align 12\n" \
"zp6:\ns_setpc_b64 s[98:99]\n99:\nRa 20,19,2048\nRa 8,18,4096\nMvaa 240,112,44\nRa 24,19,4096\nMvaa 236,108,44\n" \
"Ra 12,18,6144\nMvaa 236,140,60\nRa 28,19,6144\nMvaa 232,136,60\nD 28,68,s91,3072\nMvaa 232,104,44\nMvaa 228,100,44\nMvaa 228,132,60\n" \
"v_add_u32 v14, s72, v14\nMvaa 224,128,60\nv_add_u32 v28, s72, v28\nMvaa 224,96,44\ns_waitcnt lgkmcnt(6)\nMvaa 128,64,0\nMvaa 128,160,16\ns_add_u32 m0, s88, 131072\n" \
"Mvaa 132,164,16\nD 13,64,0,0\nMvaa 132,68,0\nMvaa 136,72,0\nMvaa 136,168,16\nMvaa 140,172,16\nMvaa 140,76,0\ns_waitcnt lgkmcnt(0)\n" \
"D 13,64,s73,1024\nMvaa 144,80,0\nMvaa 144,176,16\nMvaa 148,180,16\nbuffer_store_dwordx4 v[84:87], v0, s[60:63], s95 offen offset:1024 sc1\nMvaa 148,84,0\nMvaa 152,88,0\nD 13,64,s74,2048\n" \
"Mvaa 152,184,16\nMvaa 156,188,16\nMvaa 156,92,0\nMvaa 188,92,4\ns_waitcnt vmcnt(17)\ns_barrier\nMvaa 188,188,20\nTa 96,20,0\n" \
"Ta 98,20,1024\nMvaa 184,184,20\nD 13,64,s75,3072\nMvaa 184,88,4\nTa 128,20,8192\nMvaa 180,84,4\nTa 130,20,9216\nMvaa 180,180,20\n" \
"Ra 32,16,0\nRa 48,17,0\nMvaa 176,176,20\ns_add_u32 m0, s88, 135168\nMvaa 176,80,4\nD 13,64,s76,0\nMvaa 172,76,4\nTa 100,21,0\n" \
"Mvaa 172,172,20\nTa 102,21,1024\nTa 132,21,8192\nMvaa 168,168,20\nTa 134,21,9216\nMvaa 168,72,4\nTa 104,22,0\nMvaa 164,68,4\n" \
"D 13,64,s77,1024\nMvaa 164,164,20\nTa 106,22,1024\nTa 136,22,8192\nMvaa 160,160,20\nTa 138,22,9216\nMvaa 160,64,4\nTa 108,23,0\n" \
"Mvaa 192,64,8\nTa 110,23,1024\nMvaa 192,160,24\nD 13,64,s78,2048\nMvaa 196,164,24\nTa 140,23,8192\nTa 142,23,9216\nMvaa 196,68,8\n" \
"Ta 112,24,0\nMvaa 200,72,8\nTa 114,24,1024\nMvaa 200,168,24\nTa 144,24,8192\nMvaa 204,172,24\nD 13,64,s79,3072\nMvaa 204,76,8\n" \
"Ta 146,24,9216\nTa 116,25,0\nMvaa 208,80,8\nTa 118,25,1024\nMvaa 208,176,24\nv_add_u32 v13, 128, v13\nTa 148,25,8192\nMvaa 212,180,24\n" \
"s_add_u32 m0, s32, 81920\nMvaa 212,84,8\nD 14,68,0,0\nMvaa 216,88,8\nTa 150,25,9216\nTa 120,26,0\nMvaa 216,184,24\nTa 122,26,1024\n" \
"Mvaa 220,188,24\nTa 152,26,8192\nMvaa 220,92,8\nTa 154,26,9216\nMvaa 252,92,12\nD 14,68,s89,1024\nMvaa 252,188,28\nTa 124,27,0\n" \
"Ta 126,27,1024\nMvaa 248,184,28\nTa 156,27,8192\nMvaa 248,88,12\nTa 158,27,9216\nMvaa 244,84,12\nRa 36,16,2048\nMvaa 244,180,28\n" \
"D 28,68,s90,2048\nMvaa 240,176,28\nRa 52,17,2048\nRa 40,16,4096\nMvaa 240,80,12\nRa 56,17,4096\nMvaa 236,76,12\nRa 44,16,6144\n" \
"Mvaa 236,172,28\nRa 60,17,6144\nMvaa 232,168,28\nD 28,68,s91,3072\nMvaa 232,72,12\nMvaa 228,68,12\nMvaa 228,164,28\nv_add_u32 v14, s72, v14\n" \
"Mvaa 224,160,28\nv_add_u32 v28, s72, v28\nMvaa 224,64,12\ns_waitcnt lgkmcnt(6)\nMvaa 128,96,32\nMvaa 128,128,48\nbuffer_load_dwordx2 v[10:11], v1, s[60:63], 0 offen offset:0 sc1\nMvaa 132,132,48\n" \
"v_subrev_u32 v9, s32, v0\nv_add_u32 v8, 0x1004200, v0\nMvaa 132,100,32\ns_add_u32 s77, s38, s32\nv_mov_b32 v12, 0xc2200000\nMvaa 136,104,32\nv_mov_b32 v13, 0xc2200000\nv_mov_b32 v14, 0xc2200000\n" \
"Mvaa 136,136,48\nv_mov_b32 v15, 0xc2200000\nv_bfe_u32 v29, v0, 4, 4\nMvaa 140,140,48\nv_lshrrev_b32 v28, 8, v0\ns_lshr_b32 s96, s32, 8\nMvaa 140,108,32\ns_waitcnt lgkmcnt(0)\n" \
"v_lshlrev_b32 v6, 2, v28\nMvaa 144,112,32\nv_add_u32 v30, s96, v29\nv_sub_u32 v30, v30, v6\nMvaa 144,144,48\nv_mov_b32 v6, 0xff800000\nv_cmp_gt_i32 s[90:91], 0, v30\nMvaa 148,148,48\n" \
"buffer_store_dwordx4 v[88:91], v0, s[60:63], s95 offen offset:2048 sc1\nMvaa 148,116,32\nv_cmp_gt_i32 s[92:93], 1, v30\nv_cmp_gt_i32 s[74:75], 2, v30\nMvaa 152,120,32\nv_cmp_gt_i32 s[78:79], 3, v30\nv_cndmask_b32 v112, v12, v6, s[90:91]\nMvaa 152,152,48\n" \
"v_cndmask_b32 v113, v12, v6, s[92:93]\nv_cndmask_b32 v114, v12, v6, s[74:75]\nMvaa 156,156,48\nv_cndmask_b32 v115, v12, v6, s[78:79]\nv_cmp_gt_i32 s[90:91], 16, v30\nMvaa 156,124,32\nv_cmp_gt_i32 s[92:93], 17, v30\nv_cmp_gt_i32 s[74:75], 18, v30\n" \
"Mvaa 188,124,36\ns_waitcnt vmcnt(15)\ns_barrier\nMvaa 188,156,52\nTa 64,20,32768\nv_cmp_gt_i32 s[78:79], 19, v30\nMvaa 184,152,52\nTa 66,20,33792\n" \
"v_cndmask_b32 v116, v12, v6, s[90:91]\nMvaa 184,120,36\nTa 160,20,40960\nv_cndmask_b32 v117, v12, v6, s[92:93]\nMvaa 180,116,36\nTa 162,20,41984\nv_cndmask_b32 v118, v12, v6, s[74:75]\nMvaa 180,148,52\n" \
"Ra 0,16,32768\nv_cndmask_b32 v119, v12, v6, s[78:79]\nMvaa 176,144,52\nRa 16,17,32768\nv_cmp_gt_i32 s[90:91], 32, v30\nMvaa 176,112,36\nTa 68,21,32768\nv_cmp_gt_i32 s[92:93], 33, v30\n" \
"Mvaa 172,108,36\nTa 70,21,33792\nv_cmp_gt_i32 s[74:75], 34, v30\nMvaa 172,140,52\nTa 164,21,40960\nv_cmp_gt_i32 s[78:79], 35, v30\nMvaa 168,136,52\nTa 166,21,41984\n" \
"v_cndmask_b32 v120, v12, v6, s[90:91]\nMvaa 168,104,36\nTa 72,22,32768\nv_cndmask_b32 v121, v12, v6, s[92:93]\nMvaa 164,100,36\nTa 74,22,33792\nv_cndmask_b32 v122, v12, v6, s[74:75]\nMvaa 164,132,52\n" \
"Ta 168,22,40960\nv_cndmask_b32 v123, v12, v6, s[78:79]\nMvaa 160,128,52\nTa 170,22,41984\nv_cmp_gt_i32 s[90:91], 48, v30\nMvaa 160,96,36\nTa 76,23,32768\nv_cmp_gt_i32 s[92:93], 49, v30\n" \
"Mvaa 192,96,40\nTa 78,23,33792\nv_cmp_gt_i32 s[74:75], 50, v30\nMvaa 192,128,56\nTa 172,23,40960\nv_cmp_gt_i32 s[78:79], 51, v30\nMvaa 196,132,56\nTa 174,23,41984\n" \
"v_cndmask_b32 v124, v12, v6, s[90:91]\nMvaa 196,100,40\nTa 80,24,32768\nv_cndmask_b32 v125, v12, v6, s[92:93]\nMvaa 200,104,40\nTa 82,24,33792\nv_cndmask_b32 v126, v12, v6, s[74:75]\nMvaa 200,136,56\n" \
"v_cndmask_b32 v127, v12, v6, s[78:79]\nMvaa 204,140,56\nTa 176,24,40960\nMvaa 204,108,40\nTa 178,24,41984\nMvaa 208,112,40\nTa 84,25,32768\nMvaa 208,144,56\n" \
"Ta 86,25,33792\nMvaa 212,148,56\nTa 180,25,40960\nMvaa 212,116,40\nTa 182,25,41984\nMvaa 216,120,40\nTa 88,26,32768\nMvaa 216,152,56\n" \
"Ta 90,26,33792\nMvaa 220,156,56\nTa 184,26,40960\nMvaa 220,124,40\nTa 186,26,41984\nMvaa 252,124,44\nTa 92,27,32768\nMvaa 252,156,60\n" \
"Ta 94,27,33792\nMvaa 248,152,60\nTa 188,27,40960\nMvaa 248,120,44\nTa 190,27,41984\nMvaa 244,116,44\nRa 4,16,34816\nMvaa 244,148,60\n" \
"Ra 20,17,34816\nMvaa 240,144,60\nRa 8,16,36864\nMvaa 240,112,44\nRa 24,17,36864\nMvaa 236,108,44\nRa 12,16,38912\nMvaa 236,140,60\n" \
"Ra 28,17,38912\nMvaa 232,136,60\nMvaa 232,104,44\nMvaa 228,100,44\nMvaa 228,132,60\nMvaa 224,128,60\nMvaa 224,96,44\ns_waitcnt lgkmcnt(6)\n" \
"Mvaa 128,64,0\nv_and_b32 v30, 3, v29\nv_bfe_u32 v6, v29, 1, 1\nMvaa 128,160,16\nv_lshrrev_b32 v28, 1, v28\nv_cmp_eq_u32 s[90:91], 2, v30\nMvaa 132,164,16\nv_cmp_eq_u32 s[92:93], v6, v28\n" \
"v_and_b32 v30, 1, v29\nMvaa 132,68,0\nv_mov_b32 v6, 0x3f803f80\nv_cmp_eq_u32 s[78:79], 1, v30\nMvaa 136,72,0\nv_mov_b32 v7, 0x4444\nv_mov_b32 v30, 0xeeee\nMvaa 136,168,16\n" \
"v_cndmask_b32 v108, 0, v6, s[90:91]\nv_cndmask_b32 v109, 0, v6, s[90:91]\nMvaa 140,172,16\nv_cndmask_b32 v110, 0, v6, s[90:91]\nv_cndmask_b32 v111, 0, v6, s[90:91]\nMvaa 140,76,0\ns_waitcnt lgkmcnt(0)\nv_cndmask_b32 v104, 0, v6, s[92:93]\n" \
"Mvaa 144,80,0\nv_cndmask_b32 v105, 0, v6, s[92:93]\nv_cndmask_b32 v106, 0, v6, s[92:93]\nMvaa 144,176,16\nv_cndmask_b32 v107, 0, v6, s[92:93]\nv_cndmask_b32 v7, v7, v30, s[78:79]\nMvaa 148,180,16\nbuffer_store_dwordx4 v[92:95], v0, s[60:63], s95 offen offset:3072 sc1\n" \
"Mvaa 148,84,0\nv_mov_b32 v96, 0\nv_mov_b32 v97, 0\nMvaa 152,88,0\nv_mov_b32 v98, 0\nv_mov_b32 v99, 0\nMvaa 152,184,16\nv_mov_b32 v100, 0\n" \
"v_mov_b32 v101, 0\nMvaa 156,188,16\nv_mov_b32 v102, 0\nv_mov_b32 v103, 0\nMvaa 156,92,0\nv_mov_b32 v5, v0\ns_mov_b32 s73, s32\nMvaa 188,92,4\n" \
"s_waitcnt vmcnt(2)\ns_barrier\nv_cmp_ne_u64 vcc, v[10:11], v[2:3]\ns_cbranch_vccnz zs101\nzr101:\nMvaa 188,188,20\ns_mov_b32 m0, s32\nTa 96,20,16384\n" \
"Mvaa 184,184,20\nDS 0,60,s77,0\nMvaa 184,88,4\nTa 98,20,17408\nTa 128,20,24576\nMvaa 180,84,4\nTa 130,20,25600\ns_add_u32 s74, s32, 0x4000\n" \
"Mvaa 180,180,20\nRa 32,18,0\ns_add_u32 s75, s32, 0x8000\nMvaa 176,176,20\nDS 0,60,s77,1024\nMvaa 176,80,4\nRa 48,19,0\nTa 100,21,16384\n" \
"Mvaa 172,76,4\nTa 102,21,17408\ns_mov_b32 s97, 0x3e0293ee\nMvaa 172,172,20\nTa 132,21,24576\nMvaa 168,168,20\nDS 0,60,s77,2048\nMvaa 168,72,4\n" \
"Ta 134,21,25600\nTa 104,22,16384\nMvaa 164,68,4\nTa 106,22,17408\nMvaa 164,164,20\nTa 136,22,24576\nTa 138,22,25600\nMvaa 160,160,20\n" \
"DS 0,60,s77,3072\nMvaa 160,64,4\nTa 108,23,16384\nMvaa 192,64,8\nTa 110,23,17408\nMvaa 192,160,24\ns_add_u32 m0, s32, 16384\ns_add_u32 s77, s77, 0x4000\n" \
"Mvaa 196,164,24\nDS 0,60,s77,0\nMvaa 196,68,8\nTa 140,23,24576\nTa 142,23,25600\nMvaa 200,72,8\nTa 112,24,16384\nMvaa 200,168,24\n" \
"Ta 114,24,17408\nMvaa 204,172,24\nDS 0,60,s77,1024\nMvaa 204,76,8\nTa 144,24,24576\nTa 146,24,25600\nMvaa 208,80,8\nTa 116,25,16384\n" \
"Mvaa 208,176,24\nTa 118,25,17408\nTa 148,25,24576\nMvaa 212,180,24\nDS 0,60,s77,2048\nMvaa 212,84,8\nTa 150,25,25600\nMvaa 216,88,8\n" \
"Ta 120,26,16384\nMvaa 216,184,24\nTa 122,26,17408\nTa 152,26,24576\nMvaa 220,188,24\nDS 0,60,s77,3072\nMvaa 220,92,8\nTa 154,26,25600\n" \
"Mvaa 252,92,12\nTa 124,27,16384\nTa 126,27,17408\nMvaa 252,188,28\ns_add_u32 m0, s32, 49152\nMvaa 248,184,28\nDS 8,60,s77,0\nMvaa 248,88,12\n" \
"Ta 156,27,24576\nTa 158,27,25600\nMvaa 244,84,12\nRa 36,18,2048\nMvaa 244,180,28\nRa 52,19,2048\nMvaa 240,176,28\nDS 8,60,s77,1024\n" \
"Mvaa 240,80,12\nRa 40,18,4096\nRa 56,19,4096\nMvaa 236,76,12\nRa 44,18,6144\nMvaa 236,172,28\nRa 60,19,6144\nMvaa 232,168,28\n" \
"DS 8,60,s77,2048\nMvaa 232,72,12\nMvaa 228,68,12\nMvaa 228,164,28\nMvaa 224,160,28\nDS 8,60,s77,3072\nMvaa 224,64,12\ns_waitcnt lgkmcnt(6)\n" \
"Mvaa 128,96,32\ns_add_u32 m0, s32, 32768\ns_add_u32 s77, s77, 0x4000\nMvaa 128,128,48\nDS 0,60,s77,0\nMvaa 132,132,48\nMvaa 132,100,32\nDS 0,60,s77,1024\n" \
"Mvaa 136,104,32\nMvaa 136,136,48\nDS 0,60,s77,2048\nMvaa 140,140,48\nMvaa 140,108,32\ns_waitcnt lgkmcnt(0)\nDS 0,60,s77,3072\nMvaa 144,112,32\n" \
"s_add_u32 m0, s32, 65536\nMvaa 144,144,48\nDS 8,60,s77,0\nMvaa 148,148,48\nMvaa 148,116,32\nDS 8,60,s77,1024\nMvaa 152,120,32\nMvaa 152,152,48\n" \
"DS 8,60,s77,2048\nMvaa 156,156,48\nMvaa 156,124,32\nDS 8,60,s77,3072\nv_lshlrev_b32 v29, 2, v0\nMvaa 160,96,36\ns_add_u32 s77, s77, 0x4000\nMvaa 160,128,52\n" \
"Mvaa 164,132,52\nMvaa 164,100,36\nbuffer_load_dword v30, v29, s[64:67], s46 offen sc1\nMvaa 168,104,36\nMvaa 168,136,52\nv_mul_f32 v128, s97, v128\nv_mul_f32 v129, s97, v129\nv_mul_f32 v130, s97, v130\n" \
"Mvaa 172,140,52\nv_mul_f32 v131, s97, v131\nv_mul_f32 v132, s97, v132\nv_mul_f32 v133, s97, v133\nMvaa 172,108,36\nv_mul_f32 v134, s97, v134\nv_mul_f32 v135, s97, v135\nC 32,128,129\n" \
"Mvaa 176,112,36\nC 33,130,131\nv_mul_f32 v136, s97, v136\nv_mul_f32 v137, s97, v137\nMvaa 176,144,52\nv_mul_f32 v138, s97, v138\nv_mul_f32 v139, s97, v139\nC 34,132,133\n" \
"Mvaa 180,148,52\nC 35,134,135\nv_mul_f32 v140, s97, v140\nv_mul_f32 v141, s97, v141\nMvaa 180,116,36\nv_mul_f32 v142, s97, v142\nv_mul_f32 v143, s97, v143\nC 36,136,137\n" \
"Mvaa 184,120,36\nC 37,138,139\nv_mul_f32 v144, s97, v144\nv_mul_f32 v145, s97, v145\nMvaa 184,152,52\nv_mul_f32 v146, s97, v146\nv_mul_f32 v147, s97, v147\nC 38,140,141\n" \
"Mvaa 188,156,52\nC 39,142,143\nv_mul_f32 v148, s97, v148\nv_mul_f32 v149, s97, v149\nMvaa 188,124,36\nv_mul_f32 v150, s97, v150\nv_mul_f32 v151, s97, v151\nC 40,144,145\n" \
"Mvaa 192,96,40\nC 41,146,147\nv_mul_f32 v152, s97, v152\nv_mul_f32 v153, s97, v153\nMvaa 192,128,56\ns_branch 99f\n.p2align 12\nzp7:\n" \
"s_setpc_b64 s[98:99]\n99:\nv_mul_f32 v154, s97, v154\nv_mul_f32 v155, s97, v155\nC 42,148,149\nMvaa 196,132,56\nC 43,150,151\nv_mul_f32 v156, s97, v156\n" \
"v_mul_f32 v157, s97, v157\nMvaa 196,100,40\nv_mul_f32 v158, s97, v158\nv_mul_f32 v159, s97, v159\nC 44,152,153\nMvaa 200,104,40\nC 45,154,155\nC 46,156,157\n" \
"C 47,158,159\nMvaa 200,136,56\nv_mul_f32 v160, s97, v160\nv_mul_f32 v161, s97, v161\nv_mul_f32 v162, s97, v162\nMvaa 204,140,56\nv_mul_f32 v163, s97, v163\nv_mul_f32 v164, s97, v164\n" \
"v_mul_f32 v165, s97, v165\nMvaa 204,108,40\nv_mul_f32 v166, s97, v166\nv_mul_f32 v167, s97, v167\nC 48,160,161\nMvaa 208,112,40\ns_waitcnt vmcnt(17)\ns_barrier\n" \
"C 49,162,163\nMvaa 208,144,56\nRa 160,5,0\nv_mul_f32 v168, s97, v168\nv_mul_f32 v169, s97, v169\nMvaa 212,148,56\nv_mul_f32 v170, s97, v170\nv_mul_f32 v171, s97, v171\n" \
"C 50,164,165\nMvaa 212,116,40\nRa 164,5,1024\nC 51,166,167\nv_mul_f32 v172, s97, v172\nMvaa 216,120,40\nv_mul_f32 v173, s97, v173\nv_mul_f32 v174, s97, v174\n" \
"v_mul_f32 v175, s97, v175\nMvaa 216,152,56\nRa 168,5,2048\nC 52,168,169\nC 53,170,171\nMvaa 220,156,56\nv_mul_f32 v176, s97, v176\nv_mul_f32 v177, s97, v177\n" \
"v_mul_f32 v178, s97, v178\nMvaa 220,124,40\nRa 172,5,3072\nv_mul_f32 v179, s97, v179\nC 54,172,173\nMvaa 224,96,44\nC 55,174,175\nv_mul_f32 v180, s97, v180\n" \
"v_mul_f32 v181, s97, v181\nMvaa 224,128,60\nRa 176,5,4096\nv_mul_f32 v182, s97, v182\nv_mul_f32 v183, s97, v183\nMvaa 228,132,60\nC 56,176,177\nC 57,178,179\n" \
"v_mul_f32 v184, s97, v184\nMvaa 228,100,44\nRa 180,5,5120\nv_mul_f32 v185, s97, v185\nv_mul_f32 v186, s97, v186\nMvaa 232,104,44\nv_mul_f32 v187, s97, v187\nC 58,180,181\n" \
"C 59,182,183\nMvaa 232,136,60\nRa 184,5,6144\nv_mul_f32 v188, s97, v188\nv_mul_f32 v189, s97, v189\nMvaa 236,140,60\nv_mul_f32 v190, s97, v190\nv_mul_f32 v191, s97, v191\n" \
"C 60,184,185\nMvaa 236,108,44\nRa 188,5,7168\nC 61,186,187\nC 62,188,189\nMvaa 240,112,44\nC 63,190,191\nv_mul_f32 v192, s97, v192\n" \
"v_mul_f32 v193, s97, v193\nMvaa 240,144,60\nv_mul_f32 v194, s97, v194\nv_mul_f32 v195, s97, v195\nv_mul_f32 v196, s97, v196\nMvaa 244,148,60\nv_mul_f32 v197, s97, v197\nv_mul_f32 v198, s97, v198\n" \
"v_mul_f32 v199, s97, v199\nMvaa 244,116,44\nC 64,192,193\nC 65,194,195\nv_mul_f32 v200, s97, v200\nMvaa 248,120,44\ns_bitcmp1_b32 s33, 2\nv_mul_f32 v201, s97, v201\n" \
"v_mul_f32 v202, s97, v202\nMvaa 248,152,60\nv_mul_f32 v203, s97, v203\nC 66,196,197\nC 67,198,199\nMvaa 252,156,60\nMvaa 252,124,44\ns_waitcnt lgkmcnt(0)\n" \
"Qvav 128,160,32,12\nRa 128,5,8192\nv_mul_f32 v204, s97, v204\nv_mul_f32 v205, s97, v205\nMvav 128,164,36\nv_mul_f32 v206, s97, v206\nv_mul_f32 v207, s97, v207\nC 68,200,201\n" \
"Mvav 128,168,40\nRa 132,5,9216\nC 69,202,203\nv_mul_f32 v208, s97, v208\nMvav 128,172,44\nv_mul_f32 v209, s97, v209\nv_mul_f32 v210, s97, v210\nv_mul_f32 v211, s97, v211\n" \
"Qvav 132,176,32,12\nRa 136,5,10240\nC 70,204,205\nC 71,206,207\nMvav 132,180,36\nv_mul_f32 v212, s97, v212\nv_mul_f32 v213, s97, v213\nv_mul_f32 v214, s97, v214\n" \
"Mvav 132,184,40\nRa 140,5,11264\nv_mul_f32 v215, s97, v215\nC 72,208,209\nMvav 132,188,44\nC 73,210,211\nv_mul_f32 v216, s97, v216\nv_mul_f32 v217, s97, v217\n" \
"Qvav 136,160,48,12\nRa 144,5,12288\nv_mul_f32 v218, s97, v218\nv_mul_f32 v219, s97, v219\nMvav 136,164,52\nC 74,212,213\nC 75,214,215\nv_mul_f32 v220, s97, v220\n" \
"Mvav 136,168,56\nRa 148,5,13312\nv_mul_f32 v221, s97, v221\nv_mul_f32 v222, s97, v222\nMvav 136,172,60\nv_mul_f32 v223, s97, v223\nC 76,216,217\nC 77,218,219\n" \
"Qvav 140,176,48,12\nRa 152,5,14336\nC 78,220,221\nC 79,222,223\nMvav 140,180,52\nv_mul_f32 v224, s97, v224\nv_mul_f32 v225, s97, v225\nv_mul_f32 v226, s97, v226\n" \
"Mvav 140,184,56\nRa 156,5,15360\nv_mul_f32 v227, s97, v227\nv_mul_f32 v228, s97, v228\nMvav 140,188,60\nv_mul_f32 v229, s97, v229\nv_mul_f32 v230, s97, v230\nv_mul_f32 v231, s97, v231\n" \
"Qvav 144,160,64,12\nC 80,224,225\nC 81,226,227\nv_mul_f32 v232, s97, v232\nMvav 144,164,68\nv_mul_f32 v233, s97, v233\nv_mul_f32 v234, s97, v234\nv_mul_f32 v235, s97, v235\n" \
"Mvav 144,168,72\nC 82,228,229\nC 83,230,231\nv_mul_f32 v236, s97, v236\nMvav 144,172,76\nv_mul_f32 v237, s97, v237\nv_mul_f32 v238, s97, v238\nv_mul_f32 v239, s97, v239\n" \
"Qvav 148,176,64,12\nC 84,232,233\nC 85,234,235\nv_mul_f32 v240, s97, v240\nMvav 148,180,68\nv_mul_f32 v241, s97, v241\nv_mul_f32 v242, s97, v242\nv_mul_f32 v243, s97, v243\n" \
"C 86,236,237\nC 87,238,239\nv_mul_f32 v244, s97, v244\nv_mul_f32 v245, s97, v245\nv_mul_f32 v246, s97, v246\nv_mul_f32 v247, s97, v247\nC 88,240,241\nC 89,242,243\n" \
"v_mul_f32 v248, s97, v248\nv_mul_f32 v249, s97, v249\nv_mul_f32 v250, s97, v250\nv_mul_f32 v251, s97, v251\nC 90,244,245\nC 91,246,247\nv_mul_f32 v252, s97, v252\nv_mul_f32 v253, s97, v253\n" \
"v_mul_f32 v254, s97, v254\nv_mul_f32 v255, s97, v255\nC 92,248,249\nC 93,250,251\nC 94,252,253\nC 95,254,255\nMvav 148,184,72\nE 240,128\n" \
"Mvav 148,188,76\nE 241,129\nQvav 152,160,80,12\nC 192,240,241\nE 242,130\nMvav 152,164,84\nE 243,131\nMvav 152,168,88\n" \
"C 193,242,243\nMvav 152,172,92\nE 244,132\nQvav 156,176,80,12\nE 245,133\nMvav 156,180,84\nC 194,244,245\nE 246,134\n" \
"Mvav 156,184,88\nE 247,135\nMvav 156,188,92\ns_waitcnt lgkmcnt(0)\nC 195,246,247\nQvav 160,128,32,12\nE 240,136\nMvav 160,132,36\n" \
"E 241,137\nMvav 160,136,40\nC 196,240,241\nE 242,138\nMvav 160,140,44\nQvav 168,128,48,12\nv_mov_b32 v6, v5\nv_add_u32 v5, s74, v9\n" \
"E 243,139\nMvav 168,132,52\nC 197,242,243\nE 244,140\nMvav 168,136,56\nE 245,141\nMvav 168,140,60\nC 198,244,245\n" \
"E 246,142\nQvav 176,128,64,12\ns_waitcnt vmcnt(9)\ns_barrier\nMvav 176,132,68\nRa 160,5,0\nE 247,143\nMvav 176,136,72\n" \
"s_mov_b32 m0, s73\nC 199,246,247\nE 240,144\nMvav 176,140,76\nRa 164,5,1024\nDS 0,60,s77,0\nE 241,145\nQvav 184,128,80,12\n" \
"C 200,240,241\nMvav 184,132,84\nRa 168,5,2048\nE 242,146\nMvav 184,136,88\nE 243,147\nMvav 184,140,92\nRa 172,5,3072\n" \
"C 201,242,243\nE 244,148\nQvav 164,144,32,12\nDS 0,60,s77,1024\nE 245,149\nMvav 164,148,36\nRa 176,5,4096\nC 202,244,245\n" \
"Mvav 164,152,40\nE 246,150\nMvav 164,156,44\nRa 180,5,5120\nE 247,151\nQvav 172,144,48,12\nC 203,246,247\nE 240,152\n" \
"Mvav 172,148,52\nRa 184,5,6144\nDS 0,60,s77,2048\nMvav 172,152,56\nE 241,153\nMvav 172,156,60\nRa 188,5,7168\nC 204,240,241\n" \
"E 242,154\nQvav 180,144,64,12\nE 243,155\nMvav 180,148,68\nC 205,242,243\nE 244,156\nMvav 180,152,72\nDS 0,60,s77,3072\n" \
"Mvav 180,156,76\nE 245,157\nQvav 188,144,80,12\nC 206,244,245\nE 246,158\nMvav 188,148,84\nE 247,159\nMvav 188,152,88\n" \
"C 207,246,247\ns_mov_b32 s96, s39\nMvav 188,156,92\ns_waitcnt lgkmcnt(0)\nQvav 128,160,32,12\nE 240,160\nMvav 128,164,36\nE 241,161\n" \
"Mvav 128,168,40\nC 208,240,241\nRa 128,6,49152\nMvav 128,172,44\nE 242,162\ns_add_u32 m0, s75, 0xc000\nQvav 136,160,48,12\nDS 8,60,s77,0\n" \
"Mvav 136,164,52\nE 243,163\nMvav 136,168,56\nC 209,242,243\nRa 132,6,50176\nMvav 136,172,60\nE 244,164\nQvav 144,160,64,12\n" \
"DS 8,60,s77,1024\nMvav 144,164,68\nE 245,165\nMvav 144,168,72\nC 210,244,245\nRa 136,6,51200\nMvav 144,172,76\nE 246,166\n" \
"s_add_u32 s95, s34, 16384\nQvav 152,160,80,12\nbuffer_store_dwordx4 a[192:195], v0, s[60:63], s95 offen offset:0 sc1\nMvav 152,164,84\nE 247,167\nMvav 152,168,88\nC 211,246,247\nRa 140,6,52224\n" \
"Mvav 152,172,92\nE 240,168\nQvav 132,176,32,12\nDS 8,60,s77,2048\nMvav 132,180,36\nE 241,169\nMvav 132,184,40\nC 212,240,241\n" \
"Ra 144,6,53248\nMvav 132,188,44\nE 242,170\nQvav 140,176,48,12\nbuffer_store_dwordx4 a[196:199], v0, s[60:63], s95 offen offset:1024 sc1\nMvav 140,180,52\nE 243,171\nMvav 140,184,56\n" \
"C 213,242,243\nRa 148,6,54272\nMvav 140,188,60\nE 244,172\nQvav 148,176,64,12\nDS 8,60,s77,3072\nMvav 148,180,68\nE 245,173\n" \
"Mvav 148,184,72\nC 214,244,245\nRa 152,6,55296\nMvav 148,188,76\nE 246,174\nQvav 156,176,80,12\nRa 156,6,56320\ns_mov_b32 s76, s73\n" \
"Mvav 156,180,84\nE 247,175\nMvav 156,184,88\nC 215,246,247\ns_mov_b32 s73, s74\nMvav 156,188,92\nE 240,176\nv_smfmac_f32_16x16x64_bf16 v[96:99], v[104:107], v[192:199], v7\n" \
"s_mov_b32 s74, s75\nv_smfmac_f32_16x16x64_bf16 v[100:103], v[104:107], v[200:207], v7\ns_waitcnt lgkmcnt(0)\nZaav 96,128,204\nbuffer_store_dwordx4 a[200:203], v0, s[60:63], s95 offen offset:2048 sc1\nZaav 100,132,204\nE 241,177\nZaav 68,132,200\n" \
"C 216,240,241\nRa 160,5,8192\nZaav 64,128,200\nE 242,178\nZaav 32,128,196\nRa 164,5,9216\ns_mov_b32 s75, s76\nZaav 36,132,196\n" \
"E 243,179\nZaav 4,132,192\nC 217,242,243\ns_add_u32 s77, s77, 0x4000\nZaav 0,128,192\nE 244,180\nZaav 8,136,192\nRa 168,5,10240\n" \
"Zaav 12,140,192\nE 245,181\nZaav 44,140,196\nC 218,244,245\nRa 172,5,11264\nZaav 40,136,196\nE 246,182\nZaav 72,136,200\n" \
"Ra 176,5,12288\nZaav 76,140,200\nE 247,183\nZaav 108,140,204\nC 219,246,247\nZaav 104,136,204\nE 240,184\nZaav 112,144,204\n" \
"buffer_store_dwordx4 a[204:207], v0, s[60:63], s95 offen offset:3072 sc1\nZaav 116,148,204\nE 241,185\nZaav 84,148,200\nC 220,240,241\nRa 180,5,13312\nZaav 80,144,200\nE 242,186\n" \
"Zaav 48,144,196\nRa 184,5,14336\nZaav 52,148,196\nE 243,187\nZaav 20,148,192\nC 221,242,243\nZaav 16,144,192\nE 244,188\n" \
"Zaav 24,152,192\nRa 188,5,15360\nZaav 28,156,192\nE 245,189\nZaav 60,156,196\nC 222,244,245\nZaav 56,152,196\nE 246,190\n" \
"Zaav 88,152,200\nZaav 92,156,200\nE 247,191\nZaav 124,156,204\nC 223,246,247\ns_waitcnt lgkmcnt(0)\nZaav 120,152,204\nQvav 160,160,32,12\n" \
"E 240,128\nMvav 160,164,36\nE 241,129\nMvav 160,168,40\nC 192,240,241\nRa 128,6,57344\nMvav 160,172,44\nE 242,130\n" \
"s_add_u32 s95, s34, 20480\nQvav 168,160,48,12\nbuffer_store_dwordx4 a[208:211], v0, s[60:63], s95 offen offset:0 sc1\nMvav 168,164,52\nE 243,131\nMvav 168,168,56\nC 193,242,243\nMvav 168,172,60\n" \
"E 244,132\nQvav 176,160,64,12\nRa 132,6,58368\nMvav 176,164,68\nE 245,133\nMvav 176,168,72\nC 194,244,245\nRa 136,6,59392\n" \
"Mvav 176,172,76\nE 246,134\nQvav 184,160,80,12\nRa 140,6,60416\nMvav 184,164,84\nE 247,135\nMvav 184,168,88\nC 195,246,247\n" \
"Mvav 184,172,92\nE 240,136\nQvav 164,176,32,12\nRa 144,6,61440\nMvav 164,180,36\nE 241,137\nMvav 164,184,40\nC 196,240,241\n" \
"Ra 148,6,62464\nMvav 164,188,44\nE 242,138\nQvav 172,176,48,12\nbuffer_store_dwordx4 a[212:215], v0, s[60:63], s95 offen offset:1024 sc1\nMvav 172,180,52\nE 243,139\nMvav 172,184,56\n" \
"C 197,242,243\nMvav 172,188,60\nE 244,140\nQvav 180,176,64,12\nRa 152,6,63488\nMvav 180,180,68\nE 245,141\nMvav 180,184,72\n" \
"C 198,244,245\nRa 156,6,64512\nMvav 180,188,76\nE 246,142\nQvav 188,176,80,12\nv_mov_b32 v6, v5\nv_add_u32 v5, s74, v9\nMvav 188,180,84\n" \
"E 247,143\nMvav 188,184,88\nC 199,246,247\nMvav 188,188,92\nE 240,144\nv_smfmac_f32_16x16x64_bf16 v[96:99], v[104:107], v[208:215], v7\nv_smfmac_f32_16x16x64_bf16 v[100:103], v[104:107], v[216:223], v7\ns_waitcnt vmcnt(15) lgkmcnt(0)\n" \
"s_barrier\nMaav 96,128,220\nbuffer_store_dwordx4 a[216:219], v0, s[60:63], s95 offen offset:2048 sc1\nMaav 100,132,220\nE 241,145\nMaav 68,132,216\nC 200,240,241\nRa 160,5,0\n" \
"Maav 64,128,216\nE 242,146\ns_mov_b32 m0, s73\nMaav 32,128,212\nDS 0,60,s77,0\nMaav 36,132,212\nE 243,147\nMaav 4,132,208\n" \
"C 201,242,243\nRa 164,5,1024\nMaav 0,128,208\nE 244,148\nMaav 8,136,208\nDS 0,60,s77,1024\nMaav 12,140,208\nE 245,149\n" \
"Maav 44,140,212\nC 202,244,245\nRa 168,5,2048\nMaav 40,136,212\nE 246,150\nMaav 72,136,216\nRa 172,5,3072\nRa 176,5,4096\n" \
"Maav 76,140,216\nE 247,151\nMaav 108,140,220\nC 203,246,247\nRa 180,5,5120\nMaav 104,136,220\nE 240,152\nMaav 112,144,220\n" \
"DS 0,60,s77,2048\nMaav 116,148,220\nE 241,153\nMaav 84,148,216\nC 204,240,241\nRa 184,5,6144\nMaav 80,144,216\nE 242,154\n" \
"Maav 48,144,212\nbuffer_store_dwordx4 a[220:223], v0, s[60:63], s95 offen offset:3072 sc1\nMaav 52,148,212\nE 243,155\nMaav 20,148,208\nC 205,242,243\nRa 188,5,7168\nMaav 16,144,208\n" \
"E 244,156\nMaav 24,152,208\nDS 0,60,s77,3072\nMaav 28,156,208\nE 245,157\nMaav 60,156,212\ns_branch 99f\n.p2align 12\n" \
"zp8:\ns_setpc_b64 s[98:99]\n99:\nC 206,244,245\nMaav 56,152,212\nE 246,158\nMaav 88,152,216\nMaav 92,156,216\n" \
"E 247,159\nMaav 124,156,220\nC 207,246,247\ns_waitcnt lgkmcnt(0)\nMaav 120,152,220\nQvav 128,160,32,12\nE 240,160\nMvav 128,164,36\n" \
"E 241,161\nMvav 128,168,40\nC 208,240,241\nRa 128,6,49152\nMvav 128,172,44\nE 242,162\ns_add_u32 m0, s75, 0xc000\nQvav 136,160,48,12\n" \
"DS 8,60,s77,0\nMvav 136,164,52\nE 243,163\nMvav 136,168,56\nC 209,242,243\nRa 132,6,50176\nMvav 136,172,60\nE 244,164\n" \
"Qvav 144,160,64,12\nDS 8,60,s77,1024\nMvav 144,164,68\nE 245,165\nMvav 144,168,72\nC 210,244,245\nRa 136,6,51200\nMvav 144,172,76\n" \
"E 246,166\ns_add_u32 s95, s34, 24576\nQvav 152,160,80,12\nbuffer_store_dwordx4 a[224:227], v0, s[60:63], s95 offen offset:0 sc1\nMvav 152,164,84\nE 247,167\nMvav 152,168,88\nC 211,246,247\n" \
"Ra 140,6,52224\nMvav 152,172,92\nE 240,168\nQvav 132,176,32,12\nDS 8,60,s77,2048\nMvav 132,180,36\nE 241,169\nMvav 132,184,40\n" \
"C 212,240,241\nRa 144,6,53248\nMvav 132,188,44\nE 242,170\nQvav 140,176,48,12\nbuffer_store_dwordx4 a[228:231], v0, s[60:63], s95 offen offset:1024 sc1\nMvav 140,180,52\nE 243,171\n" \
"Mvav 140,184,56\nC 213,242,243\nRa 148,6,54272\nMvav 140,188,60\nE 244,172\nQvav 148,176,64,12\nDS 8,60,s77,3072\nMvav 148,180,68\n" \
"E 245,173\nMvav 148,184,72\nC 214,244,245\nRa 152,6,55296\nMvav 148,188,76\nE 246,174\nQvav 156,176,80,12\nRa 156,6,56320\n" \
"s_mov_b32 s76, s73\nMvav 156,180,84\nE 247,175\nMvav 156,184,88\nC 215,246,247\ns_mov_b32 s73, s74\nMvav 156,188,92\nE 240,176\n" \
"v_smfmac_f32_16x16x64_bf16 v[96:99], v[104:107], v[192:199], v7\ns_mov_b32 s74, s75\nv_smfmac_f32_16x16x64_bf16 v[100:103], v[104:107], v[200:207], v7\ns_waitcnt lgkmcnt(0)\nMaav 96,128,204\nbuffer_store_dwordx4 a[232:235], v0, s[60:63], s95 offen offset:2048 sc1\nMaav 100,132,204\nE 241,177\n" \
"Maav 68,132,200\nC 216,240,241\nRa 160,5,8192\nMaav 64,128,200\nE 242,178\nMaav 32,128,196\nRa 164,5,9216\ns_mov_b32 s75, s76\n" \
"Maav 36,132,196\nE 243,179\nMaav 4,132,192\nC 217,242,243\ns_add_u32 s77, s77, 0x4000\nMaav 0,128,192\nE 244,180\nMaav 8,136,192\n" \
"Ra 168,5,10240\nMaav 12,140,192\nE 245,181\nMaav 44,140,196\nC 218,244,245\nRa 172,5,11264\nMaav 40,136,196\nE 246,182\n" \
"Maav 72,136,200\nRa 176,5,12288\nMaav 76,140,200\nE 247,183\nMaav 108,140,204\nC 219,246,247\nMaav 104,136,204\nE 240,184\n" \
"Maav 112,144,204\nbuffer_store_dwordx4 a[236:239], v0, s[60:63], s95 offen offset:3072 sc1\nMaav 116,148,204\nE 241,185\nMaav 84,148,200\nC 220,240,241\nRa 180,5,13312\nMaav 80,144,200\n" \
"E 242,186\nMaav 48,144,196\nRa 184,5,14336\nMaav 52,148,196\nE 243,187\nMaav 20,148,192\nC 221,242,243\nMaav 16,144,192\n" \
"E 244,188\nMaav 24,152,192\nRa 188,5,15360\nMaav 28,156,192\nE 245,189\nMaav 60,156,196\nC 222,244,245\nMaav 56,152,196\n" \
"E 246,190\nMaav 88,152,200\nMaav 92,156,200\nE 247,191\nMaav 124,156,204\nC 223,246,247\ns_waitcnt lgkmcnt(0)\nMaav 120,152,204\n" \
"Qvav 160,160,32,12\nE 240,128\nMvav 160,164,36\nE 241,129\nMvav 160,168,40\nC 192,240,241\nRa 128,6,57344\nMvav 160,172,44\n" \
"E 242,130\ns_add_u32 s95, s34, 28672\nQvav 168,160,48,12\nbuffer_store_dwordx4 a[240:243], v0, s[60:63], s95 offen offset:0 sc1\nMvav 168,164,52\nE 243,131\nMvav 168,168,56\nC 193,242,243\n" \
"Mvav 168,172,60\nE 244,132\nQvav 176,160,64,12\nRa 132,6,58368\nMvav 176,164,68\nE 245,133\nMvav 176,168,72\nC 194,244,245\n" \
"Ra 136,6,59392\nMvav 176,172,76\nE 246,134\nQvav 184,160,80,12\nRa 140,6,60416\nMvav 184,164,84\nE 247,135\nMvav 184,168,88\n" \
"C 195,246,247\nMvav 184,172,92\nE 240,136\nQvav 164,176,32,12\nRa 144,6,61440\nMvav 164,180,36\nE 241,137\nMvav 164,184,40\n" \
"C 196,240,241\nRa 148,6,62464\nMvav 164,188,44\nE 242,138\nQvav 172,176,48,12\nbuffer_store_dwordx4 a[244:247], v0, s[60:63], s95 offen offset:1024 sc1\nMvav 172,180,52\nE 243,139\n" \
"Mvav 172,184,56\nC 197,242,243\nMvav 172,188,60\nE 244,140\nQvav 180,176,64,12\nRa 152,6,63488\nMvav 180,180,68\nE 245,141\n" \
"Mvav 180,184,72\nC 198,244,245\nRa 156,6,64512\nMvav 180,188,76\nE 246,142\nQvav 188,176,80,12\nv_mov_b32 v6, v5\nv_add_u32 v5, s74, v9\n" \
"Mvav 188,180,84\nE 247,143\nMvav 188,184,88\nC 199,246,247\nMvav 188,188,92\nE 240,144\nv_smfmac_f32_16x16x64_bf16 v[96:99], v[104:107], v[208:215], v7\nv_smfmac_f32_16x16x64_bf16 v[100:103], v[104:107], v[216:223], v7\n" \
"s_waitcnt vmcnt(20) lgkmcnt(0)\ns_barrier\nMaav 96,128,220\nbuffer_store_dwordx4 a[248:251], v0, s[60:63], s95 offen offset:2048 sc1\nMaav 100,132,220\nE 241,145\nMaav 68,132,216\nC 200,240,241\n" \
"Ra 160,5,0\nMaav 64,128,216\nE 242,146\ns_mov_b32 m0, s73\nMaav 32,128,212\nDS 0,60,s77,0\nMaav 36,132,212\nE 243,147\n" \
"Maav 4,132,208\nC 201,242,243\nRa 164,5,1024\nMaav 0,128,208\nE 244,148\nMaav 8,136,208\nDS 0,60,s77,1024\nMaav 12,140,208\n" \
"E 245,149\nMaav 44,140,212\nC 202,244,245\nRa 168,5,2048\nMaav 40,136,212\nE 246,150\nMaav 72,136,216\nRa 172,5,3072\n" \
"Ra 176,5,4096\nMaav 76,140,216\nE 247,151\nMaav 108,140,220\nC 203,246,247\nRa 180,5,5120\nMaav 104,136,220\nE 240,152\n" \
"Maav 112,144,220\nDS 0,60,s77,2048\nMaav 116,148,220\nE 241,153\nMaav 84,148,216\nC 204,240,241\nRa 184,5,6144\nMaav 80,144,216\n" \
"E 242,154\nMaav 48,144,212\nbuffer_store_dwordx4 a[252:255], v0, s[60:63], s95 offen offset:3072 sc1\nMaav 52,148,212\nE 243,155\nMaav 20,148,208\nC 205,242,243\nRa 188,5,7168\n" \
"Maav 16,144,208\nE 244,156\nMaav 24,152,208\nDS 0,60,s77,3072\nMaav 28,156,208\nE 245,157\nMaav 60,156,212\nC 206,244,245\n" \
"Maav 56,152,212\nE 246,158\nMaav 88,152,216\nMaav 92,156,216\nE 247,159\nMaav 124,156,220\nC 207,246,247\ns_waitcnt lgkmcnt(0)\n" \
"Maav 120,152,220\nQvav 128,160,32,12\nE 240,160\nMvav 128,164,36\nE 241,161\nMvav 128,168,40\nC 208,240,241\nRa 128,6,49152\n" \
"Mvav 128,172,44\nE 242,162\ns_add_u32 m0, s75, 0xc000\nQvav 136,160,48,12\nDS 8,60,s77,0\nMvav 136,164,52\nE 243,163\nMvav 136,168,56\n" \
"C 209,242,243\nRa 132,6,50176\nMvav 136,172,60\nE 244,164\nQvav 144,160,64,12\nDS 8,60,s77,1024\nMvav 144,164,68\nE 245,165\n" \
"Mvav 144,168,72\nC 210,244,245\nMvav 144,172,76\nE 246,166\nQvav 152,160,80,12\nRa 136,6,51200\nRa 140,6,52224\nMvav 152,164,84\n" \
"E 247,167\nMvav 152,168,88\nC 211,246,247\nMvav 152,172,92\nE 240,168\nQvav 132,176,32,12\nDS 8,60,s77,2048\nMvav 132,180,36\n" \
"E 241,169\nMvav 132,184,40\nC 212,240,241\nRa 144,6,53248\nMvav 132,188,44\nE 242,170\nQvav 140,176,48,12\nRa 148,6,54272\n" \
"Mvav 140,180,52\nE 243,171\nMvav 140,184,56\nC 213,242,243\nRa 152,6,55296\nMvav 140,188,60\nE 244,172\nQvav 148,176,64,12\n" \
"DS 8,60,s77,3072\nMvav 148,180,68\nE 245,173\nMvav 148,184,72\nC 214,244,245\ns_mov_b32 s76, s73\nMvav 148,188,76\nE 246,174\n" \
"Qvav 156,176,80,12\nRa 156,6,56320\ns_mov_b32 s73, s74\nMvav 156,180,84\nE 247,175\nMvav 156,184,88\nC 215,246,247\ns_mov_b32 s74, s75\n" \
"Mvav 156,188,92\nE 240,176\nv_smfmac_f32_16x16x64_bf16 v[96:99], v[104:107], v[192:199], v7\ns_mov_b32 s75, s76\nv_smfmac_f32_16x16x64_bf16 v[100:103], v[104:107], v[200:207], v7\ns_waitcnt lgkmcnt(0)\nMaav 96,128,204\nRa 160,5,8192\n" \
"s_add_u32 s77, s77, 0x4000\nMaav 100,132,204\nE 241,177\nMaav 68,132,200\nC 216,240,241\nRa 164,5,9216\nMaav 64,128,200\nE 242,178\n" \
"Maav 32,128,196\nMaav 36,132,196\nE 243,179\nMaav 4,132,192\nC 217,242,243\nRa 168,5,10240\nMaav 0,128,192\nE 244,180\n" \
"Maav 8,136,192\nRa 172,5,11264\nMaav 12,140,192\nE 245,181\nMaav 44,140,196\nC 218,244,245\nMaav 40,136,196\nE 246,182\n" \
"Maav 72,136,200\nRa 176,5,12288\nMaav 76,140,200\nE 247,183\nMaav 108,140,204\nC 219,246,247\nMaav 104,136,204\nE 240,184\n" \
"Maav 112,144,204\nRa 180,5,13312\nMaav 116,148,204\nE 241,185\nMaav 84,148,200\nC 220,240,241\nMaav 80,144,200\nE 242,186\n" \
"Maav 48,144,196\nRa 184,5,14336\nMaav 52,148,196\nE 243,187\nMaav 20,148,192\nC 221,242,243\nRa 188,5,15360\nMaav 16,144,192\n" \
"E 244,188\nMaav 24,152,192\nMaav 28,156,192\nE 245,189\nMaav 60,156,196\nC 222,244,245\nMaav 56,152,196\nE 246,190\n" \
"Maav 88,152,200\nMaav 92,156,200\nE 247,191\nMaav 124,156,204\nC 223,246,247\ns_waitcnt lgkmcnt(0)\nMaav 120,152,204\nQvav 160,160,32,12\n" \
"E 240,128\nMvav 160,164,36\nE 241,129\nMvav 160,168,40\nC 192,240,241\nRa 128,6,57344\nMvav 160,172,44\nE 242,130\n" \
"Qvav 168,160,48,12\nRa 132,6,58368\nMvav 168,164,52\nE 243,131\nMvav 168,168,56\nC 193,242,243\nMvav 168,172,60\nE 244,132\n" \
"Qvav 176,160,64,12\nRa 136,6,59392\nMvav 176,164,68\nE 245,133\nMvav 176,168,72\nC 194,244,245\nMvav 176,172,76\nE 246,134\n" \
"Qvav 184,160,80,12\nRa 140,6,60416\nMvav 184,164,84\nE 247,135\nMvav 184,168,88\nC 195,246,247\nMvav 184,172,92\nE 240,136\n" \
"Qvav 164,176,32,12\nRa 144,6,61440\nMvav 164,180,36\nE 241,137\nMvav 164,184,40\nC 196,240,241\nMvav 164,188,44\nE 242,138\n" \
"Qvav 172,176,48,12\nRa 148,6,62464\nMvav 172,180,52\nE 243,139\nMvav 172,184,56\nC 197,242,243\nRa 152,6,63488\nMvav 172,188,60\n" \
"E 244,140\nQvav 180,176,64,12\nMvav 180,180,68\nE 245,141\nMvav 180,184,72\nC 198,244,245\nRa 156,6,64512\nMvav 180,188,76\n" \
"E 246,142\nQvav 188,176,80,12\nv_mov_b32 v6, v5\nv_add_u32 v5, s74, v9\nMvav 188,180,84\nE 247,143\nMvav 188,184,88\nC 199,246,247\n" \
"Mvav 188,188,92\nE 240,144\nv_smfmac_f32_16x16x64_bf16 v[96:99], v[104:107], v[208:215], v7\nv_smfmac_f32_16x16x64_bf16 v[100:103], v[104:107], v[216:223], v7\ns_waitcnt vmcnt(5) lgkmcnt(0)\ns_barrier\nMaav 96,128,220\nbuffer_store_dwordx2 v[2:3], off, s[60:63], s36 sc1\n" \
"Maav 100,132,220\nE 241,145\nMaav 68,132,216\nC 200,240,241\nRa 160,5,0\nMaav 64,128,216\nE 242,146\ns_mov_b32 m0, s73\n" \
"Maav 32,128,212\nDS 0,60,s77,0\nMaav 36,132,212\nE 243,147\nMaav 4,132,208\nC 201,242,243\nRa 164,5,1024\nMaav 0,128,208\n" \
"E 244,148\nMaav 8,136,208\nDS 0,60,s77,1024\nMaav 12,140,208\nE 245,149\nMaav 44,140,212\nC 202,244,245\nRa 168,5,2048\n" \
"Maav 40,136,212\nE 246,150\nMaav 72,136,216\nRa 172,5,3072\nMaav 76,140,216\nE 247,151\nMaav 108,140,220\nC 203,246,247\n" \
"Ra 176,5,4096\nMaav 104,136,220\nE 240,152\nMaav 112,144,220\nDS 0,60,s77,2048\nMaav 116,148,220\nE 241,153\nMaav 84,148,216\n" \
"C 204,240,241\nRa 180,5,5120\nMaav 80,144,216\nE 242,154\nMaav 48,144,212\nRa 184,5,6144\nRa 188,5,7168\nMaav 52,148,212\n" \
"E 243,155\nMaav 20,148,208\nC 205,242,243\nMaav 16,144,208\nE 244,156\nMaav 24,152,208\nDS 0,60,s77,3072\nMaav 28,156,208\n" \
"E 245,157\nMaav 60,156,212\nC 206,244,245\nMaav 56,152,212\nE 246,158\nMaav 88,152,216\nMaav 92,156,216\nE 247,159\n" \
"Maav 124,156,220\nC 207,246,247\ns_waitcnt lgkmcnt(0)\nMaav 120,152,220\ns_branch 99f\n.p2align 12\nzp9:\ns_setpc_b64 s[98:99]\n" \
"99:\ns_nop 0\ns_nop 0\ns_nop 0\nzO:\nQvav 128,160,32,12\nE 240,160\nMvav 128,164,36\n" \
"E 241,161\nMvav 128,168,40\nC 208,240,241\nRa 128,6,49152\nMvav 128,172,44\nE 242,162\ns_add_u32 m0, s75, 0xc000\nQvav 136,160,48,12\n" \
"DS 8,60,s77,0\nMvav 136,164,52\nE 243,163\nMvav 136,168,56\nC 209,242,243\nRa 132,6,50176\nMvav 136,172,60\nE 244,164\n" \
"Qvav 144,160,64,12\nDS 8,60,s77,1024\nMvav 144,164,68\nE 245,165\nMvav 144,168,72\nC 210,244,245\nRa 136,6,51200\nMvav 144,172,76\n" \
"E 246,166\nQvav 152,160,80,12\nDS 8,60,s77,2048\nMvav 152,164,84\nE 247,167\nMvav 152,168,88\nC 211,246,247\nRa 140,6,52224\n" \
"Mvav 152,172,92\nE 240,168\nQvav 132,176,32,12\nDS 8,60,s77,3072\nMvav 132,180,36\nE 241,169\nMvav 132,184,40\nC 212,240,241\n" \
"Ra 144,6,53248\nMvav 132,188,44\nE 242,170\nQvav 140,176,48,12\nbuffer_load_dwordx2 v[10:11], v1, s[60:63], 0 offen offset:64 sc1\nMvav 140,180,52\nE 243,171\nMvav 140,184,56\n" \
"C 213,242,243\nRa 148,6,54272\nMvav 140,188,60\nE 244,172\nQvav 148,176,64,12\nbuffer_load_dword v30, v4, s[56:59], 0 offen sc1\nMvav 148,180,68\nE 245,173\n" \
"Mvav 148,184,72\nC 214,244,245\nRa 152,6,55296\nMvav 148,188,76\nE 246,174\nQvav 156,176,80,12\nRa 156,6,56320\ns_mov_b32 s76, s73\n" \
"Mvav 156,180,84\nE 247,175\nMvav 156,184,88\nC 215,246,247\ns_mov_b32 s73, s74\nMvav 156,188,92\nE 240,176\nv_smfmac_f32_16x16x64_bf16 v[96:99], v[104:107], v[192:199], v7\n" \
"s_mov_b32 s74, s75\nv_smfmac_f32_16x16x64_bf16 v[100:103], v[104:107], v[200:207], v7\ns_waitcnt lgkmcnt(0)\nMaav 96,128,204\nRa 160,5,8192\ns_mov_b32 s75, s76\nMaav 100,132,204\nE 241,177\n" \
"Maav 68,132,200\nC 216,240,241\nRa 164,5,9216\nMaav 64,128,200\nE 242,178\nMaav 32,128,196\ns_add_u32 s77, s77, 0x4000\nv_add_u32 v4, s45, v4\n" \
"Maav 36,132,196\nE 243,179\nMaav 4,132,192\nC 217,242,243\nRa 168,5,10240\nMaav 0,128,192\nE 244,180\nMaav 8,136,192\n" \
"Ra 172,5,11264\nMaav 12,140,192\nE 245,181\nMaav 44,140,196\nC 218,244,245\nMaav 40,136,196\nE 246,182\nMaav 72,136,200\n" \
"Ra 176,5,12288\nMaav 76,140,200\nE 247,183\nMaav 108,140,204\nC 219,246,247\nMaav 104,136,204\nE 240,184\nMaav 112,144,204\n" \
"Ra 180,5,13312\nMaav 116,148,204\nE 241,185\nMaav 84,148,200\nC 220,240,241\nMaav 80,144,200\nE 242,186\nMaav 48,144,196\n" \
"Ra 184,5,14336\nMaav 52,148,196\nE 243,187\nMaav 20,148,192\nC 221,242,243\nRa 188,5,15360\nMaav 16,144,192\nE 244,188\n" \
"Maav 24,152,192\nMaav 28,156,192\nE 245,189\nMaav 60,156,196\nC 222,244,245\nMaav 56,152,196\nE 246,190\nMaav 88,152,200\n" \
"Maav 92,156,200\nE 247,191\nMaav 124,156,204\nC 223,246,247\ns_waitcnt lgkmcnt(0)\nMaav 120,152,204\nzE:\nQvav 160,160,32,12\n" \
"E 240,128\nMvav 160,164,36\nE 241,129\nMvav 160,168,40\nC 192,240,241\nRa 128,6,57344\nMvav 160,172,44\nE 242,130\n" \
"Qvav 168,160,48,12\nRa 132,6,58368\nMvav 168,164,52\nE 243,131\nMvav 168,168,56\nC 193,242,243\nMvav 168,172,60\nE 244,132\n" \
"Qvav 176,160,64,12\nRa 136,6,59392\nMvav 176,164,68\nE 245,133\nMvav 176,168,72\nC 194,244,245\nMvav 176,172,76\nE 246,134\n" \
"Qvav 184,160,80,12\nRa 140,6,60416\nMvav 184,164,84\nE 247,135\nMvav 184,168,88\nC 195,246,247\nMvav 184,172,92\nE 240,136\n" \
"Qvav 164,176,32,12\nRa 144,6,61440\nMvav 164,180,36\nE 241,137\nMvav 164,184,40\nC 196,240,241\nMvav 164,188,44\nE 242,138\n" \
"Qvav 172,176,48,12\nRa 148,6,62464\nMvav 172,180,52\nE 243,139\nMvav 172,184,56\nC 197,242,243\nRa 152,6,63488\nMvav 172,188,60\n" \
"E 244,140\nQvav 180,176,64,12\nMvav 180,180,68\nE 245,141\nMvav 180,184,72\nC 198,244,245\nRa 156,6,64512\nMvav 180,188,76\n" \
"E 246,142\nQvav 188,176,80,12\nv_mov_b32 v6, v5\nv_add_u32 v5, s74, v9\nMvav 188,180,84\nE 247,143\nMvav 188,184,88\nC 199,246,247\n" \
"Mvav 188,188,92\nE 240,144\nv_smfmac_f32_16x16x64_bf16 v[96:99], v[104:107], v[208:215], v7\nv_smfmac_f32_16x16x64_bf16 v[100:103], v[104:107], v[216:223], v7\ns_waitcnt vmcnt(9) lgkmcnt(0)\ns_barrier\nMaav 96,128,220\nRa 160,5,0\n" \
"Ra 164,5,1024\nMaav 100,132,220\nE 241,145\nMaav 68,132,216\nC 200,240,241\nMaav 64,128,216\nE 242,146\ns_mov_b32 m0, s73\n" \
"Maav 32,128,212\nDS 0,60,s77,0\nMaav 36,132,212\nE 243,147\nMaav 4,132,208\nC 201,242,243\nRa 168,5,2048\nMaav 0,128,208\n" \
"E 244,148\nMaav 8,136,208\nDS 0,60,s77,1024\nMaav 12,140,208\nE 245,149\nMaav 44,140,212\nC 202,244,245\nRa 172,5,3072\n" \
"Maav 40,136,212\nE 246,150\nMaav 72,136,216\nRa 176,5,4096\nMaav 76,140,216\nE 247,151\nMaav 108,140,220\nC 203,246,247\n" \
"Ra 180,5,5120\nMaav 104,136,220\nE 240,152\nMaav 112,144,220\nDS 0,60,s77,2048\nMaav 116,148,220\nE 241,153\nMaav 84,148,216\n" \
"C 204,240,241\nMaav 80,144,216\nE 242,154\nMaav 48,144,212\nDS 0,60,s77,3072\nMaav 52,148,212\nE 243,155\nMaav 20,148,208\n" \
"C 205,242,243\nRa 184,5,6144\nMaav 16,144,208\nE 244,156\nMaav 24,152,208\nRa 188,5,7168\nMaav 28,156,208\nE 245,157\n" \
"Maav 60,156,212\nC 206,244,245\ns_sub_u32 s96, s96, 1\nMaav 56,152,212\nE 246,158\nMaav 88,152,216\ns_cmp_lg_u32 s96, 0\nMaav 92,156,216\n" \
"E 247,159\nMaav 124,156,220\nC 207,246,247\ns_waitcnt lgkmcnt(0)\nMaav 120,152,220\ns_cbranch_scc1 zO\ns_cmp_lg_u32 s40, 0\ns_cbranch_scc1 zL\n" \
"Qvav 128,160,32,112\nE 240,160\nMvav 128,164,36\nE 241,161\nMvav 128,168,40\nC 208,240,241\nRa 128,6,49152\nMvav 128,172,44\n" \
"E 242,162\ns_add_u32 m0, s75, 0xc000\nQvav 136,160,48,12\nDS 8,60,s77,0\nMvav 136,164,52\nE 243,163\nMvav 136,168,56\nC 209,242,243\n" \
"Ra 132,6,50176\nMvav 136,172,60\nE 244,164\nQvav 144,160,64,12\nDS 8,60,s77,1024\nMvav 144,164,68\nE 245,165\nMvav 144,168,72\n" \
"C 210,244,245\nMvav 144,172,76\nE 246,166\nQvav 152,160,80,12\nRa 136,6,51200\nRa 140,6,52224\nMvav 152,164,84\nE 247,167\n" \
"Mvav 152,168,88\nC 211,246,247\nMvav 152,172,92\nE 240,168\nQvav 132,176,32,116\nDS 8,60,s77,2048\nMvav 132,180,36\nE 241,169\n" \
"Mvav 132,184,40\nC 212,240,241\nRa 144,6,53248\nMvav 132,188,44\nE 242,170\nQvav 140,176,48,12\nRa 148,6,54272\nMvav 140,180,52\n" \
"E 243,171\nMvav 140,184,56\nC 213,242,243\nRa 152,6,55296\nMvav 140,188,60\nE 244,172\nQvav 148,176,64,12\nDS 8,60,s77,3072\n" \
"Mvav 148,180,68\nE 245,173\nMvav 148,184,72\nC 214,244,245\ns_mov_b32 s76, s73\nMvav 148,188,76\nE 246,174\nQvav 156,176,80,12\n" \
"Ra 156,6,56320\ns_mov_b32 s73, s74\nMvav 156,180,84\nE 247,175\nMvav 156,184,88\nC 215,246,247\ns_mov_b32 s74, s75\nMvav 156,188,92\n" \
"E 240,176\nv_smfmac_f32_16x16x64_bf16 v[96:99], v[104:107], v[192:199], v7\ns_mov_b32 s75, s76\nv_smfmac_f32_16x16x64_bf16 v[100:103], v[104:107], v[200:207], v7\ns_waitcnt lgkmcnt(0)\nMaav 96,128,204\nRa 160,5,8192\ns_add_u32 s77, s77, 0x4000\n" \
"Maav 100,132,204\nE 241,177\nMaav 68,132,200\nC 216,240,241\nRa 164,5,9216\nMaav 64,128,200\nE 242,178\nMaav 32,128,196\n" \
"Maav 36,132,196\nE 243,179\nMaav 4,132,192\nC 217,242,243\nRa 168,5,10240\nMaav 0,128,192\nE 244,180\nMaav 8,136,192\n" \
"Ra 172,5,11264\nMaav 12,140,192\nE 245,181\nMaav 44,140,196\nC 218,244,245\nMaav 40,136,196\nE 246,182\nMaav 72,136,200\n" \
"Ra 176,5,12288\nMaav 76,140,200\nE 247,183\nMaav 108,140,204\nC 219,246,247\nMaav 104,136,204\nE 240,184\nMaav 112,144,204\n" \
"Ra 180,5,13312\nMaav 116,148,204\nE 241,185\nMaav 84,148,200\nC 220,240,241\nMaav 80,144,200\nE 242,186\nMaav 48,144,196\n" \
"Ra 184,5,14336\nMaav 52,148,196\nE 243,187\nMaav 20,148,192\nC 221,242,243\nRa 188,5,15360\nMaav 16,144,192\nE 244,188\n" \
"Maav 24,152,192\nMaav 28,156,192\nE 245,189\nMaav 60,156,196\nC 222,244,245\nMaav 56,152,196\nE 246,190\nMaav 88,152,200\n" \
"Maav 92,156,200\nE 247,191\nMaav 124,156,204\nC 223,246,247\ns_waitcnt lgkmcnt(0)\nMaav 120,152,204\nQvav 160,160,32,120\nE 240,128\n" \
"Mvav 160,164,36\nE 241,129\nMvav 160,168,40\nC 192,240,241\nRa 128,6,57344\nMvav 160,172,44\nE 242,130\nQvav 168,160,48,12\n" \
"Ra 132,6,58368\nMvav 168,164,52\nE 243,131\nMvav 168,168,56\nC 193,242,243\nMvav 168,172,60\nE 244,132\nQvav 176,160,64,12\n" \
"Ra 136,6,59392\nMvav 176,164,68\nE 245,133\nMvav 176,168,72\nC 194,244,245\nMvav 176,172,76\nE 246,134\nQvav 184,160,80,12\n" \
"Ra 140,6,60416\nMvav 184,164,84\nE 247,135\nMvav 184,168,88\nC 195,246,247\nMvav 184,172,92\nE 240,136\nQvav 164,176,32,124\n" \
"Ra 144,6,61440\nMvav 164,180,36\nE 241,137\nMvav 164,184,40\nC 196,240,241\nMvav 164,188,44\nE 242,138\nQvav 172,176,48,12\n" \
"Ra 148,6,62464\nMvav 172,180,52\nE 243,139\nMvav 172,184,56\nC 197,242,243\nRa 152,6,63488\nMvav 172,188,60\nE 244,140\n" \
"Qvav 180,176,64,12\nMvav 180,180,68\nE 245,141\nMvav 180,184,72\nC 198,244,245\nRa 156,6,64512\nMvav 180,188,76\nE 246,142\n" \
"Qvav 188,176,80,12\nv_mov_b32 v6, v5\nv_add_u32 v5, s74, v9\nMvav 188,180,84\nE 247,143\nMvav 188,184,88\nC 199,246,247\nMvav 188,188,92\n" \
"E 240,144\nv_smfmac_f32_16x16x64_bf16 v[96:99], v[104:107], v[208:215], v7\nv_smfmac_f32_16x16x64_bf16 v[100:103], v[104:107], v[216:223], v7\ns_waitcnt vmcnt(8) lgkmcnt(0)\ns_barrier\nMaav 96,128,220\nRa 160,5,0\nRa 164,5,1024\n" \
"Maav 100,132,220\nE 241,145\nMaav 68,132,216\nC 200,240,241\nMaav 64,128,216\nE 242,146\ns_mov_b32 m0, s73\nMaav 32,128,212\n" \
"DS 0,60,s77,0\nMaav 36,132,212\nE 243,147\nMaav 4,132,208\nC 201,242,243\nRa 168,5,2048\nMaav 0,128,208\nE 244,148\n" \
"Maav 8,136,208\nDS 0,60,s77,1024\nMaav 12,140,208\nE 245,149\nMaav 44,140,212\nC 202,244,245\nRa 172,5,3072\nMaav 40,136,212\n" \
"E 246,150\nMaav 72,136,216\nRa 176,5,4096\nMaav 76,140,216\nE 247,151\nMaav 108,140,220\nC 203,246,247\nRa 180,5,5120\n" \
"Maav 104,136,220\nE 240,152\nMaav 112,144,220\nDS 0,60,s77,2048\nMaav 116,148,220\nE 241,153\nMaav 84,148,216\nC 204,240,241\n" \
"Maav 80,144,216\nE 242,154\nMaav 48,144,212\nRa 184,5,6144\nRa 188,5,7168\nMaav 52,148,212\nE 243,155\nMaav 20,148,208\n" \
"C 205,242,243\nMaav 16,144,208\nE 244,156\nMaav 24,152,208\nDS 0,60,s77,3072\nMaav 28,156,208\nE 245,157\nMaav 60,156,212\n" \
"C 206,244,245\nMaav 56,152,212\nE 246,158\nMaav 88,152,216\nMaav 92,156,216\nE 247,159\nMaav 124,156,220\nC 207,246,247\n" \
"s_waitcnt lgkmcnt(0)\nMaav 120,152,220\nQvav 136,160,48,112\nE 240,160\nRa 128,6,49152\nMvav 136,164,52\nE 241,161\nMvav 136,168,56\n" \
"C 208,240,241\nRa 132,6,50176\nMvav 136,172,60\nE 242,162\ns_add_u32 m0, s75, 0xc000\nQvav 144,160,64,12\nDS 8,60,s77,0\nMvav 144,164,68\n" \
"E 243,163\nRa 136,6,51200\nMvav 144,168,72\nC 209,242,243\nE 244,164\ns_branch 99f\n.p2align 12\nzp10:\n" \
"s_setpc_b64 s[98:99]\n99:\nMvav 144,172,76\nDS 8,60,s77,1024\nQvav 152,160,80,12\nE 245,165\nMvav 152,164,84\nC 210,244,245\n" \
"Ra 140,6,52224\nMvav 152,168,88\nE 246,166\nRa 144,6,53248\nMvav 152,172,92\nDS 8,60,s77,2048\nQvav 140,176,48,116\nE 247,167\n" \
"Mvav 140,180,52\nC 211,246,247\nE 240,168\nMvav 140,184,56\nDS 8,60,s77,3072\nMvav 140,188,60\nE 241,169\nRa 148,6,54272\n" \
"Qvav 148,176,64,12\nC 212,240,241\nRa 152,6,55296\nMvav 148,180,68\nE 242,170\nMvav 148,184,72\nE 243,171\nMvav 148,188,76\n" \
"C 213,242,243\nRa 156,6,56320\nQvav 156,176,80,12\nE 244,172\nMvav 156,180,84\ns_mov_b32 s76, s73\ns_mov_b32 s73, s74\nMvav 156,184,88\n" \
"E 245,173\nMvav 156,188,92\nC 214,244,245\ns_mov_b32 s74, s75\nv_smfmac_f32_16x16x64_bf16 v[96:99], v[104:107], v[192:199], v7\ns_mov_b32 s75, s76\nv_smfmac_f32_16x16x64_bf16 v[100:103], v[104:107], v[200:207], v7\ns_waitcnt lgkmcnt(0)\n" \
"Maav 96,128,204\nE 246,174\nMaav 100,132,204\nE 247,175\nMaav 68,132,200\nC 215,246,247\nRa 160,5,8192\nMaav 64,128,200\n" \
"E 240,176\nMaav 32,128,196\nRa 164,5,9216\ns_add_u32 s77, s77, 0x4000\nMaav 36,132,196\nE 241,177\nMaav 4,132,192\nC 216,240,241\n" \
"E 242,178\nMaav 0,128,192\nRa 168,5,10240\nMaav 8,136,192\nE 243,179\nMaav 12,140,192\nC 217,242,243\nRa 172,5,11264\n" \
"Maav 44,140,196\nE 244,180\nMaav 40,136,196\nRa 176,5,12288\nMaav 72,136,200\nE 245,181\nMaav 76,140,200\nC 218,244,245\n" \
"E 246,182\nMaav 108,140,204\nRa 180,5,13312\nMaav 104,136,204\nE 247,183\nMaav 112,144,204\nC 219,246,247\nMaav 116,148,204\n" \
"E 240,184\nMaav 84,148,200\nE 241,185\nMaav 80,144,200\nC 220,240,241\nRa 184,5,14336\nMaav 48,144,196\nE 242,186\n" \
"Maav 52,148,196\nRa 188,5,15360\nMaav 20,148,192\nE 243,187\nMaav 16,144,192\nC 221,242,243\nMaav 24,152,192\nE 244,188\n" \
"Maav 28,156,192\nE 245,189\nMaav 60,156,196\nC 222,244,245\nMaav 56,152,196\nE 246,190\nMaav 88,152,200\nMaav 92,156,200\n" \
"E 247,191\nMaav 124,156,204\nC 223,246,247\ns_waitcnt lgkmcnt(0)\nMaav 120,152,204\nzD4:\nQvav 168,160,48,120\nE 240,136\n" \
"Mvav 168,164,52\nRa 128,6,57344\nMvav 168,168,56\nE 241,137\nMvav 168,172,60\nC 196,240,241\nRa 132,6,58368\nQvav 176,160,64,12\n" \
"E 242,138\nMvav 176,164,68\nRa 136,6,59392\nMvav 176,168,72\nE 243,139\nMvav 176,172,76\nC 197,242,243\nQvav 184,160,80,12\n" \
"Ra 140,6,60416\nMvav 184,164,84\nE 244,140\nMvav 184,168,88\nRa 144,6,61440\nMvav 184,172,92\nE 245,141\nQvav 172,176,48,124\n" \
"C 198,244,245\nMvav 172,180,52\nE 246,142\nMvav 172,184,56\nRa 148,6,62464\nMvav 172,188,60\nRa 152,6,63488\nQvav 180,176,64,12\n" \
"E 247,143\nMvav 180,180,68\nC 199,246,247\nRa 156,6,64512\nMvav 180,184,72\nE 240,144\nMvav 180,188,76\nv_mov_b32 v6, v5\n" \
"v_add_u32 v5, s74, v9\nQvav 188,176,80,12\nE 241,145\nMvav 188,180,84\nC 200,240,241\nMvav 188,184,88\nE 242,146\nMvav 188,188,92\n" \
"v_smfmac_f32_16x16x64_bf16 v[96:99], v[104:107], v[208:215], v7\nv_smfmac_f32_16x16x64_bf16 v[100:103], v[104:107], v[216:223], v7\ns_waitcnt vmcnt(8) lgkmcnt(0)\ns_barrier\nMaav 96,128,220\nRa 160,5,0\nMaav 100,132,220\nE 243,147\n" \
"Maav 68,132,216\nC 201,242,243\nRa 164,5,1024\nMaav 64,128,216\nE 244,148\ns_mov_b32 m0, s73\nMaav 32,128,212\nDS 0,60,s77,0\n" \
"Maav 36,132,212\nE 245,149\nMaav 4,132,208\nC 202,244,245\nMaav 0,128,208\nRa 168,5,2048\nMaav 8,136,208\nE 246,150\n" \
"Maav 12,140,208\nDS 0,60,s77,1024\nMaav 44,140,212\nE 247,151\nMaav 40,136,212\nC 203,246,247\nRa 172,5,3072\nMaav 72,136,216\n" \
"E 240,152\nMaav 76,140,216\nRa 176,5,4096\nMaav 108,140,220\nE 241,153\nMaav 104,136,220\nC 204,240,241\nMaav 112,144,220\n" \
"DS 0,60,s77,2048\nMaav 116,148,220\nE 242,154\nMaav 84,148,216\nRa 180,5,5120\nMaav 80,144,216\nE 243,155\nMaav 48,144,212\n" \
"C 205,242,243\nRa 184,5,6144\nMaav 52,148,212\nE 244,156\nMaav 20,148,208\nDS 0,60,s77,3072\nMaav 16,144,208\nRa 188,5,7168\n" \
"Maav 24,152,208\nE 245,157\nMaav 28,156,208\nC 206,244,245\nMaav 60,156,212\nE 246,158\nMaav 56,152,212\ns_mov_b32 s96, s41\n" \
"s_bitcmp1_b32 s33, 3\nMaav 88,152,216\nE 247,159\nMaav 92,156,216\nC 207,246,247\nMaav 124,156,220\ns_waitcnt lgkmcnt(0)\nMaav 120,152,220\n" \
"s_cbranch_scc1 zDa\nQvav 144,160,64,12\nE 240,168\nRa 128,6,49152\nMvav 144,164,68\nE 241,169\nMvav 144,168,72\nC 212,240,241\n" \
"Ra 132,6,50176\nRa 136,6,51200\nMvav 144,172,76\nE 242,170\ns_add_u32 m0, s75, 0xc000\nQvav 152,160,80,12\nDS 8,60,s77,0\nMvav 152,164,84\n" \
"E 243,171\nRa 140,6,52224\nMvav 152,168,88\nC 213,242,243\nE 244,172\nMvav 152,172,92\nDS 8,60,s77,1024\nRa 144,6,53248\n" \
"Qvav 148,176,64,12\nE 245,173\nMvav 148,180,68\nC 214,244,245\nRa 148,6,54272\nMvav 148,184,72\nE 246,174\nRa 152,6,55296\n" \
"Mvav 148,188,76\nE 247,175\nQvav 156,176,80,12\nC 215,246,247\nRa 156,6,56320\nMvav 156,180,84\nE 240,176\nMvav 156,184,88\n" \
"Mvav 156,188,92\nE 241,177\nMvvv 96,108,196\nC 216,240,241\nE 242,178\nv_smfmac_f32_16x16x64_bf16 v[100:103], v[104:107], v[200:207], v7\ns_waitcnt lgkmcnt(0)\nMaav 96,128,204\n" \
"DS 8,60,s77,2048\nMaav 100,132,204\nE 243,179\nMaav 68,132,200\nC 217,242,243\nRa 160,5,8192\nMaav 64,128,200\nE 244,180\n" \
"Maav 32,128,196\nE 245,181\nMaav 36,132,196\nC 218,244,245\nRa 164,5,9216\nMaav 40,136,196\nE 246,182\nMaav 44,140,196\n" \
"DS 8,60,s77,3072\nMaav 76,140,200\nE 247,183\nMaav 72,136,200\nC 219,246,247\nE 240,184\nMaav 104,136,204\nRa 168,5,10240\n" \
"Ra 172,5,11264\nMaav 108,140,204\nE 241,185\nMaav 112,144,204\nC 220,240,241\nRa 176,5,12288\nMaav 116,148,204\nE 242,186\n" \
"Maav 84,148,200\nE 243,187\nMaav 80,144,200\nC 221,242,243\nRa 180,5,13312\nMaav 48,144,196\nE 244,188\nMaav 52,148,196\n" \
"Ra 184,5,14336\nRa 188,5,15360\nMaav 56,152,196\nE 245,189\nMaav 60,156,196\nC 222,244,245\nE 246,190\nMaav 92,156,200\n" \
"s_mov_b32 s76, s73\ns_mov_b32 s73, s74\nMaav 88,152,200\nE 247,191\nMaav 120,152,204\nC 223,246,247\ns_waitcnt lgkmcnt(0)\nMaav 124,156,204\n" \
"s_mov_b32 s74, s75\ns_mov_b32 s75, s76\ns_add_u32 s77, s77, 0x4000\nQvav 176,160,64,12\nE 240,144\nMvav 176,164,68\nRa 128,6,57344\nRa 132,6,58368\n" \
"Mvav 176,168,72\nE 241,145\nMvav 176,172,76\nC 200,240,241\nQvav 184,160,80,12\nRa 136,6,59392\nRa 140,6,60416\nMvav 184,164,84\n" \
"E 242,146\nMvav 184,168,88\nRa 144,6,61440\nMvav 184,172,92\nE 243,147\nQvav 180,176,64,12\nC 201,242,243\nRa 148,6,62464\n" \
"Mvav 180,180,68\nRa 152,6,63488\nMvav 180,184,72\nE 244,148\nMvav 180,188,76\nRa 156,6,64512\nQvav 188,176,80,12\nE 245,149\n" \
"Mvav 188,180,84\nC 202,244,245\nv_mov_b32 v6, v5\nMvav 188,184,88\nv_add_u32 v5, s74, v9\nMvav 188,188,92\nE 246,150\nMvvv 96,108,212\n" \
"v_smfmac_f32_16x16x64_bf16 v[100:103], v[104:107], v[216:223], v7\ns_waitcnt vmcnt(8) lgkmcnt(0)\ns_barrier\nMaav 96,128,220\nE 247,151\nMaav 100,132,220\nC 203,246,247\ns_mov_b32 m0, s73\n" \
"Maav 68,132,216\nDS 0,60,s77,0\nMaav 64,128,216\nE 240,152\nMaav 32,128,212\nRa 160,5,0\nRa 164,5,1024\nMaav 36,132,212\n" \
"E 241,153\nMaav 40,136,212\nC 204,240,241\nRa 168,5,2048\nMaav 44,140,212\nDS 0,60,s77,1024\nMaav 76,140,216\nE 242,154\n" \
"Maav 72,136,216\nRa 172,5,3072\nRa 176,5,4096\nMaav 104,136,220\nE 243,155\nMaav 108,140,220\nC 205,242,243\nRa 180,5,5120\n" \
"Maav 112,144,220\nDS 0,60,s77,2048\nMaav 116,148,220\nE 244,156\nMaav 84,148,216\nRa 184,5,6144\nRa 188,5,7168\nMaav 80,144,216\n" \
"E 245,157\nMaav 48,144,212\nC 206,244,245\nMaav 52,148,212\nDS 0,60,s77,3072\nMaav 56,152,212\nE 246,158\nMaav 60,156,212\n" \
"Maav 92,156,216\nE 247,159\nMaav 88,152,216\nC 207,246,247\nMaav 120,152,220\ns_waitcnt lgkmcnt(0)\nMaav 124,156,220\ns_branch 99f\n" \
".p2align 12\nzp11:\ns_setpc_b64 s[98:99]\n99:\nzH:\nQvav 144,160,64,12\nE 240,176\nRa 128,6,49152\n" \
"Mvav 144,164,68\nE 241,177\nMvav 144,168,72\nC 216,240,241\nRa 132,6,50176\nMvav 144,172,76\nE 242,178\ns_add_u32 m0, s75, 0xc000\n" \
"Qvav 152,160,80,12\nDS 8,60,s77,0\nRa 136,6,51200\nMvav 152,164,84\nE 243,179\nMvav 152,168,88\nC 217,242,243\nRa 140,6,52224\n" \
"Mvav 152,172,92\nE 244,180\nQvav 148,176,64,12\nDS 8,60,s77,1024\nRa 144,6,53248\nMvav 148,180,68\nE 245,181\nMvav 148,184,72\n" \
"C 218,244,245\nRa 148,6,54272\nMvav 148,188,76\nE 246,182\nQvav 156,176,80,12\nRa 152,6,55296\nRa 156,6,56320\nMvav 156,180,84\n" \
"E 247,183\nMvav 156,184,88\nC 219,246,247\nMvav 156,188,92\nE 240,184\nv_smfmac_f32_16x16x64_bf16 v[100:103], v[104:107], v[200:207], v7\ns_waitcnt lgkmcnt(0)\nMaav 96,128,204\n" \
"DS 8,60,s77,2048\nRa 160,5,8192\nMaav 100,132,204\nE 241,185\nMaav 68,132,200\nC 220,240,241\nRa 164,5,9216\nMaav 64,128,200\n" \
"E 242,186\nMaav 72,136,200\nDS 8,60,s77,3072\nRa 168,5,10240\nMaav 76,140,200\nE 243,187\nMaav 108,140,204\nC 221,242,243\n" \
"Ra 172,5,11264\nMaav 104,136,204\nE 244,188\nMaav 112,144,204\nRa 176,5,12288\nRa 180,5,13312\nRa 184,5,14336\nMaav 116,148,204\n" \
"E 245,189\nMaav 84,148,200\nC 222,244,245\nRa 188,5,15360\nMaav 80,144,200\nE 246,190\nMaav 88,152,200\ns_mov_b32 s76, s73\n" \
"s_mov_b32 s73, s74\nMaav 92,156,200\nE 247,191\nMaav 124,156,204\nC 223,246,247\ns_waitcnt lgkmcnt(0)\nMaav 120,152,204\ns_mov_b32 s74, s75\n" \
"s_mov_b32 s75, s76\ns_add_u32 s77, s77, 0x4000\nQvav 176,160,64,12\nE 240,144\nMvav 176,164,68\nE 241,145\nMvav 176,168,72\nC 200,240,241\n" \
"Ra 128,6,57344\nMvav 176,172,76\nE 242,146\nQvav 184,160,80,12\nRa 132,6,58368\nRa 136,6,59392\nMvav 184,164,84\nE 243,147\n" \
"Mvav 184,168,88\nC 201,242,243\nRa 140,6,60416\nMvav 184,172,92\nE 244,148\nQvav 180,176,64,12\nRa 144,6,61440\nRa 148,6,62464\n" \
"Mvav 180,180,68\nE 245,149\nMvav 180,184,72\nC 202,244,245\nRa 152,6,63488\nMvav 180,188,76\nE 246,150\nQvav 188,176,80,12\n" \
"Ra 156,6,64512\nMvav 188,180,84\nE 247,151\nMvav 188,184,88\nC 203,246,247\nv_mov_b32 v6, v5\nMvav 188,188,92\nE 240,152\n" \
"v_smfmac_f32_16x16x64_bf16 v[100:103], v[104:107], v[216:223], v7\ns_waitcnt vmcnt(8) lgkmcnt(0)\ns_barrier\nv_add_u32 v5, s74, v9\nMaav 96,128,220\ns_mov_b32 m0, s73\nRa 160,5,0\nRa 164,5,1024\n" \
"Maav 100,132,220\nE 241,153\nDS 0,60,s77,0\nMaav 68,132,216\nC 204,240,241\nRa 168,5,2048\nMaav 64,128,216\nE 242,154\n" \
"Maav 72,136,216\nDS 0,60,s77,1024\nRa 172,5,3072\nMaav 76,140,216\nE 243,155\nMaav 108,140,220\nC 205,242,243\nRa 176,5,4096\n" \
"Maav 104,136,220\nE 244,156\nDS 0,60,s77,2048\nMaav 112,144,220\nRa 180,5,5120\nRa 184,5,6144\nRa 188,5,7168\nMaav 116,148,220\n" \
"E 245,157\nMaav 84,148,216\nC 206,244,245\ns_sub_u32 s96, s96, 1\nDS 0,60,s77,3072\nMaav 80,144,216\nE 246,158\nMaav 88,152,216\n" \
"s_cmp_lg_u32 s96, 0\nMaav 92,156,216\nE 247,159\nMaav 124,156,220\nC 207,246,247\ns_waitcnt lgkmcnt(0)\nMaav 120,152,220\ns_cbranch_scc1 zH\n" \
"Qvav 144,160,64,112\nE 240,176\nRa 128,6,49152\nMvav 144,164,68\nE 241,177\nMvav 144,168,72\nC 216,240,241\nRa 132,6,50176\n" \
"Mvav 144,172,76\nE 242,178\ns_add_u32 m0, s75, 0xc000\nQvav 152,160,80,12\nDS 8,60,s77,0\nRa 136,6,51200\nMvav 152,164,84\nE 243,179\n" \
"Mvav 152,168,88\nC 217,242,243\nRa 140,6,52224\nMvav 152,172,92\nE 244,180\nQvav 148,176,64,116\nDS 8,60,s77,1024\nRa 144,6,53248\n" \
"Mvav 148,180,68\nE 245,181\nMvav 148,184,72\nC 218,244,245\nRa 148,6,54272\nMvav 148,188,76\nE 246,182\nQvav 156,176,80,12\n" \
"Ra 152,6,55296\nRa 156,6,56320\nMvav 156,180,84\nE 247,183\nMvav 156,184,88\nC 219,246,247\nMvav 156,188,92\nE 240,184\n" \
"v_smfmac_f32_16x16x64_bf16 v[100:103], v[104:107], v[200:207], v7\ns_waitcnt lgkmcnt(0)\nMaav 96,128,204\nDS 8,60,s77,2048\nRa 160,5,8192\nMaav 100,132,204\nE 241,185\nMaav 68,132,200\n" \
"C 220,240,241\nRa 164,5,9216\nMaav 64,128,200\nE 242,186\nMaav 72,136,200\nDS 8,60,s77,3072\nRa 168,5,10240\nMaav 76,140,200\n" \
"E 243,187\nMaav 108,140,204\nC 221,242,243\nRa 172,5,11264\nMaav 104,136,204\nE 244,188\nMaav 112,144,204\nRa 176,5,12288\n" \
"Ra 180,5,13312\nRa 184,5,14336\nMaav 116,148,204\nE 245,189\nMaav 84,148,200\nC 222,244,245\nRa 188,5,15360\nMaav 80,144,200\n" \
"E 246,190\nMaav 88,152,200\ns_mov_b32 s76, s73\ns_mov_b32 s73, s74\nMaav 92,156,200\nE 247,191\nMaav 124,156,204\nC 223,246,247\n" \
"s_waitcnt lgkmcnt(0)\nMaav 120,152,204\ns_mov_b32 s74, s75\ns_mov_b32 s75, s76\ns_add_u32 s77, s77, 0x4000\nQvav 176,160,64,120\nE 240,144\nMvav 176,164,68\n" \
"E 241,145\nMvav 176,168,72\nC 200,240,241\nRa 128,6,57344\nMvav 176,172,76\nE 242,146\nQvav 184,160,80,12\nRa 132,6,58368\n" \
"Ra 136,6,59392\nMvav 184,164,84\nE 243,147\nMvav 184,168,88\nC 201,242,243\nRa 140,6,60416\nMvav 184,172,92\nE 244,148\n" \
"Qvav 180,176,64,124\nRa 144,6,61440\nRa 148,6,62464\nMvav 180,180,68\nE 245,149\nMvav 180,184,72\nC 202,244,245\nRa 152,6,63488\n" \
"Mvav 180,188,76\nE 246,150\nQvav 188,176,80,12\nRa 156,6,64512\nMvav 188,180,84\nE 247,151\nMvav 188,184,88\nC 203,246,247\n" \
"v_mov_b32 v6, v5\nMvav 188,188,92\nE 240,152\nv_smfmac_f32_16x16x64_bf16 v[100:103], v[104:107], v[216:223], v7\ns_waitcnt vmcnt(8) lgkmcnt(0)\ns_barrier\nv_add_u32 v5, s74, v9\nMaav 96,128,220\n" \
"Ra 160,5,0\nRa 164,5,1024\nMaav 100,132,220\nE 241,153\nMaav 68,132,216\nC 204,240,241\nRa 168,5,2048\nMaav 64,128,216\n" \
"E 242,154\nMaav 72,136,216\nRa 172,5,3072\nRa 176,5,4096\nMaav 76,140,216\nE 243,155\nMaav 108,140,220\nC 205,242,243\n" \
"Ra 180,5,5120\nMaav 104,136,220\nE 244,156\nMaav 112,144,220\nRa 184,5,6144\nRa 188,5,7168\nMaav 116,148,220\nE 245,157\n" \
"Maav 84,148,216\nC 206,244,245\nMaav 80,144,216\nE 246,158\nMaav 88,152,216\nMaav 92,156,216\nE 247,159\nMaav 124,156,220\n" \
"C 207,246,247\ns_waitcnt lgkmcnt(0)\nMaav 120,152,220\nzE3:\nQvav 152,160,80,112\nE 240,176\nRa 128,6,49152\nRa 132,6,50176\n" \
"Mvav 152,164,84\nE 241,177\nRa 136,6,51200\nMvav 152,168,88\nC 216,240,241\nE 242,178\nRa 140,6,52224\nMvav 152,172,92\n" \
"Ra 144,6,53248\nRa 148,6,54272\nRa 152,6,55296\nQvav 156,176,80,116\nE 243,179\nRa 156,6,56320\nMvav 156,180,84\nC 217,242,243\n" \
"E 244,180\nMvav 156,184,88\nMvav 156,188,92\nE 245,181\nv_smfmac_f32_16x16x64_bf16 v[100:103], v[104:107], v[200:207], v7\nC 218,244,245\ns_waitcnt lgkmcnt(0)\nMaav 96,128,204\n" \
"E 246,182\nRa 160,5,8192\nMaav 100,132,204\nRa 164,5,9216\nRa 168,5,10240\nMaav 68,132,200\nE 247,183\nMaav 64,128,200\n" \
"C 219,246,247\nE 240,184\nMaav 72,136,200\nE 241,185\nRa 172,5,11264\nMaav 76,140,200\nC 220,240,241\nRa 176,5,12288\n" \
"Maav 108,140,204\nE 242,186\nMaav 104,136,204\nE 243,187\nMaav 112,144,204\nC 221,242,243\nRa 180,5,13312\nRa 184,5,14336\n" \
"Maav 116,148,204\nE 244,188\nMaav 84,148,200\nE 245,189\nMaav 80,144,200\nC 222,244,245\nRa 188,5,15360\nMaav 88,152,200\n" \
"E 246,190\nMaav 92,156,200\nE 247,191\nMaav 124,156,204\nC 223,246,247\ns_waitcnt lgkmcnt(0)\nMaav 120,152,204\nQvav 184,160,80,120\n" \
"Ra 128,6,57344\nMvav 184,164,84\nRa 132,6,58368\nMvav 184,168,88\nRa 136,6,59392\nMvav 184,172,92\nRa 140,6,60416\nQvav 188,176,80,124\n" \
"Ra 144,6,61440\nMvav 188,180,84\nRa 148,6,62464\nMvav 188,184,88\nRa 152,6,63488\nMvav 188,188,92\nRa 156,6,64512\nv_smfmac_f32_16x16x64_bf16 v[100:103], v[104:107], v[216:223], v7\n" \
"s_waitcnt vmcnt(0) lgkmcnt(0)\ns_barrier\nv_mov_b32 v6, v5\nMaav 96,128,220\nRa 160,6,49152\nMaav 100,132,220\nE 240,152\nE 241,153\n" \
"Maav 68,132,216\nRa 164,6,50176\nC 204,240,241\nMaav 64,128,216\nE 242,154\nMaav 72,136,216\nRa 168,6,51200\nMaav 76,140,216\n" \
"E 243,155\nMaav 108,140,220\nRa 172,6,52224\nC 205,242,243\nMaav 104,136,220\nE 244,156\nMaav 112,144,220\nRa 176,6,53248\n" \
"Maav 116,148,220\nE 245,157\nMaav 84,148,216\nRa 180,6,54272\nC 206,244,245\nMaav 80,144,216\nE 246,158\nMaav 88,152,216\n" \
"Ra 184,6,55296\nMaav 92,156,216\nE 247,159\nMaav 124,156,220\nRa 188,6,56320\nC 207,246,247\nMaav 120,152,220\ns_waitcnt lgkmcnt(0)\n" \
"E 240,184\nMvvv 100,108,204\nRa 128,6,57344\nE 241,185\nMaav 96,160,204\nRa 132,6,58368\nC 192,240,241\nE 242,186\n" \
"Maav 100,164,204\nRa 136,6,59392\nE 243,187\nMaav 104,168,204\nRa 140,6,60416\nC 193,242,243\nE 244,188\nMaav 108,172,204\n" \
"Ra 144,6,61440\nE 245,189\nMaav 112,176,204\nRa 148,6,62464\nC 194,244,245\nE 246,190\nMaav 116,180,204\nRa 152,6,63488\n" \
"E 247,191\nMaav 120,184,204\nRa 156,6,64512\nC 195,246,247\nMaav 124,188,204\ns_nop 0\ns_waitcnt lgkmcnt(0)\nMvvv 100,108,192\n" \
"Maav 96,128,192\nMaav 100,132,192\nMaav 104,136,192\nMaav 108,140,192\nMaav 112,144,192\nMaav 116,148,192\nMaav 120,152,192\nv_add_f32 v104, v96, v97\n" \
"v_add_f32 v105, v98, v99\nv_add_f32 v106, v100, v101\nv_add_f32 v107, v102, v103\nMaav 124,156,192\nv_subrev_u32 v240, 0xf800000, v104\nv_subrev_u32 v241, 0xf800000, v105\nv_subrev_u32 v242, 0xf800000, v106\nv_subrev_u32 v243, 0xf800000, v107\n" \
"v_max3_u32 v244, v240, v241, v242\nv_max_u32 v244, v244, v243\nv_cmp_lt_u32 vcc, 0x3c000000, v244\ns_cbranch_vccnz zfb\nzend:\ns_nop 7\n.subsection 1\ns_branch 99f\n" \
".p2align 12\nzp12:\ns_setpc_b64 s[98:99]\n99:\nzV:\nZvaa 32,0,64\nTa 96,8,8192\nZvaa 36,4,64\n" \
"Ta 98,8,9216\nZvaa 44,4,68\nRa 32,27,0\nZvaa 40,0,68\nTa 100,20,8192\nZvaa 48,0,72\nTa 102,20,9216\nZvaa 52,4,72\n" \
"Ra 36,27,2048\nZvaa 60,4,76\nTa 104,21,8192\nZvaa 56,0,76\nTa 106,21,9216\nZvaa 64,0,80\nRa 40,27,4096\nZvaa 68,4,80\n" \
"Ta 108,22,8192\nZvaa 76,4,84\nTa 110,22,9216\nZvaa 72,0,84\nRa 44,27,6144\nZvaa 80,0,88\nTa 112,23,8192\nZvaa 84,4,88\n" \
"Ta 114,23,9216\nZvaa 92,4,92\nRa 48,27,8192\nZvaa 88,0,92\nTa 116,24,8192\nZvaa 152,8,92\nTa 118,24,9216\nZvaa 156,12,92\n" \
"Ra 52,27,10240\nZvaa 148,12,88\nTa 120,25,8192\nZvaa 144,8,88\nTa 122,25,9216\nZvaa 136,8,84\nRa 56,27,12288\nZvaa 140,12,84\n" \
"Ta 124,26,8192\nZvaa 132,12,80\nTa 126,26,9216\nZvaa 128,8,80\nRa 60,27,14336\nZvaa 120,8,76\nAR 2,252\nAR 3,253\n" \
"Zvaa 124,12,76\nv_readfirstlane_b32 s96, v2\nv_readfirstlane_b32 s97, v3\nZvaa 116,12,72\ns_mul_i32 s97, s97, 0x7f4a7c15\ns_mul_hi_u32 s98, s96, 0x7f4a7c15\nZvaa 112,8,72\ns_add_u32 s97, s97, s98\n" \
"s_mul_i32 s98, s96, 0x9e3779b9\nZvaa 104,8,68\ns_add_u32 s97, s97, s98\ns_mul_i32 s96, s96, 0x7f4a7c15\nZvaa 108,12,68\ns_add_u32 s96, s96, 0xd9b4e019\ns_addc_u32 s97, s97, 0x632be59b\nZvaa 100,12,64\n" \
"v_mov_b32 v2, s96\nv_mov_b32 v3, s97\nZvaa 96,8,64\ns_mov_b32 s96, 3\nZvaa 160,16,64\ns_waitcnt lgkmcnt(0)\ns_barrier\nZvaa 164,20,64\n" \
"s_add_u32 m0, s88, 0\nZvaa 172,20,68\nD 5,64,0,0\nZvaa 168,16,68\nZvaa 176,16,72\nZvaa 180,20,72\nZvaa 188,20,76\nD 5,64,s73,1024\n" \
"Zvaa 184,16,76\nZvaa 192,16,80\nZvaa 196,20,80\nZvaa 204,20,84\nD 5,64,s74,2048\nZvaa 200,16,84\nZvaa 208,16,88\nZvaa 212,20,88\n" \
"Zvaa 220,20,92\nD 5,64,s75,3072\nZvaa 216,16,92\nZaaa 152,24,92\nZaaa 156,28,92\ns_add_u32 m0, s88, 4096\nZaaa 148,28,88\nD 5,64,s76,0\n" \
"Zaaa 144,24,88\nZaaa 136,24,84\nZaaa 140,28,84\nZaaa 132,28,80\nD 5,64,s77,1024\nZaaa 128,24,80\nZvaa 248,24,76\nZvaa 252,28,76\n" \
"Zvaa 244,28,72\nD 5,64,s78,2048\nZvaa 240,24,72\nZvaa 232,24,68\nZvaa 236,28,68\nZvaa 228,28,64\nD 5,64,s79,3072\nZvaa 224,24,64\n" \
"s_branch 99f\n.p2align 12\nzp13:\ns_setpc_b64 s[98:99]\n99:\ns_nop 0\nzgV:\nMvaa 32,32,96\n" \
"Mvaa 36,36,96\ns_add_u32 m0, s32, 65536\nMvaa 44,36,100\nD 6,68,0,0\nMvaa 40,32,100\nMvaa 48,32,104\nMvaa 52,36,104\nMvaa 60,36,108\n" \
"D 6,68,s89,1024\nMvaa 56,32,108\nMvaa 64,32,112\nMvaa 68,36,112\nMvaa 76,36,116\nD 28,68,s90,2048\nMvaa 72,32,116\nMvaa 80,32,120\n" \
"Mvaa 84,36,120\nMvaa 92,36,124\nD 28,68,s91,3072\nMvaa 88,32,124\nMvaa 152,40,124\nMvaa 156,44,124\ns_add_u32 m0, s32, 81920\nMvaa 148,44,120\n" \
"D 6,68,s92,0\nMvaa 144,40,120\nMvaa 136,40,116\nMvaa 140,44,116\nMvaa 132,44,112\nD 6,68,s93,1024\nMvaa 128,40,112\nMvaa 120,40,108\n" \
"Mvaa 124,44,108\nMvaa 116,44,104\nD 28,68,s94,2048\nMvaa 112,40,104\nMvaa 104,40,100\nMvaa 108,44,100\nMvaa 100,44,96\nD 28,68,s95,3072\n" \
"Mvaa 96,40,96\ns_waitcnt vmcnt(17)\ns_barrier\nMvaa 160,48,96\nbuffer_load_dword v30, v4, s[52:55], 0 offen sc1\nTa 64,8,32768\nv_add_u32 v5, 128, v5\nMvaa 164,52,96\n" \
"Ta 66,8,33792\nv_add_u32 v6, s72, v6\nMvaa 172,52,100\nRa 0,7,32768\nv_add_u32 v28, s72, v28\nMvaa 168,48,100\nTa 68,20,32768\nMvaa 176,48,104\n" \
"Ta 70,20,33792\nMvaa 180,52,104\nRa 4,7,34816\nMvaa 188,52,108\nv_add_u32 v4, s44, v4\nTa 72,21,32768\nMvaa 184,48,108\nTa 74,21,33792\n" \
"Mvaa 192,48,112\nRa 8,7,36864\nMvaa 196,52,112\nTa 76,22,32768\nMvaa 204,52,116\nTa 78,22,33792\nMvaa 200,48,116\nRa 12,7,38912\n" \
"Mvaa 208,48,120\nTa 80,23,32768\nMvaa 212,52,120\nTa 82,23,33792\nMvaa 220,52,124\nRa 16,7,40960\nMvaa 216,48,124\nTa 84,24,32768\n" \
"Maaa 152,56,124\nTa 86,24,33792\nMaaa 156,60,124\nRa 20,7,43008\nMaaa 148,60,120\nTa 88,25,32768\nMaaa 144,56,120\nTa 90,25,33792\n" \
"Maaa 136,56,116\nRa 24,7,45056\nMaaa 140,60,116\nTa 92,26,32768\nMaaa 132,60,112\nTa 94,26,33792\nMaaa 128,56,112\nRa 28,7,47104\n" \
"Mvaa 248,56,108\nMvaa 252,60,108\nMvaa 244,60,104\nMvaa 240,56,104\nMvaa 232,56,100\nMvaa 236,60,100\nMvaa 228,60,96\nMvaa 224,56,96\n" \
"s_waitcnt lgkmcnt(0)\nMvaa 32,0,64\nTa 96,8,40960\nMvaa 36,4,64\nTa 98,8,41984\nMvaa 44,4,68\nRa 32,27,32768\nMvaa 40,0,68\n" \
"Ta 100,20,40960\nMvaa 48,0,72\nTa 102,20,41984\nMvaa 52,4,72\nRa 36,27,34816\nMvaa 60,4,76\nTa 104,21,40960\nMvaa 56,0,76\n" \
"Ta 106,21,41984\nMvaa 64,0,80\nRa 40,27,36864\nMvaa 68,4,80\nTa 108,22,40960\nMvaa 76,4,84\nTa 110,22,41984\nMvaa 72,0,84\n" \
"Ra 44,27,38912\nMvaa 80,0,88\nTa 112,23,40960\nMvaa 84,4,88\nTa 114,23,41984\nMvaa 92,4,92\nRa 48,27,40960\nMvaa 88,0,92\n" \
"Ta 116,24,40960\nMvaa 152,8,92\nTa 118,24,41984\nMvaa 156,12,92\nRa 52,27,43008\nMvaa 148,12,88\nTa 120,25,40960\nMvaa 144,8,88\n" \
"Ta 122,25,41984\nMvaa 136,8,84\nRa 56,27,45056\nMvaa 140,12,84\nTa 124,26,40960\nMvaa 132,12,80\nTa 126,26,41984\nMvaa 128,8,80\n" \
"Ra 60,27,47104\nMvaa 120,8,76\nMvaa 124,12,76\nMvaa 116,12,72\nMvaa 112,8,72\nMvaa 104,8,68\nMvaa 108,12,68\nMvaa 100,12,64\n" \
"Mvaa 96,8,64\nMvaa 160,16,64\ns_waitcnt lgkmcnt(0)\ns_barrier\nMvaa 164,20,64\ns_add_u32 m0, s88, 32768\nMvaa 172,20,68\nD 5,64,0,0\n" \
"Mvaa 168,16,68\nMvaa 176,16,72\nMvaa 180,20,72\nMvaa 188,20,76\nD 5,64,s73,1024\nMvaa 184,16,76\nMvaa 192,16,80\nMvaa 196,20,80\n" \
"Mvaa 204,20,84\nD 5,64,s74,2048\nMvaa 200,16,84\nMvaa 208,16,88\nMvaa 212,20,88\nMvaa 220,20,92\nD 5,64,s75,3072\nMvaa 216,16,92\n" \
"Maaa 152,24,92\nMaaa 156,28,92\ns_add_u32 m0, s88, 36864\nMaaa 148,28,88\nD 5,64,s76,0\nMaaa 144,24,88\nMaaa 136,24,84\nMaaa 140,28,84\n" \
"Maaa 132,28,80\nD 5,64,s77,1024\nMaaa 128,24,80\nMvaa 248,24,76\nMvaa 252,28,76\nMvaa 244,28,72\nD 5,64,s78,2048\nMvaa 240,24,72\n" \
"Mvaa 232,24,68\nMvaa 236,28,68\nMvaa 228,28,64\nD 5,64,s79,3072\nMvaa 224,24,64\nMvaa 32,32,96\nMvaa 36,36,96\ns_add_u32 m0, s32, 98304\n" \
"Mvaa 44,36,100\nD 6,68,0,0\nMvaa 40,32,100\nMvaa 48,32,104\nMvaa 52,36,104\nMvaa 60,36,108\nD 6,68,s89,1024\nMvaa 56,32,108\n" \
"Mvaa 64,32,112\nMvaa 68,36,112\nMvaa 76,36,116\nD 28,68,s90,2048\nMvaa 72,32,116\nMvaa 80,32,120\nMvaa 84,36,120\nMvaa 92,36,124\n" \
"D 28,68,s91,3072\nMvaa 88,32,124\nMvaa 152,40,124\nMvaa 156,44,124\ns_add_u32 m0, s32, 114688\nMvaa 148,44,120\nD 6,68,s92,0\nMvaa 144,40,120\n" \
"Mvaa 136,40,116\nMvaa 140,44,116\nMvaa 132,44,112\nD 6,68,s93,1024\nMvaa 128,40,112\nMvaa 120,40,108\nMvaa 124,44,108\nMvaa 116,44,104\n" \
"D 28,68,s94,2048\nMvaa 112,40,104\nMvaa 104,40,100\nMvaa 108,44,100\nMvaa 100,44,96\nD 28,68,s95,3072\nMvaa 96,40,96\ns_waitcnt vmcnt(17)\n" \
"s_barrier\nMvaa 160,48,96\nbuffer_load_dword v30, v4, s[52:55], 0 offen sc1\nTa 64,8,0\nv_add_u32 v5, 128, v5\nMvaa 164,52,96\nTa 66,8,1024\nv_add_u32 v6, s72, v6\n" \
"Mvaa 172,52,100\nRa 0,7,0\nv_add_u32 v28, s72, v28\nMvaa 168,48,100\nTa 68,20,0\nMvaa 176,48,104\nTa 70,20,1024\nMvaa 180,52,104\n" \
"Ra 4,7,2048\nMvaa 188,52,108\nv_add_u32 v4, s44, v4\nTa 72,21,0\nMvaa 184,48,108\nTa 74,21,1024\nMvaa 192,48,112\nRa 8,7,4096\n" \
"Mvaa 196,52,112\nTa 76,22,0\nMvaa 204,52,116\nTa 78,22,1024\nMvaa 200,48,116\nRa 12,7,6144\nMvaa 208,48,120\nTa 80,23,0\n" \
"Mvaa 212,52,120\nTa 82,23,1024\nMvaa 220,52,124\nRa 16,7,8192\nMvaa 216,48,124\nTa 84,24,0\nMaaa 152,56,124\nTa 86,24,1024\n" \
"Maaa 156,60,124\nRa 20,7,10240\nMaaa 148,60,120\nTa 88,25,0\nMaaa 144,56,120\nTa 90,25,1024\nMaaa 136,56,116\nRa 24,7,12288\n" \
"Maaa 140,60,116\nTa 92,26,0\nMaaa 132,60,112\nTa 94,26,1024\nMaaa 128,56,112\nRa 28,7,14336\nMvaa 248,56,108\nMvaa 252,60,108\n" \
"Mvaa 244,60,104\nMvaa 240,56,104\ns_sub_u32 s96, s96, 1\nMvaa 232,56,100\ns_cmp_lg_u32 s96, 0\nMvaa 236,60,100\nMvaa 228,60,96\nMvaa 224,56,96\n" \
"s_waitcnt lgkmcnt(0)\ns_cbranch_scc0 zhV\nMvaa 32,0,64\nTa 96,8,8192\nMvaa 36,4,64\nTa 98,8,9216\nMvaa 44,4,68\nRa 32,27,0\n" \
"Mvaa 40,0,68\nTa 100,20,8192\nMvaa 48,0,72\nTa 102,20,9216\nMvaa 52,4,72\nRa 36,27,2048\nMvaa 60,4,76\nTa 104,21,8192\n" \
"Mvaa 56,0,76\nTa 106,21,9216\nMvaa 64,0,80\nRa 40,27,4096\nMvaa 68,4,80\nTa 108,22,8192\nMvaa 76,4,84\nTa 110,22,9216\n" \
"Mvaa 72,0,84\nRa 44,27,6144\nMvaa 80,0,88\nTa 112,23,8192\nMvaa 84,4,88\nTa 114,23,9216\nMvaa 92,4,92\nRa 48,27,8192\n" \
"Mvaa 88,0,92\nTa 116,24,8192\nMvaa 152,8,92\nTa 118,24,9216\nMvaa 156,12,92\nRa 52,27,10240\nMvaa 148,12,88\nTa 120,25,8192\n" \
"Mvaa 144,8,88\nTa 122,25,9216\nMvaa 136,8,84\nRa 56,27,12288\nMvaa 140,12,84\nTa 124,26,8192\nMvaa 132,12,80\nTa 126,26,9216\n" \
"Mvaa 128,8,80\nRa 60,27,14336\nMvaa 120,8,76\nMvaa 124,12,76\nMvaa 116,12,72\nMvaa 112,8,72\nMvaa 104,8,68\nMvaa 108,12,68\n" \
"Mvaa 100,12,64\nMvaa 96,8,64\nMvaa 160,16,64\ns_waitcnt lgkmcnt(0)\ns_barrier\nMvaa 164,20,64\ns_add_u32 m0, s88, 0\nMvaa 172,20,68\n" \
"D 5,64,0,0\nMvaa 168,16,68\nMvaa 176,16,72\nMvaa 180,20,72\nMvaa 188,20,76\nD 5,64,s73,1024\nMvaa 184,16,76\nMvaa 192,16,80\n" \
"Mvaa 196,20,80\nMvaa 204,20,84\nD 5,64,s74,2048\nMvaa 200,16,84\nMvaa 208,16,88\nMvaa 212,20,88\nMvaa 220,20,92\nD 5,64,s75,3072\n" \
"Mvaa 216,16,92\nMaaa 152,24,92\nMaaa 156,28,92\ns_add_u32 m0, s88, 4096\nMaaa 148,28,88\nD 5,64,s76,0\nMaaa 144,24,88\nMaaa 136,24,84\n" \
"Maaa 140,28,84\nMaaa 132,28,80\nD 5,64,s77,1024\nMaaa 128,24,80\nMvaa 248,24,76\nMvaa 252,28,76\nMvaa 244,28,72\nD 5,64,s78,2048\n" \
"Mvaa 240,24,72\nMvaa 232,24,68\nMvaa 236,28,68\nMvaa 228,28,64\nD 5,64,s79,3072\nMvaa 224,24,64\ns_branch zgV\nzhV:\n" \
"s_bitcmp1_b32 s33, 1\ns_cbranch_scc1 zxV\nMvaa 32,0,64\nTa 96,8,8192\ns_mov_b64 s[64:65], s[84:85]\nMvaa 36,4,64\nTa 98,8,9216\ns_mov_b32 s66, 4194304\n" \
"Mvaa 44,4,68\nRa 32,27,0\ns_mov_b64 s[68:69], s[86:87]\nMvaa 40,0,68\nTa 100,20,8192\ns_mov_b32 s70, 8388608\nMvaa 48,0,72\nTa 102,20,9216\n" \
"s_mov_b32 s73, 15360\nMvaa 52,4,72\nRa 36,27,2048\ns_mov_b32 s74, 129024\nMvaa 60,4,76\nTa 104,21,8192\ns_mov_b32 s75, 144384\nMvaa 56,0,76\n" \
"Ta 106,21,9216\ns_add_u32 s76, s37, 0\nMvaa 64,0,80\nRa 40,27,4096\ns_add_u32 s77, s37, 15360\nMvaa 68,4,80\nTa 108,22,8192\ns_add_u32 s78, s37, 129024\n" \
"Mvaa 76,4,84\nTa 110,22,9216\ns_add_u32 s79, s37, 144384\nMvaa 72,0,84\nRa 44,27,6144\ns_mov_b32 s89, 31744\nMvaa 80,0,88\nTa 112,23,8192\n" \
"s_mov_b32 s90, 63488\nMvaa 84,4,88\nTa 114,23,9216\ns_mov_b32 s91, 95232\nMvaa 92,4,92\nRa 48,27,8192\ns_mov_b32 s72, 524288\nMvaa 88,0,92\n" \
"Ta 116,24,8192\nv_xor_b32 v28, 128, v14\nMvaa 152,8,92\nTa 118,24,9216\nv_mov_b32 v4, v15\nMvaa 156,12,92\nRa 52,27,10240\nMvaa 148,12,88\n" \
"Ta 120,25,8192\nMvaa 144,8,88\nTa 122,25,9216\nMvaa 136,8,84\nRa 56,27,12288\nMvaa 140,12,84\nTa 124,26,8192\nMvaa 132,12,80\n" \
"Ta 126,26,9216\nMvaa 128,8,80\nRa 60,27,14336\nMvaa 120,8,76\nMvaa 124,12,76\nMvaa 116,12,72\nMvaa 112,8,72\nMvaa 104,8,68\n" \
"Mvaa 108,12,68\nMvaa 100,12,64\nMvaa 96,8,64\nMvaa 160,16,64\ns_waitcnt lgkmcnt(0)\ns_barrier\nMvaa 164,20,64\ns_add_u32 m0, s88, 131072\n" \
"Mvaa 172,20,68\nD 13,64,0,0\nMvaa 168,16,68\nMvaa 176,16,72\nMvaa 180,20,72\nMvaa 188,20,76\nD 13,64,s73,1024\nMvaa 184,16,76\n" \
"Mvaa 192,16,80\nMvaa 196,20,80\nMvaa 204,20,84\nD 13,64,s74,2048\nMvaa 200,16,84\nMvaa 208,16,88\nMvaa 212,20,88\nMvaa 220,20,92\n" \
"D 13,64,s75,3072\nMvaa 216,16,92\nMaaa 152,24,92\nMaaa 156,28,92\ns_add_u32 m0, s88, 135168\nMaaa 148,28,88\nD 13,64,s76,0\nMaaa 144,24,88\n" \
"Maaa 136,24,84\nMaaa 140,28,84\nMaaa 132,28,80\nD 13,64,s77,1024\nMaaa 128,24,80\ns_branch 99f\n.p2align 12\nzp14:\n" \
"s_setpc_b64 s[98:99]\n99:\nMvaa 248,24,76\nMvaa 252,28,76\nMvaa 244,28,72\nD 13,64,s78,2048\nMvaa 240,24,72\nMvaa 232,24,68\n" \
"Mvaa 236,28,68\nMvaa 228,28,64\nD 13,64,s79,3072\nMvaa 224,24,64\nMvaa 32,32,96\nMvaa 36,36,96\ns_add_u32 m0, s32, 81920\nMvaa 44,36,100\n" \
"D 14,68,0,0\nMvaa 40,32,100\nMvaa 48,32,104\nMvaa 52,36,104\nMvaa 60,36,108\nD 14,68,s89,1024\nMvaa 56,32,108\nMvaa 64,32,112\n" \
"Mvaa 68,36,112\nMvaa 76,36,116\nD 28,68,s90,2048\nMvaa 72,32,116\nMvaa 80,32,120\nMvaa 84,36,120\nMvaa 92,36,124\nD 28,68,s91,3072\n" \
"Mvaa 88,32,124\nMvaa 152,40,124\nMvaa 156,44,124\ns_add_u32 m0, s88, 0\nv_add_u32 v13, 128, v13\nMvaa 148,44,120\nD 13,64,0,0\nMvaa 144,40,120\n" \
"Mvaa 136,40,116\nMvaa 140,44,116\nMvaa 132,44,112\nD 13,64,s73,1024\nMvaa 128,40,112\nMvaa 120,40,108\nMvaa 124,44,108\nMvaa 116,44,104\n" \
"D 13,64,s74,2048\nMvaa 112,40,104\nMvaa 104,40,100\nMvaa 108,44,100\nMvaa 100,44,96\nD 13,64,s75,3072\nMvaa 96,40,96\ns_waitcnt vmcnt(17)\n" \
"s_barrier\nMvaa 160,48,96\nTa 64,8,32768\nMvaa 164,52,96\nTa 66,8,33792\nMvaa 172,52,100\nRa 0,7,32768\nMvaa 168,48,100\n" \
"Ta 68,20,32768\nMvaa 176,48,104\nTa 70,20,33792\nMvaa 180,52,104\nRa 4,7,34816\nMvaa 188,52,108\nTa 72,21,32768\nMvaa 184,48,108\n" \
"Ta 74,21,33792\nMvaa 192,48,112\nRa 8,7,36864\nMvaa 196,52,112\nTa 76,22,32768\nMvaa 204,52,116\nTa 78,22,33792\nMvaa 200,48,116\n" \
"Ra 12,7,38912\nMvaa 208,48,120\nTa 80,23,32768\nMvaa 212,52,120\nTa 82,23,33792\nMvaa 220,52,124\nRa 16,7,40960\nMvaa 216,48,124\n" \
"Ta 84,24,32768\nMaaa 152,56,124\nTa 86,24,33792\nMaaa 156,60,124\nRa 20,7,43008\nMaaa 148,60,120\nTa 88,25,32768\nMaaa 144,56,120\n" \
"Ta 90,25,33792\nMaaa 136,56,116\nRa 24,7,45056\nMaaa 140,60,116\nTa 92,26,32768\nMaaa 132,60,112\nTa 94,26,33792\nMaaa 128,56,112\n" \
"Ra 28,7,47104\nMvaa 248,56,108\nMvaa 252,60,108\nMvaa 244,60,104\nMvaa 240,56,104\nMvaa 232,56,100\nMvaa 236,60,100\nMvaa 228,60,96\n" \
"Mvaa 224,56,96\ns_waitcnt lgkmcnt(0)\nMvaa 32,0,64\nTa 96,8,40960\nMvaa 36,4,64\nTa 98,8,41984\nMvaa 44,4,68\nRa 32,27,32768\n" \
"Mvaa 40,0,68\nTa 100,20,40960\nMvaa 48,0,72\nTa 102,20,41984\nMvaa 52,4,72\nRa 36,27,34816\nMvaa 60,4,76\nTa 104,21,40960\n" \
"Mvaa 56,0,76\nTa 106,21,41984\nMvaa 64,0,80\nRa 40,27,36864\nMvaa 68,4,80\nTa 108,22,40960\nMvaa 76,4,84\nTa 110,22,41984\n" \
"Mvaa 72,0,84\nRa 44,27,38912\nMvaa 80,0,88\nTa 112,23,40960\nMvaa 84,4,88\nTa 114,23,41984\nMvaa 92,4,92\nRa 48,27,40960\n" \
"Mvaa 88,0,92\nTa 116,24,40960\nMvaa 152,8,92\nTa 118,24,41984\nMvaa 156,12,92\nRa 52,27,43008\nMvaa 148,12,88\nTa 120,25,40960\n" \
"Mvaa 144,8,88\nTa 122,25,41984\nMvaa 136,8,84\nRa 56,27,45056\nMvaa 140,12,84\nTa 124,26,40960\nMvaa 132,12,80\nTa 126,26,41984\n" \
"Mvaa 128,8,80\nRa 60,27,47104\nMvaa 120,8,76\ns_and_b32 s96, s32, 0x1000\ns_lshl_b32 s97, s96, 1\ns_lshl_b32 s96, s96, 2\nMvaa 124,12,76\nv_add_u32 v16, s97, v7\n" \
"v_subrev_u32 v20, s96, v8\nv_xor_b32 v17, 64, v16\nMvaa 116,12,72\nv_add_u32 v18, 0x20000, v16\nv_add_u32 v19, 0x20000, v17\nv_xor_b32 v21, 32, v20\nMvaa 112,8,72\nv_xor_b32 v22, 64, v20\n" \
"v_xor_b32 v23, 96, v20\nv_xor_b32 v24, 128, v20\nMvaa 104,8,68\nv_xor_b32 v25, 160, v20\nv_xor_b32 v26, 192, v20\nv_xor_b32 v27, 224, v20\nMvaa 108,12,68\nMvaa 100,12,64\n" \
"Mvaa 96,8,64\nMvaa 160,16,64\ns_waitcnt vmcnt(4) lgkmcnt(0)\ns_barrier\nMvaa 164,20,64\ns_add_u32 m0, s88, 4096\nMvaa 172,20,68\nD 13,64,s76,0\n" \
"Mvaa 168,16,68\nTa 160,20,24576\nMvaa 176,16,72\nTa 162,20,25600\nMvaa 180,20,72\nD 13,64,s77,1024\nMvaa 188,20,76\nTa 164,21,24576\n" \
"Mvaa 184,16,76\nTa 166,21,25600\nMvaa 192,16,80\nD 13,64,s78,2048\nMvaa 196,20,80\nTa 168,22,24576\nMvaa 204,20,84\nTa 170,22,25600\n" \
"Mvaa 200,16,84\nD 13,64,s79,3072\nMvaa 208,16,88\nTa 172,23,24576\nMvaa 212,20,88\ns_add_u32 m0, s32, 65536\nv_add_u32 v14, s72, v14\nv_add_u32 v28, s72, v28\n" \
"Mvaa 220,20,92\nD 14,68,0,0\nMvaa 216,16,92\nTa 174,23,25600\nMaaa 152,24,92\nTa 176,24,24576\nMaaa 156,28,92\nD 14,68,s89,1024\n" \
"Maaa 148,28,88\nTa 178,24,25600\nMaaa 144,24,88\nTa 180,25,24576\nMaaa 136,24,84\nD 28,68,s90,2048\nMaaa 140,28,84\nTa 182,25,25600\n" \
"Maaa 132,28,80\nTa 184,26,24576\nMaaa 128,24,80\nD 28,68,s91,3072\nMvaa 248,24,76\nTa 186,26,25600\nMvaa 252,28,76\ns_add_u32 m0, s88, 32768\n" \
"v_add_u32 v13, 128, v13\nMvaa 244,28,72\nD 13,64,0,0\nMvaa 240,24,72\nTa 188,27,24576\nMvaa 232,24,68\nTa 190,27,25600\nMvaa 236,28,68\n" \
"D 13,64,s73,1024\nMvaa 228,28,64\nMvaa 224,24,64\nMvaa 32,32,96\nD 13,64,s74,2048\nMvaa 36,36,96\nTa 64,20,16384\nMvaa 40,32,100\n" \
"Ta 66,20,17408\nMvaa 44,36,100\nD 13,64,s75,3072\nMvaa 48,32,104\nMvaa 52,36,104\ns_add_u32 m0, s88, 36864\nMvaa 56,32,108\nD 13,64,s76,0\n" \
"Mvaa 60,36,108\nRa 0,18,0\nMvaa 64,32,112\nMvaa 68,36,112\nD 13,64,s77,1024\nMvaa 72,32,116\nRa 16,19,0\nC 32,32,33\n" \
"C 33,34,35\nMvaa 76,36,116\nC 34,36,37\nC 35,38,39\nC 36,40,41\nMvaa 80,32,120\nD 13,64,s78,2048\nC 37,42,43\n" \
"Mvaa 84,36,120\nTa 68,21,16384\nC 38,44,45\nC 39,46,47\nMvaa 88,32,124\nC 40,48,49\nC 41,50,51\nC 42,52,53\n" \
"Mvaa 92,36,124\nD 13,64,s79,3072\nC 43,54,55\nMvaa 96,40,96\nTa 70,21,17408\nC 44,56,57\nC 45,58,59\nMvaa 100,44,96\n" \
"s_add_u32 m0, s32, 98304\nv_add_u32 v14, s72, v14\nv_add_u32 v28, s72, v28\nMvaa 104,40,100\nD 14,68,0,0\nC 46,60,61\nMvaa 108,44,100\nC 47,62,63\n" \
"C 48,64,65\nC 49,66,67\nMvaa 112,40,104\nTa 72,22,16384\nC 50,68,69\nC 51,70,71\nMvaa 116,44,104\nD 14,68,s89,1024\n" \
"C 52,72,73\nMvaa 120,40,108\nC 53,74,75\nC 54,76,77\nC 55,78,79\nMvaa 124,44,108\nTa 74,22,17408\nC 56,80,81\n" \
"C 57,82,83\nMvaa 128,40,112\nD 28,68,s90,2048\nC 58,84,85\nMvaa 132,44,112\nC 59,86,87\nC 60,88,89\nC 61,90,91\n" \
"Mvaa 136,40,116\nTa 76,23,16384\nC 62,92,93\nC 63,94,95\nMvaa 140,44,116\nD 28,68,s91,3072\nC 64,96,97\nMvaa 144,40,120\n" \
"C 65,98,99\nC 66,100,101\nC 67,102,103\nMvaa 148,44,120\nTa 78,23,17408\nC 68,104,105\nC 69,106,107\nMvaa 152,40,124\n" \
"C 70,108,109\nC 71,110,111\nC 72,112,113\nMvaa 156,44,124\nTa 80,24,16384\nC 73,114,115\nC 74,116,117\nMaaa 128,56,112\n" \
"C 75,118,119\nC 76,120,121\nC 77,122,123\nMaaa 132,60,112\nTa 82,24,17408\nC 78,124,125\nC 79,126,127\nMaaa 136,56,116\n" \
"C 80,128,129\nC 81,130,131\nC 82,132,133\nMaaa 140,60,116\nTa 84,25,16384\nC 83,134,135\nC 84,136,137\nMaaa 144,56,120\n" \
"C 85,138,139\nC 86,140,141\nC 87,142,143\nMaaa 148,60,120\nTa 86,25,17408\nC 88,144,145\nC 89,146,147\nMaaa 152,56,124\n" \
"C 90,148,149\nC 91,150,151\nC 92,152,153\nMaaa 156,60,124\nTa 88,26,16384\nC 93,154,155\nC 94,156,157\nMvaa 160,48,96\n" \
"C 95,158,159\nMvaa 164,52,96\nTa 90,26,17408\nMvaa 168,48,100\nAR 96,128\nAR 97,129\nAR 98,130\nMvaa 172,52,100\n" \
"Ta 92,27,16384\nAR 99,131\nAR 100,132\nMvaa 176,48,104\nAR 101,133\nAR 102,134\nAR 103,135\nMvaa 180,52,104\n" \
"Ta 94,27,17408\nC 96,96,97\nC 97,98,99\nMvaa 184,48,108\nC 98,100,101\nC 99,102,103\nAW 240,96\nMvaa 188,52,108\n" \
"Ra 4,18,2048\nAW 241,97\nAW 242,98\nMvaa 192,48,112\nAW 243,99\nAR 104,136\nAR 105,137\nMvaa 196,52,112\n" \
"Ra 20,19,2048\nAR 106,138\nAR 107,139\nMvaa 200,48,116\nAR 108,140\nAR 109,141\nAR 110,142\nMvaa 204,52,116\n" \
"Ra 8,18,4096\nAR 111,143\nC 104,104,105\nMvaa 208,48,120\nC 105,106,107\nC 106,108,109\nC 107,110,111\nMvaa 212,52,120\n" \
"Ra 24,19,4096\nAW 244,104\nAW 245,105\nMvaa 216,48,124\nAW 246,106\nAW 247,107\nAR 112,144\nMvaa 220,52,124\n" \
"Ra 12,18,6144\nAR 113,145\nAR 114,146\nMvaa 224,56,96\nAR 115,147\nAR 116,148\nAR 117,149\nMvaa 228,60,96\n" \
"Ra 28,19,6144\nAR 118,150\nAR 119,151\nMvaa 232,56,100\nC 112,112,113\nC 113,114,115\nC 114,116,117\nMvaa 236,60,100\n" \
"C 115,118,119\nAW 248,112\nAW 249,113\nMvaa 240,56,104\nAW 250,114\nAW 251,115\nAR 120,152\nMvaa 244,60,104\n" \
"AR 121,153\nAR 122,154\nAR 123,155\nMvaa 248,56,108\nAR 124,156\nAR 125,157\nAR 126,158\nMvaa 252,60,108\n" \
"AR 127,159\nC 120,120,121\nC 121,122,123\ns_waitcnt lgkmcnt(6)\ns_branch zY\nzxV:\nMvaa 32,0,64\nTa 96,8,8192\n" \
"s_mov_b64 s[64:65], s[80:81]\nMvaa 36,4,64\nTa 98,8,9216\ns_mov_b32 s66, 4194304\nMvaa 44,4,68\nRa 32,27,0\ns_mov_b64 s[68:69], s[82:83]\nMvaa 40,0,68\n" \
"Ta 100,20,8192\ns_mov_b32 s70, 6291456\nMvaa 48,0,72\nTa 102,20,9216\ns_mov_b32 s73, 15360\nMvaa 52,4,72\nRa 36,27,2048\ns_mov_b32 s74, 30720\n" \
"Mvaa 60,4,76\nTa 104,21,8192\ns_mov_b32 s75, 46080\nMvaa 56,0,76\nTa 106,21,9216\ns_mov_b32 s76, 65536\nMvaa 64,0,80\nRa 40,27,4096\n" \
"s_mov_b32 s77, 80896\nMvaa 68,4,80\nTa 108,22,8192\ns_mov_b32 s78, 96256\nMvaa 76,4,84\nTa 110,22,9216\ns_mov_b32 s79, 111616\nMvaa 72,0,84\n" \
"Ra 44,27,6144\ns_mov_b32 s89, 23552\nMvaa 80,0,88\nTa 112,23,8192\ns_mov_b32 s90, 47104\nMvaa 84,4,88\nTa 114,23,9216\ns_mov_b32 s91, 70656\n" \
"Mvaa 92,4,92\nRa 48,27,8192\ns_mov_b32 s72, 393216\nMvaa 88,0,92\ns_branch 99f\n.p2align 12\nzp15:\ns_setpc_b64 s[98:99]\n" \
"99:\nTa 116,24,8192\ns_lshr_b32 s92, s32, 1\nMvaa 152,8,92\nTa 118,24,9216\nMvaa 156,12,92\nRa 52,27,10240\nMvaa 148,12,88\n" \
"Ta 120,25,8192\nMvaa 144,8,88\nTa 122,25,9216\nMvaa 136,8,84\nRa 56,27,12288\nMvaa 140,12,84\nTa 124,26,8192\nMvaa 132,12,80\n" \
"Ta 126,26,9216\nMvaa 128,8,80\nRa 60,27,14336\nMvaa 120,8,76\nMvaa 124,12,76\nMvaa 116,12,72\nMvaa 112,8,72\nMvaa 104,8,68\n" \
"Mvaa 108,12,68\nMvaa 100,12,64\nMvaa 96,8,64\nMvaa 160,16,64\ns_waitcnt lgkmcnt(0)\ns_barrier\nMvaa 164,20,64\ns_add_u32 m0, s88, 0\n" \
"Mvaa 172,20,68\nD 9,64,0,0\nMvaa 168,16,68\nMvaa 176,16,72\nMvaa 180,20,72\nMvaa 188,20,76\nD 9,64,s73,1024\nMvaa 184,16,76\n" \
"Mvaa 192,16,80\nMvaa 196,20,80\nMvaa 204,20,84\nD 9,64,s74,2048\nMvaa 200,16,84\nMvaa 208,16,88\nMvaa 212,20,88\nMvaa 220,20,92\n" \
"D 9,64,s75,3072\nMvaa 216,16,92\nMaaa 152,24,92\nMaaa 156,28,92\ns_add_u32 m0, s88, 4096\nMaaa 148,28,88\nD 9,64,s76,0\nMaaa 144,24,88\n" \
"Maaa 136,24,84\nMaaa 140,28,84\nMaaa 132,28,80\nD 9,64,s77,1024\nMaaa 128,24,80\nMvaa 248,24,76\nMvaa 252,28,76\nMvaa 244,28,72\n" \
"D 9,64,s78,2048\nMvaa 240,24,72\nMvaa 232,24,68\nMvaa 236,28,68\nMvaa 228,28,64\nD 9,64,s79,3072\nMvaa 224,24,64\nMvaa 32,32,96\n" \
"Mvaa 36,36,96\ns_add_u32 m0, s32, 65536\nMvaa 44,36,100\nD 10,68,0,0\nMvaa 40,32,100\nMvaa 48,32,104\nMvaa 52,36,104\nMvaa 60,36,108\n" \
"D 10,68,s89,1024\nMvaa 56,32,108\nMvaa 64,32,112\nMvaa 68,36,112\nMvaa 76,36,116\nD 11,68,s90,2048\nMvaa 72,32,116\nMvaa 80,32,120\n" \
"Mvaa 84,36,120\nMvaa 92,36,124\nD 11,68,s91,3072\nMvaa 88,32,124\nMvaa 152,40,124\nMvaa 156,44,124\ns_add_u32 m0, s92, 81920\nMvaa 148,44,120\n" \
"D 12,68,0,0\nMvaa 144,40,120\nMvaa 136,40,116\nMvaa 140,44,116\nMvaa 132,44,112\nD 12,68,s89,1024\nMvaa 128,40,112\nMvaa 120,40,108\n" \
"Mvaa 124,44,108\nMvaa 116,44,104\nMvaa 112,40,104\nMvaa 104,40,100\nMvaa 108,44,100\nMvaa 100,44,96\nbuffer_load_dword v30, v4, s[52:55], 0 offen sc1\nMvaa 96,40,96\n" \
"s_waitcnt vmcnt(16)\ns_barrier\nMvaa 160,48,96\nTa 64,8,32768\nv_add_u32 v9, 128, v9\nMvaa 164,52,96\nTa 66,8,33792\nv_add_u32 v10, s72, v10\n" \
"Mvaa 172,52,100\nRa 0,7,32768\nv_add_u32 v11, s72, v11\nMvaa 168,48,100\nTa 68,20,32768\nv_add_u32 v12, s72, v12\nMvaa 176,48,104\nv_add_u32 v4, s44, v4\n" \
"Ta 70,20,33792\nMvaa 180,52,104\nRa 4,7,34816\nMvaa 188,52,108\nTa 72,21,32768\nMvaa 184,48,108\nTa 74,21,33792\nMvaa 192,48,112\n" \
"Ra 8,7,36864\nMvaa 196,52,112\nTa 76,22,32768\nMvaa 204,52,116\nTa 78,22,33792\nMvaa 200,48,116\nRa 12,7,38912\nMvaa 208,48,120\n" \
"Ta 80,23,32768\nMvaa 212,52,120\nTa 82,23,33792\nMvaa 220,52,124\nRa 16,7,40960\nMvaa 216,48,124\nTa 84,24,32768\nMaaa 152,56,124\n" \
"Ta 86,24,33792\nMaaa 156,60,124\nRa 20,7,43008\nMaaa 148,60,120\nTa 88,25,32768\nMaaa 144,56,120\nTa 90,25,33792\nMaaa 136,56,116\n" \
"Ra 24,7,45056\nMaaa 140,60,116\nTa 92,26,32768\nMaaa 132,60,112\nTa 94,26,33792\nMaaa 128,56,112\nRa 28,7,47104\nMvaa 248,56,108\n" \
"Mvaa 252,60,108\nMvaa 244,60,104\nMvaa 240,56,104\nMvaa 232,56,100\nMvaa 236,60,100\nMvaa 228,60,96\nMvaa 224,56,96\ns_waitcnt lgkmcnt(0)\n" \
"Mvaa 32,0,64\nTa 96,8,40960\nMvaa 36,4,64\nTa 98,8,41984\nMvaa 44,4,68\nRa 32,27,32768\nMvaa 40,0,68\nTa 100,20,40960\n" \
"Mvaa 48,0,72\nTa 102,20,41984\nMvaa 52,4,72\nRa 36,27,34816\nMvaa 60,4,76\nTa 104,21,40960\nMvaa 56,0,76\nTa 106,21,41984\n" \
"Mvaa 64,0,80\nRa 40,27,36864\nMvaa 68,4,80\nTa 108,22,40960\nMvaa 76,4,84\nTa 110,22,41984\nMvaa 72,0,84\nRa 44,27,38912\n" \
"Mvaa 80,0,88\nTa 112,23,40960\nMvaa 84,4,88\nTa 114,23,41984\nMvaa 92,4,92\nRa 48,27,40960\nMvaa 88,0,92\nTa 116,24,40960\n" \
"Mvaa 152,8,92\nTa 118,24,41984\nMvaa 156,12,92\nRa 52,27,43008\nMvaa 148,12,88\nTa 120,25,40960\nMvaa 144,8,88\nTa 122,25,41984\n" \
"Mvaa 136,8,84\nRa 56,27,45056\nMvaa 140,12,84\nTa 124,26,40960\nMvaa 132,12,80\nTa 126,26,41984\nMvaa 128,8,80\nRa 60,27,47104\n" \
"Mvaa 120,8,76\ns_and_b32 s96, s32, 0x1000\ns_lshr_b32 s97, s96, 6\nMvaa 124,12,76\ns_lshl_b32 s96, s96, 2\nv_subrev_u32 v20, s96, v8\nMvaa 116,12,72\nv_xor_b32 v20, s97, v20\n" \
"v_xor_b32 v21, 32, v20\nMvaa 112,8,72\nv_xor_b32 v22, 0x80, v20\nv_xor_b32 v23, 0xa0, v20\nMvaa 104,8,68\nMvaa 108,12,68\nMvaa 100,12,64\nMvaa 96,8,64\n" \
"Mvaa 160,16,64\ns_waitcnt lgkmcnt(0)\ns_barrier\nMvaa 164,20,64\ns_add_u32 m0, s88, 32768\nMvaa 172,20,68\nD 9,64,0,0\nMvaa 168,16,68\n" \
"Mvaa 176,16,72\nMvaa 180,20,72\nMvaa 188,20,76\nD 9,64,s73,1024\nMvaa 184,16,76\nMvaa 192,16,80\nMvaa 196,20,80\nMvaa 204,20,84\n" \
"D 9,64,s74,2048\nMvaa 200,16,84\nMvaa 208,16,88\nMvaa 212,20,88\nMvaa 220,20,92\nD 9,64,s75,3072\nMvaa 216,16,92\nMaaa 152,24,92\n" \
"Maaa 156,28,92\ns_add_u32 m0, s88, 36864\nMaaa 148,28,88\nD 9,64,s76,0\nMaaa 144,24,88\nMaaa 136,24,84\nMaaa 140,28,84\nMaaa 132,28,80\n" \
"D 9,64,s77,1024\nMaaa 128,24,80\nMvaa 248,24,76\nMvaa 252,28,76\nMvaa 244,28,72\nD 9,64,s78,2048\nMvaa 240,24,72\nMvaa 232,24,68\n" \
"Mvaa 236,28,68\nMvaa 228,28,64\nD 9,64,s79,3072\nMvaa 224,24,64\nMvaa 32,32,96\nMvaa 36,36,96\ns_add_u32 m0, s32, 98304\nMvaa 40,32,100\n" \
"D 10,68,0,0\nMvaa 44,36,100\nMvaa 48,32,104\nMvaa 52,36,104\nMvaa 56,32,108\nD 10,68,s89,1024\nMvaa 60,36,108\nMvaa 64,32,112\n" \
"Mvaa 68,36,112\nMvaa 72,32,116\nD 11,68,s90,2048\nC 32,32,33\nMvaa 76,36,116\nC 33,34,35\nC 34,36,37\nC 35,38,39\n" \
"Mvaa 80,32,120\nC 36,40,41\nC 37,42,43\nC 38,44,45\nMvaa 84,36,120\nC 39,46,47\nC 40,48,49\nC 41,50,51\n" \
"Mvaa 88,32,124\nD 11,68,s91,3072\nC 42,52,53\nMvaa 92,36,124\nC 43,54,55\nC 44,56,57\nC 45,58,59\nMvaa 96,40,96\n" \
"C 46,60,61\nC 47,62,63\nMvaa 100,44,96\ns_add_u32 m0, s92, 114688\nMvaa 104,40,100\nD 12,68,0,0\nC 48,64,65\nMvaa 108,44,100\n" \
"C 49,66,67\nC 50,68,69\nC 51,70,71\nMvaa 112,40,104\nC 52,72,73\nC 53,74,75\nC 54,76,77\nMvaa 116,44,104\n" \
"C 55,78,79\nC 56,80,81\nC 57,82,83\nMvaa 120,40,108\nD 12,68,s89,1024\nC 58,84,85\nMvaa 124,44,108\nC 59,86,87\n" \
"C 60,88,89\nC 61,90,91\nMvaa 128,40,112\nC 62,92,93\nC 63,94,95\nMvaa 132,44,112\nMvaa 136,40,116\nC 96,96,97\n" \
"C 97,98,99\nC 98,100,101\nMvaa 140,44,116\nC 99,102,103\nAW 160,96\nAW 161,97\nMvaa 144,40,120\nAW 162,98\n" \
"AW 163,99\nC 104,104,105\nMvaa 148,44,120\nC 105,106,107\nC 106,108,109\nC 107,110,111\nMvaa 152,40,124\nbuffer_load_dword v30, v4, s[52:55], 0 offen sc1\n" \
"AW 164,104\nMvaa 156,44,124\ns_waitcnt vmcnt(16)\ns_barrier\nAW 165,105\nMvaa 160,48,96\nTa 64,20,0\nv_add_u32 v9, 128, v9\n" \
"AW 166,106\nMvaa 164,52,96\nTa 66,20,1024\nv_add_u32 v10, s72, v10\nAW 167,107\nMvaa 168,48,100\nRa 0,7,0\nv_add_u32 v11, s72, v11\n" \
"C 112,112,113\nMvaa 172,52,100\nTa 68,21,0\nv_add_u32 v12, s72, v12\nC 113,114,115\nMvaa 176,48,104\nv_add_u32 v4, s44, v4\nTa 70,21,1024\n" \
"C 114,116,117\nMvaa 180,52,104\nRa 4,7,2048\nC 115,118,119\nAW 168,112\nMvaa 184,48,108\nTa 72,22,0\nAW 169,113\n" \
"AW 170,114\nMvaa 188,52,108\nTa 74,22,1024\nAW 171,115\nC 120,120,121\nMvaa 192,48,112\nRa 8,7,4096\nC 121,122,123\n" \
"C 122,124,125\nMvaa 196,52,112\nTa 76,23,0\nC 123,126,127\nAW 172,120\nMvaa 200,48,116\nTa 78,23,1024\nAW 173,121\n" \
"AW 174,122\nMvaa 204,52,116\nRa 12,7,6144\nAW 175,123\nC 128,128,129\nMvaa 208,48,120\nTa 80,20,16384\nC 129,130,131\n" \
"C 130,132,133\nMvaa 212,52,120\nTa 82,20,17408\nC 131,134,135\nAW 176,128\nMvaa 216,48,124\nRa 16,7,8192\nAW 177,129\n" \
"AW 178,130\nMvaa 220,52,124\nTa 84,21,16384\nAW 179,131\nC 136,136,137\nMvaa 224,56,96\nTa 86,21,17408\nC 137,138,139\n" \
"C 138,140,141\nMvaa 228,60,96\nRa 20,7,10240\nC 139,142,143\nAW 180,136\nMvaa 232,56,100\nRa 24,7,12288\nAW 181,137\n" \
"AW 182,138\nMvaa 236,60,100\nRa 28,7,14336\nAW 183,139\nC 144,144,145\nMvaa 240,56,104\nC 145,146,147\nC 146,148,149\n" \
"C 147,150,151\nMvaa 244,60,104\nAW 184,144\nAW 185,145\nAW 186,146\nMvaa 248,56,108\nAW 187,147\nC 152,152,153\n" \
"C 153,154,155\nMvaa 252,60,108\nC 154,156,157\nC 155,158,159\nAW 188,152\nMaaa 128,56,112\nAW 189,153\nAW 190,154\n" \
"AW 191,155\nMaaa 132,60,112\nC 160,160,161\nC 161,162,163\nC 162,164,165\nMaaa 136,56,116\nC 163,166,167\nAW 192,160\n" \
"AW 193,161\nMaaa 140,60,116\nAW 194,162\nAW 195,163\nC 168,168,169\nMaaa 144,56,120\nC 169,170,171\nC 170,172,173\n" \
"C 171,174,175\nMaaa 148,60,120\nAW 196,168\nAW 197,169\nAW 198,170\nMaaa 152,56,124\nAW 199,171\nC 176,176,177\n" \
"C 177,178,179\nMaaa 156,60,124\nC 178,180,181\nC 179,182,183\nAW 200,176\ns_waitcnt lgkmcnt(0)\ns_branch zA\nzxK:\n" \
"Mvaa 32,64,0\nTa 96,8,8192\ns_mov_b64 s[64:65], s[80:81]\nMvaa 64,64,4\nTa 98,8,9216\ns_mov_b32 s66, 4194304\nMvaa 68,68,4\nRa 32,27,0\n" \
"s_mov_b64 s[68:69], s[82:83]\nMvaa 36,68,0\nTa 100,20,8192\ns_mov_b32 s70, 6291456\nMvaa 40,72,0\nTa 102,20,9216\ns_mov_b32 s73, 15360\nMvaa 72,72,4\n" \
"Ra 36,27,2048\ns_mov_b32 s74, 30720\nMvaa 76,76,4\nTa 104,21,8192\ns_mov_b32 s75, 46080\nMvaa 44,76,0\nTa 106,21,9216\ns_branch 99f\n" \
".p2align 12\nzp16:\ns_setpc_b64 s[98:99]\n99:\ns_mov_b32 s76, 65536\nMvaa 48,80,0\nRa 40,27,4096\ns_mov_b32 s77, 80896\n" \
"Mvaa 80,80,4\nTa 108,22,8192\ns_mov_b32 s78, 96256\nMvaa 84,84,4\nTa 110,22,9216\ns_mov_b32 s79, 111616\nMvaa 52,84,0\nRa 44,27,6144\n" \
"s_mov_b32 s89, 23552\nMvaa 56,88,0\nTa 112,23,8192\ns_mov_b32 s90, 47104\nMvaa 88,88,4\nTa 114,23,9216\ns_mov_b32 s91, 70656\nMvaa 92,92,4\n" \
"Ra 48,27,8192\ns_mov_b32 s72, 393216\nMvaa 60,92,0\nTa 116,24,8192\ns_lshr_b32 s92, s32, 1\nMvaa 124,92,8\nTa 118,24,9216\nMvaa 156,92,12\n" \
"Ra 52,27,10240\nMvaa 152,88,12\nTa 120,25,8192\nMvaa 120,88,8\nTa 122,25,9216\nMvaa 116,84,8\nRa 56,27,12288\nMvaa 148,84,12\n" \
"Ta 124,26,8192\nMvaa 144,80,12\nTa 126,26,9216\nMvaa 112,80,8\nRa 60,27,14336\nMvaa 108,76,8\nMvaa 140,76,12\nMvaa 136,72,12\n" \
"Mvaa 104,72,8\nMvaa 100,68,8\nMvaa 132,68,12\nMvaa 128,64,12\nMvaa 96,64,8\nMvaa 160,64,16\ns_waitcnt lgkmcnt(0)\ns_barrier\n" \
"Mvaa 192,64,20\ns_add_u32 m0, s88, 0\nMvaa 196,68,20\nD 9,64,0,0\nMvaa 164,68,16\nMvaa 168,72,16\nMvaa 200,72,20\nMvaa 204,76,20\n" \
"D 9,64,s73,1024\nMvaa 172,76,16\nMvaa 176,80,16\nMvaa 208,80,20\nMvaa 212,84,20\nD 9,64,s74,2048\nMvaa 180,84,16\nMvaa 184,88,16\n" \
"Mvaa 216,88,20\nMvaa 220,92,20\nD 9,64,s75,3072\nMvaa 188,92,16\nMvaa 252,92,24\nMaaa 156,92,28\ns_add_u32 m0, s88, 4096\nMaaa 152,88,28\n" \
"D 9,64,s76,0\nMvaa 248,88,24\nMvaa 244,84,24\nMaaa 148,84,28\nMaaa 144,80,28\nD 9,64,s77,1024\nMvaa 240,80,24\nMvaa 236,76,24\n" \
"Maaa 140,76,28\nMaaa 136,72,28\nD 9,64,s78,2048\nMvaa 232,72,24\nMvaa 228,68,24\nMaaa 132,68,28\nMaaa 128,64,28\nD 9,64,s79,3072\n" \
"Mvaa 224,64,24\nMvaa 32,96,32\nMvaa 64,96,36\ns_add_u32 m0, s32, 65536\nMvaa 68,100,36\nD 10,68,0,0\nMvaa 36,100,32\nMvaa 40,104,32\n" \
"Mvaa 72,104,36\nMvaa 76,108,36\nD 10,68,s89,1024\nMvaa 44,108,32\nMvaa 48,112,32\nMvaa 80,112,36\nMvaa 84,116,36\nD 11,68,s90,2048\n" \
"Mvaa 52,116,32\nMvaa 56,120,32\nMvaa 88,120,36\nMvaa 92,124,36\nD 11,68,s91,3072\nMvaa 60,124,32\nMvaa 124,124,40\nMvaa 156,124,44\n" \
"s_add_u32 m0, s92, 81920\nMvaa 152,120,44\nD 12,68,0,0\nMvaa 120,120,40\nMvaa 116,116,40\nMvaa 148,116,44\nMvaa 144,112,44\nD 12,68,s89,1024\n" \
"Mvaa 112,112,40\nMvaa 108,108,40\nMvaa 140,108,44\nMvaa 136,104,44\nMvaa 104,104,40\nMvaa 100,100,40\nMvaa 132,100,44\nMvaa 128,96,44\n" \
"buffer_load_dword v30, v4, s[52:55], 0 offen sc1\nMvaa 96,96,40\ns_waitcnt vmcnt(16)\ns_barrier\nMvaa 160,96,48\nTa 64,8,32768\nv_add_u32 v9, 128, v9\nMvaa 192,96,52\n" \
"Ta 66,8,33792\nv_add_u32 v10, s72, v10\nMvaa 196,100,52\nRa 0,7,32768\nv_add_u32 v11, s72, v11\nMvaa 164,100,48\nTa 68,20,32768\nv_add_u32 v12, s72, v12\n" \
"Mvaa 168,104,48\nv_add_u32 v4, s44, v4\nTa 70,20,33792\nMvaa 200,104,52\nRa 4,7,34816\nMvaa 204,108,52\nTa 72,21,32768\nMvaa 172,108,48\n" \
"Ta 74,21,33792\nMvaa 176,112,48\nRa 8,7,36864\nMvaa 208,112,52\nTa 76,22,32768\nMvaa 212,116,52\nTa 78,22,33792\nMvaa 180,116,48\n" \
"Ra 12,7,38912\nMvaa 184,120,48\nTa 80,23,32768\nMvaa 216,120,52\nTa 82,23,33792\nMvaa 220,124,52\nRa 16,7,40960\nMvaa 188,124,48\n" \
"Ta 84,24,32768\nMvaa 252,124,56\nTa 86,24,33792\nMaaa 156,124,60\nRa 20,7,43008\nMaaa 152,120,60\nTa 88,25,32768\nMvaa 248,120,56\n" \
"Ta 90,25,33792\nMvaa 244,116,56\nRa 24,7,45056\nMaaa 148,116,60\nTa 92,26,32768\nMaaa 144,112,60\nTa 94,26,33792\nMvaa 240,112,56\n" \
"Ra 28,7,47104\nMvaa 236,108,56\nMaaa 140,108,60\nMaaa 136,104,60\nMvaa 232,104,56\nMvaa 228,100,56\nMaaa 132,100,60\nMaaa 128,96,60\n" \
"Mvaa 224,96,56\ns_waitcnt lgkmcnt(0)\nMvaa 32,64,0\nTa 96,8,40960\nMvaa 64,64,4\nTa 98,8,41984\nMvaa 68,68,4\nRa 32,27,32768\n" \
"Mvaa 36,68,0\nTa 100,20,40960\nMvaa 40,72,0\nTa 102,20,41984\nMvaa 72,72,4\nRa 36,27,34816\nMvaa 76,76,4\nTa 104,21,40960\n" \
"Mvaa 44,76,0\nTa 106,21,41984\nMvaa 48,80,0\nRa 40,27,36864\nMvaa 80,80,4\nTa 108,22,40960\nMvaa 84,84,4\nTa 110,22,41984\n" \
"Mvaa 52,84,0\nRa 44,27,38912\nMvaa 56,88,0\nTa 112,23,40960\nMvaa 88,88,4\nTa 114,23,41984\nMvaa 92,92,4\nRa 48,27,40960\n" \
"Mvaa 60,92,0\nTa 116,24,40960\nMvaa 124,92,8\nTa 118,24,41984\nMvaa 156,92,12\nRa 52,27,43008\nMvaa 152,88,12\nTa 120,25,40960\n" \
"Mvaa 120,88,8\nTa 122,25,41984\nMvaa 116,84,8\nRa 56,27,45056\nMvaa 148,84,12\nTa 124,26,40960\nMvaa 144,80,12\nTa 126,26,41984\n" \
"Mvaa 112,80,8\nRa 60,27,47104\nMvaa 108,76,8\ns_and_b32 s96, s32, 0x1000\ns_lshr_b32 s97, s96, 6\nMvaa 140,76,12\ns_lshl_b32 s96, s96, 2\nv_subrev_u32 v20, s96, v8\n" \
"Mvaa 136,72,12\nv_xor_b32 v20, s97, v20\nv_xor_b32 v21, 32, v20\nMvaa 104,72,8\nv_xor_b32 v22, 0x80, v20\nv_xor_b32 v23, 0xa0, v20\nMvaa 100,68,8\nMvaa 132,68,12\n" \
"Mvaa 128,64,12\nMvaa 96,64,8\nMvaa 160,64,16\ns_waitcnt lgkmcnt(0)\ns_barrier\nMvaa 192,64,20\ns_add_u32 m0, s88, 32768\nMvaa 196,68,20\n" \
"D 9,64,0,0\nMvaa 164,68,16\nMvaa 168,72,16\nMvaa 200,72,20\nMvaa 204,76,20\nD 9,64,s73,1024\nMvaa 172,76,16\nMvaa 176,80,16\n" \
"Mvaa 208,80,20\nMvaa 212,84,20\nD 9,64,s74,2048\nMvaa 180,84,16\nMvaa 184,88,16\nMvaa 216,88,20\nMvaa 220,92,20\nD 9,64,s75,3072\n" \
"Mvaa 188,92,16\nMvaa 252,92,24\nMaaa 156,92,28\ns_add_u32 m0, s88, 36864\nMaaa 152,88,28\nD 9,64,s76,0\nMvaa 248,88,24\nMvaa 244,84,24\n" \
"Maaa 148,84,28\nMaaa 144,80,28\nD 9,64,s77,1024\nMvaa 240,80,24\nMvaa 236,76,24\nMaaa 140,76,28\nMaaa 136,72,28\nD 9,64,s78,2048\n" \
"Mvaa 232,72,24\nMvaa 228,68,24\nMaaa 132,68,28\nMaaa 128,64,28\nD 9,64,s79,3072\nMvaa 224,64,24\nMvaa 32,96,32\nMvaa 36,100,32\n" \
"s_add_u32 m0, s32, 98304\nMvaa 40,104,32\nD 10,68,0,0\nMvaa 44,108,32\nMvaa 48,112,32\nMvaa 52,116,32\nMvaa 56,120,32\nD 10,68,s89,1024\n" \
"Mvaa 60,124,32\nMvaa 64,96,36\nMvaa 68,100,36\nMvaa 72,104,36\nD 11,68,s90,2048\nC 32,32,33\nMvaa 76,108,36\nC 33,34,35\n" \
"C 34,36,37\nC 35,38,39\nMvaa 80,112,36\nC 36,40,41\nC 37,42,43\nC 38,44,45\nMvaa 84,116,36\nC 39,46,47\n" \
"C 40,48,49\nC 41,50,51\nMvaa 88,120,36\nD 11,68,s91,3072\nC 42,52,53\nMvaa 92,124,36\nC 43,54,55\nC 44,56,57\n" \
"C 45,58,59\nMvaa 96,96,40\nC 46,60,61\nC 47,62,63\nMvaa 100,100,40\ns_add_u32 m0, s92, 114688\nMvaa 104,104,40\nD 12,68,0,0\n" \
"C 48,64,65\nMvaa 108,108,40\nC 49,66,67\nC 50,68,69\nC 51,70,71\nMvaa 112,112,40\nC 52,72,73\nC 53,74,75\n" \
"C 54,76,77\nMvaa 116,116,40\nC 55,78,79\nC 56,80,81\nC 57,82,83\nMvaa 120,120,40\nD 12,68,s89,1024\nC 58,84,85\n" \
"Mvaa 124,124,40\nC 59,86,87\nC 60,88,89\nC 61,90,91\nMvaa 128,96,44\nC 62,92,93\nC 63,94,95\nMvaa 132,100,44\n" \
"Mvaa 136,104,44\nC 96,96,97\nC 97,98,99\nC 98,100,101\nMvaa 140,108,44\nC 99,102,103\nAW 160,96\nAW 161,97\n" \
"Mvaa 144,112,44\nAW 162,98\nAW 163,99\nC 104,104,105\nMvaa 148,116,44\nC 105,106,107\nC 106,108,109\nC 107,110,111\n" \
"Mvaa 152,120,44\nbuffer_load_dword v30, v4, s[52:55], 0 offen sc1\nAW 164,104\nMvaa 156,124,44\ns_waitcnt vmcnt(16)\ns_barrier\nAW 165,105\nMvaa 160,96,48\n" \
"Ta 64,20,0\nv_add_u32 v9, 128, v9\nAW 166,106\nMvaa 164,100,48\nTa 66,20,1024\nv_add_u32 v10, s72, v10\nAW 167,107\nMvaa 168,104,48\n" \
"Ra 0,7,0\nv_add_u32 v11, s72, v11\nC 112,112,113\nMvaa 172,108,48\nTa 68,21,0\nv_add_u32 v12, s72, v12\nC 113,114,115\nMvaa 176,112,48\n" \
"v_add_u32 v4, s44, v4\nTa 70,21,1024\nC 114,116,117\nMvaa 180,116,48\nRa 4,7,2048\nC 115,118,119\nAW 168,112\nMvaa 184,120,48\n" \
"Ta 72,22,0\nAW 169,113\nAW 170,114\nMvaa 188,124,48\nTa 74,22,1024\nAW 171,115\nC 120,120,121\nMvaa 192,96,52\n" \
"Ra 8,7,4096\nC 121,122,123\nC 122,124,125\nMvaa 196,100,52\nTa 76,23,0\nC 123,126,127\nAW 172,120\nMvaa 200,104,52\n" \
"Ta 78,23,1024\nAW 173,121\nAW 174,122\nMvaa 204,108,52\nRa 12,7,6144\nAW 175,123\nC 128,128,129\nMvaa 208,112,52\n" \
"Ta 80,20,16384\nC 129,130,131\nC 130,132,133\nMvaa 212,116,52\nTa 82,20,17408\nC 131,134,135\nAW 176,128\nMvaa 216,120,52\n" \
"Ra 16,7,8192\nAW 177,129\nAW 178,130\nMvaa 220,124,52\nTa 84,21,16384\nAW 179,131\nC 136,136,137\nMvaa 224,96,56\n" \
"Ta 86,21,17408\nC 137,138,139\nC 138,140,141\nMvaa 228,100,56\nRa 20,7,10240\nC 139,142,143\nAW 180,136\nMvaa 232,104,56\n" \
"Ra 24,7,12288\nAW 181,137\nAW 182,138\nMvaa 236,108,56\nRa 28,7,14336\nAW 183,139\nC 144,144,145\nMvaa 240,112,56\n" \
"C 145,146,147\nC 146,148,149\nC 147,150,151\nMvaa 244,116,56\nAW 184,144\nAW 185,145\nAW 186,146\nMvaa 248,120,56\n" \
"AW 187,147\nC 152,152,153\nC 153,154,155\nMvaa 252,124,56\nC 154,156,157\nC 155,158,159\nAW 188,152\nMaaa 128,96,60\n" \
"AW 189,153\nAW 190,154\nAW 191,155\nMaaa 132,100,60\nC 160,160,161\nC 161,162,163\nC 162,164,165\nMaaa 136,104,60\n" \
"C 163,166,167\nAW 192,160\nAW 193,161\nMaaa 140,108,60\nAW 194,162\nAW 195,163\nC 168,168,169\nMaaa 144,112,60\n" \
"C 169,170,171\nC 170,172,173\nC 171,174,175\nMaaa 148,116,60\nAW 196,168\nAW 197,169\nAW 198,170\nMaaa 152,120,60\n" \
"AW 199,171\nC 176,176,177\nC 177,178,179\nMaaa 156,124,60\nC 178,180,181\nC 179,182,183\nAW 200,176\ns_branch 99f\n" \
".p2align 12\nzp17:\ns_setpc_b64 s[98:99]\n99:\ns_waitcnt lgkmcnt(0)\nzA:\nZvaa 64,64,0\nTa 96,20,8192\n" \
"AW 201,177\nAW 202,178\nZvaa 88,64,4\nTa 98,20,9216\nAW 203,179\nC 184,184,185\nZvaa 92,68,4\nRa 32,27,0\n" \
"C 185,186,187\nC 186,188,189\nZvaa 68,68,0\nTa 100,21,8192\nC 187,190,191\nAW 204,184\nZvaa 72,72,0\nTa 102,21,9216\n" \
"AW 205,185\nAW 206,186\nZvaa 96,72,4\nRa 36,27,2048\nAW 207,187\nC 192,192,193\nZvaa 100,76,4\nTa 104,22,8192\n" \
"C 193,194,195\nC 194,196,197\nZvaa 76,76,0\nTa 106,22,9216\nC 195,198,199\nAW 208,192\nZvaa 80,0,80\nRa 40,27,4096\n" \
"AW 209,193\nAW 210,194\nZvaa 104,4,80\nTa 108,23,8192\nAW 211,195\nC 200,200,201\nZvaa 108,4,84\nTa 110,23,9216\n" \
"C 201,202,203\nC 202,204,205\nZvaa 84,0,84\nRa 44,27,6144\nC 203,206,207\nAW 212,200\nZvaa 132,8,84\nTa 112,22,16384\n" \
"AW 213,201\nAW 214,202\nZvaa 156,12,84\nTa 114,22,17408\nAW 215,203\nC 208,208,209\nZvaa 152,12,80\nRa 48,27,8192\n" \
"C 209,210,211\nC 210,212,213\nZvaa 128,8,80\nTa 116,23,16384\nC 211,214,215\nAW 216,208\nZvaa 124,76,8\nTa 118,23,17408\n" \
"AW 217,209\nAW 218,210\nZvaa 148,76,12\nRa 52,27,10240\nAW 219,211\nC 216,216,217\nZvaa 144,72,12\nRa 56,27,12288\n" \
"C 217,218,219\nC 218,220,221\nZvaa 120,72,8\nRa 60,27,14336\nC 219,222,223\nAW 220,216\nZvaa 116,68,8\nAW 221,217\n" \
"AW 222,218\nAW 223,219\nZvaa 140,68,12\nC 224,224,225\nC 225,226,227\nC 226,228,229\nZvaa 136,64,12\nC 227,230,231\n" \
"AW 224,224\nAW 225,225\nZvaa 112,64,8\nAW 226,226\nAW 227,227\nC 232,232,233\nZvaa 160,64,16\nC 233,234,235\n" \
"C 234,236,237\nC 235,238,239\nZvaa 184,64,20\nAW 228,232\nAW 229,233\nAW 230,234\nZvaa 188,68,20\ns_waitcnt lgkmcnt(0)\n" \
"s_barrier\nAW 231,235\nZvaa 164,68,16\ns_add_u32 m0, s88, 0\nC 240,240,241\nC 241,242,243\nZvaa 168,72,16\nD 9,64,0,0\n" \
"C 242,244,245\nZvaa 192,72,20\nC 243,246,247\nAW 232,240\nAW 233,241\nZvaa 196,76,20\nAW 234,242\nAW 235,243\n" \
"C 248,248,249\nZvaa 172,76,16\nC 249,250,251\nC 250,252,253\nC 251,254,255\nZvaa 176,16,80\nD 9,64,s73,1024\nAW 236,248\n" \
"Zvaa 200,20,80\nAW 237,249\nAW 238,250\nAW 239,251\nZvaa 204,20,84\nZvaa 180,16,84\nZvaa 228,24,84\nD 9,64,s74,2048\n" \
"Zvaa 252,28,84\ns_add_u32 s95, s34, 0\nZvaa 248,28,80\nbuffer_store_dwordx4 v[32:35], v0, s[60:63], s95 offen offset:0 sc1\nZvaa 224,24,80\nZvaa 220,76,24\nD 9,64,s75,3072\nZvaa 244,76,28\n" \
"Zvaa 240,72,28\nZvaa 216,72,24\ns_add_u32 m0, s88, 4096\nZvaa 212,68,24\nD 9,64,s76,0\nZvaa 236,68,28\nZvaa 232,64,28\nZvaa 208,64,24\n" \
"Mvaa 64,96,32\nD 9,64,s77,1024\nMvaa 88,96,36\nMvaa 92,100,36\nbuffer_store_dwordx4 v[36:39], v0, s[60:63], s95 offen offset:1024 sc1\nMvaa 68,100,32\nMvaa 72,104,32\nD 9,64,s78,2048\n" \
"Mvaa 96,104,36\nMvaa 100,108,36\nMvaa 76,108,32\nMvaa 80,32,112\nD 9,64,s79,3072\nMvaa 104,36,112\nMvaa 108,36,116\nMvaa 84,32,116\n" \
"s_add_u32 m0, s32, 65536\nMvaa 132,40,116\nD 10,68,0,0\nMvaa 156,44,116\nMvaa 152,44,112\nbuffer_store_dwordx4 v[40:43], v0, s[60:63], s95 offen offset:2048 sc1\nMvaa 128,40,112\nMvaa 124,108,40\n" \
"D 10,68,s89,1024\nMvaa 148,108,44\nMvaa 144,104,44\nMvaa 120,104,40\nMvaa 116,100,40\nD 11,68,s90,2048\nMvaa 140,100,44\nMvaa 136,96,44\n" \
"s_waitcnt vmcnt(15)\ns_barrier\nMvaa 112,96,40\nTa 64,20,32768\nMvaa 160,96,48\nD 11,68,s91,3072\nTa 66,20,33792\nMvaa 184,96,52\n" \
"Ra 0,7,32768\nMvaa 188,100,52\nTa 68,21,32768\nMvaa 164,100,48\ns_add_u32 m0, s92, 81920\nTa 70,21,33792\nMvaa 168,104,48\nD 12,68,0,0\n" \
"Ra 4,7,34816\nMvaa 192,104,52\nTa 72,22,32768\nMvaa 196,108,52\nTa 74,22,33792\nMvaa 172,108,48\nRa 8,7,36864\nMvaa 176,48,112\n" \
"D 12,68,s89,1024\nTa 76,23,32768\nMvaa 200,52,112\nTa 78,23,33792\nv_add_u32 v9, 128, v9\nMvaa 204,52,116\nRa 12,7,38912\nv_add_u32 v10, s72, v10\n" \
"Mvaa 180,48,116\nTa 80,20,49152\nv_add_u32 v11, s72, v11\nMvaa 228,56,116\nTa 82,20,50176\nv_add_u32 v12, s72, v12\nMvaa 252,60,116\nRa 16,7,40960\n" \
"Mvaa 248,60,112\nTa 84,21,49152\nMvaa 224,56,112\nTa 86,21,50176\nMvaa 220,108,56\nRa 20,7,43008\nMvaa 244,108,60\nRa 24,7,45056\n" \
"Mvaa 240,104,60\nRa 28,7,47104\nMvaa 216,104,56\nbuffer_store_dwordx4 v[44:47], v0, s[60:63], s95 offen offset:3072 sc1\nMvaa 212,100,56\ns_add_u32 s95, s34, 4096\nMvaa 236,100,60\nbuffer_store_dwordx4 v[48:51], v0, s[60:63], s95 offen offset:0 sc1\n" \
"Mvaa 232,96,60\nMvaa 208,96,56\ns_waitcnt lgkmcnt(0)\nMvaa 64,64,0\nTa 96,20,40960\nMvaa 88,64,4\nTa 98,20,41984\nMvaa 92,68,4\n" \
"Ra 32,27,32768\nMvaa 68,68,0\nTa 100,21,40960\nMvaa 72,72,0\nTa 102,21,41984\nMvaa 96,72,4\nRa 36,27,34816\nMvaa 100,76,4\n" \
"Ta 104,22,40960\nMvaa 76,76,0\nTa 106,22,41984\nMvaa 80,0,80\nRa 40,27,36864\nMvaa 104,4,80\nTa 108,23,40960\nMvaa 108,4,84\n" \
"Ta 110,23,41984\nMvaa 84,0,84\nRa 44,27,38912\nMvaa 132,8,84\nTa 112,22,49152\nMvaa 156,12,84\nTa 114,22,50176\nMvaa 152,12,80\n" \
"Ra 48,27,40960\nMvaa 128,8,80\nTa 116,23,49152\nMvaa 124,76,8\nTa 118,23,50176\nMvaa 148,76,12\nRa 52,27,43008\nMvaa 144,72,12\n" \
"Ra 56,27,45056\nMvaa 120,72,8\nRa 60,27,47104\nMvaa 116,68,8\nbuffer_store_dwordx4 v[52:55], v0, s[60:63], s95 offen offset:1024 sc1\nMvaa 140,68,12\nMvaa 136,64,12\nbuffer_store_dwordx4 v[56:59], v0, s[60:63], s95 offen offset:2048 sc1\n" \
"Mvaa 112,64,8\nMvaa 160,64,16\nMvaa 184,64,20\nMvaa 188,68,20\ns_waitcnt lgkmcnt(0)\ns_barrier\nMvaa 164,68,16\ns_add_u32 m0, s88, 32768\n" \
"Mvaa 168,72,16\nD 9,64,0,0\nMvaa 192,72,20\nMvaa 196,76,20\nMvaa 172,76,16\nMvaa 176,16,80\nD 9,64,s73,1024\nMvaa 200,20,80\n" \
"Mvaa 204,20,84\nbuffer_store_dwordx4 v[60:63], v0, s[60:63], s95 offen offset:3072 sc1\nMvaa 180,16,84\nMvaa 228,24,84\nD 9,64,s74,2048\nMvaa 252,28,84\nMvaa 248,28,80\nMvaa 224,24,80\n" \
"Mvaa 220,76,24\nD 9,64,s75,3072\nMvaa 244,76,28\nMvaa 240,72,28\nMvaa 216,72,24\ns_add_u32 m0, s88, 36864\nMvaa 212,68,24\nD 9,64,s76,0\n" \
"Mvaa 236,68,28\nMvaa 232,64,28\ns_add_u32 s95, s34, 8192\nMvaa 208,64,24\nbuffer_store_dwordx4 a[160:163], v0, s[60:63], s95 offen offset:0 sc1\nMvaa 64,96,32\nD 9,64,s77,1024\nMvaa 88,96,36\n" \
"AR 48,128\nAR 49,129\nMvaa 92,100,36\nAR 50,130\nAR 51,131\nMvaa 68,100,32\nAR 52,132\nAR 53,133\n" \
"Mvaa 72,104,32\nD 9,64,s78,2048\nMvaa 96,104,36\nAR 54,134\nAR 55,135\nMvaa 100,108,36\nC 32,48,49\nC 33,50,51\n" \
"Mvaa 76,108,32\nC 34,52,53\nC 35,54,55\nMvaa 80,32,112\nD 9,64,s79,3072\nMvaa 104,36,112\nAR 56,136\nAR 57,137\n" \
"Mvaa 108,36,116\nAR 58,138\nAR 59,139\nMvaa 84,32,116\ns_add_u32 m0, s32, 98304\nAR 60,140\nMvaa 132,40,116\nD 10,68,0,0\n" \
"Mvaa 156,44,116\nAR 61,141\nAR 62,142\nMvaa 152,44,112\nAR 63,143\nC 36,56,57\nMvaa 128,40,112\nbuffer_store_dwordx4 a[164:167], v0, s[60:63], s95 offen offset:1024 sc1\n" \
"Mvaa 124,108,40\nD 10,68,s89,1024\nMvaa 148,108,44\nC 37,58,59\nC 38,60,61\nMvaa 144,104,44\nC 39,62,63\nAR 48,144\n" \
"Mvaa 120,104,40\nAR 49,145\nAR 50,146\nMvaa 116,100,40\nD 11,68,s90,2048\nMvaa 140,100,44\nAR 51,147\nAR 52,148\n" \
"Mvaa 136,96,44\ns_waitcnt vmcnt(18)\ns_barrier\nMvaa 112,96,40\nTa 64,20,0\nAR 53,149\nMvaa 160,96,48\nD 11,68,s91,3072\n" \
"Ta 66,20,1024\nMvaa 184,96,52\nRa 0,7,0\nAR 54,150\nMvaa 188,100,52\nTa 68,21,0\nAR 55,151\nMvaa 164,100,48\n" \
"s_add_u32 m0, s92, 114688\nTa 70,21,1024\nMvaa 168,104,48\nD 12,68,0,0\nRa 4,7,2048\nMvaa 192,104,52\nTa 72,22,0\nC 40,48,49\n" \
"Mvaa 196,108,52\nTa 74,22,1024\nC 41,50,51\nMvaa 172,108,48\nRa 8,7,4096\nC 42,52,53\nMvaa 176,48,112\nD 12,68,s89,1024\n" \
"Ta 76,23,0\nMvaa 200,52,112\nTa 78,23,1024\nv_add_u32 v9, 128, v9\nMvaa 204,52,116\nRa 12,7,6144\nv_add_u32 v10, s72, v10\nMvaa 180,48,116\n" \
"Ta 80,20,16384\nv_add_u32 v11, s72, v11\nMvaa 228,56,116\nTa 82,20,17408\nv_add_u32 v12, s72, v12\nMvaa 252,60,116\nRa 16,7,8192\nC 43,54,55\n" \
"Mvaa 248,60,112\nTa 84,21,16384\nAR 56,152\nMvaa 224,56,112\nTa 86,21,17408\nAR 57,153\nMvaa 220,108,56\nRa 20,7,10240\n" \
"AR 58,154\nMvaa 244,108,60\nRa 24,7,12288\nAR 59,155\nMvaa 240,104,60\nRa 28,7,14336\nAR 60,156\nMvaa 216,104,56\n" \
"buffer_store_dwordx4 a[168:171], v0, s[60:63], s95 offen offset:2048 sc1\nMvaa 212,100,56\nAR 61,157\nAR 62,158\nMvaa 236,100,60\nAR 63,159\nC 44,56,57\nMvaa 232,96,60\n" \
"C 45,58,59\nC 46,60,61\nMvaa 208,96,56\ns_waitcnt lgkmcnt(0)\nC 47,62,63\nMvaa 64,64,0\nTa 96,20,8192\nMvaa 88,64,4\n" \
"Ta 98,20,9216\nMvaa 92,68,4\nRa 32,27,0\nMvaa 68,68,0\nTa 100,21,8192\nMvaa 72,72,0\nTa 102,21,9216\nMvaa 96,72,4\n" \
"Ra 36,27,2048\nMvaa 100,76,4\nTa 104,22,8192\nMvaa 76,76,0\nTa 106,22,9216\nMvaa 80,0,80\nRa 40,27,4096\nMvaa 104,4,80\n" \
"Ta 108,23,8192\nMvaa 108,4,84\nTa 110,23,9216\nMvaa 84,0,84\nRa 44,27,6144\nMvaa 132,8,84\nTa 112,22,16384\nMvaa 156,12,84\n" \
"Ta 114,22,17408\nMvaa 152,12,80\nRa 48,27,8192\nMvaa 128,8,80\nTa 116,23,16384\nMvaa 124,76,8\nTa 118,23,17408\nMvaa 148,76,12\n" \
"Ra 52,27,10240\nMvaa 144,72,12\nRa 56,27,12288\nMvaa 120,72,8\nRa 60,27,14336\nMvaa 116,68,8\nbuffer_store_dwordx4 a[172:175], v0, s[60:63], s95 offen offset:3072 sc1\nMvaa 140,68,12\n" \
"s_add_u32 s95, s34, 12288\nMvaa 136,64,12\nbuffer_store_dwordx4 a[176:179], v0, s[60:63], s95 offen offset:0 sc1\nMvaa 112,64,8\nMvaa 160,64,16\nMvaa 184,64,20\nMvaa 188,68,20\ns_waitcnt lgkmcnt(0)\n" \
"s_barrier\nMvaa 164,68,16\ns_add_u32 m0, s88, 0\nMvaa 168,72,16\nD 9,64,0,0\nMvaa 192,72,20\nMvaa 196,76,20\ns_branch 99f\n" \
".p2align 12\nzp18:\ns_setpc_b64 s[98:99]\n99:\nMvaa 172,76,16\nMvaa 176,16,80\nD 9,64,s73,1024\nMvaa 200,20,80\n" \
"Mvaa 204,20,84\nbuffer_store_dwordx4 a[180:183], v0, s[60:63], s95 offen offset:1024 sc1\nMvaa 180,16,84\nMvaa 228,24,84\nD 9,64,s74,2048\nMvaa 252,28,84\nMvaa 248,28,80\nMvaa 224,24,80\n" \
"Mvaa 220,76,24\nD 9,64,s75,3072\nMvaa 244,76,28\nMvaa 240,72,28\nMvaa 216,72,24\ns_add_u32 m0, s88, 4096\nMvaa 212,68,24\nD 9,64,s76,0\n" \
"Mvaa 236,68,28\nMvaa 232,64,28\nMvaa 208,64,24\nbuffer_store_dwordx4 a[184:187], v0, s[60:63], s95 offen offset:2048 sc1\nMvaa 64,96,32\nD 9,64,s77,1024\nMvaa 88,96,36\nMvaa 92,100,36\n" \
"Mvaa 68,100,32\nMvaa 72,104,32\nD 9,64,s78,2048\nMvaa 96,104,36\nMvaa 100,108,36\nMvaa 76,108,32\nMvaa 80,32,112\nD 9,64,s79,3072\n" \
"Mvaa 104,36,112\nMvaa 108,36,116\nMvaa 84,32,116\ns_add_u32 m0, s32, 65536\nMvaa 132,40,116\nD 10,68,0,0\nMvaa 156,44,116\nMvaa 152,44,112\n" \
"Mvaa 128,40,112\nbuffer_store_dwordx4 a[188:191], v0, s[60:63], s95 offen offset:3072 sc1\nMvaa 124,108,40\nD 10,68,s89,1024\nMvaa 148,108,44\nMvaa 144,104,44\nMvaa 120,104,40\nMvaa 116,100,40\n" \
"D 11,68,s90,2048\nMvaa 140,100,44\nMvaa 136,96,44\ns_waitcnt vmcnt(17)\ns_barrier\nMvaa 112,96,40\nTa 64,20,32768\nMvaa 160,96,48\n" \
"D 11,68,s91,3072\nTa 66,20,33792\nMvaa 184,96,52\nRa 0,7,32768\nMvaa 188,100,52\nTa 68,21,32768\nMvaa 164,100,48\ns_add_u32 m0, s92, 81920\n" \
"Ta 70,21,33792\nMvaa 168,104,48\nD 12,68,0,0\nRa 4,7,34816\nMvaa 192,104,52\nTa 72,22,32768\nMvaa 196,108,52\nTa 74,22,33792\n" \
"Mvaa 172,108,48\nRa 8,7,36864\nMvaa 176,48,112\nD 12,68,s89,1024\nTa 76,23,32768\nMvaa 200,52,112\nTa 78,23,33792\nv_add_u32 v9, 128, v9\n" \
"Mvaa 204,52,116\nRa 12,7,38912\nv_add_u32 v10, s72, v10\nMvaa 180,48,116\nTa 80,20,49152\nv_add_u32 v11, s72, v11\nMvaa 228,56,116\nTa 82,20,50176\n" \
"v_add_u32 v12, s72, v12\nMvaa 252,60,116\nRa 16,7,40960\nMvaa 248,60,112\nTa 84,21,49152\nMvaa 224,56,112\nTa 86,21,50176\nMvaa 220,108,56\n" \
"Ra 20,7,43008\nMvaa 244,108,60\nRa 24,7,45056\nMvaa 240,104,60\nRa 28,7,47104\ns_add_u32 s95, s34, 16384\nMvaa 216,104,56\nbuffer_store_dwordx4 a[192:195], v0, s[60:63], s95 offen offset:0 sc1\n" \
"Mvaa 212,100,56\nMvaa 236,100,60\nMvaa 232,96,60\nMvaa 208,96,56\ns_waitcnt lgkmcnt(0)\nMvaa 64,64,0\nTa 96,20,40960\nMvaa 88,64,4\n" \
"Ta 98,20,41984\nMvaa 92,68,4\nRa 32,27,32768\nMvaa 68,68,0\nTa 100,21,40960\nMvaa 72,72,0\nTa 102,21,41984\nMvaa 96,72,4\n" \
"Ra 36,27,34816\nMvaa 100,76,4\nTa 104,22,40960\nMvaa 76,76,0\nTa 106,22,41984\nMvaa 80,0,80\nRa 40,27,36864\nMvaa 104,4,80\n" \
"Ta 108,23,40960\nMvaa 108,4,84\nTa 110,23,41984\nMvaa 84,0,84\nRa 44,27,38912\nMvaa 132,8,84\nTa 112,22,49152\nMvaa 156,12,84\n" \
"Ta 114,22,50176\nMvaa 152,12,80\nRa 48,27,40960\nMvaa 128,8,80\nTa 116,23,49152\nMvaa 124,76,8\nTa 118,23,50176\nMvaa 148,76,12\n" \
"Ra 52,27,43008\nMvaa 144,72,12\nRa 56,27,45056\nMvaa 120,72,8\nRa 60,27,47104\nMvaa 116,68,8\nbuffer_store_dwordx4 a[196:199], v0, s[60:63], s95 offen offset:1024 sc1\nMvaa 140,68,12\n" \
"Mvaa 136,64,12\nbuffer_store_dwordx4 a[200:203], v0, s[60:63], s95 offen offset:2048 sc1\nMvaa 112,64,8\nMvaa 160,64,16\nMvaa 184,64,20\nMvaa 188,68,20\ns_waitcnt lgkmcnt(0)\ns_barrier\n" \
"Mvaa 164,68,16\ns_add_u32 m0, s88, 32768\nMvaa 168,72,16\nD 9,64,0,0\nMvaa 192,72,20\nMvaa 196,76,20\nMvaa 172,76,16\nMvaa 176,16,80\n" \
"D 9,64,s73,1024\nMvaa 200,20,80\nMvaa 204,20,84\nbuffer_store_dwordx4 a[204:207], v0, s[60:63], s95 offen offset:3072 sc1\nMvaa 180,16,84\nMvaa 228,24,84\nD 9,64,s74,2048\nMvaa 252,28,84\n" \
"Mvaa 248,28,80\nMvaa 224,24,80\nMvaa 220,76,24\nD 9,64,s75,3072\nMvaa 244,76,28\nMvaa 240,72,28\nMvaa 216,72,24\ns_add_u32 m0, s88, 36864\n" \
"Mvaa 212,68,24\nD 9,64,s76,0\nMvaa 236,68,28\nMvaa 232,64,28\ns_add_u32 s95, s34, 20480\nMvaa 208,64,24\nbuffer_store_dwordx4 a[208:211], v0, s[60:63], s95 offen offset:0 sc1\nMvaa 64,96,32\n" \
"D 9,64,s77,1024\nMvaa 88,96,36\nMvaa 92,100,36\nMvaa 68,100,32\nMvaa 72,104,32\nD 9,64,s78,2048\nMvaa 96,104,36\nMvaa 100,108,36\n" \
"Mvaa 76,108,32\nMvaa 80,32,112\nD 9,64,s79,3072\nMvaa 104,36,112\nMvaa 108,36,116\nMvaa 84,32,116\ns_add_u32 m0, s32, 98304\nMvaa 132,40,116\n" \
"D 10,68,0,0\nMvaa 156,44,116\nMvaa 152,44,112\nMvaa 128,40,112\nbuffer_store_dwordx4 a[212:215], v0, s[60:63], s95 offen offset:1024 sc1\nMvaa 124,108,40\nD 10,68,s89,1024\nMvaa 148,108,44\n" \
"Mvaa 144,104,44\nMvaa 120,104,40\nMvaa 116,100,40\nD 11,68,s90,2048\nMvaa 140,100,44\nMvaa 136,96,44\ns_waitcnt vmcnt(17)\ns_barrier\n" \
"Mvaa 112,96,40\nTa 64,20,0\nMvaa 160,96,48\nD 11,68,s91,3072\nTa 66,20,1024\nMvaa 184,96,52\nRa 0,7,0\nMvaa 188,100,52\n" \
"Ta 68,21,0\nMvaa 164,100,48\ns_add_u32 m0, s92, 114688\nTa 70,21,1024\nMvaa 168,104,48\nD 12,68,0,0\nRa 4,7,2048\nMvaa 192,104,52\n" \
"Ta 72,22,0\nMvaa 196,108,52\nTa 74,22,1024\nMvaa 172,108,48\nRa 8,7,4096\nMvaa 176,48,112\nD 12,68,s89,1024\nTa 76,23,0\n" \
"Mvaa 200,52,112\nTa 78,23,1024\nv_add_u32 v9, 128, v9\nMvaa 204,52,116\nRa 12,7,6144\nv_add_u32 v10, s72, v10\nMvaa 180,48,116\nTa 80,20,16384\n" \
"v_add_u32 v11, s72, v11\nMvaa 228,56,116\nTa 82,20,17408\nv_add_u32 v12, s72, v12\nMvaa 252,60,116\nRa 16,7,8192\nMvaa 248,60,112\nTa 84,21,16384\n" \
"Mvaa 224,56,112\nTa 86,21,17408\nMvaa 220,108,56\nRa 20,7,10240\nMvaa 244,108,60\nRa 24,7,12288\nMvaa 240,104,60\nRa 28,7,14336\n" \
"Mvaa 216,104,56\nbuffer_store_dwordx4 a[216:219], v0, s[60:63], s95 offen offset:2048 sc1\nMvaa 212,100,56\nMvaa 236,100,60\nMvaa 232,96,60\nMvaa 208,96,56\ns_waitcnt lgkmcnt(0)\nMvaa 64,64,0\n" \
"Ta 96,20,8192\nMvaa 88,64,4\nTa 98,20,9216\nMvaa 92,68,4\nRa 32,27,0\nMvaa 68,68,0\nTa 100,21,8192\nMvaa 72,72,0\n" \
"Ta 102,21,9216\nMvaa 96,72,4\nRa 36,27,2048\nMvaa 100,76,4\nTa 104,22,8192\nMvaa 76,76,0\nTa 106,22,9216\nMvaa 80,0,80\n" \
"Ra 40,27,4096\nMvaa 104,4,80\nTa 108,23,8192\nMvaa 108,4,84\nTa 110,23,9216\nMvaa 84,0,84\nRa 44,27,6144\nMvaa 132,8,84\n" \
"Ta 112,22,16384\nMvaa 156,12,84\nTa 114,22,17408\nMvaa 152,12,80\nRa 48,27,8192\nMvaa 128,8,80\nTa 116,23,16384\nMvaa 124,76,8\n" \
"Ta 118,23,17408\nMvaa 148,76,12\nRa 52,27,10240\nMvaa 144,72,12\nRa 56,27,12288\nMvaa 120,72,8\nRa 60,27,14336\nMvaa 116,68,8\n" \
"buffer_store_dwordx4 a[220:223], v0, s[60:63], s95 offen offset:3072 sc1\nMvaa 140,68,12\ns_add_u32 s95, s34, 24576\nMvaa 136,64,12\nbuffer_store_dwordx4 a[224:227], v0, s[60:63], s95 offen offset:0 sc1\nMvaa 112,64,8\nMvaa 160,64,16\nMvaa 184,64,20\n" \
"Mvaa 188,68,20\ns_waitcnt lgkmcnt(0)\ns_barrier\nMvaa 164,68,16\ns_add_u32 m0, s88, 0\nMvaa 168,72,16\nD 9,64,0,0\nMvaa 192,72,20\n" \
"Mvaa 196,76,20\nMvaa 172,76,16\nMvaa 176,16,80\nD 9,64,s73,1024\nMvaa 200,20,80\nMvaa 204,20,84\nbuffer_store_dwordx4 a[228:231], v0, s[60:63], s95 offen offset:1024 sc1\nMvaa 180,16,84\n" \
"Mvaa 228,24,84\nD 9,64,s74,2048\nMvaa 252,28,84\nMvaa 248,28,80\nMvaa 224,24,80\nMvaa 220,76,24\nD 9,64,s75,3072\nMvaa 244,76,28\n" \
"Mvaa 240,72,28\nMvaa 216,72,24\ns_add_u32 m0, s88, 4096\nMvaa 212,68,24\nD 9,64,s76,0\nMvaa 236,68,28\nMvaa 232,64,28\nMvaa 208,64,24\n" \
"buffer_store_dwordx4 a[232:235], v0, s[60:63], s95 offen offset:2048 sc1\nMvaa 64,96,32\nD 9,64,s77,1024\nMvaa 88,96,36\nMvaa 92,100,36\nMvaa 68,100,32\nMvaa 72,104,32\nD 9,64,s78,2048\n" \
"Mvaa 96,104,36\nMvaa 100,108,36\nMvaa 76,108,32\nMvaa 80,32,112\nD 9,64,s79,3072\nMvaa 104,36,112\nMvaa 108,36,116\nMvaa 84,32,116\n" \
"s_add_u32 m0, s32, 65536\nMvaa 132,40,116\nD 10,68,0,0\nMvaa 156,44,116\nMvaa 152,44,112\nMvaa 128,40,112\nbuffer_store_dwordx4 a[236:239], v0, s[60:63], s95 offen offset:3072 sc1\nMvaa 124,108,40\n" \
"D 10,68,s89,1024\nMvaa 148,108,44\nMvaa 144,104,44\nMvaa 120,104,40\nMvaa 116,100,40\nD 11,68,s90,2048\nMvaa 140,100,44\nMvaa 136,96,44\n" \
"s_waitcnt vmcnt(17)\ns_barrier\nMvaa 112,96,40\nTa 64,20,32768\nMvaa 160,96,48\nD 11,68,s91,3072\nTa 66,20,33792\nMvaa 184,96,52\n" \
"Ra 0,7,32768\nMvaa 188,100,52\nTa 68,21,32768\nMvaa 164,100,48\ns_add_u32 m0, s92, 81920\nTa 70,21,33792\nMvaa 168,104,48\nD 12,68,0,0\n" \
"Ra 4,7,34816\nMvaa 192,104,52\nTa 72,22,32768\nMvaa 196,108,52\nTa 74,22,33792\nMvaa 172,108,48\nRa 8,7,36864\nMvaa 176,48,112\n" \
"D 12,68,s89,1024\nTa 76,23,32768\nMvaa 200,52,112\nTa 78,23,33792\nv_add_u32 v9, 128, v9\nMvaa 204,52,116\nRa 12,7,38912\nv_add_u32 v10, s72, v10\n" \
"Mvaa 180,48,116\nTa 80,20,49152\nv_add_u32 v11, s72, v11\nMvaa 228,56,116\nTa 82,20,50176\nv_add_u32 v12, s72, v12\nMvaa 252,60,116\nRa 16,7,40960\n" \
"Mvaa 248,60,112\nTa 84,21,49152\nMvaa 224,56,112\nTa 86,21,50176\nMvaa 220,108,56\nRa 20,7,43008\nMvaa 244,108,60\nRa 24,7,45056\n" \
"Mvaa 240,104,60\nRa 28,7,47104\ns_add_u32 s95, s34, 28672\nMvaa 216,104,56\nbuffer_store_dwordx4 v[32:35], v0, s[60:63], s95 offen offset:0 sc1\nMvaa 212,100,56\nMvaa 236,100,60\nMvaa 232,96,60\n" \
"Mvaa 208,96,56\ns_waitcnt lgkmcnt(0)\nMvaa 64,64,0\nTa 96,20,40960\nMvaa 88,64,4\nTa 98,20,41984\nMvaa 92,68,4\nRa 32,27,32768\n" \
"Mvaa 68,68,0\nTa 100,21,40960\nMvaa 72,72,0\nTa 102,21,41984\nMvaa 96,72,4\nRa 36,27,34816\nMvaa 100,76,4\nTa 104,22,40960\n" \
"Mvaa 76,76,0\nTa 106,22,41984\nMvaa 80,0,80\nRa 40,27,36864\nMvaa 104,4,80\nTa 108,23,40960\nMvaa 108,4,84\nTa 110,23,41984\n" \
"Mvaa 84,0,84\nRa 44,27,38912\nMvaa 132,8,84\nTa 112,22,49152\nMvaa 156,12,84\nTa 114,22,50176\nMvaa 152,12,80\nRa 48,27,40960\n" \
"Mvaa 128,8,80\nTa 116,23,49152\nMvaa 124,76,8\nTa 118,23,50176\nMvaa 148,76,12\nRa 52,27,43008\nMvaa 144,72,12\nRa 56,27,45056\n" \
"Mvaa 120,72,8\nRa 60,27,47104\nMvaa 116,68,8\nbuffer_store_dwordx4 v[36:39], v0, s[60:63], s95 offen offset:1024 sc1\nMvaa 140,68,12\nMvaa 136,64,12\nbuffer_store_dwordx4 v[40:43], v0, s[60:63], s95 offen offset:2048 sc1\nMvaa 112,64,8\n" \
"Mvaa 160,64,16\nMvaa 184,64,20\nMvaa 188,68,20\ns_waitcnt lgkmcnt(0)\ns_barrier\nMvaa 164,68,16\ns_add_u32 m0, s88, 32768\nMvaa 168,72,16\n" \
"D 9,64,0,0\nMvaa 192,72,20\nMvaa 196,76,20\nbuffer_store_dwordx4 v[44:47], v0, s[60:63], s95 offen offset:3072 sc1\nMvaa 172,76,16\nMvaa 176,16,80\nD 9,64,s73,1024\nMvaa 200,20,80\n" \
"Mvaa 204,20,84\nMvaa 180,16,84\nMvaa 228,24,84\nD 9,64,s74,2048\nMvaa 252,28,84\nMvaa 248,28,80\nMvaa 224,24,80\ns_branch 99f\n" \
".p2align 12\nzp19:\ns_setpc_b64 s[98:99]\n99:\nMvaa 220,76,24\nD 9,64,s75,3072\nMvaa 244,76,28\nMvaa 240,72,28\n" \
"Mvaa 216,72,24\ns_add_u32 m0, s88, 36864\nMvaa 212,68,24\nD 9,64,s76,0\nMvaa 236,68,28\nMvaa 232,64,28\nMvaa 208,64,24\nMvaa 64,96,32\n" \
"D 9,64,s77,1024\nMvaa 88,96,36\nMvaa 92,100,36\ns_mov_b32 s96, 4\nMvaa 68,100,32\nMvaa 72,104,32\nD 9,64,s78,2048\nMvaa 96,104,36\n" \
"Mvaa 100,108,36\nMvaa 76,108,32\nMvaa 80,32,112\nD 9,64,s79,3072\nMvaa 104,36,112\nMvaa 108,36,116\nMvaa 84,32,116\ns_add_u32 m0, s32, 98304\n" \
"Mvaa 132,40,116\nD 10,68,0,0\nMvaa 156,44,116\nMvaa 152,44,112\nMvaa 128,40,112\nMvaa 124,108,40\nD 10,68,s89,1024\nMvaa 148,108,44\n" \
"Mvaa 144,104,44\nMvaa 120,104,40\nMvaa 116,100,40\nD 11,68,s90,2048\nMvaa 140,100,44\nMvaa 136,96,44\ns_waitcnt vmcnt(10)\ns_barrier\n" \
"Mvaa 112,96,40\nTa 64,20,0\nMvaa 160,96,48\nD 11,68,s91,3072\nTa 66,20,1024\nMvaa 184,96,52\nRa 0,7,0\nbuffer_store_dwordx2 v[2:3], off, s[60:63], s35 sc1\n" \
"Mvaa 188,100,52\nTa 68,21,0\nMvaa 164,100,48\ns_add_u32 m0, s92, 114688\nTa 70,21,1024\nMvaa 168,104,48\nD 12,68,0,0\nRa 4,7,2048\n" \
"Mvaa 192,104,52\nTa 72,22,0\nMvaa 196,108,52\nTa 74,22,1024\nMvaa 172,108,48\nRa 8,7,4096\nMvaa 176,48,112\nD 12,68,s89,1024\n" \
"Ta 76,23,0\nMvaa 200,52,112\nTa 78,23,1024\nv_add_u32 v9, 128, v9\nMvaa 204,52,116\nRa 12,7,6144\nv_add_u32 v10, s72, v10\nMvaa 180,48,116\n" \
"Ta 80,20,16384\nv_add_u32 v11, s72, v11\nMvaa 228,56,116\nTa 82,20,17408\nv_add_u32 v12, s72, v12\nMvaa 252,60,116\nRa 16,7,8192\nMvaa 248,60,112\n" \
"Ta 84,21,16384\nMvaa 224,56,112\nTa 86,21,17408\nMvaa 220,108,56\nRa 20,7,10240\nMvaa 244,108,60\nRa 24,7,12288\nMvaa 240,104,60\n" \
"Ra 28,7,14336\nMvaa 216,104,56\nMvaa 212,100,56\nMvaa 236,100,60\nMvaa 232,96,60\nMvaa 208,96,56\ns_waitcnt lgkmcnt(0)\ns_branch 99f\n" \
".p2align 12\nzp20:\ns_setpc_b64 s[98:99]\n99:\ns_nop 0\nzal:\nMvaa 64,64,0\nTa 96,20,8192\n" \
"Mvaa 88,64,4\nTa 98,20,9216\nMvaa 92,68,4\nRa 32,27,0\nMvaa 68,68,0\nTa 100,21,8192\nMvaa 72,72,0\nTa 102,21,9216\n" \
"Mvaa 96,72,4\nRa 36,27,2048\nMvaa 100,76,4\nTa 104,22,8192\nMvaa 76,76,0\nTa 106,22,9216\nMvaa 80,0,80\nRa 40,27,4096\n" \
"Mvaa 104,4,80\nTa 108,23,8192\nMvaa 108,4,84\nTa 110,23,9216\nMvaa 84,0,84\nRa 44,27,6144\nMvaa 132,8,84\nTa 112,22,16384\n" \
"Mvaa 156,12,84\nTa 114,22,17408\nMvaa 152,12,80\nRa 48,27,8192\nMvaa 128,8,80\nTa 116,23,16384\nMvaa 124,76,8\nTa 118,23,17408\n" \
"Mvaa 148,76,12\nRa 52,27,10240\nMvaa 144,72,12\nRa 56,27,12288\nMvaa 120,72,8\nRa 60,27,14336\nMvaa 116,68,8\nMvaa 140,68,12\n" \
"Mvaa 136,64,12\nMvaa 112,64,8\nMvaa 160,64,16\nMvaa 184,64,20\nMvaa 188,68,20\ns_waitcnt lgkmcnt(0)\ns_barrier\nMvaa 164,68,16\n" \
"s_add_u32 m0, s88, 0\nMvaa 168,72,16\nD 9,64,0,0\nMvaa 192,72,20\nMvaa 196,76,20\nMvaa 172,76,16\nMvaa 176,16,80\nD 9,64,s73,1024\n" \
"Mvaa 200,20,80\nMvaa 204,20,84\nMvaa 180,16,84\nMvaa 228,24,84\nD 9,64,s74,2048\nMvaa 252,28,84\nMvaa 248,28,80\nMvaa 224,24,80\n" \
"Mvaa 220,76,24\nD 9,64,s75,3072\nMvaa 244,76,28\nMvaa 240,72,28\nMvaa 216,72,24\ns_add_u32 m0, s88, 4096\nMvaa 212,68,24\nD 9,64,s76,0\n" \
"Mvaa 236,68,28\nMvaa 232,64,28\nMvaa 208,64,24\nMvaa 64,96,32\nD 9,64,s77,1024\nMvaa 88,96,36\nMvaa 92,100,36\nMvaa 68,100,32\n" \
"Mvaa 72,104,32\nD 9,64,s78,2048\nMvaa 96,104,36\nMvaa 100,108,36\nMvaa 76,108,32\nMvaa 80,32,112\nD 9,64,s79,3072\nMvaa 104,36,112\n" \
"Mvaa 108,36,116\nMvaa 84,32,116\ns_add_u32 m0, s32, 65536\nMvaa 132,40,116\nD 10,68,0,0\nMvaa 156,44,116\nMvaa 152,44,112\nMvaa 128,40,112\n" \
"Mvaa 124,108,40\nD 10,68,s89,1024\nMvaa 148,108,44\nMvaa 144,104,44\nMvaa 120,104,40\nMvaa 116,100,40\nD 11,68,s90,2048\nMvaa 140,100,44\n" \
"Mvaa 136,96,44\ns_waitcnt vmcnt(11)\ns_barrier\nMvaa 112,96,40\nTa 64,20,32768\nMvaa 160,96,48\nD 11,68,s91,3072\nTa 66,20,33792\n" \
"Mvaa 184,96,52\nRa 0,7,32768\nMvaa 188,100,52\nTa 68,21,32768\nMvaa 164,100,48\ns_add_u32 m0, s92, 81920\nTa 70,21,33792\nMvaa 168,104,48\n" \
"D 12,68,0,0\nRa 4,7,34816\nMvaa 192,104,52\nTa 72,22,32768\nMvaa 196,108,52\nTa 74,22,33792\nMvaa 172,108,48\nRa 8,7,36864\n" \
"Mvaa 176,48,112\nD 12,68,s89,1024\nTa 76,23,32768\nMvaa 200,52,112\nTa 78,23,33792\nv_add_u32 v9, 128, v9\nMvaa 204,52,116\nRa 12,7,38912\n" \
"v_add_u32 v10, s72, v10\nMvaa 180,48,116\nTa 80,20,49152\nv_add_u32 v11, s72, v11\nMvaa 228,56,116\nTa 82,20,50176\nv_add_u32 v12, s72, v12\nMvaa 252,60,116\n" \
"Ra 16,7,40960\nMvaa 248,60,112\nTa 84,21,49152\nMvaa 224,56,112\nTa 86,21,50176\nMvaa 220,108,56\nRa 20,7,43008\nMvaa 244,108,60\n" \
"Ra 24,7,45056\nMvaa 240,104,60\nRa 28,7,47104\nMvaa 216,104,56\nMvaa 212,100,56\nMvaa 236,100,60\nMvaa 232,96,60\nMvaa 208,96,56\n" \
"s_waitcnt lgkmcnt(0)\nMvaa 64,64,0\nTa 96,20,40960\nMvaa 88,64,4\nTa 98,20,41984\nMvaa 92,68,4\nRa 32,27,32768\nMvaa 68,68,0\n" \
"Ta 100,21,40960\nMvaa 72,72,0\nTa 102,21,41984\nMvaa 96,72,4\nRa 36,27,34816\nMvaa 100,76,4\nTa 104,22,40960\nMvaa 76,76,0\n" \
"Ta 106,22,41984\nMvaa 80,0,80\nRa 40,27,36864\nMvaa 104,4,80\nTa 108,23,40960\nMvaa 108,4,84\nTa 110,23,41984\nMvaa 84,0,84\n" \
"Ra 44,27,38912\nMvaa 132,8,84\nTa 112,22,49152\nMvaa 156,12,84\nTa 114,22,50176\nMvaa 152,12,80\nRa 48,27,40960\nMvaa 128,8,80\n" \
"Ta 116,23,49152\nMvaa 124,76,8\nTa 118,23,50176\nMvaa 148,76,12\nRa 52,27,43008\nMvaa 144,72,12\nRa 56,27,45056\nMvaa 120,72,8\n" \
"Ra 60,27,47104\nMvaa 116,68,8\nMvaa 140,68,12\nMvaa 136,64,12\nMvaa 112,64,8\nMvaa 160,64,16\nMvaa 184,64,20\nMvaa 188,68,20\n" \
"s_waitcnt lgkmcnt(0)\ns_barrier\nMvaa 164,68,16\ns_add_u32 m0, s88, 32768\nMvaa 168,72,16\nD 9,64,0,0\nMvaa 192,72,20\nMvaa 196,76,20\n" \
"Mvaa 172,76,16\nMvaa 176,16,80\nD 9,64,s73,1024\nMvaa 200,20,80\nMvaa 204,20,84\nMvaa 180,16,84\nMvaa 228,24,84\nD 9,64,s74,2048\n" \
"Mvaa 252,28,84\nMvaa 248,28,80\nMvaa 224,24,80\nMvaa 220,76,24\nD 9,64,s75,3072\nMvaa 244,76,28\nMvaa 240,72,28\nMvaa 216,72,24\n" \
"s_add_u32 m0, s88, 36864\nMvaa 212,68,24\nD 9,64,s76,0\nMvaa 236,68,28\nMvaa 232,64,28\nMvaa 208,64,24\nMvaa 64,96,32\nD 9,64,s77,1024\n" \
"Mvaa 88,96,36\nMvaa 92,100,36\nMvaa 68,100,32\nMvaa 72,104,32\nD 9,64,s78,2048\nMvaa 96,104,36\nMvaa 100,108,36\nMvaa 76,108,32\n" \
"Mvaa 80,32,112\nD 9,64,s79,3072\nMvaa 104,36,112\nMvaa 108,36,116\nMvaa 84,32,116\ns_add_u32 m0, s32, 98304\nMvaa 132,40,116\nD 10,68,0,0\n" \
"Mvaa 156,44,116\nMvaa 152,44,112\nMvaa 128,40,112\nMvaa 124,108,40\nD 10,68,s89,1024\nMvaa 148,108,44\nMvaa 144,104,44\nMvaa 120,104,40\n" \
"Mvaa 116,100,40\nD 11,68,s90,2048\nMvaa 140,100,44\nMvaa 136,96,44\ns_waitcnt vmcnt(11)\ns_barrier\nMvaa 112,96,40\nTa 64,20,0\n" \
"Mvaa 160,96,48\nD 11,68,s91,3072\nTa 66,20,1024\nMvaa 184,96,52\nRa 0,7,0\nMvaa 188,100,52\nTa 68,21,0\nMvaa 164,100,48\n" \
"s_add_u32 m0, s92, 114688\nTa 70,21,1024\nMvaa 168,104,48\nD 12,68,0,0\nRa 4,7,2048\nMvaa 192,104,52\nTa 72,22,0\nMvaa 196,108,52\n" \
"Ta 74,22,1024\nMvaa 172,108,48\nRa 8,7,4096\nMvaa 176,48,112\nD 12,68,s89,1024\nTa 76,23,0\nMvaa 200,52,112\nTa 78,23,1024\n" \
"v_add_u32 v9, 128, v9\nMvaa 204,52,116\nRa 12,7,6144\nv_add_u32 v10, s72, v10\nMvaa 180,48,116\nTa 80,20,16384\nv_add_u32 v11, s72, v11\nMvaa 228,56,116\n" \
"Ta 82,20,17408\nv_add_u32 v12, s72, v12\nMvaa 252,60,116\nRa 16,7,8192\nMvaa 248,60,112\nTa 84,21,16384\nMvaa 224,56,112\nTa 86,21,17408\n" \
"Mvaa 220,108,56\nRa 20,7,10240\nMvaa 244,108,60\nRa 24,7,12288\nMvaa 240,104,60\nRa 28,7,14336\nMvaa 216,104,56\ns_sub_u32 s96, s96, 1\n" \
"Mvaa 212,100,56\ns_cmp_lg_u32 s96, 0\nMvaa 236,100,60\nMvaa 232,96,60\nMvaa 208,96,56\ns_waitcnt lgkmcnt(0)\ns_cbranch_scc1 zal\nMvaa 64,64,0\n" \
"Ta 96,20,8192\ns_mov_b64 s[64:65], s[84:85]\ns_mov_b32 s66, 4194304\nMvaa 88,64,4\nTa 98,20,9216\ns_mov_b64 s[68:69], s[86:87]\ns_mov_b32 s70, 8388608\nMvaa 92,68,4\n" \
"Ra 32,27,0\ns_mov_b32 s73, 15360\ns_mov_b32 s74, 129024\nMvaa 68,68,0\nTa 100,21,8192\ns_mov_b32 s75, 144384\ns_add_u32 s76, s37, 0\nMvaa 72,72,0\n" \
"Ta 102,21,9216\ns_add_u32 s77, s37, 15360\ns_add_u32 s78, s37, 129024\nMvaa 96,72,4\nRa 36,27,2048\ns_add_u32 s79, s37, 144384\ns_mov_b32 s89, 31744\nMvaa 100,76,4\n" \
"Ta 104,22,8192\ns_mov_b32 s90, 63488\ns_mov_b32 s91, 95232\nMvaa 76,76,0\nTa 106,22,9216\ns_mov_b32 s72, 524288\nv_xor_b32 v28, 128, v14\nMvaa 80,0,80\n" \
"Ra 40,27,4096\nMvaa 104,4,80\nTa 108,23,8192\nMvaa 108,4,84\nTa 110,23,9216\nMvaa 84,0,84\nRa 44,27,6144\nMvaa 132,8,84\n" \
"Ta 112,22,16384\nMvaa 156,12,84\nTa 114,22,17408\nMvaa 152,12,80\nRa 48,27,8192\nMvaa 128,8,80\nTa 116,23,16384\nMvaa 124,76,8\n" \
"Ta 118,23,17408\nMvaa 148,76,12\nRa 52,27,10240\nMvaa 144,72,12\nRa 56,27,12288\nMvaa 120,72,8\nRa 60,27,14336\nMvaa 116,68,8\n" \
"Mvaa 140,68,12\nMvaa 136,64,12\nMvaa 112,64,8\nMvaa 160,64,16\nMvaa 184,64,20\nMvaa 188,68,20\ns_waitcnt lgkmcnt(0)\ns_barrier\n" \
"Mvaa 164,68,16\ns_add_u32 m0, s88, 131072\nMvaa 168,72,16\nD 13,64,0,0\nMvaa 192,72,20\nMvaa 196,76,20\nMvaa 172,76,16\nMvaa 176,16,80\n" \
"D 13,64,s73,1024\nMvaa 200,20,80\nMvaa 204,20,84\nMvaa 180,16,84\nMvaa 228,24,84\nD 13,64,s74,2048\nMvaa 252,28,84\nMvaa 248,28,80\n" \
"Mvaa 224,24,80\nMvaa 220,76,24\nD 13,64,s75,3072\nMvaa 244,76,28\nMvaa 240,72,28\nMvaa 216,72,24\ns_add_u32 m0, s88, 135168\nMvaa 212,68,24\n" \
"D 13,64,s76,0\nMvaa 236,68,28\nMvaa 232,64,28\nMvaa 208,64,24\nMvaa 64,96,32\nD 13,64,s77,1024\nMvaa 88,96,36\nMvaa 92,100,36\n" \
"Mvaa 68,100,32\nMvaa 72,104,32\nD 13,64,s78,2048\nMvaa 96,104,36\nMvaa 100,108,36\nMvaa 76,108,32\nMvaa 80,32,112\nD 13,64,s79,3072\n" \
"Mvaa 104,36,112\nMvaa 108,36,116\nMvaa 84,32,116\ns_add_u32 m0, s32, 81920\nMvaa 132,40,116\nD 14,68,0,0\nMvaa 156,44,116\nMvaa 152,44,112\n" \
"Mvaa 128,40,112\nMvaa 124,108,40\nD 14,68,s89,1024\nMvaa 148,108,44\nMvaa 144,104,44\nMvaa 120,104,40\nMvaa 116,100,40\nD 28,68,s90,2048\n" \
"Mvaa 140,100,44\nMvaa 136,96,44\ns_waitcnt vmcnt(11)\ns_barrier\nMvaa 112,96,40\nTa 64,20,32768\nMvaa 160,96,48\nD 28,68,s91,3072\n" \
"Ta 66,20,33792\nMvaa 184,96,52\nRa 0,7,32768\nMvaa 188,100,52\nTa 68,21,32768\nMvaa 164,100,48\ns_add_u32 m0, s88, 0\nv_add_u32 v13, 128, v13\n" \
"Ta 70,21,33792\nMvaa 168,104,48\nD 13,64,0,0\nRa 4,7,34816\nMvaa 192,104,52\nTa 72,22,32768\nMvaa 196,108,52\nTa 74,22,33792\n" \
"Mvaa 172,108,48\nRa 8,7,36864\nMvaa 176,48,112\nD 13,64,s73,1024\nTa 76,23,32768\nMvaa 200,52,112\nTa 78,23,33792\nMvaa 204,52,116\n" \
"Ra 12,7,38912\nMvaa 180,48,116\nTa 80,20,49152\nMvaa 228,56,116\nD 13,64,s74,2048\nTa 82,20,50176\nMvaa 252,60,116\nRa 16,7,40960\n" \
"Mvaa 248,60,112\nTa 84,21,49152\nMvaa 224,56,112\nTa 86,21,50176\nMvaa 220,108,56\nD 13,64,s75,3072\nRa 20,7,43008\nMvaa 244,108,60\n" \
"Ra 24,7,45056\nMvaa 240,104,60\nRa 28,7,47104\nMvaa 216,104,56\nMvaa 212,100,56\nMvaa 236,100,60\nMvaa 232,96,60\nMvaa 208,96,56\n" \
"s_waitcnt lgkmcnt(0)\nMvaa 64,64,0\nTa 96,20,40960\nMvaa 88,64,4\nTa 98,20,41984\nMvaa 92,68,4\nRa 32,27,32768\nMvaa 68,68,0\n" \
"Ta 100,21,40960\nMvaa 72,72,0\nTa 102,21,41984\nMvaa 96,72,4\nRa 36,27,34816\nMvaa 100,76,4\nTa 104,22,40960\nMvaa 76,76,0\n" \
"Ta 106,22,41984\nMvaa 80,0,80\nRa 40,27,36864\ns_branch 99f\n.p2align 12\nzp21:\ns_setpc_b64 s[98:99]\n99:\n" \
"Mvaa 104,4,80\nTa 108,23,40960\nMvaa 108,4,84\nTa 110,23,41984\nMvaa 84,0,84\nRa 44,27,38912\nMvaa 132,8,84\nTa 112,22,49152\n" \
"Mvaa 156,12,84\nTa 114,22,50176\nMvaa 152,12,80\nRa 48,27,40960\nMvaa 128,8,80\nTa 116,23,49152\nMvaa 124,76,8\nTa 118,23,50176\n" \
"Mvaa 148,76,12\nRa 52,27,43008\nMvaa 144,72,12\nRa 56,27,45056\ns_and_b32 s96, s32, 0x1000\ns_lshl_b32 s97, s96, 1\nMvaa 120,72,8\nRa 60,27,47104\n" \
"s_lshl_b32 s96, s96, 2\nv_add_u32 v16, s97, v7\nMvaa 116,68,8\nv_subrev_u32 v20, s96, v8\nv_xor_b32 v17, 64, v16\nv_add_u32 v18, 0x20000, v16\nMvaa 140,68,12\nv_add_u32 v19, 0x20000, v17\n" \
"v_xor_b32 v21, 32, v20\nv_xor_b32 v22, 64, v20\nMvaa 136,64,12\nv_xor_b32 v23, 96, v20\nv_xor_b32 v24, 128, v20\nv_xor_b32 v25, 160, v20\nMvaa 112,64,8\nv_xor_b32 v26, 192, v20\n" \
"v_xor_b32 v27, 224, v20\nMvaa 160,64,16\nMvaa 184,64,20\nMvaa 188,68,20\ns_waitcnt vmcnt(4) lgkmcnt(0)\ns_barrier\nMvaa 164,68,16\ns_add_u32 m0, s88, 4096\n" \
"Mvaa 168,72,16\nD 13,64,s76,0\nMvaa 192,72,20\nTa 160,20,24576\nTa 162,20,25600\nMvaa 196,76,20\nTa 164,21,24576\nMvaa 172,76,16\n" \
"D 13,64,s77,1024\nMvaa 176,16,80\nTa 166,21,25600\nTa 168,22,24576\nMvaa 200,20,80\nTa 170,22,25600\nMvaa 204,20,84\nD 13,64,s78,2048\n" \
"Mvaa 180,16,84\nTa 172,23,24576\nTa 174,23,25600\nMvaa 228,24,84\nTa 176,24,24576\nMvaa 252,28,84\nD 13,64,s79,3072\nMvaa 248,28,80\n" \
"Ta 178,24,25600\nTa 180,25,24576\nMvaa 224,24,80\ns_add_u32 m0, s32, 65536\nv_add_u32 v14, s72, v14\nv_add_u32 v28, s72, v28\nMvaa 220,76,24\nD 14,68,0,0\n" \
"Mvaa 244,76,28\nTa 182,25,25600\nMvaa 240,72,28\nTa 184,26,24576\nTa 186,26,25600\nMvaa 216,72,24\nD 14,68,s89,1024\nMvaa 212,68,24\n" \
"Ta 188,27,24576\nMvaa 236,68,28\nTa 190,27,25600\nMvaa 232,64,28\nD 28,68,s90,2048\nMvaa 208,64,24\nMvaa 112,96,40\nTa 64,20,16384\n" \
"Mvaa 116,100,40\nD 28,68,s91,3072\nMvaa 120,104,40\nTa 66,20,17408\nMvaa 124,108,40\ns_add_u32 m0, s88, 32768\nv_add_u32 v13, 128, v13\nMvaa 128,40,112\n" \
"D 13,64,0,0\nMvaa 132,40,116\nRa 0,18,0\nMvaa 136,96,44\nRa 16,19,0\nMvaa 140,100,44\nD 13,64,s73,1024\nMvaa 144,104,44\n" \
"Ta 68,21,16384\nMvaa 148,108,44\nTa 70,21,17408\nC 112,112,113\nC 113,114,115\nMvaa 152,44,112\nD 13,64,s74,2048\nC 114,116,117\n" \
"Mvaa 156,44,116\nTa 72,22,16384\nC 115,118,119\nC 120,120,121\nMvaa 160,96,48\nC 121,122,123\nC 122,124,125\nC 123,126,127\n" \
"Mvaa 164,100,48\nD 13,64,s75,3072\nC 38,128,129\nMvaa 168,104,48\nTa 74,22,17408\nC 39,130,131\nC 56,132,133\nMvaa 172,108,48\n" \
"s_add_u32 m0, s88, 36864\nTa 76,23,16384\nC 57,134,135\nMvaa 176,48,112\nD 13,64,s76,0\nAW 200,112\nMvaa 180,48,116\nTa 78,23,17408\n" \
"AW 201,113\nAW 202,114\nMvaa 184,96,52\nTa 80,24,16384\nAW 203,115\nAW 232,120\nMvaa 188,100,52\nD 13,64,s77,1024\n" \
"AW 233,121\nMvaa 192,104,52\nAW 234,122\nAW 235,123\nC 136,136,137\nMvaa 196,108,52\nTa 82,24,17408\nC 137,138,139\n" \
"C 138,140,141\nMvaa 200,52,112\nD 13,64,s78,2048\nC 139,142,143\nMvaa 204,52,116\nTa 84,25,16384\nC 144,144,145\nC 145,146,147\n" \
"Mvaa 64,96,32\nTa 86,25,17408\nC 146,148,149\nC 147,150,151\nMvaa 68,100,32\nD 13,64,s79,3072\nC 40,152,153\nMvaa 72,104,32\n" \
"Ta 88,26,16384\nC 41,154,155\nC 58,156,157\nMvaa 76,108,32\ns_add_u32 m0, s32, 98304\nv_add_u32 v14, s72, v14\nv_add_u32 v28, s72, v28\nMvaa 80,32,112\n" \
"D 14,68,0,0\nC 59,158,159\nMvaa 84,32,116\nTa 90,26,17408\nAW 204,136\nAW 205,137\nMvaa 208,96,56\nTa 92,27,16384\n" \
"AW 206,138\nAW 207,139\nMvaa 212,100,56\nD 14,68,s89,1024\nAW 236,144\nMvaa 216,104,56\nTa 94,27,17408\nAW 237,145\n" \
"AW 238,146\nMvaa 220,108,56\nAW 239,147\nC 160,160,161\nC 161,162,163\nMvaa 224,56,112\nD 28,68,s90,2048\nC 162,164,165\n" \
"Mvaa 228,56,116\nRa 4,18,2048\nC 163,166,167\nC 168,168,169\nMvaa 232,96,60\nRa 20,19,2048\nC 169,170,171\nC 170,172,173\n" \
"Mvaa 236,100,60\nD 28,68,s91,3072\nC 171,174,175\nMvaa 240,104,60\nRa 8,18,4096\nC 42,176,177\nC 43,178,179\nMvaa 244,108,60\n" \
"Ra 24,19,4096\nC 60,180,181\nC 61,182,183\nMvaa 248,60,112\nRa 12,18,6144\nAW 208,160\nAW 209,161\nMvaa 252,60,116\n" \
"Ra 28,19,6144\nAW 210,162\nAW 211,163\nMvaa 88,96,36\nAW 240,168\nAW 241,169\nAW 242,170\nMvaa 92,100,36\n" \
"AW 243,171\nC 184,184,185\nC 185,186,187\nMvaa 96,104,36\nC 186,188,189\nC 187,190,191\nC 192,192,193\nMvaa 100,108,36\n" \
"C 193,194,195\nC 194,196,197\nC 195,198,199\nMvaa 104,36,112\nC 44,200,201\nC 45,202,203\nC 62,204,205\nMvaa 108,36,116\n" \
"C 63,206,207\nAW 212,184\nAW 213,185\ns_waitcnt lgkmcnt(6)\nZvaa 128,64,0\nv_add_u32 v13, 128, v13\nAW 214,186\nAW 215,187\n" \
"Mvaa 128,160,16\ns_add_u32 m0, s88, 131072\nAW 244,192\nAW 245,193\nZvaa 132,164,16\nD 13,64,0,0\nAW 246,194\nMvaa 132,68,0\n" \
"v_add_u32 v14, s72, v14\nAW 247,195\nC 64,64,65\nZvaa 136,72,0\nv_add_u32 v28, s72, v28\nC 65,66,67\nC 66,68,69\nMvaa 136,168,16\n" \
"C 67,70,71\nC 72,72,73\nC 73,74,75\nZvaa 140,172,16\nC 74,76,77\nC 75,78,79\nC 34,80,81\nMvaa 140,76,0\n" \
"s_waitcnt lgkmcnt(0)\nD 13,64,s73,1024\nZvaa 144,80,0\nC 35,82,83\nC 52,84,85\nC 53,86,87\nMvaa 144,176,16\nAW 192,64\n" \
"AW 193,65\nAW 194,66\nZvaa 148,180,16\nAW 195,67\nAW 224,72\nAW 225,73\nMvaa 148,84,0\nAW 226,74\n" \
"AW 227,75\nC 208,208,209\nZvaa 152,88,0\nD 13,64,s74,2048\nC 209,210,211\nMvaa 152,184,16\nC 210,212,213\nC 211,214,215\n" \
"C 216,216,217\nZvaa 156,188,16\nC 217,218,219\nC 218,220,221\nC 219,222,223\nMvaa 156,92,0\nC 46,224,225\nC 47,226,227\n" \
"C 64,228,229\nZvaa 188,92,4\ns_waitcnt vmcnt(15)\ns_barrier\nC 65,230,231\nMvaa 188,188,20\nTa 96,20,0\nTa 98,20,1024\n" \
"AW 216,208\nZvaa 184,184,20\nD 13,64,s75,3072\nAW 217,209\nMvaa 184,88,4\nTa 128,20,8192\nTa 130,20,9216\nAW 218,210\n" \
"Zvaa 180,84,4\nAW 219,211\nAW 248,216\nAW 249,217\nMvaa 180,180,20\nRa 32,16,0\nRa 48,17,0\nAW 250,218\n" \
"Zvaa 176,176,20\ns_add_u32 m0, s88, 135168\nTa 100,21,0\nAW 251,219\nMvaa 176,80,4\nD 13,64,s76,0\nC 232,232,233\nZvaa 172,76,4\n" \
"Ta 102,21,1024\nTa 132,21,8192\nC 233,234,235\nMvaa 172,172,20\nC 234,236,237\nC 235,238,239\nC 240,240,241\nZvaa 168,168,20\n" \
"Ta 134,21,9216\nTa 104,22,0\nC 241,242,243\nMvaa 168,72,4\nTa 106,22,1024\nTa 136,22,8192\nC 242,244,245\nZvaa 164,68,4\n" \
"D 13,64,s77,1024\nC 243,246,247\nMvaa 164,164,20\nC 48,248,249\nC 49,250,251\nC 66,252,253\nZvaa 160,160,20\nTa 138,22,9216\n" \
"Ta 108,23,0\nC 67,254,255\nMvaa 160,64,4\nAW 220,232\nAW 221,233\nAW 222,234\nZvaa 192,64,8\nTa 110,23,1024\n" \
"Ta 140,23,8192\nAW 223,235\nMvaa 192,160,24\nD 13,64,s78,2048\nAW 252,240\nZvaa 196,164,24\nTa 142,23,9216\nTa 112,24,0\n" \
"AW 253,241\nMvaa 196,68,8\nAW 254,242\nAW 255,243\nC 88,88,89\nZvaa 200,72,8\nTa 114,24,1024\nTa 144,24,8192\n" \
"C 89,90,91\nMvaa 200,168,24\nTa 146,24,9216\nTa 116,25,0\nC 90,92,93\nZvaa 204,172,24\nD 13,64,s79,3072\nC 91,94,95\n" \
"Mvaa 204,76,8\nC 96,96,97\nC 97,98,99\nC 98,100,101\nZvaa 208,80,8\nTa 118,25,1024\nTa 148,25,8192\nC 99,102,103\n" \
"Mvaa 208,176,24\nv_add_u32 v13, 128, v13\nTa 150,25,9216\nC 36,104,105\nZvaa 212,180,24\ns_add_u32 m0, s32, 81920\nTa 120,26,0\nC 37,106,107\n" \
"Mvaa 212,84,8\nD 14,68,0,0\nC 54,108,109\nZvaa 216,88,8\nTa 122,26,1024\nTa 152,26,8192\nC 55,110,111\nMvaa 216,184,24\n" \
"AW 196,88\nAW 197,89\nAW 198,90\nZvaa 220,188,24\nTa 154,26,9216\nTa 124,27,0\nAW 199,91\nMvaa 220,92,8\n" \
"AW 228,96\nAW 229,97\nAW 230,98\nZvaa 252,92,12\nD 14,68,s89,1024\nAW 231,99\nMvaa 252,188,28\nTa 126,27,1024\n" \
"Ta 156,27,8192\nZvaa 248,184,28\nTa 158,27,9216\nRa 36,16,2048\nMvaa 248,88,12\nZvaa 244,84,12\nRa 52,17,2048\nRa 40,16,4096\n" \
"Mvaa 244,180,28\nD 28,68,s90,2048\nZvaa 240,176,28\nRa 56,17,4096\nRa 44,16,6144\nMvaa 240,80,12\nZvaa 236,76,12\nRa 60,17,6144\n" \
"Mvaa 236,172,28\nZvaa 232,168,28\nD 28,68,s91,3072\nMvaa 232,72,12\nZvaa 228,68,12\nMvaa 228,164,28\nv_add_u32 v14, s72, v14\nZvaa 224,160,28\n" \
"v_add_u32 v28, s72, v28\nMvaa 224,64,12\ns_waitcnt lgkmcnt(6)\nMvaa 128,96,32\nbuffer_store_dwordx4 a[252:255], v0, s[60:63], s46 offen sc1\nMvaa 128,128,48\ns_add_u32 m0, s88, 0\nMvaa 132,132,48\n" \
"D 13,64,0,0\nMvaa 132,100,32\nbuffer_store_dwordx2 v[48:49], v0, s[60:63], s46 offen offset:1024 sc1\nMvaa 136,104,32\nbuffer_store_dwordx2 v[66:67], v0, s[60:63], s46 offen offset:1032 sc1\nMvaa 136,136,48\nv_mov_b32 v100, 0\nv_bfe_u32 v104, v0, 4, 4\n" \
"Mvaa 140,140,48\nv_lshrrev_b32 v105, 6, v0\nv_and_b32 v105, 12, v105\nMvaa 140,108,32\ns_waitcnt lgkmcnt(0)\nD 13,64,s73,1024\nMvaa 144,112,32\nv_sub_u32 v104, v104, v105\n" \
"v_mov_b32 v107, 0xff800000\nMvaa 144,144,48\ns_mov_b32 s93, 0x3e8293ee\nv_subrev_u32 v105, 0, v104\nMvaa 148,148,48\nv_and_b32 v106, 0x80000001, v105\nv_cmp_eq_u32 vcc, 0, v106\nMvaa 148,116,32\n" \
"v_mov_b32 v84, 0x3f803f80\ns_nop 0\nMvaa 152,120,32\nD 13,64,s74,2048\nMvaa 152,152,48\nv_cndmask_b32 v76, v107, v100, vcc\nv_cmp_eq_u32 vcc, 0x80000000, v106\nMvaa 156,156,48\n" \
"s_nop 1\nv_cndmask_b32 v72, v107, v100, vcc\nMvaa 156,124,32\nv_subrev_u32 v105, 1, v104\nv_and_b32 v106, 0x80000001, v105\nMvaa 188,124,36\ns_waitcnt vmcnt(18)\ns_barrier\n" \
"Mvaa 188,156,52\nTa 64,20,32768\nTa 66,20,33792\nMvaa 184,152,52\nD 13,64,s75,3072\nMvaa 184,120,36\nTa 160,20,40960\nTa 162,20,41984\n" \
"Mvaa 180,116,36\nv_cmp_eq_u32 vcc, 0, v106\ns_branch 99f\n.p2align 12\nzp22:\ns_setpc_b64 s[98:99]\n99:\nv_mov_b32 v85, 0x3f803f80\n" \
"Mvaa 180,148,52\nRa 0,16,32768\nRa 16,17,32768\nMvaa 176,144,52\ns_add_u32 m0, s88, 4096\nTa 68,21,32768\nMvaa 176,112,36\nD 13,64,s76,0\n" \
"Mvaa 172,108,36\nTa 70,21,33792\nTa 164,21,40960\nMvaa 172,140,52\ns_nop 0\nv_cndmask_b32 v77, v107, v100, vcc\nMvaa 168,136,52\nTa 166,21,41984\n" \
"Ta 72,22,32768\nMvaa 168,104,36\nTa 74,22,33792\nTa 168,22,40960\nMvaa 164,100,36\nD 13,64,s77,1024\nMvaa 164,132,52\nv_cmp_eq_u32 vcc, 0x80000000, v106\n" \
"s_nop 1\nMvaa 160,128,52\nTa 170,22,41984\nTa 76,23,32768\nMvaa 160,96,36\nv_cndmask_b32 v73, v107, v100, vcc\nv_subrev_u32 v105, 2, v104\nMvaa 192,96,40\n" \
"Ta 78,23,33792\nTa 172,23,40960\nMvaa 192,128,56\nD 13,64,s78,2048\nMvaa 196,132,56\nTa 174,23,41984\nTa 80,24,32768\nMvaa 196,100,40\n" \
"v_and_b32 v106, 0x80000001, v105\nv_cmp_eq_u32 vcc, 0, v106\nMvaa 200,104,40\nTa 82,24,33792\nTa 176,24,40960\nMvaa 200,136,56\nTa 178,24,41984\nTa 84,25,32768\n" \
"Mvaa 204,140,56\nD 13,64,s79,3072\nMvaa 204,108,40\nv_mov_b32 v86, 0x3f803f80\ns_nop 0\nMvaa 208,112,40\nTa 86,25,33792\nTa 180,25,40960\n" \
"Mvaa 208,144,56\nv_add_u32 v13, 128, v13\nTa 182,25,41984\nMvaa 212,148,56\ns_add_u32 m0, s32, 65536\nTa 88,26,32768\nMvaa 212,116,40\nD 14,68,0,0\n" \
"Mvaa 216,120,40\nTa 90,26,33792\nTa 184,26,40960\nMvaa 216,152,56\nv_cndmask_b32 v78, v107, v100, vcc\nv_cmp_eq_u32 vcc, 0x80000000, v106\nMvaa 220,156,56\nTa 186,26,41984\n" \
"Ta 92,27,32768\nMvaa 220,124,40\ns_nop 1\nv_cndmask_b32 v74, v107, v100, vcc\nMvaa 252,124,44\nD 14,68,s89,1024\nMvaa 252,156,60\nTa 94,27,33792\n" \
"Ta 188,27,40960\nMvaa 248,152,60\nTa 190,27,41984\nRa 4,16,34816\nMvaa 248,120,44\nv_subrev_u32 v105, 3, v104\nv_and_b32 v106, 0x80000001, v105\nMvaa 244,116,44\n" \
"Ra 20,17,34816\nRa 8,16,36864\nMvaa 244,148,60\nD 28,68,s90,2048\nMvaa 240,144,60\nRa 24,17,36864\nRa 12,16,38912\nMvaa 240,112,44\n" \
"v_cmp_eq_u32 vcc, 0, v106\nv_mov_b32 v87, 0x3f803f80\nMvaa 236,108,44\nRa 28,17,38912\ns_nop 0\nMvaa 236,140,60\nv_cndmask_b32 v79, v107, v100, vcc\nv_cmp_eq_u32 vcc, 0x80000000, v106\n" \
"Mvaa 232,136,60\nD 28,68,s91,3072\nMvaa 232,104,44\ns_nop 1\nv_cndmask_b32 v75, v107, v100, vcc\nMvaa 228,100,44\ns_cmp_eq_u32 s51, 0\ns_cselect_b64 vcc, -1, 0\n" \
"Mvaa 228,132,60\nv_add_u32 v14, s72, v14\nv_cndmask_b32 v80, v107, v72, vcc\nMvaa 224,128,60\nv_add_u32 v28, s72, v28\nv_cndmask_b32 v81, v107, v73, vcc\nMvaa 224,96,44\ns_waitcnt lgkmcnt(6)\n" \
"v_cndmask_b32 v82, v107, v74, vcc\nMvaa 128,64,0\nv_cndmask_b32 v83, v107, v75, vcc\nQvaa 88,224,196,72\nQvaa 92,228,196,76\nQvaa 108,228,200,72\nQvaa 112,232,200,76\nMvaa 128,160,16\n" \
"s_add_u32 m0, s88, 32768\nMvaa 132,164,16\nD 13,64,0,0\nMvaa 132,68,0\nMvaa 136,72,0\nv_max3_f32 v104, v88, v89, v90\nv_max3_f32 v105, v91, v92, v93\nMvaa 136,168,16\n" \
"v_max3_f32 v104, v104, v94, v95\nv_max_f32 v104, v104, v105\nMvaa 140,172,16\nv_mov_b32 v105, v104\nv_max3_f32 v124, v108, v109, v110\nMvaa 140,76,0\ns_waitcnt lgkmcnt(0)\nD 13,64,s73,1024\n" \
"Mvaa 144,80,0\nv_max3_f32 v125, v111, v112, v113\nv_max3_f32 v124, v124, v114, v115\nMvaa 144,176,16\nv_max_f32 v124, v124, v125\nv_mov_b32 v125, v124\nMvaa 148,180,16\nv_permlane16_swap_b32 v104, v105\n" \
"v_permlane16_swap_b32 v124, v125\nMvaa 148,84,0\nv_max_f32 v104, v104, v105\nv_mov_b32 v105, v104\nMvaa 152,88,0\nD 13,64,s74,2048\nMvaa 152,184,16\nv_max_f32 v124, v124, v125\n" \
"v_mov_b32 v125, v124\nMvaa 156,188,16\nv_permlane32_swap_b32 v104, v105\nv_permlane32_swap_b32 v124, v125\nMvaa 156,92,0\nv_max_f32 v104, v104, v105\nv_mul_f32 v104, s93, v104\nMvaa 188,92,4\n" \
"s_waitcnt vmcnt(18)\ns_barrier\nMvaa 188,188,20\nTa 96,20,16384\nTa 98,20,17408\nMvaa 184,184,20\nD 13,64,s75,3072\nMvaa 184,88,4\n" \
"Ta 128,20,24576\nTa 130,20,25600\nMvaa 180,84,4\nv_fma_f32 v88, v88, s93, -v104\nv_fma_f32 v89, v89, s93, -v104\nMvaa 180,180,20\nRa 32,18,0\nRa 48,19,0\n" \
"Mvaa 176,176,20\ns_add_u32 m0, s88, 36864\nTa 100,21,16384\nMvaa 176,80,4\nD 13,64,s76,0\nMvaa 172,76,4\nTa 102,21,17408\nTa 132,21,24576\n" \
"Mvaa 172,172,20\nv_fma_f32 v90, v90, s93, -v104\nv_fma_f32 v91, v91, s93, -v104\nMvaa 168,168,20\nTa 134,21,25600\nTa 104,22,16384\nMvaa 168,72,4\nTa 106,22,17408\n" \
"Ta 136,22,24576\nMvaa 164,68,4\nD 13,64,s77,1024\nMvaa 164,164,20\nv_fma_f32 v92, v92, s93, -v104\nv_fma_f32 v93, v93, s93, -v104\nMvaa 160,160,20\nTa 138,22,25600\n" \
"Ta 108,23,16384\nMvaa 160,64,4\nv_fma_f32 v94, v94, s93, -v104\nv_fma_f32 v95, v95, s93, -v104\nMvaa 192,64,8\nTa 110,23,17408\nTa 140,23,24576\nMvaa 192,160,24\n" \
"D 13,64,s78,2048\nMvaa 196,164,24\nTa 142,23,25600\nTa 112,24,16384\nMvaa 196,68,8\nE 88,88\nMvaa 200,72,8\nTa 114,24,17408\n" \
"Ta 144,24,24576\nMvaa 200,168,24\nTa 146,24,25600\nTa 116,25,16384\nMvaa 204,172,24\nD 13,64,s79,3072\nMvaa 204,76,8\nE 89,89\n" \
"Mvaa 208,80,8\nTa 118,25,17408\nTa 148,25,24576\nMvaa 208,176,24\nv_add_u32 v13, 128, v13\nTa 150,25,25600\nMvaa 212,180,24\ns_add_u32 m0, s32, 98304\n" \
"Ta 120,26,16384\nMvaa 212,84,8\nD 14,68,0,0\nMvaa 216,88,8\nTa 122,26,17408\nTa 152,26,24576\nMvaa 216,184,24\nE 90,90\n" \
"Mvaa 220,188,24\nTa 154,26,25600\nTa 124,27,16384\nMvaa 220,92,8\nC 96,88,89\nMvaa 252,92,12\nD 14,68,s89,1024\nMvaa 252,188,28\n" \
"Ta 126,27,17408\nTa 156,27,24576\nMvaa 248,184,28\nTa 158,27,25600\nRa 36,18,2048\nMvaa 248,88,12\nE 91,91\nMvaa 244,84,12\n" \
"Ra 52,19,2048\nRa 40,18,4096\nMvaa 244,180,28\nD 28,68,s90,2048\nMvaa 240,176,28\nRa 56,19,4096\nRa 44,18,6144\nMvaa 240,80,12\n" \
"E 92,92\nMvaa 236,76,12\nRa 60,19,6144\nC 97,90,91\nMvaa 236,172,28\nE 93,93\nMvaa 232,168,28\nD 28,68,s91,3072\n" \
"Mvaa 232,72,12\nE 94,94\nMvaa 228,68,12\nC 98,92,93\nMvaa 228,164,28\nv_add_u32 v14, s72, v14\nMvaa 224,160,28\nv_add_u32 v28, s72, v28\n" \
"Mvaa 224,64,12\ns_waitcnt lgkmcnt(6)\nMvaa 128,96,32\nE 95,95\nMvaa 128,128,48\ns_add_u32 m0, s88, 131072\nv_max_f32 v124, v124, v125\nMvaa 132,132,48\n" \
"D 13,64,0,0\nMvaa 132,100,32\nv_mul_f32 v124, s93, v124\nv_fma_f32 v108, v108, s93, -v124\nMvaa 136,104,32\nv_fma_f32 v109, v109, s93, -v124\nv_fma_f32 v110, v110, s93, -v124\nMvaa 136,136,48\n" \
"v_fma_f32 v111, v111, s93, -v124\nv_fma_f32 v112, v112, s93, -v124\nMvaa 140,140,48\nv_fma_f32 v113, v113, s93, -v124\nv_fma_f32 v114, v114, s93, -v124\nMvaa 140,108,32\ns_waitcnt lgkmcnt(0)\nD 13,64,s73,1024\n" \
"Mvaa 144,112,32\nv_fma_f32 v115, v115, s93, -v124\nMvaa 144,144,48\nE 108,108\nMvaa 148,148,48\nE 109,109\nMvaa 148,116,32\nE 110,110\n" \
"Mvaa 152,120,32\nD 13,64,s74,2048\nMvaa 152,152,48\nC 116,108,109\nMvaa 156,156,48\nE 111,111\nMvaa 156,124,32\nE 112,112\n" \
"Mvaa 188,124,36\ns_waitcnt vmcnt(15)\ns_barrier\nMvaa 188,156,52\nTa 64,20,0\nTa 66,20,1024\nMvaa 184,152,52\nD 13,64,s75,3072\n" \
"Mvaa 184,120,36\nTa 160,20,8192\nTa 162,20,9216\nbuffer_store_dwordx2 v[2:3], off, s[60:63], s48 sc1\nMvaa 180,116,36\nC 117,110,111\nMvaa 180,148,52\nRa 0,16,0\n" \
"Ra 16,17,0\nMvaa 176,144,52\ns_add_u32 m0, s88, 135168\nTa 68,21,0\nMvaa 176,112,36\nD 13,64,s76,0\nMvaa 172,108,36\nTa 70,21,1024\n" \
"Ta 164,21,8192\nMvaa 172,140,52\nE 113,113\nMvaa 168,136,52\nTa 166,21,9216\nTa 72,22,0\nMvaa 168,104,36\nTa 74,22,1024\n" \
"Ta 168,22,8192\nMvaa 164,100,36\nD 13,64,s77,1024\nMvaa 164,132,52\nE 114,114\nMvaa 160,128,52\nTa 170,22,9216\nTa 76,23,0\n" \
"Mvaa 160,96,36\nC 118,112,113\nMvaa 192,96,40\nTa 78,23,1024\nTa 172,23,8192\nMvaa 192,128,56\nD 13,64,s78,2048\nMvaa 196,132,56\n" \
"Ta 174,23,9216\nTa 80,24,0\nMvaa 196,100,40\nE 115,115\nMvaa 200,104,40\nTa 82,24,1024\nTa 176,24,8192\nMvaa 200,136,56\n" \
"Ta 178,24,9216\nTa 84,25,0\nMvaa 204,140,56\nD 13,64,s79,3072\nMvaa 204,108,40\nC 99,94,95\nC 119,114,115\nMvaa 208,112,40\n" \
"Ta 86,25,1024\nTa 180,25,8192\nZvvv 100,84,96\nZvvv 88,34,96\nZvvv 92,52,96\nZvvv 120,84,116\nZvvv 108,36,116\nZvvv 112,54,116\n" \
"Mvaa 208,144,56\nv_add_u32 v13, 128, v13\nTa 182,25,9216\nMvaa 212,148,56\ns_add_u32 m0, s32, 81920\nTa 88,26,0\nMvaa 212,116,40\nD 14,68,0,0\n" \
"Mvaa 216,120,40\nTa 90,26,1024\nTa 184,26,8192\nMvaa 216,152,56\nv_rcp_f32 v104, v100\nMvaa 220,156,56\nTa 186,26,9216\nTa 92,27,0\n" \
"Mvaa 220,124,40\nv_rcp_f32 v124, v120\nMvaa 252,124,44\nD 14,68,s89,1024\nMvaa 252,156,60\nTa 94,27,1024\nTa 188,27,8192\nMvaa 248,152,60\n" \
"Ta 190,27,9216\nRa 4,16,2048\nMvaa 248,120,44\nv_mul_f32 v88, v88, v104\nv_mul_f32 v89, v89, v104\nMvaa 244,116,44\nRa 20,17,2048\nRa 8,16,4096\n" \
"Mvaa 244,148,60\nD 28,68,s90,2048\nMvaa 240,144,60\nRa 24,17,4096\nRa 12,16,6144\nMvaa 240,112,44\nv_mul_f32 v90, v90, v104\nv_mul_f32 v91, v91, v104\n" \
"Mvaa 236,108,44\nRa 28,17,6144\nv_mul_f32 v92, v92, v104\nMvaa 236,140,60\nv_mul_f32 v93, v93, v104\nv_mul_f32 v94, v94, v104\nMvaa 232,136,60\nD 28,68,s91,3072\n" \
"Mvaa 232,104,44\nv_mul_f32 v95, v95, v104\nC 96,88,89\nMvaa 228,100,44\nC 97,90,91\nC 98,92,93\nMvaa 228,132,60\nv_add_u32 v14, s72, v14\n" \
"C 99,94,95\nMvaa 224,128,60\nv_add_u32 v28, s72, v28\nMvaa 224,96,44\ns_waitcnt lgkmcnt(6)\nMvaa 128,64,0\ns_add_u32 s95, s50, 2048\nbuffer_store_dwordx4 v[96:99], v0, s[60:63], s95 offen sc1\n" \
"Mvaa 128,160,16\ns_add_u32 m0, s88, 0\nv_mul_f32 v108, v108, v124\nMvaa 132,164,16\nD 13,64,0,0\nMvaa 132,68,0\nv_mul_f32 v109, v109, v124\nv_mul_f32 v110, v110, v124\n" \
"Mvaa 136,72,0\nv_mul_f32 v111, v111, v124\nv_mul_f32 v112, v112, v124\nMvaa 136,168,16\nv_mul_f32 v113, v113, v124\nv_mul_f32 v114, v114, v124\nMvaa 140,172,16\nv_mul_f32 v115, v115, v124\n" \
"C 116,108,109\nMvaa 140,76,0\ns_waitcnt lgkmcnt(0)\nD 13,64,s73,1024\nMvaa 144,80,0\nC 117,110,111\nC 118,112,113\nMvaa 144,176,16\n" \
"C 119,114,115\nMvaa 148,180,16\ns_add_u32 s95, s50, 4096\nbuffer_store_dwordx4 v[116:119], v0, s[60:63], s95 offen sc1\nMvaa 148,84,0\nQvaa 88,232,204,72\nQvaa 92,236,204,76\nQvaa 108,236,208,72\n" \
"Qvaa 112,240,208,76\nMvaa 152,88,0\nD 13,64,s74,2048\nMvaa 152,184,16\nMvaa 156,188,16\nMvaa 156,92,0\nMvaa 188,92,4\ns_waitcnt vmcnt(18)\n" \
"s_barrier\nMvaa 188,188,20\nTa 96,20,32768\nTa 98,20,33792\nMvaa 184,184,20\nD 13,64,s75,3072\nMvaa 184,88,4\nTa 128,20,40960\n" \
"Ta 130,20,41984\nMvaa 180,84,4\nv_max3_f32 v104, v88, v89, v90\nv_max3_f32 v105, v91, v92, v93\nMvaa 180,180,20\nRa 32,16,32768\nRa 48,17,32768\nMvaa 176,176,20\n" \
"s_add_u32 m0, s88, 4096\nTa 100,21,32768\nMvaa 176,80,4\nD 13,64,s76,0\nMvaa 172,76,4\nTa 102,21,33792\nTa 132,21,40960\nMvaa 172,172,20\n" \
"v_max3_f32 v104, v104, v94, v95\nv_max_f32 v104, v104, v105\nMvaa 168,168,20\nTa 134,21,41984\nTa 104,22,32768\nMvaa 168,72,4\nTa 106,22,33792\nTa 136,22,40960\n" \
"Mvaa 164,68,4\nD 13,64,s77,1024\nMvaa 164,164,20\nv_mov_b32 v105, v104\nv_max3_f32 v124, v108, v109, v110\nMvaa 160,160,20\nTa 138,22,41984\nTa 108,23,32768\n" \
"Mvaa 160,64,4\nv_max3_f32 v125, v111, v112, v113\nv_max3_f32 v124, v124, v114, v115\nMvaa 192,64,8\nTa 110,23,33792\nTa 140,23,40960\nMvaa 192,160,24\nD 13,64,s78,2048\n" \
"Mvaa 196,164,24\nTa 142,23,41984\nTa 112,24,32768\nMvaa 196,68,8\nv_max_f32 v124, v124, v125\ns_branch 99f\n.p2align 12\nzp23:\n" \
"s_setpc_b64 s[98:99]\n99:\nv_mov_b32 v125, v124\nMvaa 200,72,8\nTa 114,24,33792\nTa 144,24,40960\nMvaa 200,168,24\nTa 146,24,41984\n" \
"Ta 116,25,32768\nMvaa 204,172,24\nD 13,64,s79,3072\nMvaa 204,76,8\nv_permlane16_swap_b32 v104, v105\nv_permlane16_swap_b32 v124, v125\nMvaa 208,80,8\nTa 118,25,33792\n" \
"Ta 148,25,40960\nMvaa 208,176,24\nv_add_u32 v13, 128, v13\nTa 150,25,41984\nMvaa 212,180,24\ns_add_u32 m0, s32, 65536\nTa 120,26,32768\nMvaa 212,84,8\n" \
"D 14,68,0,0\nMvaa 216,88,8\nTa 122,26,33792\nTa 152,26,40960\nMvaa 216,184,24\nv_max_f32 v104, v104, v105\nv_mov_b32 v105, v104\nMvaa 220,188,24\n" \
"Ta 154,26,41984\nTa 124,27,32768\nMvaa 220,92,8\nv_max_f32 v124, v124, v125\nv_mov_b32 v125, v124\nMvaa 252,92,12\nD 14,68,s89,1024\nMvaa 252,188,28\n" \
"Ta 126,27,33792\nTa 156,27,40960\nMvaa 248,184,28\nTa 158,27,41984\nRa 36,16,34816\nMvaa 248,88,12\nv_permlane32_swap_b32 v104, v105\nv_permlane32_swap_b32 v124, v125\n" \
"Mvaa 244,84,12\nRa 52,17,34816\nRa 40,16,36864\nMvaa 244,180,28\nD 28,68,s90,2048\nMvaa 240,176,28\nRa 56,17,36864\nRa 44,16,38912\n" \
"Mvaa 240,80,12\nv_max_f32 v104, v104, v105\nv_mul_f32 v104, s93, v104\nMvaa 236,76,12\nRa 60,17,38912\nv_fma_f32 v88, v88, s93, -v104\nMvaa 236,172,28\nv_fma_f32 v89, v89, s93, -v104\n" \
"v_fma_f32 v90, v90, s93, -v104\nMvaa 232,168,28\nD 28,68,s91,3072\nMvaa 232,72,12\nv_fma_f32 v91, v91, s93, -v104\nv_fma_f32 v92, v92, s93, -v104\nMvaa 228,68,12\nv_fma_f32 v93, v93, s93, -v104\n" \
"v_fma_f32 v94, v94, s93, -v104\nMvaa 228,164,28\nv_add_u32 v14, s72, v14\nv_fma_f32 v95, v95, s93, -v104\nMvaa 224,160,28\nv_add_u32 v28, s72, v28\nMvaa 224,64,12\ns_waitcnt lgkmcnt(6)\n" \
"Mvaa 128,96,32\nE 88,88\nMvaa 128,128,48\ns_add_u32 m0, s88, 32768\nMvaa 132,132,48\nD 13,64,0,0\nMvaa 132,100,32\nE 89,89\n" \
"Mvaa 136,104,32\nE 90,90\nMvaa 136,136,48\nC 96,88,89\nMvaa 140,140,48\nE 91,91\nMvaa 140,108,32\ns_waitcnt lgkmcnt(0)\n" \
"D 13,64,s73,1024\nMvaa 144,112,32\nE 92,92\nMvaa 144,144,48\nC 97,90,91\nMvaa 148,148,48\nE 93,93\nMvaa 148,116,32\n" \
"E 94,94\nMvaa 152,120,32\nD 13,64,s74,2048\nMvaa 152,152,48\nC 98,92,93\nMvaa 156,156,48\nE 95,95\nMvaa 156,124,32\n" \
"v_max_f32 v124, v124, v125\nv_mul_f32 v124, s93, v124\nMvaa 188,124,36\ns_waitcnt vmcnt(17)\ns_barrier\nMvaa 188,156,52\nTa 64,20,16384\nTa 66,20,17408\n" \
"Mvaa 184,152,52\nD 13,64,s75,3072\nMvaa 184,120,36\nTa 160,20,24576\nTa 162,20,25600\nMvaa 180,116,36\nv_fma_f32 v108, v108, s93, -v124\nv_fma_f32 v109, v109, s93, -v124\n" \
"Mvaa 180,148,52\nRa 0,18,0\nRa 16,19,0\nMvaa 176,144,52\ns_add_u32 m0, s88, 36864\nTa 68,21,16384\nMvaa 176,112,36\nD 13,64,s76,0\n" \
"Mvaa 172,108,36\nTa 70,21,17408\nTa 164,21,24576\nMvaa 172,140,52\nv_fma_f32 v110, v110, s93, -v124\nv_fma_f32 v111, v111, s93, -v124\nMvaa 168,136,52\nTa 166,21,25600\n" \
"Ta 72,22,16384\nMvaa 168,104,36\nTa 74,22,17408\nTa 168,22,24576\nMvaa 164,100,36\nD 13,64,s77,1024\nMvaa 164,132,52\nv_fma_f32 v112, v112, s93, -v124\n" \
"v_fma_f32 v113, v113, s93, -v124\nMvaa 160,128,52\nTa 170,22,25600\nTa 76,23,16384\nMvaa 160,96,36\nv_fma_f32 v114, v114, s93, -v124\nv_fma_f32 v115, v115, s93, -v124\nMvaa 192,96,40\n" \
"Ta 78,23,17408\nTa 172,23,24576\nMvaa 192,128,56\nD 13,64,s78,2048\nMvaa 196,132,56\nTa 174,23,25600\nTa 80,24,16384\nMvaa 196,100,40\n" \
"E 108,108\nMvaa 200,104,40\nTa 82,24,17408\nTa 176,24,24576\nMvaa 200,136,56\nTa 178,24,25600\nTa 84,25,16384\nMvaa 204,140,56\n" \
"D 13,64,s79,3072\nMvaa 204,108,40\nE 109,109\nMvaa 208,112,40\nTa 86,25,17408\nTa 180,25,24576\nMvaa 208,144,56\nv_add_u32 v13, 128, v13\n" \
"Ta 182,25,25600\nMvaa 212,148,56\ns_add_u32 m0, s32, 98304\nTa 88,26,16384\nMvaa 212,116,40\nD 14,68,0,0\nMvaa 216,120,40\nTa 90,26,17408\n" \
"Ta 184,26,24576\nMvaa 216,152,56\nE 110,110\nMvaa 220,156,56\nTa 186,26,25600\nTa 92,27,16384\nMvaa 220,124,40\nC 116,108,109\n" \
"Mvaa 252,124,44\nD 14,68,s89,1024\nMvaa 252,156,60\nTa 94,27,17408\nTa 188,27,24576\nMvaa 248,152,60\nTa 190,27,25600\nRa 4,18,2048\n" \
"Mvaa 248,120,44\nE 111,111\nMvaa 244,116,44\nRa 20,19,2048\nRa 8,18,4096\nMvaa 244,148,60\nD 28,68,s90,2048\nMvaa 240,144,60\n" \
"Ra 24,19,4096\nRa 12,18,6144\nMvaa 240,112,44\nE 112,112\nMvaa 236,108,44\nRa 28,19,6144\nC 117,110,111\nMvaa 236,140,60\n" \
"E 113,113\nMvaa 232,136,60\nD 28,68,s91,3072\nMvaa 232,104,44\nE 114,114\nMvaa 228,100,44\nC 118,112,113\nMvaa 228,132,60\n" \
"v_add_u32 v14, s72, v14\nMvaa 224,128,60\nv_add_u32 v28, s72, v28\nMvaa 224,96,44\ns_waitcnt lgkmcnt(6)\nMvaa 128,64,0\nE 115,115\nMvaa 128,160,16\n" \
"s_add_u32 m0, s88, 131072\nC 99,94,95\nMvaa 132,164,16\nD 13,64,0,0\nMvaa 132,68,0\nC 119,114,115\nZvvv 100,84,96\nZvvv 88,38,96\n" \
"Zvvv 92,56,96\nZvvv 120,84,116\nZvvv 108,40,116\nZvvv 112,58,116\nMvaa 136,72,0\nMvaa 136,168,16\nMvaa 140,172,16\nv_rcp_f32 v104, v100\n" \
"Mvaa 140,76,0\ns_waitcnt lgkmcnt(0)\nD 13,64,s73,1024\nMvaa 144,80,0\nv_rcp_f32 v124, v120\nMvaa 144,176,16\nv_mul_f32 v88, v88, v104\nv_mul_f32 v89, v89, v104\n" \
"Mvaa 148,180,16\nv_mul_f32 v90, v90, v104\nv_mul_f32 v91, v91, v104\nMvaa 148,84,0\nv_mul_f32 v92, v92, v104\nv_mul_f32 v93, v93, v104\nMvaa 152,88,0\nD 13,64,s74,2048\n" \
"Mvaa 152,184,16\nv_mul_f32 v94, v94, v104\nv_mul_f32 v95, v95, v104\nMvaa 156,188,16\nC 96,88,89\nC 97,90,91\nMvaa 156,92,0\nC 98,92,93\n" \
"C 99,94,95\nMvaa 188,92,4\ns_waitcnt vmcnt(15)\ns_barrier\nMvaa 188,188,20\nTa 96,20,0\nTa 98,20,1024\nMvaa 184,184,20\n" \
"D 13,64,s75,3072\nMvaa 184,88,4\nTa 128,20,8192\nTa 130,20,9216\nMvaa 180,84,4\ns_add_u32 s95, s50, 6144\nbuffer_store_dwordx4 v[96:99], v0, s[60:63], s95 offen sc1\nMvaa 180,180,20\n" \
"Ra 32,16,0\nRa 48,17,0\nMvaa 176,176,20\ns_add_u32 m0, s88, 135168\nTa 100,21,0\nMvaa 176,80,4\nD 13,64,s76,0\nMvaa 172,76,4\n" \
"Ta 102,21,1024\nTa 132,21,8192\nMvaa 172,172,20\nv_mul_f32 v108, v108, v124\nv_mul_f32 v109, v109, v124\nMvaa 168,168,20\nTa 134,21,9216\nTa 104,22,0\n" \
"Mvaa 168,72,4\nTa 106,22,1024\nTa 136,22,8192\nMvaa 164,68,4\nD 13,64,s77,1024\nMvaa 164,164,20\nv_mul_f32 v110, v110, v124\nv_mul_f32 v111, v111, v124\n" \
"Mvaa 160,160,20\nTa 138,22,9216\nTa 108,23,0\nMvaa 160,64,4\nv_mul_f32 v112, v112, v124\nv_mul_f32 v113, v113, v124\nMvaa 192,64,8\nTa 110,23,1024\n" \
"Ta 140,23,8192\nMvaa 192,160,24\nD 13,64,s78,2048\nMvaa 196,164,24\nTa 142,23,9216\nTa 112,24,0\nMvaa 196,68,8\nv_mul_f32 v114, v114, v124\n" \
"v_mul_f32 v115, v115, v124\nMvaa 200,72,8\nTa 114,24,1024\nTa 144,24,8192\nMvaa 200,168,24\nTa 146,24,9216\nTa 116,25,0\nMvaa 204,172,24\n" \
"D 13,64,s79,3072\nMvaa 204,76,8\nC 116,108,109\nC 117,110,111\nMvaa 208,80,8\nTa 118,25,1024\nTa 148,25,8192\nMvaa 208,176,24\n" \
"v_add_u32 v13, 128, v13\nTa 150,25,9216\nMvaa 212,180,24\ns_add_u32 m0, s32, 81920\nTa 120,26,0\nMvaa 212,84,8\nD 14,68,0,0\nMvaa 216,88,8\n" \
"Ta 122,26,1024\nTa 152,26,8192\nMvaa 216,184,24\nC 118,112,113\nC 119,114,115\nMvaa 220,188,24\nTa 154,26,9216\nTa 124,27,0\n" \
"Mvaa 220,92,8\ns_add_u32 s95, s50, 8192\nbuffer_store_dwordx4 v[116:119], v0, s[60:63], s95 offen sc1\nMvaa 252,92,12\nD 14,68,s89,1024\nQvaa 88,240,212,72\nQvaa 92,244,212,76\nQvaa 108,244,216,72\n" \
"Qvaa 112,248,216,76\nMvaa 252,188,28\nTa 126,27,1024\nTa 156,27,8192\nMvaa 248,184,28\nTa 158,27,9216\nRa 36,16,2048\nMvaa 248,88,12\n" \
"v_max3_f32 v104, v88, v89, v90\nv_max3_f32 v105, v91, v92, v93\nMvaa 244,84,12\nRa 52,17,2048\nRa 40,16,4096\nMvaa 244,180,28\nD 28,68,s90,2048\nMvaa 240,176,28\n" \
"Ra 56,17,4096\nRa 44,16,6144\nMvaa 240,80,12\nv_max3_f32 v104, v104, v94, v95\nv_max_f32 v104, v104, v105\nMvaa 236,76,12\nRa 60,17,6144\nv_mov_b32 v105, v104\n" \
"Mvaa 236,172,28\nv_max3_f32 v124, v108, v109, v110\nv_max3_f32 v125, v111, v112, v113\nMvaa 232,168,28\nD 28,68,s91,3072\nMvaa 232,72,12\nv_max3_f32 v124, v124, v114, v115\nv_max_f32 v124, v124, v125\n" \
"Mvaa 228,68,12\nv_mov_b32 v125, v124\nv_permlane16_swap_b32 v104, v105\nMvaa 228,164,28\nv_add_u32 v14, s72, v14\nv_permlane16_swap_b32 v124, v125\nMvaa 224,160,28\nv_add_u32 v28, s72, v28\n" \
"v_max_f32 v104, v104, v105\nMvaa 224,64,12\ns_waitcnt lgkmcnt(6)\nv_mov_b32 v105, v104\nMvaa 128,96,32\nv_max_f32 v124, v124, v125\nv_mov_b32 v125, v124\nMvaa 128,128,48\n" \
"s_add_u32 m0, s88, 0\nv_permlane32_swap_b32 v104, v105\nMvaa 132,132,48\nD 13,64,0,0\nMvaa 132,100,32\nv_permlane32_swap_b32 v124, v125\nv_max_f32 v104, v104, v105\nMvaa 136,104,32\n" \
"buffer_load_dwordx2 v[10:11], off, s[60:63], s49 sc1\nMvaa 136,136,48\nv_mul_f32 v104, s93, v104\nv_fma_f32 v88, v88, s93, -v104\nMvaa 140,140,48\nv_fma_f32 v89, v89, s93, -v104\nv_fma_f32 v90, v90, s93, -v104\nMvaa 140,108,32\n" \
"s_waitcnt lgkmcnt(0)\nD 13,64,s73,1024\nMvaa 144,112,32\nv_fma_f32 v91, v91, s93, -v104\nv_fma_f32 v92, v92, s93, -v104\nMvaa 144,144,48\nv_fma_f32 v93, v93, s93, -v104\nv_fma_f32 v94, v94, s93, -v104\n" \
"Mvaa 148,148,48\nv_fma_f32 v95, v95, s93, -v104\nMvaa 148,116,32\nE 88,88\nMvaa 152,120,32\nD 13,64,s74,2048\nMvaa 152,152,48\nE 89,89\n" \
"Mvaa 156,156,48\nE 90,90\nMvaa 156,124,32\nC 96,88,89\nMvaa 188,124,36\ns_waitcnt vmcnt(18)\ns_barrier\nMvaa 188,156,52\n" \
"Ta 64,20,32768\nTa 66,20,33792\nMvaa 184,152,52\nD 13,64,s75,3072\nMvaa 184,120,36\nTa 160,20,40960\nTa 162,20,41984\nMvaa 180,116,36\n" \
"E 91,91\nMvaa 180,148,52\nRa 0,16,32768\nRa 16,17,32768\nMvaa 176,144,52\ns_add_u32 m0, s88, 4096\nTa 68,21,32768\nMvaa 176,112,36\n" \
"D 13,64,s76,0\nMvaa 172,108,36\nTa 70,21,33792\nTa 164,21,40960\nMvaa 172,140,52\nE 92,92\nMvaa 168,136,52\nTa 166,21,41984\n" \
"Ta 72,22,32768\nMvaa 168,104,36\nTa 74,22,33792\nTa 168,22,40960\nMvaa 164,100,36\nD 13,64,s77,1024\nMvaa 164,132,52\nC 97,90,91\n" \
"Mvaa 160,128,52\nTa 170,22,41984\nTa 76,23,32768\nMvaa 160,96,36\nE 93,93\nMvaa 192,96,40\nTa 78,23,33792\nTa 172,23,40960\n" \
"Mvaa 192,128,56\nD 13,64,s78,2048\nMvaa 196,132,56\nTa 174,23,41984\nTa 80,24,32768\nMvaa 196,100,40\nE 94,94\nMvaa 200,104,40\n" \
"Ta 82,24,33792\nTa 176,24,40960\nMvaa 200,136,56\nTa 178,24,41984\nTa 84,25,32768\nMvaa 204,140,56\nD 13,64,s79,3072\nMvaa 204,108,40\n" \
"C 98,92,93\nMvaa 208,112,40\nTa 86,25,33792\nTa 180,25,40960\nMvaa 208,144,56\nv_add_u32 v13, 128, v13\nTa 182,25,41984\nMvaa 212,148,56\n" \
"s_add_u32 m0, s32, 65536\nTa 88,26,32768\nMvaa 212,116,40\nD 14,68,0,0\nMvaa 216,120,40\nTa 90,26,33792\nTa 184,26,40960\nMvaa 216,152,56\n" \
"E 95,95\nMvaa 220,156,56\nTa 186,26,41984\nTa 92,27,32768\nMvaa 220,124,40\nv_max_f32 v124, v124, v125\nv_mul_f32 v124, s93, v124\nMvaa 252,124,44\n" \
"D 14,68,s89,1024\nMvaa 252,156,60\nTa 94,27,33792\nTa 188,27,40960\nMvaa 248,152,60\nTa 190,27,41984\nRa 4,16,34816\nMvaa 248,120,44\n" \
"v_fma_f32 v108, v108, s93, -v124\nv_fma_f32 v109, v109, s93, -v124\nMvaa 244,116,44\nRa 20,17,34816\nRa 8,16,36864\nMvaa 244,148,60\ns_branch 99f\n.p2align 12\n" \
"zp24:\ns_setpc_b64 s[98:99]\n99:\nD 28,68,s90,2048\nMvaa 240,144,60\nRa 24,17,36864\nRa 12,16,38912\nMvaa 240,112,44\n" \
"v_fma_f32 v110, v110, s93, -v124\nv_fma_f32 v111, v111, s93, -v124\nMvaa 236,108,44\nRa 28,17,38912\nv_fma_f32 v112, v112, s93, -v124\nMvaa 236,140,60\nv_fma_f32 v113, v113, s93, -v124\nv_fma_f32 v114, v114, s93, -v124\n" \
"Mvaa 232,136,60\nD 28,68,s91,3072\nMvaa 232,104,44\nv_fma_f32 v115, v115, s93, -v124\nMvaa 228,100,44\nE 108,108\nMvaa 228,132,60\nv_add_u32 v14, s72, v14\n" \
"Mvaa 224,128,60\nv_add_u32 v28, s72, v28\nMvaa 224,96,44\ns_waitcnt lgkmcnt(6)\nMvaa 128,64,0\nE 109,109\nMvaa 128,160,16\ns_add_u32 m0, s88, 32768\n" \
"Mvaa 132,164,16\nD 13,64,0,0\nMvaa 132,68,0\nE 110,110\nMvaa 136,72,0\ns_waitcnt vmcnt(12)\nv_cmp_ne_u64 vcc, v[10:11], v[2:3]\ns_cbranch_vccnz zs103\n" \
"zr103:\nMvaa 136,168,16\nbuffer_load_dwordx4 v[68:71], v0, s[60:63], s47 offen sc1\nMvaa 140,172,16\nbuffer_load_dwordx2 v[32:33], v0, s[60:63], s47 offen offset:1024 sc1\nMvaa 140,76,0\ns_waitcnt lgkmcnt(0)\nD 13,64,s73,1024\n" \
"buffer_load_dwordx2 v[50:51], v0, s[60:63], s47 offen offset:1032 sc1\nMvaa 144,80,0\nC 116,108,109\nMvaa 144,176,16\nE 111,111\nMvaa 148,180,16\nE 112,112\nMvaa 148,84,0\n" \
"C 117,110,111\nMvaa 152,88,0\nD 13,64,s74,2048\nMvaa 152,184,16\nE 113,113\nMvaa 156,188,16\nE 114,114\nMvaa 156,92,0\n" \
"C 118,112,113\nMvaa 188,92,4\ns_waitcnt vmcnt(19)\ns_barrier\nMvaa 188,188,20\nTa 96,20,16384\nTa 98,20,17408\nMvaa 184,184,20\n" \
"D 13,64,s75,3072\nMvaa 184,88,4\nTa 128,20,24576\nTa 130,20,25600\nMvaa 180,84,4\nE 115,115\nMvaa 180,180,20\nRa 32,18,0\n" \
"Ra 48,19,0\nMvaa 176,176,20\ns_add_u32 m0, s88, 36864\nTa 100,21,16384\nMvaa 176,80,4\nD 13,64,s76,0\nMvaa 172,76,4\nTa 102,21,17408\n" \
"Ta 132,21,24576\nMvaa 172,172,20\nC 99,94,95\nC 119,114,115\nMvaa 168,168,20\nTa 134,21,25600\nTa 104,22,16384\nZvvv 100,84,96\n" \
"Zvvv 88,42,96\nZvvv 92,60,96\nZvvv 120,84,116\nZvvv 108,44,116\nZvvv 112,62,116\nMvaa 168,72,4\nTa 106,22,17408\nTa 136,22,24576\n" \
"Mvaa 164,68,4\nD 13,64,s77,1024\nMvaa 164,164,20\nv_rcp_f32 v104, v100\nMvaa 160,160,20\nTa 138,22,25600\nTa 108,23,16384\nMvaa 160,64,4\n" \
"v_rcp_f32 v124, v120\nMvaa 192,64,8\nTa 110,23,17408\nTa 140,23,24576\nMvaa 192,160,24\nD 13,64,s78,2048\nMvaa 196,164,24\nTa 142,23,25600\n" \
"Ta 112,24,16384\nMvaa 196,68,8\nv_mul_f32 v88, v88, v104\nv_mul_f32 v89, v89, v104\nMvaa 200,72,8\nTa 114,24,17408\nTa 144,24,24576\nMvaa 200,168,24\n" \
"Ta 146,24,25600\nTa 116,25,16384\nMvaa 204,172,24\nD 13,64,s79,3072\nMvaa 204,76,8\nv_mul_f32 v90, v90, v104\nv_mul_f32 v91, v91, v104\nMvaa 208,80,8\n" \
"Ta 118,25,17408\nTa 148,25,24576\nMvaa 208,176,24\nv_add_u32 v13, 128, v13\nTa 150,25,25600\nMvaa 212,180,24\ns_add_u32 m0, s32, 98304\nTa 120,26,16384\n" \
"Mvaa 212,84,8\nD 14,68,0,0\nMvaa 216,88,8\nTa 122,26,17408\nTa 152,26,24576\nMvaa 216,184,24\nv_mul_f32 v92, v92, v104\nv_mul_f32 v93, v93, v104\n" \
"Mvaa 220,188,24\nTa 154,26,25600\nTa 124,27,16384\nMvaa 220,92,8\nv_mul_f32 v94, v94, v104\nv_mul_f32 v95, v95, v104\nMvaa 252,92,12\nD 14,68,s89,1024\n" \
"Mvaa 252,188,28\nTa 126,27,17408\nTa 156,27,24576\nMvaa 248,184,28\nTa 158,27,25600\nRa 36,18,2048\nMvaa 248,88,12\nC 96,88,89\n" \
"C 97,90,91\nMvaa 244,84,12\nRa 52,19,2048\nRa 40,18,4096\nMvaa 244,180,28\nD 28,68,s90,2048\nMvaa 240,176,28\nRa 56,19,4096\n" \
"Ra 44,18,6144\nMvaa 240,80,12\nC 98,92,93\nC 99,94,95\nMvaa 236,76,12\nRa 60,19,6144\nMvaa 236,172,28\ns_add_u32 s95, s50, 10240\n" \
"buffer_store_dwordx4 v[96:99], v0, s[60:63], s95 offen sc1\nMvaa 232,168,28\nD 28,68,s91,3072\nMvaa 232,72,12\nv_mul_f32 v108, v108, v124\nv_mul_f32 v109, v109, v124\nMvaa 228,68,12\nv_mul_f32 v110, v110, v124\n" \
"v_mul_f32 v111, v111, v124\nMvaa 228,164,28\nv_add_u32 v14, s72, v14\nv_mul_f32 v112, v112, v124\nMvaa 224,160,28\nv_add_u32 v28, s72, v28\nv_mul_f32 v113, v113, v124\nMvaa 224,64,12\n" \
"s_waitcnt lgkmcnt(6)\nv_mul_f32 v114, v114, v124\nMvaa 128,96,32\nv_mul_f32 v115, v115, v124\nC 116,108,109\nMvaa 128,128,48\ns_add_u32 m0, s88, 131072\nC 117,110,111\n" \
"Mvaa 132,132,48\nD 13,64,0,0\nMvaa 132,100,32\nC 118,112,113\nC 119,114,115\nMvaa 136,104,32\ns_add_u32 s95, s50, 12288\nbuffer_store_dwordx4 v[116:119], v0, s[60:63], s95 offen sc1\n" \
"Mvaa 136,136,48\nMvaa 140,140,48\nMvaa 140,108,32\ns_waitcnt lgkmcnt(0)\nD 13,64,s73,1024\nMvaa 144,112,32\nMvaa 144,144,48\nMvaa 148,148,48\n" \
"Mvaa 148,116,32\nMvaa 152,120,32\nD 13,64,s74,2048\nMvaa 152,152,48\nMvaa 156,156,48\nMvaa 156,124,32\nMvaa 188,124,36\ns_waitcnt vmcnt(20)\n" \
"s_barrier\nMvaa 188,156,52\nTa 64,20,0\nTa 66,20,1024\nMvaa 184,152,52\nD 13,64,s75,3072\nMvaa 184,120,36\nTa 160,20,8192\n" \
"Ta 162,20,9216\nMvaa 180,116,36\ns_waitcnt vmcnt(16)\nQvaa 88,248,220,72\nQvaa 92,252,220,76\nQvva 108,68,192,80\nQvaa 112,224,192,76\nMvaa 180,148,52\n" \
"Ra 0,16,0\nRa 16,17,0\nMvaa 176,144,52\ns_add_u32 m0, s88, 135168\nTa 68,21,0\nMvaa 176,112,36\nD 13,64,s76,0\nMvaa 172,108,36\n" \
"Ta 70,21,1024\nTa 164,21,8192\nMvaa 172,140,52\nv_max3_f32 v104, v88, v89, v90\nv_max3_f32 v105, v91, v92, v93\nMvaa 168,136,52\nTa 166,21,9216\nTa 72,22,0\n" \
"Mvaa 168,104,36\nTa 74,22,1024\nTa 168,22,8192\nMvaa 164,100,36\nD 13,64,s77,1024\nMvaa 164,132,52\nv_max3_f32 v104, v104, v94, v95\nv_max_f32 v104, v104, v105\n" \
"Mvaa 160,128,52\nTa 170,22,9216\nTa 76,23,0\nMvaa 160,96,36\nv_mov_b32 v105, v104\nv_max3_f32 v124, v108, v109, v110\nMvaa 192,96,40\nTa 78,23,1024\n" \
"Ta 172,23,8192\nMvaa 192,128,56\nD 13,64,s78,2048\nMvaa 196,132,56\nTa 174,23,9216\nTa 80,24,0\nMvaa 196,100,40\nv_max3_f32 v125, v111, v112, v113\n" \
"v_max3_f32 v124, v124, v114, v115\nMvaa 200,104,40\nTa 82,24,1024\nTa 176,24,8192\nMvaa 200,136,56\nTa 178,24,9216\nTa 84,25,0\nMvaa 204,140,56\n" \
"D 13,64,s79,3072\nMvaa 204,108,40\nv_max_f32 v124, v124, v125\nv_mov_b32 v125, v124\nMvaa 208,112,40\nTa 86,25,1024\nTa 180,25,8192\nMvaa 208,144,56\n" \
"v_add_u32 v13, 128, v13\nTa 182,25,9216\nMvaa 212,148,56\ns_add_u32 m0, s32, 81920\nTa 88,26,0\nMvaa 212,116,40\nD 14,68,0,0\nMvaa 216,120,40\n" \
"Ta 90,26,1024\nTa 184,26,8192\nMvaa 216,152,56\nv_permlane16_swap_b32 v104, v105\nv_permlane16_swap_b32 v124, v125\nMvaa 220,156,56\nTa 186,26,9216\nTa 92,27,0\n" \
"Mvaa 220,124,40\nv_max_f32 v104, v104, v105\nv_mov_b32 v105, v104\nMvaa 252,124,44\nD 14,68,s89,1024\nMvaa 252,156,60\nTa 94,27,1024\nTa 188,27,8192\n" \
"Mvaa 248,152,60\nTa 190,27,9216\nRa 4,16,2048\nMvaa 248,120,44\nv_max_f32 v124, v124, v125\nv_mov_b32 v125, v124\nMvaa 244,116,44\nRa 20,17,2048\n" \
"Ra 8,16,4096\nMvaa 244,148,60\nD 28,68,s90,2048\nMvaa 240,144,60\nRa 24,17,4096\nRa 12,16,6144\nMvaa 240,112,44\nv_permlane32_swap_b32 v104, v105\n" \
"v_permlane32_swap_b32 v124, v125\nMvaa 236,108,44\nRa 28,17,6144\nv_max_f32 v104, v104, v105\nMvaa 236,140,60\nv_mul_f32 v104, s93, v104\nv_fma_f32 v88, v88, s93, -v104\nMvaa 232,136,60\n" \
"D 28,68,s91,3072\nMvaa 232,104,44\nv_fma_f32 v89, v89, s93, -v104\nv_fma_f32 v90, v90, s93, -v104\nMvaa 228,100,44\nv_fma_f32 v91, v91, s93, -v104\nv_fma_f32 v92, v92, s93, -v104\nMvaa 228,132,60\n" \
"v_add_u32 v14, s72, v14\nv_fma_f32 v93, v93, s93, -v104\nMvaa 224,128,60\nv_add_u32 v28, s72, v28\nv_fma_f32 v94, v94, s93, -v104\nMvaa 224,96,44\ns_waitcnt lgkmcnt(6)\nv_fma_f32 v95, v95, s93, -v104\n" \
"Mvaa 128,64,0\nE 88,88\nMvaa 128,160,16\ns_add_u32 m0, s88, 0\nMvaa 132,164,16\nD 13,64,0,0\nMvaa 132,68,0\nE 89,89\n" \
"Mvaa 136,72,0\nE 90,90\nMvaa 136,168,16\nC 96,88,89\nMvaa 140,172,16\nE 91,91\nMvaa 140,76,0\ns_waitcnt lgkmcnt(0)\n" \
"D 13,64,s73,1024\nMvaa 144,80,0\nE 92,92\nMvaa 144,176,16\nC 97,90,91\nMvaa 148,180,16\nE 93,93\nMvaa 148,84,0\n" \
"E 94,94\nMvaa 152,88,0\nD 13,64,s74,2048\nMvaa 152,184,16\nC 98,92,93\nMvaa 156,188,16\nE 95,95\nMvaa 156,92,0\n" \
"v_max_f32 v124, v124, v125\nv_mul_f32 v124, s93, v124\nMvaa 188,92,4\ns_waitcnt vmcnt(16)\ns_barrier\nMvaa 188,188,20\nTa 96,20,32768\nTa 98,20,33792\n" \
"Mvaa 184,184,20\nD 13,64,s75,3072\nMvaa 184,88,4\nTa 128,20,40960\nTa 130,20,41984\nMvaa 180,84,4\nv_fma_f32 v108, v108, s93, -v124\nv_fma_f32 v109, v109, s93, -v124\n" \
"Mvaa 180,180,20\nRa 32,16,32768\nRa 48,17,32768\nMvaa 176,176,20\ns_add_u32 m0, s88, 4096\nTa 100,21,32768\nMvaa 176,80,4\nD 13,64,s76,0\n" \
"Mvaa 172,76,4\nTa 102,21,33792\nTa 132,21,40960\nMvaa 172,172,20\nv_fma_f32 v110, v110, s93, -v124\nv_fma_f32 v111, v111, s93, -v124\nMvaa 168,168,20\nTa 134,21,41984\n" \
"Ta 104,22,32768\nMvaa 168,72,4\nTa 106,22,33792\nTa 136,22,40960\nMvaa 164,68,4\nD 13,64,s77,1024\nMvaa 164,164,20\nv_fma_f32 v112, v112, s93, -v124\n" \
"v_fma_f32 v113, v113, s93, -v124\nMvaa 160,160,20\nTa 138,22,41984\nTa 108,23,32768\nMvaa 160,64,4\nv_fma_f32 v114, v114, s93, -v124\nv_fma_f32 v115, v115, s93, -v124\nMvaa 192,64,8\n" \
"Ta 110,23,33792\nTa 140,23,40960\nMvaa 192,160,24\nD 13,64,s78,2048\nMvaa 196,164,24\nTa 142,23,41984\nTa 112,24,32768\nMvaa 196,68,8\n" \
"E 108,108\nMvaa 200,72,8\nTa 114,24,33792\nTa 144,24,40960\nMvaa 200,168,24\nTa 146,24,41984\nTa 116,25,32768\nMvaa 204,172,24\n" \
"D 13,64,s79,3072\nMvaa 204,76,8\nE 109,109\nMvaa 208,80,8\nTa 118,25,33792\nTa 148,25,40960\nMvaa 208,176,24\nv_add_u32 v13, 128, v13\n" \
"Ta 150,25,41984\nMvaa 212,180,24\ns_add_u32 m0, s32, 65536\nTa 120,26,32768\nMvaa 212,84,8\nD 14,68,0,0\nMvaa 216,88,8\nTa 122,26,33792\n" \
"Ta 152,26,40960\nMvaa 216,184,24\nE 110,110\nMvaa 220,188,24\nTa 154,26,41984\nTa 124,27,32768\nMvaa 220,92,8\nC 116,108,109\n" \
"Mvaa 252,92,12\nD 14,68,s89,1024\nMvaa 252,188,28\nTa 126,27,33792\nTa 156,27,40960\nMvaa 248,184,28\nTa 158,27,41984\nRa 36,16,34816\n" \
"Mvaa 248,88,12\nE 111,111\nMvaa 244,84,12\nRa 52,17,34816\nRa 40,16,36864\nMvaa 244,180,28\nD 28,68,s90,2048\nMvaa 240,176,28\n" \
"Ra 56,17,36864\nRa 44,16,38912\nMvaa 240,80,12\nE 112,112\nMvaa 236,76,12\nRa 60,17,38912\nC 117,110,111\nMvaa 236,172,28\n" \
"E 113,113\nMvaa 232,168,28\nD 28,68,s91,3072\nMvaa 232,72,12\nE 114,114\nMvaa 228,68,12\nC 118,112,113\nMvaa 228,164,28\n" \
"v_add_u32 v14, s72, v14\nMvaa 224,160,28\nv_add_u32 v28, s72, v28\nMvaa 224,64,12\ns_waitcnt lgkmcnt(6)\nMvaa 128,96,32\nE 115,115\nMvaa 128,128,48\n" \
"s_add_u32 m0, s88, 32768\nC 99,94,95\nMvaa 132,132,48\nD 13,64,0,0\nMvaa 132,100,32\nC 119,114,115\nZvvv 100,84,96\nZvvv 88,46,96\n" \
"Zvvv 92,64,96\nZvvv 120,84,116\nZvvv 108,32,116\nZvvv 112,50,116\nMvaa 136,104,32\nMvaa 136,136,48\nMvaa 140,140,48\nv_rcp_f32 v104, v100\n" \
"Mvaa 140,108,32\ns_waitcnt lgkmcnt(0)\nD 13,64,s73,1024\nMvaa 144,112,32\nv_rcp_f32 v124, v120\nMvaa 144,144,48\nv_mul_f32 v88, v88, v104\nv_mul_f32 v89, v89, v104\n" \
"Mvaa 148,148,48\nv_mul_f32 v90, v90, v104\nv_mul_f32 v91, v91, v104\nMvaa 148,116,32\nv_mul_f32 v92, v92, v104\nv_mul_f32 v93, v93, v104\nMvaa 152,120,32\nD 13,64,s74,2048\n" \
"Mvaa 152,152,48\nv_mul_f32 v94, v94, v104\ns_branch 99f\n.p2align 12\nzp25:\ns_setpc_b64 s[98:99]\n99:\nv_mul_f32 v95, v95, v104\n" \
"Mvaa 156,156,48\nC 96,88,89\nC 97,90,91\nMvaa 156,124,32\nC 98,92,93\nC 99,94,95\nMvaa 188,124,36\ns_waitcnt vmcnt(15)\n" \
"s_barrier\nMvaa 188,156,52\nTa 64,20,16384\nTa 66,20,17408\nMvaa 184,152,52\nD 13,64,s75,3072\nMvaa 184,120,36\nTa 160,20,24576\n" \
"Ta 162,20,25600\nMvaa 180,116,36\ns_add_u32 s95, s50, 14336\nbuffer_store_dwordx4 v[96:99], v0, s[60:63], s95 offen sc1\nMvaa 180,148,52\nRa 0,18,0\nRa 16,19,0\nMvaa 176,144,52\n" \
"s_add_u32 m0, s88, 36864\nTa 68,21,16384\nMvaa 176,112,36\nD 13,64,s76,0\nMvaa 172,108,36\nTa 70,21,17408\nTa 164,21,24576\nMvaa 172,140,52\n" \
"v_mul_f32 v108, v108, v124\nv_mul_f32 v109, v109, v124\nMvaa 168,136,52\nTa 166,21,25600\nTa 72,22,16384\nMvaa 168,104,36\nTa 74,22,17408\nTa 168,22,24576\n" \
"Mvaa 164,100,36\nD 13,64,s77,1024\nMvaa 164,132,52\nv_mul_f32 v110, v110, v124\nv_mul_f32 v111, v111, v124\nMvaa 160,128,52\nTa 170,22,25600\nTa 76,23,16384\n" \
"Mvaa 160,96,36\nv_mul_f32 v112, v112, v124\nv_mul_f32 v113, v113, v124\nMvaa 192,96,40\nTa 78,23,17408\nTa 172,23,24576\nMvaa 192,128,56\nD 13,64,s78,2048\n" \
"Mvaa 196,132,56\nTa 174,23,25600\nTa 80,24,16384\nMvaa 196,100,40\nv_mul_f32 v114, v114, v124\nv_mul_f32 v115, v115, v124\nMvaa 200,104,40\nTa 82,24,17408\n" \
"Ta 176,24,24576\nMvaa 200,136,56\nTa 178,24,25600\nTa 84,25,16384\nMvaa 204,140,56\nD 13,64,s79,3072\nMvaa 204,108,40\nC 116,108,109\n" \
"C 117,110,111\nMvaa 208,112,40\nTa 86,25,17408\nTa 180,25,24576\nMvaa 208,144,56\nv_add_u32 v13, 128, v13\nTa 182,25,25600\nMvaa 212,148,56\n" \
"s_add_u32 m0, s32, 98304\nTa 88,26,16384\nMvaa 212,116,40\nD 14,68,0,0\nMvaa 216,120,40\nTa 90,26,17408\nTa 184,26,24576\nMvaa 216,152,56\n" \
"C 118,112,113\nC 119,114,115\nMvaa 220,156,56\nTa 186,26,25600\nTa 92,27,16384\nMvaa 220,124,40\ns_add_u32 s95, s50, 0\nbuffer_store_dwordx4 v[116:119], v0, s[60:63], s95 offen sc1\n" \
"Mvaa 252,124,44\nD 14,68,s89,1024\nMvaa 252,156,60\nTa 94,27,17408\nTa 188,27,24576\nMvaa 248,152,60\nTa 190,27,25600\nRa 4,18,2048\n" \
"Mvaa 248,120,44\nMvaa 244,116,44\nRa 20,19,2048\nRa 8,18,4096\nMvaa 244,148,60\nD 28,68,s90,2048\nMvaa 240,144,60\nRa 24,19,4096\n" \
"Ra 12,18,6144\nMvaa 240,112,44\nMvaa 236,108,44\nRa 28,19,6144\nMvaa 236,140,60\nMvaa 232,136,60\nD 28,68,s91,3072\nMvaa 232,104,44\n" \
"Mvaa 228,100,44\nMvaa 228,132,60\nv_add_u32 v14, s72, v14\nMvaa 224,128,60\nv_add_u32 v28, s72, v28\nMvaa 224,96,44\ns_waitcnt lgkmcnt(6)\nMvaa 128,64,0\n" \
"Mvaa 128,160,16\ns_add_u32 m0, s88, 131072\nMvaa 132,164,16\nD 13,64,0,0\nMvaa 132,68,0\nMvaa 136,72,0\nMvaa 136,168,16\nMvaa 140,172,16\n" \
"Mvaa 140,76,0\ns_waitcnt lgkmcnt(0)\nD 13,64,s73,1024\nMvaa 144,80,0\nMvaa 144,176,16\nMvaa 148,180,16\nMvaa 148,84,0\nMvaa 152,88,0\n" \
"D 13,64,s74,2048\nMvaa 152,184,16\nMvaa 156,188,16\nMvaa 156,92,0\nMvaa 188,92,4\ns_waitcnt vmcnt(17)\ns_barrier\nMvaa 188,188,20\n" \
"Ta 96,20,0\nTa 98,20,1024\nMvaa 184,184,20\nD 13,64,s75,3072\nMvaa 184,88,4\nTa 128,20,8192\nTa 130,20,9216\nMvaa 180,84,4\n" \
"Mvaa 180,180,20\nRa 32,16,0\nRa 48,17,0\nMvaa 176,176,20\ns_add_u32 m0, s88, 135168\nTa 100,21,0\nMvaa 176,80,4\nD 13,64,s76,0\n" \
"Mvaa 172,76,4\nTa 102,21,1024\nTa 132,21,8192\nMvaa 172,172,20\nMvaa 168,168,20\nTa 134,21,9216\nTa 104,22,0\nMvaa 168,72,4\n" \
"Ta 106,22,1024\nTa 136,22,8192\nMvaa 164,68,4\nD 13,64,s77,1024\nMvaa 164,164,20\nMvaa 160,160,20\nTa 138,22,9216\nTa 108,23,0\n" \
"Mvaa 160,64,4\nMvaa 192,64,8\nTa 110,23,1024\nTa 140,23,8192\nMvaa 192,160,24\nD 13,64,s78,2048\nMvaa 196,164,24\nTa 142,23,9216\n" \
"Ta 112,24,0\nMvaa 196,68,8\nMvaa 200,72,8\nTa 114,24,1024\nTa 144,24,8192\nMvaa 200,168,24\nTa 146,24,9216\nTa 116,25,0\n" \
"Mvaa 204,172,24\nD 13,64,s79,3072\nMvaa 204,76,8\nMvaa 208,80,8\nTa 118,25,1024\nTa 148,25,8192\nMvaa 208,176,24\nv_add_u32 v13, 128, v13\n" \
"Ta 150,25,9216\nMvaa 212,180,24\ns_add_u32 m0, s32, 81920\nTa 120,26,0\nMvaa 212,84,8\nD 14,68,0,0\nMvaa 216,88,8\nTa 122,26,1024\n" \
"Ta 152,26,8192\nMvaa 216,184,24\nMvaa 220,188,24\nTa 154,26,9216\nTa 124,27,0\nMvaa 220,92,8\nMvaa 252,92,12\nD 14,68,s89,1024\n" \
"Mvaa 252,188,28\nTa 126,27,1024\nTa 156,27,8192\nMvaa 248,184,28\nTa 158,27,9216\nRa 36,16,2048\nMvaa 248,88,12\nMvaa 244,84,12\n" \
"Ra 52,17,2048\nRa 40,16,4096\nMvaa 244,180,28\nD 28,68,s90,2048\nMvaa 240,176,28\nRa 56,17,4096\nRa 44,16,6144\nMvaa 240,80,12\n" \
"Mvaa 236,76,12\nRa 60,17,6144\nMvaa 236,172,28\nMvaa 232,168,28\nD 28,68,s91,3072\nMvaa 232,72,12\nMvaa 228,68,12\nMvaa 228,164,28\n" \
"v_add_u32 v14, s72, v14\nMvaa 224,160,28\nv_add_u32 v28, s72, v28\nMvaa 224,64,12\ns_waitcnt lgkmcnt(6)\nMvaa 128,96,32\nMvaa 128,128,48\nbuffer_load_dwordx2 v[10:11], v1, s[60:63], 0 offen offset:0 sc1\n" \
"Mvaa 132,132,48\nv_subrev_u32 v9, s32, v0\nv_add_u32 v8, 0x1004200, v0\nMvaa 132,100,32\ns_add_u32 s77, s38, s32\nv_mov_b32 v12, 0xc2200000\nMvaa 136,104,32\nv_mov_b32 v13, 0xc2200000\n" \
"v_mov_b32 v14, 0xc2200000\nMvaa 136,136,48\nv_mov_b32 v15, 0xc2200000\nv_bfe_u32 v29, v0, 4, 4\nMvaa 140,140,48\nv_lshrrev_b32 v28, 8, v0\ns_lshr_b32 s96, s32, 8\nMvaa 140,108,32\n" \
"s_waitcnt lgkmcnt(0)\nv_lshlrev_b32 v6, 2, v28\nMvaa 144,112,32\nv_add_u32 v30, s96, v29\nv_sub_u32 v30, v30, v6\nMvaa 144,144,48\nv_mov_b32 v6, 0xff800000\nv_cmp_gt_i32 s[90:91], 0, v30\n" \
"Mvaa 148,148,48\nv_cmp_gt_i32 s[92:93], 1, v30\nv_cmp_gt_i32 s[74:75], 2, v30\nMvaa 148,116,32\nv_cmp_gt_i32 s[78:79], 3, v30\nv_cndmask_b32 v112, v12, v6, s[90:91]\nMvaa 152,120,32\nv_cndmask_b32 v113, v12, v6, s[92:93]\n" \
"v_cndmask_b32 v114, v12, v6, s[74:75]\nMvaa 152,152,48\nv_cndmask_b32 v115, v12, v6, s[78:79]\nv_cmp_gt_i32 s[90:91], 16, v30\nMvaa 156,156,48\nv_cmp_gt_i32 s[92:93], 17, v30\nv_cmp_gt_i32 s[74:75], 18, v30\nMvaa 156,124,32\n" \
"v_cmp_gt_i32 s[78:79], 19, v30\nv_cndmask_b32 v116, v12, v6, s[90:91]\nMvaa 188,124,36\ns_waitcnt vmcnt(13)\ns_barrier\nMvaa 188,156,52\nTa 64,20,32768\nv_cndmask_b32 v117, v12, v6, s[92:93]\n" \
"Mvaa 184,152,52\nTa 66,20,33792\nv_cndmask_b32 v118, v12, v6, s[74:75]\nMvaa 184,120,36\nTa 160,20,40960\nv_cndmask_b32 v119, v12, v6, s[78:79]\nMvaa 180,116,36\nTa 162,20,41984\n" \
"v_cmp_gt_i32 s[90:91], 32, v30\nMvaa 180,148,52\nRa 0,16,32768\nv_cmp_gt_i32 s[92:93], 33, v30\nMvaa 176,144,52\nRa 16,17,32768\nv_cmp_gt_i32 s[74:75], 34, v30\nMvaa 176,112,36\n" \
"Ta 68,21,32768\nv_cmp_gt_i32 s[78:79], 35, v30\nMvaa 172,108,36\nTa 70,21,33792\nv_cndmask_b32 v120, v12, v6, s[90:91]\nMvaa 172,140,52\nTa 164,21,40960\nv_cndmask_b32 v121, v12, v6, s[92:93]\n" \
"Mvaa 168,136,52\nTa 166,21,41984\nv_cndmask_b32 v122, v12, v6, s[74:75]\nMvaa 168,104,36\nTa 72,22,32768\nv_cndmask_b32 v123, v12, v6, s[78:79]\nMvaa 164,100,36\nTa 74,22,33792\n" \
"v_cmp_gt_i32 s[90:91], 48, v30\nMvaa 164,132,52\nTa 168,22,40960\nv_cmp_gt_i32 s[92:93], 49, v30\nMvaa 160,128,52\nTa 170,22,41984\nv_cmp_gt_i32 s[74:75], 50, v30\nMvaa 160,96,36\n" \
"Ta 76,23,32768\nv_cmp_gt_i32 s[78:79], 51, v30\nMvaa 192,96,40\nTa 78,23,33792\nv_cndmask_b32 v124, v12, v6, s[90:91]\nMvaa 192,128,56\nTa 172,23,40960\nv_cndmask_b32 v125, v12, v6, s[92:93]\n" \
"Mvaa 196,132,56\nTa 174,23,41984\nv_cndmask_b32 v126, v12, v6, s[74:75]\nMvaa 196,100,40\nTa 80,24,32768\nv_cndmask_b32 v127, v12, v6, s[78:79]\nMvaa 200,104,40\nTa 82,24,33792\n" \
"Mvaa 200,136,56\nMvaa 204,140,56\nTa 176,24,40960\nMvaa 204,108,40\nTa 178,24,41984\nMvaa 208,112,40\nTa 84,25,32768\nMvaa 208,144,56\n" \
"Ta 86,25,33792\nMvaa 212,148,56\nTa 180,25,40960\nMvaa 212,116,40\nTa 182,25,41984\nMvaa 216,120,40\nTa 88,26,32768\nMvaa 216,152,56\n" \
"Ta 90,26,33792\nMvaa 220,156,56\nTa 184,26,40960\nMvaa 220,124,40\nTa 186,26,41984\nMvaa 252,124,44\nTa 92,27,32768\nMvaa 252,156,60\n" \
"Ta 94,27,33792\nMvaa 248,152,60\nTa 188,27,40960\nMvaa 248,120,44\nTa 190,27,41984\nMvaa 244,116,44\nRa 4,16,34816\nMvaa 244,148,60\n" \
"Ra 20,17,34816\nMvaa 240,144,60\nRa 8,16,36864\nMvaa 240,112,44\nRa 24,17,36864\nMvaa 236,108,44\nRa 12,16,38912\nMvaa 236,140,60\n" \
"Ra 28,17,38912\nMvaa 232,136,60\nMvaa 232,104,44\nMvaa 228,100,44\nMvaa 228,132,60\nMvaa 224,128,60\nMvaa 224,96,44\ns_waitcnt lgkmcnt(6)\n" \
"Mvaa 128,64,0\nv_and_b32 v30, 3, v29\nv_bfe_u32 v6, v29, 1, 1\nMvaa 128,160,16\nv_lshrrev_b32 v28, 1, v28\nv_cmp_eq_u32 s[90:91], 2, v30\nMvaa 132,164,16\nv_cmp_eq_u32 s[92:93], v6, v28\n" \
"v_and_b32 v30, 1, v29\nMvaa 132,68,0\nv_mov_b32 v6, 0x3f803f80\nv_cmp_eq_u32 s[78:79], 1, v30\nMvaa 136,72,0\nv_mov_b32 v7, 0x4444\nv_mov_b32 v30, 0xeeee\nMvaa 136,168,16\n" \
"v_cndmask_b32 v108, 0, v6, s[90:91]\nv_cndmask_b32 v109, 0, v6, s[90:91]\nMvaa 140,172,16\nv_cndmask_b32 v110, 0, v6, s[90:91]\nv_cndmask_b32 v111, 0, v6, s[90:91]\nMvaa 140,76,0\ns_waitcnt lgkmcnt(0)\nv_cndmask_b32 v104, 0, v6, s[92:93]\n" \
"Mvaa 144,80,0\nv_cndmask_b32 v105, 0, v6, s[92:93]\nv_cndmask_b32 v106, 0, v6, s[92:93]\nMvaa 144,176,16\nv_cndmask_b32 v107, 0, v6, s[92:93]\nv_cndmask_b32 v7, v7, v30, s[78:79]\nMvaa 148,180,16\nv_mov_b32 v96, 0\n" \
"v_mov_b32 v97, 0\nMvaa 148,84,0\nv_mov_b32 v98, 0\nv_mov_b32 v99, 0\nMvaa 152,88,0\nv_mov_b32 v100, 0\nv_mov_b32 v101, 0\nMvaa 152,184,16\n" \
"v_mov_b32 v102, 0\nv_mov_b32 v103, 0\nMvaa 156,188,16\nv_mov_b32 v5, v0\ns_mov_b32 s73, s32\nMvaa 156,92,0\ns_add_u32 s74, s32, 0x4000\ns_add_u32 s75, s32, 0x8000\n" \
"Mvaa 188,92,4\ns_waitcnt vmcnt(0)\ns_barrier\nv_cmp_ne_u64 vcc, v[10:11], v[2:3]\ns_cbranch_vccnz zs102\nzr102:\nMvaa 188,188,20\ns_mov_b32 m0, s32\n" \
"Ta 96,20,16384\nMvaa 184,184,20\nDS 0,60,s77,0\nMvaa 184,88,4\nTa 98,20,17408\nTa 128,20,24576\nMvaa 180,84,4\nTa 130,20,25600\n" \
"s_mov_b32 s97, 0x3e0293ee\nMvaa 180,180,20\nRa 32,18,0\nMvaa 176,176,20\nDS 0,60,s77,1024\nMvaa 176,80,4\nRa 48,19,0\nTa 100,21,16384\n" \
"Mvaa 172,76,4\nTa 102,21,17408\nMvaa 172,172,20\nTa 132,21,24576\nMvaa 168,168,20\nDS 0,60,s77,2048\nMvaa 168,72,4\nTa 134,21,25600\n" \
"Ta 104,22,16384\nMvaa 164,68,4\nTa 106,22,17408\nMvaa 164,164,20\nTa 136,22,24576\nTa 138,22,25600\nMvaa 160,160,20\nDS 0,60,s77,3072\n" \
"Mvaa 160,64,4\nTa 108,23,16384\nMvaa 192,64,8\nTa 110,23,17408\nMvaa 192,160,24\ns_add_u32 m0, s32, 16384\ns_add_u32 s77, s77, 0x4000\nMvaa 196,164,24\n" \
"DS 0,60,s77,0\nMvaa 196,68,8\nTa 140,23,24576\nTa 142,23,25600\nMvaa 200,72,8\nTa 112,24,16384\nMvaa 200,168,24\nTa 114,24,17408\n" \
"Mvaa 204,172,24\nDS 0,60,s77,1024\nMvaa 204,76,8\nTa 144,24,24576\nTa 146,24,25600\nMvaa 208,80,8\nTa 116,25,16384\nMvaa 208,176,24\n" \
"Ta 118,25,17408\nTa 148,25,24576\nMvaa 212,180,24\nDS 0,60,s77,2048\nMvaa 212,84,8\nTa 150,25,25600\nMvaa 216,88,8\nTa 120,26,16384\n" \
"Mvaa 216,184,24\nTa 122,26,17408\nTa 152,26,24576\nMvaa 220,188,24\nDS 0,60,s77,3072\nMvaa 220,92,8\nTa 154,26,25600\nMvaa 252,92,12\n" \
"s_branch 99f\n.p2align 12\nzp26:\ns_setpc_b64 s[98:99]\n99:\nTa 124,27,16384\nTa 126,27,17408\nMvaa 252,188,28\n" \
"s_add_u32 m0, s32, 49152\nMvaa 248,184,28\nDS 8,60,s77,0\nMvaa 248,88,12\nTa 156,27,24576\nTa 158,27,25600\nMvaa 244,84,12\nRa 36,18,2048\n" \
"Mvaa 244,180,28\nRa 52,19,2048\nMvaa 240,176,28\nDS 8,60,s77,1024\nMvaa 240,80,12\nRa 40,18,4096\nRa 56,19,4096\nMvaa 236,76,12\n" \
"Ra 44,18,6144\nMvaa 236,172,28\nRa 60,19,6144\nMvaa 232,168,28\nDS 8,60,s77,2048\nMvaa 232,72,12\nMvaa 228,68,12\nMvaa 228,164,28\n" \
"Mvaa 224,160,28\nDS 8,60,s77,3072\nMvaa 224,64,12\ns_waitcnt lgkmcnt(6)\nMvaa 128,96,32\ns_add_u32 m0, s32, 32768\ns_add_u32 s77, s77, 0x4000\nMvaa 128,128,48\n" \
"DS 0,60,s77,0\nMvaa 132,132,48\nMvaa 132,100,32\nDS 0,60,s77,1024\nMvaa 136,104,32\nMvaa 136,136,48\nDS 0,60,s77,2048\nMvaa 140,140,48\n" \
"Mvaa 140,108,32\ns_waitcnt lgkmcnt(0)\nDS 0,60,s77,3072\nMvaa 144,112,32\ns_add_u32 m0, s32, 65536\nMvaa 144,144,48\nDS 8,60,s77,0\nMvaa 148,148,48\n" \
"Mvaa 148,116,32\nDS 8,60,s77,1024\nMvaa 152,120,32\nMvaa 152,152,48\nDS 8,60,s77,2048\nMvaa 156,156,48\nMvaa 156,124,32\nDS 8,60,s77,3072\n" \
"Mvaa 160,96,36\ns_add_u32 s77, s77, 0x4000\nMvaa 160,128,52\nMvaa 164,132,52\nMvaa 164,100,36\nMvaa 168,104,36\nMvaa 168,136,52\nv_mul_f32 v128, s97, v128\n" \
"v_mul_f32 v129, s97, v129\nv_mul_f32 v130, s97, v130\nMvaa 172,140,52\nv_mul_f32 v131, s97, v131\nv_mul_f32 v132, s97, v132\nv_mul_f32 v133, s97, v133\nMvaa 172,108,36\nv_mul_f32 v134, s97, v134\n" \
"v_mul_f32 v135, s97, v135\nC 32,128,129\nMvaa 176,112,36\nC 33,130,131\nv_mul_f32 v136, s97, v136\nv_mul_f32 v137, s97, v137\nMvaa 176,144,52\nv_mul_f32 v138, s97, v138\n" \
"v_mul_f32 v139, s97, v139\nC 34,132,133\nMvaa 180,148,52\nC 35,134,135\nv_mul_f32 v140, s97, v140\nv_mul_f32 v141, s97, v141\nMvaa 180,116,36\nv_mul_f32 v142, s97, v142\n" \
"v_mul_f32 v143, s97, v143\nC 36,136,137\nMvaa 184,120,36\nC 37,138,139\nv_mul_f32 v144, s97, v144\nv_mul_f32 v145, s97, v145\nMvaa 184,152,52\nv_mul_f32 v146, s97, v146\n" \
"v_mul_f32 v147, s97, v147\nC 38,140,141\nMvaa 188,156,52\nC 39,142,143\nv_mul_f32 v148, s97, v148\nv_mul_f32 v149, s97, v149\nMvaa 188,124,36\nv_mul_f32 v150, s97, v150\n" \
"v_mul_f32 v151, s97, v151\nC 40,144,145\nMvaa 192,96,40\nC 41,146,147\nv_mul_f32 v152, s97, v152\nv_mul_f32 v153, s97, v153\nMvaa 192,128,56\nv_mul_f32 v154, s97, v154\n" \
"v_mul_f32 v155, s97, v155\nC 42,148,149\nMvaa 196,132,56\nC 43,150,151\nv_mul_f32 v156, s97, v156\nv_mul_f32 v157, s97, v157\nMvaa 196,100,40\nv_mul_f32 v158, s97, v158\n" \
"v_mul_f32 v159, s97, v159\nC 44,152,153\nMvaa 200,104,40\nC 45,154,155\nC 46,156,157\nC 47,158,159\nMvaa 200,136,56\nv_mul_f32 v160, s97, v160\n" \
"v_mul_f32 v161, s97, v161\nv_mul_f32 v162, s97, v162\nMvaa 204,140,56\nv_mul_f32 v163, s97, v163\nv_mul_f32 v164, s97, v164\nv_mul_f32 v165, s97, v165\nMvaa 204,108,40\nv_mul_f32 v166, s97, v166\n" \
"v_mul_f32 v167, s97, v167\nC 48,160,161\nMvaa 208,112,40\ns_waitcnt vmcnt(16)\ns_barrier\nC 49,162,163\nMvaa 208,144,56\nRa 160,5,0\n" \
"v_mul_f32 v168, s97, v168\nv_mul_f32 v169, s97, v169\nMvaa 212,148,56\nv_mul_f32 v170, s97, v170\nv_mul_f32 v171, s97, v171\nC 50,164,165\nMvaa 212,116,40\nRa 164,5,1024\n" \
"C 51,166,167\nv_mul_f32 v172, s97, v172\nMvaa 216,120,40\nv_mul_f32 v173, s97, v173\nv_mul_f32 v174, s97, v174\nv_mul_f32 v175, s97, v175\nMvaa 216,152,56\nRa 168,5,2048\n" \
"C 52,168,169\nC 53,170,171\nMvaa 220,156,56\nv_mul_f32 v176, s97, v176\nv_mul_f32 v177, s97, v177\nv_mul_f32 v178, s97, v178\nMvaa 220,124,40\nRa 172,5,3072\n" \
"v_mul_f32 v179, s97, v179\nC 54,172,173\nMvaa 224,96,44\nC 55,174,175\nv_mul_f32 v180, s97, v180\nv_mul_f32 v181, s97, v181\nMvaa 224,128,60\nRa 176,5,4096\n" \
"v_mul_f32 v182, s97, v182\nv_mul_f32 v183, s97, v183\nMvaa 228,132,60\nC 56,176,177\nC 57,178,179\nv_mul_f32 v184, s97, v184\nMvaa 228,100,44\nRa 180,5,5120\n" \
"v_mul_f32 v185, s97, v185\nv_mul_f32 v186, s97, v186\nMvaa 232,104,44\nv_mul_f32 v187, s97, v187\nC 58,180,181\nC 59,182,183\nMvaa 232,136,60\nRa 184,5,6144\n" \
"v_mul_f32 v188, s97, v188\nv_mul_f32 v189, s97, v189\nMvaa 236,140,60\nv_mul_f32 v190, s97, v190\nv_mul_f32 v191, s97, v191\nC 60,184,185\nMvaa 236,108,44\nRa 188,5,7168\n" \
"C 61,186,187\nC 62,188,189\nMvaa 240,112,44\nC 63,190,191\nv_mul_f32 v192, s97, v192\nv_mul_f32 v193, s97, v193\nMvaa 240,144,60\nv_mul_f32 v194, s97, v194\n" \
"v_mul_f32 v195, s97, v195\nv_mul_f32 v196, s97, v196\nMvaa 244,148,60\nv_mul_f32 v197, s97, v197\nv_mul_f32 v198, s97, v198\nv_mul_f32 v199, s97, v199\nMvaa 244,116,44\nC 64,192,193\n" \
"C 65,194,195\nv_mul_f32 v200, s97, v200\nMvaa 248,120,44\ns_bitcmp1_b32 s33, 2\nv_mul_f32 v201, s97, v201\nv_mul_f32 v202, s97, v202\nMvaa 248,152,60\nv_mul_f32 v203, s97, v203\n" \
"C 66,196,197\nC 67,198,199\nMvaa 252,156,60\nMvaa 252,124,44\ns_waitcnt lgkmcnt(0)\ns_cbranch_scc1 zZ\nQvav 128,160,32,12\nRa 128,5,8192\n" \
"v_mul_f32 v204, s97, v204\nv_mul_f32 v205, s97, v205\nMvav 128,164,36\nv_mul_f32 v206, s97, v206\nv_mul_f32 v207, s97, v207\nC 68,200,201\nMvav 128,168,40\nRa 132,5,9216\n" \
"C 69,202,203\nv_mul_f32 v208, s97, v208\nMvav 128,172,44\nv_mul_f32 v209, s97, v209\nv_mul_f32 v210, s97, v210\nv_mul_f32 v211, s97, v211\nQvav 132,176,32,12\nRa 136,5,10240\n" \
"C 70,204,205\nC 71,206,207\nMvav 132,180,36\nv_mul_f32 v212, s97, v212\nv_mul_f32 v213, s97, v213\nv_mul_f32 v214, s97, v214\nMvav 132,184,40\nRa 140,5,11264\n" \
"v_mul_f32 v215, s97, v215\nC 72,208,209\nMvav 132,188,44\nC 73,210,211\nv_mul_f32 v216, s97, v216\nv_mul_f32 v217, s97, v217\nQvav 136,160,48,12\nRa 144,5,12288\n" \
"v_mul_f32 v218, s97, v218\nv_mul_f32 v219, s97, v219\nMvav 136,164,52\nC 74,212,213\nC 75,214,215\nv_mul_f32 v220, s97, v220\nMvav 136,168,56\nRa 148,5,13312\n" \
"v_mul_f32 v221, s97, v221\nv_mul_f32 v222, s97, v222\nMvav 136,172,60\nv_mul_f32 v223, s97, v223\nC 76,216,217\nC 77,218,219\nQvav 140,176,48,12\nRa 152,5,14336\n" \
"C 78,220,221\nC 79,222,223\nMvav 140,180,52\nv_mul_f32 v224, s97, v224\nv_mul_f32 v225, s97, v225\nv_mul_f32 v226, s97, v226\nMvav 140,184,56\nRa 156,5,15360\n" \
"v_mul_f32 v227, s97, v227\nv_mul_f32 v228, s97, v228\nMvav 140,188,60\nv_mul_f32 v229, s97, v229\nv_mul_f32 v230, s97, v230\nv_mul_f32 v231, s97, v231\nQvav 144,160,64,12\nC 80,224,225\n" \
"C 81,226,227\nv_mul_f32 v232, s97, v232\nMvav 144,164,68\nv_mul_f32 v233, s97, v233\nv_mul_f32 v234, s97, v234\nv_mul_f32 v235, s97, v235\nMvav 144,168,72\nC 82,228,229\n" \
"C 83,230,231\nv_mul_f32 v236, s97, v236\nMvav 144,172,76\nv_mul_f32 v237, s97, v237\nv_mul_f32 v238, s97, v238\nv_mul_f32 v239, s97, v239\nQvav 148,176,64,12\nC 84,232,233\n" \
"C 85,234,235\nv_mul_f32 v240, s97, v240\nMvav 148,180,68\nv_mul_f32 v241, s97, v241\nv_mul_f32 v242, s97, v242\nv_mul_f32 v243, s97, v243\nC 86,236,237\nC 87,238,239\n" \
"v_mul_f32 v244, s97, v244\nv_mul_f32 v245, s97, v245\nv_mul_f32 v246, s97, v246\nv_mul_f32 v247, s97, v247\nC 88,240,241\nC 89,242,243\nv_mul_f32 v248, s97, v248\nv_mul_f32 v249, s97, v249\n" \
"v_mul_f32 v250, s97, v250\nv_mul_f32 v251, s97, v251\nC 90,244,245\nC 91,246,247\nv_mul_f32 v252, s97, v252\nv_mul_f32 v253, s97, v253\nv_mul_f32 v254, s97, v254\nv_mul_f32 v255, s97, v255\n" \
"C 92,248,249\nC 93,250,251\nC 94,252,253\nC 95,254,255\nMvav 148,184,72\nE 240,128\nMvav 148,188,76\nE 241,129\n" \
"Qvav 152,160,80,12\nC 192,240,241\nE 242,130\nMvav 152,164,84\nE 243,131\nMvav 152,168,88\nC 193,242,243\nMvav 152,172,92\n" \
"E 244,132\nQvav 156,176,80,12\nE 245,133\nMvav 156,180,84\nC 194,244,245\nE 246,134\nMvav 156,184,88\nE 247,135\n" \
"Mvav 156,188,92\ns_waitcnt lgkmcnt(0)\nC 195,246,247\nQvav 160,128,32,12\nE 240,136\nMvav 160,132,36\nE 241,137\nMvav 160,136,40\n" \
"C 196,240,241\nE 242,138\nMvav 160,140,44\nQvav 168,128,48,12\nv_mov_b32 v6, v5\nv_add_u32 v5, s74, v9\nE 243,139\nMvav 168,132,52\n" \
"C 197,242,243\nE 244,140\nMvav 168,136,56\nE 245,141\nMvav 168,140,60\nC 198,244,245\nE 246,142\nQvav 176,128,64,12\n" \
"s_waitcnt vmcnt(8)\ns_barrier\nbuffer_store_dwordx2 v[2:3], off, s[60:63], s36 sc1\nMvav 176,132,68\nRa 160,5,0\nE 247,143\nMvav 176,136,72\ns_mov_b32 m0, s73\n" \
"C 199,246,247\nE 240,144\nMvav 176,140,76\nRa 164,5,1024\nDS 0,60,s77,0\nE 241,145\nQvav 184,128,80,12\nC 200,240,241\n" \
"Mvav 184,132,84\nRa 168,5,2048\nE 242,146\nMvav 184,136,88\nE 243,147\nMvav 184,140,92\nRa 172,5,3072\nC 201,242,243\n" \
"E 244,148\nQvav 164,144,32,12\nDS 0,60,s77,1024\nE 245,149\nMvav 164,148,36\nRa 176,5,4096\nC 202,244,245\nMvav 164,152,40\n" \
"E 246,150\nMvav 164,156,44\nRa 180,5,5120\nE 247,151\nQvav 172,144,48,12\nC 203,246,247\nE 240,152\nMvav 172,148,52\n" \
"Ra 184,5,6144\nDS 0,60,s77,2048\nMvav 172,152,56\nE 241,153\nMvav 172,156,60\nRa 188,5,7168\nC 204,240,241\nE 242,154\n" \
"Qvav 180,144,64,12\nE 243,155\nMvav 180,148,68\nC 205,242,243\nE 244,156\nMvav 180,152,72\nDS 0,60,s77,3072\nMvav 180,156,76\n" \
"E 245,157\nQvav 188,144,80,12\nC 206,244,245\nE 246,158\nMvav 188,148,84\nE 247,159\nMvav 188,152,88\nC 207,246,247\n" \
"s_mov_b32 s96, s39\nMvav 188,156,92\ns_waitcnt lgkmcnt(0)\nQvav 128,160,32,12\nE 240,160\nMvav 128,164,36\nE 241,161\nMvav 128,168,40\n" \
"C 208,240,241\nRa 128,6,49152\nMvav 128,172,44\nE 242,162\ns_add_u32 m0, s75, 0xc000\nQvav 136,160,48,12\nDS 8,60,s77,0\nMvav 136,164,52\n" \
"E 243,163\nMvav 136,168,56\nC 209,242,243\nRa 132,6,50176\nMvav 136,172,60\nE 244,164\nQvav 144,160,64,12\nDS 8,60,s77,1024\n" \
"Mvav 144,164,68\nE 245,165\nMvav 144,168,72\nC 210,244,245\nMvav 144,172,76\nE 246,166\nQvav 152,160,80,12\nRa 136,6,51200\n" \
"Ra 140,6,52224\nMvav 152,164,84\nE 247,167\nMvav 152,168,88\nC 211,246,247\nMvav 152,172,92\nE 240,168\nQvav 132,176,32,12\n" \
"DS 8,60,s77,2048\nMvav 132,180,36\nE 241,169\nMvav 132,184,40\nC 212,240,241\nRa 144,6,53248\nMvav 132,188,44\nE 242,170\n" \
"Qvav 140,176,48,12\nRa 148,6,54272\nMvav 140,180,52\nE 243,171\nMvav 140,184,56\nC 213,242,243\nRa 152,6,55296\nMvav 140,188,60\n" \
"E 244,172\nQvav 148,176,64,12\nDS 8,60,s77,3072\nMvav 148,180,68\nE 245,173\nMvav 148,184,72\nC 214,244,245\ns_mov_b32 s76, s73\n" \
"Mvav 148,188,76\nE 246,174\nQvav 156,176,80,12\nRa 156,6,56320\ns_mov_b32 s73, s74\nMvav 156,180,84\nE 247,175\nMvav 156,184,88\n" \
"C 215,246,247\ns_mov_b32 s74, s75\nMvav 156,188,92\nE 240,176\nv_smfmac_f32_16x16x64_bf16 v[96:99], v[104:107], v[192:199], v7\ns_mov_b32 s75, s76\nv_smfmac_f32_16x16x64_bf16 v[100:103], v[104:107], v[200:207], v7\ns_waitcnt lgkmcnt(0)\n" \
"Zaav 96,128,204\nRa 160,5,8192\ns_add_u32 s77, s77, 0x4000\nZaav 100,132,204\nE 241,177\nZaav 68,132,200\nC 216,240,241\nRa 164,5,9216\n" \
"Zaav 64,128,200\nE 242,178\nZaav 32,128,196\nZaav 36,132,196\nE 243,179\nZaav 4,132,192\nC 217,242,243\nRa 168,5,10240\n" \
"Zaav 0,128,192\nE 244,180\nZaav 8,136,192\nRa 172,5,11264\nZaav 12,140,192\nE 245,181\nZaav 44,140,196\nC 218,244,245\n" \
"Zaav 40,136,196\nE 246,182\nZaav 72,136,200\nRa 176,5,12288\nZaav 76,140,200\nE 247,183\nZaav 108,140,204\nC 219,246,247\n" \
"Zaav 104,136,204\nE 240,184\nZaav 112,144,204\nRa 180,5,13312\nZaav 116,148,204\nE 241,185\nZaav 84,148,200\nC 220,240,241\n" \
"Zaav 80,144,200\nE 242,186\nZaav 48,144,196\nRa 184,5,14336\nZaav 52,148,196\nE 243,187\nZaav 20,148,192\nC 221,242,243\n" \
"Ra 188,5,15360\nZaav 16,144,192\nE 244,188\nZaav 24,152,192\nZaav 28,156,192\nE 245,189\nZaav 60,156,196\nC 222,244,245\n" \
"Zaav 56,152,196\nE 246,190\nZaav 88,152,200\nZaav 92,156,200\nE 247,191\nZaav 124,156,204\nC 223,246,247\ns_waitcnt lgkmcnt(0)\n" \
"Zaav 120,152,204\ns_branch zE\nzZ:\nQvav 128,160,32,112\nRa 128,5,8192\nv_mul_f32 v204, s97, v204\nv_mul_f32 v205, s97, v205\nMvav 128,164,36\n" \
"v_mul_f32 v206, s97, v206\nv_mul_f32 v207, s97, v207\nC 68,200,201\nMvav 128,168,40\nRa 132,5,9216\nC 69,202,203\nv_mul_f32 v208, s97, v208\nMvav 128,172,44\n" \
"v_mul_f32 v209, s97, v209\nv_mul_f32 v210, s97, v210\nv_mul_f32 v211, s97, v211\nQvav 132,176,32,116\nRa 136,5,10240\nC 70,204,205\nC 71,206,207\nMvav 132,180,36\n" \
"v_mul_f32 v212, s97, v212\nv_mul_f32 v213, s97, v213\nv_mul_f32 v214, s97, v214\nMvav 132,184,40\nRa 140,5,11264\nv_mul_f32 v215, s97, v215\ns_branch 99f\n.p2align 12\n" \
"zp27:\ns_setpc_b64 s[98:99]\n99:\nC 72,208,209\nMvav 132,188,44\nC 73,210,211\nv_mul_f32 v216, s97, v216\nv_mul_f32 v217, s97, v217\n" \
"Qvav 136,160,48,12\nRa 144,5,12288\nv_mul_f32 v218, s97, v218\nv_mul_f32 v219, s97, v219\nMvav 136,164,52\nC 74,212,213\nC 75,214,215\nv_mul_f32 v220, s97, v220\n" \
"Mvav 136,168,56\nRa 148,5,13312\nv_mul_f32 v221, s97, v221\nv_mul_f32 v222, s97, v222\nMvav 136,172,60\nv_mul_f32 v223, s97, v223\nC 76,216,217\nC 77,218,219\n" \
"Qvav 140,176,48,12\nRa 152,5,14336\nC 78,220,221\nC 79,222,223\nMvav 140,180,52\nv_mul_f32 v224, s97, v224\nv_mul_f32 v225, s97, v225\nv_mul_f32 v226, s97, v226\n" \
"Mvav 140,184,56\nRa 156,5,15360\nv_mul_f32 v227, s97, v227\nv_mul_f32 v228, s97, v228\nMvav 140,188,60\nv_mul_f32 v229, s97, v229\nv_mul_f32 v230, s97, v230\nv_mul_f32 v231, s97, v231\n" \
"Qvav 144,160,64,12\nC 80,224,225\nC 81,226,227\nv_mul_f32 v232, s97, v232\nMvav 144,164,68\nv_mul_f32 v233, s97, v233\nv_mul_f32 v234, s97, v234\nv_mul_f32 v235, s97, v235\n" \
"Mvav 144,168,72\nC 82,228,229\nC 83,230,231\nv_mul_f32 v236, s97, v236\nMvav 144,172,76\nv_mul_f32 v237, s97, v237\nv_mul_f32 v238, s97, v238\nv_mul_f32 v239, s97, v239\n" \
"Qvav 148,176,64,12\nC 84,232,233\nC 85,234,235\nv_mul_f32 v240, s97, v240\nMvav 148,180,68\nv_mul_f32 v241, s97, v241\nv_mul_f32 v242, s97, v242\nv_mul_f32 v243, s97, v243\n" \
"C 86,236,237\nC 87,238,239\nv_mul_f32 v244, s97, v244\nv_mul_f32 v245, s97, v245\nv_mul_f32 v246, s97, v246\nv_mul_f32 v247, s97, v247\nC 88,240,241\nC 89,242,243\n" \
"v_mul_f32 v248, s97, v248\nv_mul_f32 v249, s97, v249\nv_mul_f32 v250, s97, v250\nv_mul_f32 v251, s97, v251\nC 90,244,245\nC 91,246,247\nv_mul_f32 v252, s97, v252\nv_mul_f32 v253, s97, v253\n" \
"v_mul_f32 v254, s97, v254\nv_mul_f32 v255, s97, v255\nC 92,248,249\nC 93,250,251\nC 94,252,253\nC 95,254,255\nMvav 148,184,72\nE 240,128\n" \
"Mvav 148,188,76\nE 241,129\nQvav 152,160,80,12\nC 192,240,241\nE 242,130\nMvav 152,164,84\nE 243,131\nMvav 152,168,88\n" \
"C 193,242,243\nMvav 152,172,92\nE 244,132\nQvav 156,176,80,12\nE 245,133\nMvav 156,180,84\nC 194,244,245\nE 246,134\n" \
"Mvav 156,184,88\nE 247,135\nMvav 156,188,92\ns_waitcnt lgkmcnt(0)\nC 195,246,247\nQvav 160,128,32,120\nE 240,136\nMvav 160,132,36\n" \
"E 241,137\nMvav 160,136,40\nC 196,240,241\nE 242,138\nMvav 160,140,44\nQvav 168,128,48,12\nv_mov_b32 v6, v5\nv_add_u32 v5, s74, v9\n" \
"E 243,139\nMvav 168,132,52\nC 197,242,243\nE 244,140\nMvav 168,136,56\nE 245,141\nMvav 168,140,60\nC 198,244,245\n" \
"E 246,142\nQvav 176,128,64,12\ns_waitcnt vmcnt(8)\ns_barrier\nbuffer_store_dwordx2 v[2:3], off, s[60:63], s36 sc1\nMvav 176,132,68\nRa 160,5,0\nE 247,143\n" \
"Mvav 176,136,72\ns_mov_b32 m0, s73\nC 199,246,247\nE 240,144\nMvav 176,140,76\nRa 164,5,1024\nDS 0,60,s77,0\nE 241,145\n" \
"Qvav 184,128,80,12\nC 200,240,241\nMvav 184,132,84\nRa 168,5,2048\nE 242,146\nMvav 184,136,88\nE 243,147\nMvav 184,140,92\n" \
"Ra 172,5,3072\nC 201,242,243\nE 244,148\nQvav 164,144,32,124\nDS 0,60,s77,1024\nE 245,149\nMvav 164,148,36\nRa 176,5,4096\n" \
"C 202,244,245\nMvav 164,152,40\nE 246,150\nMvav 164,156,44\nRa 180,5,5120\nE 247,151\nQvav 172,144,48,12\nC 203,246,247\n" \
"E 240,152\nMvav 172,148,52\nRa 184,5,6144\nDS 0,60,s77,2048\nMvav 172,152,56\nE 241,153\nMvav 172,156,60\nRa 188,5,7168\n" \
"C 204,240,241\nE 242,154\nQvav 180,144,64,12\nE 243,155\nMvav 180,148,68\nC 205,242,243\nE 244,156\nMvav 180,152,72\n" \
"DS 0,60,s77,3072\nMvav 180,156,76\nE 245,157\nQvav 188,144,80,12\nC 206,244,245\nE 246,158\nMvav 188,148,84\nE 247,159\n" \
"Mvav 188,152,88\nC 207,246,247\ns_mov_b32 s96, s39\nMvav 188,156,92\ns_waitcnt lgkmcnt(0)\nQvav 136,160,48,112\nE 240,160\nRa 128,6,49152\n" \
"Mvav 136,164,52\nE 241,161\nMvav 136,168,56\nC 208,240,241\nRa 132,6,50176\nMvav 136,172,60\nE 242,162\ns_add_u32 m0, s75, 0xc000\n" \
"Qvav 144,160,64,12\nDS 8,60,s77,0\nMvav 144,164,68\nE 243,163\nRa 136,6,51200\nMvav 144,168,72\nC 209,242,243\nE 244,164\n" \
"Mvav 144,172,76\nDS 8,60,s77,1024\nQvav 152,160,80,12\nE 245,165\nMvav 152,164,84\nC 210,244,245\nRa 140,6,52224\nMvav 152,168,88\n" \
"E 246,166\nRa 144,6,53248\nMvav 152,172,92\nDS 8,60,s77,2048\nQvav 140,176,48,116\nE 247,167\nMvav 140,180,52\nC 211,246,247\n" \
"E 240,168\nMvav 140,184,56\nDS 8,60,s77,3072\nMvav 140,188,60\nE 241,169\nRa 148,6,54272\nQvav 148,176,64,12\nC 212,240,241\n" \
"Ra 152,6,55296\nMvav 148,180,68\nE 242,170\nMvav 148,184,72\nE 243,171\nMvav 148,188,76\nC 213,242,243\nRa 156,6,56320\n" \
"Qvav 156,176,80,12\nE 244,172\nMvav 156,180,84\ns_mov_b32 s76, s73\ns_mov_b32 s73, s74\nMvav 156,184,88\nE 245,173\nMvav 156,188,92\n" \
"C 214,244,245\ns_mov_b32 s74, s75\nv_smfmac_f32_16x16x64_bf16 v[96:99], v[104:107], v[192:199], v7\ns_mov_b32 s75, s76\nv_smfmac_f32_16x16x64_bf16 v[100:103], v[104:107], v[200:207], v7\ns_waitcnt lgkmcnt(0)\nZaav 96,128,204\nE 246,174\n" \
"Zaav 100,132,204\nE 247,175\nZaav 68,132,200\nC 215,246,247\nRa 160,5,8192\nZaav 64,128,200\nE 240,176\nZaav 32,128,196\n" \
"Ra 164,5,9216\ns_add_u32 s77, s77, 0x4000\nZaav 36,132,196\nE 241,177\nZaav 4,132,192\nC 216,240,241\nE 242,178\nZaav 0,128,192\n" \
"Ra 168,5,10240\nZaav 8,136,192\nE 243,179\nZaav 12,140,192\nC 217,242,243\nRa 172,5,11264\nZaav 44,140,196\nE 244,180\n" \
"Zaav 40,136,196\nRa 176,5,12288\nZaav 72,136,200\nE 245,181\nZaav 76,140,200\nC 218,244,245\nE 246,182\nZaav 108,140,204\n" \
"Ra 180,5,13312\nZaav 104,136,204\nE 247,183\nZaav 112,144,204\nC 219,246,247\nZaav 116,148,204\nE 240,184\nZaav 84,148,200\n" \
"E 241,185\nZaav 80,144,200\nC 220,240,241\nRa 184,5,14336\nZaav 48,144,196\nE 242,186\nZaav 52,148,196\nRa 188,5,15360\n" \
"Zaav 20,148,192\nE 243,187\nZaav 16,144,192\nC 221,242,243\nZaav 24,152,192\nE 244,188\nZaav 28,156,192\nE 245,189\n" \
"Zaav 60,156,196\nC 222,244,245\nZaav 56,152,196\nE 246,190\nZaav 88,152,200\nZaav 92,156,200\nE 247,191\nZaav 124,156,204\n" \
"C 223,246,247\ns_waitcnt lgkmcnt(0)\nZaav 120,152,204\ns_branch zD4\ns_branch 99f\n.p2align 12\nzp28:\ns_setpc_b64 s[98:99]\n" \
"99:\nzDa:\nQvav 144,160,64,112\nE 240,168\nRa 128,6,49152\nMvav 144,164,68\nE 241,169\nMvav 144,168,72\n" \
"C 212,240,241\nRa 132,6,50176\nRa 136,6,51200\nMvav 144,172,76\nE 242,170\ns_add_u32 m0, s75, 0xc000\nQvav 152,160,80,12\nDS 8,60,s77,0\n" \
"Mvav 152,164,84\nE 243,171\nRa 140,6,52224\nMvav 152,168,88\nC 213,242,243\nE 244,172\nMvav 152,172,92\nDS 8,60,s77,1024\n" \
"Ra 144,6,53248\nQvav 148,176,64,116\nE 245,173\nMvav 148,180,68\nC 214,244,245\nRa 148,6,54272\nMvav 148,184,72\nE 246,174\n" \
"Ra 152,6,55296\nMvav 148,188,76\nE 247,175\nQvav 156,176,80,12\nC 215,246,247\nRa 156,6,56320\nMvav 156,180,84\nE 240,176\n" \
"Mvav 156,184,88\nMvav 156,188,92\nE 241,177\nMvvv 96,108,196\nC 216,240,241\nE 242,178\nv_smfmac_f32_16x16x64_bf16 v[100:103], v[104:107], v[200:207], v7\ns_waitcnt lgkmcnt(0)\n" \
"Maav 96,128,204\nDS 8,60,s77,2048\nMaav 100,132,204\nE 243,179\nMaav 68,132,200\nC 217,242,243\nRa 160,5,8192\nMaav 64,128,200\n" \
"E 244,180\nMaav 32,128,196\nE 245,181\nMaav 36,132,196\nC 218,244,245\nRa 164,5,9216\nMaav 40,136,196\nE 246,182\n" \
"Maav 44,140,196\nDS 8,60,s77,3072\nMaav 76,140,200\nE 247,183\nMaav 72,136,200\nC 219,246,247\nE 240,184\nMaav 104,136,204\n" \
"Ra 168,5,10240\nRa 172,5,11264\nMaav 108,140,204\nE 241,185\nMaav 112,144,204\nC 220,240,241\nRa 176,5,12288\nMaav 116,148,204\n" \
"E 242,186\nMaav 84,148,200\nE 243,187\nMaav 80,144,200\nC 221,242,243\nRa 180,5,13312\nMaav 48,144,196\nE 244,188\n" \
"Maav 52,148,196\nRa 184,5,14336\nRa 188,5,15360\nMaav 56,152,196\nE 245,189\nMaav 60,156,196\nC 222,244,245\nE 246,190\n" \
"Maav 92,156,200\ns_mov_b32 s76, s73\ns_mov_b32 s73, s74\nMaav 88,152,200\nE 247,191\nMaav 120,152,204\nC 223,246,247\ns_waitcnt lgkmcnt(0)\n" \
"Maav 124,156,204\ns_mov_b32 s74, s75\ns_mov_b32 s75, s76\ns_add_u32 s77, s77, 0x4000\nQvav 176,160,64,120\nE 240,144\nMvav 176,164,68\nRa 128,6,57344\n" \
"Ra 132,6,58368\nMvav 176,168,72\nE 241,145\nMvav 176,172,76\nC 200,240,241\nQvav 184,160,80,12\nRa 136,6,59392\nRa 140,6,60416\n" \
"Mvav 184,164,84\nE 242,146\nMvav 184,168,88\nRa 144,6,61440\nMvav 184,172,92\nE 243,147\nQvav 180,176,64,124\nC 201,242,243\n" \
"Ra 148,6,62464\nMvav 180,180,68\nRa 152,6,63488\nMvav 180,184,72\nE 244,148\nMvav 180,188,76\nRa 156,6,64512\nQvav 188,176,80,12\n" \
"E 245,149\nMvav 188,180,84\nC 202,244,245\nv_mov_b32 v6, v5\nMvav 188,184,88\nv_add_u32 v5, s74, v9\nMvav 188,188,92\nE 246,150\n" \
"Mvvv 96,108,212\nv_smfmac_f32_16x16x64_bf16 v[100:103], v[104:107], v[216:223], v7\ns_waitcnt vmcnt(8) lgkmcnt(0)\ns_barrier\nMaav 96,128,220\nE 247,151\nMaav 100,132,220\nC 203,246,247\n" \
"Ra 160,5,0\nMaav 68,132,216\nRa 164,5,1024\nMaav 64,128,216\nE 240,152\nMaav 32,128,212\nRa 168,5,2048\nMaav 36,132,212\n" \
"E 241,153\nMaav 40,136,212\nC 204,240,241\nMaav 44,140,212\nRa 172,5,3072\nMaav 76,140,216\nE 242,154\nMaav 72,136,216\n" \
"Ra 176,5,4096\nMaav 104,136,220\nE 243,155\nMaav 108,140,220\nC 205,242,243\nMaav 112,144,220\nRa 180,5,5120\nMaav 116,148,220\n" \
"E 244,156\nMaav 84,148,216\nRa 184,5,6144\nMaav 80,144,216\nE 245,157\nMaav 48,144,212\nC 206,244,245\nRa 188,5,7168\n" \
"Maav 52,148,212\nMaav 56,152,212\nE 246,158\nMaav 60,156,212\nMaav 92,156,216\nE 247,159\nMaav 88,152,216\nC 207,246,247\n" \
"Maav 120,152,220\ns_waitcnt lgkmcnt(0)\nMaav 124,156,220\ns_branch zE3\nzL:\ns_waitcnt vmcnt(5)\ns_mov_b32 s96, s40\ns_mov_b32 s40, 0\n" \
"v_cmp_ne_u64 vcc, v[10:11], v[2:3]\ns_cbranch_vccz zO\ns_mov_b32 s94, 0x400000\nzt99:\ns_sleep 2\nbuffer_load_dwordx2 v[10:11], v1, s[60:63], 0 offen offset:64 sc1\ns_waitcnt vmcnt(0)\nv_cmp_ne_u64 vcc, v[10:11], v[2:3]\n" \
"s_cbranch_vccz zO\ns_sub_u32 s94, s94, 1\ns_cbranch_scc0 zt99\ns_branch zO\nzs101:\ns_mov_b32 s94, 0x400000\nzt101:\ns_sleep 2\n" \
"buffer_load_dwordx2 v[10:11], v1, s[60:63], 0 offen offset:0 sc1\ns_waitcnt vmcnt(0)\nv_cmp_ne_u64 vcc, v[10:11], v[2:3]\ns_cbranch_vccz zr101\ns_sub_u32 s94, s94, 1\ns_cbranch_scc0 zt101\ns_branch zr101\nzs102:\n" \
"s_mov_b32 s94, 0x400000\nzt102:\ns_sleep 2\nbuffer_load_dwordx2 v[10:11], v1, s[60:63], 0 offen offset:0 sc1\ns_waitcnt vmcnt(0)\nv_cmp_ne_u64 vcc, v[10:11], v[2:3]\ns_cbranch_vccz zr102\ns_sub_u32 s94, s94, 1\n" \
"s_cbranch_scc0 zt102\ns_branch zr102\nzs103:\ns_mov_b32 s94, 0x400000\nzt103:\ns_sleep 2\nbuffer_load_dwordx2 v[10:11], off, s[60:63], s49 sc1\ns_waitcnt vmcnt(0)\n" \
"v_cmp_ne_u64 vcc, v[10:11], v[2:3]\ns_cbranch_vccz zr103\ns_sub_u32 s94, s94, 1\ns_cbranch_scc0 zt103\ns_branch zr103\nzfb:\nv_cmp_lt_u32 vcc, 0x3c000000, v240\ns_cbranch_vccz zf0e\n" \
"s_add_u32 s96, s42, 2\ns_mov_b32 s77, s38\nv_mov_b32 v136, 0xff800000\nv_mov_b32 v138, 0\nv_add_u32 v140, 0x4000, v8\nv_accvgpr_write_b32 a0, 0\nv_accvgpr_write_b32 a1, 0\nv_accvgpr_write_b32 a2, 0\n" \
"v_accvgpr_write_b32 a3, 0\nv_accvgpr_write_b32 a4, 0\nv_accvgpr_write_b32 a5, 0\nv_accvgpr_write_b32 a6, 0\nv_accvgpr_write_b32 a7, 0\nv_accvgpr_write_b32 a8, 0\nv_accvgpr_write_b32 a9, 0\nv_accvgpr_write_b32 a10, 0\n" \
"v_accvgpr_write_b32 a11, 0\nv_accvgpr_write_b32 a12, 0\nv_accvgpr_write_b32 a13, 0\nv_accvgpr_write_b32 a14, 0\nv_accvgpr_write_b32 a15, 0\nv_accvgpr_write_b32 a16, 0\nv_accvgpr_write_b32 a17, 0\nv_accvgpr_write_b32 a18, 0\n" \
"v_accvgpr_write_b32 a19, 0\nv_accvgpr_write_b32 a20, 0\nv_accvgpr_write_b32 a21, 0\nv_accvgpr_write_b32 a22, 0\nv_accvgpr_write_b32 a23, 0\nv_accvgpr_write_b32 a24, 0\nv_accvgpr_write_b32 a25, 0\nv_accvgpr_write_b32 a26, 0\n" \
"v_accvgpr_write_b32 a27, 0\nv_accvgpr_write_b32 a28, 0\nv_accvgpr_write_b32 a29, 0\nv_accvgpr_write_b32 a30, 0\nv_accvgpr_write_b32 a31, 0\nzf0l:\ns_add_u32 s76, s77, 4096\nbuffer_load_dwordx4 a[160:163], v0, s[60:63], s77 offen offset:0 sc1\n" \
"buffer_load_dwordx4 a[164:167], v0, s[60:63], s77 offen offset:1024 sc1\nbuffer_load_dwordx4 a[168:171], v0, s[60:63], s77 offen offset:2048 sc1\nbuffer_load_dwordx4 a[172:175], v0, s[60:63], s77 offen offset:3072 sc1\nbuffer_load_dwordx4 a[176:179], v0, s[60:63], s76 offen offset:0 sc1\nbuffer_load_dwordx4 a[180:183], v0, s[60:63], s76 offen offset:1024 sc1\nbuffer_load_dwordx4 a[184:187], v0, s[60:63], s76 offen offset:2048 sc1\nbuffer_load_dwordx4 a[188:191], v0, s[60:63], s76 offen offset:3072 sc1\nbuffer_load_dwordx4 a[128:131], v140, s[60:63], s77 offen offset:0 sc1\n" \
"buffer_load_dwordx4 a[132:135], v140, s[60:63], s77 offen offset:1024 sc1\nbuffer_load_dwordx4 a[136:139], v140, s[60:63], s77 offen offset:2048 sc1\nbuffer_load_dwordx4 a[140:143], v140, s[60:63], s77 offen offset:3072 sc1\nbuffer_load_dwordx4 a[144:147], v140, s[60:63], s76 offen offset:0 sc1\nbuffer_load_dwordx4 a[148:151], v140, s[60:63], s76 offen offset:1024 sc1\nbuffer_load_dwordx4 a[152:155], v140, s[60:63], s76 offen offset:2048 sc1\nbuffer_load_dwordx4 a[156:159], v140, s[60:63], s76 offen offset:3072 sc1\nv_mov_b32 v144, v12\n" \
"v_mov_b32 v145, v12\nv_mov_b32 v146, v12\nv_mov_b32 v147, v12\nv_mov_b32 v148, v12\nv_mov_b32 v149, v12\nv_mov_b32 v150, v12\nv_mov_b32 v151, v12\ns_cmp_gt_u32 s96, 2\n" \
"s_cbranch_scc1 zf0m\nv_mov_b32 v144, v112\nv_mov_b32 v145, v113\nv_mov_b32 v146, v114\nv_mov_b32 v147, v115\nv_mov_b32 v148, v116\nv_mov_b32 v149, v117\nv_mov_b32 v150, v118\n" \
"v_mov_b32 v151, v119\ns_cmp_eq_u32 s96, 2\ns_cbranch_scc1 zf0m\nv_mov_b32 v144, v120\nv_mov_b32 v145, v121\nv_mov_b32 v146, v122\nv_mov_b32 v147, v123\nv_mov_b32 v148, v124\n" \
"v_mov_b32 v149, v125\nv_mov_b32 v150, v126\nv_mov_b32 v151, v127\nzf0m:\ns_waitcnt vmcnt(0)\nQvav 128,160,32,144\nMvav 128,164,36\nMvav 128,168,40\n" \
"Mvav 128,172,44\nQvav 132,176,32,148\nMvav 132,180,36\nMvav 132,184,40\nMvav 132,188,44\ns_nop 7\nv_max3_f32 v141, v128, v129, v130\nv_max3_f32 v142, v131, v132, v133\n" \
"v_max3_f32 v141, v141, v134, v135\nv_max3_f32 v141, v141, v142, v136\nv_mov_b32 v142, v141\ns_nop 1\nv_permlane16_swap_b32 v141, v142\ns_nop 0\nv_max_f32 v141, v141, v142\nv_mov_b32 v142, v141\n" \
"s_nop 1\nv_permlane32_swap_b32 v141, v142\ns_nop 0\nv_max_f32 v141, v141, v142\nv_sub_f32 v137, v136, v141\nE 137,137\nv_mov_b32 v136, v141\nv_sub_f32 v128, v128, v141\n" \
"v_sub_f32 v129, v129, v141\nv_sub_f32 v130, v130, v141\nv_sub_f32 v131, v131, v141\nv_sub_f32 v132, v132, v141\nv_sub_f32 v133, v133, v141\nv_sub_f32 v134, v134, v141\nv_sub_f32 v135, v135, v141\nE 128,128\n" \
"E 129,129\nE 130,130\nE 131,131\nE 132,132\nE 133,133\nE 134,134\nE 135,135\nv_mul_f32 v138, v138, v137\n" \
"C 192,128,129\nC 193,130,131\nC 194,132,133\nC 195,134,135\nv_add_f32 v138, v138, v128\nv_add_f32 v138, v138, v129\nv_add_f32 v138, v138, v130\nv_add_f32 v138, v138, v131\n" \
"v_add_f32 v138, v138, v132\nv_add_f32 v138, v138, v133\nv_add_f32 v138, v138, v134\nv_add_f32 v138, v138, v135\nAR 141,0\nv_mul_f32 v141, v141, v137\nAW 0,141\nAR 141,1\n" \
"v_mul_f32 v141, v141, v137\nAW 1,141\nAR 141,2\nv_mul_f32 v141, v141, v137\nAW 2,141\nAR 141,3\nv_mul_f32 v141, v141, v137\nAW 3,141\n" \
"AR 141,4\nv_mul_f32 v141, v141, v137\nAW 4,141\nAR 141,5\nv_mul_f32 v141, v141, v137\nAW 5,141\nAR 141,6\nv_mul_f32 v141, v141, v137\n" \
"AW 6,141\nAR 141,7\nv_mul_f32 v141, v141, v137\nAW 7,141\nAR 141,8\nv_mul_f32 v141, v141, v137\nAW 8,141\nAR 141,9\n" \
"v_mul_f32 v141, v141, v137\nAW 9,141\nAR 141,10\nv_mul_f32 v141, v141, v137\nAW 10,141\nAR 141,11\nv_mul_f32 v141, v141, v137\nAW 11,141\n" \
"AR 141,12\nv_mul_f32 v141, v141, v137\nAW 12,141\nAR 141,13\nv_mul_f32 v141, v141, v137\nAW 13,141\nAR 141,14\nv_mul_f32 v141, v141, v137\n" \
"AW 14,141\nAR 141,15\nv_mul_f32 v141, v141, v137\nAW 15,141\nAR 141,16\nv_mul_f32 v141, v141, v137\nAW 16,141\nAR 141,17\n" \
"v_mul_f32 v141, v141, v137\nAW 17,141\nAR 141,18\nv_mul_f32 v141, v141, v137\nAW 18,141\nAR 141,19\nv_mul_f32 v141, v141, v137\nAW 19,141\n" \
"AR 141,20\nv_mul_f32 v141, v141, v137\nAW 20,141\nAR 141,21\nv_mul_f32 v141, v141, v137\nAW 21,141\nAR 141,22\nv_mul_f32 v141, v141, v137\n" \
"AW 22,141\nAR 141,23\nv_mul_f32 v141, v141, v137\nAW 23,141\nAR 141,24\nv_mul_f32 v141, v141, v137\nAW 24,141\nAR 141,25\n" \
"v_mul_f32 v141, v141, v137\nAW 25,141\nAR 141,26\nv_mul_f32 v141, v141, v137\nAW 26,141\nAR 141,27\nv_mul_f32 v141, v141, v137\nAW 27,141\n" \
"AR 141,28\nv_mul_f32 v141, v141, v137\nAW 28,141\nAR 141,29\nv_mul_f32 v141, v141, v137\nAW 29,141\nAR 141,30\nv_mul_f32 v141, v141, v137\n" \
"AW 30,141\nAR 141,31\nv_mul_f32 v141, v141, v137\nAW 31,141\ns_nop 1\nMaav 0,128,192\nMaav 4,132,192\nMaav 8,136,192\n" \
"Maav 12,140,192\nMaav 16,144,192\nMaav 20,148,192\nMaav 24,152,192\nMaav 28,156,192\ns_add_u32 s77, s77, 8192\ns_sub_u32 s96, s96, 1\ns_cmp_lg_u32 s96, 0\n" \
"s_nop 7\ns_cbranch_scc1 zf0l\nv_mov_b32 v141, v138\ns_nop 1\nv_permlane16_swap_b32 v138, v141\ns_nop 0\nv_add_f32 v138, v138, v141\nv_mov_b32 v141, v138\n" \
"s_nop 1\nv_permlane32_swap_b32 v138, v141\ns_nop 0\nv_add_f32 v138, v138, v141\nv_mov_b32 v104, v138\nzf0e:\nv_cmp_lt_u32 vcc, 0x3c000000, v241\ns_cbranch_vccz zf1e\n" \
"s_add_u32 s96, s42, 4\ns_mov_b32 s77, s38\nv_mov_b32 v136, 0xff800000\nv_mov_b32 v138, 0\nv_add_u32 v140, 0x4000, v8\nv_accvgpr_write_b32 a32, 0\nv_accvgpr_write_b32 a33, 0\nv_accvgpr_write_b32 a34, 0\n" \
"v_accvgpr_write_b32 a35, 0\nv_accvgpr_write_b32 a36, 0\nv_accvgpr_write_b32 a37, 0\nv_accvgpr_write_b32 a38, 0\nv_accvgpr_write_b32 a39, 0\nv_accvgpr_write_b32 a40, 0\nv_accvgpr_write_b32 a41, 0\nv_accvgpr_write_b32 a42, 0\n" \
"v_accvgpr_write_b32 a43, 0\nv_accvgpr_write_b32 a44, 0\nv_accvgpr_write_b32 a45, 0\nv_accvgpr_write_b32 a46, 0\nv_accvgpr_write_b32 a47, 0\nv_accvgpr_write_b32 a48, 0\nv_accvgpr_write_b32 a49, 0\nv_accvgpr_write_b32 a50, 0\n" \
"v_accvgpr_write_b32 a51, 0\nv_accvgpr_write_b32 a52, 0\nv_accvgpr_write_b32 a53, 0\nv_accvgpr_write_b32 a54, 0\nv_accvgpr_write_b32 a55, 0\nv_accvgpr_write_b32 a56, 0\nv_accvgpr_write_b32 a57, 0\nv_accvgpr_write_b32 a58, 0\n" \
"v_accvgpr_write_b32 a59, 0\nv_accvgpr_write_b32 a60, 0\nv_accvgpr_write_b32 a61, 0\nv_accvgpr_write_b32 a62, 0\nv_accvgpr_write_b32 a63, 0\nzf1l:\ns_add_u32 s76, s77, 4096\nbuffer_load_dwordx4 a[160:163], v0, s[60:63], s77 offen offset:0 sc1\n" \
"buffer_load_dwordx4 a[164:167], v0, s[60:63], s77 offen offset:1024 sc1\nbuffer_load_dwordx4 a[168:171], v0, s[60:63], s77 offen offset:2048 sc1\nbuffer_load_dwordx4 a[172:175], v0, s[60:63], s77 offen offset:3072 sc1\nbuffer_load_dwordx4 a[176:179], v0, s[60:63], s76 offen offset:0 sc1\nbuffer_load_dwordx4 a[180:183], v0, s[60:63], s76 offen offset:1024 sc1\nbuffer_load_dwordx4 a[184:187], v0, s[60:63], s76 offen offset:2048 sc1\nbuffer_load_dwordx4 a[188:191], v0, s[60:63], s76 offen offset:3072 sc1\nbuffer_load_dwordx4 a[128:131], v140, s[60:63], s77 offen offset:0 sc1\n" \
"buffer_load_dwordx4 a[132:135], v140, s[60:63], s77 offen offset:1024 sc1\nbuffer_load_dwordx4 a[136:139], v140, s[60:63], s77 offen offset:2048 sc1\nbuffer_load_dwordx4 a[140:143], v140, s[60:63], s77 offen offset:3072 sc1\nbuffer_load_dwordx4 a[144:147], v140, s[60:63], s76 offen offset:0 sc1\nbuffer_load_dwordx4 a[148:151], v140, s[60:63], s76 offen offset:1024 sc1\nbuffer_load_dwordx4 a[152:155], v140, s[60:63], s76 offen offset:2048 sc1\nbuffer_load_dwordx4 a[156:159], v140, s[60:63], s76 offen offset:3072 sc1\nv_mov_b32 v144, v12\n" \
"v_mov_b32 v145, v12\nv_mov_b32 v146, v12\nv_mov_b32 v147, v12\nv_mov_b32 v148, v12\nv_mov_b32 v149, v12\nv_mov_b32 v150, v12\nv_mov_b32 v151, v12\ns_cmp_gt_u32 s96, 2\n" \
"s_cbranch_scc1 zf1m\nv_mov_b32 v144, v112\nv_mov_b32 v145, v113\nv_mov_b32 v146, v114\nv_mov_b32 v147, v115\nv_mov_b32 v148, v116\nv_mov_b32 v149, v117\nv_mov_b32 v150, v118\n" \
"v_mov_b32 v151, v119\ns_cmp_eq_u32 s96, 2\ns_cbranch_scc1 zf1m\nv_mov_b32 v144, v120\nv_mov_b32 v145, v121\nv_mov_b32 v146, v122\nv_mov_b32 v147, v123\nv_mov_b32 v148, v124\n" \
"v_mov_b32 v149, v125\nv_mov_b32 v150, v126\nv_mov_b32 v151, v127\nzf1m:\ns_waitcnt vmcnt(0)\nQvav 128,160,48,144\nMvav 128,164,52\nMvav 128,168,56\n" \
"Mvav 128,172,60\nQvav 132,176,48,148\nMvav 132,180,52\nMvav 132,184,56\nMvav 132,188,60\ns_nop 7\nv_max3_f32 v141, v128, v129, v130\nv_max3_f32 v142, v131, v132, v133\n" \
"v_max3_f32 v141, v141, v134, v135\nv_max3_f32 v141, v141, v142, v136\nv_mov_b32 v142, v141\ns_nop 1\nv_permlane16_swap_b32 v141, v142\ns_nop 0\nv_max_f32 v141, v141, v142\nv_mov_b32 v142, v141\n" \
"s_nop 1\nv_permlane32_swap_b32 v141, v142\ns_nop 0\nv_max_f32 v141, v141, v142\nv_sub_f32 v137, v136, v141\nE 137,137\nv_mov_b32 v136, v141\nv_sub_f32 v128, v128, v141\n" \
"v_sub_f32 v129, v129, v141\nv_sub_f32 v130, v130, v141\nv_sub_f32 v131, v131, v141\nv_sub_f32 v132, v132, v141\nv_sub_f32 v133, v133, v141\nv_sub_f32 v134, v134, v141\nv_sub_f32 v135, v135, v141\nE 128,128\n" \
"E 129,129\nE 130,130\nE 131,131\nE 132,132\nE 133,133\nE 134,134\nE 135,135\nv_mul_f32 v138, v138, v137\n" \
"C 192,128,129\nC 193,130,131\nC 194,132,133\nC 195,134,135\nv_add_f32 v138, v138, v128\nv_add_f32 v138, v138, v129\nv_add_f32 v138, v138, v130\nv_add_f32 v138, v138, v131\n" \
"v_add_f32 v138, v138, v132\nv_add_f32 v138, v138, v133\nv_add_f32 v138, v138, v134\nv_add_f32 v138, v138, v135\nAR 141,32\nv_mul_f32 v141, v141, v137\nAW 32,141\nAR 141,33\n" \
"v_mul_f32 v141, v141, v137\nAW 33,141\nAR 141,34\nv_mul_f32 v141, v141, v137\nAW 34,141\nAR 141,35\nv_mul_f32 v141, v141, v137\nAW 35,141\n" \
"AR 141,36\nv_mul_f32 v141, v141, v137\nAW 36,141\nAR 141,37\ns_branch 99f\n.p2align 12\nzp29:\ns_setpc_b64 s[98:99]\n" \
"99:\nv_mul_f32 v141, v141, v137\nAW 37,141\nAR 141,38\nv_mul_f32 v141, v141, v137\nAW 38,141\nAR 141,39\nv_mul_f32 v141, v141, v137\n" \
"AW 39,141\nAR 141,40\nv_mul_f32 v141, v141, v137\nAW 40,141\nAR 141,41\nv_mul_f32 v141, v141, v137\nAW 41,141\nAR 141,42\n" \
"v_mul_f32 v141, v141, v137\nAW 42,141\nAR 141,43\nv_mul_f32 v141, v141, v137\nAW 43,141\nAR 141,44\nv_mul_f32 v141, v141, v137\nAW 44,141\n" \
"AR 141,45\nv_mul_f32 v141, v141, v137\nAW 45,141\nAR 141,46\nv_mul_f32 v141, v141, v137\nAW 46,141\nAR 141,47\nv_mul_f32 v141, v141, v137\n" \
"AW 47,141\nAR 141,48\nv_mul_f32 v141, v141, v137\nAW 48,141\nAR 141,49\nv_mul_f32 v141, v141, v137\nAW 49,141\nAR 141,50\n" \
"v_mul_f32 v141, v141, v137\nAW 50,141\nAR 141,51\nv_mul_f32 v141, v141, v137\nAW 51,141\nAR 141,52\nv_mul_f32 v141, v141, v137\nAW 52,141\n" \
"AR 141,53\nv_mul_f32 v141, v141, v137\nAW 53,141\nAR 141,54\nv_mul_f32 v141, v141, v137\nAW 54,141\nAR 141,55\nv_mul_f32 v141, v141, v137\n" \
"AW 55,141\nAR 141,56\nv_mul_f32 v141, v141, v137\nAW 56,141\nAR 141,57\nv_mul_f32 v141, v141, v137\nAW 57,141\nAR 141,58\n" \
"v_mul_f32 v141, v141, v137\nAW 58,141\nAR 141,59\nv_mul_f32 v141, v141, v137\nAW 59,141\nAR 141,60\nv_mul_f32 v141, v141, v137\nAW 60,141\n" \
"AR 141,61\nv_mul_f32 v141, v141, v137\nAW 61,141\nAR 141,62\nv_mul_f32 v141, v141, v137\nAW 62,141\nAR 141,63\nv_mul_f32 v141, v141, v137\n" \
"AW 63,141\ns_nop 1\nMaav 32,128,192\nMaav 36,132,192\nMaav 40,136,192\nMaav 44,140,192\nMaav 48,144,192\nMaav 52,148,192\n" \
"Maav 56,152,192\nMaav 60,156,192\ns_add_u32 s77, s77, 8192\ns_sub_u32 s96, s96, 1\ns_cmp_lg_u32 s96, 0\ns_nop 7\ns_cbranch_scc1 zf1l\nv_mov_b32 v141, v138\n" \
"s_nop 1\nv_permlane16_swap_b32 v138, v141\ns_nop 0\nv_add_f32 v138, v138, v141\nv_mov_b32 v141, v138\ns_nop 1\nv_permlane32_swap_b32 v138, v141\ns_nop 0\n" \
"v_add_f32 v138, v138, v141\nv_mov_b32 v105, v138\nzf1e:\nv_cmp_lt_u32 vcc, 0x3c000000, v242\ns_cbranch_vccz zf2e\ns_add_u32 s96, s43, 2\ns_mov_b32 s77, s38\nv_mov_b32 v136, 0xff800000\n" \
"v_mov_b32 v138, 0\nv_add_u32 v140, 0x4000, v8\nv_accvgpr_write_b32 a64, 0\nv_accvgpr_write_b32 a65, 0\nv_accvgpr_write_b32 a66, 0\nv_accvgpr_write_b32 a67, 0\nv_accvgpr_write_b32 a68, 0\nv_accvgpr_write_b32 a69, 0\n" \
"v_accvgpr_write_b32 a70, 0\nv_accvgpr_write_b32 a71, 0\nv_accvgpr_write_b32 a72, 0\nv_accvgpr_write_b32 a73, 0\nv_accvgpr_write_b32 a74, 0\nv_accvgpr_write_b32 a75, 0\nv_accvgpr_write_b32 a76, 0\nv_accvgpr_write_b32 a77, 0\n" \
"v_accvgpr_write_b32 a78, 0\nv_accvgpr_write_b32 a79, 0\nv_accvgpr_write_b32 a80, 0\nv_accvgpr_write_b32 a81, 0\nv_accvgpr_write_b32 a82, 0\nv_accvgpr_write_b32 a83, 0\nv_accvgpr_write_b32 a84, 0\nv_accvgpr_write_b32 a85, 0\n" \
"v_accvgpr_write_b32 a86, 0\nv_accvgpr_write_b32 a87, 0\nv_accvgpr_write_b32 a88, 0\nv_accvgpr_write_b32 a89, 0\nv_accvgpr_write_b32 a90, 0\nv_accvgpr_write_b32 a91, 0\nv_accvgpr_write_b32 a92, 0\nv_accvgpr_write_b32 a93, 0\n" \
"v_accvgpr_write_b32 a94, 0\nv_accvgpr_write_b32 a95, 0\nzf2l:\ns_add_u32 s76, s77, 4096\nbuffer_load_dwordx4 a[160:163], v0, s[60:63], s77 offen offset:0 sc1\nbuffer_load_dwordx4 a[164:167], v0, s[60:63], s77 offen offset:1024 sc1\nbuffer_load_dwordx4 a[168:171], v0, s[60:63], s77 offen offset:2048 sc1\nbuffer_load_dwordx4 a[172:175], v0, s[60:63], s77 offen offset:3072 sc1\n" \
"buffer_load_dwordx4 a[176:179], v0, s[60:63], s76 offen offset:0 sc1\nbuffer_load_dwordx4 a[180:183], v0, s[60:63], s76 offen offset:1024 sc1\nbuffer_load_dwordx4 a[184:187], v0, s[60:63], s76 offen offset:2048 sc1\nbuffer_load_dwordx4 a[188:191], v0, s[60:63], s76 offen offset:3072 sc1\nbuffer_load_dwordx4 a[128:131], v140, s[60:63], s77 offen offset:0 sc1\nbuffer_load_dwordx4 a[132:135], v140, s[60:63], s77 offen offset:1024 sc1\nbuffer_load_dwordx4 a[136:139], v140, s[60:63], s77 offen offset:2048 sc1\nbuffer_load_dwordx4 a[140:143], v140, s[60:63], s77 offen offset:3072 sc1\n" \
"buffer_load_dwordx4 a[144:147], v140, s[60:63], s76 offen offset:0 sc1\nbuffer_load_dwordx4 a[148:151], v140, s[60:63], s76 offen offset:1024 sc1\nbuffer_load_dwordx4 a[152:155], v140, s[60:63], s76 offen offset:2048 sc1\nbuffer_load_dwordx4 a[156:159], v140, s[60:63], s76 offen offset:3072 sc1\nv_mov_b32 v144, v12\nv_mov_b32 v145, v12\nv_mov_b32 v146, v12\nv_mov_b32 v147, v12\n" \
"v_mov_b32 v148, v12\nv_mov_b32 v149, v12\nv_mov_b32 v150, v12\nv_mov_b32 v151, v12\ns_cmp_gt_u32 s96, 2\ns_cbranch_scc1 zf2m\nv_mov_b32 v144, v112\nv_mov_b32 v145, v113\n" \
"v_mov_b32 v146, v114\nv_mov_b32 v147, v115\nv_mov_b32 v148, v116\nv_mov_b32 v149, v117\nv_mov_b32 v150, v118\nv_mov_b32 v151, v119\ns_cmp_eq_u32 s96, 2\ns_cbranch_scc1 zf2m\n" \
"v_mov_b32 v144, v120\nv_mov_b32 v145, v121\nv_mov_b32 v146, v122\nv_mov_b32 v147, v123\nv_mov_b32 v148, v124\nv_mov_b32 v149, v125\nv_mov_b32 v150, v126\nv_mov_b32 v151, v127\n" \
"zf2m:\ns_waitcnt vmcnt(0)\nQvav 128,160,64,144\nMvav 128,164,68\nMvav 128,168,72\nMvav 128,172,76\nQvav 132,176,64,148\nMvav 132,180,68\n" \
"Mvav 132,184,72\nMvav 132,188,76\ns_nop 7\nv_max3_f32 v141, v128, v129, v130\nv_max3_f32 v142, v131, v132, v133\nv_max3_f32 v141, v141, v134, v135\nv_max3_f32 v141, v141, v142, v136\nv_mov_b32 v142, v141\n" \
"s_nop 1\nv_permlane16_swap_b32 v141, v142\ns_nop 0\nv_max_f32 v141, v141, v142\nv_mov_b32 v142, v141\ns_nop 1\nv_permlane32_swap_b32 v141, v142\ns_nop 0\n" \
"v_max_f32 v141, v141, v142\nv_sub_f32 v137, v136, v141\nE 137,137\nv_mov_b32 v136, v141\nv_sub_f32 v128, v128, v141\nv_sub_f32 v129, v129, v141\nv_sub_f32 v130, v130, v141\nv_sub_f32 v131, v131, v141\n" \
"v_sub_f32 v132, v132, v141\nv_sub_f32 v133, v133, v141\nv_sub_f32 v134, v134, v141\nv_sub_f32 v135, v135, v141\nE 128,128\nE 129,129\nE 130,130\nE 131,131\n" \
"E 132,132\nE 133,133\nE 134,134\nE 135,135\nv_mul_f32 v138, v138, v137\nC 192,128,129\nC 193,130,131\nC 194,132,133\n" \
"C 195,134,135\nv_add_f32 v138, v138, v128\nv_add_f32 v138, v138, v129\nv_add_f32 v138, v138, v130\nv_add_f32 v138, v138, v131\nv_add_f32 v138, v138, v132\nv_add_f32 v138, v138, v133\nv_add_f32 v138, v138, v134\n" \
"v_add_f32 v138, v138, v135\nAR 141,64\nv_mul_f32 v141, v141, v137\nAW 64,141\nAR 141,65\nv_mul_f32 v141, v141, v137\nAW 65,141\nAR 141,66\n" \
"v_mul_f32 v141, v141, v137\nAW 66,141\nAR 141,67\nv_mul_f32 v141, v141, v137\nAW 67,141\nAR 141,68\nv_mul_f32 v141, v141, v137\nAW 68,141\n" \
"AR 141,69\nv_mul_f32 v141, v141, v137\nAW 69,141\nAR 141,70\nv_mul_f32 v141, v141, v137\nAW 70,141\nAR 141,71\nv_mul_f32 v141, v141, v137\n" \
"AW 71,141\nAR 141,72\nv_mul_f32 v141, v141, v137\nAW 72,141\nAR 141,73\nv_mul_f32 v141, v141, v137\nAW 73,141\nAR 141,74\n" \
"v_mul_f32 v141, v141, v137\nAW 74,141\nAR 141,75\nv_mul_f32 v141, v141, v137\nAW 75,141\nAR 141,76\nv_mul_f32 v141, v141, v137\nAW 76,141\n" \
"AR 141,77\nv_mul_f32 v141, v141, v137\nAW 77,141\nAR 141,78\nv_mul_f32 v141, v141, v137\nAW 78,141\nAR 141,79\nv_mul_f32 v141, v141, v137\n" \
"AW 79,141\nAR 141,80\nv_mul_f32 v141, v141, v137\nAW 80,141\nAR 141,81\nv_mul_f32 v141, v141, v137\nAW 81,141\nAR 141,82\n" \
"v_mul_f32 v141, v141, v137\nAW 82,141\nAR 141,83\nv_mul_f32 v141, v141, v137\nAW 83,141\nAR 141,84\nv_mul_f32 v141, v141, v137\nAW 84,141\n" \
"AR 141,85\nv_mul_f32 v141, v141, v137\nAW 85,141\nAR 141,86\nv_mul_f32 v141, v141, v137\nAW 86,141\nAR 141,87\nv_mul_f32 v141, v141, v137\n" \
"AW 87,141\nAR 141,88\nv_mul_f32 v141, v141, v137\nAW 88,141\nAR 141,89\nv_mul_f32 v141, v141, v137\nAW 89,141\nAR 141,90\n" \
"v_mul_f32 v141, v141, v137\nAW 90,141\nAR 141,91\nv_mul_f32 v141, v141, v137\nAW 91,141\nAR 141,92\nv_mul_f32 v141, v141, v137\nAW 92,141\n" \
"AR 141,93\nv_mul_f32 v141, v141, v137\nAW 93,141\nAR 141,94\nv_mul_f32 v141, v141, v137\nAW 94,141\nAR 141,95\nv_mul_f32 v141, v141, v137\n" \
"AW 95,141\ns_nop 1\nMaav 64,128,192\nMaav 68,132,192\nMaav 72,136,192\nMaav 76,140,192\nMaav 80,144,192\nMaav 84,148,192\n" \
"Maav 88,152,192\nMaav 92,156,192\ns_add_u32 s77, s77, 8192\ns_sub_u32 s96, s96, 1\ns_cmp_lg_u32 s96, 0\ns_nop 7\ns_cbranch_scc1 zf2l\nv_mov_b32 v141, v138\n" \
"s_nop 1\nv_permlane16_swap_b32 v138, v141\ns_nop 0\nv_add_f32 v138, v138, v141\nv_mov_b32 v141, v138\ns_nop 1\nv_permlane32_swap_b32 v138, v141\ns_nop 0\n" \
"v_add_f32 v138, v138, v141\nv_mov_b32 v106, v138\nzf2e:\nv_cmp_lt_u32 vcc, 0x3c000000, v243\ns_cbranch_vccz zf3e\ns_add_u32 s96, s43, 4\ns_mov_b32 s77, s38\nv_mov_b32 v136, 0xff800000\n" \
"v_mov_b32 v138, 0\nv_add_u32 v140, 0x4000, v8\nv_accvgpr_write_b32 a96, 0\nv_accvgpr_write_b32 a97, 0\nv_accvgpr_write_b32 a98, 0\nv_accvgpr_write_b32 a99, 0\nv_accvgpr_write_b32 a100, 0\nv_accvgpr_write_b32 a101, 0\n" \
"v_accvgpr_write_b32 a102, 0\nv_accvgpr_write_b32 a103, 0\nv_accvgpr_write_b32 a104, 0\nv_accvgpr_write_b32 a105, 0\nv_accvgpr_write_b32 a106, 0\nv_accvgpr_write_b32 a107, 0\nv_accvgpr_write_b32 a108, 0\nv_accvgpr_write_b32 a109, 0\n" \
"v_accvgpr_write_b32 a110, 0\nv_accvgpr_write_b32 a111, 0\nv_accvgpr_write_b32 a112, 0\nv_accvgpr_write_b32 a113, 0\nv_accvgpr_write_b32 a114, 0\nv_accvgpr_write_b32 a115, 0\nv_accvgpr_write_b32 a116, 0\nv_accvgpr_write_b32 a117, 0\n" \
"v_accvgpr_write_b32 a118, 0\nv_accvgpr_write_b32 a119, 0\nv_accvgpr_write_b32 a120, 0\nv_accvgpr_write_b32 a121, 0\nv_accvgpr_write_b32 a122, 0\nv_accvgpr_write_b32 a123, 0\nv_accvgpr_write_b32 a124, 0\nv_accvgpr_write_b32 a125, 0\n" \
"v_accvgpr_write_b32 a126, 0\nv_accvgpr_write_b32 a127, 0\nzf3l:\ns_add_u32 s76, s77, 4096\nbuffer_load_dwordx4 a[160:163], v0, s[60:63], s77 offen offset:0 sc1\nbuffer_load_dwordx4 a[164:167], v0, s[60:63], s77 offen offset:1024 sc1\nbuffer_load_dwordx4 a[168:171], v0, s[60:63], s77 offen offset:2048 sc1\nbuffer_load_dwordx4 a[172:175], v0, s[60:63], s77 offen offset:3072 sc1\n" \
"buffer_load_dwordx4 a[176:179], v0, s[60:63], s76 offen offset:0 sc1\nbuffer_load_dwordx4 a[180:183], v0, s[60:63], s76 offen offset:1024 sc1\nbuffer_load_dwordx4 a[184:187], v0, s[60:63], s76 offen offset:2048 sc1\nbuffer_load_dwordx4 a[188:191], v0, s[60:63], s76 offen offset:3072 sc1\nbuffer_load_dwordx4 a[128:131], v140, s[60:63], s77 offen offset:0 sc1\nbuffer_load_dwordx4 a[132:135], v140, s[60:63], s77 offen offset:1024 sc1\nbuffer_load_dwordx4 a[136:139], v140, s[60:63], s77 offen offset:2048 sc1\nbuffer_load_dwordx4 a[140:143], v140, s[60:63], s77 offen offset:3072 sc1\n" \
"buffer_load_dwordx4 a[144:147], v140, s[60:63], s76 offen offset:0 sc1\nbuffer_load_dwordx4 a[148:151], v140, s[60:63], s76 offen offset:1024 sc1\nbuffer_load_dwordx4 a[152:155], v140, s[60:63], s76 offen offset:2048 sc1\nbuffer_load_dwordx4 a[156:159], v140, s[60:63], s76 offen offset:3072 sc1\nv_mov_b32 v144, v12\nv_mov_b32 v145, v12\nv_mov_b32 v146, v12\nv_mov_b32 v147, v12\n" \
"v_mov_b32 v148, v12\nv_mov_b32 v149, v12\nv_mov_b32 v150, v12\nv_mov_b32 v151, v12\ns_cmp_gt_u32 s96, 2\ns_cbranch_scc1 zf3m\nv_mov_b32 v144, v112\nv_mov_b32 v145, v113\n" \
"v_mov_b32 v146, v114\nv_mov_b32 v147, v115\nv_mov_b32 v148, v116\nv_mov_b32 v149, v117\nv_mov_b32 v150, v118\nv_mov_b32 v151, v119\ns_cmp_eq_u32 s96, 2\ns_cbranch_scc1 zf3m\n" \
"v_mov_b32 v144, v120\nv_mov_b32 v145, v121\nv_mov_b32 v146, v122\nv_mov_b32 v147, v123\nv_mov_b32 v148, v124\nv_mov_b32 v149, v125\nv_mov_b32 v150, v126\nv_mov_b32 v151, v127\n" \
"zf3m:\ns_waitcnt vmcnt(0)\nQvav 128,160,80,144\nMvav 128,164,84\nMvav 128,168,88\nMvav 128,172,92\nQvav 132,176,80,148\nMvav 132,180,84\n" \
"Mvav 132,184,88\nMvav 132,188,92\ns_nop 7\nv_max3_f32 v141, v128, v129, v130\nv_max3_f32 v142, v131, v132, v133\nv_max3_f32 v141, v141, v134, v135\nv_max3_f32 v141, v141, v142, v136\nv_mov_b32 v142, v141\n" \
"s_nop 1\nv_permlane16_swap_b32 v141, v142\ns_nop 0\nv_max_f32 v141, v141, v142\nv_mov_b32 v142, v141\ns_nop 1\nv_permlane32_swap_b32 v141, v142\ns_nop 0\n" \
"v_max_f32 v141, v141, v142\nv_sub_f32 v137, v136, v141\nE 137,137\nv_mov_b32 v136, v141\nv_sub_f32 v128, v128, v141\nv_sub_f32 v129, v129, v141\nv_sub_f32 v130, v130, v141\nv_sub_f32 v131, v131, v141\n" \
"v_sub_f32 v132, v132, v141\nv_sub_f32 v133, v133, v141\nv_sub_f32 v134, v134, v141\nv_sub_f32 v135, v135, v141\nE 128,128\nE 129,129\nE 130,130\nE 131,131\n" \
"E 132,132\nE 133,133\nE 134,134\nE 135,135\nv_mul_f32 v138, v138, v137\nC 192,128,129\nC 193,130,131\nC 194,132,133\n" \
"C 195,134,135\nv_add_f32 v138, v138, v128\nv_add_f32 v138, v138, v129\nv_add_f32 v138, v138, v130\nv_add_f32 v138, v138, v131\nv_add_f32 v138, v138, v132\nv_add_f32 v138, v138, v133\nv_add_f32 v138, v138, v134\n" \
"v_add_f32 v138, v138, v135\nAR 141,96\nv_mul_f32 v141, v141, v137\nAW 96,141\nAR 141,97\nv_mul_f32 v141, v141, v137\nAW 97,141\nAR 141,98\n" \
"v_mul_f32 v141, v141, v137\nAW 98,141\nAR 141,99\nv_mul_f32 v141, v141, v137\nAW 99,141\nAR 141,100\nv_mul_f32 v141, v141, v137\nAW 100,141\n" \
"AR 141,101\nv_mul_f32 v141, v141, v137\nAW 101,141\nAR 141,102\nv_mul_f32 v141, v141, v137\nAW 102,141\nAR 141,103\nv_mul_f32 v141, v141, v137\n" \
"AW 103,141\nAR 141,104\nv_mul_f32 v141, v141, v137\nAW 104,141\nAR 141,105\nv_mul_f32 v141, v141, v137\nAW 105,141\nAR 141,106\n" \
"v_mul_f32 v141, v141, v137\nAW 106,141\nAR 141,107\nv_mul_f32 v141, v141, v137\nAW 107,141\nAR 141,108\nv_mul_f32 v141, v141, v137\nAW 108,141\n" \
"AR 141,109\nv_mul_f32 v141, v141, v137\nAW 109,141\nAR 141,110\nv_mul_f32 v141, v141, v137\nAW 110,141\nAR 141,111\nv_mul_f32 v141, v141, v137\n" \
"AW 111,141\nAR 141,112\nv_mul_f32 v141, v141, v137\nAW 112,141\nAR 141,113\nv_mul_f32 v141, v141, v137\nAW 113,141\nAR 141,114\n" \
"v_mul_f32 v141, v141, v137\nAW 114,141\nAR 141,115\nv_mul_f32 v141, v141, v137\nAW 115,141\nAR 141,116\nv_mul_f32 v141, v141, v137\nAW 116,141\n" \
"AR 141,117\nv_mul_f32 v141, v141, v137\nAW 117,141\nAR 141,118\nv_mul_f32 v141, v141, v137\nAW 118,141\nAR 141,119\nv_mul_f32 v141, v141, v137\n" \
"AW 119,141\nAR 141,120\nv_mul_f32 v141, v141, v137\nAW 120,141\nAR 141,121\nv_mul_f32 v141, v141, v137\nAW 121,141\nAR 141,122\n" \
"v_mul_f32 v141, v141, v137\nAW 122,141\nAR 141,123\nv_mul_f32 v141, v141, v137\nAW 123,141\nAR 141,124\nv_mul_f32 v141, v141, v137\nAW 124,141\n" \
"AR 141,125\nv_mul_f32 v141, v141, v137\nAW 125,141\nAR 141,126\nv_mul_f32 v141, v141, v137\nAW 126,141\nAR 141,127\nv_mul_f32 v141, v141, v137\n" \
"AW 127,141\ns_nop 1\nMaav 96,128,192\nMaav 100,132,192\nMaav 104,136,192\nMaav 108,140,192\nMaav 112,144,192\nMaav 116,148,192\n" \
"Maav 120,152,192\nMaav 124,156,192\ns_add_u32 s77, s77, 8192\ns_sub_u32 s96, s96, 1\ns_cmp_lg_u32 s96, 0\ns_nop 7\ns_cbranch_scc1 zf3l\nv_mov_b32 v141, v138\n" \
"s_nop 1\nv_permlane16_swap_b32 v138, v141\ns_nop 0\nv_add_f32 v138, v138, v141\nv_mov_b32 v141, v138\ns_nop 1\nv_permlane32_swap_b32 v138, v141\ns_nop 0\n" \
"v_add_f32 v138, v138, v141\nv_mov_b32 v107, v138\nzf3e:\ns_branch zend\nzlast:\n.subsection 0\n.purgem D\n.purgem Ta\n" \
".purgem Ra\n.purgem Zvaa\n.purgem AR\n.purgem Zaaa\n.purgem Mvaa\n.purgem Maaa\n.purgem C\n.purgem AW\n" \
".purgem DS\n.purgem Qvav\n.purgem Mvav\n.purgem E\n.purgem Zaav\n.purgem Maav\n.purgem Mvvv\n.purgem Qvaa\n" \
".purgem Zvvv\n.purgem Qvva\n" \
""
#define Z_STAMP 0
#define Z_NY 6
#define Z_CLOB "v16", "v17", "v18", "v19", "v20", "v21", "v22", "v23", "v24", "v25", "v26", "v27", "v28", "v29", "v30", "v31", "v32", "v33", "v34", "v35", "v36", "v37", "v38", "v39", "v40", "v41", "v42", "v43", "v44", "v45", "v46", "v47", "v48", "v49", "v50", "v51", "v52", "v53", "v54", "v55", "v56", "v57", "v58", "v59", "v60", "v61", "v62", "v63", "v64", "v65", "v66", "v67", "v68", "v69", "v70", "v71", "v72", "v73", "v74", "v75", "v76", "v77", "v78", "v79", "v80", "v81", "v82", "v83", "v84", "v85", "v86", "v87", "v88", "v89", "v90", "v91", "v92", "v93", "v94", "v95", "v96", "v97", "v98", "v99", "v100", "v101", "v102", "v103", "v108", "v109", "v110", "v111", "v112", "v113", "v114", "v115", "v116", "v117", "v118", "v119", "v120", "v121", "v122", "v123", "v124", "v125", "v126", "v127", "v128", "v129", "v130", "v131", "v132", "v133", "v134", "v135", "v136", "v137", "v138", "v139", "v140", "v141", "v142", "v143", "v144", "v145", "v146", "v147", "v148", "v149", "v150", "v151", "v152", "v153", "v154", "v155", "v156", "v157", "v158", "v159", "v160", "v161", "v162", "v163", "v164", "v165", "v166", "v167", "v168", "v169", "v170", "v171", "v172", "v173", "v174", "v175", "v176", "v177", "v178", "v179", "v180", "v181", "v182", "v183", "v184", "v185", "v186", "v187", "v188", "v189", "v190", "v191", "v192", "v193", "v194", "v195", "v196", "v197", "v198", "v199", "v200", "v201", "v202", "v203", "v204", "v205", "v206", "v207", "v208", "v209", "v210", "v211", "v212", "v213", "v214", "v215", "v216", "v217", "v218", "v219", "v220", "v221", "v222", "v223", "v224", "v225", "v226", "v227", "v228", "v229", "v230", "v231", "v232", "v233", "v234", "v235", "v236", "v237", "v238", "v239", "v240", "v241", "v242", "v243", "v244", "v245", "v246", "v247", "v248", "v249", "v250", "v251", "v252", "v253", "v254", "v255", "a0", "a1", "a2", "a3", "a4", "a5", "a6", "a7", "a8", "a9", "a10", "a11", "a12", "a13", "a14", "a15", "a16", "a17", "a18", "a19", "a20", "a21", "a22", "a23", "a24", "a25", "a26", "a27", "a28", "a29", "a30", "a31", "a32", "a33", "a34", "a35", "a36", "a37", "a38", "a39", "a40", "a41", "a42", "a43", "a44", "a45", "a46", "a47", "a48", "a49", "a50", "a51", "a52", "a53", "a54", "a55", "a56", "a57", "a58", "a59", "a60", "a61", "a62", "a63", "a64", "a65", "a66", "a67", "a68", "a69", "a70", "a71", "a72", "a73", "a74", "a75", "a76", "a77", "a78", "a79", "a80", "a81", "a82", "a83", "a84", "a85", "a86", "a87", "a88", "a89", "a90", "a91", "a92", "a93", "a94", "a95", "a96", "a97", "a98", "a99", "a100", "a101", "a102", "a103", "a104", "a105", "a106", "a107", "a108", "a109", "a110", "a111", "a112", "a113", "a114", "a115", "a116", "a117", "a118", "a119", "a120", "a121", "a122", "a123", "a124", "a125", "a126", "a127", "a128", "a129", "a130", "a131", "a132", "a133", "a134", "a135", "a136", "a137", "a138", "a139", "a140", "a141", "a142", "a143", "a144", "a145", "a146", "a147", "a148", "a149", "a150", "a151", "a152", "a153", "a154", "a155", "a156", "a157", "a158", "a159", "a160", "a161", "a162", "a163", "a164", "a165", "a166", "a167", "a168", "a169", "a170", "a171", "a172", "a173", "a174", "a175", "a176", "a177", "a178", "a179", "a180", "a181", "a182", "a183", "a184", "a185", "a186", "a187", "a188", "a189", "a190", "a191", "a192", "a193", "a194", "a195", "a196", "a197", "a198", "a199", "a200", "a201", "a202", "a203", "a204", "a205", "a206", "a207", "a208", "a209", "a210", "a211", "a212", "a213", "a214", "a215", "a216", "a217", "a218", "a219", "a220", "a221", "a222", "a223", "a224", "a225", "a226", "a227", "a228", "a229", "a230", "a231", "a232", "a233", "a234", "a235", "a236", "a237", "a238", "a239", "a240", "a241", "a242", "a243", "a244", "a245", "a246", "a247", "a248", "a249", "a250", "a251", "a252", "a253", "a254", "a255", "s72", "s73", "s74", "s75", "s76", "s77", "s78", "s79", "s88", "s89", "s90", "s91", "s92", "s93", "s94", "s95", "s96", "s97", "s98", "s99", "s100", "s101", "m0", "scc", "vcc", "memory"
#define Z_CLOB1 "v28", "a251", "a252", "a253", "s72", "s73", "s74", "s75", "s76", "s77", "s78", "s79", "s88", "s89", "s90", "s91", "s92", "s93", "s94", "s95", "m0", "scc", "memory"
#define H4_PROW \
  "s_nop 4\n\t" \
  "v_xor_b32 v122, 128, %[vW0]\n\t" \
  "s_mov_b32 s49, 31744\n\t" \
  "s_mov_b32 s50, 6144\n\t" \
  "s_mov_b32 s51, 37888\n\t" \
  "s_mov_b32 s40, 131072\n\t" \
  "s_add_u32 m0, %[m0W], 0\n\t" \
  "s_nop 0\n\t" \
  "buffer_load_dwordx4 %[vW0], %[srdW], 0 offen offset:0 lds\n\t" \
  "buffer_load_dwordx4 %[vW0], %[srdW], s49 offen offset:1024 lds\n\t" \
  "buffer_load_dwordx4 v122, %[srdW], s50 offen offset:2048 lds\n\t" \
  "buffer_load_dwordx4 v122, %[srdW], s51 offen offset:3072 lds\n\t" \
  "v_add_u32 %[vW0], s40, %[vW0]\n\t" \
  "v_add_u32 v122, s40, v122\n\t" \
  "s_add_u32 m0, %[m0W], 16384\n\t" \
  "s_nop 0\n\t" \
  "buffer_load_dwordx4 %[vW0], %[srdW], 0 offen offset:0 lds\n\t" \
  "buffer_load_dwordx4 %[vW0], %[srdW], s49 offen offset:1024 lds\n\t" \
  "buffer_load_dwordx4 v122, %[srdW], s50 offen offset:2048 lds\n\t" \
  "buffer_load_dwordx4 v122, %[srdW], s51 offen offset:3072 lds\n\t" \
  "v_add_u32 %[vW0], s40, %[vW0]\n\t" \
  "v_add_u32 v122, s40, v122\n\t" \
  "s_add_u32 m0, %[m0W], 32768\n\t" \
  "s_nop 0\n\t" \
  "buffer_load_dwordx4 %[vW0], %[srdW], 0 offen offset:0 lds\n\t" \
  "buffer_load_dwordx4 %[vW0], %[srdW], s49 offen offset:1024 lds\n\t" \
  "buffer_load_dwordx4 v122, %[srdW], s50 offen offset:2048 lds\n\t" \
  "buffer_load_dwordx4 v122, %[srdW], s51 offen offset:3072 lds\n\t" \
  "v_add_u32 %[vW0], s40, %[vW0]\n\t" \
  "v_add_u32 v122, s40, v122\n\t" \
  ""
#define H4_LOOP \
  "s_branch 99f\n\t" \
  ".p2align 12\n\t" \
  "att_o0:\n\t" \
  "s_setpc_b64 s[68:69]\n\t" \
  "99:\n\t" \
  "s_nop 4\n\t" \
  "v_xor_b32 v122, 128, %[vW0]\n\t" \
  "s_mov_b32 s49, 31744\n\t" \
  "s_mov_b32 s50, 6144\n\t" \
  "s_mov_b32 s51, 37888\n\t" \
  "s_mov_b32 s40, 131072\n\t" \
  "s_mov_b32 s52, %[m0W]\n\t" \
  "v_mov_b32 v112, %[wad0]\n\t" \
  "v_xor_b32 v113, 32, %[wad0]\n\t" \
  "v_xor_b32 v114, 64, %[wad0]\n\t" \
  "v_xor_b32 v115, 96, %[wad0]\n\t" \
  "v_xor_b32 v116, 128, %[wad0]\n\t" \
  "v_xor_b32 v117, 160, %[wad0]\n\t" \
  "v_xor_b32 v118, 192, %[wad0]\n\t" \
  "v_xor_b32 v119, 224, %[wad0]\n\t" \
  "s_add_u32 m0, %[m0W], 49152\n\t" \
  "buffer_load_dwordx4 a[128:131], %[vA], %[srdG], s72 offen offset:0 sc1\n\t" \
  "ds_read_b64_tr_b16 a[0:1], v112 offset:0\n\t" \
  "ds_read_b64_tr_b16 a[2:3], v112 offset:1024\n\t" \
  "buffer_load_dwordx4 a[132:135], %[vA], %[srdG], s72 offen offset:1024 sc1\n\t" \
  "ds_read_b64_tr_b16 a[32:33], v112 offset:8192\n\t" \
  "ds_read_b64_tr_b16 a[34:35], v112 offset:9216\n\t" \
  "buffer_load_dwordx4 a[136:139], %[vA], %[srdG], s73 offen offset:0 sc1\n\t" \
  "ds_read_b64_tr_b16 a[4:5], v113 offset:0\n\t" \
  "ds_read_b64_tr_b16 a[6:7], v113 offset:1024\n\t" \
  "buffer_load_dwordx4 a[140:143], %[vA], %[srdG], s73 offen offset:1024 sc1\n\t" \
  "ds_read_b64_tr_b16 a[36:37], v113 offset:8192\n\t" \
  "ds_read_b64_tr_b16 a[38:39], v113 offset:9216\n\t" \
  "buffer_load_dwordx4 a[144:147], %[vA], %[srdG], s74 offen offset:0 sc1\n\t" \
  "ds_read_b64_tr_b16 a[8:9], v114 offset:0\n\t" \
  "ds_read_b64_tr_b16 a[10:11], v114 offset:1024\n\t" \
  "buffer_load_dwordx4 a[148:151], %[vA], %[srdG], s74 offen offset:1024 sc1\n\t" \
  "ds_read_b64_tr_b16 a[40:41], v114 offset:8192\n\t" \
  "ds_read_b64_tr_b16 a[42:43], v114 offset:9216\n\t" \
  "buffer_load_dwordx4 a[152:155], %[vA], %[srdG], s75 offen offset:0 sc1\n\t" \
  "ds_read_b64_tr_b16 a[12:13], v115 offset:0\n\t" \
  "ds_read_b64_tr_b16 a[14:15], v115 offset:1024\n\t" \
  "buffer_load_dwordx4 a[156:159], %[vA], %[srdG], s75 offen offset:1024 sc1\n\t" \
  "ds_read_b64_tr_b16 a[44:45], v115 offset:8192\n\t" \
  "ds_read_b64_tr_b16 a[46:47], v115 offset:9216\n\t" \
  "v_add_u32 %[vA], 0x8000, %[vA]\n\t" \
  "ds_read_b64_tr_b16 a[16:17], v116 offset:0\n\t" \
  "ds_read_b64_tr_b16 a[18:19], v116 offset:1024\n\t" \
  "buffer_load_dwordx4 a[160:163], %[vA], %[srdG], s72 offen offset:0 sc1\n\t" \
  "ds_read_b64_tr_b16 a[48:49], v116 offset:8192\n\t" \
  "ds_read_b64_tr_b16 a[50:51], v116 offset:9216\n\t" \
  "buffer_load_dwordx4 a[164:167], %[vA], %[srdG], s72 offen offset:1024 sc1\n\t" \
  "ds_read_b64_tr_b16 a[20:21], v117 offset:0\n\t" \
  "ds_read_b64_tr_b16 a[22:23], v117 offset:1024\n\t" \
  "buffer_load_dwordx4 a[168:171], %[vA], %[srdG], s73 offen offset:0 sc1\n\t" \
  "ds_read_b64_tr_b16 a[52:53], v117 offset:8192\n\t" \
  "ds_read_b64_tr_b16 a[54:55], v117 offset:9216\n\t" \
  "buffer_load_dwordx4 a[172:175], %[vA], %[srdG], s73 offen offset:1024 sc1\n\t" \
  "ds_read_b64_tr_b16 a[24:25], v118 offset:0\n\t" \
  "ds_read_b64_tr_b16 a[26:27], v118 offset:1024\n\t" \
  "buffer_load_dwordx4 %[vW0], %[srdW], 0 offen offset:0 lds\n\t" \
  "ds_read_b64_tr_b16 a[56:57], v118 offset:8192\n\t" \
  "ds_read_b64_tr_b16 a[58:59], v118 offset:9216\n\t" \
  "buffer_load_dwordx4 %[vW0], %[srdW], s49 offen offset:1024 lds\n\t" \
  "ds_read_b64_tr_b16 a[28:29], v119 offset:0\n\t" \
  "ds_read_b64_tr_b16 a[30:31], v119 offset:1024\n\t" \
  "buffer_load_dwordx4 a[176:179], %[vA], %[srdG], s74 offen offset:0 sc1\n\t" \
  "ds_read_b64_tr_b16 a[60:61], v119 offset:8192\n\t" \
  "ds_read_b64_tr_b16 a[62:63], v119 offset:9216\n\t" \
  "buffer_load_dwordx4 a[180:183], %[vA], %[srdG], s74 offen offset:1024 sc1\n\t" \
  "buffer_load_dwordx4 v122, %[srdW], s50 offen offset:2048 lds\n\t" \
  "buffer_load_dwordx4 v122, %[srdW], s51 offen offset:3072 lds\n\t" \
  "v_add_u32 %[vW0], s40, %[vW0]\n\t" \
  "v_add_u32 v122, s40, v122\n\t" \
  "s_waitcnt lgkmcnt(0)\n\t" \
  "s_waitcnt vmcnt(16)\n\t" \
  "v_mfma_f32_16x16x32_bf16 v[128:131], a[0:3], a[128:131], 0\n\t" \
  "v_mfma_f32_16x16x32_bf16 v[128:131], a[32:35], a[132:135], v[128:131]\n\t" \
  "buffer_load_dwordx4 a[184:187], %[vA], %[srdG], s75 offen offset:0 sc1\n\t" \
  "v_mfma_f32_16x16x32_bf16 v[132:135], a[36:39], a[132:135], 0\n\t" \
  "v_mfma_f32_16x16x32_bf16 v[132:135], a[4:7], a[128:131], v[132:135]\n\t" \
  "v_mfma_f32_16x16x32_bf16 v[136:139], a[8:11], a[128:131], 0\n\t" \
  "v_mfma_f32_16x16x32_bf16 v[136:139], a[40:43], a[132:135], v[136:139]\n\t" \
  "buffer_load_dwordx4 a[188:191], %[vA], %[srdG], s75 offen offset:1024 sc1\n\t" \
  "v_mfma_f32_16x16x32_bf16 v[140:143], a[44:47], a[132:135], 0\n\t" \
  "v_mfma_f32_16x16x32_bf16 v[140:143], a[12:15], a[128:131], v[140:143]\n\t" \
  "v_add_u32 %[vA], 0x8000, %[vA]\n\t" \
  "v_mfma_f32_16x16x32_bf16 v[144:147], a[16:19], a[128:131], 0\n\t" \
  "v_mfma_f32_16x16x32_bf16 v[144:147], a[48:51], a[132:135], v[144:147]\n\t" \
  "v_mfma_f32_16x16x32_bf16 v[148:151], a[52:55], a[132:135], 0\n\t" \
  "v_mfma_f32_16x16x32_bf16 v[148:151], a[20:23], a[128:131], v[148:151]\n\t" \
  "v_mfma_f32_16x16x32_bf16 v[152:155], a[24:27], a[128:131], 0\n\t" \
  "s_cmp_eq_u32 %[cnt], %[pc]\n\t" \
  "s_cselect_b64 vcc, -1, 0\n\t" \
  "v_mfma_f32_16x16x32_bf16 v[152:155], a[56:59], a[132:135], v[152:155]\n\t" \
  "v_mfma_f32_16x16x32_bf16 v[156:159], a[60:63], a[132:135], 0\n\t" \
  "v_mfma_f32_16x16x32_bf16 v[156:159], a[28:31], a[128:131], v[156:159]\n\t" \
  "s_waitcnt vmcnt(16)\n\t" \
  "v_mfma_f32_16x16x32_bf16 v[188:191], a[28:31], a[136:139], 0\n\t" \
  "v_mfma_f32_16x16x32_bf16 v[188:191], a[60:63], a[140:143], v[188:191]\n\t" \
  "buffer_load_dwordx4 a[128:131], %[vA], %[srdG], s72 offen offset:0 sc1\n\t" \
  "v_mfma_f32_16x16x32_bf16 v[184:187], a[56:59], a[140:143], 0\n\t" \
  "v_mfma_f32_16x16x32_bf16 v[184:187], a[24:27], a[136:139], v[184:187]\n\t" \
  "v_mfma_f32_16x16x32_bf16 v[180:183], a[20:23], a[136:139], 0\n\t" \
  "v_mfma_f32_16x16x32_bf16 v[180:183], a[52:55], a[140:143], v[180:183]\n\t" \
  "buffer_load_dwordx4 a[132:135], %[vA], %[srdG], s72 offen offset:1024 sc1\n\t" \
  "v_mfma_f32_16x16x32_bf16 v[176:179], a[48:51], a[140:143], 0\n\t" \
  "v_mfma_f32_16x16x32_bf16 v[176:179], a[16:19], a[136:139], v[176:179]\n\t" \
  "v_mfma_f32_16x16x32_bf16 v[172:175], a[12:15], a[136:139], 0\n\t" \
  "v_mfma_f32_16x16x32_bf16 v[172:175], a[44:47], a[140:143], v[172:175]\n\t" \
  "v_mfma_f32_16x16x32_bf16 v[168:171], a[40:43], a[140:143], 0\n\t" \
  "s_waitcnt vmcnt(26)\n\t" \
  "s_barrier\n\t" \
  "v_mfma_f32_16x16x32_bf16 v[168:171], a[8:11], a[136:139], v[168:171]\n\t" \
  "ds_read_b64_tr_b16 a[64:65], v112 offset:16384\n\t" \
  "v_mfma_f32_16x16x32_bf16 v[164:167], a[4:7], a[136:139], 0\n\t" \
  "ds_read_b64_tr_b16 a[66:67], v112 offset:17408\n\t" \
  "v_mfma_f32_16x16x32_bf16 v[164:167], a[36:39], a[140:143], v[164:167]\n\t" \
  "ds_read_b64_tr_b16 a[96:97], v112 offset:24576\n\t" \
  "v_mfma_f32_16x16x32_bf16 v[160:163], a[32:35], a[140:143], 0\n\t" \
  "ds_read_b64_tr_b16 a[98:99], v112 offset:25600\n\t" \
  "v_mfma_f32_16x16x32_bf16 v[160:163], a[0:3], a[136:139], v[160:163]\n\t" \
  "ds_read_b64_tr_b16 a[68:69], v113 offset:16384\n\t" \
  "s_waitcnt vmcnt(16)\n\t" \
  "v_mfma_f32_16x16x32_bf16 v[192:195], a[0:3], a[144:147], 0\n\t" \
  "ds_read_b64_tr_b16 a[70:71], v113 offset:17408\n\t" \
  "v_mfma_f32_16x16x32_bf16 v[192:195], a[32:35], a[148:151], v[192:195]\n\t" \
  "ds_read_b64_tr_b16 a[100:101], v113 offset:24576\n\t" \
  "buffer_load_dwordx4 a[136:139], %[vA], %[srdG], s73 offen offset:0 sc1\n\t" \
  "v_mfma_f32_16x16x32_bf16 v[196:199], a[36:39], a[148:151], 0\n\t" \
  "ds_read_b64_tr_b16 a[102:103], v113 offset:25600\n\t" \
  "v_mfma_f32_16x16x32_bf16 v[196:199], a[4:7], a[144:147], v[196:199]\n\t" \
  "ds_read_b64_tr_b16 a[72:73], v114 offset:16384\n\t" \
  "v_mfma_f32_16x16x32_bf16 v[200:203], a[8:11], a[144:147], 0\n\t" \
  "ds_read_b64_tr_b16 a[74:75], v114 offset:17408\n\t" \
  "v_mfma_f32_16x16x32_bf16 v[200:203], a[40:43], a[148:151], v[200:203]\n\t" \
  "ds_read_b64_tr_b16 a[104:105], v114 offset:24576\n\t" \
  "buffer_load_dwordx4 a[140:143], %[vA], %[srdG], s73 offen offset:1024 sc1\n\t" \
  "v_mfma_f32_16x16x32_bf16 v[204:207], a[44:47], a[148:151], 0\n\t" \
  "ds_read_b64_tr_b16 a[106:107], v114 offset:25600\n\t" \
  "v_mfma_f32_16x16x32_bf16 v[204:207], a[12:15], a[144:147], v[204:207]\n\t" \
  "ds_read_b64_tr_b16 a[76:77], v115 offset:16384\n\t" \
  "v_mfma_f32_16x16x32_bf16 v[208:211], a[16:19], a[144:147], 0\n\t" \
  "ds_read_b64_tr_b16 a[78:79], v115 offset:17408\n\t" \
  "s_add_u32 m0, s52, 0\n\t" \
  "v_mfma_f32_16x16x32_bf16 v[208:211], a[48:51], a[148:151], v[208:211]\n\t" \
  "ds_read_b64_tr_b16 a[108:109], v115 offset:24576\n\t" \
  "buffer_load_dwordx4 %[vW0], %[srdW], 0 offen offset:0 lds\n\t" \
  "v_mfma_f32_16x16x32_bf16 v[212:215], a[52:55], a[148:151], 0\n\t" \
  "ds_read_b64_tr_b16 a[110:111], v115 offset:25600\n\t" \
  "v_mfma_f32_16x16x32_bf16 v[212:215], a[20:23], a[144:147], v[212:215]\n\t" \
  "ds_read_b64_tr_b16 a[80:81], v116 offset:16384\n\t" \
  "v_mfma_f32_16x16x32_bf16 v[216:219], a[24:27], a[144:147], 0\n\t" \
  "ds_read_b64_tr_b16 a[82:83], v116 offset:17408\n\t" \
  "v_mfma_f32_16x16x32_bf16 v[216:219], a[56:59], a[148:151], v[216:219]\n\t" \
  "ds_read_b64_tr_b16 a[112:113], v116 offset:24576\n\t" \
  "buffer_load_dwordx4 %[vW0], %[srdW], s49 offen offset:1024 lds\n\t" \
  "v_mfma_f32_16x16x32_bf16 v[220:223], a[60:63], a[148:151], 0\n\t" \
  "ds_read_b64_tr_b16 a[114:115], v116 offset:25600\n\t" \
  "v_mfma_f32_16x16x32_bf16 v[220:223], a[28:31], a[144:147], v[220:223]\n\t" \
  "ds_read_b64_tr_b16 a[84:85], v117 offset:16384\n\t" \
  "s_waitcnt vmcnt(18)\n\t" \
  "v_mfma_f32_16x16x32_bf16 v[252:255], a[28:31], a[152:155], 0\n\t" \
  "ds_read_b64_tr_b16 a[86:87], v117 offset:17408\n\t" \
  "v_mfma_f32_16x16x32_bf16 v[252:255], a[60:63], a[156:159], v[252:255]\n\t" \
  "ds_read_b64_tr_b16 a[116:117], v117 offset:24576\n\t" \
  "buffer_load_dwordx4 a[144:147], %[vA], %[srdG], s74 offen offset:0 sc1\n\t" \
  "v_mfma_f32_16x16x32_bf16 v[248:251], a[56:59], a[156:159], 0\n\t" \
  "ds_read_b64_tr_b16 a[118:119], v117 offset:25600\n\t" \
  "v_mfma_f32_16x16x32_bf16 v[248:251], a[24:27], a[152:155], v[248:251]\n\t" \
  "ds_read_b64_tr_b16 a[88:89], v118 offset:16384\n\t" \
  "v_mfma_f32_16x16x32_bf16 v[244:247], a[20:23], a[152:155], 0\n\t" \
  "ds_read_b64_tr_b16 a[90:91], v118 offset:17408\n\t" \
  "v_mfma_f32_16x16x32_bf16 v[244:247], a[52:55], a[156:159], v[244:247]\n\t" \
  "ds_read_b64_tr_b16 a[120:121], v118 offset:24576\n\t" \
  "buffer_load_dwordx4 a[148:151], %[vA], %[srdG], s74 offen offset:1024 sc1\n\t" \
  "v_mfma_f32_16x16x32_bf16 v[240:243], a[48:51], a[156:159], 0\n\t" \
  "ds_read_b64_tr_b16 a[122:123], v118 offset:25600\n\t" \
  "v_mfma_f32_16x16x32_bf16 v[240:243], a[16:19], a[152:155], v[240:243]\n\t" \
  "ds_read_b64_tr_b16 a[92:93], v119 offset:16384\n\t" \
  "v_mfma_f32_16x16x32_bf16 v[236:239], a[12:15], a[152:155], 0\n\t" \
  "ds_read_b64_tr_b16 a[94:95], v119 offset:17408\n\t" \
  "v_mfma_f32_16x16x32_bf16 v[236:239], a[44:47], a[156:159], v[236:239]\n\t" \
  "ds_read_b64_tr_b16 a[124:125], v119 offset:24576\n\t" \
  "buffer_load_dwordx4 v122, %[srdW], s50 offen offset:2048 lds\n\t" \
  "v_mfma_f32_16x16x32_bf16 v[232:235], a[40:43], a[156:159], 0\n\t" \
  "ds_read_b64_tr_b16 a[126:127], v119 offset:25600\n\t" \
  "v_mfma_f32_16x16x32_bf16 v[232:235], a[8:11], a[152:155], v[232:235]\n\t" \
  "v_mfma_f32_16x16x32_bf16 v[228:231], a[4:7], a[152:155], 0\n\t" \
  "v_mfma_f32_16x16x32_bf16 v[228:231], a[36:39], a[156:159], v[228:231]\n\t" \
  "buffer_load_dwordx4 v122, %[srdW], s51 offen offset:3072 lds\n\t" \
  "v_mfma_f32_16x16x32_bf16 v[224:227], a[32:35], a[156:159], 0\n\t" \
  "v_add_u32 %[vW0], s40, %[vW0]\n\t" \
  "v_mfma_f32_16x16x32_bf16 v[224:227], a[0:3], a[152:155], v[224:227]\n\t" \
  "v_add_u32 v122, s40, v122\n\t" \
  "s_waitcnt lgkmcnt(0)\n\t" \
  "1:\n\t" \
  "s_cbranch_vccnz 7f\n\t" \
  "8:\n\t" \
  "s_waitcnt vmcnt(20)\n\t" \
  "v_mfma_f32_16x16x32_bf16 v[128:131], a[64:67], a[160:163], v[128:131]\n\t" \
  "v_mfma_f32_16x16x32_bf16 v[128:131], a[96:99], a[164:167], v[128:131]\n\t" \
  "buffer_load_dwordx4 a[152:155], %[vA], %[srdG], s75 offen offset:0 sc1\n\t" \
  "v_mfma_f32_16x16x32_bf16 v[132:135], a[100:103], a[164:167], v[132:135]\n\t" \
  "v_mfma_f32_16x16x32_bf16 v[132:135], a[68:71], a[160:163], v[132:135]\n\t" \
  "v_mfma_f32_16x16x32_bf16 v[136:139], a[72:75], a[160:163], v[136:139]\n\t" \
  "v_mfma_f32_16x16x32_bf16 v[136:139], a[104:107], a[164:167], v[136:139]\n\t" \
  "buffer_load_dwordx4 a[156:159], %[vA], %[srdG], s75 offen offset:1024 sc1\n\t" \
  "v_mfma_f32_16x16x32_bf16 v[140:143], a[108:111], a[164:167], v[140:143]\n\t" \
  "v_mfma_f32_16x16x32_bf16 v[140:143], a[76:79], a[160:163], v[140:143]\n\t" \
  "v_add_u32 %[vA], 0x8000, %[vA]\n\t" \
  "v_mfma_f32_16x16x32_bf16 v[144:147], a[80:83], a[160:163], v[144:147]\n\t" \
  "v_xor_b32 v112, 0x8000, v112\n\t" \
  "v_mfma_f32_16x16x32_bf16 v[144:147], a[112:115], a[164:167], v[144:147]\n\t" \
  "v_mfma_f32_16x16x32_bf16 v[148:151], a[116:119], a[164:167], v[148:151]\n\t" \
  "v_xor_b32 v113, 0x8000, v113\n\t" \
  "v_mfma_f32_16x16x32_bf16 v[148:151], a[84:87], a[160:163], v[148:151]\n\t" \
  "v_mfma_f32_16x16x32_bf16 v[152:155], a[88:91], a[160:163], v[152:155]\n\t" \
  "v_xor_b32 v114, 0x8000, v114\n\t" \
  "v_mfma_f32_16x16x32_bf16 v[152:155], a[120:123], a[164:167], v[152:155]\n\t" \
  "v_mfma_f32_16x16x32_bf16 v[156:159], a[124:127], a[164:167], v[156:159]\n\t" \
  "v_xor_b32 v115, 0x8000, v115\n\t" \
  "v_mfma_f32_16x16x32_bf16 v[156:159], a[92:95], a[160:163], v[156:159]\n\t" \
  "s_waitcnt vmcnt(20)\n\t" \
  "v_mfma_f32_16x16x32_bf16 v[188:191], a[92:95], a[168:171], v[188:191]\n\t" \
  "v_xor_b32 v116, 0x8000, v116\n\t" \
  "v_mfma_f32_16x16x32_bf16 v[188:191], a[124:127], a[172:175], v[188:191]\n\t" \
  "buffer_load_dwordx4 a[160:163], %[vA], %[srdG], s72 offen offset:0 sc1\n\t" \
  "v_mfma_f32_16x16x32_bf16 v[184:187], a[120:123], a[172:175], v[184:187]\n\t" \
  "v_xor_b32 v117, 0x8000, v117\n\t" \
  "v_mfma_f32_16x16x32_bf16 v[184:187], a[88:91], a[168:171], v[184:187]\n\t" \
  "v_mfma_f32_16x16x32_bf16 v[180:183], a[84:87], a[168:171], v[180:183]\n\t" \
  "v_xor_b32 v118, 0x8000, v118\n\t" \
  "v_mfma_f32_16x16x32_bf16 v[180:183], a[116:119], a[172:175], v[180:183]\n\t" \
  "buffer_load_dwordx4 a[164:167], %[vA], %[srdG], s72 offen offset:1024 sc1\n\t" \
  "v_mfma_f32_16x16x32_bf16 v[176:179], a[112:115], a[172:175], v[176:179]\n\t" \
  "v_xor_b32 v119, 0x8000, v119\n\t" \
  "v_mfma_f32_16x16x32_bf16 v[176:179], a[80:83], a[168:171], v[176:179]\n\t" \
  "v_mfma_f32_16x16x32_bf16 v[172:175], a[76:79], a[168:171], v[172:175]\n\t" \
  "v_mfma_f32_16x16x32_bf16 v[172:175], a[108:111], a[172:175], v[172:175]\n\t" \
  "v_mfma_f32_16x16x32_bf16 v[168:171], a[104:107], a[172:175], v[168:171]\n\t" \
  "s_waitcnt vmcnt(28)\n\t" \
  "s_barrier\n\t" \
  "v_mfma_f32_16x16x32_bf16 v[168:171], a[72:75], a[168:171], v[168:171]\n\t" \
  "ds_read_b64_tr_b16 a[0:1], v112 offset:0\n\t" \
  "v_mfma_f32_16x16x32_bf16 v[164:167], a[68:71], a[168:171], v[164:167]\n\t" \
  "ds_read_b64_tr_b16 a[2:3], v112 offset:1024\n\t" \
  "v_mfma_f32_16x16x32_bf16 v[164:167], a[100:103], a[172:175], v[164:167]\n\t" \
  "ds_read_b64_tr_b16 a[32:33], v112 offset:8192\n\t" \
  "v_mfma_f32_16x16x32_bf16 v[160:163], a[96:99], a[172:175], v[160:163]\n\t" \
  "ds_read_b64_tr_b16 a[34:35], v112 offset:9216\n\t" \
  "v_mfma_f32_16x16x32_bf16 v[160:163], a[64:67], a[168:171], v[160:163]\n\t" \
  "ds_read_b64_tr_b16 a[4:5], v113 offset:0\n\t" \
  "s_waitcnt vmcnt(18)\n\t" \
  "v_mfma_f32_16x16x32_bf16 v[192:195], a[64:67], a[176:179], v[192:195]\n\t" \
  "ds_read_b64_tr_b16 a[6:7], v113 offset:1024\n\t" \
  "v_mfma_f32_16x16x32_bf16 v[192:195], a[96:99], a[180:183], v[192:195]\n\t" \
  "ds_read_b64_tr_b16 a[36:37], v113 offset:8192\n\t" \
  "buffer_load_dwordx4 a[168:171], %[vA], %[srdG], s73 offen offset:0 sc1\n\t" \
  "v_mfma_f32_16x16x32_bf16 v[196:199], a[100:103], a[180:183], v[196:199]\n\t" \
  "ds_read_b64_tr_b16 a[38:39], v113 offset:9216\n\t" \
  "v_mfma_f32_16x16x32_bf16 v[196:199], a[68:71], a[176:179], v[196:199]\n\t" \
  "ds_read_b64_tr_b16 a[8:9], v114 offset:0\n\t" \
  "v_mfma_f32_16x16x32_bf16 v[200:203], a[72:75], a[176:179], v[200:203]\n\t" \
  "ds_read_b64_tr_b16 a[10:11], v114 offset:1024\n\t" \
  "v_mfma_f32_16x16x32_bf16 v[200:203], a[104:107], a[180:183], v[200:203]\n\t" \
  "ds_read_b64_tr_b16 a[40:41], v114 offset:8192\n\t" \
  "buffer_load_dwordx4 a[172:175], %[vA], %[srdG], s73 offen offset:1024 sc1\n\t" \
  "v_mfma_f32_16x16x32_bf16 v[204:207], a[108:111], a[180:183], v[204:207]\n\t" \
  "ds_read_b64_tr_b16 a[42:43], v114 offset:9216\n\t" \
  "v_mfma_f32_16x16x32_bf16 v[204:207], a[76:79], a[176:179], v[204:207]\n\t" \
  "ds_read_b64_tr_b16 a[12:13], v115 offset:0\n\t" \
  "v_mfma_f32_16x16x32_bf16 v[208:211], a[80:83], a[176:179], v[208:211]\n\t" \
  "ds_read_b64_tr_b16 a[14:15], v115 offset:1024\n\t" \
  "s_add_u32 m0, s52, 16384\n\t" \
  "v_mfma_f32_16x16x32_bf16 v[208:211], a[112:115], a[180:183], v[208:211]\n\t" \
  "ds_read_b64_tr_b16 a[44:45], v115 offset:8192\n\t" \
  "buffer_load_dwordx4 %[vW0], %[srdW], 0 offen offset:0 lds\n\t" \
  "v_mfma_f32_16x16x32_bf16 v[212:215], a[116:119], a[180:183], v[212:215]\n\t" \
  "ds_read_b64_tr_b16 a[46:47], v115 offset:9216\n\t" \
  "s_xor_b32 s52, s52, 0x8000\n\t" \
  "v_mfma_f32_16x16x32_bf16 v[212:215], a[84:87], a[176:179], v[212:215]\n\t" \
  "ds_read_b64_tr_b16 a[16:17], v116 offset:0\n\t" \
  "v_mfma_f32_16x16x32_bf16 v[216:219], a[88:91], a[176:179], v[216:219]\n\t" \
  "ds_read_b64_tr_b16 a[18:19], v116 offset:1024\n\t" \
  "v_mfma_f32_16x16x32_bf16 v[216:219], a[120:123], a[180:183], v[216:219]\n\t" \
  "ds_read_b64_tr_b16 a[48:49], v116 offset:8192\n\t" \
  "buffer_load_dwordx4 %[vW0], %[srdW], s49 offen offset:1024 lds\n\t" \
  "v_mfma_f32_16x16x32_bf16 v[220:223], a[124:127], a[180:183], v[220:223]\n\t" \
  "ds_read_b64_tr_b16 a[50:51], v116 offset:9216\n\t" \
  "v_mfma_f32_16x16x32_bf16 v[220:223], a[92:95], a[176:179], v[220:223]\n\t" \
  "ds_read_b64_tr_b16 a[20:21], v117 offset:0\n\t" \
  "s_waitcnt vmcnt(18)\n\t" \
  "v_mfma_f32_16x16x32_bf16 v[252:255], a[92:95], a[184:187], v[252:255]\n\t" \
  "ds_read_b64_tr_b16 a[22:23], v117 offset:1024\n\t" \
  "v_mfma_f32_16x16x32_bf16 v[252:255], a[124:127], a[188:191], v[252:255]\n\t" \
  "ds_read_b64_tr_b16 a[52:53], v117 offset:8192\n\t" \
  "buffer_load_dwordx4 a[176:179], %[vA], %[srdG], s74 offen offset:0 sc1\n\t" \
  "v_mfma_f32_16x16x32_bf16 v[248:251], a[120:123], a[188:191], v[248:251]\n\t" \
  "ds_read_b64_tr_b16 a[54:55], v117 offset:9216\n\t" \
  "v_mfma_f32_16x16x32_bf16 v[248:251], a[88:91], a[184:187], v[248:251]\n\t" \
  "ds_read_b64_tr_b16 a[24:25], v118 offset:0\n\t" \
  "v_mfma_f32_16x16x32_bf16 v[244:247], a[84:87], a[184:187], v[244:247]\n\t" \
  "ds_read_b64_tr_b16 a[26:27], v118 offset:1024\n\t" \
  "v_mfma_f32_16x16x32_bf16 v[244:247], a[116:119], a[188:191], v[244:247]\n\t" \
  "ds_read_b64_tr_b16 a[56:57], v118 offset:8192\n\t" \
  "buffer_load_dwordx4 a[180:183], %[vA], %[srdG], s74 offen offset:1024 sc1\n\t" \
  "v_mfma_f32_16x16x32_bf16 v[240:243], a[112:115], a[188:191], v[240:243]\n\t" \
  "ds_read_b64_tr_b16 a[58:59], v118 offset:9216\n\t" \
  "v_mfma_f32_16x16x32_bf16 v[240:243], a[80:83], a[184:187], v[240:243]\n\t" \
  "ds_read_b64_tr_b16 a[28:29], v119 offset:0\n\t" \
  "v_mfma_f32_16x16x32_bf16 v[236:239], a[76:79], a[184:187], v[236:239]\n\t" \
  "ds_read_b64_tr_b16 a[30:31], v119 offset:1024\n\t" \
  "v_mfma_f32_16x16x32_bf16 v[236:239], a[108:111], a[188:191], v[236:239]\n\t" \
  "ds_read_b64_tr_b16 a[60:61], v119 offset:8192\n\t" \
  "buffer_load_dwordx4 v122, %[srdW], s50 offen offset:2048 lds\n\t" \
  "v_mfma_f32_16x16x32_bf16 v[232:235], a[104:107], a[188:191], v[232:235]\n\t" \
  "ds_read_b64_tr_b16 a[62:63], v119 offset:9216\n\t" \
  "v_mfma_f32_16x16x32_bf16 v[232:235], a[72:75], a[184:187], v[232:235]\n\t" \
  "v_mfma_f32_16x16x32_bf16 v[228:231], a[68:71], a[184:187], v[228:231]\n\t" \
  "v_mfma_f32_16x16x32_bf16 v[228:231], a[100:103], a[188:191], v[228:231]\n\t" \
  "buffer_load_dwordx4 v122, %[srdW], s51 offen offset:3072 lds\n\t" \
  "v_mfma_f32_16x16x32_bf16 v[224:227], a[96:99], a[188:191], v[224:227]\n\t" \
  "v_add_u32 %[vW0], s40, %[vW0]\n\t" \
  "v_mfma_f32_16x16x32_bf16 v[224:227], a[64:67], a[184:187], v[224:227]\n\t" \
  "v_add_u32 v122, s40, v122\n\t" \
  "s_waitcnt lgkmcnt(0)\n\t" \
  "s_waitcnt vmcnt(20)\n\t" \
  "v_mfma_f32_16x16x32_bf16 v[128:131], a[0:3], a[128:131], v[128:131]\n\t" \
  "v_mfma_f32_16x16x32_bf16 v[128:131], a[32:35], a[132:135], v[128:131]\n\t" \
  "buffer_load_dwordx4 a[184:187], %[vA], %[srdG], s75 offen offset:0 sc1\n\t" \
  "v_mfma_f32_16x16x32_bf16 v[132:135], a[36:39], a[132:135], v[132:135]\n\t" \
  "v_mfma_f32_16x16x32_bf16 v[132:135], a[4:7], a[128:131], v[132:135]\n\t" \
  "v_mfma_f32_16x16x32_bf16 v[136:139], a[8:11], a[128:131], v[136:139]\n\t" \
  "v_mfma_f32_16x16x32_bf16 v[136:139], a[40:43], a[132:135], v[136:139]\n\t" \
  "buffer_load_dwordx4 a[188:191], %[vA], %[srdG], s75 offen offset:1024 sc1\n\t" \
  "v_mfma_f32_16x16x32_bf16 v[140:143], a[44:47], a[132:135], v[140:143]\n\t" \
  "v_mfma_f32_16x16x32_bf16 v[140:143], a[12:15], a[128:131], v[140:143]\n\t" \
  "v_add_u32 %[vA], 0x8000, %[vA]\n\t" \
  "v_mfma_f32_16x16x32_bf16 v[144:147], a[16:19], a[128:131], v[144:147]\n\t" \
  "v_mfma_f32_16x16x32_bf16 v[144:147], a[48:51], a[132:135], v[144:147]\n\t" \
  "v_mfma_f32_16x16x32_bf16 v[148:151], a[52:55], a[132:135], v[148:151]\n\t" \
  "s_sub_u32 %[cnt], %[cnt], 2\n\t" \
  "v_mfma_f32_16x16x32_bf16 v[148:151], a[20:23], a[128:131], v[148:151]\n\t" \
  "v_mfma_f32_16x16x32_bf16 v[152:155], a[24:27], a[128:131], v[152:155]\n\t" \
  "s_cmp_eq_u32 %[cnt], %[pc]\n\t" \
  "s_cselect_b64 vcc, -1, 0\n\t" \
  "v_mfma_f32_16x16x32_bf16 v[152:155], a[56:59], a[132:135], v[152:155]\n\t" \
  "v_mfma_f32_16x16x32_bf16 v[156:159], a[60:63], a[132:135], v[156:159]\n\t" \
  "v_mfma_f32_16x16x32_bf16 v[156:159], a[28:31], a[128:131], v[156:159]\n\t" \
  "s_waitcnt vmcnt(20)\n\t" \
  "v_mfma_f32_16x16x32_bf16 v[188:191], a[28:31], a[136:139], v[188:191]\n\t" \
  "v_mfma_f32_16x16x32_bf16 v[188:191], a[60:63], a[140:143], v[188:191]\n\t" \
  "buffer_load_dwordx4 a[128:131], %[vA], %[srdG], s72 offen offset:0 sc1\n\t" \
  "v_mfma_f32_16x16x32_bf16 v[184:187], a[56:59], a[140:143], v[184:187]\n\t" \
  "v_mfma_f32_16x16x32_bf16 v[184:187], a[24:27], a[136:139], v[184:187]\n\t" \
  "v_mfma_f32_16x16x32_bf16 v[180:183], a[20:23], a[136:139], v[180:183]\n\t" \
  "v_mfma_f32_16x16x32_bf16 v[180:183], a[52:55], a[140:143], v[180:183]\n\t" \
  "buffer_load_dwordx4 a[132:135], %[vA], %[srdG], s72 offen offset:1024 sc1\n\t" \
  "v_mfma_f32_16x16x32_bf16 v[176:179], a[48:51], a[140:143], v[176:179]\n\t" \
  "v_mfma_f32_16x16x32_bf16 v[176:179], a[16:19], a[136:139], v[176:179]\n\t" \
  "v_mfma_f32_16x16x32_bf16 v[172:175], a[12:15], a[136:139], v[172:175]\n\t" \
  "v_mfma_f32_16x16x32_bf16 v[172:175], a[44:47], a[140:143], v[172:175]\n\t" \
  "v_mfma_f32_16x16x32_bf16 v[168:171], a[40:43], a[140:143], v[168:171]\n\t" \
  "s_waitcnt vmcnt(28)\n\t" \
  "s_barrier\n\t" \
  "v_mfma_f32_16x16x32_bf16 v[168:171], a[8:11], a[136:139], v[168:171]\n\t" \
  "ds_read_b64_tr_b16 a[64:65], v112 offset:16384\n\t" \
  "v_mfma_f32_16x16x32_bf16 v[164:167], a[4:7], a[136:139], v[164:167]\n\t" \
  "ds_read_b64_tr_b16 a[66:67], v112 offset:17408\n\t" \
  "v_mfma_f32_16x16x32_bf16 v[164:167], a[36:39], a[140:143], v[164:167]\n\t" \
  "ds_read_b64_tr_b16 a[96:97], v112 offset:24576\n\t" \
  "v_mfma_f32_16x16x32_bf16 v[160:163], a[32:35], a[140:143], v[160:163]\n\t" \
  "ds_read_b64_tr_b16 a[98:99], v112 offset:25600\n\t" \
  "v_mfma_f32_16x16x32_bf16 v[160:163], a[0:3], a[136:139], v[160:163]\n\t" \
  "ds_read_b64_tr_b16 a[68:69], v113 offset:16384\n\t" \
  "s_waitcnt vmcnt(18)\n\t" \
  "v_mfma_f32_16x16x32_bf16 v[192:195], a[0:3], a[144:147], v[192:195]\n\t" \
  "ds_read_b64_tr_b16 a[70:71], v113 offset:17408\n\t" \
  "v_mfma_f32_16x16x32_bf16 v[192:195], a[32:35], a[148:151], v[192:195]\n\t" \
  "ds_read_b64_tr_b16 a[100:101], v113 offset:24576\n\t" \
  "buffer_load_dwordx4 a[136:139], %[vA], %[srdG], s73 offen offset:0 sc1\n\t" \
  "v_mfma_f32_16x16x32_bf16 v[196:199], a[36:39], a[148:151], v[196:199]\n\t" \
  "ds_read_b64_tr_b16 a[102:103], v113 offset:25600\n\t" \
  "v_mfma_f32_16x16x32_bf16 v[196:199], a[4:7], a[144:147], v[196:199]\n\t" \
  "ds_read_b64_tr_b16 a[72:73], v114 offset:16384\n\t" \
  "v_mfma_f32_16x16x32_bf16 v[200:203], a[8:11], a[144:147], v[200:203]\n\t" \
  "ds_read_b64_tr_b16 a[74:75], v114 offset:17408\n\t" \
  "v_mfma_f32_16x16x32_bf16 v[200:203], a[40:43], a[148:151], v[200:203]\n\t" \
  "ds_read_b64_tr_b16 a[104:105], v114 offset:24576\n\t" \
  "buffer_load_dwordx4 a[140:143], %[vA], %[srdG], s73 offen offset:1024 sc1\n\t" \
  "v_mfma_f32_16x16x32_bf16 v[204:207], a[44:47], a[148:151], v[204:207]\n\t" \
  "ds_read_b64_tr_b16 a[106:107], v114 offset:25600\n\t" \
  "v_mfma_f32_16x16x32_bf16 v[204:207], a[12:15], a[144:147], v[204:207]\n\t" \
  "ds_read_b64_tr_b16 a[76:77], v115 offset:16384\n\t" \
  "v_mfma_f32_16x16x32_bf16 v[208:211], a[16:19], a[144:147], v[208:211]\n\t" \
  "ds_read_b64_tr_b16 a[78:79], v115 offset:17408\n\t" \
  "s_add_u32 m0, s52, 0\n\t" \
  "v_mfma_f32_16x16x32_bf16 v[208:211], a[48:51], a[148:151], v[208:211]\n\t" \
  "ds_read_b64_tr_b16 a[108:109], v115 offset:24576\n\t" \
  "buffer_load_dwordx4 %[vW0], %[srdW], 0 offen offset:0 lds\n\t" \
  "v_mfma_f32_16x16x32_bf16 v[212:215], a[52:55], a[148:151], v[212:215]\n\t" \
  "ds_read_b64_tr_b16 a[110:111], v115 offset:25600\n\t" \
  "v_mfma_f32_16x16x32_bf16 v[212:215], a[20:23], a[144:147], v[212:215]\n\t" \
  "ds_read_b64_tr_b16 a[80:81], v116 offset:16384\n\t" \
  "v_mfma_f32_16x16x32_bf16 v[216:219], a[24:27], a[144:147], v[216:219]\n\t" \
  "ds_read_b64_tr_b16 a[82:83], v116 offset:17408\n\t" \
  "v_mfma_f32_16x16x32_bf16 v[216:219], a[56:59], a[148:151], v[216:219]\n\t" \
  "ds_read_b64_tr_b16 a[112:113], v116 offset:24576\n\t" \
  "buffer_load_dwordx4 %[vW0], %[srdW], s49 offen offset:1024 lds\n\t" \
  "v_mfma_f32_16x16x32_bf16 v[220:223], a[60:63], a[148:151], v[220:223]\n\t" \
  "ds_read_b64_tr_b16 a[114:115], v116 offset:25600\n\t" \
  "v_mfma_f32_16x16x32_bf16 v[220:223], a[28:31], a[144:147], v[220:223]\n\t" \
  "ds_read_b64_tr_b16 a[84:85], v117 offset:16384\n\t" \
  "s_waitcnt vmcnt(18)\n\t" \
  "v_mfma_f32_16x16x32_bf16 v[252:255], a[28:31], a[152:155], v[252:255]\n\t" \
  "ds_read_b64_tr_b16 a[86:87], v117 offset:17408\n\t" \
  "v_mfma_f32_16x16x32_bf16 v[252:255], a[60:63], a[156:159], v[252:255]\n\t" \
  "ds_read_b64_tr_b16 a[116:117], v117 offset:24576\n\t" \
  "buffer_load_dwordx4 a[144:147], %[vA], %[srdG], s74 offen offset:0 sc1\n\t" \
  "v_mfma_f32_16x16x32_bf16 v[248:251], a[56:59], a[156:159], v[248:251]\n\t" \
  "ds_read_b64_tr_b16 a[118:119], v117 offset:25600\n\t" \
  "v_mfma_f32_16x16x32_bf16 v[248:251], a[24:27], a[152:155], v[248:251]\n\t" \
  "ds_read_b64_tr_b16 a[88:89], v118 offset:16384\n\t" \
  "v_mfma_f32_16x16x32_bf16 v[244:247], a[20:23], a[152:155], v[244:247]\n\t" \
  "ds_read_b64_tr_b16 a[90:91], v118 offset:17408\n\t" \
  "v_mfma_f32_16x16x32_bf16 v[244:247], a[52:55], a[156:159], v[244:247]\n\t" \
  "ds_read_b64_tr_b16 a[120:121], v118 offset:24576\n\t" \
  "buffer_load_dwordx4 a[148:151], %[vA], %[srdG], s74 offen offset:1024 sc1\n\t" \
  "v_mfma_f32_16x16x32_bf16 v[240:243], a[48:51], a[156:159], v[240:243]\n\t" \
  "ds_read_b64_tr_b16 a[122:123], v118 offset:25600\n\t" \
  "v_mfma_f32_16x16x32_bf16 v[240:243], a[16:19], a[152:155], v[240:243]\n\t" \
  "ds_read_b64_tr_b16 a[92:93], v119 offset:16384\n\t" \
  "v_mfma_f32_16x16x32_bf16 v[236:239], a[12:15], a[152:155], v[236:239]\n\t" \
  "ds_read_b64_tr_b16 a[94:95], v119 offset:17408\n\t" \
  "v_mfma_f32_16x16x32_bf16 v[236:239], a[44:47], a[156:159], v[236:239]\n\t" \
  "ds_read_b64_tr_b16 a[124:125], v119 offset:24576\n\t" \
  "buffer_load_dwordx4 v122, %[srdW], s50 offen offset:2048 lds\n\t" \
  "v_mfma_f32_16x16x32_bf16 v[232:235], a[40:43], a[156:159], v[232:235]\n\t" \
  "ds_read_b64_tr_b16 a[126:127], v119 offset:25600\n\t" \
  "v_mfma_f32_16x16x32_bf16 v[232:235], a[8:11], a[152:155], v[232:235]\n\t" \
  "v_mfma_f32_16x16x32_bf16 v[228:231], a[4:7], a[152:155], v[228:231]\n\t" \
  "s_cmp_lg_u32 %[cnt], 4\n\t" \
  "v_mfma_f32_16x16x32_bf16 v[228:231], a[36:39], a[156:159], v[228:231]\n\t" \
  "buffer_load_dwordx4 v122, %[srdW], s51 offen offset:3072 lds\n\t" \
  "v_mfma_f32_16x16x32_bf16 v[224:227], a[32:35], a[156:159], v[224:227]\n\t" \
  "v_add_u32 %[vW0], s40, %[vW0]\n\t" \
  "v_mfma_f32_16x16x32_bf16 v[224:227], a[0:3], a[152:155], v[224:227]\n\t" \
  "v_add_u32 v122, s40, v122\n\t" \
  "s_waitcnt lgkmcnt(0)\n\t" \
  "s_cbranch_scc1 1b\n\t" \
  "s_waitcnt vmcnt(20)\n\t" \
  "v_mfma_f32_16x16x32_bf16 v[128:131], a[64:67], a[160:163], v[128:131]\n\t" \
  "v_mfma_f32_16x16x32_bf16 v[128:131], a[96:99], a[164:167], v[128:131]\n\t" \
  "buffer_load_dwordx4 a[152:155], %[vA], %[srdG], s75 offen offset:0 sc1\n\t" \
  "v_mfma_f32_16x16x32_bf16 v[132:135], a[100:103], a[164:167], v[132:135]\n\t" \
  "v_mfma_f32_16x16x32_bf16 v[132:135], a[68:71], a[160:163], v[132:135]\n\t" \
  "v_mfma_f32_16x16x32_bf16 v[136:139], a[72:75], a[160:163], v[136:139]\n\t" \
  "v_mfma_f32_16x16x32_bf16 v[136:139], a[104:107], a[164:167], v[136:139]\n\t" \
  "buffer_load_dwordx4 a[156:159], %[vA], %[srdG], s75 offen offset:1024 sc1\n\t" \
  "v_mfma_f32_16x16x32_bf16 v[140:143], a[108:111], a[164:167], v[140:143]\n\t" \
  "v_mfma_f32_16x16x32_bf16 v[140:143], a[76:79], a[160:163], v[140:143]\n\t" \
  "v_add_u32 %[vA], 0x8000, %[vA]\n\t" \
  "v_mfma_f32_16x16x32_bf16 v[144:147], a[80:83], a[160:163], v[144:147]\n\t" \
  "v_xor_b32 v112, 0x8000, v112\n\t" \
  "v_mfma_f32_16x16x32_bf16 v[144:147], a[112:115], a[164:167], v[144:147]\n\t" \
  "v_mfma_f32_16x16x32_bf16 v[148:151], a[116:119], a[164:167], v[148:151]\n\t" \
  "v_xor_b32 v113, 0x8000, v113\n\t" \
  "v_mfma_f32_16x16x32_bf16 v[148:151], a[84:87], a[160:163], v[148:151]\n\t" \
  "v_mfma_f32_16x16x32_bf16 v[152:155], a[88:91], a[160:163], v[152:155]\n\t" \
  "v_xor_b32 v114, 0x8000, v114\n\t" \
  "v_mfma_f32_16x16x32_bf16 v[152:155], a[120:123], a[164:167], v[152:155]\n\t" \
  "v_mfma_f32_16x16x32_bf16 v[156:159], a[124:127], a[164:167], v[156:159]\n\t" \
  "v_xor_b32 v115, 0x8000, v115\n\t" \
  "v_mfma_f32_16x16x32_bf16 v[156:159], a[92:95], a[160:163], v[156:159]\n\t" \
  "s_waitcnt vmcnt(20)\n\t" \
  "v_mfma_f32_16x16x32_bf16 v[188:191], a[92:95], a[168:171], v[188:191]\n\t" \
  "v_xor_b32 v116, 0x8000, v116\n\t" \
  "v_mfma_f32_16x16x32_bf16 v[188:191], a[124:127], a[172:175], v[188:191]\n\t" \
  "buffer_load_dwordx4 a[160:163], %[vA], %[srdG], s72 offen offset:0 sc1\n\t" \
  "v_mfma_f32_16x16x32_bf16 v[184:187], a[120:123], a[172:175], v[184:187]\n\t" \
  "v_xor_b32 v117, 0x8000, v117\n\t" \
  "v_mfma_f32_16x16x32_bf16 v[184:187], a[88:91], a[168:171], v[184:187]\n\t" \
  "v_mfma_f32_16x16x32_bf16 v[180:183], a[84:87], a[168:171], v[180:183]\n\t" \
  "v_xor_b32 v118, 0x8000, v118\n\t" \
  "v_mfma_f32_16x16x32_bf16 v[180:183], a[116:119], a[172:175], v[180:183]\n\t" \
  "buffer_load_dwordx4 a[164:167], %[vA], %[srdG], s72 offen offset:1024 sc1\n\t" \
  "v_mfma_f32_16x16x32_bf16 v[176:179], a[112:115], a[172:175], v[176:179]\n\t" \
  "v_xor_b32 v119, 0x8000, v119\n\t" \
  "v_mfma_f32_16x16x32_bf16 v[176:179], a[80:83], a[168:171], v[176:179]\n\t" \
  "v_mfma_f32_16x16x32_bf16 v[172:175], a[76:79], a[168:171], v[172:175]\n\t" \
  "v_mfma_f32_16x16x32_bf16 v[172:175], a[108:111], a[172:175], v[172:175]\n\t" \
  "v_mfma_f32_16x16x32_bf16 v[168:171], a[104:107], a[172:175], v[168:171]\n\t" \
  "s_waitcnt vmcnt(28)\n\t" \
  "s_barrier\n\t" \
  "v_mfma_f32_16x16x32_bf16 v[168:171], a[72:75], a[168:171], v[168:171]\n\t" \
  "ds_read_b64_tr_b16 a[0:1], v112 offset:0\n\t" \
  "v_mfma_f32_16x16x32_bf16 v[164:167], a[68:71], a[168:171], v[164:167]\n\t" \
  "ds_read_b64_tr_b16 a[2:3], v112 offset:1024\n\t" \
  "v_mfma_f32_16x16x32_bf16 v[164:167], a[100:103], a[172:175], v[164:167]\n\t" \
  "ds_read_b64_tr_b16 a[32:33], v112 offset:8192\n\t" \
  "v_mfma_f32_16x16x32_bf16 v[160:163], a[96:99], a[172:175], v[160:163]\n\t" \
  "ds_read_b64_tr_b16 a[34:35], v112 offset:9216\n\t" \
  "v_mfma_f32_16x16x32_bf16 v[160:163], a[64:67], a[168:171], v[160:163]\n\t" \
  "ds_read_b64_tr_b16 a[4:5], v113 offset:0\n\t" \
  "s_waitcnt vmcnt(18)\n\t" \
  "v_mfma_f32_16x16x32_bf16 v[192:195], a[64:67], a[176:179], v[192:195]\n\t" \
  "ds_read_b64_tr_b16 a[6:7], v113 offset:1024\n\t" \
  "v_mfma_f32_16x16x32_bf16 v[192:195], a[96:99], a[180:183], v[192:195]\n\t" \
  "ds_read_b64_tr_b16 a[36:37], v113 offset:8192\n\t" \
  "buffer_load_dwordx4 a[168:171], %[vA], %[srdG], s73 offen offset:0 sc1\n\t" \
  "v_mfma_f32_16x16x32_bf16 v[196:199], a[100:103], a[180:183], v[196:199]\n\t" \
  "ds_read_b64_tr_b16 a[38:39], v113 offset:9216\n\t" \
  "v_mfma_f32_16x16x32_bf16 v[196:199], a[68:71], a[176:179], v[196:199]\n\t" \
  "ds_read_b64_tr_b16 a[8:9], v114 offset:0\n\t" \
  "v_mfma_f32_16x16x32_bf16 v[200:203], a[72:75], a[176:179], v[200:203]\n\t" \
  "ds_read_b64_tr_b16 a[10:11], v114 offset:1024\n\t" \
  "v_mfma_f32_16x16x32_bf16 v[200:203], a[104:107], a[180:183], v[200:203]\n\t" \
  "ds_read_b64_tr_b16 a[40:41], v114 offset:8192\n\t" \
  "buffer_load_dwordx4 a[172:175], %[vA], %[srdG], s73 offen offset:1024 sc1\n\t" \
  "v_mfma_f32_16x16x32_bf16 v[204:207], a[108:111], a[180:183], v[204:207]\n\t" \
  "ds_read_b64_tr_b16 a[42:43], v114 offset:9216\n\t" \
  "v_mfma_f32_16x16x32_bf16 v[204:207], a[76:79], a[176:179], v[204:207]\n\t" \
  "ds_read_b64_tr_b16 a[12:13], v115 offset:0\n\t" \
  "v_mfma_f32_16x16x32_bf16 v[208:211], a[80:83], a[176:179], v[208:211]\n\t" \
  "ds_read_b64_tr_b16 a[14:15], v115 offset:1024\n\t" \
  "v_mfma_f32_16x16x32_bf16 v[208:211], a[112:115], a[180:183], v[208:211]\n\t" \
  "ds_read_b64_tr_b16 a[44:45], v115 offset:8192\n\t" \
  "v_mfma_f32_16x16x32_bf16 v[212:215], a[116:119], a[180:183], v[212:215]\n\t" \
  "ds_read_b64_tr_b16 a[46:47], v115 offset:9216\n\t" \
  "v_mfma_f32_16x16x32_bf16 v[212:215], a[84:87], a[176:179], v[212:215]\n\t" \
  "ds_read_b64_tr_b16 a[16:17], v116 offset:0\n\t" \
  "v_mfma_f32_16x16x32_bf16 v[216:219], a[88:91], a[176:179], v[216:219]\n\t" \
  "ds_read_b64_tr_b16 a[18:19], v116 offset:1024\n\t" \
  "v_mfma_f32_16x16x32_bf16 v[216:219], a[120:123], a[180:183], v[216:219]\n\t" \
  "ds_read_b64_tr_b16 a[48:49], v116 offset:8192\n\t" \
  "v_mfma_f32_16x16x32_bf16 v[220:223], a[124:127], a[180:183], v[220:223]\n\t" \
  "ds_read_b64_tr_b16 a[50:51], v116 offset:9216\n\t" \
  "v_mfma_f32_16x16x32_bf16 v[220:223], a[92:95], a[176:179], v[220:223]\n\t" \
  "ds_read_b64_tr_b16 a[20:21], v117 offset:0\n\t" \
  "s_waitcnt vmcnt(16)\n\t" \
  "v_mfma_f32_16x16x32_bf16 v[252:255], a[92:95], a[184:187], v[252:255]\n\t" \
  "ds_read_b64_tr_b16 a[22:23], v117 offset:1024\n\t" \
  "s_branch 99f\n\t" \
  ".p2align 12\n\t" \
  "att_o1:\n\t" \
  "s_setpc_b64 s[68:69]\n\t" \
  "99:\n\t" \
  "v_mfma_f32_16x16x32_bf16 v[252:255], a[124:127], a[188:191], v[252:255]\n\t" \
  "ds_read_b64_tr_b16 a[52:53], v117 offset:8192\n\t" \
  "buffer_load_dwordx4 a[176:179], %[vA], %[srdG], s74 offen offset:0 sc1\n\t" \
  "v_mfma_f32_16x16x32_bf16 v[248:251], a[120:123], a[188:191], v[248:251]\n\t" \
  "ds_read_b64_tr_b16 a[54:55], v117 offset:9216\n\t" \
  "v_mfma_f32_16x16x32_bf16 v[248:251], a[88:91], a[184:187], v[248:251]\n\t" \
  "ds_read_b64_tr_b16 a[24:25], v118 offset:0\n\t" \
  "v_mfma_f32_16x16x32_bf16 v[244:247], a[84:87], a[184:187], v[244:247]\n\t" \
  "ds_read_b64_tr_b16 a[26:27], v118 offset:1024\n\t" \
  "v_mfma_f32_16x16x32_bf16 v[244:247], a[116:119], a[188:191], v[244:247]\n\t" \
  "ds_read_b64_tr_b16 a[56:57], v118 offset:8192\n\t" \
  "buffer_load_dwordx4 a[180:183], %[vA], %[srdG], s74 offen offset:1024 sc1\n\t" \
  "v_mfma_f32_16x16x32_bf16 v[240:243], a[112:115], a[188:191], v[240:243]\n\t" \
  "ds_read_b64_tr_b16 a[58:59], v118 offset:9216\n\t" \
  "v_mfma_f32_16x16x32_bf16 v[240:243], a[80:83], a[184:187], v[240:243]\n\t" \
  "ds_read_b64_tr_b16 a[28:29], v119 offset:0\n\t" \
  "v_mfma_f32_16x16x32_bf16 v[236:239], a[76:79], a[184:187], v[236:239]\n\t" \
  "ds_read_b64_tr_b16 a[30:31], v119 offset:1024\n\t" \
  "v_mfma_f32_16x16x32_bf16 v[236:239], a[108:111], a[188:191], v[236:239]\n\t" \
  "ds_read_b64_tr_b16 a[60:61], v119 offset:8192\n\t" \
  "v_mfma_f32_16x16x32_bf16 v[232:235], a[104:107], a[188:191], v[232:235]\n\t" \
  "ds_read_b64_tr_b16 a[62:63], v119 offset:9216\n\t" \
  "v_mfma_f32_16x16x32_bf16 v[232:235], a[72:75], a[184:187], v[232:235]\n\t" \
  "v_mfma_f32_16x16x32_bf16 v[228:231], a[68:71], a[184:187], v[228:231]\n\t" \
  "v_mfma_f32_16x16x32_bf16 v[228:231], a[100:103], a[188:191], v[228:231]\n\t" \
  "v_mfma_f32_16x16x32_bf16 v[224:227], a[96:99], a[188:191], v[224:227]\n\t" \
  "v_mfma_f32_16x16x32_bf16 v[224:227], a[64:67], a[184:187], v[224:227]\n\t" \
  "s_waitcnt lgkmcnt(0)\n\t" \
  "s_waitcnt vmcnt(16)\n\t" \
  "v_mfma_f32_16x16x32_bf16 v[128:131], a[0:3], a[128:131], v[128:131]\n\t" \
  "v_mfma_f32_16x16x32_bf16 v[128:131], a[32:35], a[132:135], v[128:131]\n\t" \
  "buffer_load_dwordx4 a[184:187], %[vA], %[srdG], s75 offen offset:0 sc1\n\t" \
  "v_mfma_f32_16x16x32_bf16 v[132:135], a[36:39], a[132:135], v[132:135]\n\t" \
  "v_mfma_f32_16x16x32_bf16 v[132:135], a[4:7], a[128:131], v[132:135]\n\t" \
  "v_mfma_f32_16x16x32_bf16 v[136:139], a[8:11], a[128:131], v[136:139]\n\t" \
  "v_mfma_f32_16x16x32_bf16 v[136:139], a[40:43], a[132:135], v[136:139]\n\t" \
  "buffer_load_dwordx4 a[188:191], %[vA], %[srdG], s75 offen offset:1024 sc1\n\t" \
  "v_mfma_f32_16x16x32_bf16 v[140:143], a[44:47], a[132:135], v[140:143]\n\t" \
  "v_mfma_f32_16x16x32_bf16 v[140:143], a[12:15], a[128:131], v[140:143]\n\t" \
  "v_mfma_f32_16x16x32_bf16 v[144:147], a[16:19], a[128:131], v[144:147]\n\t" \
  "v_mfma_f32_16x16x32_bf16 v[144:147], a[48:51], a[132:135], v[144:147]\n\t" \
  "v_mfma_f32_16x16x32_bf16 v[148:151], a[52:55], a[132:135], v[148:151]\n\t" \
  "s_waitcnt vmcnt(22)\n\t" \
  "s_barrier\n\t" \
  "v_mfma_f32_16x16x32_bf16 v[148:151], a[20:23], a[128:131], v[148:151]\n\t" \
  "ds_read_b64_tr_b16 a[64:65], v112 offset:16384\n\t" \
  "v_mfma_f32_16x16x32_bf16 v[152:155], a[24:27], a[128:131], v[152:155]\n\t" \
  "ds_read_b64_tr_b16 a[66:67], v112 offset:17408\n\t" \
  "v_mfma_f32_16x16x32_bf16 v[152:155], a[56:59], a[132:135], v[152:155]\n\t" \
  "ds_read_b64_tr_b16 a[96:97], v112 offset:24576\n\t" \
  "v_mfma_f32_16x16x32_bf16 v[156:159], a[60:63], a[132:135], v[156:159]\n\t" \
  "ds_read_b64_tr_b16 a[98:99], v112 offset:25600\n\t" \
  "v_mfma_f32_16x16x32_bf16 v[156:159], a[28:31], a[128:131], v[156:159]\n\t" \
  "ds_read_b64_tr_b16 a[68:69], v113 offset:16384\n\t" \
  "s_waitcnt vmcnt(16)\n\t" \
  "v_mfma_f32_16x16x32_bf16 v[188:191], a[28:31], a[136:139], v[188:191]\n\t" \
  "ds_read_b64_tr_b16 a[70:71], v113 offset:17408\n\t" \
  "v_mfma_f32_16x16x32_bf16 v[188:191], a[60:63], a[140:143], v[188:191]\n\t" \
  "ds_read_b64_tr_b16 a[100:101], v113 offset:24576\n\t" \
  "v_mfma_f32_16x16x32_bf16 v[184:187], a[56:59], a[140:143], v[184:187]\n\t" \
  "ds_read_b64_tr_b16 a[102:103], v113 offset:25600\n\t" \
  "v_mfma_f32_16x16x32_bf16 v[184:187], a[24:27], a[136:139], v[184:187]\n\t" \
  "ds_read_b64_tr_b16 a[72:73], v114 offset:16384\n\t" \
  "v_mfma_f32_16x16x32_bf16 v[180:183], a[20:23], a[136:139], v[180:183]\n\t" \
  "ds_read_b64_tr_b16 a[74:75], v114 offset:17408\n\t" \
  "v_mfma_f32_16x16x32_bf16 v[180:183], a[52:55], a[140:143], v[180:183]\n\t" \
  "ds_read_b64_tr_b16 a[104:105], v114 offset:24576\n\t" \
  "v_mfma_f32_16x16x32_bf16 v[176:179], a[48:51], a[140:143], v[176:179]\n\t" \
  "ds_read_b64_tr_b16 a[106:107], v114 offset:25600\n\t" \
  "v_mfma_f32_16x16x32_bf16 v[176:179], a[16:19], a[136:139], v[176:179]\n\t" \
  "ds_read_b64_tr_b16 a[76:77], v115 offset:16384\n\t" \
  "v_mfma_f32_16x16x32_bf16 v[172:175], a[12:15], a[136:139], v[172:175]\n\t" \
  "ds_read_b64_tr_b16 a[78:79], v115 offset:17408\n\t" \
  "v_mfma_f32_16x16x32_bf16 v[172:175], a[44:47], a[140:143], v[172:175]\n\t" \
  "ds_read_b64_tr_b16 a[108:109], v115 offset:24576\n\t" \
  "v_mfma_f32_16x16x32_bf16 v[168:171], a[40:43], a[140:143], v[168:171]\n\t" \
  "ds_read_b64_tr_b16 a[110:111], v115 offset:25600\n\t" \
  "v_mfma_f32_16x16x32_bf16 v[168:171], a[8:11], a[136:139], v[168:171]\n\t" \
  "ds_read_b64_tr_b16 a[80:81], v116 offset:16384\n\t" \
  "v_mfma_f32_16x16x32_bf16 v[164:167], a[4:7], a[136:139], v[164:167]\n\t" \
  "ds_read_b64_tr_b16 a[82:83], v116 offset:17408\n\t" \
  "v_mfma_f32_16x16x32_bf16 v[164:167], a[36:39], a[140:143], v[164:167]\n\t" \
  "ds_read_b64_tr_b16 a[112:113], v116 offset:24576\n\t" \
  "v_mfma_f32_16x16x32_bf16 v[160:163], a[32:35], a[140:143], v[160:163]\n\t" \
  "ds_read_b64_tr_b16 a[114:115], v116 offset:25600\n\t" \
  "v_mfma_f32_16x16x32_bf16 v[160:163], a[0:3], a[136:139], v[160:163]\n\t" \
  "ds_read_b64_tr_b16 a[84:85], v117 offset:16384\n\t" \
  "s_waitcnt vmcnt(12)\n\t" \
  "v_mfma_f32_16x16x32_bf16 v[192:195], a[0:3], a[144:147], v[192:195]\n\t" \
  "ds_read_b64_tr_b16 a[86:87], v117 offset:17408\n\t" \
  "v_mfma_f32_16x16x32_bf16 v[192:195], a[32:35], a[148:151], v[192:195]\n\t" \
  "ds_read_b64_tr_b16 a[116:117], v117 offset:24576\n\t" \
  "v_mfma_f32_16x16x32_bf16 v[196:199], a[36:39], a[148:151], v[196:199]\n\t" \
  "ds_read_b64_tr_b16 a[118:119], v117 offset:25600\n\t" \
  "v_mfma_f32_16x16x32_bf16 v[196:199], a[4:7], a[144:147], v[196:199]\n\t" \
  "ds_read_b64_tr_b16 a[88:89], v118 offset:16384\n\t" \
  "v_mfma_f32_16x16x32_bf16 v[200:203], a[8:11], a[144:147], v[200:203]\n\t" \
  "ds_read_b64_tr_b16 a[90:91], v118 offset:17408\n\t" \
  "v_mfma_f32_16x16x32_bf16 v[200:203], a[40:43], a[148:151], v[200:203]\n\t" \
  "ds_read_b64_tr_b16 a[120:121], v118 offset:24576\n\t" \
  "v_mfma_f32_16x16x32_bf16 v[204:207], a[44:47], a[148:151], v[204:207]\n\t" \
  "ds_read_b64_tr_b16 a[122:123], v118 offset:25600\n\t" \
  "v_mfma_f32_16x16x32_bf16 v[204:207], a[12:15], a[144:147], v[204:207]\n\t" \
  "ds_read_b64_tr_b16 a[92:93], v119 offset:16384\n\t" \
  "v_mfma_f32_16x16x32_bf16 v[208:211], a[16:19], a[144:147], v[208:211]\n\t" \
  "ds_read_b64_tr_b16 a[94:95], v119 offset:17408\n\t" \
  "v_mfma_f32_16x16x32_bf16 v[208:211], a[48:51], a[148:151], v[208:211]\n\t" \
  "ds_read_b64_tr_b16 a[124:125], v119 offset:24576\n\t" \
  "v_mfma_f32_16x16x32_bf16 v[212:215], a[52:55], a[148:151], v[212:215]\n\t" \
  "ds_read_b64_tr_b16 a[126:127], v119 offset:25600\n\t" \
  "v_mfma_f32_16x16x32_bf16 v[212:215], a[20:23], a[144:147], v[212:215]\n\t" \
  "v_mfma_f32_16x16x32_bf16 v[216:219], a[24:27], a[144:147], v[216:219]\n\t" \
  "v_mfma_f32_16x16x32_bf16 v[216:219], a[56:59], a[148:151], v[216:219]\n\t" \
  "v_mfma_f32_16x16x32_bf16 v[220:223], a[60:63], a[148:151], v[220:223]\n\t" \
  "v_mfma_f32_16x16x32_bf16 v[220:223], a[28:31], a[144:147], v[220:223]\n\t" \
  "s_waitcnt lgkmcnt(0)\n\t" \
  "s_waitcnt vmcnt(2)\n\t" \
  "v_mfma_f32_16x16x32_bf16 v[128:131], a[64:67], a[160:163], v[128:131]\n\t" \
  "v_mfma_f32_16x16x32_bf16 v[128:131], a[96:99], a[164:167], v[128:131]\n\t" \
  "v_mfma_f32_16x16x32_bf16 v[132:135], a[100:103], a[164:167], v[132:135]\n\t" \
  "v_mfma_f32_16x16x32_bf16 v[132:135], a[68:71], a[160:163], v[132:135]\n\t" \
  "v_mfma_f32_16x16x32_bf16 v[136:139], a[72:75], a[160:163], v[136:139]\n\t" \
  "v_mfma_f32_16x16x32_bf16 v[136:139], a[104:107], a[164:167], v[136:139]\n\t" \
  "v_mfma_f32_16x16x32_bf16 v[140:143], a[108:111], a[164:167], v[140:143]\n\t" \
  "v_mfma_f32_16x16x32_bf16 v[140:143], a[76:79], a[160:163], v[140:143]\n\t" \
  "v_mfma_f32_16x16x32_bf16 v[144:147], a[80:83], a[160:163], v[144:147]\n\t" \
  "v_mfma_f32_16x16x32_bf16 v[144:147], a[112:115], a[164:167], v[144:147]\n\t" \
  "v_mfma_f32_16x16x32_bf16 v[148:151], a[116:119], a[164:167], v[148:151]\n\t" \
  "v_mfma_f32_16x16x32_bf16 v[148:151], a[84:87], a[160:163], v[148:151]\n\t" \
  "v_mfma_f32_16x16x32_bf16 v[152:155], a[88:91], a[160:163], v[152:155]\n\t" \
  "v_cvt_pk_bf16_f32 v96, v128, v129\n\t" \
  "v_cvt_pk_bf16_f32 v97, v130, v131\n\t" \
  "v_mfma_f32_16x16x32_bf16 v[152:155], a[120:123], a[164:167], v[152:155]\n\t" \
  "v_cvt_pk_bf16_f32 v98, v132, v133\n\t" \
  "v_cvt_pk_bf16_f32 v99, v134, v135\n\t" \
  "s_waitcnt vmcnt(0)\n\t" \
  "v_mfma_f32_16x16x32_bf16 v[156:159], a[124:127], a[164:167], v[156:159]\n\t" \
  "v_cvt_pk_bf16_f32 v100, v136, v137\n\t" \
  "v_cvt_pk_bf16_f32 v101, v138, v139\n\t" \
  "v_mfma_f32_16x16x32_bf16 v[156:159], a[92:95], a[160:163], v[156:159]\n\t" \
  "buffer_store_dwordx4 v[96:99], %[vS], %[srdG], s80 offen offset:0 sc1\n\t" \
  "v_cvt_pk_bf16_f32 v102, v140, v141\n\t" \
  "v_cvt_pk_bf16_f32 v103, v142, v143\n\t" \
  "v_mfma_f32_16x16x32_bf16 v[160:163], a[64:67], a[168:171], v[160:163]\n\t" \
  "v_mfma_f32_16x16x32_bf16 v[160:163], a[96:99], a[172:175], v[160:163]\n\t" \
  "v_mfma_f32_16x16x32_bf16 v[164:167], a[100:103], a[172:175], v[164:167]\n\t" \
  "buffer_store_dwordx4 v[100:103], %[vS], %[srdG], s80 offen offset:1024 sc1\n\t" \
  "v_mfma_f32_16x16x32_bf16 v[164:167], a[68:71], a[168:171], v[164:167]\n\t" \
  "v_mfma_f32_16x16x32_bf16 v[168:171], a[72:75], a[168:171], v[168:171]\n\t" \
  "v_cvt_pk_bf16_f32 v104, v144, v145\n\t" \
  "v_cvt_pk_bf16_f32 v105, v146, v147\n\t" \
  "v_mfma_f32_16x16x32_bf16 v[168:171], a[104:107], a[172:175], v[168:171]\n\t" \
  "v_cvt_pk_bf16_f32 v106, v148, v149\n\t" \
  "v_cvt_pk_bf16_f32 v107, v150, v151\n\t" \
  "v_mfma_f32_16x16x32_bf16 v[172:175], a[108:111], a[172:175], v[172:175]\n\t" \
  "v_cvt_pk_bf16_f32 v108, v152, v153\n\t" \
  "v_cvt_pk_bf16_f32 v109, v154, v155\n\t" \
  "v_mfma_f32_16x16x32_bf16 v[172:175], a[76:79], a[168:171], v[172:175]\n\t" \
  "buffer_store_dwordx4 v[104:107], %[vS], %[srdG], s81 offen offset:0 sc1\n\t" \
  "v_cvt_pk_bf16_f32 v110, v156, v157\n\t" \
  "v_cvt_pk_bf16_f32 v111, v158, v159\n\t" \
  "v_mfma_f32_16x16x32_bf16 v[176:179], a[80:83], a[168:171], v[176:179]\n\t" \
  "v_mfma_f32_16x16x32_bf16 v[176:179], a[112:115], a[172:175], v[176:179]\n\t" \
  "v_mfma_f32_16x16x32_bf16 v[180:183], a[116:119], a[172:175], v[180:183]\n\t" \
  "buffer_store_dwordx4 v[108:111], %[vS], %[srdG], s81 offen offset:1024 sc1\n\t" \
  "v_mfma_f32_16x16x32_bf16 v[180:183], a[84:87], a[168:171], v[180:183]\n\t" \
  "v_mfma_f32_16x16x32_bf16 v[184:187], a[88:91], a[168:171], v[184:187]\n\t" \
  "v_cvt_pk_bf16_f32 v96, v160, v161\n\t" \
  "v_cvt_pk_bf16_f32 v97, v162, v163\n\t" \
  "v_mfma_f32_16x16x32_bf16 v[184:187], a[120:123], a[172:175], v[184:187]\n\t" \
  "v_cvt_pk_bf16_f32 v98, v164, v165\n\t" \
  "v_cvt_pk_bf16_f32 v99, v166, v167\n\t" \
  "v_mfma_f32_16x16x32_bf16 v[188:191], a[124:127], a[172:175], v[188:191]\n\t" \
  "v_cvt_pk_bf16_f32 v100, v168, v169\n\t" \
  "v_cvt_pk_bf16_f32 v101, v170, v171\n\t" \
  "v_mfma_f32_16x16x32_bf16 v[188:191], a[92:95], a[168:171], v[188:191]\n\t" \
  "buffer_store_dwordx4 v[96:99], %[vS], %[srdG], s82 offen offset:0 sc1\n\t" \
  "v_cvt_pk_bf16_f32 v102, v172, v173\n\t" \
  "v_cvt_pk_bf16_f32 v103, v174, v175\n\t" \
  "v_mfma_f32_16x16x32_bf16 v[192:195], a[64:67], a[176:179], v[192:195]\n\t" \
  "v_mfma_f32_16x16x32_bf16 v[192:195], a[96:99], a[180:183], v[192:195]\n\t" \
  "v_mfma_f32_16x16x32_bf16 v[196:199], a[100:103], a[180:183], v[196:199]\n\t" \
  "buffer_store_dwordx4 v[100:103], %[vS], %[srdG], s82 offen offset:1024 sc1\n\t" \
  "v_mfma_f32_16x16x32_bf16 v[196:199], a[68:71], a[176:179], v[196:199]\n\t" \
  "v_mfma_f32_16x16x32_bf16 v[200:203], a[72:75], a[176:179], v[200:203]\n\t" \
  "v_cvt_pk_bf16_f32 v104, v176, v177\n\t" \
  "v_cvt_pk_bf16_f32 v105, v178, v179\n\t" \
  "v_mfma_f32_16x16x32_bf16 v[200:203], a[104:107], a[180:183], v[200:203]\n\t" \
  "v_cvt_pk_bf16_f32 v106, v180, v181\n\t" \
  "v_cvt_pk_bf16_f32 v107, v182, v183\n\t" \
  "v_mfma_f32_16x16x32_bf16 v[204:207], a[108:111], a[180:183], v[204:207]\n\t" \
  "v_cvt_pk_bf16_f32 v108, v184, v185\n\t" \
  "v_cvt_pk_bf16_f32 v109, v186, v187\n\t" \
  "v_mfma_f32_16x16x32_bf16 v[204:207], a[76:79], a[176:179], v[204:207]\n\t" \
  "buffer_store_dwordx4 v[104:107], %[vS], %[srdG], s83 offen offset:0 sc1\n\t" \
  "v_cvt_pk_bf16_f32 v110, v188, v189\n\t" \
  "v_cvt_pk_bf16_f32 v111, v190, v191\n\t" \
  "v_mfma_f32_16x16x32_bf16 v[208:211], a[80:83], a[176:179], v[208:211]\n\t" \
  "v_mfma_f32_16x16x32_bf16 v[208:211], a[112:115], a[180:183], v[208:211]\n\t" \
  "v_mfma_f32_16x16x32_bf16 v[212:215], a[116:119], a[180:183], v[212:215]\n\t" \
  "buffer_store_dwordx4 v[108:111], %[vS], %[srdG], s83 offen offset:1024 sc1\n\t" \
  "v_mfma_f32_16x16x32_bf16 v[212:215], a[84:87], a[176:179], v[212:215]\n\t" \
  "v_mfma_f32_16x16x32_bf16 v[216:219], a[88:91], a[176:179], v[216:219]\n\t" \
  "v_cvt_pk_bf16_f32 v96, v192, v193\n\t" \
  "v_cvt_pk_bf16_f32 v97, v194, v195\n\t" \
  "v_mfma_f32_16x16x32_bf16 v[216:219], a[120:123], a[180:183], v[216:219]\n\t" \
  "v_cvt_pk_bf16_f32 v98, v196, v197\n\t" \
  "v_cvt_pk_bf16_f32 v99, v198, v199\n\t" \
  "v_mfma_f32_16x16x32_bf16 v[220:223], a[124:127], a[180:183], v[220:223]\n\t" \
  "v_cvt_pk_bf16_f32 v100, v200, v201\n\t" \
  "v_cvt_pk_bf16_f32 v101, v202, v203\n\t" \
  "v_mfma_f32_16x16x32_bf16 v[220:223], a[92:95], a[176:179], v[220:223]\n\t" \
  "buffer_store_dwordx4 v[96:99], %[vS], %[srdG], s84 offen offset:0 sc1\n\t" \
  "v_cvt_pk_bf16_f32 v102, v204, v205\n\t" \
  "v_cvt_pk_bf16_f32 v103, v206, v207\n\t" \
  "v_mfma_f32_16x16x32_bf16 v[224:227], a[0:3], a[152:155], v[224:227]\n\t" \
  "v_mfma_f32_16x16x32_bf16 v[224:227], a[32:35], a[156:159], v[224:227]\n\t" \
  "v_mfma_f32_16x16x32_bf16 v[224:227], a[64:67], a[184:187], v[224:227]\n\t" \
  "buffer_store_dwordx4 v[100:103], %[vS], %[srdG], s84 offen offset:1024 sc1\n\t" \
  "v_mfma_f32_16x16x32_bf16 v[224:227], a[96:99], a[188:191], v[224:227]\n\t" \
  "v_mfma_f32_16x16x32_bf16 v[228:231], a[4:7], a[152:155], v[228:231]\n\t" \
  "v_cvt_pk_bf16_f32 v104, v208, v209\n\t" \
  "v_cvt_pk_bf16_f32 v105, v210, v211\n\t" \
  "v_mfma_f32_16x16x32_bf16 v[228:231], a[36:39], a[156:159], v[228:231]\n\t" \
  "v_cvt_pk_bf16_f32 v106, v212, v213\n\t" \
  "v_cvt_pk_bf16_f32 v107, v214, v215\n\t" \
  "v_mfma_f32_16x16x32_bf16 v[228:231], a[68:71], a[184:187], v[228:231]\n\t" \
  "v_cvt_pk_bf16_f32 v108, v216, v217\n\t" \
  "v_cvt_pk_bf16_f32 v109, v218, v219\n\t" \
  "v_mfma_f32_16x16x32_bf16 v[228:231], a[100:103], a[188:191], v[228:231]\n\t" \
  "buffer_store_dwordx4 v[104:107], %[vS], %[srdG], s85 offen offset:0 sc1\n\t" \
  "v_cvt_pk_bf16_f32 v110, v220, v221\n\t" \
  "v_cvt_pk_bf16_f32 v111, v222, v223\n\t" \
  "v_mfma_f32_16x16x32_bf16 v[232:235], a[8:11], a[152:155], v[232:235]\n\t" \
  "v_mfma_f32_16x16x32_bf16 v[232:235], a[40:43], a[156:159], v[232:235]\n\t" \
  "v_mfma_f32_16x16x32_bf16 v[232:235], a[72:75], a[184:187], v[232:235]\n\t" \
  "buffer_store_dwordx4 v[108:111], %[vS], %[srdG], s85 offen offset:1024 sc1\n\t" \
  "v_mfma_f32_16x16x32_bf16 v[232:235], a[104:107], a[188:191], v[232:235]\n\t" \
  "v_mfma_f32_16x16x32_bf16 v[236:239], a[12:15], a[152:155], v[236:239]\n\t" \
  "v_mfma_f32_16x16x32_bf16 v[236:239], a[44:47], a[156:159], v[236:239]\n\t" \
  "v_mfma_f32_16x16x32_bf16 v[236:239], a[76:79], a[184:187], v[236:239]\n\t" \
  "v_mfma_f32_16x16x32_bf16 v[236:239], a[108:111], a[188:191], v[236:239]\n\t" \
  "v_mfma_f32_16x16x32_bf16 v[240:243], a[16:19], a[152:155], v[240:243]\n\t" \
  "v_mfma_f32_16x16x32_bf16 v[240:243], a[48:51], a[156:159], v[240:243]\n\t" \
  "v_mfma_f32_16x16x32_bf16 v[240:243], a[80:83], a[184:187], v[240:243]\n\t" \
  "v_mfma_f32_16x16x32_bf16 v[240:243], a[112:115], a[188:191], v[240:243]\n\t" \
  "v_mfma_f32_16x16x32_bf16 v[244:247], a[20:23], a[152:155], v[244:247]\n\t" \
  "v_mfma_f32_16x16x32_bf16 v[244:247], a[52:55], a[156:159], v[244:247]\n\t" \
  "v_mfma_f32_16x16x32_bf16 v[244:247], a[84:87], a[184:187], v[244:247]\n\t" \
  "v_mfma_f32_16x16x32_bf16 v[244:247], a[116:119], a[188:191], v[244:247]\n\t" \
  "v_mfma_f32_16x16x32_bf16 v[248:251], a[24:27], a[152:155], v[248:251]\n\t" \
  "v_mfma_f32_16x16x32_bf16 v[248:251], a[56:59], a[156:159], v[248:251]\n\t" \
  "v_mfma_f32_16x16x32_bf16 v[248:251], a[88:91], a[184:187], v[248:251]\n\t" \
  "v_mfma_f32_16x16x32_bf16 v[248:251], a[120:123], a[188:191], v[248:251]\n\t" \
  "v_mfma_f32_16x16x32_bf16 v[252:255], a[28:31], a[152:155], v[252:255]\n\t" \
  "v_mfma_f32_16x16x32_bf16 v[252:255], a[60:63], a[156:159], v[252:255]\n\t" \
  "v_mfma_f32_16x16x32_bf16 v[252:255], a[92:95], a[184:187], v[252:255]\n\t" \
  "v_mfma_f32_16x16x32_bf16 v[252:255], a[124:127], a[188:191], v[252:255]\n\t" \
  "s_nop 7\n\t" \
  ".subsection 1\n\t" \
  "att_pl:\n\t" \
  "s_setpc_b64 s[68:69]\n\t" \
  "7:\n\t" \
  "s_mov_b32 s57, 0x400000\n\t" \
  "5:\n\t" \
  "buffer_load_dwordx2 v[120:121], %[vF], %[srdG], 0 offen sc1\n\t" \
  "s_waitcnt vmcnt(0)\n\t" \
  "v_cmp_ne_u64 vcc, v[120:121], %[tag]\n\t" \
  "s_cbranch_vccz 8b\n\t" \
  "s_sleep 2\n\t" \
  "s_sub_u32 s57, s57, 1\n\t" \
  "s_cbranch_scc0 5b\n\t" \
  "s_branch 8b\n\t" \
  ".subsection 0\n\t" \
  ""
#define TOUCH_C \
  "s_getpc_b64 s[64:65]\n\t" \
  "1:\n\t" \
  "s_cmp_eq_u32 %[wid], 1\n\t" \
  "s_cbranch_scc1 2f\n\t" \
  "s_cmp_eq_u32 %[wid], 2\n\t" \
  "s_cbranch_scc1 3f\n\t" \
  "s_cmp_eq_u32 %[wid], 3\n\t" \
  "s_cbranch_scc1 4f\n\t" \
  "s_branch 9f\n\t" \
  "2:\n\t" \
  "s_add_u32 s66, s64, att_o0-1b\n\t" \
  "s_addc_u32 s67, s65, 0\n\t" \
  "s_swappc_b64 s[68:69], s[66:67]\n\t" \
  "s_branch 9f\n\t" \
  "3:\n\t" \
  "s_add_u32 s66, s64, att_o1-1b\n\t" \
  "s_addc_u32 s67, s65, 0\n\t" \
  "s_swappc_b64 s[68:69], s[66:67]\n\t" \
  "s_mov_b32 s66, att_o1-1b\n\t" \
  "s_cmp_eq_u32 %[fin], 2\n\t" \
  "s_cselect_b32 s66, att_f2-1b, s66\n\t" \
  "s_cmp_eq_u32 %[fin], 3\n\t" \
  "s_cselect_b32 s66, att_f3-1b, s66\n\t" \
  "s_add_u32 s66, s64, s66\n\t" \
  "s_addc_u32 s67, s65, 0\n\t" \
  "s_swappc_b64 s[68:69], s[66:67]\n\t" \
  "s_branch 9f\n\t" \
  "4:\n\t" \
  "s_mov_b32 s66, att_f1-1b\n\t" \
  "s_cmp_eq_u32 %[fin], 0\n\t" \
  "s_cselect_b32 s66, att_f0-1b, s66\n\t" \
  "s_add_u32 s66, s64, s66\n\t" \
  "s_addc_u32 s67, s65, 0\n\t" \
  "s_swappc_b64 s[68:69], s[66:67]\n\t" \
  "9:\n\t" \
  ""
#define H4_CLOB "v96", "v97", "v98", "v99", "v100", "v101", "v102", "v103", "v104", "v105", "v106", "v107", "v108", "v109", "v110", "v111", "v112", "v113", "v114", "v115", "v116", "v117", "v118", "v119", "v120", "v121", "v122", "v123", "v124", "v125", "v126", "v127", "a0", "a1", "a2", "a3", "a4", "a5", "a6", "a7", "a8", "a9", "a10", "a11", "a12", "a13", "a14", "a15", "a16", "a17", "a18", "a19", "a20", "a21", "a22", "a23", "a24", "a25", "a26", "a27", "a28", "a29", "a30", "a31", "a32", "a33", "a34", "a35", "a36", "a37", "a38", "a39", "a40", "a41", "a42", "a43", "a44", "a45", "a46", "a47", "a48", "a49", "a50", "a51", "a52", "a53", "a54", "a55", "a56", "a57", "a58", "a59", "a60", "a61", "a62", "a63", "a64", "a65", "a66", "a67", "a68", "a69", "a70", "a71", "a72", "a73", "a74", "a75", "a76", "a77", "a78", "a79", "a80", "a81", "a82", "a83", "a84", "a85", "a86", "a87", "a88", "a89", "a90", "a91", "a92", "a93", "a94", "a95", "a96", "a97", "a98", "a99", "a100", "a101", "a102", "a103", "a104", "a105", "a106", "a107", "a108", "a109", "a110", "a111", "a112", "a113", "a114", "a115", "a116", "a117", "a118", "a119", "a120", "a121", "a122", "a123", "a124", "a125", "a126", "a127", "a128", "a129", "a130", "a131", "a132", "a133", "a134", "a135", "a136", "a137", "a138", "a139", "a140", "a141", "a142", "a143", "a144", "a145", "a146", "a147", "a148", "a149", "a150", "a151", "a152", "a153", "a154", "a155", "a156", "a157", "a158", "a159", "a160", "a161", "a162", "a163", "a164", "a165", "a166", "a167", "a168", "a169", "a170", "a171", "a172", "a173", "a174", "a175", "a176", "a177", "a178", "a179", "a180", "a181", "a182", "a183", "a184", "a185", "a186", "a187", "a188", "a189", "a190", "a191", "a192", "a193", "a194", "a195", "a196", "a197", "a198", "a199", "a200", "a201", "a202", "a203", "a204", "a205", "a206", "a207", "a208", "a209", "a210", "a211", "a212", "a213", "a214", "a215", "a216", "a217", "a218", "a219", "a220", "a221", "a222", "a223", "a224", "a225", "a226", "a227", "a228", "a229", "a230", "a231", "a232", "a233", "a234", "a235", "a236", "a237", "a238", "a239", "a240", "a241", "a242", "a243", "a244", "a245", "a246", "a247", "a248", "a249", "a250", "a251", "a252", "a253", "a254", "a255", "s40", "s41", "s42", "s43", "s44", "s45", "s46", "s47", "s48", "s49", "s50", "s51", "s52", "s53", "s54", "s55", "s56", "s57", "s58", "s59", "s60", "s61", "m0", "scc", "vcc", "memory"
typedef unsigned long long u64;
#define HPAD 1024
constexpr unsigned HS = 2048 * 256 + HPAD;
typedef float f32x32 __attribute__((ext_vector_type(32)));
typedef unsigned long long u64x4 __attribute__((ext_vector_type(4)));
typedef unsigned u32x16 __attribute__((ext_vector_type(16)));
#define XPAGE(name) asm volatile("s_branch 99f\n\t.p2align 12\n" name ":\n\ts_setpc_b64 s[68:69]\n99:")
#define TOUCH_CLOB "s64", "s65", "s66", "s67", "s68", "s69", "scc"
__device__ __forceinline__ unsigned lane_now() { unsigned l; asm volatile("v_mbcnt_lo_u32_b32 %0, -1, 0\n\tv_mbcnt_hi_u32_b32 %0, -1, %0" : "=v"(l)); return l; }
struct G1Args { u64 ckv, wuk, wuv, ws, Ax, Awqkv, Bx, wq, Awo, Bwo, out; unsigned plan[128]; };
constexpr unsigned PART_BYTES = 65536 + 8192 + 128;
constexpr unsigned WS_QKVA = 262144, WS_Q = WS_QKVA + 2048 * 6144, WS_K = WS_Q + 64 * 65536, WS_V = WS_K + 32 * HS + HPAD / 2, WS_OA = WS_V + 32 * HS + HPAD / 2,
                   WS_CTX = WS_OA + 2048 * 2048, WS_PART = WS_CTX + 2048 * 8192, WS_APART = WS_PART + 64 * 4 * 65536, WS_END = WS_APART + 64 * PART_BYTES, NOWHERE = 0x7fff0000u;
constexpr unsigned FLG_KV = 256, FLG_PA = 4096, FLG_Q = 8192, FLG_P = 16384, FLG_C = 24576, FLG_LA = 26624, FLG_S = 32768, FLG_H = FLG_PA, WS_HALO = WS_QKVA;
typedef unsigned u32x4g __attribute__((ext_vector_type(4)));
#define STAMP(i)
#define RSTAMP(i)
#define STAMP_DECL
#define OARG
#define OPASS
#define STAMP_OUT()
template <int M> __device__ __forceinline__ float h4_get(const f32x32 (&c)[4], int i) { return c[M >> 1][16 * (M & 1) + i]; }
template <int M, int NA> __device__ __forceinline__ void h4_stage(const f32x32 (&c)[4], const u32x4g* x, char* sw, unsigned lane) {
  float v[16];
#pragma unroll
  for (int i = 0; i < 16; ++i) v[i] = h4_get<M>(c, i);
#pragma unroll
  for (int s = 0; s < NA; ++s)
#pragma unroll
    for (int i = 0; i < 8; ++i) { const unsigned w = x[2 * s + (i >> 2)][i & 3]; v[2 * i] += __uint_as_float(w << 16); v[2 * i + 1] += __uint_as_float(w & 0xffff0000u); }
#pragma unroll
  for (int n = 0; n < 4; ++n) {
    u32x2 w = {pack_bf16(v[4 * n], v[4 * n + 1]), pack_bf16(v[4 * n + 2], v[4 * n + 3])};
    *(u32x2*)(sw + (lane & 15) * 128 + (((2 * n + (lane >> 5)) ^ (lane & 7)) << 4) + ((lane >> 4) & 1) * 8) = w;
  }
}
template <int NM> __device__ __forceinline__ void h4_flush(const char* scr, unsigned lane, char* dst) {
  u32x4g r[2 * NM];
#pragma unroll
  for (int j = 0; j < 2 * NM; ++j) r[j] = *(const u32x4g*)(scr + (8 * j + (lane >> 3)) * 128 + (((lane & 7) ^ (lane >> 3)) << 4));
#pragma unroll
  for (int j = 0; j < 2 * NM; ++j)
    asm volatile("global_store_dwordx4 %0, %1, off offset:%2 sc1\n\ts_nop 1" :: "v"(dst + (long)(16 * (j >> 2) + 8 * (j & 1)) * 4096), "v"(r[j]), "n"(((j >> 1) & 1) * 128) : "memory");
}
__device__ __forceinline__ void wait_words(const u64* w, u64 tag) {
#pragma unroll 1
  for (unsigned spins = 0; spins < 4000000u; ++spins) {
    if (__all(__hip_atomic_load(w, __ATOMIC_RELAXED, __HIP_MEMORY_SCOPE_AGENT) == tag)) break;
    __builtin_amdgcn_s_sleep(1);
  }
}
template <int M0, int NA, int... Ms> __device__ __forceinline__ void h4_stage_all(const f32x32 (&c)[4], const u32x4g* x, char* scr, unsigned lane, std::integer_sequence<int, Ms...>) {
  (h4_stage<M0 + Ms, NA>(c, x + 2 * NA * Ms, scr + 2048 * Ms, lane), ...);
}
template <int NM, int NA> __device__ __forceinline__ void h4_fetch(u32x4g (&x)[2 * NM * NA], const __amdgpu_buffer_rsrc_t srd, unsigned vS, const unsigned (&so)[3], unsigned blk) {
#pragma unroll
  for (int m = 0; m < NM; ++m)
#pragma unroll
    for (int s = 0; s < NA; ++s)
#pragma unroll
      for (int h = 0; h < 2; ++h) x[(m * NA + s) * 2 + h] = __builtin_amdgcn_raw_buffer_load_b128(srd, vS + h * 1024, so[s] + (blk + m) * 8192, 16);
}
namespace att {
typedef float    f32x4  __attribute__((ext_vector_type(4)));
typedef float    f32x16 __attribute__((ext_vector_type(16)));
typedef unsigned u32x4  __attribute__((ext_vector_type(4)));
#define VRING 0x8000u
#define QSTAGE 0x10000u
#define SRD_WORD3 0x00020000
#define DI __device__ __forceinline__
#define GAP() __builtin_amdgcn_sched_barrier(0)
DI unsigned rfl(unsigned x) { return __builtin_amdgcn_readfirstlane(x); }
DI unsigned lane_id() { return lane_now(); }
template <int R> DI float acc_read1() { float r; asm volatile("v_accvgpr_read_b32 %0, a[%c1]" : "=v"(r) : "i"(R)); return r; }
template <int F, bool MERGE> DI void out_frag(const __amdgpu_buffer_rsrc_t srd, unsigned voff, unsigned sq, unsigned sd, const char* pin, const float (&c1)[4], const float (&c2)[4]) {
  float o[8] = {acc_read1<8 * F>(), acc_read1<8 * F + 1>(), acc_read1<8 * F + 2>(), acc_read1<8 * F + 3>(), acc_read1<8 * F + 4>(), acc_read1<8 * F + 5>(), acc_read1<8 * F + 6>(), acc_read1<8 * F + 7>()};
  u32x4 w;
  #pragma unroll
  for (int i = 0; i < 8; ++i) o[i] *= c1[F >> 2];
  if constexpr (MERGE) {
    asm volatile("s_waitcnt vmcnt(15)" ::: "memory");
    const u32x4 p = *(const u32x4*)(pin + 1024 * F);
    #pragma unroll
    for (int i = 0; i < 4; ++i) { const unsigned u = p[i]; o[2 * i] += __uint_as_float(u << 16) * c2[F >> 2]; o[2 * i + 1] += __uint_as_float(u & 0xffff0000u) * c2[F >> 2]; }
  }
  #pragma unroll
  for (int i = 0; i < 4; ++i) w[i] = pack_bf16(o[2 * i], o[2 * i + 1]);
  asm volatile("buffer_store_dwordx4 %0, %1, %2, %3 offen offset:%4 sc1\n\ts_nop 1" :: "v"(w), "v"(voff), "s"(srd), "s"((F >> 2) * sq + ((F >> 1) & 1) * sd), "n"((F & 1) * 1024) : "memory");
}
template <bool MERGE, int... Fs> DI void out_all(const __amdgpu_buffer_rsrc_t srd, unsigned voff, unsigned sq, unsigned sd, const char* pin, const float (&c1)[4], const float (&c2)[4], std::integer_sequence<int, Fs...>) {
  (out_frag<Fs, MERGE>(srd, voff, sq, sd, pin, c1, c2), ...);
}

__global__ __launch_bounds__(256, 1)
void attn_kernel(G1Args kernel_args) {
  __shared__ __attribute__((aligned(1024))) char lds[163840];
  STAMP_DECL; STAMP(0); RSTAMP(14);
  u64x4 a4, b4;
  u64 plan64;
  unsigned warm;
  asm volatile("s_load_dwordx8 s[88:95], %4, 0x0\n\ts_getpc_b64 s[64:65]\n1:\n\ts_mov_b32 s66, zlast+256-1b\n\ts_mov_b32 s67, 0x20000\n\tbuffer_load_dword a254, %5, s[64:67], 0 offen\n\t"
               "s_load_dwordx8 s[80:87], %4, 0x20\n\ts_load_dwordx2 s[96:97], %4, %6\n\ts_load_dword s98, %4, 0x40"
               : "=&{s[88:95]}"(a4), "=&{s[80:87]}"(b4), "=&{s[96:97]}"(plan64), "=&{s98}"(warm)
               : "s"(__builtin_amdgcn_kernarg_segment_ptr()), "v"(((blockIdx.x >> 3) * 64u + (threadIdx.x >> 6) * 16u + (threadIdx.x & 15u)) * 128u + ((threadIdx.x & 48u) << 24)),
                 "s"(88u + (blockIdx.x >> 5) * 64u + (blockIdx.x & 7u) * 8u)
               : "s64", "s65", "s66", "s67", "a254");
  u32x4 keep;
  unsigned taglo, taghi;
#define TAG64 (((u64)taghi << 32) | taglo)
  f32x4 ls;
  __amdgpu_buffer_rsrc_t srdG;
  typedef const __attribute__((address_space(4))) G1Args* KArg;
#define KARG(k) KArg k = (KArg)__builtin_amdgcn_kernarg_segment_ptr(); asm volatile("" : "+s"(k))
#define ROLES const unsigned xcd = b & 7u, jj = b >> 3, wk = 4096u * wid, head = 16u * (xcd >> 2) + 8u * (xcd & 1u) + 2u * (xcd & 2u) + (jj & 3u), slot = b >> 5
  {
  const unsigned wid = rfl(threadIdx.x >> 6), b = blockIdx.x;
  ROLES;
  const unsigned rt1 = 2u * (xcd & 1u) + (jj & 1u) + (jj < 16u ? 0u : 4u), ct1 = 4u * (xcd >> 1) + ((jj >> 1) & 3u), isv = (jj >> 3) & 1u;
  unsigned ln = lane_id();
  const unsigned l4 = ln >> 4, l15 = ln & 15u, swA = ((ln & 7u) ^ (ln >> 3)) << 4, cw = l15 ^ (l4 << 1);
  unsigned vA = (rt1 * 256u + 64u * wid + (ln >> 3)) * 1024u + swA, vW0 = (16u * wid + l4) * 8192u + ct1 * 512u + (cw << 4);
  asm volatile("s_waitcnt lgkmcnt(0)" : "+{s[88:95]}"(a4), "+{s[80:87]}"(b4), "+{s[96:97]}"(plan64), "+{s98}"(warm) : "v"(vA), "v"(vW0));
  keep = u32x4{b, wid, (unsigned)plan64, (unsigned)(plan64 >> 32)};
  srdG = __builtin_amdgcn_make_buffer_rsrc((void*)a4[3], (short)0, WS_END, SRD_WORD3);
  u64x4 dsc = {a4[0], (0x20000ull << 32) | (2048u * 1024u), isv ? a4[2] : a4[1], (0x20000ull << 32) | (512u * 8192u)};
  STAMP(22);
  asm volatile(Z_PRO1 : "+{v5}"(vA), "+{v6}"(vW0) : "{v29}"(((b * 4u + wid) * 16u + l15) * 128u + ((ln & 48u) << 24)), "{s32}"(wk), "{s[60:63]}"(srdG), "{s[64:71]}"(dsc) : Z_CLOB1);
  STAMP(23);
  const bool isX = slot < 4u;
  const unsigned lo = isX ? slot : slot + 4u, hi = (isX ? 7u : 23u) - lo;
  {
    const unsigned wr = wid >> 1, wc = wid & 1u, rta = 4u * (xcd & 1u) + (jj & 3u), cta = 4u * (xcd >> 1) + (slot & 3u), ta = rta * 16u + cta;
    const unsigned kvsig = FLG_KV + ((ct1 * 8u + rt1) * 2u + isv) * 8u;
    KARG(kp);
    const u64 pAx = b4[0], pAw = b4[1], pBx = b4[2], pWq = b4[3];
    const unsigned gw = (b & 127u) * 4u + wid;
#define PF1 400u, 24u, 40u, 256u, 22u, 22u
    constexpr unsigned pf1[6] = {PF1};
    const unsigned nw1 = isX ? pf1[3] : pf1[0];
    const bool pw = gw < nw1;
    const unsigned pl = isX ? (pw ? pf1[4] : pf1[5]) : (pw ? pf1[1] : pf1[2]);
    const __amdgpu_buffer_rsrc_t srdP = __builtin_amdgcn_make_buffer_rsrc((void*)(isX ? (pw ? pAw : pAx) : (pw ? pWq : pBx + 1024u * 2048u)), (short)0,
                                                                          isX ? (pw ? 6291456u : 4194304u) : (pw ? 8388608u : 2097152u), SRD_WORD3);
    const unsigned vP = ((pw ? gw : gw - nw1) * pl + ln) * 128u + (ln < pl ? 0u : 0x40000000u), sP = (pw ? nw1 : 512u - nw1) * pl * 128u;
#define PF2 410u, 14u, 14u
    constexpr unsigned pf2[3] = {PF2};
    const bool k2 = gw < pf2[0];
    const unsigned pl2 = k2 ? pf2[1] : pf2[2], g2 = k2 ? gw : gw - pf2[0], n2 = k2 ? pf2[0] : 512u - pf2[0];
    const __amdgpu_buffer_rsrc_t srdP2 = __builtin_amdgcn_make_buffer_rsrc((void*)(k2 ? kp->Bwo : kp->Awo), (short)0, isX ? 0u : k2 ? 8388608u : 2097152u, SRD_WORD3);
    const bool top = rta == 0u && wr == 0u;
    u32x16 sp = {wk, isv | (isX ? 2u : 0u) | (lo == 0u ? 4u : 0u) | (hi == lo + 1u ? 8u : 0u),
                 (isv ? WS_V : WS_K) + (2u * ct1 + wc) * HS + (rt1 * 2u + wr) * 32768u, kvsig, isX ? FLG_LA + ta * 8u : kvsig, (hi - lo) * 128u * 2048u, WS_K + head * HS,
                 isX ? 2u * lo - 1u : 12u - Z_NY / 2u, isX ? 0u : 2u * lo - 13u, 2u * (hi - lo) - 3u, 4u * lo, 4u * hi, sP, n2 * pl2 * 128u,
                 isX ? WS_HALO + (ta * 4u + wid) * 2048u : gw * 4096u, top ? NOWHERE : WS_HALO + ((wr ? ta : ta - 16u) * 4u + (wid ^ 2u)) * 2048u};
    u32x4 sq = {FLG_H + ta * 8u, FLG_H + (wr || top ? ta : ta - 16u) * 8u, WS_OA + rta * 524288u + cta * 32768u + wr * 16384u + wc * 1024u, top ? 1u : 0u};
    #pragma unroll
    for (int i = 0; i < 16; ++i) sp[i] = rfl(sp[i]);
    #pragma unroll
    for (int i = 0; i < 4; ++i) sq[i] = rfl(sq[i]);
    const unsigned q4 = l15 >> 2, p4 = ln & 3u, f4 = q4 | ((l4 & 1u) << 2), cv = cw ^ (wc << 3);
    u32x16 vp = {16u * ln, FLG_KV + (head >> 1) * 128u + 8u * (ln & 7u), 0u, 0u, vP, vA, vW0,
                 (wr * 128u + l15) * 128u + ((l4 ^ (ln & 7u)) << 4), 65536u + wc * 16384u + (8u * l4 + q4) * 256u + (((p4 >> 1) ^ (f4 << 1)) << 4) + (p4 & 1u) * 8u,
                 (rta * 256u + 64u * wid + (ln >> 3)) * 2048u + swA,
                 (16u * wid + l4) * 6144u + (cw >> 3) * 2048u + cta * 128u + (cw & 7u) * 16u, (16u * wid + l4) * 6144u + ((cw >> 3) ^ 1u) * 2048u + cta * 128u + (cw & 7u) * 16u,
                 (8u * wid + l4 + 32u * (cv >> 3)) * 6144u + 4096u + cta * 128u + (cv & 7u) * 16u,
                 (lo * 128u + 16u * wid + (ln >> 3)) * 2048u + swA, (16u * wid + l4) * 8192u + head * 256u + (cw << 4),
                 (g2 * pl2 + ln) * 128u + (ln < pl2 ? 0u : 0x40000000u)};
#define Z_STV
    asm volatile(Z_MAIN : "+{v[0:15]}"(vp), "+{s[32:47]}"(sp), "+{s[64:71]}"(dsc), "=&{v[104:107]}"(ls), "+{s[28:31]}"(keep), "+{s[60:63]}"(srdG) Z_STV
                 : "{s[48:51]}"(sq), "{s[52:55]}"(srdP), "{s[56:59]}"(srdP2), "{s[80:87]}"(b4) : Z_CLOB);
    taglo = vp[2]; taghi = vp[3];
  }
  }
  const unsigned b = keep[0], wid = keep[1], plan = keep[2], plan2 = keep[3];
  ROLES;
  const unsigned oct = (xcd & 4u) + (jj & 3u);
  KARG(k);
  char* const ws = (char*)k->ws;
  {
    const unsigned l = lane_id();
    float c1[4];
    #pragma unroll
    for (int q = 0; q < 4; ++q) c1[q] = __builtin_amdgcn_rcpf(ls[q]);
    out_all<false>(srdG, 16u * l + WS_CTX + ((slot * 64u + 2u * head) * 16u + wid) * 2048u, 8192u, 32768u, nullptr, c1, c1, std::make_integer_sequence<int, 16>{});
  }
  STAMP(9 - 4);
  u64* const cflag = (u64*)(ws + FLG_C) + slot * 32u + head;
  {
    const unsigned rt = (plan >> 9) & 7u, onw = (plan >> 12) & 7u, osl = (plan >> 15) & 3u, om0 = (plan >> 17) & 7u, onm = (plan >> 20) & 7u, h0 = (plan >> 23) & 31u;
    const unsigned lane = lane_id(), wr = wid >> 1, wc = wid & 1u;
    const bool isA = onw == 0u;
    const unsigned nh1 = plan2 >> 8;
    unsigned cnt = plan2 & 255u;
    unsigned vW = ((128u * h0 + 32u * wr + 8u * wc) + (lane >> 4)) * 2048u + oct * 256u + (((lane & 15u) ^ ((lane >> 4) << 1)) << 4);
    const __amdgpu_buffer_rsrc_t srdW = __builtin_amdgcn_make_buffer_rsrc((void*)(isA ? k->Awo : k->Bwo), (short)0, isA ? 1024 * 2048 : 4096 * 2048, SRD_WORD3);
    unsigned wad0;
    { const unsigned g = lane >> 4, q = (lane & 15u) >> 2, p = lane & 3u, f = q | ((g & 1u) << 2);
      wad0 = (8u * g + q) * 256u + (((p >> 1) ^ (f << 1)) << 4) + (p & 1u) * 8u; }
    unsigned oA = (isA ? WS_OA + rt * 16u * 32768u : WS_CTX + (rt * 64u + 2u * h0) * 32768u) + 2u * wk + lane * 16u;
    typedef unsigned u32x8 __attribute__((ext_vector_type(8)));
    u32x4 soA;
    u32x8 soS;
    #pragma unroll
    for (unsigned i = 0; i < 8; ++i) {
      if (i < 4) soA[i] = rfl(((2u * i + om0 + onm) & 7u) * 1024u);
      soS[i] = rfl((!isA && i + onm < 8u) ? osl * 65536u + ((i + om0 + onm) & 7u) * 8192u : NOWHERE);
    }
    const unsigned vS = WS_PART + (rt * 8u + oct) * 262144u + wid * 2048u + lane * 16u;
    const u64* wp = (const u64*)ws + (isA ? FLG_LA / 8u + rt * 16u + (lane & 15u) : FLG_C / 8u + rt * 32u + h0 + (lane < nh1 && 2u * lane < cnt ? lane : 0u));
    u64* sp = cflag;
    const unsigned plo = rt < 4u ? rt : rt + 4u, row0 = isA ? rt * 256u + wid * 64u : (wr ? (rt < 4u ? 7u : 23u) - plo : plo) * 128u + wc * 64u;
    char* dst = (char*)k->out + (long)(row0 + (lane >> 3)) * 4096 + (isA ? 0 : 2048) + oct * 256u + (lane & 7u) * 16u;
    asm volatile("s_waitcnt vmcnt(0) lgkmcnt(0)\n\ts_barrier" : "+{s[72:75]}"(soA), "+{s[80:87]}"(soS) : "v"(oA), "v"(wad0), "v"(wp), "v"(sp), "v"(vS), "v"(dst), "v"(vW), "s"(srdW) : "memory");
    STAMP(21); RSTAMP(54);
    if (wid == 0 && lane == 0u) __hip_atomic_store(sp, TAG64, __ATOMIC_RELAXED, __HIP_MEMORY_SCOPE_AGENT);
    asm volatile(H4_PROW : [vW0] "+v"(vW) : [srdW] "s"(srdW), [m0W] "s"(wk) : H4_CLOB);
    if (wid == 0) wait_words(wp, TAG64);
    asm volatile(TOUCH_C :: [wid] "s"(wid), [fin] "s"(onw) : TOUCH_CLOB);
    if (b == 0u && wid == 3u) {
      #pragma unroll 1
      for (int i = 0; i < 4; ++i) wait_words((const u64*)(ws + FLG_KV) + 64 * i + lane, TAG64);
      if (lane == 0) __hip_atomic_store((u64*)ws, TAG64, __ATOMIC_RELAXED, __HIP_MEMORY_SCOPE_AGENT);
    }
    asm volatile("s_waitcnt vmcnt(8)\n\ts_barrier" ::: "memory");
    STAMP(16); RSTAMP(55); STAMP(17);
    f32x32 oc[4];
    asm volatile(H4_LOOP
      : [vA] "+v"(oA), [vW0] "+v"(vW), [cnt] "+s"(cnt), "=&{v[128:159]}"(oc[0]), "=&{v[160:191]}"(oc[1]), "=&{v[192:223]}"(oc[2]), "=&{v[224:255]}"(oc[3])
      : [wad0] "v"(wad0), [srdG] "s"(srdG), "{s[72:75]}"(soA), "{s[80:87]}"(soS), [vS] "v"(vS), [srdW] "s"(srdW), [m0W] "s"(wk),
        [pc] "s"(cnt + 4u - 2u * nh1), [vF] "v"(FLG_C + (rt * 32u + h0 + (2u * lane < cnt ? lane : 0u)) * 8u), [tag] "v"(TAG64)
      : H4_CLOB);
    STAMP(18); RSTAMP(56);
    char* scr = lds + 65536u + 4u * wk;
    u64* fs = (u64*)(ws + FLG_S) + (rt * 8u + oct) * 16u;
    if (__builtin_expect(isA, 0)) {
      XPAGE("att_f0");
      h4_stage_all<0, 0>(oc, nullptr, scr, lane, std::make_integer_sequence<int, 8>{});
      h4_flush<8>(scr, lane, dst);
      STAMP(19); STAMP(20);
    } else {
      XPAGE("att_f1");
      STAMP(19);
      asm volatile("s_waitcnt vmcnt(0)" ::: "memory");
      if (lane == 0) __hip_atomic_store(fs + 4u * wid + osl, TAG64, __ATOMIC_RELAXED, __HIP_MEMORY_SCOPE_AGENT);
      unsigned so[3];
      #pragma unroll
      for (unsigned i = 0; i < 3; ++i) { const unsigned p = osl + 1u + i; so[i] = rfl(i + 1u < onw ? (p >= onw ? p - onw : p) * 65536u : NOWHERE); }
      const unsigned pp = osl + 1u + (lane + 1u < onw ? lane : 0u);
      const u64* pw = fs + 4u * wid + (pp >= onw ? pp - onw : pp);
      char* d0 = dst + (long)om0 * 8 * 4096;
      if (__builtin_expect(onm == 2u, 1)) {
        wait_words(pw, TAG64);
        STAMP(20);
        u32x4g x[12];
        h4_fetch<2, 3>(x, srdG, vS, so, om0);
        h4_stage_all<6, 3>(oc, x, scr, lane, std::make_integer_sequence<int, 2>{});
        h4_flush<2>(scr, lane, d0);
      } else if (onw == 2u) {
        XPAGE("att_f2");
        wait_words(pw, TAG64);
        STAMP(20);
        u32x4g x[8];
        h4_fetch<4, 1>(x, srdG, vS, so, om0);
        h4_stage_all<4, 1>(oc, x, scr, lane, std::make_integer_sequence<int, 4>{});
        h4_flush<4>(scr, lane, d0);
      } else {
        XPAGE("att_f3");
        wait_words(pw, TAG64);
        STAMP(20);
        u32x4g x[16];
        h4_fetch<4, 2>(x, srdG, vS, so, om0);
        h4_stage_all<4, 2>(oc, x, scr, lane, std::make_integer_sequence<int, 4>{});
        h4_flush<4>(scr, lane, d0);
      }
    }
  }
  STAMP(13); RSTAMP(15); STAMP_OUT();
}
}

#define WSOFF 0
extern "C" void kernel_launch(void* const* d_in, const int* in_sizes, int n_in, void* d_out, int out_size,
                              void* d_ws, size_t ws_size, hipStream_t stream) {
  const bf16_t* A_x = (const bf16_t*)d_in[0];
  const bf16_t* A_wqkv = (const bf16_t*)d_in[1];
  const bf16_t* A_wo = (const bf16_t*)d_in[2];
  const bf16_t* B_x = (const bf16_t*)d_in[3];
  const bf16_t* B_ckv = (const bf16_t*)d_in[4];
  const bf16_t* B_wq = (const bf16_t*)d_in[5];
  const bf16_t* B_wuk = (const bf16_t*)d_in[6];
  const bf16_t* B_wuv = (const bf16_t*)d_in[7];
  const bf16_t* B_wo = (const bf16_t*)d_in[8];
    G1Args g1 = {(u64)B_ckv, (u64)B_wuk, (u64)B_wuv, (u64)d_ws + WSOFF, (u64)A_x, (u64)A_wqkv, (u64)B_x, (u64)B_wq, (u64)A_wo, (u64)B_wo, (u64)d_out, {}};
  for (unsigned i = 0; i < 64; ++i) {
    const unsigned slot = i >> 3, q = i & 3;
#define BIAS 1
    const unsigned nw = q == 3 ? 0 : 3, sl = nw ? q : 0;
    const unsigned m0 = nw ? (sl == 2 ? 6 : 2 * sl) : 0, nm = nw ? (sl == 1 ? 4 : 2) : 0;
    const unsigned cut[4] = {0, 11, 21 - BIAS, 32};
    const unsigned h0 = nw ? cut[sl] : 0, nh = nw ? cut[sl + 1] - h0 : 8;
    g1.plan[2 * i] = slot << 9 | nw << 12 | sl << 15 | m0 << 17 | nm << 20 | h0 << 23;
    g1.plan[2 * i + 1] = 2 * nh | (nh + 8) << 8;
  }
  att::attn_kernel<<<256, 256, 0, stream>>>(g1);
}
```

```cpp
#include <hip/hip_runtime.h>
#include <hip/hip_bf16.h>
#include <stdint.h>
#include <utility>

typedef unsigned short bf16_t;
typedef short bf16x4 __attribute__((ext_vector_type(4)));
typedef float f32x4 __attribute__((ext_vector_type(4)));
typedef unsigned int u32x2 __attribute__((ext_vector_type(2)));

__device__ __forceinline__ unsigned pack_bf16(float a, float b) {
  typedef __bf16 bf2 __attribute__((ext_vector_type(2)));
  typedef float f2 __attribute__((ext_vector_type(2)));
  bf2 r = __builtin_convertvector((f2){a, b}, bf2);
  return __builtin_bit_cast(unsigned, r);
}
#define Z_PRO1 \
"s_nop 4\nbuffer_load_dwordx2 a[252:253], off, s[60:63], 0 sc1\ns_lshl_b32 s88, s32, 1\nv_xor_b32 v28, 128, v6\ns_mov_b32 s73, 7168\ns_mov_b32 s74, 14336\ns_mov_b32 s75, 21504\ns_mov_b32 s76, 32768\n" \
"s_mov_b32 s77, 39936\ns_mov_b32 s78, 47104\ns_mov_b32 s79, 54272\ns_mov_b32 s89, 31744\ns_mov_b32 s90, 63488\ns_mov_b32 s91, 95232\ns_mov_b32 s92, 256\ns_mov_b32 s93, 32000\n" \
"s_mov_b32 s94, 63744\ns_mov_b32 s95, 95488\ns_mov_b32 s72, 524288\ns_add_u32 m0, s88, 0\ns_nop 0\nbuffer_load_dwordx4 v5, s[64:67], 0 offen offset:0 lds\nbuffer_load_dwordx4 v5, s[64:67], s73 offen offset:1024 lds\nbuffer_load_dwordx4 v5, s[64:67], s74 offen offset:2048 lds\n" \
"buffer_load_dwordx4 v5, s[64:67], s75 offen offset:3072 lds\ns_add_u32 m0, s88, 4096\ns_nop 0\nbuffer_load_dwordx4 v5, s[64:67], s76 offen offset:0 lds\nbuffer_load_dwordx4 v5, s[64:67], s77 offen offset:1024 lds\nbuffer_load_dwordx4 v5, s[64:67], s78 offen offset:2048 lds\nbuffer_load_dwordx4 v5, s[64:67], s79 offen offset:3072 lds\ns_add_u32 m0, s32, 65536\n" \
"s_nop 0\nbuffer_load_dwordx4 v6, s[68:71], 0 offen offset:0 lds\nbuffer_load_dwordx4 v6, s[68:71], s89 offen offset:1024 lds\nbuffer_load_dwordx4 v28, s[68:71], s90 offen offset:2048 lds\nbuffer_load_dwordx4 v28, s[68:71], s91 offen offset:3072 lds\ns_add_u32 m0, s32, 81920\ns_nop 0\nbuffer_load_dwordx4 v6, s[68:71], s92 offen offset:0 lds\n" \
"buffer_load_dwordx4 v6, s[68:71], s93 offen offset:1024 lds\nbuffer_load_dwordx4 v28, s[68:71], s94 offen offset:2048 lds\nbuffer_load_dwordx4 v28, s[68:71], s95 offen offset:3072 lds\nv_add_u32 v5, 128, v5\nv_add_u32 v6, s72, v6\nv_add_u32 v28, s72, v28\nbuffer_load_dword a251, v29, s[64:67], 0 offen sc1\n" \
""
#define Z_MAIN \
".macro D v,s,o,i\nbuffer_load_dwordx4 v\\v, s[\\s:\\s+3], \\o offen offset:\\i lds\n.endm\n.macro Ta d,v,o\nds_read_b64_tr_b16 a[\\d:\\d+1], v\\v offset:\\o\n.endm\n.macro Ra d,v,o\nds_read_b128 a[\\d:\\d+3], v\\v offset:\\o\n.endm\n.macro Zvaa d,a,b\nv_mfma_f32_16x16x32_bf16 v[\\d:\\d+3], a[\\a:\\a+3], a[\\b:\\b+3], 0\n.endm\n.macro AR d,a\nv_accvgpr_read_b32 v\\d, a\\a\n.endm\n.macro Zaaa d,a,b\nv_mfma_f32_16x16x32_bf16 a[\\d:\\d+3], a[\\a:\\a+3], a[\\b:\\b+3], 0\n.endm\n.macro Mvaa d,a,b\nv_mfma_f32_16x16x32_bf16 v[\\d:\\d+3], a[\\a:\\a+3], a[\\b:\\b+3], v[\\d:\\d+3]\n.endm\n.macro Maaa d,a,b\nv_mfma_f32_16x16x32_bf16 a[\\d:\\d+3], a[\\a:\\a+3], a[\\b:\\b+3], a[\\d:\\d+3]\n.endm\n" \
".macro C d,a,b\nv_cvt_pk_bf16_f32 v\\d, v\\a, v\\b\n.endm\n.macro AW d,a\nv_accvgpr_write_b32 a\\d, v\\a\n.endm\n.macro DS v,s,o,i\nbuffer_load_dwordx4 v\\v, s[\\s:\\s+3], \\o offen offset:\\i sc1 lds\n.endm\n.macro Qvav d,a,b,c\nv_mfma_f32_16x16x32_bf16 v[\\d:\\d+3], a[\\a:\\a+3], v[\\b:\\b+3], v[\\c:\\c+3]\n.endm\n.macro Mvav d,a,b\nv_mfma_f32_16x16x32_bf16 v[\\d:\\d+3], a[\\a:\\a+3], v[\\b:\\b+3], v[\\d:\\d+3]\n.endm\n.macro E d,a\nv_exp_f32 v\\d, v\\a\n.endm\n.macro Zaav d,a,b\nv_mfma_f32_16x16x32_bf16 a[\\d:\\d+3], a[\\a:\\a+3], v[\\b:\\b+3], 0\n.endm\n.macro Maav d,a,b\nv_mfma_f32_16x16x32_bf16 a[\\d:\\d+3], a[\\a:\\a+3], v[\\b:\\b+3], a[\\d:\\d+3]\n.endm\n" \
".macro Mvvv d,a,b\nv_mfma_f32_16x16x32_bf16 v[\\d:\\d+3], v[\\a:\\a+3], v[\\b:\\b+3], v[\\d:\\d+3]\n.endm\n.macro Zvav d,a,b\nv_mfma_f32_16x16x32_bf16 v[\\d:\\d+3], a[\\a:\\a+3], v[\\b:\\b+3], 0\n.endm\n.macro Rv d,v,o\nds_read_b128 v[\\d:\\d+3], v\\v offset:\\o\n.endm\n.macro Mvva d,a,b\nv_mfma_f32_16x16x32_bf16 v[\\d:\\d+3], v[\\a:\\a+3], a[\\b:\\b+3], v[\\d:\\d+3]\n.endm\n.macro Qvaa d,a,b,c\nv_mfma_f32_16x16x32_bf16 v[\\d:\\d+3], a[\\a:\\a+3], a[\\b:\\b+3], v[\\c:\\c+3]\n.endm\n.macro Zvvv d,a,b\nv_mfma_f32_16x16x32_bf16 v[\\d:\\d+3], v[\\a:\\a+3], v[\\b:\\b+3], 0\n.endm\n.macro Qvva d,a,b,c\nv_mfma_f32_16x16x32_bf16 v[\\d:\\d+3], v[\\a:\\a+3], a[\\b:\\b+3], v[\\c:\\c+3]\n.endm\ns_load_dwordx4 s[12:15], s[0:1], 0x40\n" \
"s_load_dwordx2 s[16:17], s[0:1], 0x50\ns_lshl_b32 s88, s32, 1\nv_xor_b32 v28, 128, v6\ns_mov_b32 s73, 7168\ns_mov_b32 s74, 14336\ns_mov_b32 s75, 21504\ns_mov_b32 s76, 32768\ns_mov_b32 s77, 39936\n" \
"s_mov_b32 s78, 47104\ns_mov_b32 s79, 54272\ns_mov_b32 s89, 31744\ns_mov_b32 s90, 63488\ns_mov_b32 s91, 95232\ns_mov_b32 s92, 256\ns_mov_b32 s93, 32000\ns_mov_b32 s94, 63744\n" \
"s_mov_b32 s95, 95488\ns_mov_b32 s72, 524288\nv_xor_b32 v20, 32, v8\nv_xor_b32 v21, 64, v8\nv_xor_b32 v22, 96, v8\nv_xor_b32 v23, 128, v8\nv_xor_b32 v24, 160, v8\nv_xor_b32 v25, 192, v8\n" \
"v_xor_b32 v26, 224, v8\nv_xor_b32 v27, 64, v7\ns_add_u32 m0, s88, 32768\ns_nop 0\nD 5,64,0,0\nD 5,64,s73,1024\nD 5,64,s74,2048\nD 5,64,s75,3072\n" \
"s_add_u32 m0, s88, 36864\ns_nop 0\nD 5,64,s76,0\nD 5,64,s77,1024\nD 5,64,s78,2048\nD 5,64,s79,3072\ns_and_b32 s100, s33, 3\ns_cmp_eq_u32 s100, 1\n" \
"s_cbranch_scc1 zu1\ns_cmp_eq_u32 s100, 2\ns_cbranch_scc1 zu2\ns_cmp_eq_u32 s100, 3\ns_cbranch_scc1 zu3\ns_getpc_b64 s[96:97]\n1:\ns_cmp_eq_u32 s32, 4096\n" \
"s_cbranch_scc1 2f\ns_cmp_eq_u32 s32, 8192\ns_cbranch_scc1 3f\ns_cmp_eq_u32 s32, 12288\ns_cbranch_scc1 4f\ns_add_u32 s100, s96, zp0-1b\ns_addc_u32 s101, s97, 0\ns_swappc_b64 s[98:99], s[100:101]\n" \
"s_add_u32 s100, s96, zp4-1b\ns_addc_u32 s101, s97, 0\ns_swappc_b64 s[98:99], s[100:101]\ns_add_u32 s100, s96, zp8-1b\ns_addc_u32 s101, s97, 0\ns_swappc_b64 s[98:99], s[100:101]\ns_add_u32 s100, s96, zp12-1b\ns_addc_u32 s101, s97, 0\n" \
"s_swappc_b64 s[98:99], s[100:101]\ns_branch 9f\n2:\ns_add_u32 s100, s96, zp1-1b\ns_addc_u32 s101, s97, 0\ns_swappc_b64 s[98:99], s[100:101]\ns_add_u32 s100, s96, zp5-1b\ns_addc_u32 s101, s97, 0\n" \
"s_swappc_b64 s[98:99], s[100:101]\ns_add_u32 s100, s96, zp9-1b\ns_addc_u32 s101, s97, 0\ns_swappc_b64 s[98:99], s[100:101]\ns_add_u32 s100, s96, zp13-1b\ns_addc_u32 s101, s97, 0\ns_swappc_b64 s[98:99], s[100:101]\ns_branch 9f\n" \
"3:\ns_add_u32 s100, s96, zp2-1b\ns_addc_u32 s101, s97, 0\ns_swappc_b64 s[98:99], s[100:101]\ns_add_u32 s100, s96, zp6-1b\ns_addc_u32 s101, s97, 0\ns_swappc_b64 s[98:99], s[100:101]\ns_add_u32 s100, s96, zp10-1b\n" \
"s_addc_u32 s101, s97, 0\ns_swappc_b64 s[98:99], s[100:101]\ns_branch 9f\n4:\ns_add_u32 s100, s96, zp3-1b\ns_addc_u32 s101, s97, 0\ns_swappc_b64 s[98:99], s[100:101]\ns_add_u32 s100, s96, zp7-1b\n" \
"s_addc_u32 s101, s97, 0\ns_swappc_b64 s[98:99], s[100:101]\ns_add_u32 s100, s96, zp11-1b\ns_addc_u32 s101, s97, 0\ns_swappc_b64 s[98:99], s[100:101]\n9:\ns_branch zu4\nzu1:\n" \
"s_getpc_b64 s[96:97]\n1:\ns_cmp_eq_u32 s32, 4096\ns_cbranch_scc1 2f\ns_cmp_eq_u32 s32, 8192\ns_cbranch_scc1 3f\ns_cmp_eq_u32 s32, 12288\ns_cbranch_scc1 4f\n" \
"s_add_u32 s100, s96, zp0-1b\ns_addc_u32 s101, s97, 0\ns_swappc_b64 s[98:99], s[100:101]\ns_add_u32 s100, s96, zp24-1b\ns_addc_u32 s101, s97, 0\ns_swappc_b64 s[98:99], s[100:101]\ns_add_u32 s100, s96, zp7-1b\ns_addc_u32 s101, s97, 0\n" \
"s_swappc_b64 s[98:99], s[100:101]\ns_add_u32 s100, s96, zp11-1b\ns_addc_u32 s101, s97, 0\ns_swappc_b64 s[98:99], s[100:101]\ns_branch 9f\n2:\ns_add_u32 s100, s96, zp21-1b\ns_addc_u32 s101, s97, 0\n" \
"s_swappc_b64 s[98:99], s[100:101]\ns_add_u32 s100, s96, zp4-1b\ns_addc_u32 s101, s97, 0\ns_swappc_b64 s[98:99], s[100:101]\ns_add_u32 s100, s96, zp8-1b\ns_addc_u32 s101, s97, 0\ns_swappc_b64 s[98:99], s[100:101]\ns_add_u32 s100, s96, zp12-1b\n" \
"s_addc_u32 s101, s97, 0\ns_swappc_b64 s[98:99], s[100:101]\ns_branch 9f\n3:\ns_add_u32 s100, s96, zp22-1b\ns_addc_u32 s101, s97, 0\ns_swappc_b64 s[98:99], s[100:101]\ns_add_u32 s100, s96, zp5-1b\n" \
"s_addc_u32 s101, s97, 0\ns_swappc_b64 s[98:99], s[100:101]\ns_add_u32 s100, s96, zp9-1b\ns_addc_u32 s101, s97, 0\ns_swappc_b64 s[98:99], s[100:101]\ns_add_u32 s100, s96, zp13-1b\ns_addc_u32 s101, s97, 0\ns_swappc_b64 s[98:99], s[100:101]\n" \
"s_branch 9f\n4:\ns_add_u32 s100, s96, zp23-1b\ns_addc_u32 s101, s97, 0\ns_swappc_b64 s[98:99], s[100:101]\ns_add_u32 s100, s96, zp6-1b\ns_addc_u32 s101, s97, 0\ns_swappc_b64 s[98:99], s[100:101]\n" \
"s_add_u32 s100, s96, zp10-1b\ns_addc_u32 s101, s97, 0\ns_swappc_b64 s[98:99], s[100:101]\n9:\ns_branch zu4\nzu2:\ns_getpc_b64 s[96:97]\n1:\n" \
"s_cmp_eq_u32 s32, 4096\ns_cbranch_scc1 2f\ns_cmp_eq_u32 s32, 8192\ns_cbranch_scc1 3f\ns_cmp_eq_u32 s32, 12288\ns_cbranch_scc1 4f\ns_add_u32 s100, s96, zp0-1b\ns_addc_u32 s101, s97, 0\n" \
"s_swappc_b64 s[98:99], s[100:101]\ns_add_u32 s100, s96, zp28-1b\ns_addc_u32 s101, s97, 0\ns_swappc_b64 s[98:99], s[100:101]\ns_add_u32 s100, s96, zp32-1b\ns_addc_u32 s101, s97, 0\ns_swappc_b64 s[98:99], s[100:101]\ns_add_u32 s100, s96, zp11-1b\n" \
"s_addc_u32 s101, s97, 0\ns_swappc_b64 s[98:99], s[100:101]\ns_add_u32 s100, s96, zp37-1b\ns_addc_u32 s101, s97, 0\ns_swappc_b64 s[98:99], s[100:101]\ns_branch 9f\n2:\ns_add_u32 s100, s96, zp1-1b\n" \
"s_addc_u32 s101, s97, 0\ns_swappc_b64 s[98:99], s[100:101]\ns_add_u32 s100, s96, zp29-1b\ns_addc_u32 s101, s97, 0\ns_swappc_b64 s[98:99], s[100:101]\ns_add_u32 s100, s96, zp33-1b\ns_addc_u32 s101, s97, 0\ns_swappc_b64 s[98:99], s[100:101]\n" \
"s_add_u32 s100, s96, zp12-1b\ns_addc_u32 s101, s97, 0\ns_swappc_b64 s[98:99], s[100:101]\ns_branch 9f\n3:\ns_add_u32 s100, s96, zp26-1b\ns_addc_u32 s101, s97, 0\ns_swappc_b64 s[98:99], s[100:101]\n" \
"s_add_u32 s100, s96, zp30-1b\ns_addc_u32 s101, s97, 0\ns_swappc_b64 s[98:99], s[100:101]\ns_add_u32 s100, s96, zp34-1b\ns_addc_u32 s101, s97, 0\ns_swappc_b64 s[98:99], s[100:101]\ns_add_u32 s100, s96, zp13-1b\ns_addc_u32 s101, s97, 0\n" \
"s_swappc_b64 s[98:99], s[100:101]\ns_branch 9f\n4:\ns_add_u32 s100, s96, zp27-1b\ns_addc_u32 s101, s97, 0\ns_swappc_b64 s[98:99], s[100:101]\ns_add_u32 s100, s96, zp31-1b\ns_addc_u32 s101, s97, 0\n" \
"s_swappc_b64 s[98:99], s[100:101]\ns_add_u32 s100, s96, zp35-1b\ns_addc_u32 s101, s97, 0\ns_swappc_b64 s[98:99], s[100:101]\ns_add_u32 s100, s96, zp36-1b\ns_addc_u32 s101, s97, 0\ns_swappc_b64 s[98:99], s[100:101]\n9:\n" \
"s_branch zu4\nzu3:\ns_getpc_b64 s[96:97]\n1:\ns_cmp_eq_u32 s32, 4096\ns_cbranch_scc1 2f\ns_cmp_eq_u32 s32, 8192\ns_cbranch_scc1 3f\n" \
"s_cmp_eq_u32 s32, 12288\ns_cbranch_scc1 4f\ns_add_u32 s100, s96, zp0-1b\ns_addc_u32 s101, s97, 0\ns_swappc_b64 s[98:99], s[100:101]\ns_add_u32 s100, s96, zp26-1b\ns_addc_u32 s101, s97, 0\ns_swappc_b64 s[98:99], s[100:101]\n" \
"s_add_u32 s100, s96, zp30-1b\ns_addc_u32 s101, s97, 0\ns_swappc_b64 s[98:99], s[100:101]\ns_add_u32 s100, s96, zp34-1b\ns_addc_u32 s101, s97, 0\ns_swappc_b64 s[98:99], s[100:101]\ns_add_u32 s100, s96, zp13-1b\ns_addc_u32 s101, s97, 0\n" \
"s_swappc_b64 s[98:99], s[100:101]\ns_branch 9f\n2:\ns_add_u32 s100, s96, zp21-1b\ns_addc_u32 s101, s97, 0\ns_swappc_b64 s[98:99], s[100:101]\ns_add_u32 s100, s96, zp27-1b\ns_addc_u32 s101, s97, 0\n" \
"s_swappc_b64 s[98:99], s[100:101]\ns_add_u32 s100, s96, zp31-1b\ns_addc_u32 s101, s97, 0\ns_swappc_b64 s[98:99], s[100:101]\ns_add_u32 s100, s96, zp35-1b\ns_addc_u32 s101, s97, 0\ns_swappc_b64 s[98:99], s[100:101]\ns_add_u32 s100, s96, zp36-1b\n" \
"s_addc_u32 s101, s97, 0\ns_swappc_b64 s[98:99], s[100:101]\ns_branch 9f\n3:\ns_add_u32 s100, s96, zp22-1b\ns_addc_u32 s101, s97, 0\ns_swappc_b64 s[98:99], s[100:101]\ns_add_u32 s100, s96, zp28-1b\n" \
"s_addc_u32 s101, s97, 0\ns_swappc_b64 s[98:99], s[100:101]\ns_add_u32 s100, s96, zp32-1b\ns_addc_u32 s101, s97, 0\ns_swappc_b64 s[98:99], s[100:101]\ns_add_u32 s100, s96, zp11-1b\ns_addc_u32 s101, s97, 0\ns_swappc_b64 s[98:99], s[100:101]\n" \
"s_add_u32 s100, s96, zp37-1b\ns_addc_u32 s101, s97, 0\ns_swappc_b64 s[98:99], s[100:101]\ns_branch 9f\n4:\ns_add_u32 s100, s96, zp25-1b\ns_addc_u32 s101, s97, 0\ns_swappc_b64 s[98:99], s[100:101]\n" \
"s_add_u32 s100, s96, zp29-1b\ns_addc_u32 s101, s97, 0\ns_swappc_b64 s[98:99], s[100:101]\ns_add_u32 s100, s96, zp33-1b\ns_addc_u32 s101, s97, 0\ns_swappc_b64 s[98:99], s[100:101]\ns_add_u32 s100, s96, zp12-1b\ns_addc_u32 s101, s97, 0\n" \
"s_swappc_b64 s[98:99], s[100:101]\n9:\nzu4:\ns_branch 99f\n.p2align 12\nzp0:\ns_setpc_b64 s[98:99]\n99:\n" \
"s_waitcnt vmcnt(9)\ns_barrier\ns_add_u32 m0, s32, 98304\nTa 64,8,0\nTa 66,8,1024\nD 6,68,0,0\nRa 0,7,0\nTa 68,20,0\n" \
"D 6,68,s89,1024\nTa 70,20,1024\nRa 4,7,2048\nD 28,68,s90,2048\nTa 72,21,0\nTa 74,21,1024\nD 28,68,s91,3072\ns_add_u32 m0, s32, 114688\n" \
"Ra 8,7,4096\nTa 76,22,0\nD 6,68,s92,0\nTa 78,22,1024\nRa 12,7,6144\nD 6,68,s93,1024\nTa 80,23,0\nTa 82,23,1024\n" \
"D 28,68,s94,2048\nRa 16,7,8192\nTa 84,24,0\nD 28,68,s95,3072\nTa 86,24,1024\nRa 20,7,10240\nTa 88,25,0\nTa 90,25,1024\n" \
"Ra 24,7,12288\nTa 92,26,0\nTa 94,26,1024\nRa 28,7,14336\nv_add_u32 v5, 128, v5\nv_add_u32 v6, s72, v6\nv_add_u32 v28, s72, v28\nbuffer_load_dword v30, v4, s[52:55], 0 offen sc1\n" \
"v_add_u32 v4, s44, v4\ns_bitcmp1_b32 s33, 0\ns_waitcnt lgkmcnt(0)\ns_cbranch_scc1 zV\nZvaa 32,64,0\nTa 96,8,8192\ns_lshr_b32 s7, s3, 5\nZvaa 64,64,4\n" \
"Ta 98,8,9216\ns_lshr_b32 s5, 170, s7\nZvaa 68,68,4\nRa 32,27,0\ns_and_b32 s5, s5, 1\nZvaa 36,68,0\nTa 100,20,8192\ns_lshl_b32 s7, s7, 2\n" \
"Zvaa 40,72,0\nTa 102,20,9216\ns_lshr_b32 s4, 0x12124444, s7\nZvaa 72,72,4\nRa 36,27,2048\ns_and_b32 s4, s4, 15\nZvaa 76,76,4\nTa 104,21,8192\n" \
"s_xor_b32 s6, s5, s3\nZvaa 44,76,0\nTa 106,21,9216\ns_lshr_b32 s7, 0xff00, s4\nZvaa 48,80,0\nRa 40,27,4096\ns_and_b32 s7, s7, 0xff\nZvaa 80,80,4\n" \
"Ta 108,22,8192\ns_xor_b32 s10, s7, 0xff\nZvaa 84,84,4\nTa 110,22,9216\ns_lshl_b32 s7, s7, 8\nZvaa 52,84,0\nRa 44,27,6144\ns_or_b32 s7, s7, 0xff\n" \
"Zvaa 56,88,0\nTa 112,23,8192\ns_add_u32 s8, s4, 5\nZvaa 88,88,4\nTa 114,23,9216\ns_sub_u32 s4, 262, s4\nZvaa 92,92,4\nRa 48,27,8192\n" \
"s_add_u32 s9, s4, 0x101\nZvaa 60,92,0\nTa 116,24,8192\nZvaa 124,92,8\nTa 118,24,9216\nZvaa 156,92,12\nRa 52,27,10240\nZvaa 152,88,12\n" \
"Ta 120,25,8192\nZvaa 120,88,8\nTa 122,25,9216\nZvaa 116,84,8\nRa 56,27,12288\nZvaa 148,84,12\nTa 124,26,8192\nZvaa 144,80,12\n" \
"Ta 126,26,9216\nZvaa 112,80,8\nRa 60,27,14336\nZvaa 108,76,8\nAR 2,252\nAR 3,253\nZvaa 140,76,12\nv_readfirstlane_b32 s96, v2\n" \
"v_readfirstlane_b32 s97, v3\nZvaa 136,72,12\ns_mul_i32 s97, s97, 0x7f4a7c15\ns_mul_hi_u32 s98, s96, 0x7f4a7c15\nZvaa 104,72,8\ns_add_u32 s97, s97, s98\ns_mul_i32 s98, s96, 0x9e3779b9\nZvaa 100,68,8\n" \
"s_add_u32 s97, s97, s98\ns_mul_i32 s96, s96, 0x7f4a7c15\nZvaa 132,68,12\ns_add_u32 s96, s96, 0xd9b4e019\ns_addc_u32 s97, s97, 0x632be59b\nZvaa 128,64,12\nv_mov_b32 v2, s96\nv_mov_b32 v3, s97\n" \
"Zvaa 96,64,8\ns_mov_b32 s96, 3\nZvaa 160,64,16\ns_waitcnt lgkmcnt(0)\ns_barrier\nZvaa 192,64,20\ns_add_u32 m0, s88, 0\nZvaa 196,68,20\n" \
"D 5,64,0,0\nZvaa 164,68,16\ns_cmp_lt_u32 s3, 128\ns_cselect_b32 s4, s9, s4\nZvaa 168,72,16\ns_bitcmp1_b32 s6, 0\ns_cselect_b32 s9, s4, s8\ns_cselect_b32 s7, s10, s7\n" \
"Zvaa 200,72,20\ns_and_b32 s6, s6, 1\ns_lshr_b32 s10, s3, 1\nZvaa 204,76,20\nD 5,64,s73,1024\nZvaa 172,76,16\ns_and_b32 s10, s10, 2\ns_or_b32 s6, s6, s10\n" \
"Zvaa 176,80,16\ns_lshl_b32 s6, s6, 3\ns_lshl_b32 s8, s7, s6\nZvaa 208,80,20\ns_bfe_u32 s10, s3, 0x20003\ns_add_u32 s6, s6, s10\nZvaa 212,84,20\nD 5,64,s74,2048\n" \
"Zvaa 180,84,16\ns_bitset0_b32 s8, s6\ns_lshl_b32 s5, s5, 16\nZvaa 184,88,16\ns_or_b32 s9, s9, s5\nZvaa 216,88,20\nZvaa 220,92,20\nD 5,64,s75,3072\n" \
"Zvaa 188,92,16\nZvaa 252,92,24\nZaaa 156,92,28\ns_add_u32 m0, s88, 4096\nZaaa 152,88,28\nD 5,64,s76,0\nZvaa 248,88,24\nZvaa 244,84,24\n" \
"Zaaa 148,84,28\nZaaa 144,80,28\nD 5,64,s77,1024\nZvaa 240,80,24\nZvaa 236,76,24\nZaaa 140,76,28\nZaaa 136,72,28\nD 5,64,s78,2048\n" \
"Zvaa 232,72,24\nZvaa 228,68,24\nZaaa 132,68,28\nZaaa 128,64,28\nD 5,64,s79,3072\nZvaa 224,64,24\ns_branch 99f\n.p2align 12\n" \
"zp1:\ns_setpc_b64 s[98:99]\n99:\ns_nop 0\nzgK:\nMvaa 32,96,32\nMvaa 64,96,36\ns_add_u32 m0, s32, 65536\n" \
"Mvaa 68,100,36\nD 6,68,0,0\nMvaa 36,100,32\nMvaa 40,104,32\nMvaa 72,104,36\nMvaa 76,108,36\nD 6,68,s89,1024\nMvaa 44,108,32\n" \
"Mvaa 48,112,32\nMvaa 80,112,36\nMvaa 84,116,36\nD 28,68,s90,2048\nMvaa 52,116,32\nMvaa 56,120,32\nMvaa 88,120,36\nMvaa 92,124,36\n" \
"D 28,68,s91,3072\nMvaa 60,124,32\nMvaa 124,124,40\nMvaa 156,124,44\ns_add_u32 m0, s32, 81920\nMvaa 152,120,44\nD 6,68,s92,0\nMvaa 120,120,40\n" \
"Mvaa 116,116,40\nMvaa 148,116,44\nMvaa 144,112,44\nD 6,68,s93,1024\nMvaa 112,112,40\nMvaa 108,108,40\nMvaa 140,108,44\nMvaa 136,104,44\n" \
"D 28,68,s94,2048\nMvaa 104,104,40\nMvaa 100,100,40\nMvaa 132,100,44\nMvaa 128,96,44\nD 28,68,s95,3072\nMvaa 96,96,40\ns_waitcnt vmcnt(17)\n" \
"s_barrier\nMvaa 160,96,48\nbuffer_load_dword v30, v4, s[52:55], 0 offen sc1\nTa 64,8,32768\nv_add_u32 v5, 128, v5\nMvaa 192,96,52\nTa 66,8,33792\nv_add_u32 v6, s72, v6\n" \
"Mvaa 196,100,52\nRa 0,7,32768\nv_add_u32 v28, s72, v28\nMvaa 164,100,48\nTa 68,20,32768\nMvaa 168,104,48\nTa 70,20,33792\nMvaa 200,104,52\n" \
"Ra 4,7,34816\nMvaa 204,108,52\nv_add_u32 v4, s44, v4\nTa 72,21,32768\nMvaa 172,108,48\nTa 74,21,33792\nMvaa 176,112,48\nRa 8,7,36864\n" \
"Mvaa 208,112,52\nTa 76,22,32768\nMvaa 212,116,52\nTa 78,22,33792\nMvaa 180,116,48\nRa 12,7,38912\nMvaa 184,120,48\nTa 80,23,32768\n" \
"Mvaa 216,120,52\nTa 82,23,33792\nMvaa 220,124,52\nRa 16,7,40960\nMvaa 188,124,48\nTa 84,24,32768\nMvaa 252,124,56\nTa 86,24,33792\n" \
"Maaa 156,124,60\nRa 20,7,43008\nMaaa 152,120,60\nTa 88,25,32768\nMvaa 248,120,56\nTa 90,25,33792\nMvaa 244,116,56\nRa 24,7,45056\n" \
"Maaa 148,116,60\nTa 92,26,32768\nMaaa 144,112,60\nTa 94,26,33792\nMvaa 240,112,56\nRa 28,7,47104\nMvaa 236,108,56\nMaaa 140,108,60\n" \
"Maaa 136,104,60\nMvaa 232,104,56\nMvaa 228,100,56\nMaaa 132,100,60\nMaaa 128,96,60\nMvaa 224,96,56\ns_waitcnt lgkmcnt(0)\nMvaa 32,64,0\n" \
"Ta 96,8,40960\nMvaa 64,64,4\nTa 98,8,41984\nMvaa 68,68,4\nRa 32,27,32768\nMvaa 36,68,0\nTa 100,20,40960\nMvaa 40,72,0\n" \
"Ta 102,20,41984\nMvaa 72,72,4\nRa 36,27,34816\nMvaa 76,76,4\nTa 104,21,40960\nMvaa 44,76,0\nTa 106,21,41984\nMvaa 48,80,0\n" \
"Ra 40,27,36864\nMvaa 80,80,4\nTa 108,22,40960\nMvaa 84,84,4\nTa 110,22,41984\nMvaa 52,84,0\nRa 44,27,38912\nMvaa 56,88,0\n" \
"Ta 112,23,40960\nMvaa 88,88,4\nTa 114,23,41984\nMvaa 92,92,4\nRa 48,27,40960\nMvaa 60,92,0\nTa 116,24,40960\nMvaa 124,92,8\n" \
"Ta 118,24,41984\nMvaa 156,92,12\nRa 52,27,43008\nMvaa 152,88,12\nTa 120,25,40960\nMvaa 120,88,8\nTa 122,25,41984\nMvaa 116,84,8\n" \
"Ra 56,27,45056\nMvaa 148,84,12\nTa 124,26,40960\nMvaa 144,80,12\nTa 126,26,41984\nMvaa 112,80,8\nRa 60,27,47104\nMvaa 108,76,8\n" \
"Mvaa 140,76,12\nMvaa 136,72,12\nMvaa 104,72,8\nMvaa 100,68,8\nMvaa 132,68,12\nMvaa 128,64,12\nMvaa 96,64,8\nMvaa 160,64,16\n" \
"s_waitcnt lgkmcnt(0)\ns_barrier\nMvaa 192,64,20\ns_add_u32 m0, s88, 32768\nMvaa 196,68,20\nD 5,64,0,0\nMvaa 164,68,16\nMvaa 168,72,16\n" \
"Mvaa 200,72,20\nMvaa 204,76,20\nD 5,64,s73,1024\nMvaa 172,76,16\nMvaa 176,80,16\nMvaa 208,80,20\nMvaa 212,84,20\nD 5,64,s74,2048\n" \
"Mvaa 180,84,16\nMvaa 184,88,16\nMvaa 216,88,20\nMvaa 220,92,20\nD 5,64,s75,3072\nMvaa 188,92,16\nMvaa 252,92,24\nMaaa 156,92,28\n" \
"s_add_u32 m0, s88, 36864\nMaaa 152,88,28\nD 5,64,s76,0\nMvaa 248,88,24\nMvaa 244,84,24\nMaaa 148,84,28\nMaaa 144,80,28\nD 5,64,s77,1024\n" \
"Mvaa 240,80,24\nMvaa 236,76,24\nMaaa 140,76,28\nMaaa 136,72,28\nD 5,64,s78,2048\nMvaa 232,72,24\nMvaa 228,68,24\nMaaa 132,68,28\n" \
"Maaa 128,64,28\nD 5,64,s79,3072\nMvaa 224,64,24\nMvaa 32,96,32\nMvaa 64,96,36\ns_add_u32 m0, s32, 98304\nMvaa 68,100,36\nD 6,68,0,0\n" \
"Mvaa 36,100,32\nMvaa 40,104,32\nMvaa 72,104,36\nMvaa 76,108,36\nD 6,68,s89,1024\nMvaa 44,108,32\nMvaa 48,112,32\nMvaa 80,112,36\n" \
"Mvaa 84,116,36\nD 28,68,s90,2048\nMvaa 52,116,32\nMvaa 56,120,32\nMvaa 88,120,36\nMvaa 92,124,36\nD 28,68,s91,3072\nMvaa 60,124,32\n" \
"Mvaa 124,124,40\nMvaa 156,124,44\ns_add_u32 m0, s32, 114688\nMvaa 152,120,44\nD 6,68,s92,0\nMvaa 120,120,40\nMvaa 116,116,40\nMvaa 148,116,44\n" \
"Mvaa 144,112,44\nD 6,68,s93,1024\nMvaa 112,112,40\nMvaa 108,108,40\nMvaa 140,108,44\nMvaa 136,104,44\nD 28,68,s94,2048\nMvaa 104,104,40\n" \
"Mvaa 100,100,40\nMvaa 132,100,44\nMvaa 128,96,44\nD 28,68,s95,3072\nMvaa 96,96,40\ns_waitcnt vmcnt(17)\ns_barrier\nMvaa 160,96,48\n" \
"buffer_load_dword v30, v4, s[52:55], 0 offen sc1\nTa 64,8,0\nv_add_u32 v5, 128, v5\nMvaa 192,96,52\nTa 66,8,1024\nv_add_u32 v6, s72, v6\nMvaa 196,100,52\nRa 0,7,0\n" \
"v_add_u32 v28, s72, v28\nMvaa 164,100,48\nTa 68,20,0\nMvaa 168,104,48\nTa 70,20,1024\nMvaa 200,104,52\nRa 4,7,2048\nMvaa 204,108,52\n" \
"v_add_u32 v4, s44, v4\nTa 72,21,0\nMvaa 172,108,48\nTa 74,21,1024\nMvaa 176,112,48\nRa 8,7,4096\nMvaa 208,112,52\nTa 76,22,0\n" \
"Mvaa 212,116,52\nTa 78,22,1024\nMvaa 180,116,48\nRa 12,7,6144\nMvaa 184,120,48\nTa 80,23,0\nMvaa 216,120,52\nTa 82,23,1024\n" \
"Mvaa 220,124,52\nRa 16,7,8192\nMvaa 188,124,48\nTa 84,24,0\nMvaa 252,124,56\nTa 86,24,1024\nMaaa 156,124,60\nRa 20,7,10240\n" \
"Maaa 152,120,60\nTa 88,25,0\nMvaa 248,120,56\nTa 90,25,1024\nMvaa 244,116,56\nRa 24,7,12288\nMaaa 148,116,60\nTa 92,26,0\n" \
"Maaa 144,112,60\nTa 94,26,1024\nMvaa 240,112,56\nRa 28,7,14336\nMvaa 236,108,56\nMaaa 140,108,60\nMaaa 136,104,60\nMvaa 232,104,56\n" \
"s_sub_u32 s96, s96, 1\nMvaa 228,100,56\ns_cmp_lg_u32 s96, 0\nMaaa 132,100,60\nMaaa 128,96,60\nMvaa 224,96,56\ns_waitcnt lgkmcnt(0)\ns_cbranch_scc0 zhK\n" \
"Mvaa 32,64,0\nTa 96,8,8192\nMvaa 64,64,4\nTa 98,8,9216\nMvaa 68,68,4\nRa 32,27,0\nMvaa 36,68,0\nTa 100,20,8192\n" \
"Mvaa 40,72,0\nTa 102,20,9216\nMvaa 72,72,4\nRa 36,27,2048\nMvaa 76,76,4\nTa 104,21,8192\nMvaa 44,76,0\nTa 106,21,9216\n" \
"Mvaa 48,80,0\nRa 40,27,4096\nMvaa 80,80,4\nTa 108,22,8192\nMvaa 84,84,4\nTa 110,22,9216\nMvaa 52,84,0\nRa 44,27,6144\n" \
"Mvaa 56,88,0\nTa 112,23,8192\nMvaa 88,88,4\nTa 114,23,9216\nMvaa 92,92,4\nRa 48,27,8192\nMvaa 60,92,0\nTa 116,24,8192\n" \
"Mvaa 124,92,8\nTa 118,24,9216\nMvaa 156,92,12\nRa 52,27,10240\nMvaa 152,88,12\nTa 120,25,8192\nMvaa 120,88,8\nTa 122,25,9216\n" \
"Mvaa 116,84,8\nRa 56,27,12288\nMvaa 148,84,12\nTa 124,26,8192\nMvaa 144,80,12\nTa 126,26,9216\nMvaa 112,80,8\nRa 60,27,14336\n" \
"Mvaa 108,76,8\nMvaa 140,76,12\nMvaa 136,72,12\nMvaa 104,72,8\nMvaa 100,68,8\nMvaa 132,68,12\nMvaa 128,64,12\nMvaa 96,64,8\n" \
"Mvaa 160,64,16\ns_waitcnt lgkmcnt(0)\ns_barrier\nMvaa 192,64,20\ns_add_u32 m0, s88, 0\nMvaa 196,68,20\nD 5,64,0,0\nMvaa 164,68,16\n" \
"Mvaa 168,72,16\nMvaa 200,72,20\nMvaa 204,76,20\nD 5,64,s73,1024\nMvaa 172,76,16\nMvaa 176,80,16\nMvaa 208,80,20\nMvaa 212,84,20\n" \
"D 5,64,s74,2048\nMvaa 180,84,16\nMvaa 184,88,16\nMvaa 216,88,20\nMvaa 220,92,20\nD 5,64,s75,3072\nMvaa 188,92,16\nMvaa 252,92,24\n" \
"Maaa 156,92,28\ns_add_u32 m0, s88, 4096\nMaaa 152,88,28\nD 5,64,s76,0\nMvaa 248,88,24\nMvaa 244,84,24\nMaaa 148,84,28\nMaaa 144,80,28\n" \
"D 5,64,s77,1024\nMvaa 240,80,24\nMvaa 236,76,24\nMaaa 140,76,28\nMaaa 136,72,28\nD 5,64,s78,2048\nMvaa 232,72,24\nMvaa 228,68,24\n" \
"Maaa 132,68,28\nMaaa 128,64,28\nD 5,64,s79,3072\nMvaa 224,64,24\ns_branch zgK\nzhK:\ns_bitcmp1_b32 s33, 1\ns_cbranch_scc1 zxK\n" \
"Mvaa 32,64,0\nTa 96,8,8192\ns_mov_b64 s[64:65], s[84:85]\nMvaa 64,64,4\nTa 98,8,9216\ns_mov_b32 s66, 4194304\nMvaa 68,68,4\nRa 32,27,0\n" \
"s_mov_b64 s[68:69], s[86:87]\nMvaa 36,68,0\nTa 100,20,8192\ns_mov_b32 s70, 8388608\nMvaa 40,72,0\nTa 102,20,9216\ns_mov_b32 s73, 15360\nMvaa 72,72,4\n" \
"Ra 36,27,2048\ns_mov_b32 s74, 129024\nMvaa 76,76,4\nTa 104,21,8192\ns_mov_b32 s75, 144384\nMvaa 44,76,0\nTa 106,21,9216\ns_add_u32 s76, s37, 0\n" \
"Mvaa 48,80,0\nRa 40,27,4096\ns_add_u32 s77, s37, 15360\nMvaa 80,80,4\nTa 108,22,8192\ns_add_u32 s78, s37, 129024\nMvaa 84,84,4\nTa 110,22,9216\n" \
"s_add_u32 s79, s37, 144384\nMvaa 52,84,0\nRa 44,27,6144\ns_mov_b32 s89, 31744\nMvaa 56,88,0\nTa 112,23,8192\ns_mov_b32 s90, 63488\nMvaa 88,88,4\n" \
"Ta 114,23,9216\ns_mov_b32 s91, 95232\nMvaa 92,92,4\nRa 48,27,8192\ns_mov_b32 s72, 524288\nMvaa 60,92,0\nTa 116,24,8192\nv_xor_b32 v28, 128, v14\n" \
"Mvaa 124,92,8\nTa 118,24,9216\nv_mov_b32 v4, v15\nMvaa 156,92,12\nRa 52,27,10240\nMvaa 152,88,12\nTa 120,25,8192\nMvaa 120,88,8\n" \
"Ta 122,25,9216\nMvaa 116,84,8\nRa 56,27,12288\nMvaa 148,84,12\nTa 124,26,8192\nMvaa 144,80,12\nTa 126,26,9216\nMvaa 112,80,8\n" \
"Ra 60,27,14336\nMvaa 108,76,8\nMvaa 140,76,12\nMvaa 136,72,12\nMvaa 104,72,8\nMvaa 100,68,8\nMvaa 132,68,12\nMvaa 128,64,12\n" \
"Mvaa 96,64,8\nMvaa 160,64,16\ns_waitcnt lgkmcnt(0)\ns_barrier\nMvaa 192,64,20\ns_add_u32 m0, s88, 131072\ns_and_b32 s10, s3, 2\nMvaa 196,68,20\n" \
"D 13,64,0,0\nMvaa 164,68,16\ns_bfe_u32 s100, s3, 0x10002\ns_lshl_b32 s10, s10, 1\nMvaa 168,72,16\ns_bfe_u32 s2, s9, 0x10010\ns_xor_b32 s99, s2, s3\nMvaa 200,72,20\n" \
"s_and_b32 s99, s99, 1\ns_lshl1_add_u32 s99, s100, s99\nMvaa 204,76,20\nD 13,64,s73,1024\nMvaa 172,76,16\ns_bfe_u32 s101, s3, 0x20003\ns_lshl3_add_u32 s11, s99, s101\nMvaa 176,80,16\n" \
"s_add_u32 s101, s101, s10\ns_lshl3_add_u32 s10, s2, s10\nMvaa 208,80,20\ns_bfe_u32 s2, s3, 0x20005\ns_lshl1_add_u32 s2, s2, s100\nMvaa 212,84,20\nD 13,64,s74,2048\nMvaa 180,84,16\n" \
"s_bfe_u32 s23, s9, 0x80008\ns_and_b32 s22, s9, 255\nMvaa 184,88,16\ns_lshr_b32 s6, 4, s99\ns_lshr_b32 s25, s3, 5\nMvaa 216,88,20\ns_lshl_b32 s4, s25, 8\ns_lshl_b32 s18, s25, 21\n" \
"Mvaa 220,92,20\ns_branch 99f\n.p2align 12\nzp2:\ns_setpc_b64 s[98:99]\n99:\nD 13,64,s75,3072\nMvaa 188,92,16\n" \
"s_lshl3_add_u32 s25, s25, s101\ns_lshl_b32 s26, s25, 18\nMvaa 252,92,24\ns_lshl_b32 s27, s25, 7\ns_add_u32 s4, s4, 16384\nMaaa 156,92,28\ns_add_u32 m0, s88, 135168\ns_xor_b32 s25, s11, s10\n" \
"Maaa 152,88,28\nD 13,64,s76,0\nMvaa 248,88,24\ns_lshl3_add_u32 s31, s25, s4\ns_lshl_b32 s5, s2, 7\nMvaa 244,84,24\ns_add_u32 s5, s5, 8192\ns_lshr_b32 s25, s32, 1\n" \
"Maaa 148,84,28\ns_add_u32 s18, s18, s25\ns_add_u32 s18, s18, 54854656\nMaaa 144,80,28\nD 13,64,s77,1024\nMvaa 240,80,24\ns_lshl_b32 s19, s2, 19\ns_add_u32 s19, s19, s25\n" \
"Mvaa 236,76,24\ns_add_u32 s19, s19, 48563200\ns_lshr_b32 s25, s32, 7\nMaaa 140,76,28\ns_add_u32 s27, s27, s25\ns_lshl3_add_u32 s27, s99, s27\nMaaa 136,72,28\nD 13,64,s78,2048\n" \
"Mvaa 232,72,24\ns_add_u32 s27, s27, 32768\ns_lshl1_add_u32 s26, s32, s26\nMvaa 228,68,24\ns_add_u32 s26, s26, 71631872\ns_lshl_b32 s28, s99, 15\nMaaa 132,68,28\ns_add_u32 s24, s26, s28\n" \
"s_sub_u32 s28, 0x10000, s28\nMaaa 128,64,28\nD 13,64,s79,3072\nMvaa 224,64,24\ns_lshl_b32 s25, s100, 17\ns_add_u32 s26, s26, s25\nMvaa 32,96,32\ns_cmp_eq_u32 s99, 0\n" \
"s_cselect_b32 s21, 128, 0\nMvaa 64,96,36\ns_add_u32 m0, s32, 81920\ns_lshl_b32 s25, s21, 10\nMvaa 68,100,36\nD 14,68,0,0\nMvaa 36,100,32\ns_add_u32 s24, s24, s25\n" \
"s_add_u32 s25, s24, 0x20000\nMvaa 40,104,32\ns_lshl_b32 s101, s101, 8\ns_and_b32 s20, s32, 0x2000\nMvaa 72,104,36\ns_lshl3_add_u32 s20, s20, s101\ns_and_b32 s99, s32, 0x1000\nMvaa 76,108,36\n" \
"D 14,68,s89,1024\nMvaa 44,108,32\ns_lshl2_add_u32 s20, s99, s20\ns_lshl4_add_u32 s101, s32, s101\nMvaa 48,112,32\ns_lshl_b32 s2, s2, 20\ns_add_u32 s2, s2, s101\nMvaa 80,112,36\n" \
"s_lshl_b32 s100, s100, 7\ns_add_u32 s101, s101, s100\nMvaa 84,116,36\nD 28,68,s90,2048\nMvaa 52,116,32\ns_add_u32 s101, s101, 2048\ns_lshl_b32 s29, s42, 17\nMvaa 56,120,32\n" \
"s_lshl_b32 s30, s43, 17\ns_add_u32 s29, s29, s101\nMvaa 88,120,36\ns_add_u32 s30, s30, s101\ns_add_u32 s101, s2, 524288\nMvaa 92,124,36\nD 28,68,s91,3072\nMvaa 60,124,32\n" \
"s_cmp_eq_u32 s23, 2\ns_cselect_b32 s29, s2, s29\ns_cselect_b32 s30, s101, s30\ns_cselect_b32 s7, 0xffff, 0\ns_cselect_b32 s9, 0xff, 0\nMvaa 124,124,40\ns_and_b32 s101, s10, 8\ns_lshl_b32 s9, s9, s101\n" \
"Mvaa 156,124,44\ns_add_u32 m0, s88, 0\nv_add_u32 v13, 128, v13\nMvaa 152,120,44\nD 13,64,0,0\nMvaa 120,120,40\ns_mov_b32 s2, zqS-zq0-8192\nMvaa 116,116,40\n" \
"s_cmp_eq_u32 s23, 1\ns_cselect_b32 s2, zqR-zq0-8192, s2\nMvaa 148,116,44\ns_cmp_eq_u32 s23, 0\ns_cselect_b32 s2, zqT-zq0-8192, s2\nMvaa 144,112,44\nD 13,64,s73,1024\nMvaa 112,112,40\n" \
"s_cmp_lt_u32 s32, 0x2000\ns_cselect_b32 s2, 0, s2\nMvaa 108,108,40\ns_add_u32 s2, s2, s32\nMvaa 140,108,44\nMvaa 136,104,44\nD 13,64,s74,2048\nMvaa 104,104,40\n" \
"Mvaa 100,100,40\nMvaa 132,100,44\nMvaa 128,96,44\nD 13,64,s75,3072\nMvaa 96,96,40\ns_waitcnt vmcnt(17)\ns_barrier\nMvaa 160,96,48\n" \
"Ta 64,8,32768\nMvaa 192,96,52\nTa 66,8,33792\nMvaa 196,100,52\nRa 0,7,32768\nMvaa 164,100,48\nTa 68,20,32768\nMvaa 168,104,48\n" \
"Ta 70,20,33792\nMvaa 200,104,52\nRa 4,7,34816\nMvaa 204,108,52\nTa 72,21,32768\nMvaa 172,108,48\nTa 74,21,33792\nMvaa 176,112,48\n" \
"Ra 8,7,36864\nMvaa 208,112,52\nTa 76,22,32768\nMvaa 212,116,52\nTa 78,22,33792\nMvaa 180,116,48\nRa 12,7,38912\nMvaa 184,120,48\n" \
"Ta 80,23,32768\nMvaa 216,120,52\nTa 82,23,33792\nMvaa 220,124,52\nRa 16,7,40960\nMvaa 188,124,48\nTa 84,24,32768\nMvaa 252,124,56\n" \
"Ta 86,24,33792\nMaaa 156,124,60\nRa 20,7,43008\nMaaa 152,120,60\nTa 88,25,32768\nMvaa 248,120,56\nTa 90,25,33792\nMvaa 244,116,56\n" \
"Ra 24,7,45056\nMaaa 148,116,60\nTa 92,26,32768\nMaaa 144,112,60\nTa 94,26,33792\nMvaa 240,112,56\nRa 28,7,47104\nMvaa 236,108,56\n" \
"Maaa 140,108,60\nMaaa 136,104,60\nMvaa 232,104,56\nMvaa 228,100,56\nMaaa 132,100,60\nMaaa 128,96,60\nMvaa 224,96,56\ns_waitcnt lgkmcnt(0)\n" \
"Mvaa 32,64,0\nTa 96,8,40960\nMvaa 64,64,4\nTa 98,8,41984\nMvaa 68,68,4\nRa 32,27,32768\nMvaa 36,68,0\nTa 100,20,40960\n" \
"Mvaa 40,72,0\nTa 102,20,41984\nMvaa 72,72,4\nRa 36,27,34816\nMvaa 76,76,4\nTa 104,21,40960\nMvaa 44,76,0\nTa 106,21,41984\n" \
"Mvaa 48,80,0\nRa 40,27,36864\nMvaa 80,80,4\nTa 108,22,40960\nMvaa 84,84,4\nTa 110,22,41984\nMvaa 52,84,0\nRa 44,27,38912\n" \
"Mvaa 56,88,0\nTa 112,23,40960\nMvaa 88,88,4\nTa 114,23,41984\nMvaa 92,92,4\nRa 48,27,40960\nMvaa 60,92,0\nTa 116,24,40960\n" \
"Mvaa 124,92,8\nTa 118,24,41984\nMvaa 156,92,12\nRa 52,27,43008\nMvaa 152,88,12\nTa 120,25,40960\nMvaa 120,88,8\nTa 122,25,41984\n" \
"Mvaa 116,84,8\nRa 56,27,45056\nMvaa 148,84,12\nTa 124,26,40960\nMvaa 144,80,12\nTa 126,26,41984\nMvaa 112,80,8\nRa 60,27,47104\n" \
"Mvaa 108,76,8\ns_and_b32 s96, s32, 0x1000\ns_lshl_b32 s97, s96, 1\ns_lshl_b32 s96, s96, 2\nMvaa 140,76,12\nv_add_u32 v16, s97, v7\nv_subrev_u32 v20, s96, v8\nv_xor_b32 v17, 64, v16\n" \
"Mvaa 136,72,12\nv_add_u32 v18, 0x20000, v16\nv_add_u32 v19, 0x20000, v17\nv_xor_b32 v21, 32, v20\nMvaa 104,72,8\nv_xor_b32 v22, 64, v20\nv_xor_b32 v23, 96, v20\nv_xor_b32 v24, 128, v20\n" \
"Mvaa 100,68,8\nv_xor_b32 v25, 160, v20\nv_xor_b32 v26, 192, v20\nv_xor_b32 v27, 224, v20\nMvaa 132,68,12\nMvaa 128,64,12\nMvaa 96,64,8\nMvaa 160,64,16\n" \
"s_waitcnt vmcnt(4) lgkmcnt(0)\ns_barrier\nMvaa 192,64,20\ns_add_u32 m0, s88, 4096\nMvaa 196,68,20\nD 13,64,s76,0\nMvaa 164,68,16\nTa 160,20,24576\n" \
"Mvaa 168,72,16\nTa 162,20,25600\nMvaa 200,72,20\nD 13,64,s77,1024\nMvaa 204,76,20\nTa 164,21,24576\nMvaa 172,76,16\nTa 166,21,25600\n" \
"Mvaa 176,80,16\nD 13,64,s78,2048\nMvaa 208,80,20\nTa 168,22,24576\nMvaa 212,84,20\nTa 170,22,25600\nMvaa 180,84,16\nD 13,64,s79,3072\n" \
"Mvaa 184,88,16\nTa 172,23,24576\nMvaa 216,88,20\ns_add_u32 m0, s32, 65536\nv_add_u32 v14, s72, v14\nv_add_u32 v28, s72, v28\nMvaa 220,92,20\nD 14,68,0,0\n" \
"Mvaa 188,92,16\nTa 174,23,25600\nMvaa 252,92,24\nTa 176,24,24576\nMaaa 156,92,28\nD 14,68,s89,1024\nMaaa 152,88,28\nTa 178,24,25600\n" \
"Mvaa 248,88,24\nTa 180,25,24576\nMvaa 244,84,24\nD 28,68,s90,2048\nMaaa 148,84,28\nTa 182,25,25600\nMaaa 144,80,28\nTa 184,26,24576\n" \
"Mvaa 240,80,24\nD 28,68,s91,3072\nMvaa 236,76,24\nTa 186,26,25600\nMaaa 140,76,28\ns_add_u32 m0, s88, 32768\nv_add_u32 v13, 128, v13\nMaaa 136,72,28\n" \
"D 13,64,0,0\nMvaa 232,72,24\nTa 188,27,24576\nMvaa 228,68,24\nTa 190,27,25600\nMaaa 132,68,28\nD 13,64,s73,1024\nMaaa 128,64,28\n" \
"Mvaa 224,64,24\nMvaa 32,96,32\nD 13,64,s74,2048\nMvaa 36,100,32\nTa 64,20,16384\nMvaa 40,104,32\nTa 66,20,17408\nMvaa 44,108,32\n" \
"D 13,64,s75,3072\nMvaa 48,112,32\nMvaa 52,116,32\ns_add_u32 m0, s88, 36864\nMvaa 56,120,32\nD 13,64,s76,0\nMvaa 60,124,32\nRa 0,18,0\n" \
"Mvaa 64,96,36\nMvaa 68,100,36\nD 13,64,s77,1024\nMvaa 72,104,36\nRa 16,19,0\nC 32,32,33\nC 33,34,35\nMvaa 76,108,36\n" \
"C 34,36,37\nC 35,38,39\nC 36,40,41\nMvaa 80,112,36\nD 13,64,s78,2048\nC 37,42,43\nMvaa 84,116,36\nTa 68,21,16384\n" \
"C 38,44,45\nC 39,46,47\nMvaa 88,120,36\nC 40,48,49\nC 41,50,51\nC 42,52,53\nMvaa 92,124,36\nD 13,64,s79,3072\n" \
"C 43,54,55\nMvaa 96,96,40\nTa 70,21,17408\nC 44,56,57\nC 45,58,59\nMvaa 100,100,40\ns_add_u32 m0, s32, 98304\nv_add_u32 v14, s72, v14\n" \
"v_add_u32 v28, s72, v28\nMvaa 104,104,40\nD 14,68,0,0\nC 46,60,61\nMvaa 108,108,40\nC 47,62,63\nC 48,64,65\nC 49,66,67\n" \
"Mvaa 112,112,40\nTa 72,22,16384\nC 50,68,69\nC 51,70,71\nMvaa 116,116,40\nD 14,68,s89,1024\nC 52,72,73\nMvaa 120,120,40\n" \
"C 53,74,75\nC 54,76,77\nC 55,78,79\nMvaa 124,124,40\nTa 74,22,17408\nC 56,80,81\nC 57,82,83\nMvaa 128,96,44\n" \
"D 28,68,s90,2048\nC 58,84,85\nMvaa 132,100,44\nC 59,86,87\nC 60,88,89\nC 61,90,91\nMvaa 136,104,44\nTa 76,23,16384\n" \
"C 62,92,93\nC 63,94,95\nMvaa 140,108,44\nD 28,68,s91,3072\nC 64,96,97\nMvaa 144,112,44\nC 65,98,99\nC 66,100,101\n" \
"C 67,102,103\nMvaa 148,116,44\nTa 78,23,17408\nC 68,104,105\nC 69,106,107\nMvaa 152,120,44\nC 70,108,109\nC 71,110,111\n" \
"C 72,112,113\nMvaa 156,124,44\nTa 80,24,16384\nC 73,114,115\nC 74,116,117\nMaaa 128,96,60\nC 75,118,119\nC 76,120,121\n" \
"C 77,122,123\nMaaa 132,100,60\nTa 82,24,17408\nC 78,124,125\nC 79,126,127\nMaaa 136,104,60\nC 80,128,129\nC 81,130,131\n" \
"C 82,132,133\nMaaa 140,108,60\nTa 84,25,16384\nC 83,134,135\nC 84,136,137\nMaaa 144,112,60\nC 85,138,139\nC 86,140,141\n" \
"C 87,142,143\nMaaa 148,116,60\nTa 86,25,17408\nC 88,144,145\nC 89,146,147\nMaaa 152,120,60\nC 90,148,149\nC 91,150,151\n" \
"C 92,152,153\nMaaa 156,124,60\nTa 88,26,16384\nC 93,154,155\nC 94,156,157\nMvaa 160,96,48\nC 95,158,159\nMvaa 164,100,48\n" \
"Ta 90,26,17408\nMvaa 168,104,48\nAR 96,128\nAR 97,129\nAR 98,130\nMvaa 172,108,48\nTa 92,27,16384\nAR 99,131\n" \
"AR 100,132\nMvaa 176,112,48\nAR 101,133\nAR 102,134\nAR 103,135\nMvaa 180,116,48\nTa 94,27,17408\nC 96,96,97\n" \
"C 97,98,99\nMvaa 184,120,48\nC 98,100,101\nC 99,102,103\nAW 240,96\nMvaa 188,124,48\nRa 4,18,2048\nAW 241,97\n" \
"AW 242,98\nMvaa 192,96,52\nAW 243,99\nAR 104,136\nAR 105,137\nMvaa 196,100,52\nRa 20,19,2048\nAR 106,138\n" \
"AR 107,139\nMvaa 200,104,52\nAR 108,140\nAR 109,141\nAR 110,142\nMvaa 204,108,52\nRa 8,18,4096\nAR 111,143\n" \
"C 104,104,105\nMvaa 208,112,52\nC 105,106,107\nC 106,108,109\nC 107,110,111\nMvaa 212,116,52\nRa 24,19,4096\nAW 244,104\n" \
"AW 245,105\nMvaa 216,120,52\nAW 246,106\nAW 247,107\nAR 112,144\nMvaa 220,124,52\nRa 12,18,6144\nAR 113,145\n" \
"AR 114,146\nMvaa 224,96,56\nAR 115,147\nAR 116,148\nAR 117,149\nMvaa 228,100,56\nRa 28,19,6144\nAR 118,150\n" \
"AR 119,151\nMvaa 232,104,56\nC 112,112,113\nC 113,114,115\nC 114,116,117\nMvaa 236,108,56\nC 115,118,119\nAW 248,112\n" \
"AW 249,113\nMvaa 240,112,56\nAW 250,114\nAW 251,115\nAR 120,152\nMvaa 244,116,56\nAR 121,153\nAR 122,154\n" \
"AR 123,155\nMvaa 248,120,56\ns_branch 99f\n.p2align 12\nzp3:\ns_setpc_b64 s[98:99]\n99:\nAR 124,156\n" \
"AR 125,157\nAR 126,158\nMvaa 252,124,56\nAR 127,159\nC 120,120,121\nC 121,122,123\ns_waitcnt lgkmcnt(6)\nzY:\n" \
"Zvaa 128,64,0\nv_add_u32 v13, 128, v13\nC 122,124,125\nC 123,126,127\nMvaa 128,160,16\ns_add_u32 m0, s88, 131072\nAW 252,120\nAW 253,121\n" \
"Zvaa 132,164,16\nD 13,64,0,0\nAW 254,122\nMvaa 132,68,0\nv_add_u32 v14, s72, v14\nAW 255,123\nC 160,160,161\nZvaa 136,72,0\n" \
"v_add_u32 v28, s72, v28\nC 161,162,163\nC 162,164,165\nMvaa 136,168,16\nC 163,166,167\nAW 192,160\nAW 193,161\nZvaa 140,172,16\n" \
"AW 194,162\nAW 195,163\nC 168,168,169\nMvaa 140,76,0\ns_waitcnt lgkmcnt(0)\nD 13,64,s73,1024\nZvaa 144,80,0\nC 169,170,171\n" \
"C 170,172,173\nC 171,174,175\nMvaa 144,176,16\nAW 196,168\nAW 197,169\nAW 198,170\nZvaa 148,180,16\nAW 199,171\n" \
"C 176,176,177\nC 177,178,179\nMvaa 148,84,0\nC 178,180,181\nC 179,182,183\nAW 200,176\nZvaa 152,88,0\nD 13,64,s74,2048\n" \
"AW 201,177\nMvaa 152,184,16\nAW 202,178\nAW 203,179\nC 184,184,185\nZvaa 156,188,16\nC 185,186,187\nC 186,188,189\n" \
"C 187,190,191\nAW 204,184\nAW 205,185\nAW 206,186\nAW 207,187\nMvaa 156,92,0\nC 192,192,193\nC 193,194,195\n" \
"C 194,196,197\nZvaa 188,92,4\ns_waitcnt vmcnt(15)\ns_barrier\nC 195,198,199\nMvaa 188,188,20\nTa 96,20,0\nTa 98,20,1024\n" \
"AW 208,192\nZvaa 184,184,20\nD 13,64,s75,3072\nAW 209,193\nMvaa 184,88,4\nTa 128,20,8192\nAW 210,194\nAW 211,195\n" \
"Zvaa 180,84,4\nTa 130,20,9216\nC 200,200,201\nC 201,202,203\nMvaa 180,180,20\nRa 32,16,0\nRa 48,17,0\nC 202,204,205\n" \
"Zvaa 176,176,20\ns_add_u32 m0, s88, 135168\nC 203,206,207\nAW 212,200\nMvaa 176,80,4\nD 13,64,s76,0\nAW 213,201\nZvaa 172,76,4\n" \
"Ta 100,21,0\nAW 214,202\nAW 215,203\nMvaa 172,172,20\nTa 102,21,1024\nTa 132,21,8192\nC 208,208,209\nZvaa 168,168,20\n" \
"Ta 134,21,9216\nC 209,210,211\nC 210,212,213\nMvaa 168,72,4\nTa 104,22,0\nC 211,214,215\nAW 216,208\nZvaa 164,68,4\n" \
"D 13,64,s77,1024\nAW 217,209\nMvaa 164,164,20\nTa 106,22,1024\nTa 136,22,8192\nAW 218,210\nZvaa 160,160,20\nTa 138,22,9216\n" \
"AW 219,211\nC 216,216,217\nC 217,218,219\nC 218,220,221\nC 219,222,223\nAW 220,216\nAW 221,217\nAW 222,218\n" \
"AW 223,219\nMvaa 160,64,4\nTa 108,23,0\nC 224,224,225\nC 225,226,227\nZvaa 192,64,8\nTa 110,23,1024\nC 226,228,229\n" \
"C 227,230,231\nMvaa 192,160,24\nD 13,64,s78,2048\nAW 224,224\nZvaa 196,164,24\nTa 140,23,8192\nTa 142,23,9216\nAW 225,225\n" \
"Mvaa 196,68,8\nTa 112,24,0\nAW 226,226\nAW 227,227\nZvaa 200,72,8\nTa 114,24,1024\nC 232,232,233\nC 233,234,235\n" \
"Mvaa 200,168,24\nTa 144,24,8192\nC 234,236,237\nC 235,238,239\nZvaa 204,172,24\nD 13,64,s79,3072\nAW 228,232\nMvaa 204,76,8\n" \
"Ta 146,24,9216\nTa 116,25,0\nAW 229,233\nZvaa 208,80,8\nTa 118,25,1024\nAW 230,234\nAW 231,235\nMvaa 208,176,24\n" \
"v_add_u32 v13, 128, v13\nTa 148,25,8192\nC 240,240,241\nZvaa 212,180,24\ns_add_u32 m0, s32, 81920\nC 241,242,243\nC 242,244,245\nMvaa 212,84,8\n" \
"D 14,68,0,0\nC 243,246,247\nZvaa 216,88,8\nTa 150,25,9216\nTa 120,26,0\nAW 232,240\nMvaa 216,184,24\nTa 122,26,1024\n" \
"AW 233,241\nAW 234,242\nZvaa 220,188,24\nTa 152,26,8192\nAW 235,243\nC 248,248,249\nC 249,250,251\nC 250,252,253\n" \
"C 251,254,255\nAW 236,248\nAW 237,249\nAW 238,250\nAW 239,251\nMvaa 220,92,8\nTa 154,26,9216\nZvaa 252,92,12\n" \
"D 14,68,s89,1024\nMvaa 252,188,28\nTa 124,27,0\nTa 126,27,1024\nZvaa 248,184,28\nTa 156,27,8192\nMvaa 248,88,12\nTa 158,27,9216\n" \
"Zvaa 244,84,12\nRa 36,16,2048\nMvaa 244,180,28\nD 28,68,s90,2048\nZvaa 240,176,28\nRa 52,17,2048\nRa 40,16,4096\nMvaa 240,80,12\n" \
"Ra 56,17,4096\nZvaa 236,76,12\nRa 44,16,6144\nMvaa 236,172,28\nRa 60,17,6144\nZvaa 232,168,28\nD 28,68,s91,3072\nMvaa 232,72,12\n" \
"s_add_u32 s95, s34, 0\nZvaa 228,68,12\nbuffer_store_dwordx4 v[32:35], v0, s[60:63], s95 offen offset:0 sc1\nMvaa 228,164,28\nv_add_u32 v14, s72, v14\nZvaa 224,160,28\nv_add_u32 v28, s72, v28\nMvaa 224,64,12\n" \
"s_waitcnt lgkmcnt(6)\nMvaa 128,96,32\nMvaa 128,128,48\ns_add_u32 m0, s88, 0\nMvaa 132,132,48\nD 13,64,0,0\nMvaa 132,100,32\nMvaa 136,104,32\n" \
"Mvaa 136,136,48\nMvaa 140,140,48\nMvaa 140,108,32\ns_waitcnt lgkmcnt(0)\nD 13,64,s73,1024\nMvaa 144,112,32\nMvaa 144,144,48\nMvaa 148,148,48\n" \
"buffer_store_dwordx4 v[36:39], v0, s[60:63], s95 offen offset:1024 sc1\nMvaa 148,116,32\nMvaa 152,120,32\nD 13,64,s74,2048\nMvaa 152,152,48\nMvaa 156,156,48\nMvaa 156,124,32\nMvaa 188,124,36\n" \
"s_waitcnt vmcnt(17)\ns_barrier\nMvaa 188,156,52\nTa 64,20,32768\nTa 66,20,33792\nMvaa 184,152,52\nD 13,64,s75,3072\nMvaa 184,120,36\n" \
"Ta 160,20,40960\nMvaa 180,116,36\nTa 162,20,41984\nMvaa 180,148,52\nRa 0,16,32768\nRa 16,17,32768\nMvaa 176,144,52\ns_add_u32 m0, s88, 4096\n" \
"Mvaa 176,112,36\nD 13,64,s76,0\nMvaa 172,108,36\nTa 68,21,32768\nMvaa 172,140,52\nTa 70,21,33792\nTa 164,21,40960\nMvaa 168,136,52\n" \
"Ta 166,21,41984\nMvaa 168,104,36\nTa 72,22,32768\nMvaa 164,100,36\nD 13,64,s77,1024\nMvaa 164,132,52\nTa 74,22,33792\nTa 168,22,40960\n" \
"Mvaa 160,128,52\nTa 170,22,41984\nMvaa 160,96,36\nTa 76,23,32768\nMvaa 192,96,40\nTa 78,23,33792\nMvaa 192,128,56\nD 13,64,s78,2048\n" \
"Mvaa 196,132,56\nTa 172,23,40960\nTa 174,23,41984\nMvaa 196,100,40\nTa 80,24,32768\nMvaa 200,104,40\nTa 82,24,33792\nMvaa 200,136,56\n" \
"Ta 176,24,40960\nMvaa 204,140,56\nD 13,64,s79,3072\nMvaa 204,108,40\nTa 178,24,41984\nTa 84,25,32768\nMvaa 208,112,40\nTa 86,25,33792\n" \
"Mvaa 208,144,56\nv_add_u32 v13, 128, v13\nTa 180,25,40960\nMvaa 212,148,56\ns_add_u32 m0, s32, 65536\nMvaa 212,116,40\nD 14,68,0,0\nMvaa 216,120,40\n" \
"Ta 182,25,41984\nTa 88,26,32768\nMvaa 216,152,56\nTa 90,26,33792\nMvaa 220,156,56\nTa 184,26,40960\nMvaa 220,124,40\nTa 186,26,41984\n" \
"Mvaa 252,124,44\nD 14,68,s89,1024\nMvaa 252,156,60\nTa 92,27,32768\nTa 94,27,33792\nMvaa 248,152,60\nTa 188,27,40960\nMvaa 248,120,44\n" \
"Ta 190,27,41984\nMvaa 244,116,44\nRa 4,16,34816\nMvaa 244,148,60\nD 28,68,s90,2048\nMvaa 240,144,60\nRa 20,17,34816\nRa 8,16,36864\n" \
"Mvaa 240,112,44\nRa 24,17,36864\nMvaa 236,108,44\nRa 12,16,38912\nMvaa 236,140,60\nRa 28,17,38912\nMvaa 232,136,60\nD 28,68,s91,3072\n" \
"Mvaa 232,104,44\nMvaa 228,100,44\nMvaa 228,132,60\nv_add_u32 v14, s72, v14\nMvaa 224,128,60\nv_add_u32 v28, s72, v28\nMvaa 224,96,44\ns_waitcnt lgkmcnt(6)\n" \
"Mvaa 128,64,0\nMvaa 128,160,16\ns_add_u32 m0, s88, 32768\nMvaa 132,164,16\nD 13,64,0,0\nMvaa 132,68,0\nMvaa 136,72,0\nMvaa 136,168,16\n" \
"Mvaa 140,172,16\nMvaa 140,76,0\ns_waitcnt lgkmcnt(0)\nD 13,64,s73,1024\nMvaa 144,80,0\nMvaa 144,176,16\nMvaa 148,180,16\nbuffer_store_dwordx4 v[40:43], v0, s[60:63], s95 offen offset:2048 sc1\n" \
"Mvaa 148,84,0\nMvaa 152,88,0\nD 13,64,s74,2048\nMvaa 152,184,16\nMvaa 156,188,16\nMvaa 156,92,0\nMvaa 188,92,4\ns_waitcnt vmcnt(18)\n" \
"s_barrier\nMvaa 188,188,20\nTa 96,20,16384\nTa 98,20,17408\nMvaa 184,184,20\nD 13,64,s75,3072\nMvaa 184,88,4\nTa 128,20,24576\n" \
"Mvaa 180,84,4\nTa 130,20,25600\nMvaa 180,180,20\nRa 32,18,0\nRa 48,19,0\nMvaa 176,176,20\ns_add_u32 m0, s88, 36864\nMvaa 176,80,4\n" \
"D 13,64,s76,0\nMvaa 172,76,4\nTa 100,21,16384\nMvaa 172,172,20\nTa 102,21,17408\nTa 132,21,24576\nMvaa 168,168,20\nTa 134,21,25600\n" \
"Mvaa 168,72,4\nTa 104,22,16384\nMvaa 164,68,4\nD 13,64,s77,1024\nMvaa 164,164,20\nTa 106,22,17408\nTa 136,22,24576\nMvaa 160,160,20\n" \
"Ta 138,22,25600\nMvaa 160,64,4\nTa 108,23,16384\nMvaa 192,64,8\nTa 110,23,17408\nMvaa 192,160,24\nD 13,64,s78,2048\nMvaa 196,164,24\n" \
"Ta 140,23,24576\nTa 142,23,25600\nMvaa 196,68,8\nTa 112,24,16384\nMvaa 200,72,8\nTa 114,24,17408\nMvaa 200,168,24\nTa 144,24,24576\n" \
"Mvaa 204,172,24\nD 13,64,s79,3072\nMvaa 204,76,8\nTa 146,24,25600\nTa 116,25,16384\nMvaa 208,80,8\nTa 118,25,17408\nMvaa 208,176,24\n" \
"v_add_u32 v13, 128, v13\nTa 148,25,24576\nMvaa 212,180,24\ns_add_u32 m0, s32, 98304\nMvaa 212,84,8\nD 14,68,0,0\nMvaa 216,88,8\nTa 150,25,25600\n" \
"Ta 120,26,16384\nMvaa 216,184,24\nTa 122,26,17408\nMvaa 220,188,24\nTa 152,26,24576\nMvaa 220,92,8\nTa 154,26,25600\nMvaa 252,92,12\n" \
"D 14,68,s89,1024\nMvaa 252,188,28\nTa 124,27,16384\nTa 126,27,17408\nMvaa 248,184,28\nTa 156,27,24576\nMvaa 248,88,12\nTa 158,27,25600\n" \
"Mvaa 244,84,12\nRa 36,18,2048\nMvaa 244,180,28\nD 28,68,s90,2048\nMvaa 240,176,28\nRa 52,19,2048\nRa 40,18,4096\nMvaa 240,80,12\n" \
"Ra 56,19,4096\nMvaa 236,76,12\nRa 44,18,6144\nMvaa 236,172,28\nRa 60,19,6144\nMvaa 232,168,28\nD 28,68,s91,3072\nMvaa 232,72,12\n" \
"Mvaa 228,68,12\nMvaa 228,164,28\nv_add_u32 v14, s72, v14\nMvaa 224,160,28\nv_add_u32 v28, s72, v28\nMvaa 224,64,12\ns_waitcnt lgkmcnt(6)\nMvaa 128,96,32\n" \
"Mvaa 128,128,48\ns_add_u32 m0, s88, 131072\nMvaa 132,132,48\nD 13,64,0,0\nMvaa 132,100,32\nMvaa 136,104,32\nMvaa 136,136,48\nMvaa 140,140,48\n" \
"Mvaa 140,108,32\ns_waitcnt lgkmcnt(0)\nD 13,64,s73,1024\nMvaa 144,112,32\nMvaa 144,144,48\nMvaa 148,148,48\nbuffer_store_dwordx4 v[44:47], v0, s[60:63], s95 offen offset:3072 sc1\nMvaa 148,116,32\n" \
"Mvaa 152,120,32\nD 13,64,s74,2048\nMvaa 152,152,48\nMvaa 156,156,48\nMvaa 156,124,32\nMvaa 188,124,36\ns_waitcnt vmcnt(17)\ns_barrier\n" \
"Mvaa 188,156,52\nTa 64,20,0\nTa 66,20,1024\nMvaa 184,152,52\ns_branch 99f\n.p2align 12\nzp4:\ns_setpc_b64 s[98:99]\n" \
"99:\nD 13,64,s75,3072\nMvaa 184,120,36\nTa 160,20,8192\nMvaa 180,116,36\nTa 162,20,9216\nMvaa 180,148,52\nRa 0,16,0\n" \
"Ra 16,17,0\nMvaa 176,144,52\ns_add_u32 m0, s88, 135168\nMvaa 176,112,36\nD 13,64,s76,0\nMvaa 172,108,36\nTa 68,21,0\nMvaa 172,140,52\n" \
"Ta 70,21,1024\nTa 164,21,8192\nMvaa 168,136,52\nTa 166,21,9216\nMvaa 168,104,36\nTa 72,22,0\nMvaa 164,100,36\nD 13,64,s77,1024\n" \
"Mvaa 164,132,52\nTa 74,22,1024\nTa 168,22,8192\nMvaa 160,128,52\nTa 170,22,9216\nMvaa 160,96,36\nTa 76,23,0\nMvaa 192,96,40\n" \
"Ta 78,23,1024\nMvaa 192,128,56\nD 13,64,s78,2048\nMvaa 196,132,56\nTa 172,23,8192\nTa 174,23,9216\nMvaa 196,100,40\nTa 80,24,0\n" \
"Mvaa 200,104,40\nTa 82,24,1024\nMvaa 200,136,56\nTa 176,24,8192\nMvaa 204,140,56\nD 13,64,s79,3072\nMvaa 204,108,40\nTa 178,24,9216\n" \
"Ta 84,25,0\nMvaa 208,112,40\nTa 86,25,1024\nMvaa 208,144,56\nv_add_u32 v13, 128, v13\nTa 180,25,8192\nMvaa 212,148,56\ns_add_u32 m0, s32, 81920\n" \
"Mvaa 212,116,40\nD 14,68,0,0\nMvaa 216,120,40\nTa 182,25,9216\nTa 88,26,0\nMvaa 216,152,56\nTa 90,26,1024\nMvaa 220,156,56\n" \
"Ta 184,26,8192\nMvaa 220,124,40\nTa 186,26,9216\nMvaa 252,124,44\nD 14,68,s89,1024\nMvaa 252,156,60\nTa 92,27,0\nTa 94,27,1024\n" \
"Mvaa 248,152,60\nTa 188,27,8192\nMvaa 248,120,44\nTa 190,27,9216\nMvaa 244,116,44\nRa 4,16,2048\nMvaa 244,148,60\nD 28,68,s90,2048\n" \
"Mvaa 240,144,60\nRa 20,17,2048\nRa 8,16,4096\nMvaa 240,112,44\nRa 24,17,4096\nMvaa 236,108,44\nRa 12,16,6144\nMvaa 236,140,60\n" \
"Ra 28,17,6144\nMvaa 232,136,60\nD 28,68,s91,3072\nMvaa 232,104,44\nMvaa 228,100,44\nMvaa 228,132,60\nv_add_u32 v14, s72, v14\nMvaa 224,128,60\n" \
"v_add_u32 v28, s72, v28\nMvaa 224,96,44\ns_waitcnt lgkmcnt(6)\nMvaa 128,64,0\nMvaa 128,160,16\ns_add_u32 m0, s88, 0\nMvaa 132,164,16\nD 13,64,0,0\n" \
"Mvaa 132,68,0\nMvaa 136,72,0\nMvaa 136,168,16\nMvaa 140,172,16\nMvaa 140,76,0\ns_waitcnt lgkmcnt(0)\nD 13,64,s73,1024\nMvaa 144,80,0\n" \
"Mvaa 144,176,16\ns_add_u32 s95, s34, 4096\nMvaa 148,180,16\nbuffer_store_dwordx4 v[48:51], v0, s[60:63], s95 offen offset:0 sc1\nMvaa 148,84,0\nMvaa 152,88,0\nD 13,64,s74,2048\nMvaa 152,184,16\n" \
"Mvaa 156,188,16\nMvaa 156,92,0\nMvaa 188,92,4\ns_waitcnt vmcnt(17)\ns_barrier\nMvaa 188,188,20\nTa 96,20,32768\nTa 98,20,33792\n" \
"Mvaa 184,184,20\nD 13,64,s75,3072\nMvaa 184,88,4\nTa 128,20,40960\nMvaa 180,84,4\nTa 130,20,41984\nMvaa 180,180,20\nRa 32,16,32768\n" \
"Ra 48,17,32768\nMvaa 176,176,20\ns_add_u32 m0, s88, 4096\nMvaa 176,80,4\nD 13,64,s76,0\nMvaa 172,76,4\nTa 100,21,32768\nMvaa 172,172,20\n" \
"Ta 102,21,33792\nTa 132,21,40960\nMvaa 168,168,20\nTa 134,21,41984\nMvaa 168,72,4\nTa 104,22,32768\nMvaa 164,68,4\nD 13,64,s77,1024\n" \
"Mvaa 164,164,20\nTa 106,22,33792\nTa 136,22,40960\nMvaa 160,160,20\nTa 138,22,41984\nMvaa 160,64,4\nTa 108,23,32768\nMvaa 192,64,8\n" \
"Ta 110,23,33792\nMvaa 192,160,24\nD 13,64,s78,2048\nMvaa 196,164,24\nTa 140,23,40960\nTa 142,23,41984\nMvaa 196,68,8\nTa 112,24,32768\n" \
"Mvaa 200,72,8\nTa 114,24,33792\nMvaa 200,168,24\nTa 144,24,40960\nMvaa 204,172,24\nD 13,64,s79,3072\nMvaa 204,76,8\nTa 146,24,41984\n" \
"Ta 116,25,32768\nMvaa 208,80,8\nTa 118,25,33792\nMvaa 208,176,24\nv_add_u32 v13, 128, v13\nTa 148,25,40960\nMvaa 212,180,24\ns_add_u32 m0, s32, 65536\n" \
"Mvaa 212,84,8\nD 14,68,0,0\nMvaa 216,88,8\nTa 150,25,41984\nTa 120,26,32768\nMvaa 216,184,24\nTa 122,26,33792\nMvaa 220,188,24\n" \
"Ta 152,26,40960\nMvaa 220,92,8\nTa 154,26,41984\nMvaa 252,92,12\nD 14,68,s89,1024\nMvaa 252,188,28\nTa 124,27,32768\nTa 126,27,33792\n" \
"Mvaa 248,184,28\nTa 156,27,40960\nMvaa 248,88,12\nTa 158,27,41984\nMvaa 244,84,12\nRa 36,16,34816\nMvaa 244,180,28\nD 28,68,s90,2048\n" \
"Mvaa 240,176,28\nRa 52,17,34816\nRa 40,16,36864\nMvaa 240,80,12\nRa 56,17,36864\nMvaa 236,76,12\nRa 44,16,38912\nMvaa 236,172,28\n" \
"Ra 60,17,38912\nMvaa 232,168,28\nD 28,68,s91,3072\nMvaa 232,72,12\nMvaa 228,68,12\nMvaa 228,164,28\nv_add_u32 v14, s72, v14\nMvaa 224,160,28\n" \
"v_add_u32 v28, s72, v28\nMvaa 224,64,12\ns_waitcnt lgkmcnt(6)\nMvaa 128,96,32\nMvaa 128,128,48\ns_add_u32 m0, s88, 32768\nMvaa 132,132,48\nD 13,64,0,0\n" \
"Mvaa 132,100,32\nMvaa 136,104,32\nMvaa 136,136,48\nMvaa 140,140,48\nMvaa 140,108,32\ns_waitcnt lgkmcnt(0)\nD 13,64,s73,1024\nMvaa 144,112,32\n" \
"Mvaa 144,144,48\nMvaa 148,148,48\nbuffer_store_dwordx4 v[52:55], v0, s[60:63], s95 offen offset:1024 sc1\nMvaa 148,116,32\nMvaa 152,120,32\nD 13,64,s74,2048\nMvaa 152,152,48\nMvaa 156,156,48\n" \
"Mvaa 156,124,32\nMvaa 188,124,36\ns_waitcnt vmcnt(17)\ns_barrier\nMvaa 188,156,52\nTa 64,20,16384\nTa 66,20,17408\nMvaa 184,152,52\n" \
"D 13,64,s75,3072\nMvaa 184,120,36\nTa 160,20,24576\nMvaa 180,116,36\nTa 162,20,25600\nMvaa 180,148,52\nRa 0,18,0\nRa 16,19,0\n" \
"Mvaa 176,144,52\ns_add_u32 m0, s88, 36864\nMvaa 176,112,36\nD 13,64,s76,0\nMvaa 172,108,36\nTa 68,21,16384\nMvaa 172,140,52\nTa 70,21,17408\n" \
"Ta 164,21,24576\nMvaa 168,136,52\nTa 166,21,25600\nMvaa 168,104,36\nTa 72,22,16384\nMvaa 164,100,36\nD 13,64,s77,1024\nMvaa 164,132,52\n" \
"Ta 74,22,17408\nTa 168,22,24576\nMvaa 160,128,52\nTa 170,22,25600\nMvaa 160,96,36\nTa 76,23,16384\nMvaa 192,96,40\nTa 78,23,17408\n" \
"Mvaa 192,128,56\nD 13,64,s78,2048\nMvaa 196,132,56\nTa 172,23,24576\nTa 174,23,25600\nMvaa 196,100,40\nTa 80,24,16384\nMvaa 200,104,40\n" \
"Ta 82,24,17408\nMvaa 200,136,56\nTa 176,24,24576\nMvaa 204,140,56\nD 13,64,s79,3072\nMvaa 204,108,40\nTa 178,24,25600\nTa 84,25,16384\n" \
"Mvaa 208,112,40\nTa 86,25,17408\nMvaa 208,144,56\nv_add_u32 v13, 128, v13\nTa 180,25,24576\nMvaa 212,148,56\ns_add_u32 m0, s32, 98304\nMvaa 212,116,40\n" \
"D 14,68,0,0\nMvaa 216,120,40\nTa 182,25,25600\nTa 88,26,16384\nMvaa 216,152,56\nTa 90,26,17408\nMvaa 220,156,56\nTa 184,26,24576\n" \
"Mvaa 220,124,40\nTa 186,26,25600\nMvaa 252,124,44\nD 14,68,s89,1024\nMvaa 252,156,60\nTa 92,27,16384\nTa 94,27,17408\nMvaa 248,152,60\n" \
"Ta 188,27,24576\nMvaa 248,120,44\nTa 190,27,25600\nMvaa 244,116,44\nRa 4,18,2048\nMvaa 244,148,60\nD 28,68,s90,2048\nMvaa 240,144,60\n" \
"Ra 20,19,2048\nRa 8,18,4096\nMvaa 240,112,44\nRa 24,19,4096\nMvaa 236,108,44\nRa 12,18,6144\nMvaa 236,140,60\nRa 28,19,6144\n" \
"Mvaa 232,136,60\nD 28,68,s91,3072\nMvaa 232,104,44\nMvaa 228,100,44\nMvaa 228,132,60\nv_add_u32 v14, s72, v14\nMvaa 224,128,60\nv_add_u32 v28, s72, v28\n" \
"Mvaa 224,96,44\ns_waitcnt lgkmcnt(6)\nMvaa 128,64,0\nMvaa 128,160,16\ns_add_u32 m0, s88, 131072\nMvaa 132,164,16\nD 13,64,0,0\nMvaa 132,68,0\n" \
"Mvaa 136,72,0\nMvaa 136,168,16\nMvaa 140,172,16\nMvaa 140,76,0\ns_waitcnt lgkmcnt(0)\nD 13,64,s73,1024\nMvaa 144,80,0\nMvaa 144,176,16\n" \
"Mvaa 148,180,16\nbuffer_store_dwordx4 v[56:59], v0, s[60:63], s95 offen offset:2048 sc1\nMvaa 148,84,0\nMvaa 152,88,0\nD 13,64,s74,2048\nMvaa 152,184,16\nMvaa 156,188,16\nMvaa 156,92,0\n" \
"Mvaa 188,92,4\ns_waitcnt vmcnt(17)\ns_barrier\nMvaa 188,188,20\nTa 96,20,0\nTa 98,20,1024\nMvaa 184,184,20\nD 13,64,s75,3072\n" \
"Mvaa 184,88,4\nTa 128,20,8192\nMvaa 180,84,4\nTa 130,20,9216\nMvaa 180,180,20\nRa 32,16,0\nRa 48,17,0\nMvaa 176,176,20\n" \
"s_add_u32 m0, s88, 135168\nMvaa 176,80,4\nD 13,64,s76,0\nMvaa 172,76,4\nTa 100,21,0\nMvaa 172,172,20\nTa 102,21,1024\nTa 132,21,8192\n" \
"Mvaa 168,168,20\nTa 134,21,9216\nMvaa 168,72,4\nTa 104,22,0\nMvaa 164,68,4\nD 13,64,s77,1024\nMvaa 164,164,20\nTa 106,22,1024\n" \
"Ta 136,22,8192\nMvaa 160,160,20\nTa 138,22,9216\nMvaa 160,64,4\nTa 108,23,0\nMvaa 192,64,8\nTa 110,23,1024\nMvaa 192,160,24\n" \
"D 13,64,s78,2048\nMvaa 196,164,24\nTa 140,23,8192\nTa 142,23,9216\nMvaa 196,68,8\nTa 112,24,0\nMvaa 200,72,8\nTa 114,24,1024\n" \
"Mvaa 200,168,24\nTa 144,24,8192\nMvaa 204,172,24\nD 13,64,s79,3072\nMvaa 204,76,8\nTa 146,24,9216\nTa 116,25,0\nMvaa 208,80,8\n" \
"Ta 118,25,1024\nMvaa 208,176,24\nv_add_u32 v13, 128, v13\nTa 148,25,8192\nMvaa 212,180,24\ns_add_u32 m0, s32, 81920\nMvaa 212,84,8\nD 14,68,0,0\n" \
"Mvaa 216,88,8\nTa 150,25,9216\nTa 120,26,0\nMvaa 216,184,24\nTa 122,26,1024\nMvaa 220,188,24\nTa 152,26,8192\nMvaa 220,92,8\n" \
"Ta 154,26,9216\nMvaa 252,92,12\nD 14,68,s89,1024\nMvaa 252,188,28\nTa 124,27,0\nTa 126,27,1024\nMvaa 248,184,28\nTa 156,27,8192\n" \
"Mvaa 248,88,12\nTa 158,27,9216\nMvaa 244,84,12\nRa 36,16,2048\nMvaa 244,180,28\nD 28,68,s90,2048\nMvaa 240,176,28\nRa 52,17,2048\n" \
"Ra 40,16,4096\nMvaa 240,80,12\nRa 56,17,4096\nMvaa 236,76,12\nRa 44,16,6144\nMvaa 236,172,28\nRa 60,17,6144\nMvaa 232,168,28\n" \
"D 28,68,s91,3072\nMvaa 232,72,12\nMvaa 228,68,12\nMvaa 228,164,28\nv_add_u32 v14, s72, v14\nMvaa 224,160,28\nv_add_u32 v28, s72, v28\nMvaa 224,64,12\n" \
"s_waitcnt lgkmcnt(6)\nMvaa 128,96,32\nMvaa 128,128,48\ns_add_u32 m0, s88, 0\nMvaa 132,132,48\nD 13,64,0,0\nMvaa 132,100,32\nMvaa 136,104,32\n" \
"Mvaa 136,136,48\nMvaa 140,140,48\nMvaa 140,108,32\ns_waitcnt lgkmcnt(0)\nD 13,64,s73,1024\nMvaa 144,112,32\nMvaa 144,144,48\nMvaa 148,148,48\n" \
"buffer_store_dwordx4 v[60:63], v0, s[60:63], s95 offen offset:3072 sc1\nMvaa 148,116,32\nMvaa 152,120,32\nD 13,64,s74,2048\nMvaa 152,152,48\nMvaa 156,156,48\nMvaa 156,124,32\nMvaa 188,124,36\n" \
"s_waitcnt vmcnt(17)\ns_barrier\nMvaa 188,156,52\nTa 64,20,32768\nTa 66,20,33792\nMvaa 184,152,52\nD 13,64,s75,3072\nMvaa 184,120,36\n" \
"Ta 160,20,40960\nMvaa 180,116,36\nTa 162,20,41984\nMvaa 180,148,52\nRa 0,16,32768\nRa 16,17,32768\nMvaa 176,144,52\ns_add_u32 m0, s88, 4096\n" \
"Mvaa 176,112,36\nD 13,64,s76,0\nMvaa 172,108,36\nTa 68,21,32768\nMvaa 172,140,52\ns_branch 99f\n.p2align 12\nzp5:\n" \
"s_setpc_b64 s[98:99]\n99:\nTa 70,21,33792\nTa 164,21,40960\nMvaa 168,136,52\nTa 166,21,41984\nMvaa 168,104,36\nTa 72,22,32768\n" \
"Mvaa 164,100,36\nD 13,64,s77,1024\nMvaa 164,132,52\nTa 74,22,33792\nTa 168,22,40960\nMvaa 160,128,52\nTa 170,22,41984\nMvaa 160,96,36\n" \
"Ta 76,23,32768\nMvaa 192,96,40\nTa 78,23,33792\nMvaa 192,128,56\nD 13,64,s78,2048\nMvaa 196,132,56\nTa 172,23,40960\nTa 174,23,41984\n" \
"Mvaa 196,100,40\nTa 80,24,32768\nMvaa 200,104,40\nTa 82,24,33792\nMvaa 200,136,56\nTa 176,24,40960\nMvaa 204,140,56\nD 13,64,s79,3072\n" \
"Mvaa 204,108,40\nTa 178,24,41984\nTa 84,25,32768\nMvaa 208,112,40\nTa 86,25,33792\nMvaa 208,144,56\nv_add_u32 v13, 128, v13\nTa 180,25,40960\n" \
"Mvaa 212,148,56\ns_add_u32 m0, s32, 65536\nMvaa 212,116,40\nD 14,68,0,0\nMvaa 216,120,40\nTa 182,25,41984\nTa 88,26,32768\nMvaa 216,152,56\n" \
"Ta 90,26,33792\nMvaa 220,156,56\nTa 184,26,40960\nMvaa 220,124,40\nTa 186,26,41984\nMvaa 252,124,44\nD 14,68,s89,1024\nMvaa 252,156,60\n" \
"Ta 92,27,32768\nTa 94,27,33792\nMvaa 248,152,60\nTa 188,27,40960\nMvaa 248,120,44\nTa 190,27,41984\nMvaa 244,116,44\nRa 4,16,34816\n" \
"Mvaa 244,148,60\nD 28,68,s90,2048\nMvaa 240,144,60\nRa 20,17,34816\nRa 8,16,36864\nMvaa 240,112,44\nRa 24,17,36864\nMvaa 236,108,44\n" \
"Ra 12,16,38912\nMvaa 236,140,60\nRa 28,17,38912\nMvaa 232,136,60\nD 28,68,s91,3072\nMvaa 232,104,44\ns_add_u32 s95, s34, 8192\nMvaa 228,100,44\n" \
"buffer_store_dwordx4 v[64:67], v0, s[60:63], s95 offen offset:0 sc1\nMvaa 228,132,60\nv_add_u32 v14, s72, v14\nMvaa 224,128,60\nv_add_u32 v28, s72, v28\nMvaa 224,96,44\ns_waitcnt lgkmcnt(6)\nMvaa 128,64,0\n" \
"Mvaa 128,160,16\ns_add_u32 m0, s88, 32768\nMvaa 132,164,16\nD 13,64,0,0\nMvaa 132,68,0\nMvaa 136,72,0\nMvaa 136,168,16\nMvaa 140,172,16\n" \
"Mvaa 140,76,0\ns_waitcnt lgkmcnt(0)\nD 13,64,s73,1024\nMvaa 144,80,0\nMvaa 144,176,16\nMvaa 148,180,16\nbuffer_store_dwordx4 v[68:71], v0, s[60:63], s95 offen offset:1024 sc1\nMvaa 148,84,0\n" \
"Mvaa 152,88,0\nD 13,64,s74,2048\nMvaa 152,184,16\nMvaa 156,188,16\nMvaa 156,92,0\nMvaa 188,92,4\ns_waitcnt vmcnt(18)\ns_barrier\n" \
"Mvaa 188,188,20\nTa 96,20,16384\nTa 98,20,17408\nMvaa 184,184,20\nD 13,64,s75,3072\nMvaa 184,88,4\nTa 128,20,24576\nMvaa 180,84,4\n" \
"Ta 130,20,25600\nMvaa 180,180,20\nRa 32,18,0\nRa 48,19,0\nMvaa 176,176,20\ns_add_u32 m0, s88, 36864\nMvaa 176,80,4\nD 13,64,s76,0\n" \
"Mvaa 172,76,4\nTa 100,21,16384\nMvaa 172,172,20\nTa 102,21,17408\nTa 132,21,24576\nMvaa 168,168,20\nTa 134,21,25600\nMvaa 168,72,4\n" \
"Ta 104,22,16384\nMvaa 164,68,4\nD 13,64,s77,1024\nMvaa 164,164,20\nTa 106,22,17408\nTa 136,22,24576\nMvaa 160,160,20\nTa 138,22,25600\n" \
"Mvaa 160,64,4\nTa 108,23,16384\nMvaa 192,64,8\nTa 110,23,17408\nMvaa 192,160,24\nD 13,64,s78,2048\nMvaa 196,164,24\nTa 140,23,24576\n" \
"Ta 142,23,25600\nMvaa 196,68,8\nTa 112,24,16384\nMvaa 200,72,8\nTa 114,24,17408\nMvaa 200,168,24\nTa 144,24,24576\nMvaa 204,172,24\n" \
"D 13,64,s79,3072\nMvaa 204,76,8\nTa 146,24,25600\nTa 116,25,16384\nMvaa 208,80,8\nTa 118,25,17408\nMvaa 208,176,24\nv_add_u32 v13, 128, v13\n" \
"Ta 148,25,24576\nMvaa 212,180,24\ns_add_u32 m0, s32, 98304\nMvaa 212,84,8\nD 14,68,0,0\nMvaa 216,88,8\nTa 150,25,25600\nTa 120,26,16384\n" \
"Mvaa 216,184,24\nTa 122,26,17408\nMvaa 220,188,24\nTa 152,26,24576\nMvaa 220,92,8\nTa 154,26,25600\nMvaa 252,92,12\nD 14,68,s89,1024\n" \
"Mvaa 252,188,28\nTa 124,27,16384\nTa 126,27,17408\nMvaa 248,184,28\nTa 156,27,24576\nMvaa 248,88,12\nTa 158,27,25600\nMvaa 244,84,12\n" \
"Ra 36,18,2048\nMvaa 244,180,28\nD 28,68,s90,2048\nMvaa 240,176,28\nRa 52,19,2048\nRa 40,18,4096\nMvaa 240,80,12\nRa 56,19,4096\n" \
"Mvaa 236,76,12\nRa 44,18,6144\nMvaa 236,172,28\nRa 60,19,6144\nMvaa 232,168,28\nD 28,68,s91,3072\nMvaa 232,72,12\nMvaa 228,68,12\n" \
"Mvaa 228,164,28\nv_add_u32 v14, s72, v14\nMvaa 224,160,28\nv_add_u32 v28, s72, v28\nMvaa 224,64,12\ns_waitcnt lgkmcnt(6)\nMvaa 128,96,32\nMvaa 128,128,48\n" \
"s_add_u32 m0, s88, 131072\nMvaa 132,132,48\nD 13,64,0,0\nMvaa 132,100,32\nMvaa 136,104,32\nMvaa 136,136,48\nMvaa 140,140,48\nMvaa 140,108,32\n" \
"s_waitcnt lgkmcnt(0)\nD 13,64,s73,1024\nMvaa 144,112,32\nMvaa 144,144,48\nMvaa 148,148,48\nbuffer_store_dwordx4 v[72:75], v0, s[60:63], s95 offen offset:2048 sc1\nMvaa 148,116,32\nMvaa 152,120,32\n" \
"D 13,64,s74,2048\nMvaa 152,152,48\nMvaa 156,156,48\nMvaa 156,124,32\nMvaa 188,124,36\ns_waitcnt vmcnt(18)\ns_barrier\nMvaa 188,156,52\n" \
"Ta 64,20,0\nTa 66,20,1024\nMvaa 184,152,52\nD 13,64,s75,3072\nMvaa 184,120,36\nTa 160,20,8192\nMvaa 180,116,36\nTa 162,20,9216\n" \
"Mvaa 180,148,52\nRa 0,16,0\nRa 16,17,0\nMvaa 176,144,52\ns_add_u32 m0, s88, 135168\nMvaa 176,112,36\nD 13,64,s76,0\nMvaa 172,108,36\n" \
"Ta 68,21,0\nMvaa 172,140,52\nTa 70,21,1024\nTa 164,21,8192\nMvaa 168,136,52\nTa 166,21,9216\nMvaa 168,104,36\nTa 72,22,0\n" \
"Mvaa 164,100,36\nD 13,64,s77,1024\nMvaa 164,132,52\nTa 74,22,1024\nTa 168,22,8192\nMvaa 160,128,52\nTa 170,22,9216\nMvaa 160,96,36\n" \
"Ta 76,23,0\nMvaa 192,96,40\nTa 78,23,1024\nMvaa 192,128,56\nD 13,64,s78,2048\nMvaa 196,132,56\nTa 172,23,8192\nTa 174,23,9216\n" \
"Mvaa 196,100,40\nTa 80,24,0\nMvaa 200,104,40\nTa 82,24,1024\nMvaa 200,136,56\nTa 176,24,8192\nMvaa 204,140,56\nD 13,64,s79,3072\n" \
"Mvaa 204,108,40\nTa 178,24,9216\nTa 84,25,0\nMvaa 208,112,40\nTa 86,25,1024\nMvaa 208,144,56\nv_add_u32 v13, 128, v13\nTa 180,25,8192\n" \
"Mvaa 212,148,56\ns_add_u32 m0, s32, 81920\nMvaa 212,116,40\nD 14,68,0,0\nMvaa 216,120,40\nTa 182,25,9216\nTa 88,26,0\nMvaa 216,152,56\n" \
"Ta 90,26,1024\nMvaa 220,156,56\nTa 184,26,8192\nMvaa 220,124,40\nTa 186,26,9216\nMvaa 252,124,44\nD 14,68,s89,1024\nMvaa 252,156,60\n" \
"Ta 92,27,0\nTa 94,27,1024\nMvaa 248,152,60\nTa 188,27,8192\nMvaa 248,120,44\nTa 190,27,9216\nMvaa 244,116,44\nRa 4,16,2048\n" \
"Mvaa 244,148,60\nD 28,68,s90,2048\nMvaa 240,144,60\nRa 20,17,2048\nRa 8,16,4096\nMvaa 240,112,44\nRa 24,17,4096\nMvaa 236,108,44\n" \
"Ra 12,16,6144\nMvaa 236,140,60\nRa 28,17,6144\nMvaa 232,136,60\nD 28,68,s91,3072\nMvaa 232,104,44\nMvaa 228,100,44\nMvaa 228,132,60\n" \
"v_add_u32 v14, s72, v14\nMvaa 224,128,60\nv_add_u32 v28, s72, v28\nMvaa 224,96,44\ns_waitcnt lgkmcnt(6)\nMvaa 128,64,0\nMvaa 128,160,16\ns_add_u32 m0, s88, 0\n" \
"Mvaa 132,164,16\nD 13,64,0,0\nMvaa 132,68,0\nMvaa 136,72,0\nMvaa 136,168,16\nMvaa 140,172,16\nMvaa 140,76,0\ns_waitcnt lgkmcnt(0)\n" \
"D 13,64,s73,1024\nMvaa 144,80,0\nMvaa 144,176,16\nMvaa 148,180,16\nbuffer_store_dwordx4 v[76:79], v0, s[60:63], s95 offen offset:3072 sc1\nMvaa 148,84,0\nMvaa 152,88,0\nD 13,64,s74,2048\n" \
"Mvaa 152,184,16\nMvaa 156,188,16\nMvaa 156,92,0\nMvaa 188,92,4\ns_waitcnt vmcnt(17)\ns_barrier\nMvaa 188,188,20\nTa 96,20,32768\n" \
"Ta 98,20,33792\nMvaa 184,184,20\nD 13,64,s75,3072\nMvaa 184,88,4\nTa 128,20,40960\nMvaa 180,84,4\nTa 130,20,41984\nMvaa 180,180,20\n" \
"Ra 32,16,32768\nRa 48,17,32768\nMvaa 176,176,20\ns_add_u32 m0, s88, 4096\nMvaa 176,80,4\nD 13,64,s76,0\nMvaa 172,76,4\nTa 100,21,32768\n" \
"Mvaa 172,172,20\nTa 102,21,33792\nTa 132,21,40960\nMvaa 168,168,20\nTa 134,21,41984\nMvaa 168,72,4\nTa 104,22,32768\nMvaa 164,68,4\n" \
"D 13,64,s77,1024\nMvaa 164,164,20\nTa 106,22,33792\nTa 136,22,40960\nMvaa 160,160,20\nTa 138,22,41984\nMvaa 160,64,4\nTa 108,23,32768\n" \
"Mvaa 192,64,8\nTa 110,23,33792\nMvaa 192,160,24\nD 13,64,s78,2048\nMvaa 196,164,24\nTa 140,23,40960\nTa 142,23,41984\nMvaa 196,68,8\n" \
"Ta 112,24,32768\nMvaa 200,72,8\nTa 114,24,33792\nMvaa 200,168,24\nTa 144,24,40960\nMvaa 204,172,24\nD 13,64,s79,3072\nMvaa 204,76,8\n" \
"Ta 146,24,41984\nTa 116,25,32768\nMvaa 208,80,8\nTa 118,25,33792\nMvaa 208,176,24\nv_add_u32 v13, 128, v13\nTa 148,25,40960\nMvaa 212,180,24\n" \
"s_add_u32 m0, s32, 65536\nMvaa 212,84,8\nD 14,68,0,0\nMvaa 216,88,8\nTa 150,25,41984\nTa 120,26,32768\nMvaa 216,184,24\nTa 122,26,33792\n" \
"Mvaa 220,188,24\nTa 152,26,40960\nMvaa 220,92,8\nTa 154,26,41984\nMvaa 252,92,12\nD 14,68,s89,1024\nMvaa 252,188,28\nTa 124,27,32768\n" \
"Ta 126,27,33792\nMvaa 248,184,28\nTa 156,27,40960\nMvaa 248,88,12\nTa 158,27,41984\nMvaa 244,84,12\nRa 36,16,34816\nMvaa 244,180,28\n" \
"D 28,68,s90,2048\nMvaa 240,176,28\nRa 52,17,34816\nRa 40,16,36864\nMvaa 240,80,12\nRa 56,17,36864\nMvaa 236,76,12\nRa 44,16,38912\n" \
"Mvaa 236,172,28\nRa 60,17,38912\nMvaa 232,168,28\nD 28,68,s91,3072\nMvaa 232,72,12\nMvaa 228,68,12\nMvaa 228,164,28\nv_add_u32 v14, s72, v14\n" \
"Mvaa 224,160,28\nv_add_u32 v28, s72, v28\nMvaa 224,64,12\ns_waitcnt lgkmcnt(6)\nMvaa 128,96,32\nMvaa 128,128,48\ns_add_u32 m0, s88, 32768\nMvaa 132,132,48\n" \
"D 13,64,0,0\nMvaa 132,100,32\nMvaa 136,104,32\nMvaa 136,136,48\nMvaa 140,140,48\nMvaa 140,108,32\ns_waitcnt lgkmcnt(0)\nD 13,64,s73,1024\n" \
"Mvaa 144,112,32\nMvaa 144,144,48\ns_add_u32 s95, s34, 12288\nMvaa 148,148,48\nbuffer_store_dwordx4 v[80:83], v0, s[60:63], s95 offen offset:0 sc1\nMvaa 148,116,32\nMvaa 152,120,32\nD 13,64,s74,2048\n" \
"Mvaa 152,152,48\nMvaa 156,156,48\nMvaa 156,124,32\nMvaa 188,124,36\ns_waitcnt vmcnt(17)\ns_barrier\nMvaa 188,156,52\nTa 64,20,16384\n" \
"Ta 66,20,17408\nMvaa 184,152,52\nD 13,64,s75,3072\nMvaa 184,120,36\nTa 160,20,24576\nMvaa 180,116,36\nTa 162,20,25600\nMvaa 180,148,52\n" \
"Ra 0,18,0\nRa 16,19,0\nMvaa 176,144,52\ns_add_u32 m0, s88, 36864\nMvaa 176,112,36\nD 13,64,s76,0\nMvaa 172,108,36\nTa 68,21,16384\n" \
"Mvaa 172,140,52\nTa 70,21,17408\nTa 164,21,24576\nMvaa 168,136,52\nTa 166,21,25600\nMvaa 168,104,36\nTa 72,22,16384\nMvaa 164,100,36\n" \
"D 13,64,s77,1024\nMvaa 164,132,52\nTa 74,22,17408\nTa 168,22,24576\nMvaa 160,128,52\ns_branch 99f\n.p2align 12\nzp6:\n" \
"s_setpc_b64 s[98:99]\n99:\nTa 170,22,25600\nMvaa 160,96,36\nTa 76,23,16384\nMvaa 192,96,40\nTa 78,23,17408\nMvaa 192,128,56\n" \
"D 13,64,s78,2048\nMvaa 196,132,56\nTa 172,23,24576\nTa 174,23,25600\nMvaa 196,100,40\nTa 80,24,16384\nMvaa 200,104,40\nTa 82,24,17408\n" \
"Mvaa 200,136,56\nTa 176,24,24576\nMvaa 204,140,56\nD 13,64,s79,3072\nMvaa 204,108,40\nTa 178,24,25600\nTa 84,25,16384\nMvaa 208,112,40\n" \
"Ta 86,25,17408\nMvaa 208,144,56\nv_add_u32 v13, 128, v13\nTa 180,25,24576\nMvaa 212,148,56\ns_add_u32 m0, s32, 98304\nMvaa 212,116,40\nD 14,68,0,0\n" \
"Mvaa 216,120,40\nTa 182,25,25600\nTa 88,26,16384\nMvaa 216,152,56\nTa 90,26,17408\nMvaa 220,156,56\nTa 184,26,24576\nMvaa 220,124,40\n" \
"Ta 186,26,25600\nMvaa 252,124,44\nD 14,68,s89,1024\nMvaa 252,156,60\nTa 92,27,16384\nTa 94,27,17408\nMvaa 248,152,60\nTa 188,27,24576\n" \
"Mvaa 248,120,44\nTa 190,27,25600\nMvaa 244,116,44\nRa 4,18,2048\nMvaa 244,148,60\nD 28,68,s90,2048\nMvaa 240,144,60\nRa 20,19,2048\n" \
"Ra 8,18,4096\nMvaa 240,112,44\nRa 24,19,4096\nMvaa 236,108,44\nRa 12,18,6144\nMvaa 236,140,60\nRa 28,19,6144\nMvaa 232,136,60\n" \
"D 28,68,s91,3072\nMvaa 232,104,44\nMvaa 228,100,44\nMvaa 228,132,60\nv_add_u32 v14, s72, v14\nMvaa 224,128,60\nv_add_u32 v28, s72, v28\nMvaa 224,96,44\n" \
"s_waitcnt lgkmcnt(6)\nMvaa 128,64,0\nMvaa 128,160,16\ns_add_u32 m0, s88, 131072\nMvaa 132,164,16\nD 13,64,0,0\nMvaa 132,68,0\nMvaa 136,72,0\n" \
"Mvaa 136,168,16\nMvaa 140,172,16\nMvaa 140,76,0\ns_waitcnt lgkmcnt(0)\nD 13,64,s73,1024\nMvaa 144,80,0\nMvaa 144,176,16\nMvaa 148,180,16\n" \
"buffer_store_dwordx4 v[84:87], v0, s[60:63], s95 offen offset:1024 sc1\nMvaa 148,84,0\nMvaa 152,88,0\nD 13,64,s74,2048\nMvaa 152,184,16\nMvaa 156,188,16\nMvaa 156,92,0\nMvaa 188,92,4\n" \
"s_waitcnt vmcnt(17)\ns_barrier\nMvaa 188,188,20\nTa 96,20,0\nTa 98,20,1024\nMvaa 184,184,20\nD 13,64,s75,3072\nMvaa 184,88,4\n" \
"Ta 128,20,8192\nMvaa 180,84,4\nTa 130,20,9216\nMvaa 180,180,20\nRa 32,16,0\nRa 48,17,0\nMvaa 176,176,20\ns_add_u32 m0, s88, 135168\n" \
"Mvaa 176,80,4\nD 13,64,s76,0\nMvaa 172,76,4\nTa 100,21,0\nMvaa 172,172,20\nTa 102,21,1024\nTa 132,21,8192\nMvaa 168,168,20\n" \
"Ta 134,21,9216\nMvaa 168,72,4\nTa 104,22,0\nMvaa 164,68,4\nD 13,64,s77,1024\nMvaa 164,164,20\nTa 106,22,1024\nTa 136,22,8192\n" \
"Mvaa 160,160,20\nTa 138,22,9216\nMvaa 160,64,4\nTa 108,23,0\nMvaa 192,64,8\nTa 110,23,1024\nMvaa 192,160,24\nD 13,64,s78,2048\n" \
"Mvaa 196,164,24\nTa 140,23,8192\nTa 142,23,9216\nMvaa 196,68,8\nTa 112,24,0\nMvaa 200,72,8\nTa 114,24,1024\nMvaa 200,168,24\n" \
"Ta 144,24,8192\nMvaa 204,172,24\nD 13,64,s79,3072\nMvaa 204,76,8\nTa 146,24,9216\nTa 116,25,0\nMvaa 208,80,8\nTa 118,25,1024\n" \
"Mvaa 208,176,24\nv_add_u32 v13, 128, v13\nTa 148,25,8192\nMvaa 212,180,24\ns_add_u32 m0, s32, 81920\nMvaa 212,84,8\nD 14,68,0,0\nMvaa 216,88,8\n" \
"Ta 150,25,9216\nTa 120,26,0\nMvaa 216,184,24\nTa 122,26,1024\nMvaa 220,188,24\nTa 152,26,8192\nMvaa 220,92,8\nTa 154,26,9216\n" \
"Mvaa 252,92,12\nD 14,68,s89,1024\nMvaa 252,188,28\nTa 124,27,0\nTa 126,27,1024\nMvaa 248,184,28\nTa 156,27,8192\nMvaa 248,88,12\n" \
"Ta 158,27,9216\nMvaa 244,84,12\nRa 36,16,2048\nMvaa 244,180,28\nD 28,68,s90,2048\nMvaa 240,176,28\nRa 52,17,2048\nRa 40,16,4096\n" \
"Mvaa 240,80,12\nRa 56,17,4096\nMvaa 236,76,12\nRa 44,16,6144\nMvaa 236,172,28\nRa 60,17,6144\nMvaa 232,168,28\nD 28,68,s91,3072\n" \
"Mvaa 232,72,12\nMvaa 228,68,12\nMvaa 228,164,28\nv_add_u32 v14, s72, v14\nMvaa 224,160,28\nv_add_u32 v28, s72, v28\nMvaa 224,64,12\ns_waitcnt lgkmcnt(6)\n" \
"Mvaa 128,96,32\nMvaa 128,128,48\nbuffer_load_dwordx2 v[10:11], v1, s[60:63], 0 offen offset:0 sc1\nMvaa 132,132,48\nv_subrev_u32 v9, s32, v0\nv_add_u32 v8, 0x1004200, v0\nMvaa 132,100,32\ns_add_u32 s77, s38, s32\n" \
"v_mov_b32 v12, 0xc2200000\nMvaa 136,104,32\nv_mov_b32 v13, 0xc2200000\nv_mov_b32 v14, 0xc2200000\nMvaa 136,136,48\nv_mov_b32 v15, 0xc2200000\nv_bfe_u32 v29, v0, 4, 4\nMvaa 140,140,48\n" \
"v_lshrrev_b32 v28, 8, v0\ns_lshr_b32 s96, s32, 8\nMvaa 140,108,32\ns_waitcnt lgkmcnt(0)\nv_lshlrev_b32 v6, 2, v28\nMvaa 144,112,32\nv_add_u32 v30, s96, v29\nv_sub_u32 v30, v30, v6\n" \
"Mvaa 144,144,48\nv_mov_b32 v6, 0xff800000\nv_cmp_gt_i32 s[90:91], 0, v30\nMvaa 148,148,48\nbuffer_store_dwordx4 v[88:91], v0, s[60:63], s95 offen offset:2048 sc1\nMvaa 148,116,32\nv_cmp_gt_i32 s[92:93], 1, v30\nv_cmp_gt_i32 s[74:75], 2, v30\n" \
"Mvaa 152,120,32\nv_cmp_gt_i32 s[78:79], 3, v30\nv_cndmask_b32 v112, v12, v6, s[90:91]\nMvaa 152,152,48\nv_cndmask_b32 v113, v12, v6, s[92:93]\nv_cndmask_b32 v114, v12, v6, s[74:75]\nMvaa 156,156,48\nv_cndmask_b32 v115, v12, v6, s[78:79]\n" \
"v_cmp_gt_i32 s[90:91], 16, v30\nMvaa 156,124,32\nv_cmp_gt_i32 s[92:93], 17, v30\nv_cmp_gt_i32 s[74:75], 18, v30\nMvaa 188,124,36\ns_waitcnt vmcnt(15)\ns_barrier\nMvaa 188,156,52\n" \
"Ta 64,20,32768\nv_cmp_gt_i32 s[78:79], 19, v30\nMvaa 184,152,52\nTa 66,20,33792\nv_cndmask_b32 v116, v12, v6, s[90:91]\nMvaa 184,120,36\nTa 160,20,40960\nv_cndmask_b32 v117, v12, v6, s[92:93]\n" \
"Mvaa 180,116,36\nTa 162,20,41984\nv_cndmask_b32 v118, v12, v6, s[74:75]\nMvaa 180,148,52\nRa 0,16,32768\nv_cndmask_b32 v119, v12, v6, s[78:79]\nMvaa 176,144,52\nRa 16,17,32768\n" \
"v_cmp_gt_i32 s[90:91], 32, v30\nMvaa 176,112,36\nTa 68,21,32768\nv_cmp_gt_i32 s[92:93], 33, v30\nMvaa 172,108,36\nTa 70,21,33792\nv_cmp_gt_i32 s[74:75], 34, v30\nMvaa 172,140,52\n" \
"Ta 164,21,40960\nv_cmp_gt_i32 s[78:79], 35, v30\nMvaa 168,136,52\nTa 166,21,41984\nv_cndmask_b32 v120, v12, v6, s[90:91]\nMvaa 168,104,36\nTa 72,22,32768\nv_cndmask_b32 v121, v12, v6, s[92:93]\n" \
"Mvaa 164,100,36\nTa 74,22,33792\nv_cndmask_b32 v122, v12, v6, s[74:75]\nMvaa 164,132,52\nTa 168,22,40960\nv_cndmask_b32 v123, v12, v6, s[78:79]\nMvaa 160,128,52\nTa 170,22,41984\n" \
"v_cmp_gt_i32 s[90:91], 48, v30\nMvaa 160,96,36\nTa 76,23,32768\nv_cmp_gt_i32 s[92:93], 49, v30\nMvaa 192,96,40\nTa 78,23,33792\nv_cmp_gt_i32 s[74:75], 50, v30\nMvaa 192,128,56\n" \
"Ta 172,23,40960\nv_cmp_gt_i32 s[78:79], 51, v30\nMvaa 196,132,56\nTa 174,23,41984\nv_cndmask_b32 v124, v12, v6, s[90:91]\nMvaa 196,100,40\nTa 80,24,32768\nv_cndmask_b32 v125, v12, v6, s[92:93]\n" \
"Mvaa 200,104,40\nTa 82,24,33792\nv_cndmask_b32 v126, v12, v6, s[74:75]\nMvaa 200,136,56\nv_cndmask_b32 v127, v12, v6, s[78:79]\nMvaa 204,140,56\nTa 176,24,40960\nMvaa 204,108,40\n" \
"Ta 178,24,41984\nMvaa 208,112,40\nTa 84,25,32768\nMvaa 208,144,56\nTa 86,25,33792\nMvaa 212,148,56\nTa 180,25,40960\nMvaa 212,116,40\n" \
"Ta 182,25,41984\nMvaa 216,120,40\nTa 88,26,32768\nMvaa 216,152,56\nTa 90,26,33792\nMvaa 220,156,56\nTa 184,26,40960\nMvaa 220,124,40\n" \
"Ta 186,26,41984\nMvaa 252,124,44\nTa 92,27,32768\nMvaa 252,156,60\nTa 94,27,33792\nMvaa 248,152,60\nTa 188,27,40960\nMvaa 248,120,44\n" \
"Ta 190,27,41984\nMvaa 244,116,44\nRa 4,16,34816\nMvaa 244,148,60\nRa 20,17,34816\nMvaa 240,144,60\nRa 8,16,36864\nMvaa 240,112,44\n" \
"Ra 24,17,36864\nMvaa 236,108,44\nRa 12,16,38912\nMvaa 236,140,60\nRa 28,17,38912\nMvaa 232,136,60\nMvaa 232,104,44\nMvaa 228,100,44\n" \
"Mvaa 228,132,60\nMvaa 224,128,60\nMvaa 224,96,44\ns_waitcnt lgkmcnt(6)\nMvaa 128,64,0\nv_and_b32 v30, 3, v29\nv_bfe_u32 v6, v29, 1, 1\nMvaa 128,160,16\n" \
"v_lshrrev_b32 v28, 1, v28\nv_cmp_eq_u32 s[90:91], 2, v30\nMvaa 132,164,16\nv_cmp_eq_u32 s[92:93], v6, v28\nv_and_b32 v30, 1, v29\nMvaa 132,68,0\nv_mov_b32 v6, 0x3f803f80\nv_cmp_eq_u32 s[78:79], 1, v30\n" \
"Mvaa 136,72,0\nv_mov_b32 v7, 0x4444\nv_mov_b32 v30, 0xeeee\nMvaa 136,168,16\nv_cndmask_b32 v108, 0, v6, s[90:91]\nv_cndmask_b32 v109, 0, v6, s[90:91]\nMvaa 140,172,16\nv_cndmask_b32 v110, 0, v6, s[90:91]\n" \
"v_cndmask_b32 v111, 0, v6, s[90:91]\nMvaa 140,76,0\ns_waitcnt lgkmcnt(0)\nv_cndmask_b32 v104, 0, v6, s[92:93]\nMvaa 144,80,0\nv_cndmask_b32 v105, 0, v6, s[92:93]\nv_cndmask_b32 v106, 0, v6, s[92:93]\nMvaa 144,176,16\n" \
"v_cndmask_b32 v107, 0, v6, s[92:93]\nv_cndmask_b32 v7, v7, v30, s[78:79]\nMvaa 148,180,16\nbuffer_store_dwordx4 v[92:95], v0, s[60:63], s95 offen offset:3072 sc1\nMvaa 148,84,0\nv_mov_b32 v96, 0\nv_mov_b32 v97, 0\nMvaa 152,88,0\n" \
"v_mov_b32 v98, 0\nv_mov_b32 v99, 0\nMvaa 152,184,16\nv_mov_b32 v100, 0\nv_mov_b32 v101, 0\nMvaa 156,188,16\nv_mov_b32 v102, 0\nv_mov_b32 v103, 0\n" \
"Mvaa 156,92,0\nv_mov_b32 v5, v0\ns_mov_b32 s73, s32\nMvaa 188,92,4\ns_waitcnt vmcnt(2)\ns_barrier\nv_cmp_ne_u64 vcc, v[10:11], v[2:3]\ns_cbranch_vccnz zs101\n" \
"zr101:\nMvaa 188,188,20\ns_mov_b32 m0, s32\nTa 96,20,16384\nMvaa 184,184,20\nDS 0,60,s77,0\nMvaa 184,88,4\nTa 98,20,17408\n" \
"Ta 128,20,24576\nMvaa 180,84,4\nTa 130,20,25600\ns_add_u32 s74, s32, 0x4000\nMvaa 180,180,20\nRa 32,18,0\ns_add_u32 s75, s32, 0x8000\nMvaa 176,176,20\n" \
"DS 0,60,s77,1024\nMvaa 176,80,4\nRa 48,19,0\nTa 100,21,16384\nMvaa 172,76,4\nTa 102,21,17408\ns_mov_b32 s97, 0x3e0293ee\nMvaa 172,172,20\n" \
"Ta 132,21,24576\ns_mov_b32 s89, 31744\nMvaa 168,168,20\nDS 0,60,s77,2048\nMvaa 168,72,4\nTa 134,21,25600\nTa 104,22,16384\nMvaa 164,68,4\n" \
"Ta 106,22,17408\ns_mov_b32 s90, 6144\nMvaa 164,164,20\nTa 136,22,24576\nTa 138,22,25600\nMvaa 160,160,20\nDS 0,60,s77,3072\nMvaa 160,64,4\n" \
"Ta 108,23,16384\ns_mov_b32 s91, 37888\nMvaa 192,64,8\nTa 110,23,17408\ns_mov_b32 s92, 0x200000\nMvaa 192,160,24\ns_add_u32 m0, s32, 16384\ns_add_u32 s77, s77, 0x4000\n" \
"Mvaa 196,164,24\nDS 0,60,s77,0\nMvaa 196,68,8\nTa 140,23,24576\nTa 142,23,25600\nMvaa 200,72,8\nTa 112,24,16384\ns_mov_b32 s78, s11\n" \
"Mvaa 200,168,24\nTa 114,24,17408\ns_add_u32 s84, s32, 98304\nMvaa 204,172,24\nDS 0,60,s77,1024\nMvaa 204,76,8\nTa 144,24,24576\nTa 146,24,25600\n" \
"Mvaa 208,80,8\nTa 116,25,16384\ns_mov_b32 s85, 8192\nMvaa 208,176,24\nTa 118,25,17408\nTa 148,25,24576\nMvaa 212,180,24\nDS 0,60,s77,2048\n" \
"Mvaa 212,84,8\nTa 150,25,25600\ns_mov_b32 s86, 16384\nMvaa 216,88,8\nTa 120,26,16384\ns_mov_b32 s87, 24576\nMvaa 216,184,24\nTa 122,26,17408\n" \
"Ta 152,26,24576\nMvaa 220,188,24\nDS 0,60,s77,3072\nMvaa 220,92,8\nTa 154,26,25600\ns_xor_b32 s82, s11, s10\nMvaa 252,92,12\nTa 124,27,16384\n" \
"Ta 126,27,17408\nMvaa 252,188,28\ns_add_u32 m0, s32, 49152\ns_lshl_b32 s83, s82, 16\nMvaa 248,184,28\nDS 8,60,s77,0\nMvaa 248,88,12\nTa 156,27,24576\n" \
"Ta 158,27,25600\nMvaa 244,84,12\nRa 36,18,2048\ns_add_u32 s83, s83, s18\nMvaa 244,180,28\nRa 52,19,2048\nMvaa 240,176,28\nDS 8,60,s77,1024\n" \
"Mvaa 240,80,12\nRa 40,18,4096\nRa 56,19,4096\nMvaa 236,76,12\nRa 44,18,6144\nMvaa 236,172,28\nRa 60,19,6144\nMvaa 232,168,28\n" \
"DS 8,60,s77,2048\nMvaa 232,72,12\nMvaa 228,68,12\nMvaa 228,164,28\nMvaa 224,160,28\nDS 8,60,s77,3072\nMvaa 224,64,12\ns_waitcnt lgkmcnt(6)\n" \
"Mvaa 128,96,32\ns_add_u32 m0, s32, 32768\ns_add_u32 s77, s77, 0x4000\nMvaa 128,128,48\nDS 0,60,s77,0\nMvaa 132,132,48\nv_lshrrev_b32 v17, 8, v0\nv_and_b32 v16, 0xf0, v0\n" \
"s_branch 99f\n.p2align 12\nzp7:\ns_setpc_b64 s[98:99]\n99:\nMvaa 132,100,32\nDS 0,60,s77,1024\nMvaa 136,104,32\n" \
"v_lshlrev_b32 v18, 5, v17\nv_xor_b32 v16, v16, v18\nMvaa 136,136,48\nDS 0,60,s77,2048\nMvaa 140,140,48\nv_lshl_add_u32 v16, v17, 11, v16\nv_add_u32 v16, s20, v16\nMvaa 140,108,32\n" \
"s_waitcnt lgkmcnt(0)\nDS 0,60,s77,3072\nMvaa 144,112,32\ns_add_u32 m0, s32, 65536\ns_xor_b32 s82, s78, s10\nMvaa 144,144,48\nDS 8,60,s77,0\nMvaa 148,148,48\n" \
"s_and_b32 s80, s82, 31\ns_lshl_b32 s80, s80, 18\nMvaa 148,116,32\nDS 8,60,s77,1024\nMvaa 152,120,32\ns_cmp_lt_u32 s78, 32\ns_cselect_b64 s[68:69], s[14:15], s[12:13]\nMvaa 152,152,48\n" \
"DS 8,60,s77,2048\nMvaa 156,156,48\ns_cselect_b32 s70, 0x800000, s92\nv_add_u32 v18, s80, v16\nMvaa 156,124,32\nDS 8,60,s77,3072\nv_lshlrev_b32 v29, 2, v0\nMvaa 160,96,36\n" \
"s_add_u32 s77, s77, 0x4000\nv_xor_b32 v19, 128, v18\nMvaa 160,128,52\nMvaa 164,132,52\nMvaa 164,100,36\nbuffer_load_dword v30, v29, s[64:67], s46 offen sc1\nMvaa 168,104,36\nMvaa 168,136,52\n" \
"v_mul_f32 v128, s97, v128\nv_mul_f32 v129, s97, v129\nv_mul_f32 v130, s97, v130\nMvaa 172,140,52\nv_mul_f32 v131, s97, v131\nv_mul_f32 v132, s97, v132\nv_mul_f32 v133, s97, v133\nMvaa 172,108,36\n" \
"v_mul_f32 v134, s97, v134\nv_mul_f32 v135, s97, v135\nC 32,128,129\nMvaa 176,112,36\nC 33,130,131\nv_mul_f32 v136, s97, v136\nv_mul_f32 v137, s97, v137\nMvaa 176,144,52\n" \
"v_mul_f32 v138, s97, v138\nv_mul_f32 v139, s97, v139\nC 34,132,133\nMvaa 180,148,52\nC 35,134,135\nv_mul_f32 v140, s97, v140\nv_mul_f32 v141, s97, v141\nMvaa 180,116,36\n" \
"v_mul_f32 v142, s97, v142\nv_mul_f32 v143, s97, v143\nC 36,136,137\nMvaa 184,120,36\nC 37,138,139\nv_mul_f32 v144, s97, v144\nv_mul_f32 v145, s97, v145\nMvaa 184,152,52\n" \
"v_mul_f32 v146, s97, v146\nv_mul_f32 v147, s97, v147\nC 38,140,141\nMvaa 188,156,52\nC 39,142,143\nv_mul_f32 v148, s97, v148\nv_mul_f32 v149, s97, v149\nMvaa 188,124,36\n" \
"v_mul_f32 v150, s97, v150\nv_mul_f32 v151, s97, v151\nC 40,144,145\nMvaa 192,96,40\nC 41,146,147\nv_mul_f32 v152, s97, v152\nv_mul_f32 v153, s97, v153\nMvaa 192,128,56\n" \
"v_mul_f32 v154, s97, v154\nv_mul_f32 v155, s97, v155\nC 42,148,149\nMvaa 196,132,56\nC 43,150,151\nv_mul_f32 v156, s97, v156\nv_mul_f32 v157, s97, v157\nMvaa 196,100,40\n" \
"v_mul_f32 v158, s97, v158\nv_mul_f32 v159, s97, v159\nC 44,152,153\nMvaa 200,104,40\nC 45,154,155\nC 46,156,157\nC 47,158,159\nMvaa 200,136,56\n" \
"v_mul_f32 v160, s97, v160\nv_mul_f32 v161, s97, v161\nv_mul_f32 v162, s97, v162\nMvaa 204,140,56\nv_mul_f32 v163, s97, v163\nv_mul_f32 v164, s97, v164\nv_mul_f32 v165, s97, v165\nMvaa 204,108,40\n" \
"v_mul_f32 v166, s97, v166\nv_mul_f32 v167, s97, v167\nC 48,160,161\nMvaa 208,112,40\ns_waitcnt vmcnt(17)\ns_barrier\nC 49,162,163\nMvaa 208,144,56\n" \
"Ra 160,5,0\nv_mul_f32 v168, s97, v168\nv_mul_f32 v169, s97, v169\nMvaa 212,148,56\nv_mul_f32 v170, s97, v170\nv_mul_f32 v171, s97, v171\nC 50,164,165\nMvaa 212,116,40\n" \
"Ra 164,5,1024\nC 51,166,167\nv_mul_f32 v172, s97, v172\nMvaa 216,120,40\nv_mul_f32 v173, s97, v173\nv_mul_f32 v174, s97, v174\nv_mul_f32 v175, s97, v175\nMvaa 216,152,56\n" \
"Ra 168,5,2048\nC 52,168,169\nC 53,170,171\nMvaa 220,156,56\nv_mul_f32 v176, s97, v176\nv_mul_f32 v177, s97, v177\nv_mul_f32 v178, s97, v178\nMvaa 220,124,40\n" \
"Ra 172,5,3072\nv_mul_f32 v179, s97, v179\nC 54,172,173\nMvaa 224,96,44\nC 55,174,175\nv_mul_f32 v180, s97, v180\nv_mul_f32 v181, s97, v181\nMvaa 224,128,60\n" \
"Ra 176,5,4096\nv_mul_f32 v182, s97, v182\nv_mul_f32 v183, s97, v183\nMvaa 228,132,60\nC 56,176,177\nC 57,178,179\nv_mul_f32 v184, s97, v184\nMvaa 228,100,44\n" \
"Ra 180,5,5120\nv_mul_f32 v185, s97, v185\nv_mul_f32 v186, s97, v186\nMvaa 232,104,44\nv_mul_f32 v187, s97, v187\nC 58,180,181\nC 59,182,183\nMvaa 232,136,60\n" \
"Ra 184,5,6144\nv_mul_f32 v188, s97, v188\nv_mul_f32 v189, s97, v189\nMvaa 236,140,60\nv_mul_f32 v190, s97, v190\nv_mul_f32 v191, s97, v191\nC 60,184,185\nMvaa 236,108,44\n" \
"Ra 188,5,7168\nC 61,186,187\nC 62,188,189\nMvaa 240,112,44\nC 63,190,191\nv_mul_f32 v192, s97, v192\nv_mul_f32 v193, s97, v193\nMvaa 240,144,60\n" \
"v_mul_f32 v194, s97, v194\nv_mul_f32 v195, s97, v195\nv_mul_f32 v196, s97, v196\nMvaa 244,148,60\nv_mul_f32 v197, s97, v197\nv_mul_f32 v198, s97, v198\nv_mul_f32 v199, s97, v199\nMvaa 244,116,44\n" \
"C 64,192,193\nC 65,194,195\nv_mul_f32 v200, s97, v200\nMvaa 248,120,44\ns_bitcmp1_b32 s33, 2\nv_mul_f32 v201, s97, v201\nv_mul_f32 v202, s97, v202\nMvaa 248,152,60\n" \
"v_mul_f32 v203, s97, v203\nC 66,196,197\nC 67,198,199\nMvaa 252,156,60\nMvaa 252,124,44\ns_waitcnt lgkmcnt(0)\nQvav 128,160,32,12\nRa 128,5,8192\n" \
"v_mul_f32 v204, s97, v204\nv_mul_f32 v205, s97, v205\nMvav 128,164,36\nv_mul_f32 v206, s97, v206\nv_mul_f32 v207, s97, v207\nC 68,200,201\nMvav 128,168,40\nRa 132,5,9216\n" \
"C 69,202,203\nv_mul_f32 v208, s97, v208\nMvav 128,172,44\nv_mul_f32 v209, s97, v209\nv_mul_f32 v210, s97, v210\nv_mul_f32 v211, s97, v211\nQvav 132,176,32,12\nRa 136,5,10240\n" \
"C 70,204,205\nC 71,206,207\nMvav 132,180,36\nv_mul_f32 v212, s97, v212\nv_mul_f32 v213, s97, v213\nv_mul_f32 v214, s97, v214\nMvav 132,184,40\nRa 140,5,11264\n" \
"v_mul_f32 v215, s97, v215\nC 72,208,209\nMvav 132,188,44\nC 73,210,211\nv_mul_f32 v216, s97, v216\nv_mul_f32 v217, s97, v217\nQvav 136,160,48,12\nRa 144,5,12288\n" \
"v_mul_f32 v218, s97, v218\nv_mul_f32 v219, s97, v219\nMvav 136,164,52\nC 74,212,213\nC 75,214,215\nv_mul_f32 v220, s97, v220\nMvav 136,168,56\nRa 148,5,13312\n" \
"v_mul_f32 v221, s97, v221\nv_mul_f32 v222, s97, v222\nMvav 136,172,60\nv_mul_f32 v223, s97, v223\nC 76,216,217\nC 77,218,219\nQvav 140,176,48,12\nRa 152,5,14336\n" \
"C 78,220,221\nC 79,222,223\nMvav 140,180,52\nv_mul_f32 v224, s97, v224\nv_mul_f32 v225, s97, v225\nv_mul_f32 v226, s97, v226\nMvav 140,184,56\nRa 156,5,15360\n" \
"v_mul_f32 v227, s97, v227\nv_mul_f32 v228, s97, v228\nMvav 140,188,60\nv_mul_f32 v229, s97, v229\nv_mul_f32 v230, s97, v230\nv_mul_f32 v231, s97, v231\nQvav 144,160,64,12\nC 80,224,225\n" \
"C 81,226,227\nv_mul_f32 v232, s97, v232\nMvav 144,164,68\nv_mul_f32 v233, s97, v233\nv_mul_f32 v234, s97, v234\nv_mul_f32 v235, s97, v235\nMvav 144,168,72\nC 82,228,229\n" \
"C 83,230,231\nv_mul_f32 v236, s97, v236\nMvav 144,172,76\nv_mul_f32 v237, s97, v237\nv_mul_f32 v238, s97, v238\nv_mul_f32 v239, s97, v239\nQvav 148,176,64,12\nC 84,232,233\n" \
"C 85,234,235\nv_mul_f32 v240, s97, v240\nMvav 148,180,68\nv_mul_f32 v241, s97, v241\nv_mul_f32 v242, s97, v242\nv_mul_f32 v243, s97, v243\nC 86,236,237\nC 87,238,239\n" \
"v_mul_f32 v244, s97, v244\nv_mul_f32 v245, s97, v245\nv_mul_f32 v246, s97, v246\nv_mul_f32 v247, s97, v247\nC 88,240,241\nC 89,242,243\nv_mul_f32 v248, s97, v248\nv_mul_f32 v249, s97, v249\n" \
"v_mul_f32 v250, s97, v250\nv_mul_f32 v251, s97, v251\nC 90,244,245\nC 91,246,247\nv_mul_f32 v252, s97, v252\nv_mul_f32 v253, s97, v253\nv_mul_f32 v254, s97, v254\nv_mul_f32 v255, s97, v255\n" \
"C 92,248,249\nC 93,250,251\nC 94,252,253\nC 95,254,255\nMvav 148,184,72\nE 240,128\nMvav 148,188,76\nE 241,129\n" \
"Qvav 152,160,80,12\nC 192,240,241\nE 242,130\nMvav 152,164,84\nE 243,131\nMvav 152,168,88\nC 193,242,243\nMvav 152,172,92\n" \
"E 244,132\nQvav 156,176,80,12\nE 245,133\nMvav 156,180,84\nC 194,244,245\nE 246,134\nMvav 156,184,88\nE 247,135\n" \
"Mvav 156,188,92\ns_waitcnt lgkmcnt(0)\nC 195,246,247\nQvav 160,128,32,12\nE 240,136\nMvav 160,132,36\nE 241,137\nMvav 160,136,40\n" \
"C 196,240,241\nE 242,138\nMvav 160,140,44\nQvav 168,128,48,12\nv_mov_b32 v6, v5\nv_add_u32 v5, s74, v9\nE 243,139\nMvav 168,132,52\n" \
"C 197,242,243\nE 244,140\nMvav 168,136,56\nE 245,141\nMvav 168,140,60\nC 198,244,245\nE 246,142\nQvav 176,128,64,12\n" \
"s_waitcnt vmcnt(9)\ns_barrier\nMvav 176,132,68\nRa 160,5,0\nE 247,143\nMvav 176,136,72\ns_mov_b32 m0, s73\nC 199,246,247\n" \
"E 240,144\nMvav 176,140,76\nRa 164,5,1024\nDS 0,60,s77,0\nE 241,145\nQvav 184,128,80,12\nC 200,240,241\nMvav 184,132,84\n" \
"Ra 168,5,2048\nE 242,146\nMvav 184,136,88\nE 243,147\nMvav 184,140,92\nRa 172,5,3072\nC 201,242,243\nE 244,148\n" \
"Qvav 164,144,32,12\nDS 0,60,s77,1024\nE 245,149\nMvav 164,148,36\nRa 176,5,4096\nC 202,244,245\nMvav 164,152,40\nE 246,150\n" \
"Mvav 164,156,44\nRa 180,5,5120\nE 247,151\nQvav 172,144,48,12\nC 203,246,247\nE 240,152\nMvav 172,148,52\nRa 184,5,6144\n" \
"DS 0,60,s77,2048\nMvav 172,152,56\nE 241,153\nMvav 172,156,60\nRa 188,5,7168\nC 204,240,241\nE 242,154\nQvav 180,144,64,12\n" \
"E 243,155\nMvav 180,148,68\nC 205,242,243\nE 244,156\nMvav 180,152,72\nDS 0,60,s77,3072\nMvav 180,156,76\nE 245,157\n" \
"Qvav 188,144,80,12\nC 206,244,245\nE 246,158\nMvav 188,148,84\nE 247,159\nMvav 188,152,88\nC 207,246,247\ns_mov_b32 s96, s39\n" \
"Mvav 188,156,92\ns_waitcnt lgkmcnt(0)\nQvav 128,160,32,12\nE 240,160\nMvav 128,164,36\nE 241,161\nMvav 128,168,40\nC 208,240,241\n" \
"Ra 128,6,49152\nMvav 128,172,44\nE 242,162\ns_add_u32 m0, s75, 0xc000\nQvav 136,160,48,12\nDS 8,60,s77,0\nMvav 136,164,52\nE 243,163\n" \
"Mvav 136,168,56\nC 209,242,243\nRa 132,6,50176\nMvav 136,172,60\nE 244,164\nQvav 144,160,64,12\nDS 8,60,s77,1024\nMvav 144,164,68\n" \
"E 245,165\nMvav 144,168,72\nC 210,244,245\nRa 136,6,51200\nMvav 144,172,76\nE 246,166\ns_add_u32 s95, s34, 16384\nQvav 152,160,80,12\n" \
"buffer_store_dwordx4 a[192:195], v0, s[60:63], s95 offen offset:0 sc1\nMvav 152,164,84\nE 247,167\nMvav 152,168,88\nC 211,246,247\nRa 140,6,52224\nMvav 152,172,92\nE 240,168\n" \
"Qvav 132,176,32,12\nDS 8,60,s77,2048\nMvav 132,180,36\nE 241,169\nMvav 132,184,40\nC 212,240,241\nRa 144,6,53248\nMvav 132,188,44\n" \
"E 242,170\nQvav 140,176,48,12\nbuffer_store_dwordx4 a[196:199], v0, s[60:63], s95 offen offset:1024 sc1\nMvav 140,180,52\nE 243,171\nMvav 140,184,56\nC 213,242,243\nRa 148,6,54272\n" \
"Mvav 140,188,60\nE 244,172\nQvav 148,176,64,12\nDS 8,60,s77,3072\nMvav 148,180,68\nE 245,173\nMvav 148,184,72\nC 214,244,245\n" \
"Ra 152,6,55296\nMvav 148,188,76\nE 246,174\nQvav 156,176,80,12\nRa 156,6,56320\ns_mov_b32 s76, s73\nMvav 156,180,84\nE 247,175\n" \
"Mvav 156,184,88\nC 215,246,247\ns_mov_b32 s73, s74\nMvav 156,188,92\nE 240,176\nv_smfmac_f32_16x16x64_bf16 v[96:99], v[104:107], v[192:199], v7\ns_mov_b32 s74, s75\nv_smfmac_f32_16x16x64_bf16 v[100:103], v[104:107], v[200:207], v7\n" \
"s_waitcnt lgkmcnt(0)\nZaav 96,128,204\nbuffer_store_dwordx4 a[200:203], v0, s[60:63], s95 offen offset:2048 sc1\nZaav 100,132,204\nE 241,177\nZaav 68,132,200\nC 216,240,241\nRa 160,5,8192\n" \
"Zaav 64,128,200\nE 242,178\nZaav 32,128,196\nRa 164,5,9216\ns_mov_b32 s75, s76\nZaav 36,132,196\nE 243,179\nZaav 4,132,192\n" \
"C 217,242,243\ns_add_u32 s77, s77, 0x4000\nZaav 0,128,192\nE 244,180\nZaav 8,136,192\nRa 168,5,10240\nZaav 12,140,192\nE 245,181\n" \
"Zaav 44,140,196\nC 218,244,245\nRa 172,5,11264\nZaav 40,136,196\nE 246,182\nZaav 72,136,200\nRa 176,5,12288\nZaav 76,140,200\n" \
"E 247,183\nZaav 108,140,204\nC 219,246,247\nZaav 104,136,204\nE 240,184\nZaav 112,144,204\nbuffer_store_dwordx4 a[204:207], v0, s[60:63], s95 offen offset:3072 sc1\nZaav 116,148,204\n" \
"E 241,185\nZaav 84,148,200\nC 220,240,241\nRa 180,5,13312\nZaav 80,144,200\nE 242,186\nZaav 48,144,196\nRa 184,5,14336\n" \
"Zaav 52,148,196\nE 243,187\nZaav 20,148,192\nC 221,242,243\nZaav 16,144,192\nE 244,188\nZaav 24,152,192\nRa 188,5,15360\n" \
"Zaav 28,156,192\nE 245,189\nZaav 60,156,196\nC 222,244,245\nZaav 56,152,196\nE 246,190\nZaav 88,152,200\nZaav 92,156,200\n" \
"E 247,191\nZaav 124,156,204\nC 223,246,247\ns_waitcnt lgkmcnt(0)\nZaav 120,152,204\nQvav 160,160,32,12\nE 240,128\nMvav 160,164,36\n" \
"E 241,129\nMvav 160,168,40\nC 192,240,241\nRa 128,6,57344\nMvav 160,172,44\nE 242,130\ns_add_u32 s95, s34, 20480\nQvav 168,160,48,12\n" \
"buffer_store_dwordx4 a[208:211], v0, s[60:63], s95 offen offset:0 sc1\nMvav 168,164,52\nE 243,131\nMvav 168,168,56\nC 193,242,243\nMvav 168,172,60\nE 244,132\nQvav 176,160,64,12\n" \
"Ra 132,6,58368\nMvav 176,164,68\nE 245,133\nMvav 176,168,72\nC 194,244,245\nRa 136,6,59392\nMvav 176,172,76\nE 246,134\n" \
"Qvav 184,160,80,12\nRa 140,6,60416\nMvav 184,164,84\nE 247,135\nMvav 184,168,88\nC 195,246,247\nMvav 184,172,92\nE 240,136\n" \
"Qvav 164,176,32,12\nRa 144,6,61440\nMvav 164,180,36\nE 241,137\nMvav 164,184,40\nC 196,240,241\nRa 148,6,62464\nMvav 164,188,44\n" \
"E 242,138\nQvav 172,176,48,12\nbuffer_store_dwordx4 a[212:215], v0, s[60:63], s95 offen offset:1024 sc1\nMvav 172,180,52\nE 243,139\nMvav 172,184,56\nC 197,242,243\nMvav 172,188,60\n" \
"E 244,140\ns_branch 99f\n.p2align 12\nzp8:\ns_setpc_b64 s[98:99]\n99:\nQvav 180,176,64,12\nRa 152,6,63488\n" \
"Mvav 180,180,68\nE 245,141\nMvav 180,184,72\nC 198,244,245\nRa 156,6,64512\nMvav 180,188,76\nE 246,142\nQvav 188,176,80,12\n" \
"v_mov_b32 v6, v5\nv_add_u32 v5, s74, v9\nMvav 188,180,84\nE 247,143\nMvav 188,184,88\nC 199,246,247\nMvav 188,188,92\nE 240,144\n" \
"v_smfmac_f32_16x16x64_bf16 v[96:99], v[104:107], v[208:215], v7\nv_smfmac_f32_16x16x64_bf16 v[100:103], v[104:107], v[216:223], v7\ns_waitcnt vmcnt(15) lgkmcnt(0)\ns_barrier\nMaav 96,128,220\nbuffer_store_dwordx4 a[216:219], v0, s[60:63], s95 offen offset:2048 sc1\nMaav 100,132,220\nE 241,145\n" \
"Maav 68,132,216\nC 200,240,241\nRa 160,5,0\nMaav 64,128,216\nE 242,146\ns_mov_b32 m0, s73\nMaav 32,128,212\nDS 0,60,s77,0\n" \
"Maav 36,132,212\nE 243,147\nMaav 4,132,208\nC 201,242,243\nRa 164,5,1024\nMaav 0,128,208\nE 244,148\nMaav 8,136,208\n" \
"DS 0,60,s77,1024\nMaav 12,140,208\nE 245,149\nMaav 44,140,212\nC 202,244,245\nRa 168,5,2048\nMaav 40,136,212\nE 246,150\n" \
"Maav 72,136,216\nRa 172,5,3072\nRa 176,5,4096\nMaav 76,140,216\nE 247,151\nMaav 108,140,220\nC 203,246,247\nRa 180,5,5120\n" \
"Maav 104,136,220\nE 240,152\nMaav 112,144,220\nDS 0,60,s77,2048\nMaav 116,148,220\nE 241,153\nMaav 84,148,216\nC 204,240,241\n" \
"Ra 184,5,6144\nMaav 80,144,216\nE 242,154\nMaav 48,144,212\nbuffer_store_dwordx4 a[220:223], v0, s[60:63], s95 offen offset:3072 sc1\nMaav 52,148,212\nE 243,155\nMaav 20,148,208\n" \
"C 205,242,243\nRa 188,5,7168\nMaav 16,144,208\nE 244,156\nMaav 24,152,208\nDS 0,60,s77,3072\nMaav 28,156,208\nE 245,157\n" \
"Maav 60,156,212\nC 206,244,245\nMaav 56,152,212\nE 246,158\nMaav 88,152,216\nMaav 92,156,216\nE 247,159\nMaav 124,156,220\n" \
"C 207,246,247\ns_waitcnt lgkmcnt(0)\nMaav 120,152,220\nQvav 128,160,32,12\nE 240,160\nMvav 128,164,36\nE 241,161\nMvav 128,168,40\n" \
"C 208,240,241\nRa 128,6,49152\nMvav 128,172,44\nE 242,162\ns_add_u32 m0, s75, 0xc000\nQvav 136,160,48,12\nDS 8,60,s77,0\nMvav 136,164,52\n" \
"E 243,163\nMvav 136,168,56\nC 209,242,243\nRa 132,6,50176\nMvav 136,172,60\nE 244,164\nQvav 144,160,64,12\nDS 8,60,s77,1024\n" \
"Mvav 144,164,68\nE 245,165\nMvav 144,168,72\nC 210,244,245\nRa 136,6,51200\nMvav 144,172,76\nE 246,166\ns_add_u32 s95, s34, 24576\n" \
"Qvav 152,160,80,12\nbuffer_store_dwordx4 a[224:227], v0, s[60:63], s95 offen offset:0 sc1\nMvav 152,164,84\nE 247,167\nMvav 152,168,88\nC 211,246,247\nRa 140,6,52224\nMvav 152,172,92\n" \
"E 240,168\nQvav 132,176,32,12\nDS 8,60,s77,2048\nMvav 132,180,36\nE 241,169\nMvav 132,184,40\nC 212,240,241\nRa 144,6,53248\n" \
"Mvav 132,188,44\nE 242,170\nQvav 140,176,48,12\nbuffer_store_dwordx4 a[228:231], v0, s[60:63], s95 offen offset:1024 sc1\nMvav 140,180,52\nE 243,171\nMvav 140,184,56\nC 213,242,243\n" \
"Ra 148,6,54272\nMvav 140,188,60\nE 244,172\nQvav 148,176,64,12\nDS 8,60,s77,3072\nMvav 148,180,68\nE 245,173\nMvav 148,184,72\n" \
"C 214,244,245\nRa 152,6,55296\nMvav 148,188,76\nE 246,174\nQvav 156,176,80,12\nRa 156,6,56320\ns_mov_b32 s76, s73\nMvav 156,180,84\n" \
"E 247,175\nMvav 156,184,88\nC 215,246,247\ns_mov_b32 s73, s74\nMvav 156,188,92\nE 240,176\nv_smfmac_f32_16x16x64_bf16 v[96:99], v[104:107], v[192:199], v7\ns_mov_b32 s74, s75\n" \
"v_smfmac_f32_16x16x64_bf16 v[100:103], v[104:107], v[200:207], v7\ns_waitcnt lgkmcnt(0)\nMaav 96,128,204\nbuffer_store_dwordx4 a[232:235], v0, s[60:63], s95 offen offset:2048 sc1\nMaav 100,132,204\nE 241,177\nMaav 68,132,200\nC 216,240,241\n" \
"Ra 160,5,8192\nMaav 64,128,200\nE 242,178\nMaav 32,128,196\nRa 164,5,9216\ns_mov_b32 s75, s76\nMaav 36,132,196\nE 243,179\n" \
"Maav 4,132,192\nC 217,242,243\ns_add_u32 s77, s77, 0x4000\nMaav 0,128,192\nE 244,180\nMaav 8,136,192\nRa 168,5,10240\nMaav 12,140,192\n" \
"E 245,181\nMaav 44,140,196\nC 218,244,245\nRa 172,5,11264\nMaav 40,136,196\nE 246,182\nMaav 72,136,200\nRa 176,5,12288\n" \
"Maav 76,140,200\nE 247,183\nMaav 108,140,204\nC 219,246,247\nMaav 104,136,204\nE 240,184\nMaav 112,144,204\nbuffer_store_dwordx4 a[236:239], v0, s[60:63], s95 offen offset:3072 sc1\n" \
"Maav 116,148,204\nE 241,185\nMaav 84,148,200\nC 220,240,241\nRa 180,5,13312\nMaav 80,144,200\nE 242,186\nMaav 48,144,196\n" \
"Ra 184,5,14336\nMaav 52,148,196\nE 243,187\nMaav 20,148,192\nC 221,242,243\nMaav 16,144,192\nE 244,188\nMaav 24,152,192\n" \
"Ra 188,5,15360\nMaav 28,156,192\nE 245,189\nMaav 60,156,196\nC 222,244,245\nMaav 56,152,196\nE 246,190\nMaav 88,152,200\n" \
"Maav 92,156,200\nE 247,191\nMaav 124,156,204\nC 223,246,247\ns_waitcnt lgkmcnt(0)\nMaav 120,152,204\nQvav 160,160,32,12\nE 240,128\n" \
"Mvav 160,164,36\nE 241,129\nMvav 160,168,40\nC 192,240,241\nRa 128,6,57344\nMvav 160,172,44\nE 242,130\ns_add_u32 s95, s34, 28672\n" \
"Qvav 168,160,48,12\nbuffer_store_dwordx4 a[240:243], v0, s[60:63], s95 offen offset:0 sc1\nMvav 168,164,52\nE 243,131\nMvav 168,168,56\nC 193,242,243\nMvav 168,172,60\nE 244,132\n" \
"Qvav 176,160,64,12\nRa 132,6,58368\nMvav 176,164,68\nE 245,133\nMvav 176,168,72\nC 194,244,245\nRa 136,6,59392\nMvav 176,172,76\n" \
"E 246,134\nQvav 184,160,80,12\nRa 140,6,60416\nMvav 184,164,84\nE 247,135\nMvav 184,168,88\nC 195,246,247\nMvav 184,172,92\n" \
"E 240,136\nQvav 164,176,32,12\nRa 144,6,61440\nMvav 164,180,36\nE 241,137\nMvav 164,184,40\nC 196,240,241\nRa 148,6,62464\n" \
"Mvav 164,188,44\nE 242,138\nQvav 172,176,48,12\nbuffer_store_dwordx4 a[244:247], v0, s[60:63], s95 offen offset:1024 sc1\nMvav 172,180,52\nE 243,139\nMvav 172,184,56\nC 197,242,243\n" \
"Mvav 172,188,60\nE 244,140\nQvav 180,176,64,12\nRa 152,6,63488\nMvav 180,180,68\nE 245,141\nMvav 180,184,72\nC 198,244,245\n" \
"Ra 156,6,64512\nMvav 180,188,76\nE 246,142\nQvav 188,176,80,12\nv_mov_b32 v6, v5\nv_add_u32 v5, s74, v9\nMvav 188,180,84\nE 247,143\n" \
"Mvav 188,184,88\nC 199,246,247\nMvav 188,188,92\nE 240,144\nv_smfmac_f32_16x16x64_bf16 v[96:99], v[104:107], v[208:215], v7\nv_smfmac_f32_16x16x64_bf16 v[100:103], v[104:107], v[216:223], v7\ns_waitcnt vmcnt(20) lgkmcnt(0)\ns_barrier\n" \
"Maav 96,128,220\nbuffer_store_dwordx4 a[248:251], v0, s[60:63], s95 offen offset:2048 sc1\nMaav 100,132,220\nE 241,145\nMaav 68,132,216\nC 200,240,241\nRa 160,5,0\nMaav 64,128,216\n" \
"E 242,146\ns_mov_b32 m0, s73\nMaav 32,128,212\nDS 0,60,s77,0\nMaav 36,132,212\nE 243,147\nMaav 4,132,208\nC 201,242,243\n" \
"Ra 164,5,1024\nMaav 0,128,208\nE 244,148\nMaav 8,136,208\nDS 0,60,s77,1024\nMaav 12,140,208\nE 245,149\nMaav 44,140,212\n" \
"C 202,244,245\nRa 168,5,2048\nMaav 40,136,212\nE 246,150\nMaav 72,136,216\nRa 172,5,3072\nRa 176,5,4096\nMaav 76,140,216\n" \
"E 247,151\nMaav 108,140,220\nC 203,246,247\nRa 180,5,5120\nMaav 104,136,220\nE 240,152\nMaav 112,144,220\nDS 0,60,s77,2048\n" \
"Maav 116,148,220\nE 241,153\nMaav 84,148,216\nC 204,240,241\nRa 184,5,6144\nMaav 80,144,216\nE 242,154\nMaav 48,144,212\n" \
"buffer_store_dwordx4 a[252:255], v0, s[60:63], s95 offen offset:3072 sc1\nMaav 52,148,212\nE 243,155\nMaav 20,148,208\nC 205,242,243\nRa 188,5,7168\nMaav 16,144,208\nE 244,156\n" \
"Maav 24,152,208\nDS 0,60,s77,3072\nMaav 28,156,208\nE 245,157\nMaav 60,156,212\nC 206,244,245\nMaav 56,152,212\nE 246,158\n" \
"Maav 88,152,216\nMaav 92,156,216\nE 247,159\nMaav 124,156,220\nC 207,246,247\ns_waitcnt lgkmcnt(0)\nMaav 120,152,220\nQvav 128,160,32,12\n" \
"E 240,160\nMvav 128,164,36\nE 241,161\nMvav 128,168,40\nC 208,240,241\nRa 128,6,49152\nMvav 128,172,44\nE 242,162\n" \
"s_add_u32 m0, s75, 0xc000\nQvav 136,160,48,12\nDS 8,60,s77,0\nMvav 136,164,52\nE 243,163\nMvav 136,168,56\nC 209,242,243\nRa 132,6,50176\n" \
"Mvav 136,172,60\nE 244,164\nQvav 144,160,64,12\nDS 8,60,s77,1024\nMvav 144,164,68\nE 245,165\nMvav 144,168,72\nC 210,244,245\n" \
"Mvav 144,172,76\nE 246,166\nQvav 152,160,80,12\nRa 136,6,51200\nRa 140,6,52224\nMvav 152,164,84\nE 247,167\nMvav 152,168,88\n" \
"C 211,246,247\nMvav 152,172,92\nE 240,168\nQvav 132,176,32,12\nDS 8,60,s77,2048\nMvav 132,180,36\nE 241,169\nMvav 132,184,40\n" \
"C 212,240,241\nRa 144,6,53248\nMvav 132,188,44\nE 242,170\nQvav 140,176,48,12\nRa 148,6,54272\nMvav 140,180,52\nE 243,171\n" \
"Mvav 140,184,56\nC 213,242,243\nRa 152,6,55296\nMvav 140,188,60\nE 244,172\nQvav 148,176,64,12\nDS 8,60,s77,3072\nMvav 148,180,68\n" \
"E 245,173\nMvav 148,184,72\nC 214,244,245\ns_mov_b32 s76, s73\nMvav 148,188,76\nE 246,174\nQvav 156,176,80,12\nRa 156,6,56320\n" \
"s_mov_b32 s73, s74\nMvav 156,180,84\nE 247,175\nMvav 156,184,88\nC 215,246,247\ns_mov_b32 s74, s75\nMvav 156,188,92\nE 240,176\n" \
"v_smfmac_f32_16x16x64_bf16 v[96:99], v[104:107], v[192:199], v7\ns_mov_b32 s75, s76\nv_smfmac_f32_16x16x64_bf16 v[100:103], v[104:107], v[200:207], v7\ns_waitcnt lgkmcnt(0)\nMaav 96,128,204\nRa 160,5,8192\ns_add_u32 s77, s77, 0x4000\nMaav 100,132,204\n" \
"E 241,177\nMaav 68,132,200\nC 216,240,241\nRa 164,5,9216\nMaav 64,128,200\nE 242,178\nMaav 32,128,196\nMaav 36,132,196\n" \
"E 243,179\nMaav 4,132,192\nC 217,242,243\nRa 168,5,10240\nMaav 0,128,192\nE 244,180\nMaav 8,136,192\nRa 172,5,11264\n" \
"Maav 12,140,192\nE 245,181\nMaav 44,140,196\nC 218,244,245\nMaav 40,136,196\nE 246,182\nMaav 72,136,200\nRa 176,5,12288\n" \
"Maav 76,140,200\nE 247,183\nMaav 108,140,204\nC 219,246,247\nMaav 104,136,204\nE 240,184\nMaav 112,144,204\nRa 180,5,13312\n" \
"Maav 116,148,204\nE 241,185\nMaav 84,148,200\nC 220,240,241\nMaav 80,144,200\nE 242,186\nMaav 48,144,196\nRa 184,5,14336\n" \
"Maav 52,148,196\nE 243,187\nMaav 20,148,192\nC 221,242,243\nRa 188,5,15360\nMaav 16,144,192\nE 244,188\nMaav 24,152,192\n" \
"Maav 28,156,192\nE 245,189\nMaav 60,156,196\nC 222,244,245\nMaav 56,152,196\nE 246,190\nMaav 88,152,200\nMaav 92,156,200\n" \
"E 247,191\nMaav 124,156,204\nC 223,246,247\ns_waitcnt lgkmcnt(0)\nMaav 120,152,204\nQvav 160,160,32,12\nE 240,128\nMvav 160,164,36\n" \
"E 241,129\nMvav 160,168,40\nC 192,240,241\nRa 128,6,57344\nMvav 160,172,44\nE 242,130\nQvav 168,160,48,12\nRa 132,6,58368\n" \
"Mvav 168,164,52\nE 243,131\nMvav 168,168,56\nC 193,242,243\nMvav 168,172,60\nE 244,132\nQvav 176,160,64,12\nRa 136,6,59392\n" \
"Mvav 176,164,68\nE 245,133\nMvav 176,168,72\nC 194,244,245\nMvav 176,172,76\nE 246,134\nQvav 184,160,80,12\nRa 140,6,60416\n" \
"Mvav 184,164,84\nE 247,135\nMvav 184,168,88\nC 195,246,247\nMvav 184,172,92\nE 240,136\nQvav 164,176,32,12\nRa 144,6,61440\n" \
"Mvav 164,180,36\nE 241,137\nMvav 164,184,40\nC 196,240,241\nMvav 164,188,44\nE 242,138\nQvav 172,176,48,12\nRa 148,6,62464\n" \
"Mvav 172,180,52\nE 243,139\nMvav 172,184,56\nC 197,242,243\nRa 152,6,63488\nMvav 172,188,60\nE 244,140\nQvav 180,176,64,12\n" \
"Mvav 180,180,68\nE 245,141\nMvav 180,184,72\nC 198,244,245\nRa 156,6,64512\nMvav 180,188,76\nE 246,142\nQvav 188,176,80,12\n" \
"v_mov_b32 v6, v5\nv_add_u32 v5, s74, v9\nMvav 188,180,84\ns_branch 99f\n.p2align 12\nzp9:\ns_setpc_b64 s[98:99]\n99:\n" \
"E 247,143\nMvav 188,184,88\nC 199,246,247\nMvav 188,188,92\nE 240,144\nv_smfmac_f32_16x16x64_bf16 v[96:99], v[104:107], v[208:215], v7\nv_smfmac_f32_16x16x64_bf16 v[100:103], v[104:107], v[216:223], v7\ns_waitcnt vmcnt(5) lgkmcnt(0)\n" \
"s_barrier\nMaav 96,128,220\nbuffer_store_dwordx2 v[2:3], off, s[60:63], s36 sc1\nMaav 100,132,220\nE 241,145\nMaav 68,132,216\nC 200,240,241\nRa 160,5,0\n" \
"Maav 64,128,216\nE 242,146\ns_mov_b32 m0, s73\nMaav 32,128,212\nDS 0,60,s77,0\nMaav 36,132,212\nE 243,147\nMaav 4,132,208\n" \
"C 201,242,243\nRa 164,5,1024\nMaav 0,128,208\nE 244,148\nMaav 8,136,208\nDS 0,60,s77,1024\nMaav 12,140,208\nE 245,149\n" \
"Maav 44,140,212\nC 202,244,245\nRa 168,5,2048\nMaav 40,136,212\nE 246,150\nMaav 72,136,216\nRa 172,5,3072\nMaav 76,140,216\n" \
"E 247,151\nMaav 108,140,220\nC 203,246,247\nRa 176,5,4096\nMaav 104,136,220\nE 240,152\nMaav 112,144,220\nDS 0,60,s77,2048\n" \
"Maav 116,148,220\nE 241,153\nMaav 84,148,216\nC 204,240,241\nRa 180,5,5120\nMaav 80,144,216\nE 242,154\nMaav 48,144,212\n" \
"Ra 184,5,6144\nRa 188,5,7168\nMaav 52,148,212\nE 243,155\nMaav 20,148,208\nC 205,242,243\nMaav 16,144,208\nE 244,156\n" \
"Maav 24,152,208\nDS 0,60,s77,3072\nMaav 28,156,208\nE 245,157\nMaav 60,156,212\nC 206,244,245\nMaav 56,152,212\nE 246,158\n" \
"Maav 88,152,216\nMaav 92,156,216\nE 247,159\nMaav 124,156,220\nC 207,246,247\ns_waitcnt lgkmcnt(0)\nMaav 120,152,220\ns_branch 99f\n" \
".p2align 12\nzp10:\ns_setpc_b64 s[98:99]\n99:\ns_nop 0\ns_nop 0\ns_nop 0\nzO:\n" \
"Qvav 128,160,32,12\nE 240,160\nMvav 128,164,36\nE 241,161\nMvav 128,168,40\nC 208,240,241\nRa 128,6,49152\nMvav 128,172,44\n" \
"E 242,162\ns_add_u32 m0, s75, 0xc000\nQvav 136,160,48,12\nDS 8,60,s77,0\nMvav 136,164,52\nE 243,163\nMvav 136,168,56\nC 209,242,243\n" \
"Ra 132,6,50176\nMvav 136,172,60\nE 244,164\nQvav 144,160,64,12\nDS 8,60,s77,1024\nMvav 144,164,68\nE 245,165\nMvav 144,168,72\n" \
"C 210,244,245\nRa 136,6,51200\nMvav 144,172,76\nE 246,166\nQvav 152,160,80,12\nDS 8,60,s77,2048\nMvav 152,164,84\nE 247,167\n" \
"Mvav 152,168,88\nC 211,246,247\nRa 140,6,52224\nMvav 152,172,92\nE 240,168\nQvav 132,176,32,12\nDS 8,60,s77,3072\nMvav 132,180,36\n" \
"E 241,169\nMvav 132,184,40\nC 212,240,241\nRa 144,6,53248\nMvav 132,188,44\nE 242,170\nQvav 140,176,48,12\nbuffer_load_dwordx2 v[10:11], v1, s[60:63], 0 offen offset:64 sc1\n" \
"Mvav 140,180,52\nE 243,171\nMvav 140,184,56\nC 213,242,243\nRa 148,6,54272\nMvav 140,188,60\nE 244,172\nQvav 148,176,64,12\n" \
"buffer_load_dword v30, v4, s[56:59], 0 offen sc1\nMvav 148,180,68\nE 245,173\nMvav 148,184,72\nC 214,244,245\nRa 152,6,55296\nMvav 148,188,76\nE 246,174\n" \
"Qvav 156,176,80,12\nRa 156,6,56320\ns_mov_b32 s76, s73\nMvav 156,180,84\nE 247,175\nMvav 156,184,88\nC 215,246,247\ns_mov_b32 s73, s74\n" \
"Mvav 156,188,92\nE 240,176\nv_smfmac_f32_16x16x64_bf16 v[96:99], v[104:107], v[192:199], v7\ns_mov_b32 s74, s75\nv_smfmac_f32_16x16x64_bf16 v[100:103], v[104:107], v[200:207], v7\ns_waitcnt lgkmcnt(0)\nMaav 96,128,204\nRa 160,5,8192\n" \
"s_mov_b32 s75, s76\nMaav 100,132,204\nE 241,177\nMaav 68,132,200\nC 216,240,241\nRa 164,5,9216\nMaav 64,128,200\nE 242,178\n" \
"Maav 32,128,196\ns_add_u32 s77, s77, 0x4000\nv_add_u32 v4, s45, v4\nMaav 36,132,196\nE 243,179\nMaav 4,132,192\nC 217,242,243\nRa 168,5,10240\n" \
"Maav 0,128,192\nE 244,180\nMaav 8,136,192\nRa 172,5,11264\nMaav 12,140,192\nE 245,181\nMaav 44,140,196\nC 218,244,245\n" \
"Maav 40,136,196\nE 246,182\nMaav 72,136,200\nRa 176,5,12288\nMaav 76,140,200\nE 247,183\nMaav 108,140,204\nC 219,246,247\n" \
"Maav 104,136,204\nE 240,184\nMaav 112,144,204\nRa 180,5,13312\nMaav 116,148,204\nE 241,185\nMaav 84,148,200\nC 220,240,241\n" \
"Maav 80,144,200\nE 242,186\nMaav 48,144,196\nRa 184,5,14336\nMaav 52,148,196\nE 243,187\nMaav 20,148,192\nC 221,242,243\n" \
"Ra 188,5,15360\nMaav 16,144,192\nE 244,188\nMaav 24,152,192\nMaav 28,156,192\nE 245,189\nMaav 60,156,196\nC 222,244,245\n" \
"Maav 56,152,196\nE 246,190\nMaav 88,152,200\nMaav 92,156,200\nE 247,191\nMaav 124,156,204\nC 223,246,247\ns_waitcnt lgkmcnt(0)\n" \
"Maav 120,152,204\nzE:\nQvav 160,160,32,12\nE 240,128\nMvav 160,164,36\nE 241,129\nMvav 160,168,40\nC 192,240,241\n" \
"Ra 128,6,57344\nMvav 160,172,44\nE 242,130\nQvav 168,160,48,12\nRa 132,6,58368\nMvav 168,164,52\nE 243,131\nMvav 168,168,56\n" \
"C 193,242,243\nMvav 168,172,60\nE 244,132\nQvav 176,160,64,12\nRa 136,6,59392\nMvav 176,164,68\nE 245,133\nMvav 176,168,72\n" \
"C 194,244,245\nMvav 176,172,76\nE 246,134\nQvav 184,160,80,12\nRa 140,6,60416\nMvav 184,164,84\nE 247,135\nMvav 184,168,88\n" \
"C 195,246,247\nMvav 184,172,92\nE 240,136\nQvav 164,176,32,12\nRa 144,6,61440\nMvav 164,180,36\nE 241,137\nMvav 164,184,40\n" \
"C 196,240,241\nMvav 164,188,44\nE 242,138\nQvav 172,176,48,12\nRa 148,6,62464\nMvav 172,180,52\nE 243,139\nMvav 172,184,56\n" \
"C 197,242,243\nRa 152,6,63488\nMvav 172,188,60\nE 244,140\nQvav 180,176,64,12\nMvav 180,180,68\nE 245,141\nMvav 180,184,72\n" \
"C 198,244,245\nRa 156,6,64512\nMvav 180,188,76\nE 246,142\nQvav 188,176,80,12\nv_mov_b32 v6, v5\nv_add_u32 v5, s74, v9\nMvav 188,180,84\n" \
"E 247,143\nMvav 188,184,88\nC 199,246,247\nMvav 188,188,92\nE 240,144\nv_smfmac_f32_16x16x64_bf16 v[96:99], v[104:107], v[208:215], v7\nv_smfmac_f32_16x16x64_bf16 v[100:103], v[104:107], v[216:223], v7\ns_waitcnt vmcnt(9) lgkmcnt(0)\n" \
"s_barrier\nMaav 96,128,220\nRa 160,5,0\nRa 164,5,1024\nMaav 100,132,220\nE 241,145\nMaav 68,132,216\nC 200,240,241\n" \
"Maav 64,128,216\nE 242,146\ns_mov_b32 m0, s73\nMaav 32,128,212\nDS 0,60,s77,0\nMaav 36,132,212\nE 243,147\nMaav 4,132,208\n" \
"C 201,242,243\nRa 168,5,2048\nMaav 0,128,208\nE 244,148\nMaav 8,136,208\nDS 0,60,s77,1024\nMaav 12,140,208\nE 245,149\n" \
"Maav 44,140,212\nC 202,244,245\nRa 172,5,3072\nMaav 40,136,212\nE 246,150\nMaav 72,136,216\nRa 176,5,4096\nMaav 76,140,216\n" \
"E 247,151\nMaav 108,140,220\nC 203,246,247\nRa 180,5,5120\nMaav 104,136,220\nE 240,152\nMaav 112,144,220\nDS 0,60,s77,2048\n" \
"Maav 116,148,220\nE 241,153\nMaav 84,148,216\nC 204,240,241\nMaav 80,144,216\nE 242,154\nMaav 48,144,212\nDS 0,60,s77,3072\n" \
"Maav 52,148,212\nE 243,155\nMaav 20,148,208\nC 205,242,243\nRa 184,5,6144\nMaav 16,144,208\nE 244,156\nMaav 24,152,208\n" \
"Ra 188,5,7168\nMaav 28,156,208\nE 245,157\nMaav 60,156,212\nC 206,244,245\ns_sub_u32 s96, s96, 1\nMaav 56,152,212\nE 246,158\n" \
"Maav 88,152,216\ns_cmp_lg_u32 s96, 0\nMaav 92,156,216\nE 247,159\nMaav 124,156,220\nC 207,246,247\ns_waitcnt lgkmcnt(0)\nMaav 120,152,220\n" \
"s_cbranch_scc1 zO\ns_cmp_lg_u32 s40, 0\ns_cbranch_scc1 zL\nQvav 128,160,32,112\nE 240,160\nMvav 128,164,36\nE 241,161\nMvav 128,168,40\n" \
"C 208,240,241\nRa 128,6,49152\nMvav 128,172,44\nE 242,162\ns_add_u32 m0, s75, 0xc000\nQvav 136,160,48,12\nDS 8,60,s77,0\nMvav 136,164,52\n" \
"E 243,163\nMvav 136,168,56\nC 209,242,243\nRa 132,6,50176\nMvav 136,172,60\nE 244,164\nQvav 144,160,64,12\nDS 8,60,s77,1024\n" \
"Mvav 144,164,68\nE 245,165\nMvav 144,168,72\nC 210,244,245\nMvav 144,172,76\nE 246,166\nQvav 152,160,80,12\nRa 136,6,51200\n" \
"Ra 140,6,52224\nMvav 152,164,84\nE 247,167\nMvav 152,168,88\nC 211,246,247\nMvav 152,172,92\nE 240,168\nQvav 132,176,32,116\n" \
"DS 8,60,s77,2048\nMvav 132,180,36\nE 241,169\nMvav 132,184,40\nC 212,240,241\nRa 144,6,53248\nMvav 132,188,44\nE 242,170\n" \
"Qvav 140,176,48,12\nRa 148,6,54272\nMvav 140,180,52\nE 243,171\nMvav 140,184,56\nC 213,242,243\nRa 152,6,55296\nMvav 140,188,60\n" \
"E 244,172\nQvav 148,176,64,12\nDS 8,60,s77,3072\nMvav 148,180,68\nE 245,173\nMvav 148,184,72\nC 214,244,245\ns_mov_b32 s76, s73\n" \
"Mvav 148,188,76\nE 246,174\nQvav 156,176,80,12\nRa 156,6,56320\ns_mov_b32 s73, s74\nMvav 156,180,84\nE 247,175\nMvav 156,184,88\n" \
"C 215,246,247\ns_mov_b32 s74, s75\nMvav 156,188,92\nE 240,176\nv_smfmac_f32_16x16x64_bf16 v[96:99], v[104:107], v[192:199], v7\ns_mov_b32 s75, s76\nv_smfmac_f32_16x16x64_bf16 v[100:103], v[104:107], v[200:207], v7\ns_waitcnt lgkmcnt(0)\n" \
"Maav 96,128,204\nRa 160,5,8192\ns_add_u32 s77, s77, 0x4000\nMaav 100,132,204\nE 241,177\nMaav 68,132,200\nC 216,240,241\nRa 164,5,9216\n" \
"Maav 64,128,200\nE 242,178\nMaav 32,128,196\nMaav 36,132,196\nE 243,179\nMaav 4,132,192\nC 217,242,243\nRa 168,5,10240\n" \
"Maav 0,128,192\nE 244,180\nMaav 8,136,192\nRa 172,5,11264\nMaav 12,140,192\nE 245,181\nMaav 44,140,196\nC 218,244,245\n" \
"Maav 40,136,196\nE 246,182\nMaav 72,136,200\nRa 176,5,12288\nMaav 76,140,200\nE 247,183\nMaav 108,140,204\nC 219,246,247\n" \
"Maav 104,136,204\nE 240,184\nMaav 112,144,204\nRa 180,5,13312\nMaav 116,148,204\nE 241,185\nMaav 84,148,200\nC 220,240,241\n" \
"Maav 80,144,200\nE 242,186\nMaav 48,144,196\nRa 184,5,14336\nMaav 52,148,196\nE 243,187\nMaav 20,148,192\nC 221,242,243\n" \
"Ra 188,5,15360\nMaav 16,144,192\nE 244,188\nMaav 24,152,192\nMaav 28,156,192\nE 245,189\nMaav 60,156,196\nC 222,244,245\n" \
"Maav 56,152,196\nE 246,190\nMaav 88,152,200\nMaav 92,156,200\nE 247,191\nMaav 124,156,204\nC 223,246,247\ns_waitcnt lgkmcnt(0)\n" \
"Maav 120,152,204\nQvav 160,160,32,120\nE 240,128\nMvav 160,164,36\nE 241,129\nMvav 160,168,40\nC 192,240,241\nRa 128,6,57344\n" \
"Mvav 160,172,44\nE 242,130\nQvav 168,160,48,12\nRa 132,6,58368\nMvav 168,164,52\nE 243,131\nMvav 168,168,56\nC 193,242,243\n" \
"Mvav 168,172,60\nE 244,132\nQvav 176,160,64,12\nRa 136,6,59392\nMvav 176,164,68\nE 245,133\nMvav 176,168,72\nC 194,244,245\n" \
"Mvav 176,172,76\nE 246,134\nQvav 184,160,80,12\nRa 140,6,60416\nMvav 184,164,84\nE 247,135\nMvav 184,168,88\nC 195,246,247\n" \
"Mvav 184,172,92\nE 240,136\nQvav 164,176,32,124\nRa 144,6,61440\nMvav 164,180,36\nE 241,137\nMvav 164,184,40\nC 196,240,241\n" \
"Mvav 164,188,44\nE 242,138\nQvav 172,176,48,12\nRa 148,6,62464\nMvav 172,180,52\nE 243,139\nMvav 172,184,56\nC 197,242,243\n" \
"Ra 152,6,63488\nMvav 172,188,60\nE 244,140\nQvav 180,176,64,12\nMvav 180,180,68\nE 245,141\nMvav 180,184,72\nC 198,244,245\n" \
"Ra 156,6,64512\nMvav 180,188,76\nE 246,142\nQvav 188,176,80,12\nv_mov_b32 v6, v5\nv_add_u32 v5, s74, v9\nMvav 188,180,84\nE 247,143\n" \
"Mvav 188,184,88\nC 199,246,247\nMvav 188,188,92\nE 240,144\nv_smfmac_f32_16x16x64_bf16 v[96:99], v[104:107], v[208:215], v7\nv_smfmac_f32_16x16x64_bf16 v[100:103], v[104:107], v[216:223], v7\ns_waitcnt vmcnt(8) lgkmcnt(0)\ns_barrier\n" \
"Maav 96,128,220\nRa 160,5,0\nRa 164,5,1024\nMaav 100,132,220\nE 241,145\nMaav 68,132,216\nC 200,240,241\nMaav 64,128,216\n" \
"E 242,146\ns_mov_b32 m0, s73\nMaav 32,128,212\nDS 0,60,s77,0\nMaav 36,132,212\nE 243,147\nMaav 4,132,208\nC 201,242,243\n" \
"Ra 168,5,2048\nMaav 0,128,208\nE 244,148\nMaav 8,136,208\nDS 0,60,s77,1024\nMaav 12,140,208\nE 245,149\nMaav 44,140,212\n" \
"C 202,244,245\nRa 172,5,3072\nMaav 40,136,212\nE 246,150\nMaav 72,136,216\nRa 176,5,4096\nMaav 76,140,216\nE 247,151\n" \
"Maav 108,140,220\nC 203,246,247\nRa 180,5,5120\nMaav 104,136,220\nE 240,152\nMaav 112,144,220\nDS 0,60,s77,2048\nMaav 116,148,220\n" \
"E 241,153\nMaav 84,148,216\nC 204,240,241\nMaav 80,144,216\nE 242,154\nMaav 48,144,212\nRa 184,5,6144\nRa 188,5,7168\n" \
"Maav 52,148,212\nE 243,155\nMaav 20,148,208\nC 205,242,243\nMaav 16,144,208\nE 244,156\nMaav 24,152,208\nDS 0,60,s77,3072\n" \
"Maav 28,156,208\nE 245,157\nMaav 60,156,212\nC 206,244,245\nMaav 56,152,212\nE 246,158\nMaav 88,152,216\nMaav 92,156,216\n" \
"E 247,159\nMaav 124,156,220\nC 207,246,247\ns_waitcnt lgkmcnt(0)\nMaav 120,152,220\nQvav 136,160,48,112\nE 240,160\nRa 128,6,49152\n" \
"Mvav 136,164,52\nE 241,161\nMvav 136,168,56\nC 208,240,241\nRa 132,6,50176\nMvav 136,172,60\nE 242,162\ns_add_u32 m0, s75, 0xc000\n" \
"Qvav 144,160,64,12\nDS 8,60,s77,0\nMvav 144,164,68\nE 243,163\nRa 136,6,51200\nMvav 144,168,72\nC 209,242,243\nE 244,164\n" \
"s_branch 99f\n.p2align 12\nzp11:\ns_setpc_b64 s[98:99]\n99:\nMvav 144,172,76\nDS 8,60,s77,1024\nQvav 152,160,80,12\n" \
"E 245,165\nMvav 152,164,84\nC 210,244,245\nRa 140,6,52224\nMvav 152,168,88\nE 246,166\nRa 144,6,53248\nMvav 152,172,92\n" \
"DS 8,60,s77,2048\nQvav 140,176,48,116\nE 247,167\nMvav 140,180,52\nC 211,246,247\nE 240,168\nMvav 140,184,56\nDS 8,60,s77,3072\n" \
"Mvav 140,188,60\nE 241,169\nRa 148,6,54272\nQvav 148,176,64,12\nC 212,240,241\nRa 152,6,55296\nMvav 148,180,68\nE 242,170\n" \
"Mvav 148,184,72\nE 243,171\nMvav 148,188,76\nC 213,242,243\nRa 156,6,56320\nQvav 156,176,80,12\nE 244,172\nMvav 156,180,84\n" \
"s_mov_b32 s76, s73\ns_mov_b32 s73, s74\nMvav 156,184,88\nE 245,173\nMvav 156,188,92\nC 214,244,245\ns_mov_b32 s74, s75\nv_smfmac_f32_16x16x64_bf16 v[96:99], v[104:107], v[192:199], v7\n" \
"s_mov_b32 s75, s76\nv_smfmac_f32_16x16x64_bf16 v[100:103], v[104:107], v[200:207], v7\ns_waitcnt lgkmcnt(0)\nMaav 96,128,204\nE 246,174\nMaav 100,132,204\nE 247,175\nMaav 68,132,200\n" \
"C 215,246,247\nRa 160,5,8192\nMaav 64,128,200\nE 240,176\nMaav 32,128,196\nRa 164,5,9216\ns_add_u32 s77, s77, 0x4000\nMaav 36,132,196\n" \
"E 241,177\nMaav 4,132,192\nC 216,240,241\nE 242,178\nMaav 0,128,192\nRa 168,5,10240\nMaav 8,136,192\nE 243,179\n" \
"Maav 12,140,192\nC 217,242,243\nRa 172,5,11264\nMaav 44,140,196\nE 244,180\nMaav 40,136,196\nRa 176,5,12288\nMaav 72,136,200\n" \
"E 245,181\nMaav 76,140,200\nC 218,244,245\nE 246,182\nMaav 108,140,204\nRa 180,5,13312\nMaav 104,136,204\nE 247,183\n" \
"Maav 112,144,204\nC 219,246,247\nMaav 116,148,204\nE 240,184\nMaav 84,148,200\nE 241,185\nMaav 80,144,200\nC 220,240,241\n" \
"Ra 184,5,14336\nMaav 48,144,196\nE 242,186\nMaav 52,148,196\nRa 188,5,15360\nMaav 20,148,192\nE 243,187\nMaav 16,144,192\n" \
"C 221,242,243\nMaav 24,152,192\nE 244,188\nMaav 28,156,192\nE 245,189\nMaav 60,156,196\nC 222,244,245\nMaav 56,152,196\n" \
"E 246,190\nMaav 88,152,200\nMaav 92,156,200\nE 247,191\nMaav 124,156,204\nC 223,246,247\ns_waitcnt lgkmcnt(0)\nMaav 120,152,204\n" \
"zD4:\nQvav 168,160,48,120\nE 240,136\ns_add_u32 m0, s32, 98304\nMvav 168,164,52\nD 18,68,0,0\nMvav 168,168,56\nE 241,137\n" \
"Mvav 168,172,60\nC 196,240,241\nRa 128,6,57344\nQvav 176,160,64,12\nE 242,138\nMvav 176,164,68\nRa 132,6,58368\nRa 136,6,59392\n" \
"Mvav 176,168,72\nE 243,139\nMvav 176,172,76\nC 197,242,243\nRa 140,6,60416\nQvav 184,160,80,12\nD 18,68,s89,1024\nMvav 184,164,84\n" \
"E 244,140\nMvav 184,168,88\nRa 144,6,61440\nMvav 184,172,92\nE 245,141\nQvav 172,176,48,124\nC 198,244,245\nRa 148,6,62464\n" \
"Mvav 172,180,52\nE 246,142\nMvav 172,184,56\nD 19,68,s90,2048\nMvav 172,188,60\nRa 152,6,63488\nRa 156,6,64512\nQvav 180,176,64,12\n" \
"E 247,143\nMvav 180,180,68\nC 199,246,247\nv_mov_b32 v6, v5\nMvav 180,184,72\nE 240,144\nMvav 180,188,76\nD 19,68,s91,3072\n" \
"Qvav 188,176,80,12\nE 241,145\nMvav 188,180,84\nC 200,240,241\nv_add_u32 v18, 131072, v18\nMvav 188,184,88\nE 242,146\nMvav 188,188,92\n" \
"v_add_u32 v19, 131072, v19\nv_add_u32 v5, s74, v9\nv_smfmac_f32_16x16x64_bf16 v[96:99], v[104:107], v[208:215], v7\nv_smfmac_f32_16x16x64_bf16 v[100:103], v[104:107], v[216:223], v7\ns_waitcnt vmcnt(12) lgkmcnt(0)\ns_barrier\nMaav 96,128,220\nRa 160,5,0\n" \
"Maav 100,132,220\nE 243,147\nMaav 68,132,216\nC 201,242,243\nRa 164,5,1024\nMaav 64,128,216\nE 244,148\ns_mov_b32 m0, s73\n" \
"Maav 32,128,212\nDS 0,60,s77,0\ns_getpc_b64 s[100:101]\n7:\ns_add_u32 s100, s100, s2\ns_addc_u32 s101, s101, 0\nMaav 36,132,212\nE 245,149\n" \
"s_add_u32 s100, s100, zq0-7b\ns_addc_u32 s101, s101, 0\nMaav 4,132,208\nC 202,244,245\ns_swappc_b64 s[98:99], s[100:101]\nMaav 0,128,208\nRa 168,5,2048\nMaav 8,136,208\n" \
"E 246,150\nMaav 12,140,208\nDS 0,60,s77,1024\nMaav 44,140,212\nE 247,151\nMaav 40,136,212\nC 203,246,247\nRa 172,5,3072\n" \
"Maav 72,136,216\nE 240,152\nMaav 76,140,216\nRa 176,5,4096\nMaav 108,140,220\nE 241,153\nMaav 104,136,220\nC 204,240,241\n" \
"Maav 112,144,220\nDS 0,60,s77,2048\nMaav 116,148,220\nE 242,154\nMaav 84,148,216\nRa 180,5,5120\nMaav 80,144,216\nE 243,155\n" \
"Maav 48,144,212\nC 205,242,243\nRa 184,5,6144\nMaav 52,148,212\nE 244,156\nMaav 20,148,208\nDS 0,60,s77,3072\nMaav 16,144,208\n" \
"Ra 188,5,7168\nMaav 24,152,208\nE 245,157\nMaav 28,156,208\nC 206,244,245\nMaav 60,156,212\nE 246,158\nMaav 56,152,212\n" \
"s_mov_b32 s96, s41\ns_bitcmp1_b32 s33, 3\nMaav 88,152,216\nE 247,159\nMaav 92,156,216\nC 207,246,247\nMaav 124,156,220\ns_waitcnt lgkmcnt(0)\n" \
"Maav 120,152,220\ns_cbranch_scc1 zDa\nQvav 144,160,64,12\nE 240,168\nRa 128,6,49152\nMvav 144,164,68\nE 241,169\nMvav 144,168,72\n" \
"C 212,240,241\nRa 132,6,50176\nRa 136,6,51200\nMvav 144,172,76\nE 242,170\ns_add_u32 m0, s75, 0xc000\nQvav 152,160,80,12\nDS 8,60,s77,0\n" \
"Mvav 152,164,84\nE 243,171\nRa 140,6,52224\nMvav 152,168,88\nC 213,242,243\nE 244,172\nMvav 152,172,92\nDS 8,60,s77,1024\n" \
"Ra 144,6,53248\nQvav 148,176,64,12\nE 245,173\nMvav 148,180,68\nC 214,244,245\nRa 148,6,54272\nMvav 148,184,72\nE 246,174\n" \
"Ra 152,6,55296\nMvav 148,188,76\nE 247,175\nQvav 156,176,80,12\nC 215,246,247\nRa 156,6,56320\nMvav 156,180,84\nE 240,176\n" \
"Mvav 156,184,88\nMvav 156,188,92\nE 241,177\nMvvv 96,108,196\nC 216,240,241\nE 242,178\nv_smfmac_f32_16x16x64_bf16 v[100:103], v[104:107], v[200:207], v7\ns_waitcnt lgkmcnt(0)\n" \
"Maav 96,128,204\nDS 8,60,s77,2048\nRa 160,5,8192\nMaav 100,132,204\nE 243,179\nMaav 68,132,200\nC 217,242,243\nRa 164,5,9216\n" \
"Maav 64,128,200\nE 244,180\nMaav 32,128,196\nE 245,181\nMaav 36,132,196\nC 218,244,245\nRa 168,5,10240\nMaav 40,136,196\n" \
"E 246,182\nRa 172,5,11264\nMaav 44,140,196\nDS 8,60,s77,3072\nMaav 76,140,200\nE 247,183\ns_add_u32 m0, s32, 114688\nMaav 72,136,200\n" \
"C 219,246,247\nE 240,184\nD 18,68,0,0\nMaav 104,136,204\nRa 176,5,12288\nRa 180,5,13312\nMaav 108,140,204\nE 241,185\n" \
"D 18,68,s89,1024\nMaav 112,144,204\nC 220,240,241\nRa 184,5,14336\nRa 188,5,15360\nMaav 116,148,204\nE 242,186\nD 19,68,s90,2048\n" \
"Maav 84,148,200\nE 243,187\nMaav 80,144,200\nC 221,242,243\nD 19,68,s91,3072\nMaav 48,144,196\nE 244,188\nMaav 52,148,196\n" \
"s_ff1_i32_b64 s78, s[8:9]\ns_bitset0_b64 s[8:9], s78\nMaav 56,152,196\nE 245,189\nMaav 60,156,196\nC 222,244,245\nE 246,190\nMaav 92,156,200\n" \
"s_xor_b32 s82, s78, s10\ns_and_b32 s80, s82, 31\nMaav 88,152,200\nE 247,191\ns_lshl_b32 s80, s80, 18\ns_cmp_lt_u32 s78, 32\ns_cselect_b64 s[68:69], s[14:15], s[12:13]\ns_cselect_b32 s70, 0x800000, s92\n" \
"v_add_u32 v18, s80, v16\nv_xor_b32 v19, 128, v18\nMaav 120,152,204\nC 223,246,247\ns_waitcnt lgkmcnt(0)\nMaav 124,156,204\ns_mov_b32 s76, s73\ns_mov_b32 s73, s74\n" \
"s_mov_b32 s74, s75\ns_mov_b32 s75, s76\ns_add_u32 s77, s77, 0x4000\nQvav 176,160,64,12\nE 240,144\ns_add_u32 m0, s32, 131072\nRa 128,6,57344\nMvav 176,164,68\n" \
"D 18,68,0,0\nMvav 176,168,72\nE 241,145\nMvav 176,172,76\nC 200,240,241\nRa 132,6,58368\nRa 136,6,59392\nQvav 184,160,80,12\n" \
"D 18,68,s89,1024\nMvav 184,164,84\nE 242,146\nMvav 184,168,88\nRa 140,6,60416\nRa 144,6,61440\nRa 148,6,62464\nMvav 184,172,92\n" \
"E 243,147\nQvav 180,176,64,12\nC 201,242,243\nRa 152,6,63488\nMvav 180,180,68\nD 19,68,s90,2048\nRa 156,6,64512\nMvav 180,184,72\n" \
"E 244,148\nMvav 180,188,76\nD 19,68,s91,3072\nQvav 188,176,80,12\nE 245,149\nMvav 188,180,84\nC 202,244,245\nv_add_u32 v18, 131072, v18\n" \
"Mvav 188,184,88\nv_add_u32 v19, 131072, v19\nv_mov_b32 v6, v5\nMvav 188,188,92\nE 246,150\nMvvv 96,108,212\nv_add_u32 v5, s74, v9\nv_smfmac_f32_16x16x64_bf16 v[100:103], v[104:107], v[216:223], v7\n" \
"s_waitcnt vmcnt(20) lgkmcnt(0)\ns_barrier\nMaav 96,128,220\nE 247,151\nMaav 100,132,220\nC 203,246,247\ns_mov_b32 m0, s73\nMaav 68,132,216\n" \
"DS 0,60,s77,0\nMaav 64,128,216\nE 240,152\nMaav 32,128,212\nRa 160,5,0\nRa 164,5,1024\nMaav 36,132,212\nE 241,153\n" \
"Maav 40,136,212\nC 204,240,241\nRa 168,5,2048\nMaav 44,140,212\nDS 0,60,s77,1024\nv_add_f32 v196, v96, v97\nMaav 76,140,216\nE 242,154\n" \
"v_add_f32 v197, v98, v99\nMaav 72,136,216\nRa 172,5,3072\nRa 176,5,4096\nv_subrev_u32 v198, 0xf800000, v196\nMaav 104,136,220\nE 243,155\nv_subrev_u32 v199, 0xf800000, v197\n" \
"Maav 108,140,220\nC 205,242,243\nRa 180,5,5120\nv_max_u32 v198, v198, v199\nMaav 112,144,220\nDS 0,60,s77,2048\nv_cmp_lt_u32 vcc, 0x3c000000, v198\nMaav 116,148,220\n" \
"E 244,156\nMaav 84,148,216\nRa 184,5,6144\nRa 188,5,7168\nMaav 80,144,216\nE 245,157\nMaav 48,144,212\nC 206,244,245\n" \
"Maav 52,148,212\nDS 0,60,s77,3072\nMaav 56,152,212\nE 246,158\nMaav 60,156,212\nMaav 92,156,216\nE 247,159\nMaav 88,152,216\n" \
"C 207,246,247\nMaav 120,152,220\ns_waitcnt lgkmcnt(0)\nMaav 124,156,220\ns_cbranch_vccnz zHs\nQvav 144,160,64,12\nE 240,176\nRa 128,6,49152\n" \
"Mvav 144,164,68\nE 241,177\nMvav 144,168,72\nC 216,240,241\nRa 132,6,50176\nv_rcp_f32 v196, v196\nv_rcp_f32 v197, v197\nMvav 144,172,76\n" \
"E 242,178\ns_add_u32 m0, s75, 0xc000\ns_add_u32 s95, s83, 0\nAR 128,0\nQvav 152,160,80,12\nDS 8,60,s77,0\nRa 136,6,51200\nAR 129,1\n" \
"Mvav 152,164,84\nE 243,179\nAR 130,2\nAR 131,3\nMvav 152,168,88\nC 217,242,243\nRa 140,6,52224\nAR 132,4\n" \
"AR 133,5\nMvav 152,172,92\nE 244,180\nAR 134,6\nQvav 148,176,64,12\nDS 8,60,s77,1024\nRa 144,6,53248\nAR 135,7\n" \
"v_mul_f32 v128, v128, v196\nMvav 148,180,68\nE 245,181\nv_mul_f32 v129, v129, v196\nMvav 148,184,72\nC 218,244,245\nRa 148,6,54272\nv_mul_f32 v130, v130, v196\n" \
"v_mul_f32 v131, v131, v196\nMvav 148,188,76\nE 246,182\nv_mul_f32 v132, v132, v196\nv_mul_f32 v133, v133, v196\nQvav 156,176,80,12\nRa 152,6,55296\nRa 156,6,56320\n" \
"v_mul_f32 v134, v134, v196\nMvav 156,180,84\nE 247,183\nv_mul_f32 v135, v135, v196\nC 32,128,129\nMvav 156,184,88\nC 219,246,247\nC 33,130,131\n" \
"Mvav 156,188,92\nE 240,184\nC 34,132,133\nC 35,134,135\nv_smfmac_f32_16x16x64_bf16 v[100:103], v[104:107], v[200:207], v7\ns_waitcnt lgkmcnt(0)\nbuffer_store_dwordx4 v[32:35], v0, s[60:63], s95 offen offset:0 sc1\nAR 136,8\n" \
"Maav 96,128,204\nDS 8,60,s77,2048\nRa 160,5,8192\nAR 137,9\nMaav 100,132,204\nE 241,185\nAR 138,10\nAR 139,11\n" \
"Maav 68,132,200\nC 220,240,241\nRa 164,5,9216\nAR 140,12\nMaav 64,128,200\nE 242,186\nAR 141,13\nAR 142,14\n" \
"Maav 72,136,200\nDS 8,60,s77,3072\nRa 168,5,10240\nAR 143,15\nv_mul_f32 v136, v136, v196\nMaav 76,140,200\nE 243,187\nv_mul_f32 v137, v137, v196\n" \
"Maav 108,140,204\nC 221,242,243\nRa 172,5,11264\nv_mul_f32 v138, v138, v196\nv_mul_f32 v139, v139, v196\nMaav 104,136,204\nE 244,188\nv_mul_f32 v140, v140, v196\n" \
"Maav 112,144,204\nRa 176,5,12288\nRa 180,5,13312\nRa 184,5,14336\nv_mul_f32 v141, v141, v196\nv_mul_f32 v142, v142, v196\nMaav 116,148,204\nE 245,189\n" \
"v_mul_f32 v143, v143, v196\nC 36,136,137\nMaav 84,148,200\nC 222,244,245\nRa 188,5,15360\nC 37,138,139\nMaav 80,144,200\nE 246,190\n" \
"C 38,140,141\nC 39,142,143\nMaav 88,152,200\ns_branch 99f\n.p2align 12\nzp12:\ns_setpc_b64 s[98:99]\n99:\n" \
"s_mov_b32 s76, s73\ns_mov_b32 s73, s74\nbuffer_store_dwordx4 v[36:39], v0, s[60:63], s95 offen offset:1024 sc1\nMaav 92,156,200\nE 247,191\nMaav 124,156,204\nC 223,246,247\ns_waitcnt lgkmcnt(0)\n" \
"Maav 120,152,204\ns_mov_b32 s74, s75\ns_mov_b32 s75, s76\ns_add_u32 s77, s77, 0x4000\nQvav 176,160,64,12\nE 240,144\nMvav 176,164,68\nE 241,145\n" \
"Mvav 176,168,72\nC 200,240,241\nRa 128,6,57344\ns_add_u32 s95, s83, 32768\nAR 128,16\nMvav 176,172,76\nE 242,146\nAR 129,17\n" \
"AR 130,18\nQvav 184,160,80,12\nRa 132,6,58368\nRa 136,6,59392\nAR 131,19\nMvav 184,164,84\nE 243,147\nAR 132,20\n" \
"AR 133,21\nMvav 184,168,88\nC 201,242,243\nRa 140,6,60416\nAR 134,22\nMvav 184,172,92\nE 244,148\nAR 135,23\n" \
"v_mul_f32 v128, v128, v196\nQvav 180,176,64,12\nRa 144,6,61440\nRa 148,6,62464\nv_mul_f32 v129, v129, v196\nMvav 180,180,68\nE 245,149\nv_mul_f32 v130, v130, v196\n" \
"v_mul_f32 v131, v131, v196\nMvav 180,184,72\nC 202,244,245\nRa 152,6,63488\nv_mul_f32 v132, v132, v196\nMvav 180,188,76\nE 246,150\nv_mul_f32 v133, v133, v196\n" \
"v_mul_f32 v134, v134, v196\nQvav 188,176,80,12\nRa 156,6,64512\nv_mul_f32 v135, v135, v196\nMvav 188,180,84\nE 247,151\nC 40,128,129\nC 41,130,131\n" \
"Mvav 188,184,88\nC 203,246,247\nv_mov_b32 v6, v5\nC 42,132,133\nMvav 188,188,92\nE 240,152\nC 43,134,135\nbuffer_store_dwordx4 v[40:43], v0, s[60:63], s95 offen offset:0 sc1\n" \
"v_smfmac_f32_16x16x64_bf16 v[100:103], v[104:107], v[216:223], v7\ns_waitcnt vmcnt(19) lgkmcnt(0)\ns_barrier\nv_add_u32 v5, s74, v9\nAR 136,24\nAR 137,25\nMaav 96,128,220\ns_mov_b32 m0, s73\n" \
"Ra 160,5,0\nRa 164,5,1024\nAR 138,26\nMaav 100,132,220\nE 241,153\nDS 0,60,s77,0\nAR 139,27\nAR 140,28\n" \
"Maav 68,132,216\nC 204,240,241\nRa 168,5,2048\nAR 141,29\nMaav 64,128,216\nE 242,154\nAR 142,30\nAR 143,31\n" \
"Maav 72,136,216\nDS 0,60,s77,1024\nRa 172,5,3072\nv_mul_f32 v136, v136, v196\nMaav 76,140,216\nE 243,155\nv_mul_f32 v137, v137, v196\nv_mul_f32 v138, v138, v196\n" \
"Maav 108,140,220\nC 205,242,243\nRa 176,5,4096\nv_mul_f32 v139, v139, v196\nMaav 104,136,220\nE 244,156\nDS 0,60,s77,2048\nv_mul_f32 v140, v140, v196\n" \
"v_mul_f32 v141, v141, v196\nMaav 112,144,220\nRa 180,5,5120\nRa 184,5,6144\nRa 188,5,7168\nv_mul_f32 v142, v142, v196\nMaav 116,148,220\nE 245,157\n" \
"v_mul_f32 v143, v143, v196\nC 44,136,137\nMaav 84,148,216\nC 206,244,245\nDS 0,60,s77,3072\nC 45,138,139\nMaav 80,144,216\nE 246,158\n" \
"C 46,140,141\nC 47,142,143\nMaav 88,152,216\nbuffer_store_dwordx4 v[44:47], v0, s[60:63], s95 offen offset:1024 sc1\nMaav 92,156,216\nE 247,159\nMaav 124,156,220\nC 207,246,247\n" \
"s_waitcnt lgkmcnt(0)\nMaav 120,152,220\nQvav 144,160,64,12\nE 240,176\nRa 128,6,49152\nMvav 144,164,68\nE 241,177\nMvav 144,168,72\n" \
"C 216,240,241\nRa 132,6,50176\ns_add_u32 s95, s83, 8192\nAR 128,32\nMvav 144,172,76\nE 242,178\ns_add_u32 m0, s75, 0xc000\nAR 129,33\n" \
"AR 130,34\nQvav 152,160,80,12\nDS 8,60,s77,0\nRa 136,6,51200\nAR 131,35\nMvav 152,164,84\nE 243,179\nAR 132,36\n" \
"AR 133,37\nMvav 152,168,88\nC 217,242,243\nRa 140,6,52224\nAR 134,38\nMvav 152,172,92\nE 244,180\nAR 135,39\n" \
"v_mul_f32 v128, v128, v197\nQvav 148,176,64,12\nDS 8,60,s77,1024\nRa 144,6,53248\nv_mul_f32 v129, v129, v197\nMvav 148,180,68\nE 245,181\nv_mul_f32 v130, v130, v197\n" \
"v_mul_f32 v131, v131, v197\nMvav 148,184,72\nC 218,244,245\nRa 148,6,54272\nv_mul_f32 v132, v132, v197\nMvav 148,188,76\nE 246,182\nv_mul_f32 v133, v133, v197\n" \
"v_mul_f32 v134, v134, v197\nQvav 156,176,80,12\nRa 152,6,55296\nRa 156,6,56320\nv_mul_f32 v135, v135, v197\nMvav 156,180,84\nE 247,183\nC 48,128,129\n" \
"C 49,130,131\nMvav 156,184,88\nC 219,246,247\nC 50,132,133\nMvav 156,188,92\nE 240,184\nC 51,134,135\nbuffer_store_dwordx4 v[48:51], v0, s[60:63], s95 offen offset:0 sc1\n" \
"v_smfmac_f32_16x16x64_bf16 v[100:103], v[104:107], v[200:207], v7\ns_waitcnt lgkmcnt(0)\nAR 136,40\nAR 137,41\nMaav 96,128,204\nDS 8,60,s77,2048\nRa 160,5,8192\nAR 138,42\n" \
"Maav 100,132,204\nE 241,185\nAR 139,43\nAR 140,44\nMaav 68,132,200\nC 220,240,241\nRa 164,5,9216\nAR 141,45\n" \
"Maav 64,128,200\nE 242,186\nAR 142,46\nAR 143,47\nMaav 72,136,200\nDS 8,60,s77,3072\nRa 168,5,10240\nv_mul_f32 v136, v136, v197\n" \
"Maav 76,140,200\nE 243,187\nv_mul_f32 v137, v137, v197\nv_mul_f32 v138, v138, v197\nMaav 108,140,204\nC 221,242,243\nRa 172,5,11264\nv_mul_f32 v139, v139, v197\n" \
"Maav 104,136,204\nE 244,188\nv_mul_f32 v140, v140, v197\nv_mul_f32 v141, v141, v197\nMaav 112,144,204\nRa 176,5,12288\nRa 180,5,13312\nRa 184,5,14336\n" \
"v_mul_f32 v142, v142, v197\nMaav 116,148,204\nE 245,189\nv_mul_f32 v143, v143, v197\nC 52,136,137\nMaav 84,148,200\nC 222,244,245\nRa 188,5,15360\n" \
"C 53,138,139\nMaav 80,144,200\nE 246,190\nC 54,140,141\nC 55,142,143\nMaav 88,152,200\ns_mov_b32 s76, s73\ns_mov_b32 s73, s74\n" \
"buffer_store_dwordx4 v[52:55], v0, s[60:63], s95 offen offset:1024 sc1\nMaav 92,156,200\nE 247,191\nMaav 124,156,204\nC 223,246,247\ns_waitcnt lgkmcnt(0)\nMaav 120,152,204\ns_mov_b32 s74, s75\n" \
"s_mov_b32 s75, s76\ns_add_u32 s77, s77, 0x4000\nQvav 176,160,64,12\nE 240,144\nMvav 176,164,68\nE 241,145\nMvav 176,168,72\nC 200,240,241\n" \
"Ra 128,6,57344\ns_add_u32 s95, s83, 40960\nAR 128,48\nMvav 176,172,76\nE 242,146\nAR 129,49\nAR 130,50\nQvav 184,160,80,12\n" \
"Ra 132,6,58368\nRa 136,6,59392\nAR 131,51\nMvav 184,164,84\nE 243,147\nAR 132,52\nAR 133,53\nMvav 184,168,88\n" \
"C 201,242,243\nRa 140,6,60416\nAR 134,54\nMvav 184,172,92\nE 244,148\nAR 135,55\nv_mul_f32 v128, v128, v197\nQvav 180,176,64,12\n" \
"Ra 144,6,61440\nRa 148,6,62464\nv_mul_f32 v129, v129, v197\nMvav 180,180,68\nE 245,149\nv_mul_f32 v130, v130, v197\nv_mul_f32 v131, v131, v197\nMvav 180,184,72\n" \
"C 202,244,245\nRa 152,6,63488\nv_mul_f32 v132, v132, v197\nMvav 180,188,76\nE 246,150\nv_mul_f32 v133, v133, v197\nv_mul_f32 v134, v134, v197\nQvav 188,176,80,12\n" \
"Ra 156,6,64512\nv_mul_f32 v135, v135, v197\nMvav 188,180,84\nE 247,151\nC 56,128,129\nC 57,130,131\nMvav 188,184,88\nC 203,246,247\n" \
"v_mov_b32 v6, v5\nC 58,132,133\nMvav 188,188,92\nE 240,152\nC 59,134,135\nbuffer_store_dwordx4 v[56:59], v0, s[60:63], s95 offen offset:0 sc1\nv_smfmac_f32_16x16x64_bf16 v[100:103], v[104:107], v[216:223], v7\ns_waitcnt vmcnt(14) lgkmcnt(0)\n" \
"s_barrier\nv_add_u32 v5, s74, v9\nAR 136,56\nAR 137,57\nMaav 96,128,220\ns_mov_b32 m0, s73\nRa 160,5,0\nRa 164,5,1024\n" \
"AR 138,58\nMaav 100,132,220\nE 241,153\nDS 0,60,s77,0\nAR 139,59\nAR 140,60\nMaav 68,132,216\nC 204,240,241\n" \
"Ra 168,5,2048\nAR 141,61\nMaav 64,128,216\nE 242,154\nAR 142,62\nAR 143,63\nMaav 72,136,216\nDS 0,60,s77,1024\n" \
"Ra 172,5,3072\nv_mul_f32 v136, v136, v197\nMaav 76,140,216\nE 243,155\nv_mul_f32 v137, v137, v197\nv_mul_f32 v138, v138, v197\nMaav 108,140,220\nC 205,242,243\n" \
"Ra 176,5,4096\nv_mul_f32 v139, v139, v197\nMaav 104,136,220\nE 244,156\nDS 0,60,s77,2048\nv_mul_f32 v140, v140, v197\nv_mul_f32 v141, v141, v197\nMaav 112,144,220\n" \
"Ra 180,5,5120\nRa 184,5,6144\nRa 188,5,7168\nv_mul_f32 v142, v142, v197\nMaav 116,148,220\nE 245,157\nv_mul_f32 v143, v143, v197\nC 60,136,137\n" \
"Maav 84,148,216\nC 206,244,245\nDS 0,60,s77,3072\nC 61,138,139\nMaav 80,144,216\nE 246,158\nC 62,140,141\nC 63,142,143\n" \
"Maav 88,152,216\nbuffer_store_dwordx4 v[60:63], v0, s[60:63], s95 offen offset:1024 sc1\nMaav 92,156,216\nE 247,159\nMaav 124,156,220\nC 207,246,247\ns_waitcnt lgkmcnt(0)\nMaav 120,152,220\n" \
"s_branch 99f\n.p2align 12\nzp13:\ns_setpc_b64 s[98:99]\n99:\nzH:\nQvav 144,160,64,12\nE 240,176\n" \
"Ra 128,6,49152\nMvav 144,164,68\nE 241,177\nMvav 144,168,72\nC 216,240,241\nRa 132,6,50176\nMvav 144,172,76\nE 242,178\n" \
"s_add_u32 m0, s75, 0xc000\nQvav 152,160,80,12\nDS 8,60,s77,0\nRa 136,6,51200\nMvav 152,164,84\nE 243,179\nMvav 152,168,88\nC 217,242,243\n" \
"Ra 140,6,52224\nMvav 152,172,92\nE 244,180\nQvav 148,176,64,12\nDS 8,60,s77,1024\nRa 144,6,53248\nMvav 148,180,68\nE 245,181\n" \
"Mvav 148,184,72\nC 218,244,245\nRa 148,6,54272\nMvav 148,188,76\nE 246,182\nQvav 156,176,80,12\nRa 152,6,55296\nRa 156,6,56320\n" \
"Mvav 156,180,84\nE 247,183\nMvav 156,184,88\nC 219,246,247\nMvav 156,188,92\nE 240,184\nv_smfmac_f32_16x16x64_bf16 v[100:103], v[104:107], v[200:207], v7\ns_waitcnt lgkmcnt(0)\n" \
"Maav 96,128,204\nDS 8,60,s77,2048\nRa 160,5,8192\nMaav 100,132,204\nE 241,185\nMaav 68,132,200\nC 220,240,241\nRa 164,5,9216\n" \
"Maav 64,128,200\nE 242,186\nMaav 72,136,200\nDS 8,60,s77,3072\nRa 168,5,10240\nMaav 76,140,200\nE 243,187\nMaav 108,140,204\n" \
"C 221,242,243\nRa 172,5,11264\nMaav 104,136,204\nE 244,188\nMaav 112,144,204\nRa 176,5,12288\nRa 180,5,13312\nRa 184,5,14336\n" \
"Maav 116,148,204\nE 245,189\nMaav 84,148,200\nC 222,244,245\nRa 188,5,15360\nMaav 80,144,200\nE 246,190\nMaav 88,152,200\n" \
"s_mov_b32 s76, s73\ns_mov_b32 s73, s74\nMaav 92,156,200\nE 247,191\nMaav 124,156,204\nC 223,246,247\ns_waitcnt lgkmcnt(0)\nMaav 120,152,204\n" \
"s_mov_b32 s74, s75\ns_mov_b32 s75, s76\ns_add_u32 s77, s77, 0x4000\nQvav 176,160,64,12\nE 240,144\nMvav 176,164,68\nE 241,145\nMvav 176,168,72\n" \
"C 200,240,241\nRa 128,6,57344\nMvav 176,172,76\nE 242,146\nQvav 184,160,80,12\nRa 132,6,58368\nRa 136,6,59392\nMvav 184,164,84\n" \
"E 243,147\nMvav 184,168,88\nC 201,242,243\nRa 140,6,60416\nMvav 184,172,92\nE 244,148\nQvav 180,176,64,12\nRa 144,6,61440\n" \
"Ra 148,6,62464\nMvav 180,180,68\nE 245,149\nMvav 180,184,72\nC 202,244,245\nRa 152,6,63488\nMvav 180,188,76\nE 246,150\n" \
"Qvav 188,176,80,12\nRa 156,6,64512\nMvav 188,180,84\nE 247,151\nMvav 188,184,88\nC 203,246,247\nv_mov_b32 v6, v5\nMvav 188,188,92\n" \
"E 240,152\nv_smfmac_f32_16x16x64_bf16 v[100:103], v[104:107], v[216:223], v7\ns_waitcnt vmcnt(8) lgkmcnt(0)\ns_barrier\nv_add_u32 v5, s74, v9\nMaav 96,128,220\ns_mov_b32 m0, s73\nRa 160,5,0\n" \
"Ra 164,5,1024\nMaav 100,132,220\nE 241,153\nDS 0,60,s77,0\nMaav 68,132,216\nC 204,240,241\nRa 168,5,2048\nMaav 64,128,216\n" \
"E 242,154\nMaav 72,136,216\nDS 0,60,s77,1024\nRa 172,5,3072\nMaav 76,140,216\nE 243,155\nMaav 108,140,220\nC 205,242,243\n" \
"Ra 176,5,4096\nMaav 104,136,220\nE 244,156\nDS 0,60,s77,2048\nMaav 112,144,220\nRa 180,5,5120\nRa 184,5,6144\nRa 188,5,7168\n" \
"Maav 116,148,220\nE 245,157\nMaav 84,148,216\nC 206,244,245\ns_sub_u32 s96, s96, 1\nDS 0,60,s77,3072\nMaav 80,144,216\nE 246,158\n" \
"Maav 88,152,216\ns_cmp_lg_u32 s96, 0\nMaav 92,156,216\nE 247,159\nMaav 124,156,220\nC 207,246,247\ns_waitcnt lgkmcnt(0)\nMaav 120,152,220\n" \
"s_cbranch_scc1 zH\nQvav 144,160,64,112\nE 240,176\nRa 128,6,49152\nMvav 144,164,68\nE 241,177\nMvav 144,168,72\nC 216,240,241\n" \
"Ra 132,6,50176\nMvav 144,172,76\nE 242,178\ns_add_u32 m0, s75, 0xc000\nQvav 152,160,80,12\nDS 8,60,s77,0\nRa 136,6,51200\nMvav 152,164,84\n" \
"E 243,179\nMvav 152,168,88\nC 217,242,243\nRa 140,6,52224\nMvav 152,172,92\nE 244,180\nQvav 148,176,64,116\nDS 8,60,s77,1024\n" \
"Ra 144,6,53248\nMvav 148,180,68\nE 245,181\nMvav 148,184,72\nC 218,244,245\nRa 148,6,54272\nMvav 148,188,76\nE 246,182\n" \
"Qvav 156,176,80,12\nRa 152,6,55296\nRa 156,6,56320\nMvav 156,180,84\nE 247,183\nMvav 156,184,88\nC 219,246,247\nMvav 156,188,92\n" \
"E 240,184\nv_smfmac_f32_16x16x64_bf16 v[100:103], v[104:107], v[200:207], v7\ns_waitcnt lgkmcnt(0)\nMaav 96,128,204\nDS 8,60,s77,2048\nRa 160,5,8192\nMaav 100,132,204\nE 241,185\n" \
"Maav 68,132,200\nC 220,240,241\nRa 164,5,9216\nMaav 64,128,200\nE 242,186\nMaav 72,136,200\nDS 8,60,s77,3072\nRa 168,5,10240\n" \
"Maav 76,140,200\nE 243,187\nMaav 108,140,204\nC 221,242,243\nRa 172,5,11264\nMaav 104,136,204\nE 244,188\nMaav 112,144,204\n" \
"Ra 176,5,12288\nRa 180,5,13312\nRa 184,5,14336\nMaav 116,148,204\nE 245,189\nMaav 84,148,200\nC 222,244,245\nRa 188,5,15360\n" \
"Maav 80,144,200\nE 246,190\nMaav 88,152,200\ns_mov_b32 s76, s73\ns_mov_b32 s73, s74\nMaav 92,156,200\nE 247,191\nMaav 124,156,204\n" \
"C 223,246,247\ns_waitcnt lgkmcnt(0)\nMaav 120,152,204\ns_mov_b32 s74, s75\ns_mov_b32 s75, s76\ns_add_u32 s77, s77, 0x4000\nQvav 176,160,64,120\nE 240,144\n" \
"Mvav 176,164,68\nE 241,145\nMvav 176,168,72\nC 200,240,241\nRa 128,6,57344\nMvav 176,172,76\nE 242,146\nQvav 184,160,80,12\n" \
"Ra 132,6,58368\nRa 136,6,59392\nMvav 184,164,84\nE 243,147\nMvav 184,168,88\nC 201,242,243\nRa 140,6,60416\nMvav 184,172,92\n" \
"E 244,148\nQvav 180,176,64,124\nRa 144,6,61440\nRa 148,6,62464\nMvav 180,180,68\nE 245,149\nMvav 180,184,72\nC 202,244,245\n" \
"Ra 152,6,63488\nMvav 180,188,76\nE 246,150\nQvav 188,176,80,12\nRa 156,6,64512\nMvav 188,180,84\nE 247,151\nMvav 188,184,88\n" \
"C 203,246,247\nv_mov_b32 v6, v5\nMvav 188,188,92\nE 240,152\nv_smfmac_f32_16x16x64_bf16 v[100:103], v[104:107], v[216:223], v7\ns_waitcnt vmcnt(8) lgkmcnt(0)\ns_barrier\nv_add_u32 v5, s74, v9\n" \
"Maav 96,128,220\nRa 160,5,0\nRa 164,5,1024\nMaav 100,132,220\nE 241,153\nMaav 68,132,216\nC 204,240,241\nRa 168,5,2048\n" \
"Maav 64,128,216\nE 242,154\nMaav 72,136,216\nRa 172,5,3072\nRa 176,5,4096\nMaav 76,140,216\nE 243,155\nMaav 108,140,220\n" \
"C 205,242,243\nRa 180,5,5120\nMaav 104,136,220\nE 244,156\nMaav 112,144,220\nRa 184,5,6144\nRa 188,5,7168\nMaav 116,148,220\n" \
"E 245,157\nMaav 84,148,216\nC 206,244,245\nMaav 80,144,216\nE 246,158\nMaav 88,152,216\nMaav 92,156,216\nE 247,159\n" \
"Maav 124,156,220\nC 207,246,247\ns_waitcnt lgkmcnt(0)\nMaav 120,152,220\nzE3:\nQvav 152,160,80,112\nE 240,176\nRa 128,6,49152\n" \
"Ra 132,6,50176\nMvav 152,164,84\nE 241,177\ns_add_u32 m0, s32, 147456\nRa 136,6,51200\nMvav 152,168,88\nC 216,240,241\nE 242,178\n" \
"D 18,68,0,0\nRa 140,6,52224\nMvav 152,172,92\nRa 144,6,53248\nRa 148,6,54272\nRa 152,6,55296\nQvav 156,176,80,116\nE 243,179\n" \
"Ra 156,6,56320\nMvav 156,180,84\nC 217,242,243\nE 244,180\nMvav 156,184,88\nD 18,68,s89,1024\nMvav 156,188,92\nE 245,181\n" \
"v_smfmac_f32_16x16x64_bf16 v[100:103], v[104:107], v[200:207], v7\nC 218,244,245\ns_waitcnt lgkmcnt(0)\nMaav 96,128,204\nE 246,182\nRa 160,5,8192\nMaav 100,132,204\nD 19,68,s90,2048\n" \
"Maav 68,132,200\nE 247,183\nRa 164,5,9216\nMaav 64,128,200\nC 219,246,247\nE 240,184\nMaav 72,136,200\nE 241,185\n" \
"Ra 168,5,10240\nMaav 76,140,200\nC 220,240,241\nD 19,68,s91,3072\nMaav 108,140,204\nE 242,186\nRa 172,5,11264\nMaav 104,136,204\n" \
"E 243,187\nMaav 112,144,204\nC 221,242,243\nRa 176,5,12288\nRa 180,5,13312\nMaav 116,148,204\nE 244,188\nMaav 84,148,200\n" \
"E 245,189\nRa 184,5,14336\nMaav 80,144,200\nC 222,244,245\nRa 188,5,15360\nMaav 88,152,200\nE 246,190\nMaav 92,156,200\n" \
"E 247,191\nMaav 124,156,204\nC 223,246,247\ns_waitcnt lgkmcnt(0)\nMaav 120,152,204\nQvav 184,160,80,120\nRa 128,6,57344\nMvav 184,164,84\n" \
"Ra 132,6,58368\nMvav 184,168,88\nRa 136,6,59392\nMvav 184,172,92\nRa 140,6,60416\nQvav 188,176,80,124\nRa 144,6,61440\nMvav 188,180,84\n" \
"Ra 148,6,62464\nMvav 188,184,88\nRa 152,6,63488\nMvav 188,188,92\nRa 156,6,64512\nv_smfmac_f32_16x16x64_bf16 v[100:103], v[104:107], v[216:223], v7\ns_waitcnt vmcnt(4) lgkmcnt(0)\ns_barrier\n" \
"v_mov_b32 v6, v5\nMaav 96,128,220\nRa 160,6,49152\nMaav 100,132,220\nE 240,152\nE 241,153\nMaav 68,132,216\nRa 164,6,50176\n" \
"C 204,240,241\nMaav 64,128,216\nE 242,154\nMaav 72,136,216\nRa 168,6,51200\nMaav 76,140,216\nE 243,155\nMaav 108,140,220\n" \
"Ra 172,6,52224\nC 205,242,243\nMaav 104,136,220\nE 244,156\nMaav 112,144,220\nRa 176,6,53248\nMaav 116,148,220\nE 245,157\n" \
"Maav 84,148,216\nRa 180,6,54272\nC 206,244,245\nMaav 80,144,216\nE 246,158\nMaav 88,152,216\nRa 184,6,55296\nMaav 92,156,216\n" \
"E 247,159\nMaav 124,156,220\nRa 188,6,56320\nC 207,246,247\nMaav 120,152,220\ns_waitcnt lgkmcnt(0)\nE 240,184\nMvvv 100,108,204\n" \
"Ra 128,6,57344\nE 241,185\nMaav 96,160,204\nRa 132,6,58368\nC 192,240,241\nE 242,186\nMaav 100,164,204\nRa 136,6,59392\n" \
"E 243,187\nMaav 104,168,204\nRa 140,6,60416\nC 193,242,243\nE 244,188\nMaav 108,172,204\nRa 144,6,61440\nE 245,189\n" \
"Maav 112,176,204\nRa 148,6,62464\nC 194,244,245\nE 246,190\nMaav 116,180,204\nRa 152,6,63488\nE 247,191\nMaav 120,184,204\n" \
"Ra 156,6,64512\nC 195,246,247\nMaav 124,188,204\ns_nop 0\ns_waitcnt lgkmcnt(0)\nMvvv 100,108,192\nMaav 96,128,192\nMaav 100,132,192\n" \
"Maav 104,136,192\nMaav 108,140,192\nMaav 112,144,192\nMaav 116,148,192\nMaav 120,152,192\nv_add_f32 v104, v96, v97\nv_add_f32 v105, v98, v99\nv_add_f32 v106, v100, v101\n" \
"v_add_f32 v107, v102, v103\nMaav 124,156,192\nv_subrev_u32 v240, 0xf800000, v104\nv_subrev_u32 v241, 0xf800000, v105\nv_subrev_u32 v242, 0xf800000, v106\nv_subrev_u32 v243, 0xf800000, v107\nv_max3_u32 v244, v240, v241, v242\nv_max_u32 v244, v244, v243\n" \
"v_cmp_lt_u32 vcc, 0x3c000000, v244\ns_cbranch_vccnz zfb\nzend:\ns_nop 4\nv_rcp_f32 v110, v106\nv_rcp_f32 v111, v107\nv_xor_b32 v20, s21, v20\nv_xor_b32 v21, s21, v21\n" \
"v_xor_b32 v22, s21, v22\nv_xor_b32 v23, s21, v23\nv_xor_b32 v24, s21, v24\nv_xor_b32 v25, s21, v25\nv_xor_b32 v26, s21, v26\nv_xor_b32 v27, s21, v27\ns_add_u32 s95, s83, 16384\nAR 112,64\n" \
"AR 113,65\nAR 114,66\nAR 115,67\nAR 116,68\nAR 117,69\nAR 118,70\nAR 119,71\nv_pk_mul_f32 v[112:113], v[112:113], v[110:111] op_sel:[0,0] op_sel_hi:[1,0]\n" \
"v_pk_mul_f32 v[114:115], v[114:115], v[110:111] op_sel:[0,0] op_sel_hi:[1,0]\nv_pk_mul_f32 v[116:117], v[116:117], v[110:111] op_sel:[0,0] op_sel_hi:[1,0]\nv_pk_mul_f32 v[118:119], v[118:119], v[110:111] op_sel:[0,0] op_sel_hi:[1,0]\nC 64,112,113\nC 65,114,115\nC 66,116,117\nC 67,118,119\nTa 192,20,32768\n" \
"Ta 194,20,33792\nTa 224,20,40960\nTa 226,20,41984\nbuffer_store_dwordx4 v[64:67], v0, s[60:63], s95 offen offset:0 sc1\nAR 120,72\nAR 121,73\nAR 122,74\nAR 123,75\n" \
"AR 124,76\nAR 125,77\nAR 126,78\nAR 127,79\nv_pk_mul_f32 v[120:121], v[120:121], v[110:111] op_sel:[0,0] op_sel_hi:[1,0]\nv_pk_mul_f32 v[122:123], v[122:123], v[110:111] op_sel:[0,0] op_sel_hi:[1,0]\nv_pk_mul_f32 v[124:125], v[124:125], v[110:111] op_sel:[0,0] op_sel_hi:[1,0]\nv_pk_mul_f32 v[126:127], v[126:127], v[110:111] op_sel:[0,0] op_sel_hi:[1,0]\n" \
"C 68,120,121\nC 69,122,123\nC 70,124,125\nC 71,126,127\nTa 196,21,32768\nTa 198,21,33792\nTa 228,21,40960\nTa 230,21,41984\n" \
"buffer_store_dwordx4 v[68:71], v0, s[60:63], s95 offen offset:1024 sc1\ns_add_u32 s95, s83, 24576\nAR 112,96\nAR 113,97\nAR 114,98\nAR 115,99\nAR 116,100\nAR 117,101\n" \
"AR 118,102\nAR 119,103\nv_pk_mul_f32 v[112:113], v[112:113], v[110:111] op_sel:[0,1] op_sel_hi:[1,1]\nv_pk_mul_f32 v[114:115], v[114:115], v[110:111] op_sel:[0,1] op_sel_hi:[1,1]\nv_pk_mul_f32 v[116:117], v[116:117], v[110:111] op_sel:[0,1] op_sel_hi:[1,1]\nv_pk_mul_f32 v[118:119], v[118:119], v[110:111] op_sel:[0,1] op_sel_hi:[1,1]\nC 80,112,113\nC 81,114,115\n" \
"s_branch 99f\n.p2align 12\nzq0:\nzp14:\ns_setpc_b64 s[98:99]\n99:\nC 82,116,117\nC 83,118,119\n" \
"Ta 200,22,32768\nTa 202,22,33792\nTa 232,22,40960\nTa 234,22,41984\nbuffer_store_dwordx4 v[80:83], v0, s[60:63], s95 offen offset:0 sc1\nAR 120,104\nAR 121,105\nAR 122,106\n" \
"AR 123,107\nAR 124,108\nAR 125,109\nAR 126,110\nAR 127,111\nv_pk_mul_f32 v[120:121], v[120:121], v[110:111] op_sel:[0,1] op_sel_hi:[1,1]\nv_pk_mul_f32 v[122:123], v[122:123], v[110:111] op_sel:[0,1] op_sel_hi:[1,1]\nv_pk_mul_f32 v[124:125], v[124:125], v[110:111] op_sel:[0,1] op_sel_hi:[1,1]\n" \
"v_pk_mul_f32 v[126:127], v[126:127], v[110:111] op_sel:[0,1] op_sel_hi:[1,1]\nC 84,120,121\nC 85,122,123\nC 86,124,125\nC 87,126,127\nTa 204,23,32768\nTa 206,23,33792\nTa 236,23,40960\n" \
"Ta 238,23,41984\nbuffer_store_dwordx4 v[84:87], v0, s[60:63], s95 offen offset:1024 sc1\ns_add_u32 s95, s83, 49152\nAR 112,80\nAR 113,81\nAR 114,82\nAR 115,83\nAR 116,84\n" \
"AR 117,85\nAR 118,86\nAR 119,87\nv_pk_mul_f32 v[112:113], v[112:113], v[110:111] op_sel:[0,0] op_sel_hi:[1,0]\nv_pk_mul_f32 v[114:115], v[114:115], v[110:111] op_sel:[0,0] op_sel_hi:[1,0]\nv_pk_mul_f32 v[116:117], v[116:117], v[110:111] op_sel:[0,0] op_sel_hi:[1,0]\nv_pk_mul_f32 v[118:119], v[118:119], v[110:111] op_sel:[0,0] op_sel_hi:[1,0]\nC 72,112,113\n" \
"C 73,114,115\nC 74,116,117\nC 75,118,119\nTa 208,24,32768\nTa 210,24,33792\nTa 240,24,40960\nTa 242,24,41984\nbuffer_store_dwordx4 v[72:75], v0, s[60:63], s95 offen offset:0 sc1\n" \
"AR 120,88\nAR 121,89\nAR 122,90\nAR 123,91\nAR 124,92\nAR 125,93\nAR 126,94\nAR 127,95\n" \
"v_pk_mul_f32 v[120:121], v[120:121], v[110:111] op_sel:[0,0] op_sel_hi:[1,0]\nv_pk_mul_f32 v[122:123], v[122:123], v[110:111] op_sel:[0,0] op_sel_hi:[1,0]\nv_pk_mul_f32 v[124:125], v[124:125], v[110:111] op_sel:[0,0] op_sel_hi:[1,0]\nv_pk_mul_f32 v[126:127], v[126:127], v[110:111] op_sel:[0,0] op_sel_hi:[1,0]\nC 76,120,121\nC 77,122,123\nC 78,124,125\nC 79,126,127\n" \
"Ta 212,25,32768\nTa 214,25,33792\nTa 244,25,40960\nTa 246,25,41984\nbuffer_store_dwordx4 v[76:79], v0, s[60:63], s95 offen offset:1024 sc1\ns_add_u32 s95, s83, 57344\nAR 112,112\nAR 113,113\n" \
"AR 114,114\nAR 115,115\nAR 116,116\nAR 117,117\nAR 118,118\nAR 119,119\nv_pk_mul_f32 v[112:113], v[112:113], v[110:111] op_sel:[0,1] op_sel_hi:[1,1]\nv_pk_mul_f32 v[114:115], v[114:115], v[110:111] op_sel:[0,1] op_sel_hi:[1,1]\n" \
"v_pk_mul_f32 v[116:117], v[116:117], v[110:111] op_sel:[0,1] op_sel_hi:[1,1]\nv_pk_mul_f32 v[118:119], v[118:119], v[110:111] op_sel:[0,1] op_sel_hi:[1,1]\nC 88,112,113\nC 89,114,115\nC 90,116,117\nC 91,118,119\nTa 216,26,32768\nTa 218,26,33792\n" \
"Ta 248,26,40960\nTa 250,26,41984\nbuffer_store_dwordx4 v[88:91], v0, s[60:63], s95 offen offset:0 sc1\nAR 120,120\nAR 121,121\nAR 122,122\nAR 123,123\nAR 124,124\n" \
"AR 125,125\nAR 126,126\nAR 127,127\nv_pk_mul_f32 v[120:121], v[120:121], v[110:111] op_sel:[0,1] op_sel_hi:[1,1]\nv_pk_mul_f32 v[122:123], v[122:123], v[110:111] op_sel:[0,1] op_sel_hi:[1,1]\nv_pk_mul_f32 v[124:125], v[124:125], v[110:111] op_sel:[0,1] op_sel_hi:[1,1]\nv_pk_mul_f32 v[126:127], v[126:127], v[110:111] op_sel:[0,1] op_sel_hi:[1,1]\nC 92,120,121\n" \
"C 93,122,123\nC 94,124,125\nC 95,126,127\nTa 220,27,32768\nTa 222,27,33792\nTa 252,27,40960\nTa 254,27,41984\nbuffer_store_dwordx4 v[92:95], v0, s[60:63], s95 offen offset:1024 sc1\n" \
"s_waitcnt lgkmcnt(0)\nZvav 128,192,32\nv_lshrrev_b32 v28, 1, v0\ns_lshl_b32 s82, s10, 3\nMvav 128,224,36\nv_xor_b32 v29, s82, v28\nv_and_b32 v28, 0x78, v28\nZvav 132,228,36\n" \
"v_add_u32 v29, s4, v29\nv_add_u32 v28, s5, v28\nMvav 132,196,32\nv_cmp_gt_u32 vcc, 512, v0\ns_nop 0\nZvav 136,200,32\nv_cndmask_b32 v1, v28, v29, vcc\ns_mov_b32 s79, s78\n" \
"Mvav 136,232,36\ns_xor_b32 s82, s79, s10\ns_lshl_b32 s81, s82, 16\nZvav 140,236,36\ns_cmp_lt_u32 s79, 32\ns_cselect_b32 s82, s18, s19\nMvav 140,204,32\ns_add_u32 s81, s81, s82\n" \
"Zvav 144,208,32\nMvav 144,240,36\nZvav 148,244,36\nMvav 148,212,32\nZvav 152,216,32\nMvav 152,248,36\nZvav 156,252,36\nMvav 156,220,32\n" \
"Zvav 188,220,48\nMvav 188,252,52\nZvav 184,248,52\nMvav 184,216,48\nZvav 180,212,48\ns_ff1_i32_b64 s78, s[8:9]\ns_bitset0_b64 s[8:9], s78\nMvav 180,244,52\n" \
"s_xor_b32 s82, s78, s10\ns_and_b32 s80, s82, 31\nZvav 176,240,52\ns_lshl_b32 s80, s80, 18\ns_cmp_lt_u32 s78, 32\nMvav 176,208,48\ns_cselect_b64 s[68:69], s[14:15], s[12:13]\ns_cselect_b32 s70, 0x800000, s92\n" \
"Zvav 172,204,48\nv_add_u32 v18, s80, v16\nv_xor_b32 v19, 128, v18\nMvav 172,236,52\nZvav 168,232,52\ns_waitcnt vmcnt(24)\ns_barrier\nMvav 168,200,48\n" \
"Ta 128,20,49152\nTa 130,20,50176\nZvav 164,196,48\nTa 160,20,57344\nMvav 164,228,52\nTa 162,20,58368\nZvav 160,224,52\nTa 132,21,49152\n" \
"Mvav 160,192,48\nTa 134,21,50176\nZvav 192,192,64\nTa 164,21,57344\nTa 166,21,58368\nMvav 192,224,68\nTa 136,22,49152\nZvav 196,228,68\n" \
"Ta 138,22,50176\nMvav 196,196,64\nTa 168,22,57344\nZvav 200,200,64\nTa 170,22,58368\nMvav 200,232,68\nTa 140,23,49152\nTa 142,23,50176\n" \
"Zvav 204,236,68\nTa 172,23,57344\nMvav 204,204,64\nTa 174,23,58368\nZvav 208,208,64\ns_add_u32 m0, s84, 0\nMvav 208,240,68\nD 18,68,0,0\n" \
"Zvav 212,244,68\nTa 144,24,49152\nTa 146,24,50176\nMvav 212,212,64\nTa 176,24,57344\nZvav 216,216,64\nTa 178,24,58368\nMvav 216,248,68\n" \
"D 18,68,s89,1024\nZvav 220,252,68\nTa 148,25,49152\nMvav 220,220,64\nTa 150,25,50176\nZvav 252,220,80\nTa 180,25,57344\nTa 182,25,58368\n" \
"Mvav 252,252,84\nTa 152,26,49152\nZvav 248,248,84\nTa 154,26,50176\nMvav 248,216,80\nTa 184,26,57344\nZvav 244,212,80\nTa 186,26,58368\n" \
"Mvav 244,244,84\nTa 156,27,49152\nTa 158,27,50176\nZvav 240,240,84\ns_cmp_lg_u32 s32, 0\nMvav 240,208,80\nTa 188,27,57344\nZvav 236,204,80\n" \
"s_waitcnt vmcnt(2)\ns_barrier\ns_cbranch_scc1 zsn\nbuffer_store_dwordx2 v[2:3], off, s[60:63], s31 sc1\nzsn:\nMvav 236,236,84\nD 19,68,s90,2048\nZvav 232,232,84\n" \
"Ta 190,27,58368\nMvav 232,200,80\nZvav 228,196,80\nMvav 228,228,84\nD 19,68,s91,3072\nZvav 224,224,84\nv_add_u32 v18, 131072, v18\nMvav 224,192,80\n" \
"v_add_u32 v19, 131072, v19\ns_waitcnt lgkmcnt(0)\nMvav 128,128,40\nMvav 128,160,44\nMvav 132,164,44\nMvav 132,132,40\nMvav 136,136,40\nMvav 136,168,44\n" \
"Mvav 140,172,44\nMvav 140,140,40\nMvav 144,144,40\nv_xor_b32 v20, 0x8000, v20\nv_xor_b32 v21, 0x8000, v21\nMvav 144,176,44\nv_xor_b32 v22, 0x8000, v22\nv_xor_b32 v23, 0x8000, v23\n" \
"Mvav 148,180,44\nv_xor_b32 v24, 0x8000, v24\nv_xor_b32 v25, 0x8000, v25\nMvav 148,148,40\nv_xor_b32 v26, 0x8000, v26\nv_xor_b32 v27, 0x8000, v27\nMvav 152,152,40\nMvav 152,184,44\n" \
"Mvav 156,188,44\nMvav 156,156,40\nMvav 188,156,56\nbuffer_load_dwordx2 v[10:11], v1, s[60:63], 0 offen sc1\nMvav 188,188,60\nMvav 184,184,60\nMvav 184,152,56\nMvav 180,148,56\n" \
"Mvav 180,180,60\nMvav 176,176,60\nMvav 176,144,56\nMvav 172,140,56\nMvav 172,172,60\nMvav 168,168,60\ns_waitcnt vmcnt(25)\ns_barrier\n" \
"Mvav 168,136,56\nTa 192,20,32768\nTa 194,20,33792\nTa 224,20,40960\nMvav 164,132,56\nTa 226,20,41984\nTa 196,21,32768\nMvav 164,164,60\n" \
"Ta 198,21,33792\nTa 228,21,40960\nMvav 160,160,60\nTa 230,21,41984\nTa 200,22,32768\nMvav 160,128,56\nTa 202,22,33792\nTa 232,22,40960\n" \
"Mvav 192,128,72\nv_add_u32 v9, s81, v0\nTa 234,22,41984\nMvav 192,160,76\ns_cmp_eq_u32 s32, 0\nTa 204,23,32768\nMvav 196,164,76\nTa 206,23,33792\n" \
"Ta 236,23,40960\nMvav 196,132,72\nTa 238,23,41984\nTa 208,24,32768\nMvav 200,136,72\ns_cbranch_scc1 zsk\ns_waitcnt vmcnt(0)\nv_cmp_eq_u64 vcc, v[10:11], v[2:3]\n" \
"s_andn2_b32 s82, s7, vcc_hi\ns_cselect_b64 vcc, 0, vcc\ns_bitcmp1_b64 vcc, s79\ns_cbranch_scc0 zs104\nzr104:\nzsk:\ns_barrier\ns_bitcmp1_b32 s33, 4\n" \
"Mvav 200,168,76\ns_cbranch_scc1 zep\nzeq:\nTa 210,24,33792\nMvav 204,172,76\nbuffer_load_dwordx4 a[0:3], v9, s[60:63], 0 offen offset:0 sc1\nMvav 204,140,72\nbuffer_load_dwordx4 a[4:7], v9, s[60:63], 0 offen offset:1024 sc1\n" \
"Mvav 208,144,72\ns_add_u32 m0, s84, 16384\nTa 240,24,40960\nMvav 208,176,76\nD 18,68,0,0\nbuffer_load_dwordx2 v[10:11], v1, s[60:63], 0 offen sc1\nMvav 212,180,76\ns_xor_b32 s84, s84, 0x38000\n" \
"buffer_load_dwordx4 a[8:11], v9, s[60:63], s85 offen offset:0 sc1\nMvav 212,148,72\nTa 242,24,41984\nTa 212,25,32768\nMvav 216,152,72\nbuffer_load_dwordx4 a[12:15], v9, s[60:63], s85 offen offset:1024 sc1\nMvav 216,184,76\nD 18,68,s89,1024\n" \
"buffer_load_dwordx4 a[16:19], v9, s[60:63], s86 offen offset:0 sc1\nMvav 220,188,76\nTa 214,25,33792\nTa 244,25,40960\nMvav 220,156,72\nbuffer_load_dwordx4 a[20:23], v9, s[60:63], s86 offen offset:1024 sc1\nMvav 252,156,88\nbuffer_load_dwordx4 a[24:27], v9, s[60:63], s87 offen offset:0 sc1\n" \
"Mvav 252,188,92\nTa 246,25,41984\nTa 216,26,32768\nMvav 248,184,92\nbuffer_load_dwordx4 a[28:31], v9, s[60:63], s87 offen offset:1024 sc1\nMvav 248,152,88\nv_add_u32 v9, 0x8000, v9\nTa 218,26,33792\n" \
"Mvav 244,148,88\nTa 248,26,40960\nTa 250,26,41984\nMvav 244,180,92\nbuffer_load_dwordx4 a[32:35], v9, s[60:63], 0 offen offset:0 sc1\nMvav 240,176,92\nbuffer_load_dwordx4 a[36:39], v9, s[60:63], 0 offen offset:1024 sc1\nMvav 240,144,88\n" \
"Ta 220,27,32768\nTa 222,27,33792\nMvav 236,140,88\nbuffer_load_dwordx4 a[40:43], v9, s[60:63], s85 offen offset:0 sc1\ns_mov_b32 s96, s22\nMvav 236,172,92\nD 19,68,s90,2048\nbuffer_load_dwordx4 a[44:47], v9, s[60:63], s85 offen offset:1024 sc1\n" \
"s_cmp_eq_u32 s96, 0\nMvav 232,168,92\nTa 252,27,40960\nTa 254,27,41984\nMvav 232,136,88\nbuffer_load_dwordx4 a[48:51], v9, s[60:63], s86 offen offset:0 sc1\nMvav 228,132,88\nbuffer_load_dwordx4 a[52:55], v9, s[60:63], s86 offen offset:1024 sc1\n" \
"Mvav 228,164,92\nD 19,68,s91,3072\nMvav 224,160,92\nMvav 224,128,88\ns_waitcnt vmcnt(17) lgkmcnt(0)\ns_cbranch_scc1 zox\ns_branch 99f\n.p2align 12\n" \
"zp15:\ns_setpc_b64 s[98:99]\n99:\nzol:\nMvaa 128,192,0\ns_waitcnt vmcnt(10)\nv_cmp_eq_u64 vcc, v[10:11], v[2:3]\ns_mov_b32 s79, s78\n" \
"Mvaa 128,224,4\nbuffer_load_dwordx4 a[56:59], v9, s[60:63], s87 offen offset:0 sc1\ns_xor_b32 s82, s79, s10\nMvaa 132,228,4\ns_lshl_b32 s81, s82, 16\ns_cmp_lt_u32 s79, 32\ns_cselect_b32 s82, s18, s19\nMvaa 132,196,0\n" \
"s_add_u32 s81, s81, s82\ns_bitcmp1_b64 vcc, s79\nMvaa 136,200,0\nMvaa 136,232,4\nbuffer_load_dwordx4 a[60:63], v9, s[60:63], s87 offen offset:1024 sc1\nMvaa 140,236,4\nMvaa 140,204,0\nv_add_u32 v9, s81, v0\n" \
"Mvaa 144,208,0\nMvaa 144,240,4\nMvaa 148,244,4\ns_cbranch_scc0 zs107\nzr107:\nMvaa 148,212,0\ns_ff1_i32_b64 s78, s[8:9]\ns_bitset0_b64 s[8:9], s78\n" \
"Mvaa 152,216,0\ns_xor_b32 s82, s78, s10\ns_and_b32 s80, s82, 31\nMvaa 152,248,4\ns_lshl_b32 s80, s80, 18\ns_cmp_lt_u32 s78, 32\nMvaa 156,252,4\ns_cselect_b64 s[68:69], s[14:15], s[12:13]\n" \
"s_cselect_b32 s70, 0x800000, s92\nMvaa 156,220,0\nv_add_u32 v18, s80, v16\nv_xor_b32 v19, 128, v18\ns_waitcnt vmcnt(15)\nMvaa 188,220,8\nMvaa 188,252,12\nbuffer_load_dwordx4 a[0:3], v9, s[60:63], 0 offen offset:0 sc1\n" \
"Mvaa 184,248,12\nMvaa 184,216,8\nMvaa 180,212,8\nMvaa 180,244,12\nbuffer_load_dwordx4 a[4:7], v9, s[60:63], 0 offen offset:1024 sc1\nMvaa 176,240,12\nMvaa 176,208,8\nMvaa 172,204,8\n" \
"Mvaa 172,236,12\nMvaa 168,232,12\ns_waitcnt vmcnt(29)\ns_barrier\nMvaa 168,200,8\nTa 128,20,49152\nTa 130,20,50176\nMvaa 164,196,8\n" \
"Ta 160,20,57344\nMvaa 164,228,12\nTa 162,20,58368\nTa 132,21,49152\nMvaa 160,224,12\nTa 134,21,50176\nMvaa 160,192,8\ns_waitcnt vmcnt(14)\n" \
"Ta 164,21,57344\nMvaa 192,192,16\nTa 166,21,58368\nMvaa 192,224,20\nbuffer_load_dwordx4 a[8:11], v9, s[60:63], s85 offen offset:0 sc1\nMvaa 196,228,20\nTa 136,22,49152\nMvaa 196,196,16\n" \
"Ta 138,22,50176\nTa 168,22,57344\nMvaa 200,200,16\nTa 170,22,58368\nMvaa 200,232,20\nbuffer_load_dwordx4 a[12:15], v9, s[60:63], s85 offen offset:1024 sc1\nMvaa 204,236,20\nTa 140,23,49152\n" \
"Mvaa 204,204,16\nTa 142,23,50176\nTa 172,23,57344\nMvaa 208,208,16\ns_add_u32 m0, s84, 0\nMvaa 208,240,20\nD 18,68,0,0\nMvaa 212,244,20\n" \
"Ta 174,23,58368\nTa 144,24,49152\nMvaa 212,212,16\nTa 146,24,50176\nMvaa 216,216,16\nTa 176,24,57344\nTa 178,24,58368\nMvaa 216,248,20\n" \
"D 18,68,s89,1024\nMvaa 220,252,20\nTa 148,25,49152\nMvaa 220,220,16\ns_waitcnt vmcnt(16)\nTa 150,25,50176\nMvaa 252,220,24\nTa 180,25,57344\n" \
"Mvaa 252,252,28\nbuffer_load_dwordx4 a[16:19], v9, s[60:63], s86 offen offset:0 sc1\nMvaa 248,248,28\nTa 182,25,58368\nMvaa 248,216,24\nTa 152,26,49152\nTa 154,26,50176\nMvaa 244,212,24\n" \
"Ta 184,26,57344\nMvaa 244,244,28\nbuffer_load_dwordx4 a[20:23], v9, s[60:63], s86 offen offset:1024 sc1\nMvaa 240,240,28\nTa 186,26,58368\nMvaa 240,208,24\nTa 156,27,49152\nTa 158,27,50176\n" \
"Mvaa 236,204,24\nTa 188,27,57344\nMvaa 236,236,28\nD 19,68,s90,2048\nMvaa 232,232,28\nTa 190,27,58368\nMvaa 232,200,24\nMvaa 228,196,24\n" \
"Mvaa 228,228,28\nD 19,68,s91,3072\nMvaa 224,224,28\nv_add_u32 v18, 131072, v18\nMvaa 224,192,24\nv_add_u32 v19, 131072, v19\ns_waitcnt vmcnt(18) lgkmcnt(0)\nMvaa 128,128,32\n" \
"Mvaa 128,160,36\nbuffer_load_dwordx4 a[24:27], v9, s[60:63], s87 offen offset:0 sc1\nMvaa 132,164,36\nMvaa 132,132,32\nMvaa 136,136,32\nMvaa 136,168,36\nbuffer_load_dwordx4 a[28:31], v9, s[60:63], s87 offen offset:1024 sc1\nMvaa 140,172,36\n" \
"Mvaa 140,140,32\nv_add_u32 v9, 0x8000, v9\nMvaa 144,144,32\nv_xor_b32 v20, 0x8000, v20\nv_xor_b32 v21, 0x8000, v21\nMvaa 144,176,36\nv_xor_b32 v22, 0x8000, v22\nv_xor_b32 v23, 0x8000, v23\n" \
"Mvaa 148,180,36\nv_xor_b32 v24, 0x8000, v24\nv_xor_b32 v25, 0x8000, v25\nMvaa 148,148,32\nbuffer_load_dwordx2 v[10:11], v1, s[60:63], 0 offen sc1\nMvaa 152,152,32\nv_xor_b32 v26, 0x8000, v26\nv_xor_b32 v27, 0x8000, v27\n" \
"Mvaa 152,184,36\nMvaa 156,188,36\nMvaa 156,156,32\ns_waitcnt vmcnt(18)\nMvaa 188,156,40\nMvaa 188,188,44\nbuffer_load_dwordx4 a[32:35], v9, s[60:63], 0 offen offset:0 sc1\nMvaa 184,184,44\n" \
"Mvaa 184,152,40\nMvaa 180,148,40\nMvaa 180,180,44\nbuffer_load_dwordx4 a[36:39], v9, s[60:63], 0 offen offset:1024 sc1\nMvaa 176,176,44\nMvaa 176,144,40\nMvaa 172,140,40\nMvaa 172,172,44\n" \
"Mvaa 168,168,44\ns_waitcnt vmcnt(30)\ns_barrier\nMvaa 168,136,40\nTa 192,20,32768\nTa 194,20,33792\nMvaa 164,132,40\nTa 224,20,40960\n" \
"Mvaa 164,164,44\nTa 226,20,41984\nTa 196,21,32768\nMvaa 160,160,44\nTa 198,21,33792\nMvaa 160,128,40\ns_waitcnt vmcnt(18)\nTa 228,21,40960\n" \
"Mvaa 192,128,48\nTa 230,21,41984\nMvaa 192,160,52\nbuffer_load_dwordx4 a[40:43], v9, s[60:63], s85 offen offset:0 sc1\nMvaa 196,164,52\nTa 200,22,32768\nTa 202,22,33792\nMvaa 196,132,48\n" \
"Ta 232,22,40960\nMvaa 200,136,48\nTa 234,22,41984\nTa 204,23,32768\nMvaa 200,168,52\nbuffer_load_dwordx4 a[44:47], v9, s[60:63], s85 offen offset:1024 sc1\nMvaa 204,172,52\nTa 206,23,33792\n" \
"Mvaa 204,140,48\nTa 236,23,40960\nTa 238,23,41984\nMvaa 208,144,48\ns_add_u32 m0, s84, 16384\nMvaa 208,176,52\nD 18,68,0,0\nMvaa 212,180,52\n" \
"s_xor_b32 s84, s84, 0x38000\nTa 208,24,32768\nMvaa 212,148,48\nTa 210,24,33792\nTa 240,24,40960\nMvaa 216,152,48\nTa 242,24,41984\nMvaa 216,184,52\n" \
"D 18,68,s89,1024\nMvaa 220,188,52\nTa 212,25,32768\nTa 214,25,33792\nMvaa 220,156,48\ns_waitcnt vmcnt(19)\nMvaa 252,156,56\nTa 244,25,40960\n" \
"Ta 246,25,41984\nMvaa 252,188,60\nbuffer_load_dwordx4 a[48:51], v9, s[60:63], s86 offen offset:0 sc1\nMvaa 248,184,60\nTa 216,26,32768\nMvaa 248,152,56\nTa 218,26,33792\nTa 248,26,40960\n" \
"Mvaa 244,148,56\nTa 250,26,41984\nMvaa 244,180,60\nbuffer_load_dwordx4 a[52:55], v9, s[60:63], s86 offen offset:1024 sc1\nMvaa 240,176,60\nTa 220,27,32768\nTa 222,27,33792\nMvaa 240,144,56\n" \
"Ta 252,27,40960\nMvaa 236,140,56\ns_sub_u32 s96, s96, 1\nTa 254,27,41984\nMvaa 236,172,60\nD 19,68,s90,2048\nMvaa 232,168,60\ns_cmp_lg_u32 s96, 0\n" \
"Mvaa 232,136,56\nMvaa 228,132,56\nMvaa 228,164,60\nD 19,68,s91,3072\nMvaa 224,160,60\nMvaa 224,128,56\ns_waitcnt vmcnt(21) lgkmcnt(0)\ns_cbranch_scc1 zol\n" \
"zox:\ns_cmp_eq_u32 s23, 0\ns_cbranch_scc1 zot\ns_cmp_eq_u32 s23, 1\ns_cbranch_scc1 zof\ns_cmp_eq_u32 s23, 3\ns_cbranch_scc1 zo1\ns_branch zos\n" \
"s_branch 99f\n.p2align 12\nzqR:\nzp16:\ns_setpc_b64 s[98:99]\n99:\nzof:\nMvaa 128,192,0\n" \
"Mvaa 128,224,4\nbuffer_load_dwordx4 a[56:59], v9, s[60:63], s87 offen offset:0 sc1\nMvaa 132,228,4\nMvaa 132,196,0\nMvaa 136,200,0\ns_waitcnt vmcnt(26)\ns_barrier\nMvaa 136,232,4\n" \
"buffer_load_dwordx4 a[60:63], v9, s[60:63], s87 offen offset:1024 sc1\nMvaa 140,236,4\nTa 128,20,49152\nTa 130,20,50176\nMvaa 140,204,0\nTa 160,20,57344\nTa 162,20,58368\nMvaa 144,208,0\n" \
"Ta 132,21,49152\nMvaa 144,240,4\nTa 134,21,50176\nTa 164,21,57344\nMvaa 148,244,4\nTa 166,21,58368\nMvaa 148,212,0\nTa 136,22,49152\n" \
"Ta 138,22,50176\nMvaa 152,216,0\nTa 168,22,57344\nMvaa 152,248,4\nTa 170,22,58368\nTa 140,23,49152\nMvaa 156,252,4\nTa 142,23,50176\n" \
"Mvaa 156,220,0\nTa 172,23,57344\nTa 174,23,58368\ns_waitcnt vmcnt(15)\nMvaa 160,192,8\nTa 144,24,49152\nMvaa 160,224,12\nTa 146,24,50176\n" \
"Ta 176,24,57344\nMvaa 164,228,12\nTa 178,24,58368\nMvaa 164,196,8\nTa 148,25,49152\nTa 150,25,50176\nMvaa 168,200,8\nTa 180,25,57344\n" \
"Mvaa 168,232,12\nTa 182,25,58368\nTa 152,26,49152\nMvaa 172,236,12\nTa 154,26,50176\nMvaa 172,204,8\nTa 184,26,57344\nTa 186,26,58368\n" \
"Mvaa 176,208,8\nTa 156,27,49152\nMvaa 176,240,12\nTa 158,27,50176\nTa 188,27,57344\nMvaa 180,244,12\nTa 190,27,58368\nMvaa 180,212,8\n" \
"Mvaa 184,216,8\nMvaa 184,248,12\nMvaa 188,252,12\nMvaa 188,220,8\ns_waitcnt vmcnt(8) lgkmcnt(0)\nMvaa 128,128,32\nMvaa 128,160,36\nMvaa 132,164,36\n" \
"Mvaa 132,132,32\nMvaa 136,136,32\nMvaa 136,168,36\nMvaa 140,172,36\nMvaa 140,140,32\nMvaa 144,144,32\nMvaa 144,176,36\nMvaa 148,180,36\n" \
"Mvaa 148,148,32\nMvaa 152,152,32\nMvaa 152,184,36\nMvaa 156,188,36\nMvaa 156,156,32\ns_waitcnt vmcnt(5)\nMvaa 160,128,40\nMvaa 160,160,44\n" \
"Mvaa 164,164,44\nMvaa 164,132,40\nC 32,128,129\nC 33,130,131\nMvaa 168,136,40\nC 34,132,133\nC 35,134,135\nMvaa 168,168,44\n" \
"C 36,136,137\nC 37,138,139\nMvaa 172,172,44\nC 38,140,141\nC 39,142,143\nMvaa 172,140,40\nC 40,144,145\nC 41,146,147\n" \
"Mvaa 176,144,40\nC 42,148,149\nC 43,150,151\nMvaa 176,176,44\nC 44,152,153\nC 45,154,155\nMvaa 180,180,44\nC 46,156,157\n" \
"C 47,158,159\nMvaa 180,148,40\ns_add_u32 s95, s24, 0\nMvaa 184,152,40\nbuffer_store_dwordx4 v[32:35], v0, s[60:63], s95 offen offset:0 sc1\nMvaa 184,184,44\nMvaa 188,188,44\nbuffer_store_dwordx4 v[36:39], v0, s[60:63], s95 offen offset:1024 sc1\n" \
"Mvaa 188,156,40\ns_waitcnt vmcnt(5)\nMvaa 192,192,16\ns_add_u32 s95, s25, 0\nMvaa 192,224,20\nbuffer_store_dwordx4 v[40:43], v0, s[60:63], s95 offen offset:0 sc1\nMvaa 192,128,48\nMvaa 192,160,52\n" \
"buffer_store_dwordx4 v[44:47], v0, s[60:63], s95 offen offset:1024 sc1\nMvaa 196,228,20\nMvaa 196,196,16\nC 48,160,161\nC 49,162,163\nMvaa 196,164,52\nC 50,164,165\nC 51,166,167\n" \
"Mvaa 196,132,48\nC 52,168,169\nC 53,170,171\nMvaa 200,200,16\nC 54,172,173\nC 55,174,175\nMvaa 200,232,20\nC 56,176,177\n" \
"C 57,178,179\nMvaa 200,136,48\nC 58,180,181\nC 59,182,183\nMvaa 200,168,52\nC 60,184,185\nC 61,186,187\nMvaa 204,236,20\n" \
"C 62,188,189\nC 63,190,191\nMvaa 204,204,16\ns_add_u32 s95, s24, 2048\nMvaa 204,172,52\nbuffer_store_dwordx4 v[48:51], v0, s[60:63], s95 offen offset:0 sc1\nMvaa 204,140,48\nMvaa 208,208,16\n" \
"buffer_store_dwordx4 v[52:55], v0, s[60:63], s95 offen offset:1024 sc1\nMvaa 208,240,20\nMvaa 208,144,48\ns_add_u32 s95, s25, 2048\nMvaa 208,176,52\nbuffer_store_dwordx4 v[56:59], v0, s[60:63], s95 offen offset:0 sc1\nMvaa 212,244,20\nMvaa 212,212,16\n" \
"buffer_store_dwordx4 v[60:63], v0, s[60:63], s95 offen offset:1024 sc1\nMvaa 212,180,52\nMvaa 212,148,48\nMvaa 216,216,16\nMvaa 216,248,20\nMvaa 216,152,48\nMvaa 216,184,52\nMvaa 220,252,20\n" \
"Mvaa 220,220,16\nMvaa 220,188,52\nMvaa 220,156,48\ns_waitcnt vmcnt(8)\nMvaa 224,192,24\nMvaa 224,224,28\nMvaa 224,128,56\nMvaa 224,160,60\n" \
"C 64,192,193\nC 65,194,195\nMvaa 228,228,28\nC 66,196,197\nC 67,198,199\nMvaa 228,196,24\nC 68,200,201\nC 69,202,203\n" \
"Mvaa 228,164,60\nC 70,204,205\nC 71,206,207\nMvaa 228,132,56\nC 72,208,209\nC 73,210,211\nMvaa 232,200,24\nC 74,212,213\n" \
"C 75,214,215\nMvaa 232,232,28\nC 76,216,217\nC 77,218,219\nMvaa 232,136,56\nC 78,220,221\nC 79,222,223\nMvaa 232,168,60\n" \
"s_add_u32 s95, s24, 4096\nMvaa 236,236,28\nbuffer_store_dwordx4 v[64:67], v0, s[60:63], s95 offen offset:0 sc1\nMvaa 236,204,24\nMvaa 236,172,60\nbuffer_store_dwordx4 v[68:71], v0, s[60:63], s95 offen offset:1024 sc1\nMvaa 236,140,56\nMvaa 240,208,24\n" \
"s_add_u32 s95, s25, 4096\nMvaa 240,240,28\nbuffer_store_dwordx4 v[72:75], v0, s[60:63], s95 offen offset:0 sc1\nMvaa 240,144,56\nMvaa 240,176,60\nbuffer_store_dwordx4 v[76:79], v0, s[60:63], s95 offen offset:1024 sc1\nMvaa 244,244,28\nMvaa 244,212,24\n" \
"Mvaa 244,180,60\nMvaa 244,148,56\nMvaa 248,216,24\nMvaa 248,248,28\nMvaa 248,152,56\nMvaa 248,184,60\nMvaa 252,252,28\nMvaa 252,220,24\n" \
"Mvaa 252,188,60\nMvaa 252,156,56\ns_nop 4\nC 80,224,225\nC 81,226,227\nC 82,228,229\nC 83,230,231\nC 84,232,233\n" \
"C 85,234,235\nC 86,236,237\nC 87,238,239\nC 88,240,241\nC 89,242,243\nC 90,244,245\nC 91,246,247\nC 92,248,249\n" \
"C 93,250,251\nC 94,252,253\nC 95,254,255\ns_add_u32 s95, s24, 6144\nbuffer_store_dwordx4 v[80:83], v0, s[60:63], s95 offen offset:0 sc1\nbuffer_store_dwordx4 v[84:87], v0, s[60:63], s95 offen offset:1024 sc1\ns_add_u32 s95, s25, 6144\nbuffer_store_dwordx4 v[88:91], v0, s[60:63], s95 offen offset:0 sc1\n" \
"buffer_store_dwordx4 v[92:95], v0, s[60:63], s95 offen offset:1024 sc1\ns_waitcnt vmcnt(0)\nbuffer_store_dwordx2 v[2:3], off, s[60:63], s27 sc1\ns_branch zfin\ns_branch 99f\n.p2align 12\nzqS:\nzp17:\n" \
"s_setpc_b64 s[98:99]\n99:\nzos:\nC 32,128,129\nC 33,130,131\nC 34,132,133\nC 35,134,135\nC 36,136,137\n" \
"C 37,138,139\nC 38,140,141\nC 39,142,143\nC 40,144,145\nC 41,146,147\nC 42,148,149\nC 43,150,151\nC 44,152,153\n" \
"C 45,154,155\nC 46,156,157\nC 47,158,159\nZvaa 128,192,0\ns_waitcnt vmcnt(10)\nv_cmp_eq_u64 vcc, v[10:11], v[2:3]\ns_mov_b32 s79, s78\nMvaa 128,224,4\n" \
"buffer_load_dwordx4 a[56:59], v9, s[60:63], s87 offen offset:0 sc1\ns_xor_b32 s82, s79, s10\nZvaa 132,228,4\ns_lshl_b32 s81, s82, 16\ns_cmp_lt_u32 s79, 32\ns_cselect_b32 s82, s18, s19\nMvaa 132,196,0\ns_add_u32 s81, s81, s82\n" \
"s_bitcmp1_b64 vcc, s79\nC 48,160,161\nZvaa 136,200,0\nC 49,162,163\nC 50,164,165\nC 51,166,167\nMvaa 136,232,4\nbuffer_load_dwordx4 a[60:63], v9, s[60:63], s87 offen offset:1024 sc1\n" \
"C 52,168,169\nZvaa 140,236,4\nC 53,170,171\nC 54,172,173\nC 55,174,175\nMvaa 140,204,0\nv_add_u32 v9, s81, v0\nC 56,176,177\n" \
"C 57,178,179\nZvaa 144,208,0\nC 58,180,181\nC 59,182,183\nC 60,184,185\nMvaa 144,240,4\nC 61,186,187\nC 62,188,189\n" \
"C 63,190,191\nZvaa 148,244,4\ns_cbranch_scc0 zs108\nzr108:\ns_add_u32 s95, s24, 0\nbuffer_store_dwordx4 v[32:35], v0, s[60:63], s95 offen offset:0 sc1\nMvaa 148,212,0\ns_ff1_i32_b64 s78, s[8:9]\n" \
"s_bitset0_b64 s[8:9], s78\nZvaa 152,216,0\ns_xor_b32 s82, s78, s10\ns_and_b32 s80, s82, 31\nMvaa 152,248,4\ns_lshl_b32 s80, s80, 18\ns_cmp_lt_u32 s78, 32\nZvaa 156,252,4\n" \
"s_cselect_b64 s[68:69], s[14:15], s[12:13]\ns_cselect_b32 s70, 0x800000, s92\nMvaa 156,220,0\nv_add_u32 v18, s80, v16\nv_xor_b32 v19, 128, v18\ns_waitcnt vmcnt(16)\nZvaa 188,220,8\nC 64,192,193\n" \
"C 65,194,195\nC 66,196,197\nMvaa 188,252,12\nbuffer_load_dwordx4 a[0:3], v9, s[60:63], 0 offen offset:0 sc1\nC 67,198,199\nZvaa 184,248,12\nC 68,200,201\nC 69,202,203\n" \
"C 70,204,205\nMvaa 184,216,8\nC 71,206,207\nC 72,208,209\nC 73,210,211\nZvaa 180,212,8\nC 74,212,213\nC 75,214,215\n" \
"C 76,216,217\nMvaa 180,244,12\nbuffer_load_dwordx4 a[4:7], v9, s[60:63], 0 offen offset:1024 sc1\nC 77,218,219\nZvaa 176,240,12\nC 78,220,221\nC 79,222,223\nMvaa 176,208,8\n" \
"Zvaa 172,204,8\nbuffer_store_dwordx4 v[36:39], v0, s[60:63], s95 offen offset:1024 sc1\nMvaa 172,236,12\nZvaa 168,232,12\ns_waitcnt vmcnt(31)\ns_barrier\nMvaa 168,200,8\nTa 128,20,49152\n" \
"Ta 130,20,50176\nZvaa 164,196,8\nTa 160,20,57344\ns_add_u32 s95, s25, 0\nbuffer_store_dwordx4 v[40:43], v0, s[60:63], s95 offen offset:0 sc1\nMvaa 164,228,12\nTa 162,20,58368\nTa 132,21,49152\n" \
"Zvaa 160,224,12\nTa 134,21,50176\nMvaa 160,192,8\ns_waitcnt vmcnt(17)\nTa 164,21,57344\nZvaa 192,192,16\nTa 166,21,58368\nC 80,224,225\n" \
"C 81,226,227\nMvaa 192,224,20\nbuffer_load_dwordx4 a[8:11], v9, s[60:63], s85 offen offset:0 sc1\nC 82,228,229\nZvaa 196,228,20\nTa 136,22,49152\nC 83,230,231\nC 84,232,233\n" \
"Mvaa 196,196,16\nTa 138,22,50176\nTa 168,22,57344\nC 85,234,235\nZvaa 200,200,16\nTa 170,22,58368\nC 86,236,237\nC 87,238,239\n" \
"Mvaa 200,232,20\nbuffer_load_dwordx4 a[12:15], v9, s[60:63], s85 offen offset:1024 sc1\nC 88,240,241\nZvaa 204,236,20\nTa 140,23,49152\nC 89,242,243\nC 90,244,245\nMvaa 204,204,16\n" \
"Ta 142,23,50176\nTa 172,23,57344\nC 91,246,247\nZvaa 208,208,16\ns_add_u32 m0, s84, 0\nC 92,248,249\nC 93,250,251\nMvaa 208,240,20\n" \
"D 18,68,0,0\nC 94,252,253\nZvaa 212,244,20\nTa 174,23,58368\nTa 144,24,49152\nC 95,254,255\nMvaa 212,212,16\nTa 146,24,50176\n" \
"Zvaa 216,216,16\nTa 176,24,57344\nTa 178,24,58368\nMvaa 216,248,20\nD 18,68,s89,1024\nbuffer_store_dwordx4 v[44:47], v0, s[60:63], s95 offen offset:1024 sc1\nZvaa 220,252,20\nTa 148,25,49152\n" \
"Mvaa 220,220,16\ns_waitcnt vmcnt(20)\nTa 150,25,50176\nZvaa 252,220,24\nTa 180,25,57344\nMvaa 252,252,28\nbuffer_load_dwordx4 a[16:19], v9, s[60:63], s86 offen offset:0 sc1\nZvaa 248,248,28\n" \
"Ta 182,25,58368\nMvaa 248,216,24\nTa 152,26,49152\nTa 154,26,50176\nZvaa 244,212,24\nTa 184,26,57344\nMvaa 244,244,28\nbuffer_load_dwordx4 a[20:23], v9, s[60:63], s86 offen offset:1024 sc1\n" \
"s_add_u32 s95, s24, 2048\nbuffer_store_dwordx4 v[48:51], v0, s[60:63], s95 offen offset:0 sc1\nZvaa 240,240,28\nTa 186,26,58368\nMvaa 240,208,24\nTa 156,27,49152\nTa 158,27,50176\nZvaa 236,204,24\n" \
"Ta 188,27,57344\nMvaa 236,236,28\nD 19,68,s90,2048\nZvaa 232,232,28\nTa 190,27,58368\nMvaa 232,200,24\nZvaa 228,196,24\nMvaa 228,228,28\n" \
"D 19,68,s91,3072\nZvaa 224,224,28\nv_add_u32 v18, 131072, v18\nMvaa 224,192,24\nv_add_u32 v19, 131072, v19\ns_waitcnt vmcnt(23) lgkmcnt(0)\nMvaa 128,128,32\nbuffer_store_dwordx4 v[52:55], v0, s[60:63], s95 offen offset:1024 sc1\n" \
"Mvaa 128,160,36\nbuffer_load_dwordx4 a[24:27], v9, s[60:63], s87 offen offset:0 sc1\nMvaa 132,164,36\nMvaa 132,132,32\nMvaa 136,136,32\nMvaa 136,168,36\nbuffer_load_dwordx4 a[28:31], v9, s[60:63], s87 offen offset:1024 sc1\nMvaa 140,172,36\n" \
"Mvaa 140,140,32\nv_add_u32 v9, 0x8000, v9\nMvaa 144,144,32\nv_xor_b32 v20, 0x8000, v20\nv_xor_b32 v21, 0x8000, v21\nMvaa 144,176,36\nv_xor_b32 v22, 0x8000, v22\nv_xor_b32 v23, 0x8000, v23\n" \
"Mvaa 148,180,36\nv_xor_b32 v24, 0x8000, v24\nv_xor_b32 v25, 0x8000, v25\nMvaa 148,148,32\nbuffer_load_dwordx2 v[10:11], v1, s[60:63], 0 offen sc1\nMvaa 152,152,32\nv_xor_b32 v26, 0x8000, v26\nv_xor_b32 v27, 0x8000, v27\n" \
"Mvaa 152,184,36\nMvaa 156,188,36\ns_add_u32 s95, s25, 2048\nbuffer_store_dwordx4 v[56:59], v0, s[60:63], s95 offen offset:0 sc1\nMvaa 156,156,32\ns_waitcnt vmcnt(25)\nMvaa 188,156,40\nMvaa 188,188,44\n" \
"buffer_load_dwordx4 a[32:35], v9, s[60:63], 0 offen offset:0 sc1\nMvaa 184,184,44\nMvaa 184,152,40\nMvaa 180,148,40\nMvaa 180,180,44\nbuffer_load_dwordx4 a[36:39], v9, s[60:63], 0 offen offset:1024 sc1\nMvaa 176,176,44\nMvaa 176,144,40\n" \
"Mvaa 172,140,40\nbuffer_store_dwordx4 v[60:63], v0, s[60:63], s95 offen offset:1024 sc1\nMvaa 172,172,44\nMvaa 168,168,44\ns_waitcnt vmcnt(38)\ns_barrier\nMvaa 168,136,40\nTa 192,20,32768\n" \
"Ta 194,20,33792\nMvaa 164,132,40\nTa 224,20,40960\nMvaa 164,164,44\nTa 226,20,41984\nTa 196,21,32768\nMvaa 160,160,44\nTa 198,21,33792\n" \
"Mvaa 160,128,40\ns_waitcnt vmcnt(26)\nTa 228,21,40960\nMvaa 192,128,48\nTa 230,21,41984\nMvaa 192,160,52\nbuffer_load_dwordx4 a[40:43], v9, s[60:63], s85 offen offset:0 sc1\nMvaa 196,164,52\n" \
"Ta 200,22,32768\nTa 202,22,33792\nMvaa 196,132,48\nTa 232,22,40960\nMvaa 200,136,48\nTa 234,22,41984\nMvaa 200,168,52\nbuffer_load_dwordx4 a[44:47], v9, s[60:63], s85 offen offset:1024 sc1\n" \
"s_add_u32 s95, s24, 4096\nbuffer_store_dwordx4 v[64:67], v0, s[60:63], s95 offen offset:0 sc1\nMvaa 204,172,52\nTa 204,23,32768\nTa 206,23,33792\nMvaa 204,140,48\nTa 236,23,40960\nMvaa 208,144,48\n" \
"s_add_u32 m0, s84, 16384\nTa 238,23,41984\nMvaa 208,176,52\nD 18,68,0,0\nMvaa 212,180,52\ns_xor_b32 s84, s84, 0x38000\nMvaa 212,148,48\nTa 208,24,32768\n" \
"Ta 210,24,33792\nMvaa 216,152,48\nTa 240,24,40960\nMvaa 216,184,52\nD 18,68,s89,1024\nMvaa 220,188,52\nTa 242,24,41984\nTa 212,25,32768\n" \
"Mvaa 220,156,48\ns_waitcnt vmcnt(28)\nMvaa 252,156,56\nTa 214,25,33792\nTa 244,25,40960\nMvaa 252,188,60\nbuffer_load_dwordx4 a[48:51], v9, s[60:63], s86 offen offset:0 sc1\nbuffer_store_dwordx4 v[68:71], v0, s[60:63], s95 offen offset:1024 sc1\n" \
"Mvaa 248,184,60\nTa 246,25,41984\nMvaa 248,152,56\nTa 216,26,32768\nTa 218,26,33792\nMvaa 244,148,56\nTa 248,26,40960\nMvaa 244,180,60\n" \
"buffer_load_dwordx4 a[52:55], v9, s[60:63], s86 offen offset:1024 sc1\nMvaa 240,176,60\nTa 250,26,41984\nMvaa 240,144,56\nTa 220,27,32768\nTa 222,27,33792\nMvaa 236,140,56\nTa 252,27,40960\n" \
"Mvaa 236,172,60\nD 19,68,s90,2048\nMvaa 232,168,60\nTa 254,27,41984\nMvaa 232,136,56\ns_add_u32 s95, s25, 4096\nbuffer_store_dwordx4 v[72:75], v0, s[60:63], s95 offen offset:0 sc1\nMvaa 228,132,56\n" \
"Mvaa 228,164,60\nD 19,68,s91,3072\nMvaa 224,160,60\nMvaa 224,128,56\ns_waitcnt vmcnt(31) lgkmcnt(0)\nMvaa 128,192,0\ns_waitcnt vmcnt(15)\nv_cmp_eq_u64 vcc, v[10:11], v[2:3]\n" \
"s_mov_b32 s79, s78\nMvaa 128,224,4\nbuffer_load_dwordx4 a[56:59], v9, s[60:63], s87 offen offset:0 sc1\ns_xor_b32 s82, s79, s10\nMvaa 132,228,4\ns_lshl_b32 s81, s82, 16\ns_cmp_lt_u32 s79, 32\ns_cselect_b32 s82, s18, s19\n" \
"Mvaa 132,196,0\ns_add_u32 s81, s81, s82\ns_bitcmp1_b64 vcc, s79\nMvaa 136,200,0\nMvaa 136,232,4\nbuffer_load_dwordx4 a[60:63], v9, s[60:63], s87 offen offset:1024 sc1\nMvaa 140,236,4\nMvaa 140,204,0\n" \
"v_add_u32 v9, s81, v0\nMvaa 144,208,0\nbuffer_store_dwordx4 v[76:79], v0, s[60:63], s95 offen offset:1024 sc1\nMvaa 144,240,4\nMvaa 148,244,4\ns_cbranch_scc0 zs109\nzr109:\nMvaa 148,212,0\n" \
"s_ff1_i32_b64 s78, s[8:9]\ns_bitset0_b64 s[8:9], s78\nMvaa 152,216,0\ns_xor_b32 s82, s78, s10\ns_and_b32 s80, s82, 31\nMvaa 152,248,4\ns_lshl_b32 s80, s80, 18\ns_cmp_lt_u32 s78, 32\n" \
"Mvaa 156,252,4\ns_cselect_b64 s[68:69], s[14:15], s[12:13]\ns_cselect_b32 s70, 0x800000, s92\nMvaa 156,220,0\nv_add_u32 v18, s80, v16\nv_xor_b32 v19, 128, v18\ns_waitcnt vmcnt(30)\nMvaa 188,220,8\n" \
"Mvaa 188,252,12\nbuffer_load_dwordx4 a[0:3], v9, s[60:63], 0 offen offset:0 sc1\nMvaa 184,248,12\nMvaa 184,216,8\nMvaa 180,212,8\nMvaa 180,244,12\nbuffer_load_dwordx4 a[4:7], v9, s[60:63], 0 offen offset:1024 sc1\nMvaa 176,240,12\n" \
"Mvaa 176,208,8\nMvaa 172,204,8\ns_add_u32 s95, s24, 6144\nbuffer_store_dwordx4 v[80:83], v0, s[60:63], s95 offen offset:0 sc1\nMvaa 172,236,12\nMvaa 168,232,12\ns_waitcnt vmcnt(42)\ns_barrier\n" \
"Mvaa 168,200,8\nTa 128,20,49152\nTa 130,20,50176\nMvaa 164,196,8\nTa 160,20,57344\nMvaa 164,228,12\nTa 162,20,58368\nTa 132,21,49152\n" \
"Mvaa 160,224,12\nTa 134,21,50176\nMvaa 160,192,8\ns_waitcnt vmcnt(28)\nTa 164,21,57344\nMvaa 192,192,16\nTa 166,21,58368\nMvaa 192,224,20\n" \
"buffer_load_dwordx4 a[8:11], v9, s[60:63], s85 offen offset:0 sc1\nbuffer_store_dwordx4 v[84:87], v0, s[60:63], s95 offen offset:1024 sc1\nMvaa 196,228,20\nTa 136,22,49152\nMvaa 196,196,16\nTa 138,22,50176\nTa 168,22,57344\nMvaa 200,200,16\n" \
"Ta 170,22,58368\nMvaa 200,232,20\nbuffer_load_dwordx4 a[12:15], v9, s[60:63], s85 offen offset:1024 sc1\nMvaa 204,236,20\nTa 140,23,49152\nMvaa 204,204,16\nTa 142,23,50176\nTa 172,23,57344\n" \
"Mvaa 208,208,16\ns_add_u32 m0, s84, 0\nMvaa 208,240,20\nD 18,68,0,0\nMvaa 212,244,20\nTa 174,23,58368\nTa 144,24,49152\nMvaa 212,212,16\n" \
"Ta 146,24,50176\nMvaa 216,216,16\nTa 176,24,57344\nTa 178,24,58368\nMvaa 216,248,20\nD 18,68,s89,1024\ns_add_u32 s95, s25, 6144\nbuffer_store_dwordx4 v[88:91], v0, s[60:63], s95 offen offset:0 sc1\n" \
"Mvaa 220,252,20\nTa 148,25,49152\nMvaa 220,220,16\ns_waitcnt vmcnt(28)\nTa 150,25,50176\nMvaa 252,220,24\nTa 180,25,57344\nMvaa 252,252,28\n" \
"buffer_load_dwordx4 a[16:19], v9, s[60:63], s86 offen offset:0 sc1\nMvaa 248,248,28\nTa 182,25,58368\nMvaa 248,216,24\nTa 152,26,49152\nTa 154,26,50176\nMvaa 244,212,24\nTa 184,26,57344\n" \
"Mvaa 244,244,28\nbuffer_load_dwordx4 a[20:23], v9, s[60:63], s86 offen offset:1024 sc1\nMvaa 240,240,28\nTa 186,26,58368\nMvaa 240,208,24\nTa 156,27,49152\nTa 158,27,50176\nMvaa 236,204,24\n" \
"Ta 188,27,57344\nMvaa 236,236,28\nD 19,68,s90,2048\nbuffer_store_dwordx4 v[92:95], v0, s[60:63], s95 offen offset:1024 sc1\nMvaa 232,232,28\nTa 190,27,58368\nMvaa 232,200,24\nMvaa 228,196,24\n" \
"Mvaa 228,228,28\nD 19,68,s91,3072\nMvaa 224,224,28\nv_add_u32 v18, 131072, v18\nMvaa 224,192,24\nv_add_u32 v19, 131072, v19\ns_waitcnt vmcnt(29) lgkmcnt(0)\nMvaa 128,128,32\n" \
"Mvaa 128,160,36\nbuffer_load_dwordx4 a[24:27], v9, s[60:63], s87 offen offset:0 sc1\nMvaa 132,164,36\nMvaa 132,132,32\nMvaa 136,136,32\nMvaa 136,168,36\nbuffer_load_dwordx4 a[28:31], v9, s[60:63], s87 offen offset:1024 sc1\nMvaa 140,172,36\n" \
"Mvaa 140,140,32\nv_add_u32 v9, 0x8000, v9\nMvaa 144,144,32\nv_xor_b32 v20, 0x8000, v20\nv_xor_b32 v21, 0x8000, v21\nMvaa 144,176,36\nv_xor_b32 v22, 0x8000, v22\nv_xor_b32 v23, 0x8000, v23\n" \
"Mvaa 148,180,36\nv_xor_b32 v24, 0x8000, v24\nv_xor_b32 v25, 0x8000, v25\nMvaa 148,148,32\nbuffer_load_dwordx2 v[10:11], v1, s[60:63], 0 offen sc1\nMvaa 152,152,32\nv_xor_b32 v26, 0x8000, v26\nv_xor_b32 v27, 0x8000, v27\n" \
"Mvaa 152,184,36\nMvaa 156,188,36\nMvaa 156,156,32\ns_waitcnt vmcnt(29)\nMvaa 188,156,40\nMvaa 188,188,44\nbuffer_load_dwordx4 a[32:35], v9, s[60:63], 0 offen offset:0 sc1\nMvaa 184,184,44\n" \
"Mvaa 184,152,40\nMvaa 180,148,40\nMvaa 180,180,44\nbuffer_load_dwordx4 a[36:39], v9, s[60:63], 0 offen offset:1024 sc1\nMvaa 176,176,44\nMvaa 176,144,40\nMvaa 172,140,40\nMvaa 172,172,44\n" \
"Mvaa 168,168,44\ns_waitcnt vmcnt(41)\ns_barrier\nMvaa 168,136,40\nTa 192,20,32768\nTa 194,20,33792\nMvaa 164,132,40\nTa 224,20,40960\n" \
"Mvaa 164,164,44\nTa 226,20,41984\nTa 196,21,32768\ns_branch 99f\n.p2align 12\nzp18:\ns_setpc_b64 s[98:99]\n99:\n" \
"Mvaa 160,160,44\nTa 198,21,33792\nMvaa 160,128,40\ns_waitcnt vmcnt(25)\nTa 228,21,40960\nMvaa 192,128,48\nTa 230,21,41984\nMvaa 192,160,52\n" \
"buffer_load_dwordx4 a[40:43], v9, s[60:63], s85 offen offset:0 sc1\nMvaa 196,164,52\nTa 200,22,32768\nTa 202,22,33792\nMvaa 196,132,48\nTa 232,22,40960\nMvaa 200,136,48\nTa 234,22,41984\n" \
"Mvaa 200,168,52\nbuffer_load_dwordx4 a[44:47], v9, s[60:63], s85 offen offset:1024 sc1\nMvaa 204,172,52\nTa 204,23,32768\nTa 206,23,33792\nMvaa 204,140,48\nTa 236,23,40960\nMvaa 208,144,48\n" \
"s_add_u32 m0, s84, 16384\nTa 238,23,41984\nMvaa 208,176,52\nD 18,68,0,0\nMvaa 212,180,52\ns_xor_b32 s84, s84, 0x38000\nMvaa 212,148,48\nTa 208,24,32768\n" \
"Ta 210,24,33792\nMvaa 216,152,48\nTa 240,24,40960\nMvaa 216,184,52\nD 18,68,s89,1024\nMvaa 220,188,52\nTa 242,24,41984\nTa 212,25,32768\n" \
"Mvaa 220,156,48\ns_waitcnt vmcnt(24)\nMvaa 252,156,56\nTa 214,25,33792\nTa 244,25,40960\nMvaa 252,188,60\nbuffer_load_dwordx4 a[48:51], v9, s[60:63], s86 offen offset:0 sc1\nMvaa 248,184,60\n" \
"Ta 246,25,41984\ns_mov_b32 s96, 5\ns_mov_b32 s23, 3\nMvaa 248,152,56\nTa 216,26,32768\nTa 218,26,33792\nMvaa 244,148,56\nTa 248,26,40960\n" \
"Mvaa 244,180,60\nbuffer_load_dwordx4 a[52:55], v9, s[60:63], s86 offen offset:1024 sc1\nMvaa 240,176,60\nTa 250,26,41984\nMvaa 240,144,56\nTa 220,27,32768\nTa 222,27,33792\nMvaa 236,140,56\n" \
"Ta 252,27,40960\nMvaa 236,172,60\nD 19,68,s90,2048\nMvaa 232,168,60\nTa 254,27,41984\nMvaa 232,136,56\ns_waitcnt vmcnt(13)\nbuffer_store_dwordx2 v[2:3], off, s[60:63], s27 sc1\n" \
"Mvaa 228,132,56\nMvaa 228,164,60\nD 19,68,s91,3072\nMvaa 224,160,60\nMvaa 224,128,56\ns_waitcnt vmcnt(26) lgkmcnt(0)\ns_branch zol\nzo1:\n" \
"Mvaa 128,192,0\nMvaa 128,224,4\nbuffer_load_dwordx4 a[56:59], v9, s[60:63], s87 offen offset:0 sc1\nMvaa 132,228,4\nMvaa 132,196,0\nMvaa 136,200,0\ns_waitcnt vmcnt(26)\ns_barrier\n" \
"Mvaa 136,232,4\nbuffer_load_dwordx4 a[60:63], v9, s[60:63], s87 offen offset:1024 sc1\nMvaa 140,236,4\nTa 128,20,49152\nTa 130,20,50176\nMvaa 140,204,0\nTa 160,20,57344\nTa 162,20,58368\n" \
"Mvaa 144,208,0\nTa 132,21,49152\nMvaa 144,240,4\nTa 134,21,50176\nTa 164,21,57344\nMvaa 148,244,4\nTa 166,21,58368\nMvaa 148,212,0\n" \
"Ta 136,22,49152\nTa 138,22,50176\nMvaa 152,216,0\nTa 168,22,57344\nMvaa 152,248,4\nTa 170,22,58368\nTa 140,23,49152\nMvaa 156,252,4\n" \
"Ta 142,23,50176\nMvaa 156,220,0\nTa 172,23,57344\nTa 174,23,58368\ns_waitcnt vmcnt(21)\nMvaa 160,192,8\nTa 144,24,49152\nMvaa 160,224,12\n" \
"Ta 146,24,50176\nTa 176,24,57344\nMvaa 164,228,12\nTa 178,24,58368\nMvaa 164,196,8\nTa 148,25,49152\nTa 150,25,50176\nMvaa 168,200,8\n" \
"Ta 180,25,57344\nMvaa 168,232,12\nTa 182,25,58368\nTa 152,26,49152\nMvaa 172,236,12\nTa 154,26,50176\nMvaa 172,204,8\nTa 184,26,57344\n" \
"Ta 186,26,58368\nMvaa 176,208,8\nTa 156,27,49152\nMvaa 176,240,12\nTa 158,27,50176\nTa 188,27,57344\nMvaa 180,244,12\nTa 190,27,58368\n" \
"Mvaa 180,212,8\nMvaa 184,216,8\ns_mov_b64 s[64:65], s[16:17]\ns_mov_b32 s66, 8388608\nMvaa 184,248,12\nv_bfe_u32 v28, v0, 4, 4\nv_lshrrev_b32 v29, 9, v0\nMvaa 188,252,12\n" \
"v_and_b32 v30, 7, v28\nv_xor_b32 v29, v29, v30\nMvaa 188,220,8\ns_waitcnt vmcnt(10) lgkmcnt(0)\ns_lshl_b32 s82, s32, 2\nMvaa 128,128,32\nv_lshrrev_b32 v30, 5, v0\nv_lshlrev_b32 v28, 7, v28\n" \
"Mvaa 128,160,36\nv_and_b32 v30, 8, v30\nv_lshl_add_u32 v28, v29, 4, v28\nMvaa 132,164,36\nv_add3_u32 v12, v28, v30, s82\nv_xor_b32 v13, 32, v12\nMvaa 132,132,32\nv_xor_b32 v14, 64, v12\n" \
"v_xor_b32 v15, 96, v12\nMvaa 136,136,32\nv_lshrrev_b32 v28, 7, v0\nv_lshlrev_b32 v29, 4, v28\nMvaa 136,168,36\nv_xor_b32 v29, v29, v0\nv_add_u32 v5, s82, v29\nMvaa 140,172,36\n" \
"v_and_b32 v29, 0x70, v0\nv_lshl_add_u32 v6, v28, 12, v29\nMvaa 140,140,32\nv_add_u32 v8, 0x8000, v6\nMvaa 144,144,32\nMvaa 144,176,36\nMvaa 148,180,36\nMvaa 148,148,32\n" \
"Mvaa 152,152,32\nMvaa 152,184,36\nMvaa 156,188,36\nMvaa 156,156,32\ns_waitcnt vmcnt(8)\nMvaa 160,128,40\nMvaa 160,160,44\nMvaa 164,164,44\n" \
"Mvaa 164,132,40\nC 32,128,129\nC 33,130,131\nMvaa 168,136,40\nC 34,132,133\nC 35,134,135\nMvaa 168,168,44\nds_write_b64 v12, v[32:33] offset:0\n" \
"C 36,136,137\nMvaa 172,172,44\nC 37,138,139\nds_write_b64 v13, v[34:35] offset:0\nMvaa 172,140,40\nC 38,140,141\nC 39,142,143\nMvaa 176,144,40\n" \
"ds_write_b64 v14, v[36:37] offset:0\nC 40,144,145\nMvaa 176,176,44\nC 41,146,147\nds_write_b64 v15, v[38:39] offset:0\nMvaa 180,180,44\nC 42,148,149\nC 43,150,151\n" \
"Mvaa 180,148,40\nds_write_b64 v12, v[40:41] offset:2048\nC 44,152,153\nMvaa 184,152,40\nC 45,154,155\nds_write_b64 v13, v[42:43] offset:2048\nMvaa 184,184,44\nC 46,156,157\n" \
"C 47,158,159\nMvaa 188,188,44\nds_write_b64 v14, v[44:45] offset:2048\nds_write_b64 v15, v[46:47] offset:2048\nMvaa 188,156,40\ns_waitcnt vmcnt(4)\ns_add_u32 s95, s29, 0\nMvaa 192,192,16\n" \
"Mvaa 192,224,20\ns_waitcnt lgkmcnt(0)\nRv 32,5,0\nMvaa 192,128,48\nRv 36,5,1024\nRv 40,5,2048\nMvaa 192,160,52\nRv 44,5,3072\n" \
"Mvaa 196,228,20\nMvaa 196,196,16\nMvaa 196,164,52\nMvaa 196,132,48\nMvaa 200,200,16\ns_waitcnt lgkmcnt(0)\nMvaa 200,232,20\nbuffer_store_dwordx4 v[32:35], v6, s[64:67], s95 offen offset:0 sc1\n" \
"Mvaa 200,136,48\nbuffer_store_dwordx4 v[36:39], v8, s[64:67], s95 offen offset:0 sc1\nMvaa 200,168,52\nbuffer_store_dwordx4 v[40:43], v6, s[64:67], s95 offen offset:128 sc1\nMvaa 204,236,20\nbuffer_store_dwordx4 v[44:47], v8, s[64:67], s95 offen offset:128 sc1\nMvaa 204,204,16\nC 48,160,161\n" \
"C 49,162,163\nMvaa 204,172,52\nC 50,164,165\nC 51,166,167\nMvaa 204,140,48\nds_write_b64 v12, v[48:49] offset:4096\nC 52,168,169\nMvaa 208,208,16\n" \
"C 53,170,171\nds_write_b64 v13, v[50:51] offset:4096\nMvaa 208,240,20\nC 54,172,173\nC 55,174,175\nMvaa 208,144,48\nds_write_b64 v14, v[52:53] offset:4096\nC 56,176,177\n" \
"Mvaa 208,176,52\nC 57,178,179\nds_write_b64 v15, v[54:55] offset:4096\nMvaa 212,244,20\nC 58,180,181\nC 59,182,183\nMvaa 212,212,16\nds_write_b64 v12, v[56:57] offset:6144\n" \
"C 60,184,185\nMvaa 212,180,52\nC 61,186,187\nds_write_b64 v13, v[58:59] offset:6144\nMvaa 212,148,48\nC 62,188,189\nC 63,190,191\nMvaa 216,216,16\n" \
"ds_write_b64 v14, v[60:61] offset:6144\nds_write_b64 v15, v[62:63] offset:6144\nMvaa 216,248,20\ns_add_u32 s95, s29, 262144\nMvaa 216,152,48\nMvaa 216,184,52\ns_waitcnt lgkmcnt(0)\nRv 48,5,4096\n" \
"Mvaa 220,252,20\nRv 52,5,5120\nRv 56,5,6144\nMvaa 220,220,16\nRv 60,5,7168\nMvaa 220,188,52\nMvaa 220,156,48\ns_waitcnt vmcnt(4)\n" \
"Mvaa 224,192,24\nMvaa 224,224,28\nMvaa 224,128,56\ns_waitcnt lgkmcnt(0)\nMvaa 224,160,60\nbuffer_store_dwordx4 v[48:51], v6, s[64:67], s95 offen offset:0 sc1\nMvaa 228,228,28\nbuffer_store_dwordx4 v[52:55], v8, s[64:67], s95 offen offset:0 sc1\n" \
"Mvaa 228,196,24\nbuffer_store_dwordx4 v[56:59], v6, s[64:67], s95 offen offset:128 sc1\nMvaa 228,164,60\nbuffer_store_dwordx4 v[60:63], v8, s[64:67], s95 offen offset:128 sc1\nMvaa 228,132,56\nC 64,192,193\nC 65,194,195\nMvaa 232,200,24\n" \
"C 66,196,197\nC 67,198,199\nMvaa 232,232,28\nds_write_b64 v12, v[64:65] offset:8192\nC 68,200,201\nMvaa 232,136,56\nC 69,202,203\nds_write_b64 v13, v[66:67] offset:8192\n" \
"Mvaa 232,168,60\nC 70,204,205\nC 71,206,207\nMvaa 236,236,28\nds_write_b64 v14, v[68:69] offset:8192\nC 72,208,209\nMvaa 236,204,24\nC 73,210,211\n" \
"ds_write_b64 v15, v[70:71] offset:8192\nMvaa 236,172,60\nC 74,212,213\nC 75,214,215\nMvaa 236,140,56\nds_write_b64 v12, v[72:73] offset:10240\nC 76,216,217\nMvaa 240,208,24\n" \
"C 77,218,219\nds_write_b64 v13, v[74:75] offset:10240\nMvaa 240,240,28\nC 78,220,221\nC 79,222,223\nMvaa 240,144,56\nds_write_b64 v14, v[76:77] offset:10240\nds_write_b64 v15, v[78:79] offset:10240\n" \
"Mvaa 240,176,60\ns_add_u32 s95, s30, 0\nMvaa 244,244,28\nMvaa 244,212,24\ns_waitcnt lgkmcnt(0)\nRv 64,5,8192\nMvaa 244,180,60\nRv 68,5,9216\n" \
"Rv 72,5,10240\nMvaa 244,148,56\nRv 76,5,11264\nMvaa 248,216,24\nMvaa 248,248,28\nMvaa 248,152,56\nMvaa 248,184,60\nMvaa 252,252,28\n" \
"s_waitcnt lgkmcnt(0)\nMvaa 252,220,24\nbuffer_store_dwordx4 v[64:67], v6, s[64:67], s95 offen offset:0 sc1\nMvaa 252,188,60\nbuffer_store_dwordx4 v[68:71], v8, s[64:67], s95 offen offset:0 sc1\nMvaa 252,156,56\nbuffer_store_dwordx4 v[72:75], v6, s[64:67], s95 offen offset:128 sc1\nbuffer_store_dwordx4 v[76:79], v8, s[64:67], s95 offen offset:128 sc1\n" \
"s_nop 4\nC 80,224,225\nC 81,226,227\nC 82,228,229\nC 83,230,231\nds_write_b64 v12, v[80:81] offset:12288\nC 84,232,233\nC 85,234,235\n" \
"ds_write_b64 v13, v[82:83] offset:12288\nC 86,236,237\nC 87,238,239\nds_write_b64 v14, v[84:85] offset:12288\nC 88,240,241\nC 89,242,243\nds_write_b64 v15, v[86:87] offset:12288\nC 90,244,245\n" \
"C 91,246,247\nds_write_b64 v12, v[88:89] offset:14336\nC 92,248,249\nC 93,250,251\nds_write_b64 v13, v[90:91] offset:14336\nC 94,252,253\nC 95,254,255\nds_write_b64 v14, v[92:93] offset:14336\n" \
"ds_write_b64 v15, v[94:95] offset:14336\ns_add_u32 s95, s30, 262144\ns_waitcnt lgkmcnt(0)\nRv 80,5,12288\nRv 84,5,13312\nRv 88,5,14336\nRv 92,5,15360\ns_waitcnt lgkmcnt(0)\n" \
"buffer_store_dwordx4 v[80:83], v6, s[64:67], s95 offen offset:0 sc1\nbuffer_store_dwordx4 v[84:87], v8, s[64:67], s95 offen offset:0 sc1\nbuffer_store_dwordx4 v[88:91], v6, s[64:67], s95 offen offset:128 sc1\nbuffer_store_dwordx4 v[92:95], v8, s[64:67], s95 offen offset:128 sc1\ns_branch zfin\ns_branch 99f\n.p2align 12\nzqT:\n" \
"zp19:\ns_setpc_b64 s[98:99]\n99:\nzot:\nMvaa 128,192,0\ns_waitcnt vmcnt(10)\nv_cmp_eq_u64 vcc, v[10:11], v[2:3]\ns_mov_b32 s79, s78\n" \
"Mvaa 128,224,4\nbuffer_load_dwordx4 a[56:59], v9, s[60:63], s87 offen offset:0 sc1\ns_xor_b32 s82, s79, s10\nMvaa 132,228,4\ns_lshl_b32 s81, s82, 16\ns_cmp_lt_u32 s79, 32\ns_cselect_b32 s82, s18, s19\nMvaa 132,196,0\n" \
"s_add_u32 s81, s81, s82\ns_bitcmp1_b64 vcc, s79\nMvaa 136,200,0\nMvaa 136,232,4\nbuffer_load_dwordx4 a[60:63], v9, s[60:63], s87 offen offset:1024 sc1\nMvaa 140,236,4\nMvaa 140,204,0\nv_add_u32 v9, s81, v0\n" \
"s_waitcnt vmcnt(15)\ns_waitcnt vmcnt(27)\ns_barrier\nMvaa 172,204,8\nTa 128,20,49152\nTa 130,20,50176\nMvaa 172,236,12\nTa 160,20,57344\n" \
"Mvaa 168,232,12\ns_cbranch_scc0 zs105\nzr105:\nMvaa 168,200,8\nbuffer_load_dwordx4 a[64:67], v9, s[60:63], 0 offen offset:0 sc1\nMvaa 164,196,8\nTa 162,20,58368\nMvaa 164,228,12\n" \
"Ta 132,21,49152\nMvaa 160,224,12\nTa 134,21,50176\nTa 164,21,57344\nMvaa 160,192,8\nbuffer_load_dwordx4 a[68:71], v9, s[60:63], 0 offen offset:1024 sc1\ns_waitcnt vmcnt(14)\nMvaa 192,192,16\n" \
"Ta 166,21,58368\nMvaa 192,224,20\nTa 136,22,49152\nMvaa 196,228,20\nTa 138,22,50176\nMvaa 196,196,16\nbuffer_load_dwordx4 a[72:75], v9, s[60:63], s85 offen offset:0 sc1\nMvaa 200,200,16\n" \
"Ta 168,22,57344\nMvaa 200,232,20\nTa 170,22,58368\nMvaa 204,236,20\nTa 140,23,49152\nMvaa 204,204,16\nbuffer_load_dwordx4 a[76:79], v9, s[60:63], s85 offen offset:1024 sc1\ns_waitcnt vmcnt(14)\n" \
"Mvaa 236,204,24\nTa 142,23,50176\nMvaa 236,236,28\nTa 172,23,57344\nMvaa 232,232,28\nTa 174,23,58368\nMvaa 232,200,24\nbuffer_load_dwordx4 a[80:83], v9, s[60:63], s86 offen offset:0 sc1\n" \
"Mvaa 228,196,24\nMvaa 228,228,28\nMvaa 224,224,28\nMvaa 224,192,24\nbuffer_load_dwordx4 a[84:87], v9, s[60:63], s86 offen offset:1024 sc1\ns_waitcnt vmcnt(14) lgkmcnt(0)\nMvaa 128,128,32\nv_xor_b32 v20, 0x8000, v20\n" \
"v_xor_b32 v21, 0x8000, v21\nMvaa 128,160,36\nv_xor_b32 v22, 0x8000, v22\nv_xor_b32 v23, 0x8000, v23\nMvaa 132,164,36\nMvaa 132,132,32\nbuffer_load_dwordx4 a[88:91], v9, s[60:63], s87 offen offset:0 sc1\nMvaa 136,136,32\n" \
"Mvaa 136,168,36\nMvaa 140,172,36\nMvaa 140,140,32\nbuffer_load_dwordx4 a[92:95], v9, s[60:63], s87 offen offset:1024 sc1\ns_waitcnt vmcnt(13)\ns_waitcnt vmcnt(23)\ns_barrier\nMvaa 172,140,40\n" \
"Ta 192,20,32768\nTa 194,20,33792\nMvaa 172,172,44\nTa 224,20,40960\nMvaa 168,168,44\nTa 226,20,41984\nMvaa 168,136,40\nv_add_u32 v9, 0x8000, v9\n" \
"Mvaa 164,132,40\nTa 196,21,32768\nMvaa 164,164,44\nTa 198,21,33792\nMvaa 160,160,44\nTa 228,21,40960\nMvaa 160,128,40\nbuffer_load_dwordx4 a[96:99], v9, s[60:63], 0 offen offset:0 sc1\n" \
"s_waitcnt vmcnt(12)\nMvaa 192,128,48\nTa 230,21,41984\nMvaa 192,160,52\nTa 200,22,32768\nMvaa 196,164,52\nTa 202,22,33792\nMvaa 196,132,48\n" \
"buffer_load_dwordx4 a[100:103], v9, s[60:63], 0 offen offset:1024 sc1\nMvaa 200,136,48\nTa 232,22,40960\nMvaa 200,168,52\nTa 234,22,41984\nMvaa 204,172,52\nTa 204,23,32768\nMvaa 204,140,48\n" \
"buffer_load_dwordx4 a[104:107], v9, s[60:63], s85 offen offset:0 sc1\ns_waitcnt vmcnt(11)\nMvaa 236,140,56\nTa 206,23,33792\nMvaa 236,172,60\nTa 236,23,40960\nMvaa 232,168,60\nTa 238,23,41984\n" \
"Mvaa 232,136,56\nbuffer_load_dwordx4 a[108:111], v9, s[60:63], s85 offen offset:1024 sc1\nMvaa 228,132,56\nMvaa 228,164,60\nMvaa 224,160,60\nMvaa 224,128,56\nbuffer_load_dwordx4 a[112:115], v9, s[60:63], s86 offen offset:0 sc1\ns_waitcnt vmcnt(11) lgkmcnt(0)\n" \
"Mvaa 128,192,64\nv_bfe_u32 v28, v0, 4, 2\ns_and_b32 s82, s27, -32\nMvaa 128,224,68\nv_lshl_add_u32 v7, v28, 3, s82\nv_bfe_u32 v28, v0, 6, 2\nMvaa 132,228,68\nv_lshrrev_b32 v29, 8, v0\n" \
"v_bfe_u32 v30, v0, 4, 2\nMvaa 132,196,64\nbuffer_load_dwordx4 a[116:119], v9, s[60:63], s86 offen offset:1024 sc1\nMvaa 136,200,64\nv_cmp_eq_u32 vcc, v28, v29\nv_lshlrev_b32 v30, 4, v30\nMvaa 136,232,68\nv_mov_b32 v28, 0x3f80\n" \
"v_mov_b32 v29, 0\nMvaa 140,236,68\nv_cndmask_b32 v28, 0, v28, vcc\nv_lshlrev_b64 v[98:99], v30, v[28:29]\nMvaa 140,204,64\nbuffer_load_dwordx4 a[120:123], v9, s[60:63], s87 offen offset:0 sc1\ns_waitcnt vmcnt(11)\ns_waitcnt vmcnt(17)\n" \
"s_barrier\nMvaa 172,204,72\nTa 128,20,49152\nv_mov_b32 v96, 0\nMvaa 172,236,76\nTa 130,20,50176\nv_mov_b32 v97, 0\nMvaa 168,232,76\n" \
"Ta 160,20,57344\nv_mov_b32 v100, 0\nMvaa 168,200,72\nbuffer_load_dwordx4 a[124:127], v9, s[60:63], s87 offen offset:1024 sc1\nMvaa 164,196,72\nTa 162,20,58368\nv_mov_b32 v101, 0\nMvaa 164,228,76\n" \
"Ta 132,21,49152\ns_mov_b64 s[64:65], s[16:17]\nMvaa 160,224,76\nTa 134,21,50176\ns_mov_b32 s66, 8388608\nMvaa 160,192,72\ns_waitcnt vmcnt(10)\nTa 164,21,57344\n" \
"Mvaa 192,192,80\nTa 166,21,58368\nv_bfe_u32 v28, v0, 4, 4\nMvaa 192,224,84\nv_lshrrev_b32 v29, 9, v0\nv_and_b32 v30, 7, v28\nMvaa 196,228,84\nTa 136,22,49152\n" \
"v_xor_b32 v29, v29, v30\nMvaa 196,196,80\nTa 138,22,50176\ns_lshl_b32 s82, s32, 2\nMvaa 200,200,80\nTa 168,22,57344\nv_lshrrev_b32 v30, 5, v0\nMvaa 200,232,84\n" \
"Ta 170,22,58368\nv_lshlrev_b32 v28, 7, v28\nMvaa 204,236,84\nTa 140,23,49152\nv_and_b32 v30, 8, v30\nMvaa 204,204,80\ns_waitcnt vmcnt(8)\nTa 142,23,50176\n" \
"Mvaa 236,204,88\nTa 172,23,57344\nv_lshl_add_u32 v28, v29, 4, v28\nMvaa 236,236,92\nTa 174,23,58368\nv_add3_u32 v12, v28, v30, s82\nMvaa 232,232,92\nv_xor_b32 v13, 32, v12\n" \
"v_xor_b32 v14, 64, v12\nMvaa 232,200,88\nv_xor_b32 v15, 96, v12\nv_lshrrev_b32 v28, 7, v0\nMvaa 228,196,88\nv_lshlrev_b32 v29, 4, v28\nv_xor_b32 v29, v29, v0\nMvaa 228,228,92\n" \
"v_add_u32 v5, s82, v29\nv_and_b32 v29, 0x70, v0\nMvaa 224,224,92\nv_lshl_add_u32 v6, v28, 12, v29\nv_add_u32 v8, 0x8000, v6\nMvaa 224,192,88\ns_waitcnt vmcnt(6) lgkmcnt(0)\nMvaa 128,128,96\n" \
"Mvaa 128,160,100\nMvaa 132,164,100\nMvaa 132,132,96\nMvaa 136,136,96\nTa 144,24,49152\nMvaa 136,168,100\nTa 146,24,50176\nMvaa 140,172,100\n" \
"Ta 176,24,57344\nbuffer_load_dwordx2 v[10:11], v7, s[60:63], 0 offen sc1\nMvaa 140,140,96\ns_waitcnt vmcnt(5)\nMvaa 172,140,104\nTa 178,24,58368\nMvaa 172,172,108\nTa 148,25,49152\n" \
"Mvaa 168,168,108\nMvaa 168,136,104\nTa 150,25,50176\nMvaa 164,132,104\nTa 180,25,57344\nC 104,128,129\nC 105,130,131\nMvaa 164,164,108\n" \
"Ta 182,25,58368\nC 106,132,133\nC 107,134,135\nMvaa 160,160,108\nC 108,136,137\nC 109,138,139\nC 110,140,141\nMvaa 160,128,104\n" \
"s_waitcnt vmcnt(3)\nTa 152,26,49152\nC 111,142,143\nMvaa 192,128,112\ns_add_u32 s95, s24, 0\nbuffer_store_dwordx4 v[104:107], v0, s[60:63], s95 offen offset:0 sc1\nMvaa 192,160,116\nTa 154,26,50176\n" \
"buffer_store_dwordx4 v[108:111], v0, s[60:63], s95 offen offset:1024 sc1\nMvaa 196,164,116\nTa 184,26,57344\nMvaa 196,132,112\nTa 186,26,58368\nMvaa 200,136,112\nC 112,160,161\nC 113,162,163\n" \
"C 114,164,165\nMvaa 200,168,116\nTa 156,27,49152\nC 115,166,167\nC 116,168,169\nMvaa 204,172,116\nTa 158,27,50176\nC 117,170,171\n" \
"C 118,172,173\nMvaa 204,140,112\ns_waitcnt vmcnt(3)\nC 119,174,175\nMvaa 236,140,120\nTa 188,27,57344\nbuffer_store_dwordx4 v[112:115], v0, s[60:63], s95 offen offset:2048 sc1\nMvaa 236,172,124\n" \
"Ta 190,27,58368\nbuffer_store_dwordx4 v[116:119], v0, s[60:63], s95 offen offset:3072 sc1\nMvaa 232,168,124\nMvaa 232,136,120\nMvaa 228,132,120\nv_xor_b32 v24, 0x8000, v24\nv_xor_b32 v25, 0x8000, v25\nC 104,192,193\n" \
"Mvaa 228,164,124\nv_xor_b32 v26, 0x8000, v26\nv_xor_b32 v27, 0x8000, v27\nC 105,194,195\nMvaa 224,160,124\nC 106,196,197\nC 107,198,199\nC 108,200,201\n" \
"Mvaa 224,128,120\nC 109,202,203\nC 110,204,205\nC 111,206,207\nMvaa 144,208,0\ns_add_u32 s95, s24, 4096\nbuffer_store_dwordx4 v[104:107], v0, s[60:63], s95 offen offset:0 sc1\nMvaa 144,240,4\n" \
"buffer_store_dwordx4 v[108:111], v0, s[60:63], s95 offen offset:1024 sc1\nMvaa 148,244,4\nMvaa 148,212,0\nMvaa 152,216,0\nC 112,224,225\nC 113,226,227\nC 114,228,229\nMvaa 152,248,4\n" \
"C 115,230,231\nC 116,232,233\nC 117,234,235\nMvaa 156,252,4\nC 118,236,237\nC 119,238,239\nMvaa 156,220,0\nbuffer_store_dwordx4 v[112:115], v0, s[60:63], s95 offen offset:2048 sc1\n" \
"Mvaa 188,220,8\nbuffer_store_dwordx4 v[116:119], v0, s[60:63], s95 offen offset:3072 sc1\nMvaa 188,252,12\ns_waitcnt vmcnt(8)\nv_cmp_eq_u64 vcc, v[10:11], v[2:3]\ns_andn2_b32 s82, 10, vcc_lo\nMvaa 184,248,12\nMvaa 184,216,8\n" \
"Mvaa 180,212,8\ns_cbranch_scc1 zs106\nzr106:\nMvaa 180,244,12\ns_add_u32 s82, s26, 32768\ns_add_u32 s81, s82, 0\nMvaa 176,240,12\nbuffer_load_dwordx4 v[32:35], v0, s[60:63], s81 offen offset:0 sc1\n" \
"Mvaa 176,208,8\nMvaa 208,208,16\nMvaa 208,240,20\nMvaa 212,244,20\nbuffer_load_dwordx4 v[36:39], v0, s[60:63], s81 offen offset:1024 sc1\nMvaa 212,212,16\nMvaa 216,216,16\nMvaa 216,248,20\n" \
"Mvaa 220,252,20\nbuffer_load_dwordx4 v[40:43], v0, s[60:63], s81 offen offset:2048 sc1\nMvaa 220,220,16\nMvaa 252,220,24\nMvaa 252,252,28\nMvaa 248,248,28\nbuffer_load_dwordx4 v[44:47], v0, s[60:63], s81 offen offset:3072 sc1\nMvaa 248,216,24\n" \
"Mvaa 244,212,24\nMvaa 244,244,28\nMvaa 240,240,28\ns_add_u32 s81, s82, 4096\nMvaa 240,208,24\ns_waitcnt lgkmcnt(0)\nMvaa 144,144,32\nbuffer_load_dwordx4 v[48:51], v0, s[60:63], s81 offen offset:0 sc1\n" \
"Mvaa 144,176,36\nTa 208,24,32768\nMvaa 148,180,36\nTa 210,24,33792\nMvaa 148,148,32\nMvaa 152,152,32\nTa 240,24,40960\nMvaa 152,184,36\n" \
"Ta 242,24,41984\nMvaa 156,188,36\nbuffer_load_dwordx4 v[52:55], v0, s[60:63], s81 offen offset:1024 sc1\nMvaa 156,156,32\nTa 212,25,32768\nMvaa 188,156,40\nTa 214,25,33792\nMvaa 188,188,44\n" \
"Mvaa 184,184,44\nTa 244,25,40960\nMvaa 184,152,40\nTa 246,25,41984\nMvaa 180,148,40\nbuffer_load_dwordx4 v[56:59], v0, s[60:63], s81 offen offset:2048 sc1\nMvaa 180,180,44\nTa 216,26,32768\n" \
"Mvaa 176,176,44\nMvaa 176,144,40\nTa 218,26,33792\nMvaa 208,144,48\nTa 248,26,40960\nMvaa 208,176,52\nbuffer_load_dwordx4 v[60:63], v0, s[60:63], s81 offen offset:3072 sc1\nMvaa 212,180,52\n" \
"Ta 250,26,41984\nMvaa 212,148,48\nTa 220,27,32768\nMvaa 216,152,48\nMvaa 216,184,52\nTa 222,27,33792\ns_add_u32 s82, s26, 98304\nMvaa 220,188,52\n" \
"Ta 252,27,40960\ns_add_u32 s81, s82, 0\nMvaa 220,156,48\nbuffer_load_dwordx4 v[64:67], v0, s[60:63], s81 offen offset:0 sc1\nMvaa 252,156,56\nTa 254,27,41984\nMvaa 252,188,60\nMvaa 248,184,60\n" \
"Mvaa 248,152,56\nbuffer_load_dwordx4 v[68:71], v0, s[60:63], s81 offen offset:1024 sc1\nMvaa 244,148,56\nMvaa 244,180,60\nMvaa 240,176,60\nMvaa 240,144,56\ns_waitcnt lgkmcnt(0)\nMvaa 144,208,64\n" \
"buffer_load_dwordx4 v[72:75], v0, s[60:63], s81 offen offset:2048 sc1\nMvaa 144,240,68\nTa 144,24,49152\nMvaa 148,244,68\nTa 146,24,50176\nMvaa 148,212,64\nMvaa 152,216,64\nTa 176,24,57344\n" \
"s_waitcnt vmcnt(11)\nbuffer_store_dwordx2 v[2:3], off, s[60:63], s27 sc1\nMvaa 152,248,68\nTa 178,24,58368\nMvaa 156,252,68\nbuffer_load_dwordx4 v[76:79], v0, s[60:63], s81 offen offset:3072 sc1\nMvaa 156,220,64\nTa 148,25,49152\n" \
"Mvaa 188,220,72\nTa 150,25,50176\nMvaa 188,252,76\nMvaa 184,248,76\nTa 180,25,57344\ns_add_u32 s81, s82, 4096\nMvaa 184,216,72\nTa 182,25,58368\n" \
"Mvaa 180,212,72\nbuffer_load_dwordx4 v[80:83], v0, s[60:63], s81 offen offset:0 sc1\nMvaa 180,244,76\nTa 152,26,49152\nMvaa 176,240,76\nMvaa 176,208,72\nTa 154,26,50176\nMvaa 208,208,80\n" \
"Ta 184,26,57344\nMvaa 208,240,84\nbuffer_load_dwordx4 v[84:87], v0, s[60:63], s81 offen offset:1024 sc1\nMvaa 212,244,84\nTa 186,26,58368\nMvaa 212,212,80\nTa 156,27,49152\nMvaa 216,216,80\n" \
"Mvaa 216,248,84\nTa 158,27,50176\nMvaa 220,252,84\nTa 188,27,57344\nMvaa 220,220,80\nbuffer_load_dwordx4 v[88:91], v0, s[60:63], s81 offen offset:2048 sc1\nMvaa 252,220,88\nTa 190,27,58368\n" \
"Mvaa 252,252,92\nMvaa 248,248,92\nMvaa 248,216,88\nbuffer_load_dwordx4 v[92:95], v0, s[60:63], s81 offen offset:3072 sc1\nMvaa 244,212,88\nMvaa 244,244,92\nMvaa 240,240,92\nMvaa 240,208,88\n" \
"s_waitcnt lgkmcnt(0)\nMvaa 144,144,96\nbuffer_load_dwordx2 v[10:11], v7, s[60:63], 0 offen sc1\nMvaa 144,176,100\nMvaa 148,180,100\nMvaa 148,148,96\nMvaa 152,152,96\nMvaa 152,184,100\n" \
"Mvaa 156,188,100\nMvaa 156,156,96\nMvaa 188,156,104\nMvaa 188,188,108\nMvaa 184,184,108\nMvaa 184,152,104\nMvaa 180,148,104\nMvaa 180,180,108\n" \
"Mvaa 176,176,108\nMvaa 176,144,104\nMvaa 208,144,112\nbuffer_load_dwordx2 v[102:103], v7, s[60:63], 0 offen sc1\nMvaa 208,176,116\nMvaa 212,180,116\nMvaa 212,148,112\nMvaa 216,152,112\n" \
"Mvaa 216,184,116\nMvaa 220,188,116\nMvaa 220,156,112\nMvaa 252,156,120\nMvaa 252,188,124\nMvaa 248,184,124\nMvaa 248,152,120\nMvaa 244,148,120\n" \
"Mvaa 244,180,124\nMvaa 240,176,124\nMvaa 240,144,120\nbuffer_load_dwordx2 v[120:121], v7, s[60:63], 0 offen sc1\ns_waitcnt vmcnt(2)\nv_cmp_eq_u64 vcc, v[10:11], v[2:3]\ns_andn2_b32 s82, s6, vcc_lo\ns_cbranch_scc1 zpl\n" \
"zpe:\ns_waitcnt vmcnt(18)\nMvvv 144,98,32\ns_add_u32 s82, s26, s28\ns_add_u32 s81, s82, 0\nbuffer_load_dwordx4 a[0:3], v0, s[60:63], s81 offen offset:0 sc1\nMvvv 148,96,32\nMvvv 152,98,36\n" \
"Mvvv 156,96,36\nbuffer_load_dwordx4 a[4:7], v0, s[60:63], s81 offen offset:1024 sc1\ns_waitcnt vmcnt(18)\nMvvv 176,98,40\nMvvv 180,96,40\nMvvv 184,98,44\nbuffer_load_dwordx4 a[8:11], v0, s[60:63], s81 offen offset:2048 sc1\nMvvv 188,96,44\n" \
"s_waitcnt vmcnt(17)\nMvvv 208,98,48\nMvvv 212,96,48\ns_branch 99f\n.p2align 12\nzp20:\ns_setpc_b64 s[98:99]\n99:\n" \
"buffer_load_dwordx4 a[12:15], v0, s[60:63], s81 offen offset:3072 sc1\nMvvv 216,98,52\nMvvv 220,96,52\ns_waitcnt vmcnt(16)\nMvvv 240,98,56\ns_add_u32 s81, s82, 4096\nbuffer_load_dwordx4 a[16:19], v0, s[60:63], s81 offen offset:0 sc1\nMvvv 244,96,56\n" \
"Mvvv 248,98,60\nMvvv 252,96,60\nbuffer_load_dwordx4 a[20:23], v0, s[60:63], s81 offen offset:1024 sc1\ns_waitcnt vmcnt(16)\nMvvv 144,98,64\nMvvv 148,96,64\nMvvv 152,98,68\nbuffer_load_dwordx4 a[24:27], v0, s[60:63], s81 offen offset:2048 sc1\n" \
"Mvvv 156,96,68\ns_waitcnt vmcnt(14)\nMvvv 176,98,72\nMvvv 180,96,72\nbuffer_load_dwordx4 a[28:31], v0, s[60:63], s81 offen offset:3072 sc1\nMvvv 184,98,76\nMvvv 188,96,76\ns_waitcnt vmcnt(13)\n" \
"Mvvv 208,98,80\nMvvv 212,96,80\nMvvv 216,98,84\nMvvv 220,96,84\ns_waitcnt vmcnt(11)\nMvvv 240,98,88\nMvvv 244,96,88\nMvvv 248,98,92\n" \
"Mvvv 252,96,92\ns_waitcnt vmcnt(6)\nMvva 144,98,0\nMvva 148,96,0\nMvva 152,98,4\nMvva 156,96,4\ns_waitcnt vmcnt(4)\nMvva 176,98,8\n" \
"Mvva 180,96,8\nMvva 184,98,12\nMvva 188,96,12\nC 64,144,145\nC 65,146,147\nC 66,148,149\nC 67,150,151\nds_write_b64 v12, v[64:65] offset:0\n" \
"C 68,152,153\nC 69,154,155\nds_write_b64 v13, v[66:67] offset:0\nC 70,156,157\nC 71,158,159\nds_write_b64 v14, v[68:69] offset:0\nds_write_b64 v15, v[70:71] offset:0\ns_waitcnt vmcnt(2)\n" \
"Mvva 208,98,16\nMvva 212,96,16\nMvva 216,98,20\nMvva 220,96,20\ns_waitcnt lgkmcnt(0)\nRv 64,5,0\nRv 68,5,1024\nC 72,176,177\n" \
"C 73,178,179\nC 74,180,181\nC 75,182,183\nds_write_b64 v12, v[72:73] offset:2048\nC 76,184,185\nC 77,186,187\nds_write_b64 v13, v[74:75] offset:2048\nC 78,188,189\n" \
"C 79,190,191\nds_write_b64 v14, v[76:77] offset:2048\nds_write_b64 v15, v[78:79] offset:2048\ns_waitcnt vmcnt(0)\nMvva 240,98,24\nMvva 244,96,24\nMvva 248,98,28\nMvva 252,96,28\n" \
"s_waitcnt lgkmcnt(0)\nRv 72,5,2048\nRv 76,5,3072\nC 80,208,209\nC 81,210,211\nC 82,212,213\nC 83,214,215\nds_write_b64 v12, v[80:81] offset:4096\n" \
"C 84,216,217\nC 85,218,219\nds_write_b64 v13, v[82:83] offset:4096\nC 86,220,221\nC 87,222,223\nds_write_b64 v14, v[84:85] offset:4096\nds_write_b64 v15, v[86:87] offset:4096\ns_add_u32 s95, s29, 0\n" \
"s_waitcnt lgkmcnt(10)\nbuffer_store_dwordx4 v[64:67], v6, s[64:67], s95 offen offset:0 sc1\nbuffer_store_dwordx4 v[68:71], v8, s[64:67], s95 offen offset:0 sc1\ns_nop 4\ns_waitcnt lgkmcnt(0)\nRv 80,5,4096\nRv 84,5,5120\nC 88,240,241\n" \
"C 89,242,243\nC 90,244,245\nC 91,246,247\nds_write_b64 v12, v[88:89] offset:6144\nC 92,248,249\nC 93,250,251\nds_write_b64 v13, v[90:91] offset:6144\nC 94,252,253\n" \
"C 95,254,255\nds_write_b64 v14, v[92:93] offset:6144\nds_write_b64 v15, v[94:95] offset:6144\ns_add_u32 s95, s29, 262144\ns_waitcnt lgkmcnt(10)\nbuffer_store_dwordx4 v[72:75], v6, s[64:67], s95 offen offset:0 sc1\nbuffer_store_dwordx4 v[76:79], v8, s[64:67], s95 offen offset:0 sc1\ns_waitcnt lgkmcnt(0)\n" \
"Rv 88,5,6144\nRv 92,5,7168\ns_add_u32 s95, s30, 0\ns_waitcnt lgkmcnt(6)\nbuffer_store_dwordx4 v[80:83], v6, s[64:67], s95 offen offset:0 sc1\nbuffer_store_dwordx4 v[84:87], v8, s[64:67], s95 offen offset:0 sc1\ns_add_u32 s95, s30, 262144\ns_waitcnt lgkmcnt(0)\n" \
"buffer_store_dwordx4 v[88:91], v6, s[64:67], s95 offen offset:0 sc1\nbuffer_store_dwordx4 v[92:95], v8, s[64:67], s95 offen offset:0 sc1\nzfin:\n.subsection 1\ns_branch 99f\n.p2align 12\nzp21:\ns_setpc_b64 s[98:99]\n" \
"99:\nzV:\nZvaa 32,0,64\nTa 96,8,8192\ns_lshr_b32 s7, s3, 5\nZvaa 36,4,64\nTa 98,8,9216\ns_lshr_b32 s5, 170, s7\n" \
"Zvaa 44,4,68\nRa 32,27,0\ns_and_b32 s5, s5, 1\nZvaa 40,0,68\nTa 100,20,8192\ns_lshl_b32 s7, s7, 2\nZvaa 48,0,72\nTa 102,20,9216\n" \
"s_lshr_b32 s4, 0x12124444, s7\nZvaa 52,4,72\nRa 36,27,2048\ns_and_b32 s4, s4, 15\nZvaa 60,4,76\nTa 104,21,8192\ns_xor_b32 s6, s5, s3\nZvaa 56,0,76\n" \
"Ta 106,21,9216\ns_lshr_b32 s7, 0xff00, s4\nZvaa 64,0,80\nRa 40,27,4096\ns_and_b32 s7, s7, 0xff\nZvaa 68,4,80\nTa 108,22,8192\ns_xor_b32 s10, s7, 0xff\n" \
"Zvaa 76,4,84\nTa 110,22,9216\ns_lshl_b32 s7, s7, 8\nZvaa 72,0,84\nRa 44,27,6144\ns_or_b32 s7, s7, 0xff\nZvaa 80,0,88\nTa 112,23,8192\n" \
"s_add_u32 s8, s4, 5\nZvaa 84,4,88\nTa 114,23,9216\ns_sub_u32 s4, 262, s4\nZvaa 92,4,92\nRa 48,27,8192\ns_add_u32 s9, s4, 0x101\nZvaa 88,0,92\n" \
"Ta 116,24,8192\nZvaa 152,8,92\nTa 118,24,9216\nZvaa 156,12,92\nRa 52,27,10240\nZvaa 148,12,88\nTa 120,25,8192\nZvaa 144,8,88\n" \
"Ta 122,25,9216\nZvaa 136,8,84\nRa 56,27,12288\nZvaa 140,12,84\nTa 124,26,8192\nZvaa 132,12,80\nTa 126,26,9216\nZvaa 128,8,80\n" \
"Ra 60,27,14336\nZvaa 120,8,76\nAR 2,252\nAR 3,253\nZvaa 124,12,76\nv_readfirstlane_b32 s96, v2\nv_readfirstlane_b32 s97, v3\nZvaa 116,12,72\n" \
"s_mul_i32 s97, s97, 0x7f4a7c15\ns_mul_hi_u32 s98, s96, 0x7f4a7c15\nZvaa 112,8,72\ns_add_u32 s97, s97, s98\ns_mul_i32 s98, s96, 0x9e3779b9\nZvaa 104,8,68\ns_add_u32 s97, s97, s98\ns_mul_i32 s96, s96, 0x7f4a7c15\n" \
"Zvaa 108,12,68\ns_add_u32 s96, s96, 0xd9b4e019\ns_addc_u32 s97, s97, 0x632be59b\nZvaa 100,12,64\nv_mov_b32 v2, s96\nv_mov_b32 v3, s97\nZvaa 96,8,64\ns_mov_b32 s96, 3\n" \
"Zvaa 160,16,64\ns_waitcnt lgkmcnt(0)\ns_barrier\nZvaa 164,20,64\ns_add_u32 m0, s88, 0\nZvaa 172,20,68\nD 5,64,0,0\nZvaa 168,16,68\n" \
"s_cmp_lt_u32 s3, 128\ns_cselect_b32 s4, s9, s4\nZvaa 176,16,72\ns_bitcmp1_b32 s6, 0\ns_cselect_b32 s9, s4, s8\ns_cselect_b32 s7, s10, s7\nZvaa 180,20,72\ns_and_b32 s6, s6, 1\n" \
"s_lshr_b32 s10, s3, 1\nZvaa 188,20,76\nD 5,64,s73,1024\nZvaa 184,16,76\ns_and_b32 s10, s10, 2\ns_or_b32 s6, s6, s10\nZvaa 192,16,80\ns_lshl_b32 s6, s6, 3\n" \
"s_lshl_b32 s8, s7, s6\nZvaa 196,20,80\ns_bfe_u32 s10, s3, 0x20003\ns_add_u32 s6, s6, s10\nZvaa 204,20,84\nD 5,64,s74,2048\nZvaa 200,16,84\ns_bitset0_b32 s8, s6\n" \
"s_lshl_b32 s5, s5, 16\nZvaa 208,16,88\ns_or_b32 s9, s9, s5\nZvaa 212,20,88\nZvaa 220,20,92\nD 5,64,s75,3072\nZvaa 216,16,92\nZaaa 152,24,92\n" \
"Zaaa 156,28,92\ns_add_u32 m0, s88, 4096\nZaaa 148,28,88\nD 5,64,s76,0\nZaaa 144,24,88\nZaaa 136,24,84\nZaaa 140,28,84\nZaaa 132,28,80\n" \
"D 5,64,s77,1024\nZaaa 128,24,80\nZvaa 248,24,76\nZvaa 252,28,76\nZvaa 244,28,72\nD 5,64,s78,2048\nZvaa 240,24,72\nZvaa 232,24,68\n" \
"Zvaa 236,28,68\nZvaa 228,28,64\nD 5,64,s79,3072\nZvaa 224,24,64\ns_branch 99f\n.p2align 12\nzp22:\ns_setpc_b64 s[98:99]\n" \
"99:\ns_nop 0\nzgV:\nMvaa 32,32,96\nMvaa 36,36,96\ns_add_u32 m0, s32, 65536\nMvaa 44,36,100\nD 6,68,0,0\n" \
"Mvaa 40,32,100\nMvaa 48,32,104\nMvaa 52,36,104\nMvaa 60,36,108\nD 6,68,s89,1024\nMvaa 56,32,108\nMvaa 64,32,112\nMvaa 68,36,112\n" \
"Mvaa 76,36,116\nD 28,68,s90,2048\nMvaa 72,32,116\nMvaa 80,32,120\nMvaa 84,36,120\nMvaa 92,36,124\nD 28,68,s91,3072\nMvaa 88,32,124\n" \
"Mvaa 152,40,124\nMvaa 156,44,124\ns_add_u32 m0, s32, 81920\nMvaa 148,44,120\nD 6,68,s92,0\nMvaa 144,40,120\nMvaa 136,40,116\nMvaa 140,44,116\n" \
"Mvaa 132,44,112\nD 6,68,s93,1024\nMvaa 128,40,112\nMvaa 120,40,108\nMvaa 124,44,108\nMvaa 116,44,104\nD 28,68,s94,2048\nMvaa 112,40,104\n" \
"Mvaa 104,40,100\nMvaa 108,44,100\nMvaa 100,44,96\nD 28,68,s95,3072\nMvaa 96,40,96\ns_waitcnt vmcnt(17)\ns_barrier\nMvaa 160,48,96\n" \
"buffer_load_dword v30, v4, s[52:55], 0 offen sc1\nTa 64,8,32768\nv_add_u32 v5, 128, v5\nMvaa 164,52,96\nTa 66,8,33792\nv_add_u32 v6, s72, v6\nMvaa 172,52,100\nRa 0,7,32768\n" \
"v_add_u32 v28, s72, v28\nMvaa 168,48,100\nTa 68,20,32768\nMvaa 176,48,104\nTa 70,20,33792\nMvaa 180,52,104\nRa 4,7,34816\nMvaa 188,52,108\n" \
"v_add_u32 v4, s44, v4\nTa 72,21,32768\nMvaa 184,48,108\nTa 74,21,33792\nMvaa 192,48,112\nRa 8,7,36864\nMvaa 196,52,112\nTa 76,22,32768\n" \
"Mvaa 204,52,116\nTa 78,22,33792\nMvaa 200,48,116\nRa 12,7,38912\nMvaa 208,48,120\nTa 80,23,32768\nMvaa 212,52,120\nTa 82,23,33792\n" \
"Mvaa 220,52,124\nRa 16,7,40960\nMvaa 216,48,124\nTa 84,24,32768\nMaaa 152,56,124\nTa 86,24,33792\nMaaa 156,60,124\nRa 20,7,43008\n" \
"Maaa 148,60,120\nTa 88,25,32768\nMaaa 144,56,120\nTa 90,25,33792\nMaaa 136,56,116\nRa 24,7,45056\nMaaa 140,60,116\nTa 92,26,32768\n" \
"Maaa 132,60,112\nTa 94,26,33792\nMaaa 128,56,112\nRa 28,7,47104\nMvaa 248,56,108\nMvaa 252,60,108\nMvaa 244,60,104\nMvaa 240,56,104\n" \
"Mvaa 232,56,100\nMvaa 236,60,100\nMvaa 228,60,96\nMvaa 224,56,96\ns_waitcnt lgkmcnt(0)\nMvaa 32,0,64\nTa 96,8,40960\nMvaa 36,4,64\n" \
"Ta 98,8,41984\nMvaa 44,4,68\nRa 32,27,32768\nMvaa 40,0,68\nTa 100,20,40960\nMvaa 48,0,72\nTa 102,20,41984\nMvaa 52,4,72\n" \
"Ra 36,27,34816\nMvaa 60,4,76\nTa 104,21,40960\nMvaa 56,0,76\nTa 106,21,41984\nMvaa 64,0,80\nRa 40,27,36864\nMvaa 68,4,80\n" \
"Ta 108,22,40960\nMvaa 76,4,84\nTa 110,22,41984\nMvaa 72,0,84\nRa 44,27,38912\nMvaa 80,0,88\nTa 112,23,40960\nMvaa 84,4,88\n" \
"Ta 114,23,41984\nMvaa 92,4,92\nRa 48,27,40960\nMvaa 88,0,92\nTa 116,24,40960\nMvaa 152,8,92\nTa 118,24,41984\nMvaa 156,12,92\n" \
"Ra 52,27,43008\nMvaa 148,12,88\nTa 120,25,40960\nMvaa 144,8,88\nTa 122,25,41984\nMvaa 136,8,84\nRa 56,27,45056\nMvaa 140,12,84\n" \
"Ta 124,26,40960\nMvaa 132,12,80\nTa 126,26,41984\nMvaa 128,8,80\nRa 60,27,47104\nMvaa 120,8,76\nMvaa 124,12,76\nMvaa 116,12,72\n" \
"Mvaa 112,8,72\nMvaa 104,8,68\nMvaa 108,12,68\nMvaa 100,12,64\nMvaa 96,8,64\nMvaa 160,16,64\ns_waitcnt lgkmcnt(0)\ns_barrier\n" \
"Mvaa 164,20,64\ns_add_u32 m0, s88, 32768\nMvaa 172,20,68\nD 5,64,0,0\nMvaa 168,16,68\nMvaa 176,16,72\nMvaa 180,20,72\nMvaa 188,20,76\n" \
"D 5,64,s73,1024\nMvaa 184,16,76\nMvaa 192,16,80\nMvaa 196,20,80\nMvaa 204,20,84\nD 5,64,s74,2048\nMvaa 200,16,84\nMvaa 208,16,88\n" \
"Mvaa 212,20,88\nMvaa 220,20,92\nD 5,64,s75,3072\nMvaa 216,16,92\nMaaa 152,24,92\nMaaa 156,28,92\ns_add_u32 m0, s88, 36864\nMaaa 148,28,88\n" \
"D 5,64,s76,0\nMaaa 144,24,88\nMaaa 136,24,84\nMaaa 140,28,84\nMaaa 132,28,80\nD 5,64,s77,1024\nMaaa 128,24,80\nMvaa 248,24,76\n" \
"Mvaa 252,28,76\nMvaa 244,28,72\nD 5,64,s78,2048\nMvaa 240,24,72\nMvaa 232,24,68\nMvaa 236,28,68\nMvaa 228,28,64\nD 5,64,s79,3072\n" \
"Mvaa 224,24,64\nMvaa 32,32,96\nMvaa 36,36,96\ns_add_u32 m0, s32, 98304\nMvaa 44,36,100\nD 6,68,0,0\nMvaa 40,32,100\nMvaa 48,32,104\n" \
"Mvaa 52,36,104\nMvaa 60,36,108\nD 6,68,s89,1024\nMvaa 56,32,108\nMvaa 64,32,112\nMvaa 68,36,112\nMvaa 76,36,116\nD 28,68,s90,2048\n" \
"Mvaa 72,32,116\nMvaa 80,32,120\nMvaa 84,36,120\nMvaa 92,36,124\nD 28,68,s91,3072\nMvaa 88,32,124\nMvaa 152,40,124\nMvaa 156,44,124\n" \
"s_add_u32 m0, s32, 114688\nMvaa 148,44,120\nD 6,68,s92,0\nMvaa 144,40,120\nMvaa 136,40,116\nMvaa 140,44,116\nMvaa 132,44,112\nD 6,68,s93,1024\n" \
"Mvaa 128,40,112\nMvaa 120,40,108\nMvaa 124,44,108\nMvaa 116,44,104\nD 28,68,s94,2048\nMvaa 112,40,104\nMvaa 104,40,100\nMvaa 108,44,100\n" \
"Mvaa 100,44,96\nD 28,68,s95,3072\nMvaa 96,40,96\ns_waitcnt vmcnt(17)\ns_barrier\nMvaa 160,48,96\nbuffer_load_dword v30, v4, s[52:55], 0 offen sc1\nTa 64,8,0\n" \
"v_add_u32 v5, 128, v5\nMvaa 164,52,96\nTa 66,8,1024\nv_add_u32 v6, s72, v6\nMvaa 172,52,100\nRa 0,7,0\nv_add_u32 v28, s72, v28\nMvaa 168,48,100\n" \
"Ta 68,20,0\nMvaa 176,48,104\nTa 70,20,1024\nMvaa 180,52,104\nRa 4,7,2048\nMvaa 188,52,108\nv_add_u32 v4, s44, v4\nTa 72,21,0\n" \
"Mvaa 184,48,108\nTa 74,21,1024\nMvaa 192,48,112\nRa 8,7,4096\nMvaa 196,52,112\nTa 76,22,0\nMvaa 204,52,116\nTa 78,22,1024\n" \
"Mvaa 200,48,116\nRa 12,7,6144\nMvaa 208,48,120\nTa 80,23,0\nMvaa 212,52,120\nTa 82,23,1024\nMvaa 220,52,124\nRa 16,7,8192\n" \
"Mvaa 216,48,124\nTa 84,24,0\nMaaa 152,56,124\nTa 86,24,1024\nMaaa 156,60,124\nRa 20,7,10240\nMaaa 148,60,120\nTa 88,25,0\n" \
"Maaa 144,56,120\nTa 90,25,1024\nMaaa 136,56,116\nRa 24,7,12288\nMaaa 140,60,116\nTa 92,26,0\nMaaa 132,60,112\nTa 94,26,1024\n" \
"Maaa 128,56,112\nRa 28,7,14336\nMvaa 248,56,108\nMvaa 252,60,108\nMvaa 244,60,104\nMvaa 240,56,104\ns_sub_u32 s96, s96, 1\nMvaa 232,56,100\n" \
"s_cmp_lg_u32 s96, 0\nMvaa 236,60,100\nMvaa 228,60,96\nMvaa 224,56,96\ns_waitcnt lgkmcnt(0)\ns_cbranch_scc0 zhV\nMvaa 32,0,64\nTa 96,8,8192\n" \
"Mvaa 36,4,64\nTa 98,8,9216\nMvaa 44,4,68\nRa 32,27,0\nMvaa 40,0,68\nTa 100,20,8192\nMvaa 48,0,72\nTa 102,20,9216\n" \
"Mvaa 52,4,72\nRa 36,27,2048\nMvaa 60,4,76\nTa 104,21,8192\nMvaa 56,0,76\nTa 106,21,9216\nMvaa 64,0,80\nRa 40,27,4096\n" \
"Mvaa 68,4,80\nTa 108,22,8192\nMvaa 76,4,84\nTa 110,22,9216\nMvaa 72,0,84\nRa 44,27,6144\nMvaa 80,0,88\nTa 112,23,8192\n" \
"Mvaa 84,4,88\nTa 114,23,9216\nMvaa 92,4,92\nRa 48,27,8192\nMvaa 88,0,92\nTa 116,24,8192\nMvaa 152,8,92\nTa 118,24,9216\n" \
"Mvaa 156,12,92\nRa 52,27,10240\nMvaa 148,12,88\nTa 120,25,8192\nMvaa 144,8,88\nTa 122,25,9216\nMvaa 136,8,84\nRa 56,27,12288\n" \
"Mvaa 140,12,84\nTa 124,26,8192\nMvaa 132,12,80\nTa 126,26,9216\nMvaa 128,8,80\nRa 60,27,14336\nMvaa 120,8,76\nMvaa 124,12,76\n" \
"Mvaa 116,12,72\nMvaa 112,8,72\nMvaa 104,8,68\nMvaa 108,12,68\nMvaa 100,12,64\nMvaa 96,8,64\nMvaa 160,16,64\ns_waitcnt lgkmcnt(0)\n" \
"s_barrier\nMvaa 164,20,64\ns_add_u32 m0, s88, 0\nMvaa 172,20,68\nD 5,64,0,0\nMvaa 168,16,68\nMvaa 176,16,72\nMvaa 180,20,72\n" \
"Mvaa 188,20,76\nD 5,64,s73,1024\nMvaa 184,16,76\nMvaa 192,16,80\nMvaa 196,20,80\nMvaa 204,20,84\nD 5,64,s74,2048\nMvaa 200,16,84\n" \
"Mvaa 208,16,88\nMvaa 212,20,88\nMvaa 220,20,92\nD 5,64,s75,3072\nMvaa 216,16,92\nMaaa 152,24,92\nMaaa 156,28,92\ns_add_u32 m0, s88, 4096\n" \
"Maaa 148,28,88\nD 5,64,s76,0\nMaaa 144,24,88\nMaaa 136,24,84\nMaaa 140,28,84\nMaaa 132,28,80\nD 5,64,s77,1024\nMaaa 128,24,80\n" \
"Mvaa 248,24,76\nMvaa 252,28,76\nMvaa 244,28,72\nD 5,64,s78,2048\nMvaa 240,24,72\nMvaa 232,24,68\nMvaa 236,28,68\nMvaa 228,28,64\n" \
"D 5,64,s79,3072\nMvaa 224,24,64\ns_branch zgV\nzhV:\ns_bitcmp1_b32 s33, 1\ns_cbranch_scc1 zxV\nMvaa 32,0,64\nTa 96,8,8192\n" \
"s_mov_b64 s[64:65], s[84:85]\nMvaa 36,4,64\nTa 98,8,9216\ns_mov_b32 s66, 4194304\nMvaa 44,4,68\nRa 32,27,0\ns_mov_b64 s[68:69], s[86:87]\nMvaa 40,0,68\n" \
"Ta 100,20,8192\ns_mov_b32 s70, 8388608\nMvaa 48,0,72\nTa 102,20,9216\ns_mov_b32 s73, 15360\nMvaa 52,4,72\nRa 36,27,2048\ns_mov_b32 s74, 129024\n" \
"Mvaa 60,4,76\nTa 104,21,8192\ns_mov_b32 s75, 144384\nMvaa 56,0,76\nTa 106,21,9216\ns_add_u32 s76, s37, 0\nMvaa 64,0,80\nRa 40,27,4096\n" \
"s_add_u32 s77, s37, 15360\nMvaa 68,4,80\nTa 108,22,8192\ns_add_u32 s78, s37, 129024\nMvaa 76,4,84\nTa 110,22,9216\ns_add_u32 s79, s37, 144384\nMvaa 72,0,84\n" \
"Ra 44,27,6144\ns_mov_b32 s89, 31744\nMvaa 80,0,88\nTa 112,23,8192\ns_mov_b32 s90, 63488\nMvaa 84,4,88\nTa 114,23,9216\ns_mov_b32 s91, 95232\n" \
"Mvaa 92,4,92\nRa 48,27,8192\ns_mov_b32 s72, 524288\nMvaa 88,0,92\nTa 116,24,8192\nv_xor_b32 v28, 128, v14\nMvaa 152,8,92\nTa 118,24,9216\n" \
"v_mov_b32 v4, v15\nMvaa 156,12,92\nRa 52,27,10240\nMvaa 148,12,88\nTa 120,25,8192\nMvaa 144,8,88\nTa 122,25,9216\nMvaa 136,8,84\n" \
"Ra 56,27,12288\nMvaa 140,12,84\nTa 124,26,8192\nMvaa 132,12,80\nTa 126,26,9216\nMvaa 128,8,80\nRa 60,27,14336\nMvaa 120,8,76\n" \
"Mvaa 124,12,76\nMvaa 116,12,72\nMvaa 112,8,72\nMvaa 104,8,68\nMvaa 108,12,68\nMvaa 100,12,64\nMvaa 96,8,64\nMvaa 160,16,64\n" \
"s_waitcnt lgkmcnt(0)\ns_barrier\nMvaa 164,20,64\ns_add_u32 m0, s88, 131072\ns_and_b32 s10, s3, 2\nMvaa 172,20,68\nD 13,64,0,0\nMvaa 168,16,68\n" \
"s_bfe_u32 s100, s3, 0x10002\ns_lshl_b32 s10, s10, 1\nMvaa 176,16,72\ns_bfe_u32 s2, s9, 0x10010\ns_xor_b32 s99, s2, s3\nMvaa 180,20,72\ns_and_b32 s99, s99, 1\ns_lshl1_add_u32 s99, s100, s99\n" \
"Mvaa 188,20,76\nD 13,64,s73,1024\nMvaa 184,16,76\ns_bfe_u32 s101, s3, 0x20003\ns_lshl3_add_u32 s11, s99, s101\nMvaa 192,16,80\ns_add_u32 s101, s101, s10\ns_lshl3_add_u32 s10, s2, s10\n" \
"Mvaa 196,20,80\ns_bfe_u32 s2, s3, 0x20005\ns_lshl1_add_u32 s2, s2, s100\nMvaa 204,20,84\nD 13,64,s74,2048\nMvaa 200,16,84\ns_bfe_u32 s23, s9, 0x80008\ns_and_b32 s22, s9, 255\n" \
"Mvaa 208,16,88\ns_lshr_b32 s6, 4, s99\ns_lshr_b32 s25, s3, 5\nMvaa 212,20,88\ns_lshl_b32 s4, s25, 8\ns_lshl_b32 s18, s25, 21\nMvaa 220,20,92\ns_branch 99f\n" \
".p2align 12\nzp23:\ns_setpc_b64 s[98:99]\n99:\nD 13,64,s75,3072\nMvaa 216,16,92\ns_lshl3_add_u32 s25, s25, s101\ns_lshl_b32 s26, s25, 18\n" \
"Maaa 152,24,92\ns_lshl_b32 s27, s25, 7\ns_add_u32 s4, s4, 16384\nMaaa 156,28,92\ns_add_u32 m0, s88, 135168\ns_xor_b32 s25, s11, s10\nMaaa 148,28,88\nD 13,64,s76,0\n" \
"Maaa 144,24,88\ns_lshl3_add_u32 s31, s25, s4\ns_lshl_b32 s5, s2, 7\nMaaa 136,24,84\ns_add_u32 s5, s5, 8192\ns_lshr_b32 s25, s32, 1\nMaaa 140,28,84\ns_add_u32 s18, s18, s25\n" \
"s_add_u32 s18, s18, 54854656\nMaaa 132,28,80\nD 13,64,s77,1024\nMaaa 128,24,80\ns_lshl_b32 s19, s2, 19\ns_add_u32 s19, s19, s25\nMvaa 248,24,76\ns_add_u32 s19, s19, 48563200\n" \
"s_lshr_b32 s25, s32, 7\nMvaa 252,28,76\ns_add_u32 s27, s27, s25\ns_lshl3_add_u32 s27, s99, s27\nMvaa 244,28,72\nD 13,64,s78,2048\nMvaa 240,24,72\ns_add_u32 s27, s27, 32768\n" \
"s_lshl1_add_u32 s26, s32, s26\nMvaa 232,24,68\ns_add_u32 s26, s26, 71631872\ns_lshl_b32 s28, s99, 15\nMvaa 236,28,68\ns_add_u32 s24, s26, s28\ns_sub_u32 s28, 0x10000, s28\nMvaa 228,28,64\n" \
"D 13,64,s79,3072\nMvaa 224,24,64\ns_lshl_b32 s25, s100, 17\ns_add_u32 s26, s26, s25\nMvaa 32,32,96\ns_cmp_eq_u32 s99, 0\ns_cselect_b32 s21, 128, 0\nMvaa 36,36,96\n" \
"s_add_u32 m0, s32, 81920\ns_lshl_b32 s25, s21, 10\nMvaa 44,36,100\nD 14,68,0,0\nMvaa 40,32,100\ns_add_u32 s24, s24, s25\ns_add_u32 s25, s24, 0x20000\nMvaa 48,32,104\n" \
"s_lshl_b32 s101, s101, 8\ns_and_b32 s20, s32, 0x2000\nMvaa 52,36,104\ns_lshl3_add_u32 s20, s20, s101\ns_and_b32 s99, s32, 0x1000\nMvaa 60,36,108\nD 14,68,s89,1024\nMvaa 56,32,108\n" \
"s_lshl2_add_u32 s20, s99, s20\ns_lshl4_add_u32 s101, s32, s101\nMvaa 64,32,112\ns_lshl_b32 s2, s2, 20\ns_add_u32 s2, s2, s101\nMvaa 68,36,112\ns_lshl_b32 s100, s100, 7\ns_add_u32 s101, s101, s100\n" \
"Mvaa 76,36,116\nD 28,68,s90,2048\nMvaa 72,32,116\ns_add_u32 s101, s101, 2048\ns_lshl_b32 s29, s42, 17\nMvaa 80,32,120\ns_lshl_b32 s30, s43, 17\ns_add_u32 s29, s29, s101\n" \
"Mvaa 84,36,120\ns_add_u32 s30, s30, s101\ns_add_u32 s101, s2, 524288\nMvaa 92,36,124\nD 28,68,s91,3072\nMvaa 88,32,124\ns_cmp_eq_u32 s23, 2\ns_cselect_b32 s29, s2, s29\n" \
"s_cselect_b32 s30, s101, s30\ns_cselect_b32 s7, 0xffff, 0\ns_cselect_b32 s9, 0xff, 0\nMvaa 152,40,124\ns_and_b32 s101, s10, 8\ns_lshl_b32 s9, s9, s101\nMvaa 156,44,124\ns_add_u32 m0, s88, 0\n" \
"v_add_u32 v13, 128, v13\nMvaa 148,44,120\nD 13,64,0,0\nMvaa 144,40,120\ns_mov_b32 s2, zqS-zq0-8192\nMvaa 136,40,116\ns_cmp_eq_u32 s23, 1\ns_cselect_b32 s2, zqR-zq0-8192, s2\n" \
"Mvaa 140,44,116\ns_cmp_eq_u32 s23, 0\ns_cselect_b32 s2, zqT-zq0-8192, s2\nMvaa 132,44,112\nD 13,64,s73,1024\nMvaa 128,40,112\ns_cmp_lt_u32 s32, 0x2000\ns_cselect_b32 s2, 0, s2\n" \
"Mvaa 120,40,108\ns_add_u32 s2, s2, s32\nMvaa 124,44,108\nMvaa 116,44,104\nD 13,64,s74,2048\nMvaa 112,40,104\nMvaa 104,40,100\nMvaa 108,44,100\n" \
"Mvaa 100,44,96\nD 13,64,s75,3072\nMvaa 96,40,96\ns_waitcnt vmcnt(17)\ns_barrier\nMvaa 160,48,96\nTa 64,8,32768\nMvaa 164,52,96\n" \
"Ta 66,8,33792\nMvaa 172,52,100\nRa 0,7,32768\nMvaa 168,48,100\nTa 68,20,32768\nMvaa 176,48,104\nTa 70,20,33792\nMvaa 180,52,104\n" \
"Ra 4,7,34816\nMvaa 188,52,108\nTa 72,21,32768\nMvaa 184,48,108\nTa 74,21,33792\nMvaa 192,48,112\nRa 8,7,36864\nMvaa 196,52,112\n" \
"Ta 76,22,32768\nMvaa 204,52,116\nTa 78,22,33792\nMvaa 200,48,116\nRa 12,7,38912\nMvaa 208,48,120\nTa 80,23,32768\nMvaa 212,52,120\n" \
"Ta 82,23,33792\nMvaa 220,52,124\nRa 16,7,40960\nMvaa 216,48,124\nTa 84,24,32768\nMaaa 152,56,124\nTa 86,24,33792\nMaaa 156,60,124\n" \
"Ra 20,7,43008\nMaaa 148,60,120\nTa 88,25,32768\nMaaa 144,56,120\nTa 90,25,33792\nMaaa 136,56,116\nRa 24,7,45056\nMaaa 140,60,116\n" \
"Ta 92,26,32768\nMaaa 132,60,112\nTa 94,26,33792\nMaaa 128,56,112\nRa 28,7,47104\nMvaa 248,56,108\nMvaa 252,60,108\nMvaa 244,60,104\n" \
"Mvaa 240,56,104\nMvaa 232,56,100\nMvaa 236,60,100\nMvaa 228,60,96\nMvaa 224,56,96\ns_waitcnt lgkmcnt(0)\nMvaa 32,0,64\nTa 96,8,40960\n" \
"Mvaa 36,4,64\nTa 98,8,41984\nMvaa 44,4,68\nRa 32,27,32768\nMvaa 40,0,68\nTa 100,20,40960\nMvaa 48,0,72\nTa 102,20,41984\n" \
"Mvaa 52,4,72\nRa 36,27,34816\nMvaa 60,4,76\nTa 104,21,40960\nMvaa 56,0,76\nTa 106,21,41984\nMvaa 64,0,80\nRa 40,27,36864\n" \
"Mvaa 68,4,80\nTa 108,22,40960\nMvaa 76,4,84\nTa 110,22,41984\nMvaa 72,0,84\nRa 44,27,38912\nMvaa 80,0,88\nTa 112,23,40960\n" \
"Mvaa 84,4,88\nTa 114,23,41984\nMvaa 92,4,92\nRa 48,27,40960\nMvaa 88,0,92\nTa 116,24,40960\nMvaa 152,8,92\nTa 118,24,41984\n" \
"Mvaa 156,12,92\nRa 52,27,43008\nMvaa 148,12,88\nTa 120,25,40960\nMvaa 144,8,88\nTa 122,25,41984\nMvaa 136,8,84\nRa 56,27,45056\n" \
"Mvaa 140,12,84\nTa 124,26,40960\nMvaa 132,12,80\nTa 126,26,41984\nMvaa 128,8,80\nRa 60,27,47104\nMvaa 120,8,76\ns_and_b32 s96, s32, 0x1000\n" \
"s_lshl_b32 s97, s96, 1\ns_lshl_b32 s96, s96, 2\nMvaa 124,12,76\nv_add_u32 v16, s97, v7\nv_subrev_u32 v20, s96, v8\nv_xor_b32 v17, 64, v16\nMvaa 116,12,72\nv_add_u32 v18, 0x20000, v16\n" \
"v_add_u32 v19, 0x20000, v17\nv_xor_b32 v21, 32, v20\nMvaa 112,8,72\nv_xor_b32 v22, 64, v20\nv_xor_b32 v23, 96, v20\nv_xor_b32 v24, 128, v20\nMvaa 104,8,68\nv_xor_b32 v25, 160, v20\n" \
"v_xor_b32 v26, 192, v20\nv_xor_b32 v27, 224, v20\nMvaa 108,12,68\nMvaa 100,12,64\nMvaa 96,8,64\nMvaa 160,16,64\ns_waitcnt vmcnt(4) lgkmcnt(0)\ns_barrier\n" \
"Mvaa 164,20,64\ns_add_u32 m0, s88, 4096\nMvaa 172,20,68\nD 13,64,s76,0\nMvaa 168,16,68\nTa 160,20,24576\nMvaa 176,16,72\nTa 162,20,25600\n" \
"Mvaa 180,20,72\nD 13,64,s77,1024\nMvaa 188,20,76\nTa 164,21,24576\nMvaa 184,16,76\nTa 166,21,25600\nMvaa 192,16,80\nD 13,64,s78,2048\n" \
"Mvaa 196,20,80\nTa 168,22,24576\nMvaa 204,20,84\nTa 170,22,25600\nMvaa 200,16,84\nD 13,64,s79,3072\nMvaa 208,16,88\nTa 172,23,24576\n" \
"Mvaa 212,20,88\ns_add_u32 m0, s32, 65536\nv_add_u32 v14, s72, v14\nv_add_u32 v28, s72, v28\nMvaa 220,20,92\nD 14,68,0,0\nMvaa 216,16,92\nTa 174,23,25600\n" \
"Maaa 152,24,92\nTa 176,24,24576\nMaaa 156,28,92\nD 14,68,s89,1024\nMaaa 148,28,88\nTa 178,24,25600\nMaaa 144,24,88\nTa 180,25,24576\n" \
"Maaa 136,24,84\nD 28,68,s90,2048\nMaaa 140,28,84\nTa 182,25,25600\nMaaa 132,28,80\nTa 184,26,24576\nMaaa 128,24,80\nD 28,68,s91,3072\n" \
"Mvaa 248,24,76\nTa 186,26,25600\nMvaa 252,28,76\ns_add_u32 m0, s88, 32768\nv_add_u32 v13, 128, v13\nMvaa 244,28,72\nD 13,64,0,0\nMvaa 240,24,72\n" \
"Ta 188,27,24576\nMvaa 232,24,68\nTa 190,27,25600\nMvaa 236,28,68\nD 13,64,s73,1024\nMvaa 228,28,64\nMvaa 224,24,64\nMvaa 32,32,96\n" \
"D 13,64,s74,2048\nMvaa 36,36,96\nTa 64,20,16384\nMvaa 40,32,100\nTa 66,20,17408\nMvaa 44,36,100\nD 13,64,s75,3072\nMvaa 48,32,104\n" \
"Mvaa 52,36,104\ns_add_u32 m0, s88, 36864\nMvaa 56,32,108\nD 13,64,s76,0\nMvaa 60,36,108\nRa 0,18,0\nMvaa 64,32,112\nMvaa 68,36,112\n" \
"D 13,64,s77,1024\nMvaa 72,32,116\nRa 16,19,0\nC 32,32,33\nC 33,34,35\nMvaa 76,36,116\nC 34,36,37\nC 35,38,39\n" \
"C 36,40,41\nMvaa 80,32,120\nD 13,64,s78,2048\nC 37,42,43\nMvaa 84,36,120\nTa 68,21,16384\nC 38,44,45\nC 39,46,47\n" \
"Mvaa 88,32,124\nC 40,48,49\nC 41,50,51\nC 42,52,53\nMvaa 92,36,124\nD 13,64,s79,3072\nC 43,54,55\nMvaa 96,40,96\n" \
"Ta 70,21,17408\nC 44,56,57\nC 45,58,59\nMvaa 100,44,96\ns_add_u32 m0, s32, 98304\nv_add_u32 v14, s72, v14\nv_add_u32 v28, s72, v28\nMvaa 104,40,100\n" \
"D 14,68,0,0\nC 46,60,61\nMvaa 108,44,100\nC 47,62,63\nC 48,64,65\nC 49,66,67\nMvaa 112,40,104\nTa 72,22,16384\n" \
"C 50,68,69\nC 51,70,71\nMvaa 116,44,104\nD 14,68,s89,1024\nC 52,72,73\nMvaa 120,40,108\nC 53,74,75\nC 54,76,77\n" \
"C 55,78,79\nMvaa 124,44,108\nTa 74,22,17408\nC 56,80,81\nC 57,82,83\nMvaa 128,40,112\nD 28,68,s90,2048\nC 58,84,85\n" \
"Mvaa 132,44,112\nC 59,86,87\nC 60,88,89\nC 61,90,91\nMvaa 136,40,116\nTa 76,23,16384\nC 62,92,93\nC 63,94,95\n" \
"Mvaa 140,44,116\nD 28,68,s91,3072\nC 64,96,97\nMvaa 144,40,120\nC 65,98,99\nC 66,100,101\nC 67,102,103\nMvaa 148,44,120\n" \
"Ta 78,23,17408\nC 68,104,105\nC 69,106,107\nMvaa 152,40,124\nC 70,108,109\nC 71,110,111\nC 72,112,113\nMvaa 156,44,124\n" \
"Ta 80,24,16384\nC 73,114,115\nC 74,116,117\nMaaa 128,56,112\nC 75,118,119\nC 76,120,121\nC 77,122,123\nMaaa 132,60,112\n" \
"Ta 82,24,17408\nC 78,124,125\nC 79,126,127\nMaaa 136,56,116\nC 80,128,129\nC 81,130,131\nC 82,132,133\nMaaa 140,60,116\n" \
"Ta 84,25,16384\nC 83,134,135\nC 84,136,137\nMaaa 144,56,120\nC 85,138,139\nC 86,140,141\nC 87,142,143\nMaaa 148,60,120\n" \
"Ta 86,25,17408\nC 88,144,145\nC 89,146,147\nMaaa 152,56,124\nC 90,148,149\nC 91,150,151\nC 92,152,153\nMaaa 156,60,124\n" \
"Ta 88,26,16384\nC 93,154,155\nC 94,156,157\nMvaa 160,48,96\nC 95,158,159\nMvaa 164,52,96\nTa 90,26,17408\nMvaa 168,48,100\n" \
"AR 96,128\nAR 97,129\nAR 98,130\nMvaa 172,52,100\nTa 92,27,16384\nAR 99,131\nAR 100,132\nMvaa 176,48,104\n" \
"AR 101,133\nAR 102,134\nAR 103,135\nMvaa 180,52,104\nTa 94,27,17408\nC 96,96,97\nC 97,98,99\nMvaa 184,48,108\n" \
"C 98,100,101\nC 99,102,103\nAW 240,96\nMvaa 188,52,108\nRa 4,18,2048\nAW 241,97\nAW 242,98\nMvaa 192,48,112\n" \
"AW 243,99\nAR 104,136\nAR 105,137\nMvaa 196,52,112\nRa 20,19,2048\nAR 106,138\nAR 107,139\nMvaa 200,48,116\n" \
"AR 108,140\nAR 109,141\nAR 110,142\nMvaa 204,52,116\nRa 8,18,4096\nAR 111,143\nC 104,104,105\nMvaa 208,48,120\n" \
"C 105,106,107\nC 106,108,109\nC 107,110,111\nMvaa 212,52,120\nRa 24,19,4096\nAW 244,104\nAW 245,105\nMvaa 216,48,124\n" \
"AW 246,106\nAW 247,107\nAR 112,144\nMvaa 220,52,124\nRa 12,18,6144\nAR 113,145\nAR 114,146\nMvaa 224,56,96\n" \
"AR 115,147\nAR 116,148\nAR 117,149\nMvaa 228,60,96\nRa 28,19,6144\nAR 118,150\nAR 119,151\nMvaa 232,56,100\n" \
"C 112,112,113\nC 113,114,115\nC 114,116,117\nMvaa 236,60,100\nC 115,118,119\nAW 248,112\nAW 249,113\nMvaa 240,56,104\n" \
"AW 250,114\nAW 251,115\nAR 120,152\nMvaa 244,60,104\nAR 121,153\nAR 122,154\nAR 123,155\nMvaa 248,56,108\n" \
"s_branch 99f\n.p2align 12\nzp24:\ns_setpc_b64 s[98:99]\n99:\nAR 124,156\nAR 125,157\nAR 126,158\n" \
"Mvaa 252,60,108\nAR 127,159\nC 120,120,121\nC 121,122,123\ns_waitcnt lgkmcnt(6)\ns_branch zY\nzxV:\nMvaa 32,0,64\n" \
"Ta 96,8,8192\ns_mov_b64 s[64:65], s[80:81]\nMvaa 36,4,64\nTa 98,8,9216\ns_mov_b32 s66, 4194304\nMvaa 44,4,68\nRa 32,27,0\ns_mov_b64 s[68:69], s[82:83]\n" \
"Mvaa 40,0,68\nTa 100,20,8192\ns_mov_b32 s70, 6291456\nMvaa 48,0,72\nTa 102,20,9216\ns_mov_b32 s73, 15360\nMvaa 52,4,72\nRa 36,27,2048\n" \
"s_mov_b32 s74, 30720\nMvaa 60,4,76\nTa 104,21,8192\ns_mov_b32 s75, 46080\nMvaa 56,0,76\nTa 106,21,9216\ns_mov_b32 s76, 65536\nMvaa 64,0,80\n" \
"Ra 40,27,4096\ns_mov_b32 s77, 80896\nMvaa 68,4,80\nTa 108,22,8192\ns_mov_b32 s78, 96256\nMvaa 76,4,84\nTa 110,22,9216\ns_mov_b32 s79, 111616\n" \
"Mvaa 72,0,84\nRa 44,27,6144\ns_mov_b32 s89, 23552\nMvaa 80,0,88\nTa 112,23,8192\ns_mov_b32 s90, 47104\nMvaa 84,4,88\nTa 114,23,9216\n" \
"s_mov_b32 s91, 70656\nMvaa 92,4,92\nRa 48,27,8192\ns_mov_b32 s72, 393216\nMvaa 88,0,92\nTa 116,24,8192\ns_lshr_b32 s92, s32, 1\nMvaa 152,8,92\n" \
"Ta 118,24,9216\nMvaa 156,12,92\nRa 52,27,10240\nMvaa 148,12,88\nTa 120,25,8192\nMvaa 144,8,88\nTa 122,25,9216\nMvaa 136,8,84\n" \
"Ra 56,27,12288\nMvaa 140,12,84\nTa 124,26,8192\nMvaa 132,12,80\nTa 126,26,9216\nMvaa 128,8,80\nRa 60,27,14336\nMvaa 120,8,76\n" \
"Mvaa 124,12,76\nMvaa 116,12,72\nMvaa 112,8,72\nMvaa 104,8,68\nMvaa 108,12,68\nMvaa 100,12,64\nMvaa 96,8,64\nMvaa 160,16,64\n" \
"s_waitcnt lgkmcnt(0)\ns_barrier\nMvaa 164,20,64\ns_add_u32 m0, s88, 0\ns_and_b32 s10, s3, 2\nMvaa 172,20,68\nD 9,64,0,0\nMvaa 168,16,68\n" \
"s_bfe_u32 s100, s3, 0x10002\ns_lshl_b32 s10, s10, 1\nMvaa 176,16,72\ns_bfe_u32 s2, s9, 0x10010\ns_xor_b32 s99, s2, s3\nMvaa 180,20,72\ns_and_b32 s99, s99, 1\ns_lshl1_add_u32 s99, s100, s99\n" \
"Mvaa 188,20,76\nD 9,64,s73,1024\nMvaa 184,16,76\ns_bfe_u32 s101, s3, 0x20003\ns_lshl3_add_u32 s11, s99, s101\nMvaa 192,16,80\ns_add_u32 s101, s101, s10\ns_lshl3_add_u32 s10, s2, s10\n" \
"Mvaa 196,20,80\ns_bfe_u32 s2, s3, 0x20005\ns_lshl1_add_u32 s2, s2, s100\nMvaa 204,20,84\nD 9,64,s74,2048\nMvaa 200,16,84\ns_bfe_u32 s23, s9, 0x80008\ns_and_b32 s22, s9, 255\n" \
"Mvaa 208,16,88\ns_lshr_b32 s6, 4, s99\ns_lshr_b32 s25, s3, 5\nMvaa 212,20,88\ns_lshl_b32 s4, s25, 8\ns_lshl_b32 s18, s25, 21\nMvaa 220,20,92\nD 9,64,s75,3072\n" \
"Mvaa 216,16,92\ns_lshl3_add_u32 s25, s25, s101\ns_lshl_b32 s26, s25, 18\nMaaa 152,24,92\ns_lshl_b32 s27, s25, 7\ns_add_u32 s4, s4, 16384\nMaaa 156,28,92\ns_add_u32 m0, s88, 4096\n" \
"s_xor_b32 s25, s11, s10\nMaaa 148,28,88\nD 9,64,s76,0\nMaaa 144,24,88\ns_lshl3_add_u32 s31, s25, s4\ns_lshl_b32 s5, s2, 7\nMaaa 136,24,84\ns_add_u32 s5, s5, 8192\n" \
"s_lshr_b32 s25, s32, 1\nMaaa 140,28,84\ns_add_u32 s18, s18, s25\ns_add_u32 s18, s18, 54854656\nMaaa 132,28,80\nD 9,64,s77,1024\nMaaa 128,24,80\ns_lshl_b32 s19, s2, 19\n" \
"s_add_u32 s19, s19, s25\nMvaa 248,24,76\ns_add_u32 s19, s19, 48563200\ns_lshr_b32 s25, s32, 7\nMvaa 252,28,76\ns_add_u32 s27, s27, s25\ns_lshl3_add_u32 s27, s99, s27\nMvaa 244,28,72\n" \
"D 9,64,s78,2048\nMvaa 240,24,72\ns_add_u32 s27, s27, 32768\ns_lshl1_add_u32 s26, s32, s26\nMvaa 232,24,68\ns_add_u32 s26, s26, 71631872\ns_lshl_b32 s28, s99, 15\nMvaa 236,28,68\n" \
"s_add_u32 s24, s26, s28\ns_sub_u32 s28, 0x10000, s28\nMvaa 228,28,64\nD 9,64,s79,3072\nMvaa 224,24,64\ns_lshl_b32 s25, s100, 17\ns_add_u32 s26, s26, s25\nMvaa 32,32,96\n" \
"s_cmp_eq_u32 s99, 0\ns_cselect_b32 s21, 128, 0\nMvaa 36,36,96\ns_add_u32 m0, s32, 65536\ns_lshl_b32 s25, s21, 10\nMvaa 44,36,100\nD 10,68,0,0\nMvaa 40,32,100\n" \
"s_add_u32 s24, s24, s25\ns_add_u32 s25, s24, 0x20000\nMvaa 48,32,104\ns_lshl_b32 s101, s101, 8\ns_and_b32 s20, s32, 0x2000\nMvaa 52,36,104\ns_lshl3_add_u32 s20, s20, s101\ns_and_b32 s99, s32, 0x1000\n" \
"Mvaa 60,36,108\nD 10,68,s89,1024\nMvaa 56,32,108\ns_lshl2_add_u32 s20, s99, s20\ns_lshl4_add_u32 s101, s32, s101\nMvaa 64,32,112\ns_lshl_b32 s2, s2, 20\ns_add_u32 s2, s2, s101\n" \
"Mvaa 68,36,112\ns_lshl_b32 s100, s100, 7\ns_add_u32 s101, s101, s100\nMvaa 76,36,116\nD 11,68,s90,2048\nMvaa 72,32,116\ns_add_u32 s101, s101, 2048\ns_lshl_b32 s29, s42, 17\n" \
"Mvaa 80,32,120\ns_lshl_b32 s30, s43, 17\ns_add_u32 s29, s29, s101\nMvaa 84,36,120\ns_add_u32 s30, s30, s101\ns_add_u32 s101, s2, 524288\nMvaa 92,36,124\nD 11,68,s91,3072\n" \
"Mvaa 88,32,124\ns_cmp_eq_u32 s23, 2\ns_cselect_b32 s29, s2, s29\ns_cselect_b32 s30, s101, s30\ns_cselect_b32 s7, 0xffff, 0\ns_cselect_b32 s9, 0xff, 0\nMvaa 152,40,124\ns_and_b32 s101, s10, 8\n" \
"s_lshl_b32 s9, s9, s101\nMvaa 156,44,124\ns_add_u32 m0, s92, 81920\ns_mov_b32 s2, zqS-zq0-8192\nMvaa 148,44,120\nD 12,68,0,0\nMvaa 144,40,120\ns_cmp_eq_u32 s23, 1\n" \
"s_cselect_b32 s2, zqR-zq0-8192, s2\nMvaa 136,40,116\ns_cmp_eq_u32 s23, 0\ns_cselect_b32 s2, zqT-zq0-8192, s2\nMvaa 140,44,116\ns_cmp_lt_u32 s32, 0x2000\ns_cselect_b32 s2, 0, s2\nMvaa 132,44,112\n" \
"D 12,68,s89,1024\nMvaa 128,40,112\ns_add_u32 s2, s2, s32\nMvaa 120,40,108\nMvaa 124,44,108\nMvaa 116,44,104\nMvaa 112,40,104\nMvaa 104,40,100\n" \
"Mvaa 108,44,100\nMvaa 100,44,96\nbuffer_load_dword v30, v4, s[52:55], 0 offen sc1\nMvaa 96,40,96\ns_waitcnt vmcnt(16)\ns_barrier\nMvaa 160,48,96\nTa 64,8,32768\n" \
"v_add_u32 v9, 128, v9\nMvaa 164,52,96\nTa 66,8,33792\nv_add_u32 v10, s72, v10\nMvaa 172,52,100\nRa 0,7,32768\nv_add_u32 v11, s72, v11\nMvaa 168,48,100\n" \
"Ta 68,20,32768\nv_add_u32 v12, s72, v12\nMvaa 176,48,104\nv_add_u32 v4, s44, v4\nTa 70,20,33792\nMvaa 180,52,104\nRa 4,7,34816\nMvaa 188,52,108\n" \
"Ta 72,21,32768\nMvaa 184,48,108\nTa 74,21,33792\nMvaa 192,48,112\nRa 8,7,36864\nMvaa 196,52,112\nTa 76,22,32768\nMvaa 204,52,116\n" \
"Ta 78,22,33792\nMvaa 200,48,116\nRa 12,7,38912\nMvaa 208,48,120\nTa 80,23,32768\nMvaa 212,52,120\nTa 82,23,33792\nMvaa 220,52,124\n" \
"Ra 16,7,40960\nMvaa 216,48,124\nTa 84,24,32768\nMaaa 152,56,124\nTa 86,24,33792\nMaaa 156,60,124\nRa 20,7,43008\nMaaa 148,60,120\n" \
"Ta 88,25,32768\nMaaa 144,56,120\nTa 90,25,33792\nMaaa 136,56,116\nRa 24,7,45056\nMaaa 140,60,116\nTa 92,26,32768\nMaaa 132,60,112\n" \
"Ta 94,26,33792\nMaaa 128,56,112\nRa 28,7,47104\nMvaa 248,56,108\nMvaa 252,60,108\nMvaa 244,60,104\nMvaa 240,56,104\nMvaa 232,56,100\n" \
"Mvaa 236,60,100\nMvaa 228,60,96\nMvaa 224,56,96\ns_waitcnt lgkmcnt(0)\nMvaa 32,0,64\nTa 96,8,40960\nMvaa 36,4,64\nTa 98,8,41984\n" \
"Mvaa 44,4,68\nRa 32,27,32768\nMvaa 40,0,68\nTa 100,20,40960\nMvaa 48,0,72\nTa 102,20,41984\nMvaa 52,4,72\nRa 36,27,34816\n" \
"Mvaa 60,4,76\nTa 104,21,40960\nMvaa 56,0,76\nTa 106,21,41984\nMvaa 64,0,80\nRa 40,27,36864\nMvaa 68,4,80\nTa 108,22,40960\n" \
"Mvaa 76,4,84\nTa 110,22,41984\nMvaa 72,0,84\nRa 44,27,38912\nMvaa 80,0,88\nTa 112,23,40960\nMvaa 84,4,88\nTa 114,23,41984\n" \
"Mvaa 92,4,92\nRa 48,27,40960\nMvaa 88,0,92\nTa 116,24,40960\nMvaa 152,8,92\nTa 118,24,41984\nMvaa 156,12,92\nRa 52,27,43008\n" \
"Mvaa 148,12,88\nTa 120,25,40960\nMvaa 144,8,88\nTa 122,25,41984\nMvaa 136,8,84\nRa 56,27,45056\nMvaa 140,12,84\nTa 124,26,40960\n" \
"Mvaa 132,12,80\nTa 126,26,41984\nMvaa 128,8,80\nRa 60,27,47104\nMvaa 120,8,76\ns_and_b32 s96, s32, 0x1000\ns_lshr_b32 s97, s96, 6\nMvaa 124,12,76\n" \
"s_lshl_b32 s96, s96, 2\nv_subrev_u32 v20, s96, v8\nMvaa 116,12,72\nv_xor_b32 v20, s97, v20\nv_xor_b32 v21, 32, v20\nMvaa 112,8,72\nv_xor_b32 v22, 0x80, v20\nv_xor_b32 v23, 0xa0, v20\n" \
"Mvaa 104,8,68\nMvaa 108,12,68\nMvaa 100,12,64\nMvaa 96,8,64\nMvaa 160,16,64\ns_waitcnt lgkmcnt(0)\ns_barrier\nMvaa 164,20,64\n" \
"s_add_u32 m0, s88, 32768\nMvaa 172,20,68\nD 9,64,0,0\nMvaa 168,16,68\nMvaa 176,16,72\nMvaa 180,20,72\nMvaa 188,20,76\nD 9,64,s73,1024\n" \
"Mvaa 184,16,76\nMvaa 192,16,80\nMvaa 196,20,80\nMvaa 204,20,84\nD 9,64,s74,2048\nMvaa 200,16,84\nMvaa 208,16,88\nMvaa 212,20,88\n" \
"Mvaa 220,20,92\nD 9,64,s75,3072\nMvaa 216,16,92\nMaaa 152,24,92\nMaaa 156,28,92\ns_add_u32 m0, s88, 36864\nMaaa 148,28,88\nD 9,64,s76,0\n" \
"Maaa 144,24,88\nMaaa 136,24,84\nMaaa 140,28,84\nMaaa 132,28,80\nD 9,64,s77,1024\nMaaa 128,24,80\nMvaa 248,24,76\nMvaa 252,28,76\n" \
"Mvaa 244,28,72\nD 9,64,s78,2048\nMvaa 240,24,72\nMvaa 232,24,68\nMvaa 236,28,68\nMvaa 228,28,64\nD 9,64,s79,3072\nMvaa 224,24,64\n" \
"Mvaa 32,32,96\nMvaa 36,36,96\ns_add_u32 m0, s32, 98304\nMvaa 40,32,100\nD 10,68,0,0\nMvaa 44,36,100\nMvaa 48,32,104\nMvaa 52,36,104\n" \
"Mvaa 56,32,108\nD 10,68,s89,1024\nMvaa 60,36,108\nMvaa 64,32,112\nMvaa 68,36,112\nMvaa 72,32,116\nD 11,68,s90,2048\nC 32,32,33\n" \
"Mvaa 76,36,116\nC 33,34,35\nC 34,36,37\nC 35,38,39\nMvaa 80,32,120\nC 36,40,41\nC 37,42,43\nC 38,44,45\n" \
"Mvaa 84,36,120\nC 39,46,47\nC 40,48,49\nC 41,50,51\nMvaa 88,32,124\nD 11,68,s91,3072\nC 42,52,53\nMvaa 92,36,124\n" \
"C 43,54,55\nC 44,56,57\nC 45,58,59\nMvaa 96,40,96\nC 46,60,61\nC 47,62,63\nMvaa 100,44,96\ns_add_u32 m0, s92, 114688\n" \
"Mvaa 104,40,100\nD 12,68,0,0\nC 48,64,65\nMvaa 108,44,100\nC 49,66,67\nC 50,68,69\nC 51,70,71\nMvaa 112,40,104\n" \
"C 52,72,73\nC 53,74,75\nC 54,76,77\nMvaa 116,44,104\nC 55,78,79\nC 56,80,81\nC 57,82,83\nMvaa 120,40,108\n" \
"D 12,68,s89,1024\nC 58,84,85\nMvaa 124,44,108\nC 59,86,87\nC 60,88,89\nC 61,90,91\nMvaa 128,40,112\nC 62,92,93\n" \
"C 63,94,95\nMvaa 132,44,112\nMvaa 136,40,116\nC 96,96,97\nC 97,98,99\nC 98,100,101\nMvaa 140,44,116\nC 99,102,103\n" \
"AW 160,96\nAW 161,97\nMvaa 144,40,120\nAW 162,98\nAW 163,99\nC 104,104,105\nMvaa 148,44,120\nC 105,106,107\n" \
"C 106,108,109\nC 107,110,111\nMvaa 152,40,124\nbuffer_load_dword v30, v4, s[52:55], 0 offen sc1\nAW 164,104\nMvaa 156,44,124\ns_waitcnt vmcnt(16)\ns_barrier\n" \
"AW 165,105\nMvaa 160,48,96\nTa 64,20,0\nv_add_u32 v9, 128, v9\nAW 166,106\nMvaa 164,52,96\nTa 66,20,1024\nv_add_u32 v10, s72, v10\n" \
"AW 167,107\nMvaa 168,48,100\nRa 0,7,0\nv_add_u32 v11, s72, v11\nC 112,112,113\nMvaa 172,52,100\nTa 68,21,0\nv_add_u32 v12, s72, v12\n" \
"C 113,114,115\nMvaa 176,48,104\nv_add_u32 v4, s44, v4\nTa 70,21,1024\nC 114,116,117\nMvaa 180,52,104\nRa 4,7,2048\nC 115,118,119\n" \
"AW 168,112\nMvaa 184,48,108\nTa 72,22,0\nAW 169,113\nAW 170,114\nMvaa 188,52,108\nTa 74,22,1024\nAW 171,115\n" \
"C 120,120,121\nMvaa 192,48,112\nRa 8,7,4096\nC 121,122,123\nC 122,124,125\nMvaa 196,52,112\nTa 76,23,0\nC 123,126,127\n" \
"AW 172,120\nMvaa 200,48,116\nTa 78,23,1024\nAW 173,121\nAW 174,122\nMvaa 204,52,116\ns_branch 99f\n.p2align 12\n" \
"zp25:\ns_setpc_b64 s[98:99]\n99:\nRa 12,7,6144\nAW 175,123\nC 128,128,129\nMvaa 208,48,120\nTa 80,20,16384\n" \
"C 129,130,131\nC 130,132,133\nMvaa 212,52,120\nTa 82,20,17408\nC 131,134,135\nAW 176,128\nMvaa 216,48,124\nRa 16,7,8192\n" \
"AW 177,129\nAW 178,130\nMvaa 220,52,124\nTa 84,21,16384\nAW 179,131\nC 136,136,137\nMvaa 224,56,96\nTa 86,21,17408\n" \
"C 137,138,139\nC 138,140,141\nMvaa 228,60,96\nRa 20,7,10240\nC 139,142,143\nAW 180,136\nMvaa 232,56,100\nRa 24,7,12288\n" \
"AW 181,137\nAW 182,138\nMvaa 236,60,100\nRa 28,7,14336\nAW 183,139\nC 144,144,145\nMvaa 240,56,104\nC 145,146,147\n" \
"C 146,148,149\nC 147,150,151\nMvaa 244,60,104\nAW 184,144\nAW 185,145\nAW 186,146\nMvaa 248,56,108\nAW 187,147\n" \
"C 152,152,153\nC 153,154,155\nMvaa 252,60,108\nC 154,156,157\nC 155,158,159\nAW 188,152\nMaaa 128,56,112\nAW 189,153\n" \
"AW 190,154\nAW 191,155\nMaaa 132,60,112\nC 160,160,161\nC 161,162,163\nC 162,164,165\nMaaa 136,56,116\nC 163,166,167\n" \
"AW 192,160\nAW 193,161\nMaaa 140,60,116\nAW 194,162\nAW 195,163\nC 168,168,169\nMaaa 144,56,120\nC 169,170,171\n" \
"C 170,172,173\nC 171,174,175\nMaaa 148,60,120\nAW 196,168\nAW 197,169\nAW 198,170\nMaaa 152,56,124\nAW 199,171\n" \
"C 176,176,177\nC 177,178,179\nMaaa 156,60,124\nC 178,180,181\nC 179,182,183\nAW 200,176\ns_waitcnt lgkmcnt(0)\ns_branch zA\n" \
"zxK:\nMvaa 32,64,0\nTa 96,8,8192\ns_mov_b64 s[64:65], s[80:81]\nMvaa 64,64,4\nTa 98,8,9216\ns_mov_b32 s66, 4194304\nMvaa 68,68,4\n" \
"Ra 32,27,0\ns_mov_b64 s[68:69], s[82:83]\nMvaa 36,68,0\nTa 100,20,8192\ns_mov_b32 s70, 6291456\nMvaa 40,72,0\nTa 102,20,9216\ns_mov_b32 s73, 15360\n" \
"Mvaa 72,72,4\nRa 36,27,2048\ns_mov_b32 s74, 30720\nMvaa 76,76,4\nTa 104,21,8192\ns_mov_b32 s75, 46080\nMvaa 44,76,0\nTa 106,21,9216\n" \
"s_mov_b32 s76, 65536\nMvaa 48,80,0\nRa 40,27,4096\ns_mov_b32 s77, 80896\nMvaa 80,80,4\nTa 108,22,8192\ns_mov_b32 s78, 96256\nMvaa 84,84,4\n" \
"Ta 110,22,9216\ns_mov_b32 s79, 111616\nMvaa 52,84,0\nRa 44,27,6144\ns_mov_b32 s89, 23552\nMvaa 56,88,0\nTa 112,23,8192\ns_mov_b32 s90, 47104\n" \
"Mvaa 88,88,4\nTa 114,23,9216\ns_mov_b32 s91, 70656\nMvaa 92,92,4\nRa 48,27,8192\ns_mov_b32 s72, 393216\nMvaa 60,92,0\nTa 116,24,8192\n" \
"s_lshr_b32 s92, s32, 1\nMvaa 124,92,8\nTa 118,24,9216\nMvaa 156,92,12\nRa 52,27,10240\nMvaa 152,88,12\nTa 120,25,8192\nMvaa 120,88,8\n" \
"Ta 122,25,9216\nMvaa 116,84,8\nRa 56,27,12288\nMvaa 148,84,12\nTa 124,26,8192\nMvaa 144,80,12\nTa 126,26,9216\nMvaa 112,80,8\n" \
"Ra 60,27,14336\nMvaa 108,76,8\nMvaa 140,76,12\nMvaa 136,72,12\nMvaa 104,72,8\nMvaa 100,68,8\nMvaa 132,68,12\nMvaa 128,64,12\n" \
"Mvaa 96,64,8\nMvaa 160,64,16\ns_waitcnt lgkmcnt(0)\ns_barrier\nMvaa 192,64,20\ns_add_u32 m0, s88, 0\ns_and_b32 s10, s3, 2\nMvaa 196,68,20\n" \
"D 9,64,0,0\nMvaa 164,68,16\ns_bfe_u32 s100, s3, 0x10002\ns_lshl_b32 s10, s10, 1\nMvaa 168,72,16\ns_bfe_u32 s2, s9, 0x10010\ns_xor_b32 s99, s2, s3\nMvaa 200,72,20\n" \
"s_and_b32 s99, s99, 1\ns_lshl1_add_u32 s99, s100, s99\nMvaa 204,76,20\nD 9,64,s73,1024\nMvaa 172,76,16\ns_bfe_u32 s101, s3, 0x20003\ns_lshl3_add_u32 s11, s99, s101\nMvaa 176,80,16\n" \
"s_add_u32 s101, s101, s10\ns_lshl3_add_u32 s10, s2, s10\nMvaa 208,80,20\ns_bfe_u32 s2, s3, 0x20005\ns_lshl1_add_u32 s2, s2, s100\nMvaa 212,84,20\nD 9,64,s74,2048\nMvaa 180,84,16\n" \
"s_bfe_u32 s23, s9, 0x80008\ns_and_b32 s22, s9, 255\nMvaa 184,88,16\ns_lshr_b32 s6, 4, s99\ns_lshr_b32 s25, s3, 5\nMvaa 216,88,20\ns_lshl_b32 s4, s25, 8\ns_lshl_b32 s18, s25, 21\n" \
"Mvaa 220,92,20\nD 9,64,s75,3072\nMvaa 188,92,16\ns_lshl3_add_u32 s25, s25, s101\ns_lshl_b32 s26, s25, 18\nMvaa 252,92,24\ns_lshl_b32 s27, s25, 7\ns_add_u32 s4, s4, 16384\n" \
"Maaa 156,92,28\ns_add_u32 m0, s88, 4096\ns_xor_b32 s25, s11, s10\nMaaa 152,88,28\nD 9,64,s76,0\nMvaa 248,88,24\ns_lshl3_add_u32 s31, s25, s4\ns_lshl_b32 s5, s2, 7\n" \
"Mvaa 244,84,24\ns_add_u32 s5, s5, 8192\ns_lshr_b32 s25, s32, 1\nMaaa 148,84,28\ns_add_u32 s18, s18, s25\ns_add_u32 s18, s18, 54854656\nMaaa 144,80,28\nD 9,64,s77,1024\n" \
"Mvaa 240,80,24\ns_lshl_b32 s19, s2, 19\ns_add_u32 s19, s19, s25\nMvaa 236,76,24\ns_add_u32 s19, s19, 48563200\ns_lshr_b32 s25, s32, 7\nMaaa 140,76,28\ns_add_u32 s27, s27, s25\n" \
"s_lshl3_add_u32 s27, s99, s27\nMaaa 136,72,28\nD 9,64,s78,2048\nMvaa 232,72,24\ns_add_u32 s27, s27, 32768\ns_lshl1_add_u32 s26, s32, s26\nMvaa 228,68,24\ns_add_u32 s26, s26, 71631872\n" \
"s_lshl_b32 s28, s99, 15\nMaaa 132,68,28\ns_add_u32 s24, s26, s28\ns_sub_u32 s28, 0x10000, s28\nMaaa 128,64,28\nD 9,64,s79,3072\nMvaa 224,64,24\ns_lshl_b32 s25, s100, 17\n" \
"s_add_u32 s26, s26, s25\nMvaa 32,96,32\ns_cmp_eq_u32 s99, 0\ns_cselect_b32 s21, 128, 0\nMvaa 64,96,36\ns_add_u32 m0, s32, 65536\ns_lshl_b32 s25, s21, 10\nMvaa 68,100,36\n" \
"D 10,68,0,0\nMvaa 36,100,32\ns_add_u32 s24, s24, s25\ns_add_u32 s25, s24, 0x20000\nMvaa 40,104,32\ns_lshl_b32 s101, s101, 8\ns_and_b32 s20, s32, 0x2000\nMvaa 72,104,36\n" \
"s_lshl3_add_u32 s20, s20, s101\ns_and_b32 s99, s32, 0x1000\nMvaa 76,108,36\nD 10,68,s89,1024\nMvaa 44,108,32\ns_lshl2_add_u32 s20, s99, s20\ns_lshl4_add_u32 s101, s32, s101\nMvaa 48,112,32\n" \
"s_lshl_b32 s2, s2, 20\ns_add_u32 s2, s2, s101\nMvaa 80,112,36\ns_lshl_b32 s100, s100, 7\ns_add_u32 s101, s101, s100\nMvaa 84,116,36\nD 11,68,s90,2048\nMvaa 52,116,32\n" \
"s_add_u32 s101, s101, 2048\ns_lshl_b32 s29, s42, 17\nMvaa 56,120,32\ns_lshl_b32 s30, s43, 17\ns_add_u32 s29, s29, s101\nMvaa 88,120,36\ns_add_u32 s30, s30, s101\ns_add_u32 s101, s2, 524288\n" \
"Mvaa 92,124,36\nD 11,68,s91,3072\nMvaa 60,124,32\ns_cmp_eq_u32 s23, 2\ns_cselect_b32 s29, s2, s29\ns_cselect_b32 s30, s101, s30\ns_cselect_b32 s7, 0xffff, 0\ns_cselect_b32 s9, 0xff, 0\n" \
"Mvaa 124,124,40\ns_and_b32 s101, s10, 8\ns_lshl_b32 s9, s9, s101\nMvaa 156,124,44\ns_add_u32 m0, s92, 81920\ns_mov_b32 s2, zqS-zq0-8192\nMvaa 152,120,44\nD 12,68,0,0\n" \
"Mvaa 120,120,40\ns_cmp_eq_u32 s23, 1\ns_cselect_b32 s2, zqR-zq0-8192, s2\nMvaa 116,116,40\ns_cmp_eq_u32 s23, 0\ns_cselect_b32 s2, zqT-zq0-8192, s2\nMvaa 148,116,44\ns_cmp_lt_u32 s32, 0x2000\n" \
"s_cselect_b32 s2, 0, s2\nMvaa 144,112,44\nD 12,68,s89,1024\nMvaa 112,112,40\ns_add_u32 s2, s2, s32\nMvaa 108,108,40\nMvaa 140,108,44\nMvaa 136,104,44\n" \
"Mvaa 104,104,40\nMvaa 100,100,40\nMvaa 132,100,44\nMvaa 128,96,44\nbuffer_load_dword v30, v4, s[52:55], 0 offen sc1\nMvaa 96,96,40\ns_waitcnt vmcnt(16)\ns_barrier\n" \
"Mvaa 160,96,48\nTa 64,8,32768\nv_add_u32 v9, 128, v9\nMvaa 192,96,52\nTa 66,8,33792\nv_add_u32 v10, s72, v10\nMvaa 196,100,52\nRa 0,7,32768\n" \
"v_add_u32 v11, s72, v11\nMvaa 164,100,48\nTa 68,20,32768\nv_add_u32 v12, s72, v12\nMvaa 168,104,48\nv_add_u32 v4, s44, v4\nTa 70,20,33792\nMvaa 200,104,52\n" \
"Ra 4,7,34816\nMvaa 204,108,52\nTa 72,21,32768\nMvaa 172,108,48\nTa 74,21,33792\nMvaa 176,112,48\nRa 8,7,36864\nMvaa 208,112,52\n" \
"Ta 76,22,32768\nMvaa 212,116,52\nTa 78,22,33792\nMvaa 180,116,48\nRa 12,7,38912\nMvaa 184,120,48\nTa 80,23,32768\nMvaa 216,120,52\n" \
"Ta 82,23,33792\nMvaa 220,124,52\nRa 16,7,40960\nMvaa 188,124,48\nTa 84,24,32768\nMvaa 252,124,56\nTa 86,24,33792\nMaaa 156,124,60\n" \
"Ra 20,7,43008\nMaaa 152,120,60\nTa 88,25,32768\nMvaa 248,120,56\nTa 90,25,33792\nMvaa 244,116,56\nRa 24,7,45056\nMaaa 148,116,60\n" \
"Ta 92,26,32768\nMaaa 144,112,60\nTa 94,26,33792\nMvaa 240,112,56\nRa 28,7,47104\nMvaa 236,108,56\nMaaa 140,108,60\nMaaa 136,104,60\n" \
"Mvaa 232,104,56\nMvaa 228,100,56\nMaaa 132,100,60\nMaaa 128,96,60\nMvaa 224,96,56\ns_waitcnt lgkmcnt(0)\nMvaa 32,64,0\nTa 96,8,40960\n" \
"Mvaa 64,64,4\nTa 98,8,41984\nMvaa 68,68,4\nRa 32,27,32768\nMvaa 36,68,0\nTa 100,20,40960\nMvaa 40,72,0\nTa 102,20,41984\n" \
"Mvaa 72,72,4\nRa 36,27,34816\nMvaa 76,76,4\nTa 104,21,40960\nMvaa 44,76,0\nTa 106,21,41984\nMvaa 48,80,0\nRa 40,27,36864\n" \
"Mvaa 80,80,4\nTa 108,22,40960\nMvaa 84,84,4\nTa 110,22,41984\nMvaa 52,84,0\nRa 44,27,38912\nMvaa 56,88,0\nTa 112,23,40960\n" \
"Mvaa 88,88,4\nTa 114,23,41984\nMvaa 92,92,4\nRa 48,27,40960\nMvaa 60,92,0\nTa 116,24,40960\nMvaa 124,92,8\nTa 118,24,41984\n" \
"Mvaa 156,92,12\nRa 52,27,43008\nMvaa 152,88,12\nTa 120,25,40960\nMvaa 120,88,8\nTa 122,25,41984\nMvaa 116,84,8\nRa 56,27,45056\n" \
"Mvaa 148,84,12\nTa 124,26,40960\nMvaa 144,80,12\nTa 126,26,41984\nMvaa 112,80,8\nRa 60,27,47104\nMvaa 108,76,8\ns_and_b32 s96, s32, 0x1000\n" \
"s_lshr_b32 s97, s96, 6\nMvaa 140,76,12\ns_lshl_b32 s96, s96, 2\nv_subrev_u32 v20, s96, v8\nMvaa 136,72,12\nv_xor_b32 v20, s97, v20\nv_xor_b32 v21, 32, v20\nMvaa 104,72,8\n" \
"v_xor_b32 v22, 0x80, v20\nv_xor_b32 v23, 0xa0, v20\nMvaa 100,68,8\nMvaa 132,68,12\nMvaa 128,64,12\nMvaa 96,64,8\nMvaa 160,64,16\ns_waitcnt lgkmcnt(0)\n" \
"s_barrier\nMvaa 192,64,20\ns_add_u32 m0, s88, 32768\nMvaa 196,68,20\nD 9,64,0,0\nMvaa 164,68,16\nMvaa 168,72,16\nMvaa 200,72,20\n" \
"Mvaa 204,76,20\nD 9,64,s73,1024\nMvaa 172,76,16\nMvaa 176,80,16\nMvaa 208,80,20\nMvaa 212,84,20\nD 9,64,s74,2048\nMvaa 180,84,16\n" \
"Mvaa 184,88,16\nMvaa 216,88,20\nMvaa 220,92,20\nD 9,64,s75,3072\nMvaa 188,92,16\nMvaa 252,92,24\nMaaa 156,92,28\ns_add_u32 m0, s88, 36864\n" \
"Maaa 152,88,28\nD 9,64,s76,0\nMvaa 248,88,24\nMvaa 244,84,24\nMaaa 148,84,28\nMaaa 144,80,28\nD 9,64,s77,1024\nMvaa 240,80,24\n" \
"Mvaa 236,76,24\nMaaa 140,76,28\nMaaa 136,72,28\nD 9,64,s78,2048\nMvaa 232,72,24\nMvaa 228,68,24\nMaaa 132,68,28\nMaaa 128,64,28\n" \
"D 9,64,s79,3072\nMvaa 224,64,24\nMvaa 32,96,32\nMvaa 36,100,32\ns_add_u32 m0, s32, 98304\nMvaa 40,104,32\nD 10,68,0,0\nMvaa 44,108,32\n" \
"Mvaa 48,112,32\nMvaa 52,116,32\nMvaa 56,120,32\nD 10,68,s89,1024\nMvaa 60,124,32\nMvaa 64,96,36\nMvaa 68,100,36\nMvaa 72,104,36\n" \
"D 11,68,s90,2048\nC 32,32,33\nMvaa 76,108,36\nC 33,34,35\nC 34,36,37\nC 35,38,39\nMvaa 80,112,36\nC 36,40,41\n" \
"C 37,42,43\nC 38,44,45\nMvaa 84,116,36\nC 39,46,47\nC 40,48,49\nC 41,50,51\nMvaa 88,120,36\nD 11,68,s91,3072\n" \
"C 42,52,53\nMvaa 92,124,36\nC 43,54,55\nC 44,56,57\nC 45,58,59\nMvaa 96,96,40\nC 46,60,61\nC 47,62,63\n" \
"Mvaa 100,100,40\ns_add_u32 m0, s92, 114688\nMvaa 104,104,40\nD 12,68,0,0\nC 48,64,65\nMvaa 108,108,40\nC 49,66,67\nC 50,68,69\n" \
"C 51,70,71\nMvaa 112,112,40\nC 52,72,73\nC 53,74,75\nC 54,76,77\nMvaa 116,116,40\nC 55,78,79\nC 56,80,81\n" \
"C 57,82,83\ns_branch 99f\n.p2align 12\nzp26:\ns_setpc_b64 s[98:99]\n99:\nMvaa 120,120,40\nD 12,68,s89,1024\n" \
"C 58,84,85\nMvaa 124,124,40\nC 59,86,87\nC 60,88,89\nC 61,90,91\nMvaa 128,96,44\nC 62,92,93\nC 63,94,95\n" \
"Mvaa 132,100,44\nMvaa 136,104,44\nC 96,96,97\nC 97,98,99\nC 98,100,101\nMvaa 140,108,44\nC 99,102,103\nAW 160,96\n" \
"AW 161,97\nMvaa 144,112,44\nAW 162,98\nAW 163,99\nC 104,104,105\nMvaa 148,116,44\nC 105,106,107\nC 106,108,109\n" \
"C 107,110,111\nMvaa 152,120,44\nbuffer_load_dword v30, v4, s[52:55], 0 offen sc1\nAW 164,104\nMvaa 156,124,44\ns_waitcnt vmcnt(16)\ns_barrier\nAW 165,105\n" \
"Mvaa 160,96,48\nTa 64,20,0\nv_add_u32 v9, 128, v9\nAW 166,106\nMvaa 164,100,48\nTa 66,20,1024\nv_add_u32 v10, s72, v10\nAW 167,107\n" \
"Mvaa 168,104,48\nRa 0,7,0\nv_add_u32 v11, s72, v11\nC 112,112,113\nMvaa 172,108,48\nTa 68,21,0\nv_add_u32 v12, s72, v12\nC 113,114,115\n" \
"Mvaa 176,112,48\nv_add_u32 v4, s44, v4\nTa 70,21,1024\nC 114,116,117\nMvaa 180,116,48\nRa 4,7,2048\nC 115,118,119\nAW 168,112\n" \
"Mvaa 184,120,48\nTa 72,22,0\nAW 169,113\nAW 170,114\nMvaa 188,124,48\nTa 74,22,1024\nAW 171,115\nC 120,120,121\n" \
"Mvaa 192,96,52\nRa 8,7,4096\nC 121,122,123\nC 122,124,125\nMvaa 196,100,52\nTa 76,23,0\nC 123,126,127\nAW 172,120\n" \
"Mvaa 200,104,52\nTa 78,23,1024\nAW 173,121\nAW 174,122\nMvaa 204,108,52\nRa 12,7,6144\nAW 175,123\nC 128,128,129\n" \
"Mvaa 208,112,52\nTa 80,20,16384\nC 129,130,131\nC 130,132,133\nMvaa 212,116,52\nTa 82,20,17408\nC 131,134,135\nAW 176,128\n" \
"Mvaa 216,120,52\nRa 16,7,8192\nAW 177,129\nAW 178,130\nMvaa 220,124,52\nTa 84,21,16384\nAW 179,131\nC 136,136,137\n" \
"Mvaa 224,96,56\nTa 86,21,17408\nC 137,138,139\nC 138,140,141\nMvaa 228,100,56\nRa 20,7,10240\nC 139,142,143\nAW 180,136\n" \
"Mvaa 232,104,56\nRa 24,7,12288\nAW 181,137\nAW 182,138\nMvaa 236,108,56\nRa 28,7,14336\nAW 183,139\nC 144,144,145\n" \
"Mvaa 240,112,56\nC 145,146,147\nC 146,148,149\nC 147,150,151\nMvaa 244,116,56\nAW 184,144\nAW 185,145\nAW 186,146\n" \
"Mvaa 248,120,56\nAW 187,147\nC 152,152,153\nC 153,154,155\nMvaa 252,124,56\nC 154,156,157\nC 155,158,159\nAW 188,152\n" \
"Maaa 128,96,60\nAW 189,153\nAW 190,154\nAW 191,155\nMaaa 132,100,60\nC 160,160,161\nC 161,162,163\nC 162,164,165\n" \
"Maaa 136,104,60\nC 163,166,167\nAW 192,160\nAW 193,161\nMaaa 140,108,60\nAW 194,162\nAW 195,163\nC 168,168,169\n" \
"Maaa 144,112,60\nC 169,170,171\nC 170,172,173\nC 171,174,175\nMaaa 148,116,60\nAW 196,168\nAW 197,169\nAW 198,170\n" \
"Maaa 152,120,60\nAW 199,171\nC 176,176,177\nC 177,178,179\nMaaa 156,124,60\nC 178,180,181\nC 179,182,183\nAW 200,176\n" \
"s_waitcnt lgkmcnt(0)\nzA:\nZvaa 64,64,0\nTa 96,20,8192\nAW 201,177\nAW 202,178\nZvaa 88,64,4\nTa 98,20,9216\n" \
"AW 203,179\nC 184,184,185\nZvaa 92,68,4\nRa 32,27,0\nC 185,186,187\nC 186,188,189\nZvaa 68,68,0\nTa 100,21,8192\n" \
"C 187,190,191\nAW 204,184\nZvaa 72,72,0\nTa 102,21,9216\nAW 205,185\nAW 206,186\nZvaa 96,72,4\nRa 36,27,2048\n" \
"AW 207,187\nC 192,192,193\nZvaa 100,76,4\nTa 104,22,8192\nC 193,194,195\nC 194,196,197\nZvaa 76,76,0\nTa 106,22,9216\n" \
"C 195,198,199\nAW 208,192\nZvaa 80,0,80\nRa 40,27,4096\nAW 209,193\nAW 210,194\nZvaa 104,4,80\nTa 108,23,8192\n" \
"AW 211,195\nC 200,200,201\nZvaa 108,4,84\nTa 110,23,9216\nC 201,202,203\nC 202,204,205\nZvaa 84,0,84\nRa 44,27,6144\n" \
"C 203,206,207\nAW 212,200\nZvaa 132,8,84\nTa 112,22,16384\nAW 213,201\nAW 214,202\nZvaa 156,12,84\nTa 114,22,17408\n" \
"AW 215,203\nC 208,208,209\nZvaa 152,12,80\nRa 48,27,8192\nC 209,210,211\nC 210,212,213\nZvaa 128,8,80\nTa 116,23,16384\n" \
"C 211,214,215\nAW 216,208\nZvaa 124,76,8\nTa 118,23,17408\nAW 217,209\nAW 218,210\nZvaa 148,76,12\nRa 52,27,10240\n" \
"AW 219,211\nC 216,216,217\nZvaa 144,72,12\nRa 56,27,12288\nC 217,218,219\nC 218,220,221\nZvaa 120,72,8\nRa 60,27,14336\n" \
"C 219,222,223\nAW 220,216\nZvaa 116,68,8\nAW 221,217\nAW 222,218\nAW 223,219\nZvaa 140,68,12\nC 224,224,225\n" \
"C 225,226,227\nC 226,228,229\nZvaa 136,64,12\nC 227,230,231\nAW 224,224\nAW 225,225\nZvaa 112,64,8\nAW 226,226\n" \
"AW 227,227\nC 232,232,233\nZvaa 160,64,16\nC 233,234,235\nC 234,236,237\nC 235,238,239\nZvaa 184,64,20\nAW 228,232\n" \
"AW 229,233\nAW 230,234\nZvaa 188,68,20\ns_waitcnt lgkmcnt(0)\ns_barrier\nAW 231,235\nZvaa 164,68,16\ns_add_u32 m0, s88, 0\n" \
"C 240,240,241\nC 241,242,243\nZvaa 168,72,16\nD 9,64,0,0\nC 242,244,245\nZvaa 192,72,20\nC 243,246,247\nAW 232,240\n" \
"AW 233,241\nZvaa 196,76,20\nAW 234,242\nAW 235,243\nC 248,248,249\nZvaa 172,76,16\nC 249,250,251\nC 250,252,253\n" \
"C 251,254,255\nZvaa 176,16,80\nD 9,64,s73,1024\nAW 236,248\nZvaa 200,20,80\nAW 237,249\nAW 238,250\nAW 239,251\n" \
"Zvaa 204,20,84\nZvaa 180,16,84\nZvaa 228,24,84\nD 9,64,s74,2048\nZvaa 252,28,84\ns_add_u32 s95, s34, 0\nZvaa 248,28,80\nbuffer_store_dwordx4 v[32:35], v0, s[60:63], s95 offen offset:0 sc1\n" \
"Zvaa 224,24,80\nZvaa 220,76,24\nD 9,64,s75,3072\nZvaa 244,76,28\nZvaa 240,72,28\nZvaa 216,72,24\ns_add_u32 m0, s88, 4096\nZvaa 212,68,24\n" \
"D 9,64,s76,0\nZvaa 236,68,28\nZvaa 232,64,28\nZvaa 208,64,24\nMvaa 64,96,32\nD 9,64,s77,1024\nMvaa 88,96,36\nMvaa 92,100,36\n" \
"buffer_store_dwordx4 v[36:39], v0, s[60:63], s95 offen offset:1024 sc1\nMvaa 68,100,32\nMvaa 72,104,32\nD 9,64,s78,2048\nMvaa 96,104,36\nMvaa 100,108,36\nMvaa 76,108,32\nMvaa 80,32,112\n" \
"D 9,64,s79,3072\nMvaa 104,36,112\nMvaa 108,36,116\nMvaa 84,32,116\ns_add_u32 m0, s32, 65536\nMvaa 132,40,116\nD 10,68,0,0\nMvaa 156,44,116\n" \
"Mvaa 152,44,112\nbuffer_store_dwordx4 v[40:43], v0, s[60:63], s95 offen offset:2048 sc1\nMvaa 128,40,112\nMvaa 124,108,40\nD 10,68,s89,1024\nMvaa 148,108,44\nMvaa 144,104,44\nMvaa 120,104,40\n" \
"Mvaa 116,100,40\nD 11,68,s90,2048\nMvaa 140,100,44\nMvaa 136,96,44\ns_waitcnt vmcnt(15)\ns_barrier\nMvaa 112,96,40\nTa 64,20,32768\n" \
"Mvaa 160,96,48\nD 11,68,s91,3072\nTa 66,20,33792\nMvaa 184,96,52\nRa 0,7,32768\nMvaa 188,100,52\nTa 68,21,32768\nMvaa 164,100,48\n" \
"s_add_u32 m0, s92, 81920\nTa 70,21,33792\nMvaa 168,104,48\nD 12,68,0,0\nRa 4,7,34816\nMvaa 192,104,52\nTa 72,22,32768\nMvaa 196,108,52\n" \
"Ta 74,22,33792\nMvaa 172,108,48\nRa 8,7,36864\nMvaa 176,48,112\nD 12,68,s89,1024\nTa 76,23,32768\nMvaa 200,52,112\nTa 78,23,33792\n" \
"v_add_u32 v9, 128, v9\nMvaa 204,52,116\nRa 12,7,38912\nv_add_u32 v10, s72, v10\nMvaa 180,48,116\nTa 80,20,49152\nv_add_u32 v11, s72, v11\nMvaa 228,56,116\n" \
"Ta 82,20,50176\nv_add_u32 v12, s72, v12\nMvaa 252,60,116\nRa 16,7,40960\nMvaa 248,60,112\nTa 84,21,49152\nMvaa 224,56,112\nTa 86,21,50176\n" \
"Mvaa 220,108,56\nRa 20,7,43008\nMvaa 244,108,60\nRa 24,7,45056\nMvaa 240,104,60\nRa 28,7,47104\nMvaa 216,104,56\nbuffer_store_dwordx4 v[44:47], v0, s[60:63], s95 offen offset:3072 sc1\n" \
"Mvaa 212,100,56\ns_add_u32 s95, s34, 4096\nMvaa 236,100,60\nbuffer_store_dwordx4 v[48:51], v0, s[60:63], s95 offen offset:0 sc1\nMvaa 232,96,60\nMvaa 208,96,56\ns_waitcnt lgkmcnt(0)\nMvaa 64,64,0\n" \
"Ta 96,20,40960\nMvaa 88,64,4\nTa 98,20,41984\nMvaa 92,68,4\nRa 32,27,32768\nMvaa 68,68,0\nTa 100,21,40960\nMvaa 72,72,0\n" \
"Ta 102,21,41984\nMvaa 96,72,4\nRa 36,27,34816\nMvaa 100,76,4\nTa 104,22,40960\nMvaa 76,76,0\nTa 106,22,41984\nMvaa 80,0,80\n" \
"Ra 40,27,36864\nMvaa 104,4,80\nTa 108,23,40960\nMvaa 108,4,84\nTa 110,23,41984\nMvaa 84,0,84\nRa 44,27,38912\nMvaa 132,8,84\n" \
"Ta 112,22,49152\nMvaa 156,12,84\nTa 114,22,50176\nMvaa 152,12,80\nRa 48,27,40960\nMvaa 128,8,80\nTa 116,23,49152\nMvaa 124,76,8\n" \
"Ta 118,23,50176\nMvaa 148,76,12\nRa 52,27,43008\nMvaa 144,72,12\nRa 56,27,45056\nMvaa 120,72,8\nRa 60,27,47104\nMvaa 116,68,8\n" \
"buffer_store_dwordx4 v[52:55], v0, s[60:63], s95 offen offset:1024 sc1\nMvaa 140,68,12\nMvaa 136,64,12\nbuffer_store_dwordx4 v[56:59], v0, s[60:63], s95 offen offset:2048 sc1\nMvaa 112,64,8\nMvaa 160,64,16\nMvaa 184,64,20\nMvaa 188,68,20\n" \
"s_waitcnt lgkmcnt(0)\ns_barrier\nMvaa 164,68,16\ns_add_u32 m0, s88, 32768\nMvaa 168,72,16\nD 9,64,0,0\nMvaa 192,72,20\nMvaa 196,76,20\n" \
"Mvaa 172,76,16\nMvaa 176,16,80\nD 9,64,s73,1024\nMvaa 200,20,80\nMvaa 204,20,84\nbuffer_store_dwordx4 v[60:63], v0, s[60:63], s95 offen offset:3072 sc1\nMvaa 180,16,84\nMvaa 228,24,84\n" \
"D 9,64,s74,2048\nMvaa 252,28,84\nMvaa 248,28,80\nMvaa 224,24,80\nMvaa 220,76,24\nD 9,64,s75,3072\nMvaa 244,76,28\nMvaa 240,72,28\n" \
"Mvaa 216,72,24\ns_add_u32 m0, s88, 36864\nMvaa 212,68,24\nD 9,64,s76,0\nMvaa 236,68,28\nMvaa 232,64,28\ns_add_u32 s95, s34, 8192\nMvaa 208,64,24\n" \
"buffer_store_dwordx4 a[160:163], v0, s[60:63], s95 offen offset:0 sc1\nMvaa 64,96,32\nD 9,64,s77,1024\nMvaa 88,96,36\nAR 48,128\nAR 49,129\nMvaa 92,100,36\nAR 50,130\n" \
"AR 51,131\nMvaa 68,100,32\nAR 52,132\nAR 53,133\nMvaa 72,104,32\nD 9,64,s78,2048\nMvaa 96,104,36\nAR 54,134\n" \
"AR 55,135\nMvaa 100,108,36\nC 32,48,49\nC 33,50,51\nMvaa 76,108,32\nC 34,52,53\nC 35,54,55\nMvaa 80,32,112\n" \
"D 9,64,s79,3072\nMvaa 104,36,112\nAR 56,136\nAR 57,137\nMvaa 108,36,116\nAR 58,138\nAR 59,139\ns_branch 99f\n" \
".p2align 12\nzp27:\ns_setpc_b64 s[98:99]\n99:\nMvaa 84,32,116\ns_add_u32 m0, s32, 98304\nAR 60,140\nMvaa 132,40,116\n" \
"D 10,68,0,0\nMvaa 156,44,116\nAR 61,141\nAR 62,142\nMvaa 152,44,112\nAR 63,143\nC 36,56,57\nMvaa 128,40,112\n" \
"buffer_store_dwordx4 a[164:167], v0, s[60:63], s95 offen offset:1024 sc1\nMvaa 124,108,40\nD 10,68,s89,1024\nMvaa 148,108,44\nC 37,58,59\nC 38,60,61\nMvaa 144,104,44\nC 39,62,63\n" \
"AR 48,144\nMvaa 120,104,40\nAR 49,145\nAR 50,146\nMvaa 116,100,40\nD 11,68,s90,2048\nMvaa 140,100,44\nAR 51,147\n" \
"AR 52,148\nMvaa 136,96,44\ns_waitcnt vmcnt(18)\ns_barrier\nMvaa 112,96,40\nTa 64,20,0\nAR 53,149\nMvaa 160,96,48\n" \
"D 11,68,s91,3072\nTa 66,20,1024\nMvaa 184,96,52\nRa 0,7,0\nAR 54,150\nMvaa 188,100,52\nTa 68,21,0\nAR 55,151\n" \
"Mvaa 164,100,48\ns_add_u32 m0, s92, 114688\nTa 70,21,1024\nMvaa 168,104,48\nD 12,68,0,0\nRa 4,7,2048\nMvaa 192,104,52\nTa 72,22,0\n" \
"C 40,48,49\nMvaa 196,108,52\nTa 74,22,1024\nC 41,50,51\nMvaa 172,108,48\nRa 8,7,4096\nC 42,52,53\nMvaa 176,48,112\n" \
"D 12,68,s89,1024\nTa 76,23,0\nMvaa 200,52,112\nTa 78,23,1024\nv_add_u32 v9, 128, v9\nMvaa 204,52,116\nRa 12,7,6144\nv_add_u32 v10, s72, v10\n" \
"Mvaa 180,48,116\nTa 80,20,16384\nv_add_u32 v11, s72, v11\nMvaa 228,56,116\nTa 82,20,17408\nv_add_u32 v12, s72, v12\nMvaa 252,60,116\nRa 16,7,8192\n" \
"C 43,54,55\nMvaa 248,60,112\nTa 84,21,16384\nAR 56,152\nMvaa 224,56,112\nTa 86,21,17408\nAR 57,153\nMvaa 220,108,56\n" \
"Ra 20,7,10240\nAR 58,154\nMvaa 244,108,60\nRa 24,7,12288\nAR 59,155\nMvaa 240,104,60\nRa 28,7,14336\nAR 60,156\n" \
"Mvaa 216,104,56\nbuffer_store_dwordx4 a[168:171], v0, s[60:63], s95 offen offset:2048 sc1\nMvaa 212,100,56\nAR 61,157\nAR 62,158\nMvaa 236,100,60\nAR 63,159\nC 44,56,57\n" \
"Mvaa 232,96,60\nC 45,58,59\nC 46,60,61\nMvaa 208,96,56\ns_waitcnt lgkmcnt(0)\nC 47,62,63\nMvaa 64,64,0\nTa 96,20,8192\n" \
"Mvaa 88,64,4\nTa 98,20,9216\nMvaa 92,68,4\nRa 32,27,0\nMvaa 68,68,0\nTa 100,21,8192\nMvaa 72,72,0\nTa 102,21,9216\n" \
"Mvaa 96,72,4\nRa 36,27,2048\nMvaa 100,76,4\nTa 104,22,8192\nMvaa 76,76,0\nTa 106,22,9216\nMvaa 80,0,80\nRa 40,27,4096\n" \
"Mvaa 104,4,80\nTa 108,23,8192\nMvaa 108,4,84\nTa 110,23,9216\nMvaa 84,0,84\nRa 44,27,6144\nMvaa 132,8,84\nTa 112,22,16384\n" \
"Mvaa 156,12,84\nTa 114,22,17408\nMvaa 152,12,80\nRa 48,27,8192\nMvaa 128,8,80\nTa 116,23,16384\nMvaa 124,76,8\nTa 118,23,17408\n" \
"Mvaa 148,76,12\nRa 52,27,10240\nMvaa 144,72,12\nRa 56,27,12288\nMvaa 120,72,8\nRa 60,27,14336\nMvaa 116,68,8\nbuffer_store_dwordx4 a[172:175], v0, s[60:63], s95 offen offset:3072 sc1\n" \
"Mvaa 140,68,12\ns_add_u32 s95, s34, 12288\nMvaa 136,64,12\nbuffer_store_dwordx4 a[176:179], v0, s[60:63], s95 offen offset:0 sc1\nMvaa 112,64,8\nMvaa 160,64,16\nMvaa 184,64,20\nMvaa 188,68,20\n" \
"s_waitcnt lgkmcnt(0)\ns_barrier\nMvaa 164,68,16\ns_add_u32 m0, s88, 0\nMvaa 168,72,16\nD 9,64,0,0\nMvaa 192,72,20\nMvaa 196,76,20\n" \
"Mvaa 172,76,16\nMvaa 176,16,80\nD 9,64,s73,1024\nMvaa 200,20,80\nMvaa 204,20,84\nbuffer_store_dwordx4 a[180:183], v0, s[60:63], s95 offen offset:1024 sc1\nMvaa 180,16,84\nMvaa 228,24,84\n" \
"D 9,64,s74,2048\nMvaa 252,28,84\nMvaa 248,28,80\nMvaa 224,24,80\nMvaa 220,76,24\nD 9,64,s75,3072\nMvaa 244,76,28\nMvaa 240,72,28\n" \
"Mvaa 216,72,24\ns_add_u32 m0, s88, 4096\nMvaa 212,68,24\nD 9,64,s76,0\nMvaa 236,68,28\nMvaa 232,64,28\nMvaa 208,64,24\nbuffer_store_dwordx4 a[184:187], v0, s[60:63], s95 offen offset:2048 sc1\n" \
"Mvaa 64,96,32\nD 9,64,s77,1024\nMvaa 88,96,36\nMvaa 92,100,36\nMvaa 68,100,32\nMvaa 72,104,32\nD 9,64,s78,2048\nMvaa 96,104,36\n" \
"Mvaa 100,108,36\nMvaa 76,108,32\nMvaa 80,32,112\nD 9,64,s79,3072\nMvaa 104,36,112\nMvaa 108,36,116\nMvaa 84,32,116\ns_add_u32 m0, s32, 65536\n" \
"Mvaa 132,40,116\nD 10,68,0,0\nMvaa 156,44,116\nMvaa 152,44,112\nMvaa 128,40,112\nbuffer_store_dwordx4 a[188:191], v0, s[60:63], s95 offen offset:3072 sc1\nMvaa 124,108,40\nD 10,68,s89,1024\n" \
"Mvaa 148,108,44\nMvaa 144,104,44\nMvaa 120,104,40\nMvaa 116,100,40\nD 11,68,s90,2048\nMvaa 140,100,44\nMvaa 136,96,44\ns_waitcnt vmcnt(17)\n" \
"s_barrier\nMvaa 112,96,40\nTa 64,20,32768\nMvaa 160,96,48\nD 11,68,s91,3072\nTa 66,20,33792\nMvaa 184,96,52\nRa 0,7,32768\n" \
"Mvaa 188,100,52\nTa 68,21,32768\nMvaa 164,100,48\ns_add_u32 m0, s92, 81920\nTa 70,21,33792\nMvaa 168,104,48\nD 12,68,0,0\nRa 4,7,34816\n" \
"Mvaa 192,104,52\nTa 72,22,32768\nMvaa 196,108,52\nTa 74,22,33792\nMvaa 172,108,48\nRa 8,7,36864\nMvaa 176,48,112\nD 12,68,s89,1024\n" \
"Ta 76,23,32768\nMvaa 200,52,112\nTa 78,23,33792\nv_add_u32 v9, 128, v9\nMvaa 204,52,116\nRa 12,7,38912\nv_add_u32 v10, s72, v10\nMvaa 180,48,116\n" \
"Ta 80,20,49152\nv_add_u32 v11, s72, v11\nMvaa 228,56,116\nTa 82,20,50176\nv_add_u32 v12, s72, v12\nMvaa 252,60,116\nRa 16,7,40960\nMvaa 248,60,112\n" \
"Ta 84,21,49152\nMvaa 224,56,112\nTa 86,21,50176\nMvaa 220,108,56\nRa 20,7,43008\nMvaa 244,108,60\nRa 24,7,45056\nMvaa 240,104,60\n" \
"Ra 28,7,47104\ns_add_u32 s95, s34, 16384\nMvaa 216,104,56\nbuffer_store_dwordx4 a[192:195], v0, s[60:63], s95 offen offset:0 sc1\nMvaa 212,100,56\nMvaa 236,100,60\nMvaa 232,96,60\nMvaa 208,96,56\n" \
"s_waitcnt lgkmcnt(0)\nMvaa 64,64,0\nTa 96,20,40960\nMvaa 88,64,4\nTa 98,20,41984\nMvaa 92,68,4\nRa 32,27,32768\nMvaa 68,68,0\n" \
"Ta 100,21,40960\nMvaa 72,72,0\nTa 102,21,41984\nMvaa 96,72,4\nRa 36,27,34816\nMvaa 100,76,4\nTa 104,22,40960\nMvaa 76,76,0\n" \
"Ta 106,22,41984\nMvaa 80,0,80\nRa 40,27,36864\nMvaa 104,4,80\nTa 108,23,40960\nMvaa 108,4,84\nTa 110,23,41984\nMvaa 84,0,84\n" \
"Ra 44,27,38912\nMvaa 132,8,84\nTa 112,22,49152\nMvaa 156,12,84\nTa 114,22,50176\nMvaa 152,12,80\nRa 48,27,40960\nMvaa 128,8,80\n" \
"Ta 116,23,49152\nMvaa 124,76,8\nTa 118,23,50176\nMvaa 148,76,12\nRa 52,27,43008\nMvaa 144,72,12\nRa 56,27,45056\nMvaa 120,72,8\n" \
"Ra 60,27,47104\nMvaa 116,68,8\nbuffer_store_dwordx4 a[196:199], v0, s[60:63], s95 offen offset:1024 sc1\nMvaa 140,68,12\nMvaa 136,64,12\nbuffer_store_dwordx4 a[200:203], v0, s[60:63], s95 offen offset:2048 sc1\nMvaa 112,64,8\nMvaa 160,64,16\n" \
"Mvaa 184,64,20\nMvaa 188,68,20\ns_waitcnt lgkmcnt(0)\ns_barrier\nMvaa 164,68,16\ns_add_u32 m0, s88, 32768\nMvaa 168,72,16\nD 9,64,0,0\n" \
"Mvaa 192,72,20\nMvaa 196,76,20\nMvaa 172,76,16\nMvaa 176,16,80\nD 9,64,s73,1024\nMvaa 200,20,80\nMvaa 204,20,84\nbuffer_store_dwordx4 a[204:207], v0, s[60:63], s95 offen offset:3072 sc1\n" \
"Mvaa 180,16,84\nMvaa 228,24,84\nD 9,64,s74,2048\nMvaa 252,28,84\nMvaa 248,28,80\nMvaa 224,24,80\nMvaa 220,76,24\nD 9,64,s75,3072\n" \
"Mvaa 244,76,28\nMvaa 240,72,28\nMvaa 216,72,24\ns_add_u32 m0, s88, 36864\nMvaa 212,68,24\nD 9,64,s76,0\nMvaa 236,68,28\nMvaa 232,64,28\n" \
"s_add_u32 s95, s34, 20480\nMvaa 208,64,24\nbuffer_store_dwordx4 a[208:211], v0, s[60:63], s95 offen offset:0 sc1\nMvaa 64,96,32\nD 9,64,s77,1024\nMvaa 88,96,36\nMvaa 92,100,36\nMvaa 68,100,32\n" \
"Mvaa 72,104,32\nD 9,64,s78,2048\nMvaa 96,104,36\nMvaa 100,108,36\nMvaa 76,108,32\nMvaa 80,32,112\nD 9,64,s79,3072\nMvaa 104,36,112\n" \
"Mvaa 108,36,116\nMvaa 84,32,116\ns_add_u32 m0, s32, 98304\nMvaa 132,40,116\nD 10,68,0,0\nMvaa 156,44,116\nMvaa 152,44,112\nMvaa 128,40,112\n" \
"buffer_store_dwordx4 a[212:215], v0, s[60:63], s95 offen offset:1024 sc1\nMvaa 124,108,40\nD 10,68,s89,1024\nMvaa 148,108,44\nMvaa 144,104,44\nMvaa 120,104,40\nMvaa 116,100,40\nD 11,68,s90,2048\n" \
"Mvaa 140,100,44\nMvaa 136,96,44\ns_waitcnt vmcnt(17)\ns_barrier\nMvaa 112,96,40\nTa 64,20,0\nMvaa 160,96,48\nD 11,68,s91,3072\n" \
"Ta 66,20,1024\nMvaa 184,96,52\nRa 0,7,0\nMvaa 188,100,52\nTa 68,21,0\nMvaa 164,100,48\ns_add_u32 m0, s92, 114688\nTa 70,21,1024\n" \
"Mvaa 168,104,48\nD 12,68,0,0\nRa 4,7,2048\nMvaa 192,104,52\nTa 72,22,0\nMvaa 196,108,52\nTa 74,22,1024\nMvaa 172,108,48\n" \
"Ra 8,7,4096\nMvaa 176,48,112\nD 12,68,s89,1024\nTa 76,23,0\nMvaa 200,52,112\nTa 78,23,1024\nv_add_u32 v9, 128, v9\nMvaa 204,52,116\n" \
"Ra 12,7,6144\nv_add_u32 v10, s72, v10\nMvaa 180,48,116\nTa 80,20,16384\nv_add_u32 v11, s72, v11\nMvaa 228,56,116\nTa 82,20,17408\nv_add_u32 v12, s72, v12\n" \
"Mvaa 252,60,116\nRa 16,7,8192\nMvaa 248,60,112\nTa 84,21,16384\nMvaa 224,56,112\nTa 86,21,17408\nMvaa 220,108,56\nRa 20,7,10240\n" \
"Mvaa 244,108,60\nRa 24,7,12288\nMvaa 240,104,60\nRa 28,7,14336\nMvaa 216,104,56\nbuffer_store_dwordx4 a[216:219], v0, s[60:63], s95 offen offset:2048 sc1\nMvaa 212,100,56\nMvaa 236,100,60\n" \
"Mvaa 232,96,60\nMvaa 208,96,56\ns_waitcnt lgkmcnt(0)\nMvaa 64,64,0\nTa 96,20,8192\nMvaa 88,64,4\nTa 98,20,9216\nMvaa 92,68,4\n" \
"Ra 32,27,0\nMvaa 68,68,0\nTa 100,21,8192\nMvaa 72,72,0\nTa 102,21,9216\nMvaa 96,72,4\nRa 36,27,2048\nMvaa 100,76,4\n" \
"Ta 104,22,8192\nMvaa 76,76,0\nTa 106,22,9216\nMvaa 80,0,80\nRa 40,27,4096\nMvaa 104,4,80\nTa 108,23,8192\nMvaa 108,4,84\n" \
"Ta 110,23,9216\nMvaa 84,0,84\nRa 44,27,6144\nMvaa 132,8,84\nTa 112,22,16384\nMvaa 156,12,84\nTa 114,22,17408\nMvaa 152,12,80\n" \
"Ra 48,27,8192\nMvaa 128,8,80\nTa 116,23,16384\nMvaa 124,76,8\nTa 118,23,17408\nMvaa 148,76,12\nRa 52,27,10240\nMvaa 144,72,12\n" \
"Ra 56,27,12288\nMvaa 120,72,8\nRa 60,27,14336\nMvaa 116,68,8\nbuffer_store_dwordx4 a[220:223], v0, s[60:63], s95 offen offset:3072 sc1\nMvaa 140,68,12\ns_add_u32 s95, s34, 24576\nMvaa 136,64,12\n" \
"buffer_store_dwordx4 a[224:227], v0, s[60:63], s95 offen offset:0 sc1\nMvaa 112,64,8\nMvaa 160,64,16\nMvaa 184,64,20\nMvaa 188,68,20\ns_waitcnt lgkmcnt(0)\ns_barrier\nMvaa 164,68,16\n" \
"s_add_u32 m0, s88, 0\nMvaa 168,72,16\nD 9,64,0,0\nMvaa 192,72,20\nMvaa 196,76,20\nMvaa 172,76,16\nMvaa 176,16,80\nD 9,64,s73,1024\n" \
"Mvaa 200,20,80\nMvaa 204,20,84\nbuffer_store_dwordx4 a[228:231], v0, s[60:63], s95 offen offset:1024 sc1\nMvaa 180,16,84\nMvaa 228,24,84\nD 9,64,s74,2048\nMvaa 252,28,84\nMvaa 248,28,80\n" \
"Mvaa 224,24,80\nMvaa 220,76,24\nD 9,64,s75,3072\nMvaa 244,76,28\nMvaa 240,72,28\nMvaa 216,72,24\ns_add_u32 m0, s88, 4096\nMvaa 212,68,24\n" \
"s_branch 99f\n.p2align 12\nzp28:\ns_setpc_b64 s[98:99]\n99:\nD 9,64,s76,0\nMvaa 236,68,28\nMvaa 232,64,28\n" \
"Mvaa 208,64,24\nbuffer_store_dwordx4 a[232:235], v0, s[60:63], s95 offen offset:2048 sc1\nMvaa 64,96,32\nD 9,64,s77,1024\nMvaa 88,96,36\nMvaa 92,100,36\nMvaa 68,100,32\nMvaa 72,104,32\n" \
"D 9,64,s78,2048\nMvaa 96,104,36\nMvaa 100,108,36\nMvaa 76,108,32\nMvaa 80,32,112\nD 9,64,s79,3072\nMvaa 104,36,112\nMvaa 108,36,116\n" \
"Mvaa 84,32,116\ns_add_u32 m0, s32, 65536\nMvaa 132,40,116\nD 10,68,0,0\nMvaa 156,44,116\nMvaa 152,44,112\nMvaa 128,40,112\nbuffer_store_dwordx4 a[236:239], v0, s[60:63], s95 offen offset:3072 sc1\n" \
"Mvaa 124,108,40\nD 10,68,s89,1024\nMvaa 148,108,44\nMvaa 144,104,44\nMvaa 120,104,40\nMvaa 116,100,40\nD 11,68,s90,2048\nMvaa 140,100,44\n" \
"Mvaa 136,96,44\ns_waitcnt vmcnt(17)\ns_barrier\nMvaa 112,96,40\nTa 64,20,32768\nMvaa 160,96,48\nD 11,68,s91,3072\nTa 66,20,33792\n" \
"Mvaa 184,96,52\nRa 0,7,32768\nMvaa 188,100,52\nTa 68,21,32768\nMvaa 164,100,48\ns_add_u32 m0, s92, 81920\nTa 70,21,33792\nMvaa 168,104,48\n" \
"D 12,68,0,0\nRa 4,7,34816\nMvaa 192,104,52\nTa 72,22,32768\nMvaa 196,108,52\nTa 74,22,33792\nMvaa 172,108,48\nRa 8,7,36864\n" \
"Mvaa 176,48,112\nD 12,68,s89,1024\nTa 76,23,32768\nMvaa 200,52,112\nTa 78,23,33792\nv_add_u32 v9, 128, v9\nMvaa 204,52,116\nRa 12,7,38912\n" \
"v_add_u32 v10, s72, v10\nMvaa 180,48,116\nTa 80,20,49152\nv_add_u32 v11, s72, v11\nMvaa 228,56,116\nTa 82,20,50176\nv_add_u32 v12, s72, v12\nMvaa 252,60,116\n" \
"Ra 16,7,40960\nMvaa 248,60,112\nTa 84,21,49152\nMvaa 224,56,112\nTa 86,21,50176\nMvaa 220,108,56\nRa 20,7,43008\nMvaa 244,108,60\n" \
"Ra 24,7,45056\nMvaa 240,104,60\nRa 28,7,47104\ns_add_u32 s95, s34, 28672\nMvaa 216,104,56\nbuffer_store_dwordx4 v[32:35], v0, s[60:63], s95 offen offset:0 sc1\nMvaa 212,100,56\nMvaa 236,100,60\n" \
"Mvaa 232,96,60\nMvaa 208,96,56\ns_waitcnt lgkmcnt(0)\nMvaa 64,64,0\nTa 96,20,40960\nMvaa 88,64,4\nTa 98,20,41984\nMvaa 92,68,4\n" \
"Ra 32,27,32768\nMvaa 68,68,0\nTa 100,21,40960\nMvaa 72,72,0\nTa 102,21,41984\nMvaa 96,72,4\nRa 36,27,34816\nMvaa 100,76,4\n" \
"Ta 104,22,40960\nMvaa 76,76,0\nTa 106,22,41984\nMvaa 80,0,80\nRa 40,27,36864\nMvaa 104,4,80\nTa 108,23,40960\nMvaa 108,4,84\n" \
"Ta 110,23,41984\nMvaa 84,0,84\nRa 44,27,38912\nMvaa 132,8,84\nTa 112,22,49152\nMvaa 156,12,84\nTa 114,22,50176\nMvaa 152,12,80\n" \
"Ra 48,27,40960\nMvaa 128,8,80\nTa 116,23,49152\nMvaa 124,76,8\nTa 118,23,50176\nMvaa 148,76,12\nRa 52,27,43008\nMvaa 144,72,12\n" \
"Ra 56,27,45056\nMvaa 120,72,8\nRa 60,27,47104\nMvaa 116,68,8\nbuffer_store_dwordx4 v[36:39], v0, s[60:63], s95 offen offset:1024 sc1\nMvaa 140,68,12\nMvaa 136,64,12\nbuffer_store_dwordx4 v[40:43], v0, s[60:63], s95 offen offset:2048 sc1\n" \
"Mvaa 112,64,8\nMvaa 160,64,16\nMvaa 184,64,20\nMvaa 188,68,20\ns_waitcnt lgkmcnt(0)\ns_barrier\nMvaa 164,68,16\ns_add_u32 m0, s88, 32768\n" \
"Mvaa 168,72,16\nD 9,64,0,0\nMvaa 192,72,20\nMvaa 196,76,20\nbuffer_store_dwordx4 v[44:47], v0, s[60:63], s95 offen offset:3072 sc1\nMvaa 172,76,16\nMvaa 176,16,80\nD 9,64,s73,1024\n" \
"Mvaa 200,20,80\nMvaa 204,20,84\nMvaa 180,16,84\nMvaa 228,24,84\nD 9,64,s74,2048\nMvaa 252,28,84\nMvaa 248,28,80\nMvaa 224,24,80\n" \
"Mvaa 220,76,24\nD 9,64,s75,3072\nMvaa 244,76,28\nMvaa 240,72,28\nMvaa 216,72,24\ns_add_u32 m0, s88, 36864\nMvaa 212,68,24\nD 9,64,s76,0\n" \
"Mvaa 236,68,28\nMvaa 232,64,28\nMvaa 208,64,24\nMvaa 64,96,32\nD 9,64,s77,1024\nMvaa 88,96,36\nMvaa 92,100,36\ns_mov_b32 s96, 4\n" \
"Mvaa 68,100,32\nMvaa 72,104,32\nD 9,64,s78,2048\nMvaa 96,104,36\nMvaa 100,108,36\nMvaa 76,108,32\nMvaa 80,32,112\nD 9,64,s79,3072\n" \
"Mvaa 104,36,112\nMvaa 108,36,116\nMvaa 84,32,116\ns_add_u32 m0, s32, 98304\nMvaa 132,40,116\nD 10,68,0,0\nMvaa 156,44,116\nMvaa 152,44,112\n" \
"Mvaa 128,40,112\nMvaa 124,108,40\nD 10,68,s89,1024\nMvaa 148,108,44\nMvaa 144,104,44\nMvaa 120,104,40\nMvaa 116,100,40\nD 11,68,s90,2048\n" \
"Mvaa 140,100,44\nMvaa 136,96,44\ns_waitcnt vmcnt(10)\ns_barrier\nMvaa 112,96,40\nTa 64,20,0\nMvaa 160,96,48\nD 11,68,s91,3072\n" \
"Ta 66,20,1024\nMvaa 184,96,52\nRa 0,7,0\nbuffer_store_dwordx2 v[2:3], off, s[60:63], s35 sc1\nMvaa 188,100,52\nTa 68,21,0\nMvaa 164,100,48\ns_add_u32 m0, s92, 114688\n" \
"Ta 70,21,1024\nMvaa 168,104,48\nD 12,68,0,0\nRa 4,7,2048\nMvaa 192,104,52\nTa 72,22,0\nMvaa 196,108,52\nTa 74,22,1024\n" \
"Mvaa 172,108,48\nRa 8,7,4096\nMvaa 176,48,112\nD 12,68,s89,1024\nTa 76,23,0\nMvaa 200,52,112\nTa 78,23,1024\nv_add_u32 v9, 128, v9\n" \
"Mvaa 204,52,116\nRa 12,7,6144\nv_add_u32 v10, s72, v10\nMvaa 180,48,116\nTa 80,20,16384\nv_add_u32 v11, s72, v11\nMvaa 228,56,116\nTa 82,20,17408\n" \
"v_add_u32 v12, s72, v12\nMvaa 252,60,116\nRa 16,7,8192\nMvaa 248,60,112\nTa 84,21,16384\nMvaa 224,56,112\nTa 86,21,17408\nMvaa 220,108,56\n" \
"Ra 20,7,10240\nMvaa 244,108,60\nRa 24,7,12288\nMvaa 240,104,60\nRa 28,7,14336\nMvaa 216,104,56\nMvaa 212,100,56\nMvaa 236,100,60\n" \
"Mvaa 232,96,60\nMvaa 208,96,56\ns_waitcnt lgkmcnt(0)\ns_branch 99f\n.p2align 12\nzp29:\ns_setpc_b64 s[98:99]\n99:\n" \
"s_nop 0\nzal:\nMvaa 64,64,0\nTa 96,20,8192\nMvaa 88,64,4\nTa 98,20,9216\nMvaa 92,68,4\nRa 32,27,0\n" \
"Mvaa 68,68,0\nTa 100,21,8192\nMvaa 72,72,0\nTa 102,21,9216\nMvaa 96,72,4\nRa 36,27,2048\nMvaa 100,76,4\nTa 104,22,8192\n" \
"Mvaa 76,76,0\nTa 106,22,9216\nMvaa 80,0,80\nRa 40,27,4096\nMvaa 104,4,80\nTa 108,23,8192\nMvaa 108,4,84\nTa 110,23,9216\n" \
"Mvaa 84,0,84\nRa 44,27,6144\nMvaa 132,8,84\nTa 112,22,16384\nMvaa 156,12,84\nTa 114,22,17408\nMvaa 152,12,80\nRa 48,27,8192\n" \
"Mvaa 128,8,80\nTa 116,23,16384\nMvaa 124,76,8\nTa 118,23,17408\nMvaa 148,76,12\nRa 52,27,10240\nMvaa 144,72,12\nRa 56,27,12288\n" \
"Mvaa 120,72,8\nRa 60,27,14336\nMvaa 116,68,8\nMvaa 140,68,12\nMvaa 136,64,12\nMvaa 112,64,8\nMvaa 160,64,16\nMvaa 184,64,20\n" \
"Mvaa 188,68,20\ns_waitcnt lgkmcnt(0)\ns_barrier\nMvaa 164,68,16\ns_add_u32 m0, s88, 0\nMvaa 168,72,16\nD 9,64,0,0\nMvaa 192,72,20\n" \
"Mvaa 196,76,20\nMvaa 172,76,16\nMvaa 176,16,80\nD 9,64,s73,1024\nMvaa 200,20,80\nMvaa 204,20,84\nMvaa 180,16,84\nMvaa 228,24,84\n" \
"D 9,64,s74,2048\nMvaa 252,28,84\nMvaa 248,28,80\nMvaa 224,24,80\nMvaa 220,76,24\nD 9,64,s75,3072\nMvaa 244,76,28\nMvaa 240,72,28\n" \
"Mvaa 216,72,24\ns_add_u32 m0, s88, 4096\nMvaa 212,68,24\nD 9,64,s76,0\nMvaa 236,68,28\nMvaa 232,64,28\nMvaa 208,64,24\nMvaa 64,96,32\n" \
"D 9,64,s77,1024\nMvaa 88,96,36\nMvaa 92,100,36\nMvaa 68,100,32\nMvaa 72,104,32\nD 9,64,s78,2048\nMvaa 96,104,36\nMvaa 100,108,36\n" \
"Mvaa 76,108,32\nMvaa 80,32,112\nD 9,64,s79,3072\nMvaa 104,36,112\nMvaa 108,36,116\nMvaa 84,32,116\ns_add_u32 m0, s32, 65536\nMvaa 132,40,116\n" \
"D 10,68,0,0\nMvaa 156,44,116\nMvaa 152,44,112\nMvaa 128,40,112\nMvaa 124,108,40\nD 10,68,s89,1024\nMvaa 148,108,44\nMvaa 144,104,44\n" \
"Mvaa 120,104,40\nMvaa 116,100,40\nD 11,68,s90,2048\nMvaa 140,100,44\nMvaa 136,96,44\ns_waitcnt vmcnt(11)\ns_barrier\nMvaa 112,96,40\n" \
"Ta 64,20,32768\nMvaa 160,96,48\nD 11,68,s91,3072\nTa 66,20,33792\nMvaa 184,96,52\nRa 0,7,32768\nMvaa 188,100,52\nTa 68,21,32768\n" \
"Mvaa 164,100,48\ns_add_u32 m0, s92, 81920\nTa 70,21,33792\nMvaa 168,104,48\nD 12,68,0,0\nRa 4,7,34816\nMvaa 192,104,52\nTa 72,22,32768\n" \
"Mvaa 196,108,52\nTa 74,22,33792\nMvaa 172,108,48\nRa 8,7,36864\nMvaa 176,48,112\nD 12,68,s89,1024\nTa 76,23,32768\nMvaa 200,52,112\n" \
"Ta 78,23,33792\nv_add_u32 v9, 128, v9\nMvaa 204,52,116\nRa 12,7,38912\nv_add_u32 v10, s72, v10\nMvaa 180,48,116\nTa 80,20,49152\nv_add_u32 v11, s72, v11\n" \
"Mvaa 228,56,116\nTa 82,20,50176\nv_add_u32 v12, s72, v12\nMvaa 252,60,116\nRa 16,7,40960\nMvaa 248,60,112\nTa 84,21,49152\nMvaa 224,56,112\n" \
"Ta 86,21,50176\nMvaa 220,108,56\nRa 20,7,43008\nMvaa 244,108,60\nRa 24,7,45056\nMvaa 240,104,60\nRa 28,7,47104\nMvaa 216,104,56\n" \
"Mvaa 212,100,56\nMvaa 236,100,60\nMvaa 232,96,60\nMvaa 208,96,56\ns_waitcnt lgkmcnt(0)\nMvaa 64,64,0\nTa 96,20,40960\nMvaa 88,64,4\n" \
"Ta 98,20,41984\nMvaa 92,68,4\nRa 32,27,32768\nMvaa 68,68,0\nTa 100,21,40960\nMvaa 72,72,0\nTa 102,21,41984\nMvaa 96,72,4\n" \
"Ra 36,27,34816\nMvaa 100,76,4\nTa 104,22,40960\nMvaa 76,76,0\nTa 106,22,41984\nMvaa 80,0,80\nRa 40,27,36864\nMvaa 104,4,80\n" \
"Ta 108,23,40960\nMvaa 108,4,84\nTa 110,23,41984\nMvaa 84,0,84\nRa 44,27,38912\nMvaa 132,8,84\nTa 112,22,49152\nMvaa 156,12,84\n" \
"Ta 114,22,50176\nMvaa 152,12,80\nRa 48,27,40960\nMvaa 128,8,80\nTa 116,23,49152\nMvaa 124,76,8\nTa 118,23,50176\nMvaa 148,76,12\n" \
"Ra 52,27,43008\nMvaa 144,72,12\nRa 56,27,45056\nMvaa 120,72,8\nRa 60,27,47104\nMvaa 116,68,8\nMvaa 140,68,12\nMvaa 136,64,12\n" \
"Mvaa 112,64,8\nMvaa 160,64,16\nMvaa 184,64,20\nMvaa 188,68,20\ns_waitcnt lgkmcnt(0)\ns_barrier\nMvaa 164,68,16\ns_add_u32 m0, s88, 32768\n" \
"Mvaa 168,72,16\nD 9,64,0,0\nMvaa 192,72,20\nMvaa 196,76,20\nMvaa 172,76,16\nMvaa 176,16,80\nD 9,64,s73,1024\nMvaa 200,20,80\n" \
"Mvaa 204,20,84\nMvaa 180,16,84\nMvaa 228,24,84\nD 9,64,s74,2048\nMvaa 252,28,84\nMvaa 248,28,80\nMvaa 224,24,80\nMvaa 220,76,24\n" \
"D 9,64,s75,3072\nMvaa 244,76,28\nMvaa 240,72,28\nMvaa 216,72,24\ns_add_u32 m0, s88, 36864\nMvaa 212,68,24\nD 9,64,s76,0\nMvaa 236,68,28\n" \
"Mvaa 232,64,28\nMvaa 208,64,24\nMvaa 64,96,32\nD 9,64,s77,1024\nMvaa 88,96,36\nMvaa 92,100,36\nMvaa 68,100,32\nMvaa 72,104,32\n" \
"D 9,64,s78,2048\nMvaa 96,104,36\nMvaa 100,108,36\nMvaa 76,108,32\nMvaa 80,32,112\nD 9,64,s79,3072\nMvaa 104,36,112\nMvaa 108,36,116\n" \
"Mvaa 84,32,116\ns_add_u32 m0, s32, 98304\nMvaa 132,40,116\nD 10,68,0,0\nMvaa 156,44,116\nMvaa 152,44,112\nMvaa 128,40,112\nMvaa 124,108,40\n" \
"D 10,68,s89,1024\nMvaa 148,108,44\nMvaa 144,104,44\nMvaa 120,104,40\nMvaa 116,100,40\nD 11,68,s90,2048\nMvaa 140,100,44\nMvaa 136,96,44\n" \
"s_waitcnt vmcnt(11)\ns_barrier\nMvaa 112,96,40\nTa 64,20,0\nMvaa 160,96,48\nD 11,68,s91,3072\nTa 66,20,1024\nMvaa 184,96,52\n" \
"Ra 0,7,0\nMvaa 188,100,52\nTa 68,21,0\nMvaa 164,100,48\ns_add_u32 m0, s92, 114688\nTa 70,21,1024\nMvaa 168,104,48\nD 12,68,0,0\n" \
"Ra 4,7,2048\nMvaa 192,104,52\nTa 72,22,0\nMvaa 196,108,52\nTa 74,22,1024\nMvaa 172,108,48\nRa 8,7,4096\nMvaa 176,48,112\n" \
"D 12,68,s89,1024\nTa 76,23,0\nMvaa 200,52,112\nTa 78,23,1024\nv_add_u32 v9, 128, v9\nMvaa 204,52,116\nRa 12,7,6144\nv_add_u32 v10, s72, v10\n" \
"Mvaa 180,48,116\nTa 80,20,16384\nv_add_u32 v11, s72, v11\nMvaa 228,56,116\nTa 82,20,17408\nv_add_u32 v12, s72, v12\nMvaa 252,60,116\nRa 16,7,8192\n" \
"Mvaa 248,60,112\nTa 84,21,16384\nMvaa 224,56,112\nTa 86,21,17408\nMvaa 220,108,56\nRa 20,7,10240\nMvaa 244,108,60\nRa 24,7,12288\n" \
"Mvaa 240,104,60\nRa 28,7,14336\nMvaa 216,104,56\ns_sub_u32 s96, s96, 1\nMvaa 212,100,56\ns_cmp_lg_u32 s96, 0\nMvaa 236,100,60\nMvaa 232,96,60\n" \
"Mvaa 208,96,56\ns_waitcnt lgkmcnt(0)\ns_cbranch_scc1 zal\nMvaa 64,64,0\nTa 96,20,8192\ns_mov_b64 s[64:65], s[84:85]\ns_mov_b32 s66, 4194304\nMvaa 88,64,4\n" \
"Ta 98,20,9216\ns_mov_b64 s[68:69], s[86:87]\ns_mov_b32 s70, 8388608\nMvaa 92,68,4\nRa 32,27,0\ns_mov_b32 s73, 15360\ns_mov_b32 s74, 129024\nMvaa 68,68,0\n" \
"Ta 100,21,8192\ns_mov_b32 s75, 144384\ns_add_u32 s76, s37, 0\nMvaa 72,72,0\nTa 102,21,9216\ns_add_u32 s77, s37, 15360\ns_add_u32 s78, s37, 129024\nMvaa 96,72,4\n" \
"Ra 36,27,2048\ns_add_u32 s79, s37, 144384\ns_mov_b32 s89, 31744\nMvaa 100,76,4\nTa 104,22,8192\ns_mov_b32 s90, 63488\ns_mov_b32 s91, 95232\nMvaa 76,76,0\n" \
"Ta 106,22,9216\ns_mov_b32 s72, 524288\nv_xor_b32 v28, 128, v14\nMvaa 80,0,80\nRa 40,27,4096\nMvaa 104,4,80\nTa 108,23,8192\nMvaa 108,4,84\n" \
"Ta 110,23,9216\nMvaa 84,0,84\nRa 44,27,6144\nMvaa 132,8,84\nTa 112,22,16384\nMvaa 156,12,84\nTa 114,22,17408\nMvaa 152,12,80\n" \
"Ra 48,27,8192\nMvaa 128,8,80\nTa 116,23,16384\nMvaa 124,76,8\nTa 118,23,17408\nMvaa 148,76,12\nRa 52,27,10240\nMvaa 144,72,12\n" \
"Ra 56,27,12288\nMvaa 120,72,8\nRa 60,27,14336\nMvaa 116,68,8\nMvaa 140,68,12\nMvaa 136,64,12\nMvaa 112,64,8\nMvaa 160,64,16\n" \
"Mvaa 184,64,20\nMvaa 188,68,20\ns_waitcnt lgkmcnt(0)\ns_barrier\nMvaa 164,68,16\ns_add_u32 m0, s88, 131072\nMvaa 168,72,16\nD 13,64,0,0\n" \
"Mvaa 192,72,20\nMvaa 196,76,20\nMvaa 172,76,16\nMvaa 176,16,80\nD 13,64,s73,1024\nMvaa 200,20,80\nMvaa 204,20,84\nMvaa 180,16,84\n" \
"Mvaa 228,24,84\nD 13,64,s74,2048\nMvaa 252,28,84\nMvaa 248,28,80\nMvaa 224,24,80\nMvaa 220,76,24\nD 13,64,s75,3072\nMvaa 244,76,28\n" \
"Mvaa 240,72,28\nMvaa 216,72,24\ns_add_u32 m0, s88, 135168\nMvaa 212,68,24\nD 13,64,s76,0\nMvaa 236,68,28\nMvaa 232,64,28\nMvaa 208,64,24\n" \
"Mvaa 64,96,32\nD 13,64,s77,1024\nMvaa 88,96,36\nMvaa 92,100,36\nMvaa 68,100,32\nMvaa 72,104,32\nD 13,64,s78,2048\nMvaa 96,104,36\n" \
"Mvaa 100,108,36\nMvaa 76,108,32\nMvaa 80,32,112\nD 13,64,s79,3072\nMvaa 104,36,112\nMvaa 108,36,116\nMvaa 84,32,116\ns_add_u32 m0, s32, 81920\n" \
"Mvaa 132,40,116\nD 14,68,0,0\nMvaa 156,44,116\nMvaa 152,44,112\nMvaa 128,40,112\nMvaa 124,108,40\nD 14,68,s89,1024\nMvaa 148,108,44\n" \
"Mvaa 144,104,44\nMvaa 120,104,40\nMvaa 116,100,40\nD 28,68,s90,2048\nMvaa 140,100,44\nMvaa 136,96,44\ns_waitcnt vmcnt(11)\ns_barrier\n" \
"Mvaa 112,96,40\nTa 64,20,32768\nMvaa 160,96,48\nD 28,68,s91,3072\nTa 66,20,33792\nMvaa 184,96,52\nRa 0,7,32768\nMvaa 188,100,52\n" \
"Ta 68,21,32768\nMvaa 164,100,48\ns_add_u32 m0, s88, 0\nv_add_u32 v13, 128, v13\nTa 70,21,33792\nMvaa 168,104,48\nD 13,64,0,0\nRa 4,7,34816\n" \
"Mvaa 192,104,52\nTa 72,22,32768\nMvaa 196,108,52\nTa 74,22,33792\nMvaa 172,108,48\nRa 8,7,36864\nMvaa 176,48,112\nD 13,64,s73,1024\n" \
"Ta 76,23,32768\nMvaa 200,52,112\nTa 78,23,33792\nMvaa 204,52,116\nRa 12,7,38912\nMvaa 180,48,116\nTa 80,20,49152\nMvaa 228,56,116\n" \
"D 13,64,s74,2048\nTa 82,20,50176\nMvaa 252,60,116\nRa 16,7,40960\nMvaa 248,60,112\nTa 84,21,49152\nMvaa 224,56,112\nTa 86,21,50176\n" \
"Mvaa 220,108,56\nD 13,64,s75,3072\nRa 20,7,43008\nMvaa 244,108,60\nRa 24,7,45056\nMvaa 240,104,60\nRa 28,7,47104\nMvaa 216,104,56\n" \
"Mvaa 212,100,56\nMvaa 236,100,60\nMvaa 232,96,60\nMvaa 208,96,56\ns_waitcnt lgkmcnt(0)\nMvaa 64,64,0\nTa 96,20,40960\nMvaa 88,64,4\n" \
"Ta 98,20,41984\nMvaa 92,68,4\nRa 32,27,32768\nMvaa 68,68,0\nTa 100,21,40960\nMvaa 72,72,0\nTa 102,21,41984\nMvaa 96,72,4\n" \
"Ra 36,27,34816\nMvaa 100,76,4\nTa 104,22,40960\nMvaa 76,76,0\nTa 106,22,41984\nMvaa 80,0,80\nRa 40,27,36864\ns_branch 99f\n" \
".p2align 12\nzp30:\ns_setpc_b64 s[98:99]\n99:\nMvaa 104,4,80\nTa 108,23,40960\nMvaa 108,4,84\nTa 110,23,41984\n" \
"Mvaa 84,0,84\nRa 44,27,38912\nMvaa 132,8,84\nTa 112,22,49152\nMvaa 156,12,84\nTa 114,22,50176\nMvaa 152,12,80\nRa 48,27,40960\n" \
"Mvaa 128,8,80\nTa 116,23,49152\nMvaa 124,76,8\nTa 118,23,50176\nMvaa 148,76,12\nRa 52,27,43008\nMvaa 144,72,12\nRa 56,27,45056\n" \
"s_and_b32 s96, s32, 0x1000\ns_lshl_b32 s97, s96, 1\nMvaa 120,72,8\nRa 60,27,47104\ns_lshl_b32 s96, s96, 2\nv_add_u32 v16, s97, v7\nMvaa 116,68,8\nv_subrev_u32 v20, s96, v8\n" \
"v_xor_b32 v17, 64, v16\nv_add_u32 v18, 0x20000, v16\nMvaa 140,68,12\nv_add_u32 v19, 0x20000, v17\nv_xor_b32 v21, 32, v20\nv_xor_b32 v22, 64, v20\nMvaa 136,64,12\nv_xor_b32 v23, 96, v20\n" \
"v_xor_b32 v24, 128, v20\nv_xor_b32 v25, 160, v20\nMvaa 112,64,8\nv_xor_b32 v26, 192, v20\nv_xor_b32 v27, 224, v20\nMvaa 160,64,16\nMvaa 184,64,20\nMvaa 188,68,20\n" \
"s_waitcnt vmcnt(4) lgkmcnt(0)\ns_barrier\nMvaa 164,68,16\ns_add_u32 m0, s88, 4096\nMvaa 168,72,16\nD 13,64,s76,0\nMvaa 192,72,20\nTa 160,20,24576\n" \
"Ta 162,20,25600\nMvaa 196,76,20\nTa 164,21,24576\nMvaa 172,76,16\nD 13,64,s77,1024\nMvaa 176,16,80\nTa 166,21,25600\nTa 168,22,24576\n" \
"Mvaa 200,20,80\nTa 170,22,25600\nMvaa 204,20,84\nD 13,64,s78,2048\nMvaa 180,16,84\nTa 172,23,24576\nTa 174,23,25600\nMvaa 228,24,84\n" \
"Ta 176,24,24576\nMvaa 252,28,84\nD 13,64,s79,3072\nMvaa 248,28,80\nTa 178,24,25600\nTa 180,25,24576\nMvaa 224,24,80\ns_add_u32 m0, s32, 65536\n" \
"v_add_u32 v14, s72, v14\nv_add_u32 v28, s72, v28\nMvaa 220,76,24\nD 14,68,0,0\nMvaa 244,76,28\nTa 182,25,25600\nMvaa 240,72,28\nTa 184,26,24576\n" \
"Ta 186,26,25600\nMvaa 216,72,24\nD 14,68,s89,1024\nMvaa 212,68,24\nTa 188,27,24576\nMvaa 236,68,28\nTa 190,27,25600\nMvaa 232,64,28\n" \
"D 28,68,s90,2048\nMvaa 208,64,24\nMvaa 112,96,40\nTa 64,20,16384\nMvaa 116,100,40\nD 28,68,s91,3072\nMvaa 120,104,40\nTa 66,20,17408\n" \
"Mvaa 124,108,40\ns_add_u32 m0, s88, 32768\nv_add_u32 v13, 128, v13\nMvaa 128,40,112\nD 13,64,0,0\nMvaa 132,40,116\nRa 0,18,0\nMvaa 136,96,44\n" \
"Ra 16,19,0\nMvaa 140,100,44\nD 13,64,s73,1024\nMvaa 144,104,44\nTa 68,21,16384\nMvaa 148,108,44\nTa 70,21,17408\nC 112,112,113\n" \
"C 113,114,115\nMvaa 152,44,112\nD 13,64,s74,2048\nC 114,116,117\nMvaa 156,44,116\nTa 72,22,16384\nC 115,118,119\nC 120,120,121\n" \
"Mvaa 160,96,48\nC 121,122,123\nC 122,124,125\nC 123,126,127\nMvaa 164,100,48\nD 13,64,s75,3072\nC 38,128,129\nMvaa 168,104,48\n" \
"Ta 74,22,17408\nC 39,130,131\nC 56,132,133\nMvaa 172,108,48\ns_add_u32 m0, s88, 36864\nTa 76,23,16384\nC 57,134,135\nMvaa 176,48,112\n" \
"D 13,64,s76,0\nAW 200,112\nMvaa 180,48,116\nTa 78,23,17408\nAW 201,113\nAW 202,114\nMvaa 184,96,52\nTa 80,24,16384\n" \
"AW 203,115\nAW 232,120\nMvaa 188,100,52\nD 13,64,s77,1024\nAW 233,121\nMvaa 192,104,52\nAW 234,122\nAW 235,123\n" \
"C 136,136,137\nMvaa 196,108,52\nTa 82,24,17408\nC 137,138,139\nC 138,140,141\nMvaa 200,52,112\nD 13,64,s78,2048\nC 139,142,143\n" \
"Mvaa 204,52,116\nTa 84,25,16384\nC 144,144,145\nC 145,146,147\nMvaa 64,96,32\nTa 86,25,17408\nC 146,148,149\nC 147,150,151\n" \
"Mvaa 68,100,32\nD 13,64,s79,3072\nC 40,152,153\nMvaa 72,104,32\nTa 88,26,16384\nC 41,154,155\nC 58,156,157\nMvaa 76,108,32\n" \
"s_add_u32 m0, s32, 98304\nv_add_u32 v14, s72, v14\nv_add_u32 v28, s72, v28\nMvaa 80,32,112\nD 14,68,0,0\nC 59,158,159\nMvaa 84,32,116\nTa 90,26,17408\n" \
"AW 204,136\nAW 205,137\nMvaa 208,96,56\nTa 92,27,16384\nAW 206,138\nAW 207,139\nMvaa 212,100,56\nD 14,68,s89,1024\n" \
"AW 236,144\nMvaa 216,104,56\nTa 94,27,17408\nAW 237,145\nAW 238,146\nMvaa 220,108,56\nAW 239,147\nC 160,160,161\n" \
"C 161,162,163\nMvaa 224,56,112\nD 28,68,s90,2048\nC 162,164,165\nMvaa 228,56,116\nRa 4,18,2048\nC 163,166,167\nC 168,168,169\n" \
"Mvaa 232,96,60\nRa 20,19,2048\nC 169,170,171\nC 170,172,173\nMvaa 236,100,60\nD 28,68,s91,3072\nC 171,174,175\nMvaa 240,104,60\n" \
"Ra 8,18,4096\nC 42,176,177\nC 43,178,179\nMvaa 244,108,60\nRa 24,19,4096\nC 60,180,181\nC 61,182,183\nMvaa 248,60,112\n" \
"Ra 12,18,6144\nAW 208,160\nAW 209,161\nMvaa 252,60,116\nRa 28,19,6144\nAW 210,162\nAW 211,163\nMvaa 88,96,36\n" \
"AW 240,168\nAW 241,169\nAW 242,170\nMvaa 92,100,36\nAW 243,171\nC 184,184,185\nC 185,186,187\nMvaa 96,104,36\n" \
"C 186,188,189\nC 187,190,191\nC 192,192,193\nMvaa 100,108,36\nC 193,194,195\nC 194,196,197\nC 195,198,199\nMvaa 104,36,112\n" \
"C 44,200,201\nC 45,202,203\nC 62,204,205\nMvaa 108,36,116\nC 63,206,207\nAW 212,184\nAW 213,185\ns_waitcnt lgkmcnt(6)\n" \
"Zvaa 128,64,0\nv_add_u32 v13, 128, v13\nAW 214,186\nAW 215,187\nMvaa 128,160,16\ns_add_u32 m0, s88, 131072\nAW 244,192\nAW 245,193\n" \
"Zvaa 132,164,16\nD 13,64,0,0\nAW 246,194\nMvaa 132,68,0\nv_add_u32 v14, s72, v14\nAW 247,195\nC 64,64,65\nZvaa 136,72,0\n" \
"v_add_u32 v28, s72, v28\nC 65,66,67\nC 66,68,69\nMvaa 136,168,16\nC 67,70,71\nC 72,72,73\nC 73,74,75\nZvaa 140,172,16\n" \
"C 74,76,77\nC 75,78,79\nC 34,80,81\nMvaa 140,76,0\ns_waitcnt lgkmcnt(0)\nD 13,64,s73,1024\nZvaa 144,80,0\nC 35,82,83\n" \
"C 52,84,85\nC 53,86,87\nMvaa 144,176,16\nAW 192,64\nAW 193,65\nAW 194,66\nZvaa 148,180,16\nAW 195,67\n" \
"AW 224,72\nAW 225,73\nMvaa 148,84,0\nAW 226,74\nAW 227,75\nC 208,208,209\nZvaa 152,88,0\nD 13,64,s74,2048\n" \
"C 209,210,211\nMvaa 152,184,16\nC 210,212,213\nC 211,214,215\nC 216,216,217\nZvaa 156,188,16\nC 217,218,219\nC 218,220,221\n" \
"C 219,222,223\nMvaa 156,92,0\nC 46,224,225\nC 47,226,227\nC 64,228,229\nZvaa 188,92,4\ns_waitcnt vmcnt(15)\ns_barrier\n" \
"C 65,230,231\nMvaa 188,188,20\nTa 96,20,0\nTa 98,20,1024\nAW 216,208\nZvaa 184,184,20\nD 13,64,s75,3072\nAW 217,209\n" \
"Mvaa 184,88,4\nTa 128,20,8192\nTa 130,20,9216\nAW 218,210\nZvaa 180,84,4\nAW 219,211\nAW 248,216\nAW 249,217\n" \
"Mvaa 180,180,20\nRa 32,16,0\nRa 48,17,0\nAW 250,218\nZvaa 176,176,20\ns_add_u32 m0, s88, 135168\nTa 100,21,0\nAW 251,219\n" \
"Mvaa 176,80,4\nD 13,64,s76,0\nC 232,232,233\nZvaa 172,76,4\nTa 102,21,1024\nTa 132,21,8192\nC 233,234,235\nMvaa 172,172,20\n" \
"C 234,236,237\nC 235,238,239\nC 240,240,241\nZvaa 168,168,20\nTa 134,21,9216\nTa 104,22,0\nC 241,242,243\nMvaa 168,72,4\n" \
"Ta 106,22,1024\nTa 136,22,8192\nC 242,244,245\nZvaa 164,68,4\nD 13,64,s77,1024\nC 243,246,247\nMvaa 164,164,20\nC 48,248,249\n" \
"C 49,250,251\nC 66,252,253\nZvaa 160,160,20\nTa 138,22,9216\nTa 108,23,0\nC 67,254,255\nMvaa 160,64,4\nAW 220,232\n" \
"AW 221,233\nAW 222,234\nZvaa 192,64,8\nTa 110,23,1024\nTa 140,23,8192\nAW 223,235\nMvaa 192,160,24\nD 13,64,s78,2048\n" \
"AW 252,240\nZvaa 196,164,24\nTa 142,23,9216\nTa 112,24,0\nAW 253,241\nMvaa 196,68,8\nAW 254,242\nAW 255,243\n" \
"C 88,88,89\nZvaa 200,72,8\nTa 114,24,1024\nTa 144,24,8192\nC 89,90,91\nMvaa 200,168,24\nTa 146,24,9216\nTa 116,25,0\n" \
"C 90,92,93\nZvaa 204,172,24\nD 13,64,s79,3072\nC 91,94,95\nMvaa 204,76,8\nC 96,96,97\nC 97,98,99\nC 98,100,101\n" \
"Zvaa 208,80,8\nTa 118,25,1024\nTa 148,25,8192\nC 99,102,103\nMvaa 208,176,24\nv_add_u32 v13, 128, v13\nTa 150,25,9216\nC 36,104,105\n" \
"Zvaa 212,180,24\ns_add_u32 m0, s32, 81920\nTa 120,26,0\nC 37,106,107\nMvaa 212,84,8\nD 14,68,0,0\nC 54,108,109\nZvaa 216,88,8\n" \
"Ta 122,26,1024\nTa 152,26,8192\nC 55,110,111\nMvaa 216,184,24\nAW 196,88\nAW 197,89\nAW 198,90\nZvaa 220,188,24\n" \
"Ta 154,26,9216\nTa 124,27,0\nAW 199,91\nMvaa 220,92,8\nAW 228,96\nAW 229,97\nAW 230,98\nZvaa 252,92,12\n" \
"D 14,68,s89,1024\nAW 231,99\nMvaa 252,188,28\nTa 126,27,1024\nTa 156,27,8192\nZvaa 248,184,28\nTa 158,27,9216\nRa 36,16,2048\n" \
"Mvaa 248,88,12\nZvaa 244,84,12\nRa 52,17,2048\nRa 40,16,4096\nMvaa 244,180,28\nD 28,68,s90,2048\nZvaa 240,176,28\nRa 56,17,4096\n" \
"Ra 44,16,6144\nMvaa 240,80,12\nZvaa 236,76,12\nRa 60,17,6144\nMvaa 236,172,28\nZvaa 232,168,28\nD 28,68,s91,3072\nMvaa 232,72,12\n" \
"Zvaa 228,68,12\nMvaa 228,164,28\nv_add_u32 v14, s72, v14\nZvaa 224,160,28\nv_add_u32 v28, s72, v28\nMvaa 224,64,12\ns_waitcnt lgkmcnt(6)\nMvaa 128,96,32\n" \
"buffer_store_dwordx4 a[252:255], v0, s[60:63], s46 offen sc1\nMvaa 128,128,48\ns_add_u32 m0, s88, 0\nMvaa 132,132,48\nD 13,64,0,0\nMvaa 132,100,32\nbuffer_store_dwordx2 v[48:49], v0, s[60:63], s46 offen offset:1024 sc1\nMvaa 136,104,32\n" \
"buffer_store_dwordx2 v[66:67], v0, s[60:63], s46 offen offset:1032 sc1\nMvaa 136,136,48\nv_mov_b32 v100, 0\nv_bfe_u32 v104, v0, 4, 4\nMvaa 140,140,48\nv_lshrrev_b32 v105, 6, v0\nv_and_b32 v105, 12, v105\nMvaa 140,108,32\n" \
"s_waitcnt lgkmcnt(0)\nD 13,64,s73,1024\nMvaa 144,112,32\nv_sub_u32 v104, v104, v105\nv_mov_b32 v107, 0xff800000\nMvaa 144,144,48\ns_mov_b32 s93, 0x3e8293ee\nv_subrev_u32 v105, 0, v104\n" \
"Mvaa 148,148,48\nv_and_b32 v106, 0x80000001, v105\nv_cmp_eq_u32 vcc, 0, v106\nMvaa 148,116,32\nv_mov_b32 v84, 0x3f803f80\ns_nop 0\nMvaa 152,120,32\nD 13,64,s74,2048\n" \
"Mvaa 152,152,48\nv_cndmask_b32 v76, v107, v100, vcc\nv_cmp_eq_u32 vcc, 0x80000000, v106\nMvaa 156,156,48\ns_nop 1\nv_cndmask_b32 v72, v107, v100, vcc\nMvaa 156,124,32\nv_subrev_u32 v105, 1, v104\n" \
"v_and_b32 v106, 0x80000001, v105\nMvaa 188,124,36\ns_waitcnt vmcnt(18)\ns_barrier\nMvaa 188,156,52\nTa 64,20,32768\nTa 66,20,33792\nMvaa 184,152,52\n" \
"D 13,64,s75,3072\nMvaa 184,120,36\nTa 160,20,40960\nTa 162,20,41984\nMvaa 180,116,36\nv_cmp_eq_u32 vcc, 0, v106\ns_branch 99f\n.p2align 12\n" \
"zp31:\ns_setpc_b64 s[98:99]\n99:\nv_mov_b32 v85, 0x3f803f80\nMvaa 180,148,52\nRa 0,16,32768\nRa 16,17,32768\nMvaa 176,144,52\n" \
"s_add_u32 m0, s88, 4096\nTa 68,21,32768\nMvaa 176,112,36\nD 13,64,s76,0\nMvaa 172,108,36\nTa 70,21,33792\nTa 164,21,40960\nMvaa 172,140,52\n" \
"s_nop 0\nv_cndmask_b32 v77, v107, v100, vcc\nMvaa 168,136,52\nTa 166,21,41984\nTa 72,22,32768\nMvaa 168,104,36\nTa 74,22,33792\nTa 168,22,40960\n" \
"Mvaa 164,100,36\nD 13,64,s77,1024\nMvaa 164,132,52\nv_cmp_eq_u32 vcc, 0x80000000, v106\ns_nop 1\nMvaa 160,128,52\nTa 170,22,41984\nTa 76,23,32768\n" \
"Mvaa 160,96,36\nv_cndmask_b32 v73, v107, v100, vcc\nv_subrev_u32 v105, 2, v104\nMvaa 192,96,40\nTa 78,23,33792\nTa 172,23,40960\nMvaa 192,128,56\nD 13,64,s78,2048\n" \
"Mvaa 196,132,56\nTa 174,23,41984\nTa 80,24,32768\nMvaa 196,100,40\nv_and_b32 v106, 0x80000001, v105\nv_cmp_eq_u32 vcc, 0, v106\nMvaa 200,104,40\nTa 82,24,33792\n" \
"Ta 176,24,40960\nMvaa 200,136,56\nTa 178,24,41984\nTa 84,25,32768\nMvaa 204,140,56\nD 13,64,s79,3072\nMvaa 204,108,40\nv_mov_b32 v86, 0x3f803f80\n" \
"s_nop 0\nMvaa 208,112,40\nTa 86,25,33792\nTa 180,25,40960\nMvaa 208,144,56\nv_add_u32 v13, 128, v13\nTa 182,25,41984\nMvaa 212,148,56\n" \
"s_add_u32 m0, s32, 65536\nTa 88,26,32768\nMvaa 212,116,40\nD 14,68,0,0\nMvaa 216,120,40\nTa 90,26,33792\nTa 184,26,40960\nMvaa 216,152,56\n" \
"v_cndmask_b32 v78, v107, v100, vcc\nv_cmp_eq_u32 vcc, 0x80000000, v106\nMvaa 220,156,56\nTa 186,26,41984\nTa 92,27,32768\nMvaa 220,124,40\ns_nop 1\nv_cndmask_b32 v74, v107, v100, vcc\n" \
"Mvaa 252,124,44\nD 14,68,s89,1024\nMvaa 252,156,60\nTa 94,27,33792\nTa 188,27,40960\nMvaa 248,152,60\nTa 190,27,41984\nRa 4,16,34816\n" \
"Mvaa 248,120,44\nv_subrev_u32 v105, 3, v104\nv_and_b32 v106, 0x80000001, v105\nMvaa 244,116,44\nRa 20,17,34816\nRa 8,16,36864\nMvaa 244,148,60\nD 28,68,s90,2048\n" \
"Mvaa 240,144,60\nRa 24,17,36864\nRa 12,16,38912\nMvaa 240,112,44\nv_cmp_eq_u32 vcc, 0, v106\nv_mov_b32 v87, 0x3f803f80\nMvaa 236,108,44\nRa 28,17,38912\n" \
"s_nop 0\nMvaa 236,140,60\nv_cndmask_b32 v79, v107, v100, vcc\nv_cmp_eq_u32 vcc, 0x80000000, v106\nMvaa 232,136,60\nD 28,68,s91,3072\nMvaa 232,104,44\ns_nop 1\n" \
"v_cndmask_b32 v75, v107, v100, vcc\nMvaa 228,100,44\ns_cmp_eq_u32 s51, 0\ns_cselect_b64 vcc, -1, 0\nMvaa 228,132,60\nv_add_u32 v14, s72, v14\nv_cndmask_b32 v80, v107, v72, vcc\nMvaa 224,128,60\n" \
"v_add_u32 v28, s72, v28\nv_cndmask_b32 v81, v107, v73, vcc\nMvaa 224,96,44\ns_waitcnt lgkmcnt(6)\nv_cndmask_b32 v82, v107, v74, vcc\nMvaa 128,64,0\nv_cndmask_b32 v83, v107, v75, vcc\nQvaa 88,224,196,72\n" \
"Qvaa 92,228,196,76\nQvaa 108,228,200,72\nQvaa 112,232,200,76\nMvaa 128,160,16\ns_add_u32 m0, s88, 32768\nMvaa 132,164,16\nD 13,64,0,0\nMvaa 132,68,0\n" \
"Mvaa 136,72,0\nv_max3_f32 v104, v88, v89, v90\nv_max3_f32 v105, v91, v92, v93\nMvaa 136,168,16\nv_max3_f32 v104, v104, v94, v95\nv_max_f32 v104, v104, v105\nMvaa 140,172,16\nv_mov_b32 v105, v104\n" \
"v_max3_f32 v124, v108, v109, v110\nMvaa 140,76,0\ns_waitcnt lgkmcnt(0)\nD 13,64,s73,1024\nMvaa 144,80,0\nv_max3_f32 v125, v111, v112, v113\nv_max3_f32 v124, v124, v114, v115\nMvaa 144,176,16\n" \
"v_max_f32 v124, v124, v125\nv_mov_b32 v125, v124\nMvaa 148,180,16\nv_permlane16_swap_b32 v104, v105\nv_permlane16_swap_b32 v124, v125\nMvaa 148,84,0\nv_max_f32 v104, v104, v105\nv_mov_b32 v105, v104\n" \
"Mvaa 152,88,0\nD 13,64,s74,2048\nMvaa 152,184,16\nv_max_f32 v124, v124, v125\nv_mov_b32 v125, v124\nMvaa 156,188,16\nv_permlane32_swap_b32 v104, v105\nv_permlane32_swap_b32 v124, v125\n" \
"Mvaa 156,92,0\nv_max_f32 v104, v104, v105\nv_mul_f32 v104, s93, v104\nMvaa 188,92,4\ns_waitcnt vmcnt(18)\ns_barrier\nMvaa 188,188,20\nTa 96,20,16384\n" \
"Ta 98,20,17408\nMvaa 184,184,20\nD 13,64,s75,3072\nMvaa 184,88,4\nTa 128,20,24576\nTa 130,20,25600\nMvaa 180,84,4\nv_fma_f32 v88, v88, s93, -v104\n" \
"v_fma_f32 v89, v89, s93, -v104\nMvaa 180,180,20\nRa 32,18,0\nRa 48,19,0\nMvaa 176,176,20\ns_add_u32 m0, s88, 36864\nTa 100,21,16384\nMvaa 176,80,4\n" \
"D 13,64,s76,0\nMvaa 172,76,4\nTa 102,21,17408\nTa 132,21,24576\nMvaa 172,172,20\nv_fma_f32 v90, v90, s93, -v104\nv_fma_f32 v91, v91, s93, -v104\nMvaa 168,168,20\n" \
"Ta 134,21,25600\nTa 104,22,16384\nMvaa 168,72,4\nTa 106,22,17408\nTa 136,22,24576\nMvaa 164,68,4\nD 13,64,s77,1024\nMvaa 164,164,20\n" \
"v_fma_f32 v92, v92, s93, -v104\nv_fma_f32 v93, v93, s93, -v104\nMvaa 160,160,20\nTa 138,22,25600\nTa 108,23,16384\nMvaa 160,64,4\nv_fma_f32 v94, v94, s93, -v104\nv_fma_f32 v95, v95, s93, -v104\n" \
"Mvaa 192,64,8\nTa 110,23,17408\nTa 140,23,24576\nMvaa 192,160,24\nD 13,64,s78,2048\nMvaa 196,164,24\nTa 142,23,25600\nTa 112,24,16384\n" \
"Mvaa 196,68,8\nE 88,88\nMvaa 200,72,8\nTa 114,24,17408\nTa 144,24,24576\nMvaa 200,168,24\nTa 146,24,25600\nTa 116,25,16384\n" \
"Mvaa 204,172,24\nD 13,64,s79,3072\nMvaa 204,76,8\nE 89,89\nMvaa 208,80,8\nTa 118,25,17408\nTa 148,25,24576\nMvaa 208,176,24\n" \
"v_add_u32 v13, 128, v13\nTa 150,25,25600\nMvaa 212,180,24\ns_add_u32 m0, s32, 98304\nTa 120,26,16384\nMvaa 212,84,8\nD 14,68,0,0\nMvaa 216,88,8\n" \
"Ta 122,26,17408\nTa 152,26,24576\nMvaa 216,184,24\nE 90,90\nMvaa 220,188,24\nTa 154,26,25600\nTa 124,27,16384\nMvaa 220,92,8\n" \
"C 96,88,89\nMvaa 252,92,12\nD 14,68,s89,1024\nMvaa 252,188,28\nTa 126,27,17408\nTa 156,27,24576\nMvaa 248,184,28\nTa 158,27,25600\n" \
"Ra 36,18,2048\nMvaa 248,88,12\nE 91,91\nMvaa 244,84,12\nRa 52,19,2048\nRa 40,18,4096\nMvaa 244,180,28\nD 28,68,s90,2048\n" \
"Mvaa 240,176,28\nRa 56,19,4096\nRa 44,18,6144\nMvaa 240,80,12\nE 92,92\nMvaa 236,76,12\nRa 60,19,6144\nC 97,90,91\n" \
"Mvaa 236,172,28\nE 93,93\nMvaa 232,168,28\nD 28,68,s91,3072\nMvaa 232,72,12\nE 94,94\nMvaa 228,68,12\nC 98,92,93\n" \
"Mvaa 228,164,28\nv_add_u32 v14, s72, v14\nMvaa 224,160,28\nv_add_u32 v28, s72, v28\nMvaa 224,64,12\ns_waitcnt lgkmcnt(6)\nMvaa 128,96,32\nE 95,95\n" \
"Mvaa 128,128,48\ns_add_u32 m0, s88, 131072\nv_max_f32 v124, v124, v125\nMvaa 132,132,48\nD 13,64,0,0\nMvaa 132,100,32\nv_mul_f32 v124, s93, v124\nv_fma_f32 v108, v108, s93, -v124\n" \
"Mvaa 136,104,32\nv_fma_f32 v109, v109, s93, -v124\nv_fma_f32 v110, v110, s93, -v124\nMvaa 136,136,48\nv_fma_f32 v111, v111, s93, -v124\nv_fma_f32 v112, v112, s93, -v124\nMvaa 140,140,48\nv_fma_f32 v113, v113, s93, -v124\n" \
"v_fma_f32 v114, v114, s93, -v124\nMvaa 140,108,32\ns_waitcnt lgkmcnt(0)\nD 13,64,s73,1024\nMvaa 144,112,32\nv_fma_f32 v115, v115, s93, -v124\nMvaa 144,144,48\nE 108,108\n" \
"Mvaa 148,148,48\nE 109,109\nMvaa 148,116,32\nE 110,110\nMvaa 152,120,32\nD 13,64,s74,2048\nMvaa 152,152,48\nC 116,108,109\n" \
"Mvaa 156,156,48\nE 111,111\nMvaa 156,124,32\nE 112,112\nMvaa 188,124,36\ns_waitcnt vmcnt(15)\ns_barrier\nMvaa 188,156,52\n" \
"Ta 64,20,0\nTa 66,20,1024\nMvaa 184,152,52\nD 13,64,s75,3072\nMvaa 184,120,36\nTa 160,20,8192\nTa 162,20,9216\nbuffer_store_dwordx2 v[2:3], off, s[60:63], s48 sc1\n" \
"Mvaa 180,116,36\nC 117,110,111\nMvaa 180,148,52\nRa 0,16,0\nRa 16,17,0\nMvaa 176,144,52\ns_add_u32 m0, s88, 135168\nTa 68,21,0\n" \
"Mvaa 176,112,36\nD 13,64,s76,0\nMvaa 172,108,36\nTa 70,21,1024\nTa 164,21,8192\nMvaa 172,140,52\nE 113,113\nMvaa 168,136,52\n" \
"Ta 166,21,9216\nTa 72,22,0\nMvaa 168,104,36\nTa 74,22,1024\nTa 168,22,8192\nMvaa 164,100,36\nD 13,64,s77,1024\nMvaa 164,132,52\n" \
"E 114,114\nMvaa 160,128,52\nTa 170,22,9216\nTa 76,23,0\nMvaa 160,96,36\nC 118,112,113\nMvaa 192,96,40\nTa 78,23,1024\n" \
"Ta 172,23,8192\nMvaa 192,128,56\nD 13,64,s78,2048\nMvaa 196,132,56\nTa 174,23,9216\nTa 80,24,0\nMvaa 196,100,40\nE 115,115\n" \
"Mvaa 200,104,40\nTa 82,24,1024\nTa 176,24,8192\nMvaa 200,136,56\nTa 178,24,9216\nTa 84,25,0\nMvaa 204,140,56\nD 13,64,s79,3072\n" \
"Mvaa 204,108,40\nC 99,94,95\nC 119,114,115\nMvaa 208,112,40\nTa 86,25,1024\nTa 180,25,8192\nZvvv 100,84,96\nZvvv 88,34,96\n" \
"Zvvv 92,52,96\nZvvv 120,84,116\nZvvv 108,36,116\nZvvv 112,54,116\nMvaa 208,144,56\nv_add_u32 v13, 128, v13\nTa 182,25,9216\nMvaa 212,148,56\n" \
"s_add_u32 m0, s32, 81920\nTa 88,26,0\nMvaa 212,116,40\nD 14,68,0,0\nMvaa 216,120,40\nTa 90,26,1024\nTa 184,26,8192\nMvaa 216,152,56\n" \
"v_rcp_f32 v104, v100\nMvaa 220,156,56\nTa 186,26,9216\nTa 92,27,0\nMvaa 220,124,40\nv_rcp_f32 v124, v120\nMvaa 252,124,44\nD 14,68,s89,1024\n" \
"Mvaa 252,156,60\nTa 94,27,1024\nTa 188,27,8192\nMvaa 248,152,60\nTa 190,27,9216\nRa 4,16,2048\nMvaa 248,120,44\nv_mul_f32 v88, v88, v104\n" \
"v_mul_f32 v89, v89, v104\nMvaa 244,116,44\nRa 20,17,2048\nRa 8,16,4096\nMvaa 244,148,60\nD 28,68,s90,2048\nMvaa 240,144,60\nRa 24,17,4096\n" \
"Ra 12,16,6144\nMvaa 240,112,44\nv_mul_f32 v90, v90, v104\nv_mul_f32 v91, v91, v104\nMvaa 236,108,44\nRa 28,17,6144\nv_mul_f32 v92, v92, v104\nMvaa 236,140,60\n" \
"v_mul_f32 v93, v93, v104\nv_mul_f32 v94, v94, v104\nMvaa 232,136,60\nD 28,68,s91,3072\nMvaa 232,104,44\nv_mul_f32 v95, v95, v104\nC 96,88,89\nMvaa 228,100,44\n" \
"C 97,90,91\nC 98,92,93\nMvaa 228,132,60\nv_add_u32 v14, s72, v14\nC 99,94,95\nMvaa 224,128,60\nv_add_u32 v28, s72, v28\nMvaa 224,96,44\n" \
"s_waitcnt lgkmcnt(6)\nMvaa 128,64,0\ns_add_u32 s95, s50, 2048\nbuffer_store_dwordx4 v[96:99], v0, s[60:63], s95 offen sc1\nMvaa 128,160,16\ns_add_u32 m0, s88, 0\nv_mul_f32 v108, v108, v124\nMvaa 132,164,16\n" \
"D 13,64,0,0\nMvaa 132,68,0\nv_mul_f32 v109, v109, v124\nv_mul_f32 v110, v110, v124\nMvaa 136,72,0\nv_mul_f32 v111, v111, v124\nv_mul_f32 v112, v112, v124\nMvaa 136,168,16\n" \
"v_mul_f32 v113, v113, v124\nv_mul_f32 v114, v114, v124\nMvaa 140,172,16\nv_mul_f32 v115, v115, v124\nC 116,108,109\nMvaa 140,76,0\ns_waitcnt lgkmcnt(0)\nD 13,64,s73,1024\n" \
"Mvaa 144,80,0\nC 117,110,111\nC 118,112,113\nMvaa 144,176,16\nC 119,114,115\nMvaa 148,180,16\ns_add_u32 s95, s50, 4096\nbuffer_store_dwordx4 v[116:119], v0, s[60:63], s95 offen sc1\n" \
"Mvaa 148,84,0\nQvaa 88,232,204,72\nQvaa 92,236,204,76\nQvaa 108,236,208,72\nQvaa 112,240,208,76\nMvaa 152,88,0\nD 13,64,s74,2048\nMvaa 152,184,16\n" \
"Mvaa 156,188,16\nMvaa 156,92,0\nMvaa 188,92,4\ns_waitcnt vmcnt(18)\ns_barrier\nMvaa 188,188,20\nTa 96,20,32768\nTa 98,20,33792\n" \
"Mvaa 184,184,20\nD 13,64,s75,3072\nMvaa 184,88,4\nTa 128,20,40960\nTa 130,20,41984\nMvaa 180,84,4\nv_max3_f32 v104, v88, v89, v90\nv_max3_f32 v105, v91, v92, v93\n" \
"Mvaa 180,180,20\nRa 32,16,32768\nRa 48,17,32768\nMvaa 176,176,20\ns_add_u32 m0, s88, 4096\nTa 100,21,32768\nMvaa 176,80,4\nD 13,64,s76,0\n" \
"Mvaa 172,76,4\nTa 102,21,33792\nTa 132,21,40960\nMvaa 172,172,20\nv_max3_f32 v104, v104, v94, v95\nv_max_f32 v104, v104, v105\nMvaa 168,168,20\nTa 134,21,41984\n" \
"Ta 104,22,32768\nMvaa 168,72,4\nTa 106,22,33792\nTa 136,22,40960\nMvaa 164,68,4\nD 13,64,s77,1024\nMvaa 164,164,20\nv_mov_b32 v105, v104\n" \
"v_max3_f32 v124, v108, v109, v110\nMvaa 160,160,20\nTa 138,22,41984\nTa 108,23,32768\nMvaa 160,64,4\nv_max3_f32 v125, v111, v112, v113\nv_max3_f32 v124, v124, v114, v115\nMvaa 192,64,8\n" \
"Ta 110,23,33792\nTa 140,23,40960\nMvaa 192,160,24\nD 13,64,s78,2048\nMvaa 196,164,24\nTa 142,23,41984\nTa 112,24,32768\nMvaa 196,68,8\n" \
"v_max_f32 v124, v124, v125\ns_branch 99f\n.p2align 12\nzp32:\ns_setpc_b64 s[98:99]\n99:\nv_mov_b32 v125, v124\nMvaa 200,72,8\n" \
"Ta 114,24,33792\nTa 144,24,40960\nMvaa 200,168,24\nTa 146,24,41984\nTa 116,25,32768\nMvaa 204,172,24\nD 13,64,s79,3072\nMvaa 204,76,8\n" \
"v_permlane16_swap_b32 v104, v105\nv_permlane16_swap_b32 v124, v125\nMvaa 208,80,8\nTa 118,25,33792\nTa 148,25,40960\nMvaa 208,176,24\nv_add_u32 v13, 128, v13\nTa 150,25,41984\n" \
"Mvaa 212,180,24\ns_add_u32 m0, s32, 65536\nTa 120,26,32768\nMvaa 212,84,8\nD 14,68,0,0\nMvaa 216,88,8\nTa 122,26,33792\nTa 152,26,40960\n" \
"Mvaa 216,184,24\nv_max_f32 v104, v104, v105\nv_mov_b32 v105, v104\nMvaa 220,188,24\nTa 154,26,41984\nTa 124,27,32768\nMvaa 220,92,8\nv_max_f32 v124, v124, v125\n" \
"v_mov_b32 v125, v124\nMvaa 252,92,12\nD 14,68,s89,1024\nMvaa 252,188,28\nTa 126,27,33792\nTa 156,27,40960\nMvaa 248,184,28\nTa 158,27,41984\n" \
"Ra 36,16,34816\nMvaa 248,88,12\nv_permlane32_swap_b32 v104, v105\nv_permlane32_swap_b32 v124, v125\nMvaa 244,84,12\nRa 52,17,34816\nRa 40,16,36864\nMvaa 244,180,28\n" \
"D 28,68,s90,2048\nMvaa 240,176,28\nRa 56,17,36864\nRa 44,16,38912\nMvaa 240,80,12\nv_max_f32 v104, v104, v105\nv_mul_f32 v104, s93, v104\nMvaa 236,76,12\n" \
"Ra 60,17,38912\nv_fma_f32 v88, v88, s93, -v104\nMvaa 236,172,28\nv_fma_f32 v89, v89, s93, -v104\nv_fma_f32 v90, v90, s93, -v104\nMvaa 232,168,28\nD 28,68,s91,3072\nMvaa 232,72,12\n" \
"v_fma_f32 v91, v91, s93, -v104\nv_fma_f32 v92, v92, s93, -v104\nMvaa 228,68,12\nv_fma_f32 v93, v93, s93, -v104\nv_fma_f32 v94, v94, s93, -v104\nMvaa 228,164,28\nv_add_u32 v14, s72, v14\nv_fma_f32 v95, v95, s93, -v104\n" \
"Mvaa 224,160,28\nv_add_u32 v28, s72, v28\nMvaa 224,64,12\ns_waitcnt lgkmcnt(6)\nMvaa 128,96,32\nE 88,88\nMvaa 128,128,48\ns_add_u32 m0, s88, 32768\n" \
"Mvaa 132,132,48\nD 13,64,0,0\nMvaa 132,100,32\nE 89,89\nMvaa 136,104,32\nE 90,90\nMvaa 136,136,48\nC 96,88,89\n" \
"Mvaa 140,140,48\nE 91,91\nMvaa 140,108,32\ns_waitcnt lgkmcnt(0)\nD 13,64,s73,1024\nMvaa 144,112,32\nE 92,92\nMvaa 144,144,48\n" \
"C 97,90,91\nMvaa 148,148,48\nE 93,93\nMvaa 148,116,32\nE 94,94\nMvaa 152,120,32\nD 13,64,s74,2048\nMvaa 152,152,48\n" \
"C 98,92,93\nMvaa 156,156,48\nE 95,95\nMvaa 156,124,32\nv_max_f32 v124, v124, v125\nv_mul_f32 v124, s93, v124\nMvaa 188,124,36\ns_waitcnt vmcnt(17)\n" \
"s_barrier\nMvaa 188,156,52\nTa 64,20,16384\nTa 66,20,17408\nMvaa 184,152,52\nD 13,64,s75,3072\nMvaa 184,120,36\nTa 160,20,24576\n" \
"Ta 162,20,25600\nMvaa 180,116,36\nv_fma_f32 v108, v108, s93, -v124\nv_fma_f32 v109, v109, s93, -v124\nMvaa 180,148,52\nRa 0,18,0\nRa 16,19,0\nMvaa 176,144,52\n" \
"s_add_u32 m0, s88, 36864\nTa 68,21,16384\nMvaa 176,112,36\nD 13,64,s76,0\nMvaa 172,108,36\nTa 70,21,17408\nTa 164,21,24576\nMvaa 172,140,52\n" \
"v_fma_f32 v110, v110, s93, -v124\nv_fma_f32 v111, v111, s93, -v124\nMvaa 168,136,52\nTa 166,21,25600\nTa 72,22,16384\nMvaa 168,104,36\nTa 74,22,17408\nTa 168,22,24576\n" \
"Mvaa 164,100,36\nD 13,64,s77,1024\nMvaa 164,132,52\nv_fma_f32 v112, v112, s93, -v124\nv_fma_f32 v113, v113, s93, -v124\nMvaa 160,128,52\nTa 170,22,25600\nTa 76,23,16384\n" \
"Mvaa 160,96,36\nv_fma_f32 v114, v114, s93, -v124\nv_fma_f32 v115, v115, s93, -v124\nMvaa 192,96,40\nTa 78,23,17408\nTa 172,23,24576\nMvaa 192,128,56\nD 13,64,s78,2048\n" \
"Mvaa 196,132,56\nTa 174,23,25600\nTa 80,24,16384\nMvaa 196,100,40\nE 108,108\nMvaa 200,104,40\nTa 82,24,17408\nTa 176,24,24576\n" \
"Mvaa 200,136,56\nTa 178,24,25600\nTa 84,25,16384\nMvaa 204,140,56\nD 13,64,s79,3072\nMvaa 204,108,40\nE 109,109\nMvaa 208,112,40\n" \
"Ta 86,25,17408\nTa 180,25,24576\nMvaa 208,144,56\nv_add_u32 v13, 128, v13\nTa 182,25,25600\nMvaa 212,148,56\ns_add_u32 m0, s32, 98304\nTa 88,26,16384\n" \
"Mvaa 212,116,40\nD 14,68,0,0\nMvaa 216,120,40\nTa 90,26,17408\nTa 184,26,24576\nMvaa 216,152,56\nE 110,110\nMvaa 220,156,56\n" \
"Ta 186,26,25600\nTa 92,27,16384\nMvaa 220,124,40\nC 116,108,109\nMvaa 252,124,44\nD 14,68,s89,1024\nMvaa 252,156,60\nTa 94,27,17408\n" \
"Ta 188,27,24576\nMvaa 248,152,60\nTa 190,27,25600\nRa 4,18,2048\nMvaa 248,120,44\nE 111,111\nMvaa 244,116,44\nRa 20,19,2048\n" \
"Ra 8,18,4096\nMvaa 244,148,60\nD 28,68,s90,2048\nMvaa 240,144,60\nRa 24,19,4096\nRa 12,18,6144\nMvaa 240,112,44\nE 112,112\n" \
"Mvaa 236,108,44\nRa 28,19,6144\nC 117,110,111\nMvaa 236,140,60\nE 113,113\nMvaa 232,136,60\nD 28,68,s91,3072\nMvaa 232,104,44\n" \
"E 114,114\nMvaa 228,100,44\nC 118,112,113\nMvaa 228,132,60\nv_add_u32 v14, s72, v14\nMvaa 224,128,60\nv_add_u32 v28, s72, v28\nMvaa 224,96,44\n" \
"s_waitcnt lgkmcnt(6)\nMvaa 128,64,0\nE 115,115\nMvaa 128,160,16\ns_add_u32 m0, s88, 131072\nC 99,94,95\nMvaa 132,164,16\nD 13,64,0,0\n" \
"Mvaa 132,68,0\nC 119,114,115\nZvvv 100,84,96\nZvvv 88,38,96\nZvvv 92,56,96\nZvvv 120,84,116\nZvvv 108,40,116\nZvvv 112,58,116\n" \
"Mvaa 136,72,0\nMvaa 136,168,16\nMvaa 140,172,16\nv_rcp_f32 v104, v100\nMvaa 140,76,0\ns_waitcnt lgkmcnt(0)\nD 13,64,s73,1024\nMvaa 144,80,0\n" \
"v_rcp_f32 v124, v120\nMvaa 144,176,16\nv_mul_f32 v88, v88, v104\nv_mul_f32 v89, v89, v104\nMvaa 148,180,16\nv_mul_f32 v90, v90, v104\nv_mul_f32 v91, v91, v104\nMvaa 148,84,0\n" \
"v_mul_f32 v92, v92, v104\nv_mul_f32 v93, v93, v104\nMvaa 152,88,0\nD 13,64,s74,2048\nMvaa 152,184,16\nv_mul_f32 v94, v94, v104\nv_mul_f32 v95, v95, v104\nMvaa 156,188,16\n" \
"C 96,88,89\nC 97,90,91\nMvaa 156,92,0\nC 98,92,93\nC 99,94,95\nMvaa 188,92,4\ns_waitcnt vmcnt(15)\ns_barrier\n" \
"Mvaa 188,188,20\nTa 96,20,0\nTa 98,20,1024\nMvaa 184,184,20\nD 13,64,s75,3072\nMvaa 184,88,4\nTa 128,20,8192\nTa 130,20,9216\n" \
"Mvaa 180,84,4\ns_add_u32 s95, s50, 6144\nbuffer_store_dwordx4 v[96:99], v0, s[60:63], s95 offen sc1\nMvaa 180,180,20\nRa 32,16,0\nRa 48,17,0\nMvaa 176,176,20\ns_add_u32 m0, s88, 135168\n" \
"Ta 100,21,0\nMvaa 176,80,4\nD 13,64,s76,0\nMvaa 172,76,4\nTa 102,21,1024\nTa 132,21,8192\nMvaa 172,172,20\nv_mul_f32 v108, v108, v124\n" \
"v_mul_f32 v109, v109, v124\nMvaa 168,168,20\nTa 134,21,9216\nTa 104,22,0\nMvaa 168,72,4\nTa 106,22,1024\nTa 136,22,8192\nMvaa 164,68,4\n" \
"D 13,64,s77,1024\nMvaa 164,164,20\nv_mul_f32 v110, v110, v124\nv_mul_f32 v111, v111, v124\nMvaa 160,160,20\nTa 138,22,9216\nTa 108,23,0\nMvaa 160,64,4\n" \
"v_mul_f32 v112, v112, v124\nv_mul_f32 v113, v113, v124\nMvaa 192,64,8\nTa 110,23,1024\nTa 140,23,8192\nMvaa 192,160,24\nD 13,64,s78,2048\nMvaa 196,164,24\n" \
"Ta 142,23,9216\nTa 112,24,0\nMvaa 196,68,8\nv_mul_f32 v114, v114, v124\nv_mul_f32 v115, v115, v124\nMvaa 200,72,8\nTa 114,24,1024\nTa 144,24,8192\n" \
"Mvaa 200,168,24\nTa 146,24,9216\nTa 116,25,0\nMvaa 204,172,24\nD 13,64,s79,3072\nMvaa 204,76,8\nC 116,108,109\nC 117,110,111\n" \
"Mvaa 208,80,8\nTa 118,25,1024\nTa 148,25,8192\nMvaa 208,176,24\nv_add_u32 v13, 128, v13\nTa 150,25,9216\nMvaa 212,180,24\ns_add_u32 m0, s32, 81920\n" \
"Ta 120,26,0\nMvaa 212,84,8\nD 14,68,0,0\nMvaa 216,88,8\nTa 122,26,1024\nTa 152,26,8192\nMvaa 216,184,24\nC 118,112,113\n" \
"C 119,114,115\nMvaa 220,188,24\nTa 154,26,9216\nTa 124,27,0\nMvaa 220,92,8\ns_add_u32 s95, s50, 8192\nbuffer_store_dwordx4 v[116:119], v0, s[60:63], s95 offen sc1\nMvaa 252,92,12\n" \
"D 14,68,s89,1024\nQvaa 88,240,212,72\nQvaa 92,244,212,76\nQvaa 108,244,216,72\nQvaa 112,248,216,76\nMvaa 252,188,28\nTa 126,27,1024\nTa 156,27,8192\n" \
"Mvaa 248,184,28\nTa 158,27,9216\nRa 36,16,2048\nMvaa 248,88,12\nv_max3_f32 v104, v88, v89, v90\nv_max3_f32 v105, v91, v92, v93\nMvaa 244,84,12\nRa 52,17,2048\n" \
"Ra 40,16,4096\nMvaa 244,180,28\nD 28,68,s90,2048\nMvaa 240,176,28\nRa 56,17,4096\nRa 44,16,6144\nMvaa 240,80,12\nv_max3_f32 v104, v104, v94, v95\n" \
"v_max_f32 v104, v104, v105\nMvaa 236,76,12\nRa 60,17,6144\nv_mov_b32 v105, v104\nMvaa 236,172,28\nv_max3_f32 v124, v108, v109, v110\nv_max3_f32 v125, v111, v112, v113\nMvaa 232,168,28\n" \
"D 28,68,s91,3072\nMvaa 232,72,12\nv_max3_f32 v124, v124, v114, v115\nv_max_f32 v124, v124, v125\nMvaa 228,68,12\nv_mov_b32 v125, v124\nv_permlane16_swap_b32 v104, v105\nMvaa 228,164,28\n" \
"v_add_u32 v14, s72, v14\nv_permlane16_swap_b32 v124, v125\nMvaa 224,160,28\nv_add_u32 v28, s72, v28\nv_max_f32 v104, v104, v105\nMvaa 224,64,12\ns_waitcnt lgkmcnt(6)\nv_mov_b32 v105, v104\n" \
"Mvaa 128,96,32\nv_max_f32 v124, v124, v125\nv_mov_b32 v125, v124\nMvaa 128,128,48\ns_add_u32 m0, s88, 0\nv_permlane32_swap_b32 v104, v105\nMvaa 132,132,48\nD 13,64,0,0\n" \
"Mvaa 132,100,32\nv_permlane32_swap_b32 v124, v125\nv_max_f32 v104, v104, v105\nMvaa 136,104,32\nbuffer_load_dwordx2 v[10:11], off, s[60:63], s49 sc1\nMvaa 136,136,48\nv_mul_f32 v104, s93, v104\nv_fma_f32 v88, v88, s93, -v104\n" \
"Mvaa 140,140,48\nv_fma_f32 v89, v89, s93, -v104\nv_fma_f32 v90, v90, s93, -v104\nMvaa 140,108,32\ns_waitcnt lgkmcnt(0)\nD 13,64,s73,1024\nMvaa 144,112,32\nv_fma_f32 v91, v91, s93, -v104\n" \
"v_fma_f32 v92, v92, s93, -v104\nMvaa 144,144,48\nv_fma_f32 v93, v93, s93, -v104\nv_fma_f32 v94, v94, s93, -v104\nMvaa 148,148,48\nv_fma_f32 v95, v95, s93, -v104\nMvaa 148,116,32\nE 88,88\n" \
"Mvaa 152,120,32\nD 13,64,s74,2048\nMvaa 152,152,48\nE 89,89\nMvaa 156,156,48\nE 90,90\nMvaa 156,124,32\nC 96,88,89\n" \
"Mvaa 188,124,36\ns_waitcnt vmcnt(18)\ns_barrier\nMvaa 188,156,52\nTa 64,20,32768\nTa 66,20,33792\nMvaa 184,152,52\nD 13,64,s75,3072\n" \
"Mvaa 184,120,36\nTa 160,20,40960\nTa 162,20,41984\nMvaa 180,116,36\nE 91,91\nMvaa 180,148,52\nRa 0,16,32768\nRa 16,17,32768\n" \
"Mvaa 176,144,52\ns_add_u32 m0, s88, 4096\nTa 68,21,32768\nMvaa 176,112,36\nD 13,64,s76,0\nMvaa 172,108,36\nTa 70,21,33792\nTa 164,21,40960\n" \
"Mvaa 172,140,52\nE 92,92\nMvaa 168,136,52\nTa 166,21,41984\nTa 72,22,32768\nMvaa 168,104,36\nTa 74,22,33792\nTa 168,22,40960\n" \
"Mvaa 164,100,36\nD 13,64,s77,1024\nMvaa 164,132,52\nC 97,90,91\nMvaa 160,128,52\nTa 170,22,41984\nTa 76,23,32768\nMvaa 160,96,36\n" \
"E 93,93\nMvaa 192,96,40\nTa 78,23,33792\nTa 172,23,40960\nMvaa 192,128,56\nD 13,64,s78,2048\nMvaa 196,132,56\nTa 174,23,41984\n" \
"Ta 80,24,32768\nMvaa 196,100,40\nE 94,94\nMvaa 200,104,40\nTa 82,24,33792\nTa 176,24,40960\nMvaa 200,136,56\nTa 178,24,41984\n" \
"Ta 84,25,32768\nMvaa 204,140,56\nD 13,64,s79,3072\nMvaa 204,108,40\nC 98,92,93\nMvaa 208,112,40\nTa 86,25,33792\nTa 180,25,40960\n" \
"Mvaa 208,144,56\nv_add_u32 v13, 128, v13\nTa 182,25,41984\nMvaa 212,148,56\ns_add_u32 m0, s32, 65536\nTa 88,26,32768\nMvaa 212,116,40\nD 14,68,0,0\n" \
"Mvaa 216,120,40\nTa 90,26,33792\nTa 184,26,40960\nMvaa 216,152,56\nE 95,95\nMvaa 220,156,56\nTa 186,26,41984\nTa 92,27,32768\n" \
"Mvaa 220,124,40\nv_max_f32 v124, v124, v125\nv_mul_f32 v124, s93, v124\nMvaa 252,124,44\nD 14,68,s89,1024\nMvaa 252,156,60\nTa 94,27,33792\nTa 188,27,40960\n" \
"Mvaa 248,152,60\nTa 190,27,41984\nRa 4,16,34816\nMvaa 248,120,44\nv_fma_f32 v108, v108, s93, -v124\nv_fma_f32 v109, v109, s93, -v124\nMvaa 244,116,44\nRa 20,17,34816\n" \
"Ra 8,16,36864\nMvaa 244,148,60\ns_branch 99f\n.p2align 12\nzp33:\ns_setpc_b64 s[98:99]\n99:\nD 28,68,s90,2048\n" \
"Mvaa 240,144,60\nRa 24,17,36864\nRa 12,16,38912\nMvaa 240,112,44\nv_fma_f32 v110, v110, s93, -v124\nv_fma_f32 v111, v111, s93, -v124\nMvaa 236,108,44\nRa 28,17,38912\n" \
"v_fma_f32 v112, v112, s93, -v124\nMvaa 236,140,60\nv_fma_f32 v113, v113, s93, -v124\nv_fma_f32 v114, v114, s93, -v124\nMvaa 232,136,60\nD 28,68,s91,3072\nMvaa 232,104,44\nv_fma_f32 v115, v115, s93, -v124\n" \
"Mvaa 228,100,44\nE 108,108\nMvaa 228,132,60\nv_add_u32 v14, s72, v14\nMvaa 224,128,60\nv_add_u32 v28, s72, v28\nMvaa 224,96,44\ns_waitcnt lgkmcnt(6)\n" \
"Mvaa 128,64,0\nE 109,109\nMvaa 128,160,16\ns_add_u32 m0, s88, 32768\nMvaa 132,164,16\nD 13,64,0,0\nMvaa 132,68,0\nE 110,110\n" \
"Mvaa 136,72,0\ns_waitcnt vmcnt(12)\nv_cmp_ne_u64 vcc, v[10:11], v[2:3]\ns_cbranch_vccnz zs103\nzr103:\nMvaa 136,168,16\nbuffer_load_dwordx4 v[68:71], v0, s[60:63], s47 offen sc1\nMvaa 140,172,16\n" \
"buffer_load_dwordx2 v[32:33], v0, s[60:63], s47 offen offset:1024 sc1\nMvaa 140,76,0\ns_waitcnt lgkmcnt(0)\nD 13,64,s73,1024\nbuffer_load_dwordx2 v[50:51], v0, s[60:63], s47 offen offset:1032 sc1\nMvaa 144,80,0\nC 116,108,109\nMvaa 144,176,16\n" \
"E 111,111\nMvaa 148,180,16\nE 112,112\nMvaa 148,84,0\nC 117,110,111\nMvaa 152,88,0\nD 13,64,s74,2048\nMvaa 152,184,16\n" \
"E 113,113\nMvaa 156,188,16\nE 114,114\nMvaa 156,92,0\nC 118,112,113\nMvaa 188,92,4\ns_waitcnt vmcnt(19)\ns_barrier\n" \
"Mvaa 188,188,20\nTa 96,20,16384\nTa 98,20,17408\nMvaa 184,184,20\nD 13,64,s75,3072\nMvaa 184,88,4\nTa 128,20,24576\nTa 130,20,25600\n" \
"Mvaa 180,84,4\nE 115,115\nMvaa 180,180,20\nRa 32,18,0\nRa 48,19,0\nMvaa 176,176,20\ns_add_u32 m0, s88, 36864\nTa 100,21,16384\n" \
"Mvaa 176,80,4\nD 13,64,s76,0\nMvaa 172,76,4\nTa 102,21,17408\nTa 132,21,24576\nMvaa 172,172,20\nC 99,94,95\nC 119,114,115\n" \
"Mvaa 168,168,20\nTa 134,21,25600\nTa 104,22,16384\nZvvv 100,84,96\nZvvv 88,42,96\nZvvv 92,60,96\nZvvv 120,84,116\nZvvv 108,44,116\n" \
"Zvvv 112,62,116\nMvaa 168,72,4\nTa 106,22,17408\nTa 136,22,24576\nMvaa 164,68,4\nD 13,64,s77,1024\nMvaa 164,164,20\nv_rcp_f32 v104, v100\n" \
"Mvaa 160,160,20\nTa 138,22,25600\nTa 108,23,16384\nMvaa 160,64,4\nv_rcp_f32 v124, v120\nMvaa 192,64,8\nTa 110,23,17408\nTa 140,23,24576\n" \
"Mvaa 192,160,24\nD 13,64,s78,2048\nMvaa 196,164,24\nTa 142,23,25600\nTa 112,24,16384\nMvaa 196,68,8\nv_mul_f32 v88, v88, v104\nv_mul_f32 v89, v89, v104\n" \
"Mvaa 200,72,8\nTa 114,24,17408\nTa 144,24,24576\nMvaa 200,168,24\nTa 146,24,25600\nTa 116,25,16384\nMvaa 204,172,24\nD 13,64,s79,3072\n" \
"Mvaa 204,76,8\nv_mul_f32 v90, v90, v104\nv_mul_f32 v91, v91, v104\nMvaa 208,80,8\nTa 118,25,17408\nTa 148,25,24576\nMvaa 208,176,24\nv_add_u32 v13, 128, v13\n" \
"Ta 150,25,25600\nMvaa 212,180,24\ns_add_u32 m0, s32, 98304\nTa 120,26,16384\nMvaa 212,84,8\nD 14,68,0,0\nMvaa 216,88,8\nTa 122,26,17408\n" \
"Ta 152,26,24576\nMvaa 216,184,24\nv_mul_f32 v92, v92, v104\nv_mul_f32 v93, v93, v104\nMvaa 220,188,24\nTa 154,26,25600\nTa 124,27,16384\nMvaa 220,92,8\n" \
"v_mul_f32 v94, v94, v104\nv_mul_f32 v95, v95, v104\nMvaa 252,92,12\nD 14,68,s89,1024\nMvaa 252,188,28\nTa 126,27,17408\nTa 156,27,24576\nMvaa 248,184,28\n" \
"Ta 158,27,25600\nRa 36,18,2048\nMvaa 248,88,12\nC 96,88,89\nC 97,90,91\nMvaa 244,84,12\nRa 52,19,2048\nRa 40,18,4096\n" \
"Mvaa 244,180,28\nD 28,68,s90,2048\nMvaa 240,176,28\nRa 56,19,4096\nRa 44,18,6144\nMvaa 240,80,12\nC 98,92,93\nC 99,94,95\n" \
"Mvaa 236,76,12\nRa 60,19,6144\nMvaa 236,172,28\ns_add_u32 s95, s50, 10240\nbuffer_store_dwordx4 v[96:99], v0, s[60:63], s95 offen sc1\nMvaa 232,168,28\nD 28,68,s91,3072\nMvaa 232,72,12\n" \
"v_mul_f32 v108, v108, v124\nv_mul_f32 v109, v109, v124\nMvaa 228,68,12\nv_mul_f32 v110, v110, v124\nv_mul_f32 v111, v111, v124\nMvaa 228,164,28\nv_add_u32 v14, s72, v14\nv_mul_f32 v112, v112, v124\n" \
"Mvaa 224,160,28\nv_add_u32 v28, s72, v28\nv_mul_f32 v113, v113, v124\nMvaa 224,64,12\ns_waitcnt lgkmcnt(6)\nv_mul_f32 v114, v114, v124\nMvaa 128,96,32\nv_mul_f32 v115, v115, v124\n" \
"C 116,108,109\nMvaa 128,128,48\ns_add_u32 m0, s88, 131072\nC 117,110,111\nMvaa 132,132,48\nD 13,64,0,0\nMvaa 132,100,32\nC 118,112,113\n" \
"C 119,114,115\nMvaa 136,104,32\ns_add_u32 s95, s50, 12288\nbuffer_store_dwordx4 v[116:119], v0, s[60:63], s95 offen sc1\nMvaa 136,136,48\nMvaa 140,140,48\nMvaa 140,108,32\ns_waitcnt lgkmcnt(0)\n" \
"D 13,64,s73,1024\nMvaa 144,112,32\nMvaa 144,144,48\nMvaa 148,148,48\nMvaa 148,116,32\nMvaa 152,120,32\nD 13,64,s74,2048\nMvaa 152,152,48\n" \
"Mvaa 156,156,48\nMvaa 156,124,32\nMvaa 188,124,36\ns_waitcnt vmcnt(20)\ns_barrier\nMvaa 188,156,52\nTa 64,20,0\nTa 66,20,1024\n" \
"Mvaa 184,152,52\nD 13,64,s75,3072\nMvaa 184,120,36\nTa 160,20,8192\nTa 162,20,9216\nMvaa 180,116,36\ns_waitcnt vmcnt(16)\nQvaa 88,248,220,72\n" \
"Qvaa 92,252,220,76\nQvva 108,68,192,80\nQvaa 112,224,192,76\nMvaa 180,148,52\nRa 0,16,0\nRa 16,17,0\nMvaa 176,144,52\ns_add_u32 m0, s88, 135168\n" \
"Ta 68,21,0\nMvaa 176,112,36\nD 13,64,s76,0\nMvaa 172,108,36\nTa 70,21,1024\nTa 164,21,8192\nMvaa 172,140,52\nv_max3_f32 v104, v88, v89, v90\n" \
"v_max3_f32 v105, v91, v92, v93\nMvaa 168,136,52\nTa 166,21,9216\nTa 72,22,0\nMvaa 168,104,36\nTa 74,22,1024\nTa 168,22,8192\nMvaa 164,100,36\n" \
"D 13,64,s77,1024\nMvaa 164,132,52\nv_max3_f32 v104, v104, v94, v95\nv_max_f32 v104, v104, v105\nMvaa 160,128,52\nTa 170,22,9216\nTa 76,23,0\nMvaa 160,96,36\n" \
"v_mov_b32 v105, v104\nv_max3_f32 v124, v108, v109, v110\nMvaa 192,96,40\nTa 78,23,1024\nTa 172,23,8192\nMvaa 192,128,56\nD 13,64,s78,2048\nMvaa 196,132,56\n" \
"Ta 174,23,9216\nTa 80,24,0\nMvaa 196,100,40\nv_max3_f32 v125, v111, v112, v113\nv_max3_f32 v124, v124, v114, v115\nMvaa 200,104,40\nTa 82,24,1024\nTa 176,24,8192\n" \
"Mvaa 200,136,56\nTa 178,24,9216\nTa 84,25,0\nMvaa 204,140,56\nD 13,64,s79,3072\nMvaa 204,108,40\nv_max_f32 v124, v124, v125\nv_mov_b32 v125, v124\n" \
"Mvaa 208,112,40\nTa 86,25,1024\nTa 180,25,8192\nMvaa 208,144,56\nv_add_u32 v13, 128, v13\nTa 182,25,9216\nMvaa 212,148,56\ns_add_u32 m0, s32, 81920\n" \
"Ta 88,26,0\nMvaa 212,116,40\nD 14,68,0,0\nMvaa 216,120,40\nTa 90,26,1024\nTa 184,26,8192\nMvaa 216,152,56\nv_permlane16_swap_b32 v104, v105\n" \
"v_permlane16_swap_b32 v124, v125\nMvaa 220,156,56\nTa 186,26,9216\nTa 92,27,0\nMvaa 220,124,40\nv_max_f32 v104, v104, v105\nv_mov_b32 v105, v104\nMvaa 252,124,44\n" \
"D 14,68,s89,1024\nMvaa 252,156,60\nTa 94,27,1024\nTa 188,27,8192\nMvaa 248,152,60\nTa 190,27,9216\nRa 4,16,2048\nMvaa 248,120,44\n" \
"v_max_f32 v124, v124, v125\nv_mov_b32 v125, v124\nMvaa 244,116,44\nRa 20,17,2048\nRa 8,16,4096\nMvaa 244,148,60\nD 28,68,s90,2048\nMvaa 240,144,60\n" \
"Ra 24,17,4096\nRa 12,16,6144\nMvaa 240,112,44\nv_permlane32_swap_b32 v104, v105\nv_permlane32_swap_b32 v124, v125\nMvaa 236,108,44\nRa 28,17,6144\nv_max_f32 v104, v104, v105\n" \
"Mvaa 236,140,60\nv_mul_f32 v104, s93, v104\nv_fma_f32 v88, v88, s93, -v104\nMvaa 232,136,60\nD 28,68,s91,3072\nMvaa 232,104,44\nv_fma_f32 v89, v89, s93, -v104\nv_fma_f32 v90, v90, s93, -v104\n" \
"Mvaa 228,100,44\nv_fma_f32 v91, v91, s93, -v104\nv_fma_f32 v92, v92, s93, -v104\nMvaa 228,132,60\nv_add_u32 v14, s72, v14\nv_fma_f32 v93, v93, s93, -v104\nMvaa 224,128,60\nv_add_u32 v28, s72, v28\n" \
"v_fma_f32 v94, v94, s93, -v104\nMvaa 224,96,44\ns_waitcnt lgkmcnt(6)\nv_fma_f32 v95, v95, s93, -v104\nMvaa 128,64,0\nE 88,88\nMvaa 128,160,16\ns_add_u32 m0, s88, 0\n" \
"Mvaa 132,164,16\nD 13,64,0,0\nMvaa 132,68,0\nE 89,89\nMvaa 136,72,0\nE 90,90\nMvaa 136,168,16\nC 96,88,89\n" \
"Mvaa 140,172,16\nE 91,91\nMvaa 140,76,0\ns_waitcnt lgkmcnt(0)\nD 13,64,s73,1024\nMvaa 144,80,0\nE 92,92\nMvaa 144,176,16\n" \
"C 97,90,91\nMvaa 148,180,16\nE 93,93\nMvaa 148,84,0\nE 94,94\nMvaa 152,88,0\nD 13,64,s74,2048\nMvaa 152,184,16\n" \
"C 98,92,93\nMvaa 156,188,16\nE 95,95\nMvaa 156,92,0\nv_max_f32 v124, v124, v125\nv_mul_f32 v124, s93, v124\nMvaa 188,92,4\ns_waitcnt vmcnt(16)\n" \
"s_barrier\nMvaa 188,188,20\nTa 96,20,32768\nTa 98,20,33792\nMvaa 184,184,20\nD 13,64,s75,3072\nMvaa 184,88,4\nTa 128,20,40960\n" \
"Ta 130,20,41984\nMvaa 180,84,4\nv_fma_f32 v108, v108, s93, -v124\nv_fma_f32 v109, v109, s93, -v124\nMvaa 180,180,20\nRa 32,16,32768\nRa 48,17,32768\nMvaa 176,176,20\n" \
"s_add_u32 m0, s88, 4096\nTa 100,21,32768\nMvaa 176,80,4\nD 13,64,s76,0\nMvaa 172,76,4\nTa 102,21,33792\nTa 132,21,40960\nMvaa 172,172,20\n" \
"v_fma_f32 v110, v110, s93, -v124\nv_fma_f32 v111, v111, s93, -v124\nMvaa 168,168,20\nTa 134,21,41984\nTa 104,22,32768\nMvaa 168,72,4\nTa 106,22,33792\nTa 136,22,40960\n" \
"Mvaa 164,68,4\nD 13,64,s77,1024\nMvaa 164,164,20\nv_fma_f32 v112, v112, s93, -v124\nv_fma_f32 v113, v113, s93, -v124\nMvaa 160,160,20\nTa 138,22,41984\nTa 108,23,32768\n" \
"Mvaa 160,64,4\nv_fma_f32 v114, v114, s93, -v124\nv_fma_f32 v115, v115, s93, -v124\nMvaa 192,64,8\nTa 110,23,33792\nTa 140,23,40960\nMvaa 192,160,24\nD 13,64,s78,2048\n" \
"Mvaa 196,164,24\nTa 142,23,41984\nTa 112,24,32768\nMvaa 196,68,8\nE 108,108\nMvaa 200,72,8\nTa 114,24,33792\nTa 144,24,40960\n" \
"Mvaa 200,168,24\nTa 146,24,41984\nTa 116,25,32768\nMvaa 204,172,24\nD 13,64,s79,3072\nMvaa 204,76,8\nE 109,109\nMvaa 208,80,8\n" \
"Ta 118,25,33792\nTa 148,25,40960\nMvaa 208,176,24\nv_add_u32 v13, 128, v13\nTa 150,25,41984\nMvaa 212,180,24\ns_add_u32 m0, s32, 65536\nTa 120,26,32768\n" \
"Mvaa 212,84,8\nD 14,68,0,0\nMvaa 216,88,8\nTa 122,26,33792\nTa 152,26,40960\nMvaa 216,184,24\nE 110,110\nMvaa 220,188,24\n" \
"Ta 154,26,41984\nTa 124,27,32768\nMvaa 220,92,8\nC 116,108,109\nMvaa 252,92,12\nD 14,68,s89,1024\nMvaa 252,188,28\nTa 126,27,33792\n" \
"Ta 156,27,40960\nMvaa 248,184,28\nTa 158,27,41984\nRa 36,16,34816\nMvaa 248,88,12\nE 111,111\nMvaa 244,84,12\nRa 52,17,34816\n" \
"Ra 40,16,36864\nMvaa 244,180,28\nD 28,68,s90,2048\nMvaa 240,176,28\nRa 56,17,36864\nRa 44,16,38912\nMvaa 240,80,12\nE 112,112\n" \
"Mvaa 236,76,12\nRa 60,17,38912\nC 117,110,111\nMvaa 236,172,28\nE 113,113\nMvaa 232,168,28\nD 28,68,s91,3072\nMvaa 232,72,12\n" \
"E 114,114\nMvaa 228,68,12\nC 118,112,113\nMvaa 228,164,28\nv_add_u32 v14, s72, v14\nMvaa 224,160,28\nv_add_u32 v28, s72, v28\nMvaa 224,64,12\n" \
"s_waitcnt lgkmcnt(6)\nMvaa 128,96,32\nE 115,115\nMvaa 128,128,48\ns_add_u32 m0, s88, 32768\nC 99,94,95\nMvaa 132,132,48\nD 13,64,0,0\n" \
"Mvaa 132,100,32\nC 119,114,115\nZvvv 100,84,96\nZvvv 88,46,96\nZvvv 92,64,96\nZvvv 120,84,116\nZvvv 108,32,116\nZvvv 112,50,116\n" \
"Mvaa 136,104,32\nMvaa 136,136,48\nMvaa 140,140,48\nv_rcp_f32 v104, v100\nMvaa 140,108,32\ns_waitcnt lgkmcnt(0)\nD 13,64,s73,1024\nMvaa 144,112,32\n" \
"v_rcp_f32 v124, v120\nMvaa 144,144,48\nv_mul_f32 v88, v88, v104\nv_mul_f32 v89, v89, v104\nMvaa 148,148,48\nv_mul_f32 v90, v90, v104\nv_mul_f32 v91, v91, v104\nMvaa 148,116,32\n" \
"v_mul_f32 v92, v92, v104\nv_mul_f32 v93, v93, v104\nMvaa 152,120,32\nD 13,64,s74,2048\nMvaa 152,152,48\nv_mul_f32 v94, v94, v104\ns_branch 99f\n.p2align 12\n" \
"zp34:\ns_setpc_b64 s[98:99]\n99:\nv_mul_f32 v95, v95, v104\nMvaa 156,156,48\nC 96,88,89\nC 97,90,91\nMvaa 156,124,32\n" \
"C 98,92,93\nC 99,94,95\nMvaa 188,124,36\ns_waitcnt vmcnt(15)\ns_barrier\nMvaa 188,156,52\nTa 64,20,16384\nTa 66,20,17408\n" \
"Mvaa 184,152,52\nD 13,64,s75,3072\nMvaa 184,120,36\nTa 160,20,24576\nTa 162,20,25600\nMvaa 180,116,36\ns_add_u32 s95, s50, 14336\nbuffer_store_dwordx4 v[96:99], v0, s[60:63], s95 offen sc1\n" \
"Mvaa 180,148,52\nRa 0,18,0\nRa 16,19,0\nMvaa 176,144,52\ns_add_u32 m0, s88, 36864\nTa 68,21,16384\nMvaa 176,112,36\nD 13,64,s76,0\n" \
"Mvaa 172,108,36\nTa 70,21,17408\nTa 164,21,24576\nMvaa 172,140,52\nv_mul_f32 v108, v108, v124\nv_mul_f32 v109, v109, v124\nMvaa 168,136,52\nTa 166,21,25600\n" \
"Ta 72,22,16384\nMvaa 168,104,36\nTa 74,22,17408\nTa 168,22,24576\nMvaa 164,100,36\nD 13,64,s77,1024\nMvaa 164,132,52\nv_mul_f32 v110, v110, v124\n" \
"v_mul_f32 v111, v111, v124\nMvaa 160,128,52\nTa 170,22,25600\nTa 76,23,16384\nMvaa 160,96,36\nv_mul_f32 v112, v112, v124\nv_mul_f32 v113, v113, v124\nMvaa 192,96,40\n" \
"Ta 78,23,17408\nTa 172,23,24576\nMvaa 192,128,56\nD 13,64,s78,2048\nMvaa 196,132,56\nTa 174,23,25600\nTa 80,24,16384\nMvaa 196,100,40\n" \
"v_mul_f32 v114, v114, v124\nv_mul_f32 v115, v115, v124\nMvaa 200,104,40\nTa 82,24,17408\nTa 176,24,24576\nMvaa 200,136,56\nTa 178,24,25600\nTa 84,25,16384\n" \
"Mvaa 204,140,56\nD 13,64,s79,3072\nMvaa 204,108,40\nC 116,108,109\nC 117,110,111\nMvaa 208,112,40\nTa 86,25,17408\nTa 180,25,24576\n" \
"Mvaa 208,144,56\nv_add_u32 v13, 128, v13\nTa 182,25,25600\nMvaa 212,148,56\ns_add_u32 m0, s32, 98304\nTa 88,26,16384\nMvaa 212,116,40\nD 14,68,0,0\n" \
"Mvaa 216,120,40\nTa 90,26,17408\nTa 184,26,24576\nMvaa 216,152,56\nC 118,112,113\nC 119,114,115\nMvaa 220,156,56\nTa 186,26,25600\n" \
"Ta 92,27,16384\nMvaa 220,124,40\ns_add_u32 s95, s50, 0\nbuffer_store_dwordx4 v[116:119], v0, s[60:63], s95 offen sc1\nMvaa 252,124,44\nD 14,68,s89,1024\nMvaa 252,156,60\nTa 94,27,17408\n" \
"Ta 188,27,24576\nMvaa 248,152,60\nTa 190,27,25600\nRa 4,18,2048\nMvaa 248,120,44\nMvaa 244,116,44\nRa 20,19,2048\nRa 8,18,4096\n" \
"Mvaa 244,148,60\nD 28,68,s90,2048\nMvaa 240,144,60\nRa 24,19,4096\nRa 12,18,6144\nMvaa 240,112,44\nMvaa 236,108,44\nRa 28,19,6144\n" \
"Mvaa 236,140,60\nMvaa 232,136,60\nD 28,68,s91,3072\nMvaa 232,104,44\nMvaa 228,100,44\nMvaa 228,132,60\nv_add_u32 v14, s72, v14\nMvaa 224,128,60\n" \
"v_add_u32 v28, s72, v28\nMvaa 224,96,44\ns_waitcnt lgkmcnt(6)\nMvaa 128,64,0\nMvaa 128,160,16\ns_add_u32 m0, s88, 131072\nMvaa 132,164,16\nD 13,64,0,0\n" \
"Mvaa 132,68,0\nMvaa 136,72,0\nMvaa 136,168,16\nMvaa 140,172,16\nMvaa 140,76,0\ns_waitcnt lgkmcnt(0)\nD 13,64,s73,1024\nMvaa 144,80,0\n" \
"Mvaa 144,176,16\nMvaa 148,180,16\nMvaa 148,84,0\nMvaa 152,88,0\nD 13,64,s74,2048\nMvaa 152,184,16\nMvaa 156,188,16\nMvaa 156,92,0\n" \
"Mvaa 188,92,4\ns_waitcnt vmcnt(17)\ns_barrier\nMvaa 188,188,20\nTa 96,20,0\nTa 98,20,1024\nMvaa 184,184,20\nD 13,64,s75,3072\n" \
"Mvaa 184,88,4\nTa 128,20,8192\nTa 130,20,9216\nMvaa 180,84,4\nMvaa 180,180,20\nRa 32,16,0\nRa 48,17,0\nMvaa 176,176,20\n" \
"s_add_u32 m0, s88, 135168\nTa 100,21,0\nMvaa 176,80,4\nD 13,64,s76,0\nMvaa 172,76,4\nTa 102,21,1024\nTa 132,21,8192\nMvaa 172,172,20\n" \
"Mvaa 168,168,20\nTa 134,21,9216\nTa 104,22,0\nMvaa 168,72,4\nTa 106,22,1024\nTa 136,22,8192\nMvaa 164,68,4\nD 13,64,s77,1024\n" \
"Mvaa 164,164,20\nMvaa 160,160,20\nTa 138,22,9216\nTa 108,23,0\nMvaa 160,64,4\nMvaa 192,64,8\nTa 110,23,1024\nTa 140,23,8192\n" \
"Mvaa 192,160,24\nD 13,64,s78,2048\nMvaa 196,164,24\nTa 142,23,9216\nTa 112,24,0\nMvaa 196,68,8\nMvaa 200,72,8\nTa 114,24,1024\n" \
"Ta 144,24,8192\nMvaa 200,168,24\nTa 146,24,9216\nTa 116,25,0\nMvaa 204,172,24\nD 13,64,s79,3072\nMvaa 204,76,8\nMvaa 208,80,8\n" \
"Ta 118,25,1024\nTa 148,25,8192\nMvaa 208,176,24\nv_add_u32 v13, 128, v13\nTa 150,25,9216\nMvaa 212,180,24\ns_add_u32 m0, s32, 81920\nTa 120,26,0\n" \
"Mvaa 212,84,8\nD 14,68,0,0\nMvaa 216,88,8\nTa 122,26,1024\nTa 152,26,8192\nMvaa 216,184,24\nMvaa 220,188,24\nTa 154,26,9216\n" \
"Ta 124,27,0\nMvaa 220,92,8\nMvaa 252,92,12\nD 14,68,s89,1024\nMvaa 252,188,28\nTa 126,27,1024\nTa 156,27,8192\nMvaa 248,184,28\n" \
"Ta 158,27,9216\nRa 36,16,2048\nMvaa 248,88,12\nMvaa 244,84,12\nRa 52,17,2048\nRa 40,16,4096\nMvaa 244,180,28\nD 28,68,s90,2048\n" \
"Mvaa 240,176,28\nRa 56,17,4096\nRa 44,16,6144\nMvaa 240,80,12\nMvaa 236,76,12\nRa 60,17,6144\nMvaa 236,172,28\nMvaa 232,168,28\n" \
"D 28,68,s91,3072\nMvaa 232,72,12\nMvaa 228,68,12\nMvaa 228,164,28\nv_add_u32 v14, s72, v14\nMvaa 224,160,28\nv_add_u32 v28, s72, v28\nMvaa 224,64,12\n" \
"s_waitcnt lgkmcnt(6)\nMvaa 128,96,32\nMvaa 128,128,48\nbuffer_load_dwordx2 v[10:11], v1, s[60:63], 0 offen offset:0 sc1\nMvaa 132,132,48\nv_subrev_u32 v9, s32, v0\nv_add_u32 v8, 0x1004200, v0\nMvaa 132,100,32\n" \
"s_add_u32 s77, s38, s32\nv_mov_b32 v12, 0xc2200000\nMvaa 136,104,32\nv_mov_b32 v13, 0xc2200000\nv_mov_b32 v14, 0xc2200000\nMvaa 136,136,48\nv_mov_b32 v15, 0xc2200000\nv_bfe_u32 v29, v0, 4, 4\n" \
"Mvaa 140,140,48\nv_lshrrev_b32 v28, 8, v0\ns_lshr_b32 s96, s32, 8\nMvaa 140,108,32\ns_waitcnt lgkmcnt(0)\nv_lshlrev_b32 v6, 2, v28\nMvaa 144,112,32\nv_add_u32 v30, s96, v29\n" \
"v_sub_u32 v30, v30, v6\nMvaa 144,144,48\nv_mov_b32 v6, 0xff800000\nv_cmp_gt_i32 s[90:91], 0, v30\nMvaa 148,148,48\nv_cmp_gt_i32 s[92:93], 1, v30\nv_cmp_gt_i32 s[74:75], 2, v30\nMvaa 148,116,32\n" \
"v_cmp_gt_i32 s[78:79], 3, v30\nv_cndmask_b32 v112, v12, v6, s[90:91]\nMvaa 152,120,32\nv_cndmask_b32 v113, v12, v6, s[92:93]\nv_cndmask_b32 v114, v12, v6, s[74:75]\nMvaa 152,152,48\nv_cndmask_b32 v115, v12, v6, s[78:79]\nv_cmp_gt_i32 s[90:91], 16, v30\n" \
"Mvaa 156,156,48\nv_cmp_gt_i32 s[92:93], 17, v30\nv_cmp_gt_i32 s[74:75], 18, v30\nMvaa 156,124,32\nv_cmp_gt_i32 s[78:79], 19, v30\nv_cndmask_b32 v116, v12, v6, s[90:91]\nMvaa 188,124,36\ns_waitcnt vmcnt(13)\n" \
"s_barrier\nMvaa 188,156,52\nTa 64,20,32768\nv_cndmask_b32 v117, v12, v6, s[92:93]\nMvaa 184,152,52\nTa 66,20,33792\nv_cndmask_b32 v118, v12, v6, s[74:75]\nMvaa 184,120,36\n" \
"Ta 160,20,40960\nv_cndmask_b32 v119, v12, v6, s[78:79]\nMvaa 180,116,36\nTa 162,20,41984\nv_cmp_gt_i32 s[90:91], 32, v30\nMvaa 180,148,52\nRa 0,16,32768\nv_cmp_gt_i32 s[92:93], 33, v30\n" \
"Mvaa 176,144,52\nRa 16,17,32768\nv_cmp_gt_i32 s[74:75], 34, v30\nMvaa 176,112,36\nTa 68,21,32768\nv_cmp_gt_i32 s[78:79], 35, v30\nMvaa 172,108,36\nTa 70,21,33792\n" \
"v_cndmask_b32 v120, v12, v6, s[90:91]\nMvaa 172,140,52\nTa 164,21,40960\nv_cndmask_b32 v121, v12, v6, s[92:93]\nMvaa 168,136,52\nTa 166,21,41984\nv_cndmask_b32 v122, v12, v6, s[74:75]\nMvaa 168,104,36\n" \
"Ta 72,22,32768\nv_cndmask_b32 v123, v12, v6, s[78:79]\nMvaa 164,100,36\nTa 74,22,33792\nv_cmp_gt_i32 s[90:91], 48, v30\nMvaa 164,132,52\nTa 168,22,40960\nv_cmp_gt_i32 s[92:93], 49, v30\n" \
"Mvaa 160,128,52\nTa 170,22,41984\nv_cmp_gt_i32 s[74:75], 50, v30\nMvaa 160,96,36\nTa 76,23,32768\nv_cmp_gt_i32 s[78:79], 51, v30\nMvaa 192,96,40\nTa 78,23,33792\n" \
"v_cndmask_b32 v124, v12, v6, s[90:91]\nMvaa 192,128,56\nTa 172,23,40960\nv_cndmask_b32 v125, v12, v6, s[92:93]\nMvaa 196,132,56\nTa 174,23,41984\nv_cndmask_b32 v126, v12, v6, s[74:75]\nMvaa 196,100,40\n" \
"Ta 80,24,32768\nv_cndmask_b32 v127, v12, v6, s[78:79]\nMvaa 200,104,40\nTa 82,24,33792\nMvaa 200,136,56\nMvaa 204,140,56\nTa 176,24,40960\nMvaa 204,108,40\n" \
"Ta 178,24,41984\nMvaa 208,112,40\nTa 84,25,32768\nMvaa 208,144,56\nTa 86,25,33792\nMvaa 212,148,56\nTa 180,25,40960\nMvaa 212,116,40\n" \
"Ta 182,25,41984\nMvaa 216,120,40\nTa 88,26,32768\nMvaa 216,152,56\nTa 90,26,33792\nMvaa 220,156,56\nTa 184,26,40960\nMvaa 220,124,40\n" \
"Ta 186,26,41984\nMvaa 252,124,44\nTa 92,27,32768\nMvaa 252,156,60\nTa 94,27,33792\nMvaa 248,152,60\nTa 188,27,40960\nMvaa 248,120,44\n" \
"Ta 190,27,41984\nMvaa 244,116,44\nRa 4,16,34816\nMvaa 244,148,60\nRa 20,17,34816\nMvaa 240,144,60\nRa 8,16,36864\nMvaa 240,112,44\n" \
"Ra 24,17,36864\nMvaa 236,108,44\nRa 12,16,38912\nMvaa 236,140,60\nRa 28,17,38912\nMvaa 232,136,60\nMvaa 232,104,44\nMvaa 228,100,44\n" \
"Mvaa 228,132,60\nMvaa 224,128,60\nMvaa 224,96,44\ns_waitcnt lgkmcnt(6)\nMvaa 128,64,0\nv_and_b32 v30, 3, v29\nv_bfe_u32 v6, v29, 1, 1\nMvaa 128,160,16\n" \
"v_lshrrev_b32 v28, 1, v28\nv_cmp_eq_u32 s[90:91], 2, v30\nMvaa 132,164,16\nv_cmp_eq_u32 s[92:93], v6, v28\nv_and_b32 v30, 1, v29\nMvaa 132,68,0\nv_mov_b32 v6, 0x3f803f80\nv_cmp_eq_u32 s[78:79], 1, v30\n" \
"Mvaa 136,72,0\nv_mov_b32 v7, 0x4444\nv_mov_b32 v30, 0xeeee\nMvaa 136,168,16\nv_cndmask_b32 v108, 0, v6, s[90:91]\nv_cndmask_b32 v109, 0, v6, s[90:91]\nMvaa 140,172,16\nv_cndmask_b32 v110, 0, v6, s[90:91]\n" \
"v_cndmask_b32 v111, 0, v6, s[90:91]\nMvaa 140,76,0\ns_waitcnt lgkmcnt(0)\nv_cndmask_b32 v104, 0, v6, s[92:93]\nMvaa 144,80,0\nv_cndmask_b32 v105, 0, v6, s[92:93]\nv_cndmask_b32 v106, 0, v6, s[92:93]\nMvaa 144,176,16\n" \
"v_cndmask_b32 v107, 0, v6, s[92:93]\nv_cndmask_b32 v7, v7, v30, s[78:79]\nMvaa 148,180,16\nv_mov_b32 v96, 0\nv_mov_b32 v97, 0\nMvaa 148,84,0\nv_mov_b32 v98, 0\nv_mov_b32 v99, 0\n" \
"Mvaa 152,88,0\nv_mov_b32 v100, 0\nv_mov_b32 v101, 0\nMvaa 152,184,16\nv_mov_b32 v102, 0\nv_mov_b32 v103, 0\nMvaa 156,188,16\nv_mov_b32 v5, v0\n" \
"s_mov_b32 s73, s32\nMvaa 156,92,0\ns_add_u32 s74, s32, 0x4000\ns_add_u32 s75, s32, 0x8000\nMvaa 188,92,4\ns_waitcnt vmcnt(0)\ns_barrier\nv_cmp_ne_u64 vcc, v[10:11], v[2:3]\n" \
"s_cbranch_vccnz zs102\nzr102:\nMvaa 188,188,20\ns_mov_b32 m0, s32\nTa 96,20,16384\nMvaa 184,184,20\nDS 0,60,s77,0\nMvaa 184,88,4\n" \
"Ta 98,20,17408\nTa 128,20,24576\nMvaa 180,84,4\nTa 130,20,25600\ns_mov_b32 s97, 0x3e0293ee\nMvaa 180,180,20\nRa 32,18,0\ns_mov_b32 s89, 31744\n" \
"Mvaa 176,176,20\nDS 0,60,s77,1024\nMvaa 176,80,4\nRa 48,19,0\nTa 100,21,16384\nMvaa 172,76,4\nTa 102,21,17408\ns_mov_b32 s90, 6144\n" \
"Mvaa 172,172,20\nTa 132,21,24576\ns_mov_b32 s91, 37888\nMvaa 168,168,20\nDS 0,60,s77,2048\nMvaa 168,72,4\nTa 134,21,25600\nTa 104,22,16384\n" \
"Mvaa 164,68,4\nTa 106,22,17408\ns_mov_b32 s92, 0x200000\nMvaa 164,164,20\nTa 136,22,24576\nTa 138,22,25600\nMvaa 160,160,20\nDS 0,60,s77,3072\n" \
"Mvaa 160,64,4\nTa 108,23,16384\ns_mov_b32 s78, s11\nMvaa 192,64,8\nTa 110,23,17408\ns_add_u32 s84, s32, 98304\nMvaa 192,160,24\ns_add_u32 m0, s32, 16384\n" \
"s_add_u32 s77, s77, 0x4000\nMvaa 196,164,24\nDS 0,60,s77,0\nMvaa 196,68,8\nTa 140,23,24576\nTa 142,23,25600\nMvaa 200,72,8\nTa 112,24,16384\n" \
"s_mov_b32 s85, 8192\nMvaa 200,168,24\nTa 114,24,17408\ns_mov_b32 s86, 16384\nMvaa 204,172,24\nDS 0,60,s77,1024\nMvaa 204,76,8\nTa 144,24,24576\n" \
"Ta 146,24,25600\nMvaa 208,80,8\nTa 116,25,16384\ns_mov_b32 s87, 24576\nMvaa 208,176,24\nTa 118,25,17408\nTa 148,25,24576\nMvaa 212,180,24\n" \
"DS 0,60,s77,2048\nMvaa 212,84,8\nTa 150,25,25600\ns_xor_b32 s82, s11, s10\nMvaa 216,88,8\ns_branch 99f\n.p2align 12\nzp35:\n" \
"s_setpc_b64 s[98:99]\n99:\nTa 120,26,16384\ns_lshl_b32 s83, s82, 16\nMvaa 216,184,24\nTa 122,26,17408\nTa 152,26,24576\nMvaa 220,188,24\n" \
"DS 0,60,s77,3072\nMvaa 220,92,8\nTa 154,26,25600\ns_add_u32 s83, s83, s18\nMvaa 252,92,12\nTa 124,27,16384\nTa 126,27,17408\nMvaa 252,188,28\n" \
"s_add_u32 m0, s32, 49152\nMvaa 248,184,28\nDS 8,60,s77,0\nMvaa 248,88,12\nTa 156,27,24576\nTa 158,27,25600\nMvaa 244,84,12\nRa 36,18,2048\n" \
"Mvaa 244,180,28\nRa 52,19,2048\nMvaa 240,176,28\nDS 8,60,s77,1024\nMvaa 240,80,12\nRa 40,18,4096\nRa 56,19,4096\nMvaa 236,76,12\n" \
"Ra 44,18,6144\nMvaa 236,172,28\nRa 60,19,6144\nMvaa 232,168,28\nDS 8,60,s77,2048\nMvaa 232,72,12\nMvaa 228,68,12\nMvaa 228,164,28\n" \
"Mvaa 224,160,28\nDS 8,60,s77,3072\nMvaa 224,64,12\ns_waitcnt lgkmcnt(6)\nMvaa 128,96,32\ns_add_u32 m0, s32, 32768\ns_add_u32 s77, s77, 0x4000\nMvaa 128,128,48\n" \
"DS 0,60,s77,0\nMvaa 132,132,48\nv_lshrrev_b32 v17, 8, v0\nv_and_b32 v16, 0xf0, v0\nMvaa 132,100,32\nDS 0,60,s77,1024\nMvaa 136,104,32\nv_lshlrev_b32 v18, 5, v17\n" \
"v_xor_b32 v16, v16, v18\nMvaa 136,136,48\nDS 0,60,s77,2048\nMvaa 140,140,48\nv_lshl_add_u32 v16, v17, 11, v16\nv_add_u32 v16, s20, v16\nMvaa 140,108,32\ns_waitcnt lgkmcnt(0)\n" \
"DS 0,60,s77,3072\nMvaa 144,112,32\ns_add_u32 m0, s32, 65536\ns_xor_b32 s82, s78, s10\nMvaa 144,144,48\nDS 8,60,s77,0\nMvaa 148,148,48\ns_and_b32 s80, s82, 31\n" \
"s_lshl_b32 s80, s80, 18\nMvaa 148,116,32\nDS 8,60,s77,1024\nMvaa 152,120,32\ns_cmp_lt_u32 s78, 32\ns_cselect_b64 s[68:69], s[14:15], s[12:13]\nMvaa 152,152,48\nDS 8,60,s77,2048\n" \
"Mvaa 156,156,48\ns_cselect_b32 s70, 0x800000, s92\nv_add_u32 v18, s80, v16\nMvaa 156,124,32\nDS 8,60,s77,3072\nMvaa 160,96,36\ns_add_u32 s77, s77, 0x4000\nv_xor_b32 v19, 128, v18\n" \
"Mvaa 160,128,52\nMvaa 164,132,52\nMvaa 164,100,36\nMvaa 168,104,36\nMvaa 168,136,52\nv_mul_f32 v128, s97, v128\nv_mul_f32 v129, s97, v129\nv_mul_f32 v130, s97, v130\n" \
"Mvaa 172,140,52\nv_mul_f32 v131, s97, v131\nv_mul_f32 v132, s97, v132\nv_mul_f32 v133, s97, v133\nMvaa 172,108,36\nv_mul_f32 v134, s97, v134\nv_mul_f32 v135, s97, v135\nC 32,128,129\n" \
"Mvaa 176,112,36\nC 33,130,131\nv_mul_f32 v136, s97, v136\nv_mul_f32 v137, s97, v137\nMvaa 176,144,52\nv_mul_f32 v138, s97, v138\nv_mul_f32 v139, s97, v139\nC 34,132,133\n" \
"Mvaa 180,148,52\nC 35,134,135\nv_mul_f32 v140, s97, v140\nv_mul_f32 v141, s97, v141\nMvaa 180,116,36\nv_mul_f32 v142, s97, v142\nv_mul_f32 v143, s97, v143\nC 36,136,137\n" \
"Mvaa 184,120,36\nC 37,138,139\nv_mul_f32 v144, s97, v144\nv_mul_f32 v145, s97, v145\nMvaa 184,152,52\nv_mul_f32 v146, s97, v146\nv_mul_f32 v147, s97, v147\nC 38,140,141\n" \
"Mvaa 188,156,52\nC 39,142,143\nv_mul_f32 v148, s97, v148\nv_mul_f32 v149, s97, v149\nMvaa 188,124,36\nv_mul_f32 v150, s97, v150\nv_mul_f32 v151, s97, v151\nC 40,144,145\n" \
"Mvaa 192,96,40\nC 41,146,147\nv_mul_f32 v152, s97, v152\nv_mul_f32 v153, s97, v153\nMvaa 192,128,56\nv_mul_f32 v154, s97, v154\nv_mul_f32 v155, s97, v155\nC 42,148,149\n" \
"Mvaa 196,132,56\nC 43,150,151\nv_mul_f32 v156, s97, v156\nv_mul_f32 v157, s97, v157\nMvaa 196,100,40\nv_mul_f32 v158, s97, v158\nv_mul_f32 v159, s97, v159\nC 44,152,153\n" \
"Mvaa 200,104,40\nC 45,154,155\nC 46,156,157\nC 47,158,159\nMvaa 200,136,56\nv_mul_f32 v160, s97, v160\nv_mul_f32 v161, s97, v161\nv_mul_f32 v162, s97, v162\n" \
"Mvaa 204,140,56\nv_mul_f32 v163, s97, v163\nv_mul_f32 v164, s97, v164\nv_mul_f32 v165, s97, v165\nMvaa 204,108,40\nv_mul_f32 v166, s97, v166\nv_mul_f32 v167, s97, v167\nC 48,160,161\n" \
"Mvaa 208,112,40\ns_waitcnt vmcnt(16)\ns_barrier\nC 49,162,163\nMvaa 208,144,56\nRa 160,5,0\nv_mul_f32 v168, s97, v168\nv_mul_f32 v169, s97, v169\n" \
"Mvaa 212,148,56\nv_mul_f32 v170, s97, v170\nv_mul_f32 v171, s97, v171\nC 50,164,165\nMvaa 212,116,40\nRa 164,5,1024\nC 51,166,167\nv_mul_f32 v172, s97, v172\n" \
"Mvaa 216,120,40\nv_mul_f32 v173, s97, v173\nv_mul_f32 v174, s97, v174\nv_mul_f32 v175, s97, v175\nMvaa 216,152,56\nRa 168,5,2048\nC 52,168,169\nC 53,170,171\n" \
"Mvaa 220,156,56\nv_mul_f32 v176, s97, v176\nv_mul_f32 v177, s97, v177\nv_mul_f32 v178, s97, v178\nMvaa 220,124,40\nRa 172,5,3072\nv_mul_f32 v179, s97, v179\nC 54,172,173\n" \
"Mvaa 224,96,44\nC 55,174,175\nv_mul_f32 v180, s97, v180\nv_mul_f32 v181, s97, v181\nMvaa 224,128,60\nRa 176,5,4096\nv_mul_f32 v182, s97, v182\nv_mul_f32 v183, s97, v183\n" \
"Mvaa 228,132,60\nC 56,176,177\nC 57,178,179\nv_mul_f32 v184, s97, v184\nMvaa 228,100,44\nRa 180,5,5120\nv_mul_f32 v185, s97, v185\nv_mul_f32 v186, s97, v186\n" \
"Mvaa 232,104,44\nv_mul_f32 v187, s97, v187\nC 58,180,181\nC 59,182,183\nMvaa 232,136,60\nRa 184,5,6144\nv_mul_f32 v188, s97, v188\nv_mul_f32 v189, s97, v189\n" \
"Mvaa 236,140,60\nv_mul_f32 v190, s97, v190\nv_mul_f32 v191, s97, v191\nC 60,184,185\nMvaa 236,108,44\nRa 188,5,7168\nC 61,186,187\nC 62,188,189\n" \
"Mvaa 240,112,44\nC 63,190,191\nv_mul_f32 v192, s97, v192\nv_mul_f32 v193, s97, v193\nMvaa 240,144,60\nv_mul_f32 v194, s97, v194\nv_mul_f32 v195, s97, v195\nv_mul_f32 v196, s97, v196\n" \
"Mvaa 244,148,60\nv_mul_f32 v197, s97, v197\nv_mul_f32 v198, s97, v198\nv_mul_f32 v199, s97, v199\nMvaa 244,116,44\nC 64,192,193\nC 65,194,195\nv_mul_f32 v200, s97, v200\n" \
"Mvaa 248,120,44\ns_bitcmp1_b32 s33, 2\nv_mul_f32 v201, s97, v201\nv_mul_f32 v202, s97, v202\nMvaa 248,152,60\nv_mul_f32 v203, s97, v203\nC 66,196,197\nC 67,198,199\n" \
"Mvaa 252,156,60\nMvaa 252,124,44\ns_waitcnt lgkmcnt(0)\ns_cbranch_scc1 zZ\nQvav 128,160,32,12\nRa 128,5,8192\nv_mul_f32 v204, s97, v204\nv_mul_f32 v205, s97, v205\n" \
"Mvav 128,164,36\nv_mul_f32 v206, s97, v206\nv_mul_f32 v207, s97, v207\nC 68,200,201\nMvav 128,168,40\nRa 132,5,9216\nC 69,202,203\nv_mul_f32 v208, s97, v208\n" \
"Mvav 128,172,44\nv_mul_f32 v209, s97, v209\nv_mul_f32 v210, s97, v210\nv_mul_f32 v211, s97, v211\nQvav 132,176,32,12\nRa 136,5,10240\nC 70,204,205\nC 71,206,207\n" \
"Mvav 132,180,36\nv_mul_f32 v212, s97, v212\nv_mul_f32 v213, s97, v213\nv_mul_f32 v214, s97, v214\nMvav 132,184,40\nRa 140,5,11264\nv_mul_f32 v215, s97, v215\nC 72,208,209\n" \
"Mvav 132,188,44\nC 73,210,211\nv_mul_f32 v216, s97, v216\nv_mul_f32 v217, s97, v217\nQvav 136,160,48,12\nRa 144,5,12288\nv_mul_f32 v218, s97, v218\nv_mul_f32 v219, s97, v219\n" \
"Mvav 136,164,52\nC 74,212,213\nC 75,214,215\nv_mul_f32 v220, s97, v220\nMvav 136,168,56\nRa 148,5,13312\nv_mul_f32 v221, s97, v221\nv_mul_f32 v222, s97, v222\n" \
"Mvav 136,172,60\nv_mul_f32 v223, s97, v223\nC 76,216,217\nC 77,218,219\nQvav 140,176,48,12\nRa 152,5,14336\nC 78,220,221\nC 79,222,223\n" \
"Mvav 140,180,52\nv_mul_f32 v224, s97, v224\nv_mul_f32 v225, s97, v225\nv_mul_f32 v226, s97, v226\nMvav 140,184,56\nRa 156,5,15360\nv_mul_f32 v227, s97, v227\nv_mul_f32 v228, s97, v228\n" \
"Mvav 140,188,60\nv_mul_f32 v229, s97, v229\nv_mul_f32 v230, s97, v230\nv_mul_f32 v231, s97, v231\nQvav 144,160,64,12\nC 80,224,225\nC 81,226,227\nv_mul_f32 v232, s97, v232\n" \
"Mvav 144,164,68\nv_mul_f32 v233, s97, v233\nv_mul_f32 v234, s97, v234\nv_mul_f32 v235, s97, v235\nMvav 144,168,72\nC 82,228,229\nC 83,230,231\nv_mul_f32 v236, s97, v236\n" \
"Mvav 144,172,76\nv_mul_f32 v237, s97, v237\nv_mul_f32 v238, s97, v238\nv_mul_f32 v239, s97, v239\nQvav 148,176,64,12\nC 84,232,233\nC 85,234,235\nv_mul_f32 v240, s97, v240\n" \
"Mvav 148,180,68\nv_mul_f32 v241, s97, v241\nv_mul_f32 v242, s97, v242\nv_mul_f32 v243, s97, v243\nC 86,236,237\nC 87,238,239\nv_mul_f32 v244, s97, v244\nv_mul_f32 v245, s97, v245\n" \
"v_mul_f32 v246, s97, v246\nv_mul_f32 v247, s97, v247\nC 88,240,241\nC 89,242,243\nv_mul_f32 v248, s97, v248\nv_mul_f32 v249, s97, v249\nv_mul_f32 v250, s97, v250\nv_mul_f32 v251, s97, v251\n" \
"C 90,244,245\nC 91,246,247\nv_mul_f32 v252, s97, v252\nv_mul_f32 v253, s97, v253\nv_mul_f32 v254, s97, v254\nv_mul_f32 v255, s97, v255\nC 92,248,249\nC 93,250,251\n" \
"C 94,252,253\nC 95,254,255\nMvav 148,184,72\nE 240,128\nMvav 148,188,76\nE 241,129\nQvav 152,160,80,12\nC 192,240,241\n" \
"E 242,130\nMvav 152,164,84\nE 243,131\nMvav 152,168,88\nC 193,242,243\nMvav 152,172,92\nE 244,132\nQvav 156,176,80,12\n" \
"E 245,133\nMvav 156,180,84\nC 194,244,245\nE 246,134\nMvav 156,184,88\nE 247,135\nMvav 156,188,92\ns_waitcnt lgkmcnt(0)\n" \
"C 195,246,247\nQvav 160,128,32,12\nE 240,136\nMvav 160,132,36\nE 241,137\nMvav 160,136,40\nC 196,240,241\nE 242,138\n" \
"Mvav 160,140,44\nQvav 168,128,48,12\nv_mov_b32 v6, v5\nv_add_u32 v5, s74, v9\nE 243,139\nMvav 168,132,52\nC 197,242,243\nE 244,140\n" \
"Mvav 168,136,56\nE 245,141\nMvav 168,140,60\nC 198,244,245\nE 246,142\nQvav 176,128,64,12\ns_waitcnt vmcnt(8)\ns_barrier\n" \
"buffer_store_dwordx2 v[2:3], off, s[60:63], s36 sc1\nMvav 176,132,68\nRa 160,5,0\nE 247,143\nMvav 176,136,72\ns_mov_b32 m0, s73\nC 199,246,247\nE 240,144\n" \
"Mvav 176,140,76\nRa 164,5,1024\nDS 0,60,s77,0\nE 241,145\nQvav 184,128,80,12\nC 200,240,241\nMvav 184,132,84\nRa 168,5,2048\n" \
"E 242,146\nMvav 184,136,88\nE 243,147\nMvav 184,140,92\nRa 172,5,3072\nC 201,242,243\nE 244,148\nQvav 164,144,32,12\n" \
"DS 0,60,s77,1024\nE 245,149\nMvav 164,148,36\nRa 176,5,4096\nC 202,244,245\nMvav 164,152,40\nE 246,150\nMvav 164,156,44\n" \
"Ra 180,5,5120\nE 247,151\nQvav 172,144,48,12\nC 203,246,247\nE 240,152\nMvav 172,148,52\nRa 184,5,6144\nDS 0,60,s77,2048\n" \
"Mvav 172,152,56\nE 241,153\nMvav 172,156,60\nRa 188,5,7168\nC 204,240,241\nE 242,154\nQvav 180,144,64,12\nE 243,155\n" \
"Mvav 180,148,68\nC 205,242,243\nE 244,156\nMvav 180,152,72\nDS 0,60,s77,3072\nMvav 180,156,76\nE 245,157\nQvav 188,144,80,12\n" \
"C 206,244,245\nE 246,158\nMvav 188,148,84\nE 247,159\nMvav 188,152,88\nC 207,246,247\ns_mov_b32 s96, s39\nMvav 188,156,92\n" \
"s_waitcnt lgkmcnt(0)\nQvav 128,160,32,12\nE 240,160\nMvav 128,164,36\nE 241,161\nMvav 128,168,40\nC 208,240,241\nRa 128,6,49152\n" \
"Mvav 128,172,44\nE 242,162\ns_add_u32 m0, s75, 0xc000\nQvav 136,160,48,12\nDS 8,60,s77,0\nMvav 136,164,52\nE 243,163\nMvav 136,168,56\n" \
"C 209,242,243\nRa 132,6,50176\nMvav 136,172,60\nE 244,164\nQvav 144,160,64,12\nDS 8,60,s77,1024\nMvav 144,164,68\nE 245,165\n" \
"Mvav 144,168,72\nC 210,244,245\nMvav 144,172,76\nE 246,166\nQvav 152,160,80,12\nRa 136,6,51200\nRa 140,6,52224\nMvav 152,164,84\n" \
"E 247,167\nMvav 152,168,88\nC 211,246,247\nMvav 152,172,92\nE 240,168\nQvav 132,176,32,12\nDS 8,60,s77,2048\nMvav 132,180,36\n" \
"E 241,169\nMvav 132,184,40\nC 212,240,241\nRa 144,6,53248\nMvav 132,188,44\nE 242,170\nQvav 140,176,48,12\nRa 148,6,54272\n" \
"Mvav 140,180,52\nE 243,171\nMvav 140,184,56\nC 213,242,243\nRa 152,6,55296\nMvav 140,188,60\nE 244,172\nQvav 148,176,64,12\n" \
"DS 8,60,s77,3072\nMvav 148,180,68\nE 245,173\nMvav 148,184,72\nC 214,244,245\ns_mov_b32 s76, s73\nMvav 148,188,76\nE 246,174\n" \
"Qvav 156,176,80,12\nRa 156,6,56320\ns_mov_b32 s73, s74\nMvav 156,180,84\nE 247,175\nMvav 156,184,88\nC 215,246,247\ns_mov_b32 s74, s75\n" \
"Mvav 156,188,92\nE 240,176\nv_smfmac_f32_16x16x64_bf16 v[96:99], v[104:107], v[192:199], v7\ns_mov_b32 s75, s76\nv_smfmac_f32_16x16x64_bf16 v[100:103], v[104:107], v[200:207], v7\ns_waitcnt lgkmcnt(0)\nZaav 96,128,204\nRa 160,5,8192\n" \
"s_add_u32 s77, s77, 0x4000\nZaav 100,132,204\nE 241,177\nZaav 68,132,200\nC 216,240,241\nRa 164,5,9216\nZaav 64,128,200\nE 242,178\n" \
"Zaav 32,128,196\nZaav 36,132,196\nE 243,179\nZaav 4,132,192\nC 217,242,243\nRa 168,5,10240\nZaav 0,128,192\nE 244,180\n" \
"Zaav 8,136,192\nRa 172,5,11264\nZaav 12,140,192\nE 245,181\nZaav 44,140,196\nC 218,244,245\nZaav 40,136,196\nE 246,182\n" \
"Zaav 72,136,200\nRa 176,5,12288\nZaav 76,140,200\nE 247,183\nZaav 108,140,204\nC 219,246,247\nZaav 104,136,204\nE 240,184\n" \
"Zaav 112,144,204\nRa 180,5,13312\nZaav 116,148,204\nE 241,185\nZaav 84,148,200\nC 220,240,241\nZaav 80,144,200\nE 242,186\n" \
"Zaav 48,144,196\nRa 184,5,14336\nZaav 52,148,196\nE 243,187\nZaav 20,148,192\nC 221,242,243\nRa 188,5,15360\nZaav 16,144,192\n" \
"E 244,188\nZaav 24,152,192\nZaav 28,156,192\nE 245,189\nZaav 60,156,196\nC 222,244,245\nZaav 56,152,196\nE 246,190\n" \
"Zaav 88,152,200\nZaav 92,156,200\nE 247,191\nZaav 124,156,204\nC 223,246,247\ns_waitcnt lgkmcnt(0)\nZaav 120,152,204\ns_branch zE\n" \
"zZ:\nQvav 128,160,32,112\nRa 128,5,8192\ns_branch 99f\n.p2align 12\nzp36:\ns_setpc_b64 s[98:99]\n99:\n" \
"v_mul_f32 v204, s97, v204\nv_mul_f32 v205, s97, v205\nMvav 128,164,36\nv_mul_f32 v206, s97, v206\nv_mul_f32 v207, s97, v207\nC 68,200,201\nMvav 128,168,40\nRa 132,5,9216\n" \
"C 69,202,203\nv_mul_f32 v208, s97, v208\nMvav 128,172,44\nv_mul_f32 v209, s97, v209\nv_mul_f32 v210, s97, v210\nv_mul_f32 v211, s97, v211\nQvav 132,176,32,116\nRa 136,5,10240\n" \
"C 70,204,205\nC 71,206,207\nMvav 132,180,36\nv_mul_f32 v212, s97, v212\nv_mul_f32 v213, s97, v213\nv_mul_f32 v214, s97, v214\nMvav 132,184,40\nRa 140,5,11264\n" \
"v_mul_f32 v215, s97, v215\nC 72,208,209\nMvav 132,188,44\nC 73,210,211\nv_mul_f32 v216, s97, v216\nv_mul_f32 v217, s97, v217\nQvav 136,160,48,12\nRa 144,5,12288\n" \
"v_mul_f32 v218, s97, v218\nv_mul_f32 v219, s97, v219\nMvav 136,164,52\nC 74,212,213\nC 75,214,215\nv_mul_f32 v220, s97, v220\nMvav 136,168,56\nRa 148,5,13312\n" \
"v_mul_f32 v221, s97, v221\nv_mul_f32 v222, s97, v222\nMvav 136,172,60\nv_mul_f32 v223, s97, v223\nC 76,216,217\nC 77,218,219\nQvav 140,176,48,12\nRa 152,5,14336\n" \
"C 78,220,221\nC 79,222,223\nMvav 140,180,52\nv_mul_f32 v224, s97, v224\nv_mul_f32 v225, s97, v225\nv_mul_f32 v226, s97, v226\nMvav 140,184,56\nRa 156,5,15360\n" \
"v_mul_f32 v227, s97, v227\nv_mul_f32 v228, s97, v228\nMvav 140,188,60\nv_mul_f32 v229, s97, v229\nv_mul_f32 v230, s97, v230\nv_mul_f32 v231, s97, v231\nQvav 144,160,64,12\nC 80,224,225\n" \
"C 81,226,227\nv_mul_f32 v232, s97, v232\nMvav 144,164,68\nv_mul_f32 v233, s97, v233\nv_mul_f32 v234, s97, v234\nv_mul_f32 v235, s97, v235\nMvav 144,168,72\nC 82,228,229\n" \
"C 83,230,231\nv_mul_f32 v236, s97, v236\nMvav 144,172,76\nv_mul_f32 v237, s97, v237\nv_mul_f32 v238, s97, v238\nv_mul_f32 v239, s97, v239\nQvav 148,176,64,12\nC 84,232,233\n" \
"C 85,234,235\nv_mul_f32 v240, s97, v240\nMvav 148,180,68\nv_mul_f32 v241, s97, v241\nv_mul_f32 v242, s97, v242\nv_mul_f32 v243, s97, v243\nC 86,236,237\nC 87,238,239\n" \
"v_mul_f32 v244, s97, v244\nv_mul_f32 v245, s97, v245\nv_mul_f32 v246, s97, v246\nv_mul_f32 v247, s97, v247\nC 88,240,241\nC 89,242,243\nv_mul_f32 v248, s97, v248\nv_mul_f32 v249, s97, v249\n" \
"v_mul_f32 v250, s97, v250\nv_mul_f32 v251, s97, v251\nC 90,244,245\nC 91,246,247\nv_mul_f32 v252, s97, v252\nv_mul_f32 v253, s97, v253\nv_mul_f32 v254, s97, v254\nv_mul_f32 v255, s97, v255\n" \
"C 92,248,249\nC 93,250,251\nC 94,252,253\nC 95,254,255\nMvav 148,184,72\nE 240,128\nMvav 148,188,76\nE 241,129\n" \
"Qvav 152,160,80,12\nC 192,240,241\nE 242,130\nMvav 152,164,84\nE 243,131\nMvav 152,168,88\nC 193,242,243\nMvav 152,172,92\n" \
"E 244,132\nQvav 156,176,80,12\nE 245,133\nMvav 156,180,84\nC 194,244,245\nE 246,134\nMvav 156,184,88\nE 247,135\n" \
"Mvav 156,188,92\ns_waitcnt lgkmcnt(0)\nC 195,246,247\nQvav 160,128,32,120\nE 240,136\nMvav 160,132,36\nE 241,137\nMvav 160,136,40\n" \
"C 196,240,241\nE 242,138\nMvav 160,140,44\nQvav 168,128,48,12\nv_mov_b32 v6, v5\nv_add_u32 v5, s74, v9\nE 243,139\nMvav 168,132,52\n" \
"C 197,242,243\nE 244,140\nMvav 168,136,56\nE 245,141\nMvav 168,140,60\nC 198,244,245\nE 246,142\nQvav 176,128,64,12\n" \
"s_waitcnt vmcnt(8)\ns_barrier\nbuffer_store_dwordx2 v[2:3], off, s[60:63], s36 sc1\nMvav 176,132,68\nRa 160,5,0\nE 247,143\nMvav 176,136,72\ns_mov_b32 m0, s73\n" \
"C 199,246,247\nE 240,144\nMvav 176,140,76\nRa 164,5,1024\nDS 0,60,s77,0\nE 241,145\nQvav 184,128,80,12\nC 200,240,241\n" \
"Mvav 184,132,84\nRa 168,5,2048\nE 242,146\nMvav 184,136,88\nE 243,147\nMvav 184,140,92\nRa 172,5,3072\nC 201,242,243\n" \
"E 244,148\nQvav 164,144,32,124\nDS 0,60,s77,1024\nE 245,149\nMvav 164,148,36\nRa 176,5,4096\nC 202,244,245\nMvav 164,152,40\n" \
"E 246,150\nMvav 164,156,44\nRa 180,5,5120\nE 247,151\nQvav 172,144,48,12\nC 203,246,247\nE 240,152\nMvav 172,148,52\n" \
"Ra 184,5,6144\nDS 0,60,s77,2048\nMvav 172,152,56\nE 241,153\nMvav 172,156,60\nRa 188,5,7168\nC 204,240,241\nE 242,154\n" \
"Qvav 180,144,64,12\nE 243,155\nMvav 180,148,68\nC 205,242,243\nE 244,156\nMvav 180,152,72\nDS 0,60,s77,3072\nMvav 180,156,76\n" \
"E 245,157\nQvav 188,144,80,12\nC 206,244,245\nE 246,158\nMvav 188,148,84\nE 247,159\nMvav 188,152,88\nC 207,246,247\n" \
"s_mov_b32 s96, s39\nMvav 188,156,92\ns_waitcnt lgkmcnt(0)\nQvav 136,160,48,112\nE 240,160\nRa 128,6,49152\nMvav 136,164,52\nE 241,161\n" \
"Mvav 136,168,56\nC 208,240,241\nRa 132,6,50176\nMvav 136,172,60\nE 242,162\ns_add_u32 m0, s75, 0xc000\nQvav 144,160,64,12\nDS 8,60,s77,0\n" \
"Mvav 144,164,68\nE 243,163\nRa 136,6,51200\nMvav 144,168,72\nC 209,242,243\nE 244,164\nMvav 144,172,76\nDS 8,60,s77,1024\n" \
"Qvav 152,160,80,12\nE 245,165\nMvav 152,164,84\nC 210,244,245\nRa 140,6,52224\nMvav 152,168,88\nE 246,166\nRa 144,6,53248\n" \
"Mvav 152,172,92\nDS 8,60,s77,2048\nQvav 140,176,48,116\nE 247,167\nMvav 140,180,52\nC 211,246,247\nE 240,168\nMvav 140,184,56\n" \
"DS 8,60,s77,3072\nMvav 140,188,60\nE 241,169\nRa 148,6,54272\nQvav 148,176,64,12\nC 212,240,241\nRa 152,6,55296\nMvav 148,180,68\n" \
"E 242,170\nMvav 148,184,72\nE 243,171\nMvav 148,188,76\nC 213,242,243\nRa 156,6,56320\nQvav 156,176,80,12\nE 244,172\n" \
"Mvav 156,180,84\ns_mov_b32 s76, s73\ns_mov_b32 s73, s74\nMvav 156,184,88\nE 245,173\nMvav 156,188,92\nC 214,244,245\ns_mov_b32 s74, s75\n" \
"v_smfmac_f32_16x16x64_bf16 v[96:99], v[104:107], v[192:199], v7\ns_mov_b32 s75, s76\nv_smfmac_f32_16x16x64_bf16 v[100:103], v[104:107], v[200:207], v7\ns_waitcnt lgkmcnt(0)\nZaav 96,128,204\nE 246,174\nZaav 100,132,204\nE 247,175\n" \
"Zaav 68,132,200\nC 215,246,247\nRa 160,5,8192\nZaav 64,128,200\nE 240,176\nZaav 32,128,196\nRa 164,5,9216\ns_add_u32 s77, s77, 0x4000\n" \
"Zaav 36,132,196\nE 241,177\nZaav 4,132,192\nC 216,240,241\nE 242,178\nZaav 0,128,192\nRa 168,5,10240\nZaav 8,136,192\n" \
"E 243,179\nZaav 12,140,192\nC 217,242,243\nRa 172,5,11264\nZaav 44,140,196\nE 244,180\nZaav 40,136,196\nRa 176,5,12288\n" \
"Zaav 72,136,200\nE 245,181\nZaav 76,140,200\nC 218,244,245\nE 246,182\nZaav 108,140,204\nRa 180,5,13312\nZaav 104,136,204\n" \
"E 247,183\nZaav 112,144,204\nC 219,246,247\nZaav 116,148,204\nE 240,184\nZaav 84,148,200\nE 241,185\nZaav 80,144,200\n" \
"C 220,240,241\nRa 184,5,14336\nZaav 48,144,196\nE 242,186\nZaav 52,148,196\nRa 188,5,15360\nZaav 20,148,192\nE 243,187\n" \
"Zaav 16,144,192\nC 221,242,243\nZaav 24,152,192\nE 244,188\nZaav 28,156,192\nE 245,189\nZaav 60,156,196\nC 222,244,245\n" \
"Zaav 56,152,196\nE 246,190\nZaav 88,152,200\nZaav 92,156,200\nE 247,191\nZaav 124,156,204\nC 223,246,247\ns_waitcnt lgkmcnt(0)\n" \
"Zaav 120,152,204\ns_branch zD4\ns_branch 99f\n.p2align 12\nzp37:\ns_setpc_b64 s[98:99]\n99:\nzDa:\n" \
"Qvav 144,160,64,112\nE 240,168\nRa 128,6,49152\nMvav 144,164,68\nE 241,169\nMvav 144,168,72\nC 212,240,241\nRa 132,6,50176\n" \
"Ra 136,6,51200\nMvav 144,172,76\nE 242,170\ns_add_u32 m0, s75, 0xc000\nQvav 152,160,80,12\nDS 8,60,s77,0\nMvav 152,164,84\nE 243,171\n" \
"Ra 140,6,52224\nMvav 152,168,88\nC 213,242,243\nE 244,172\nMvav 152,172,92\nDS 8,60,s77,1024\nRa 144,6,53248\nQvav 148,176,64,116\n" \
"E 245,173\nMvav 148,180,68\nC 214,244,245\nRa 148,6,54272\nMvav 148,184,72\nE 246,174\nRa 152,6,55296\nMvav 148,188,76\n" \
"E 247,175\nQvav 156,176,80,12\nC 215,246,247\nRa 156,6,56320\nMvav 156,180,84\nE 240,176\nMvav 156,184,88\nMvav 156,188,92\n" \
"E 241,177\nMvvv 96,108,196\nC 216,240,241\nE 242,178\nv_smfmac_f32_16x16x64_bf16 v[100:103], v[104:107], v[200:207], v7\ns_waitcnt lgkmcnt(0)\nMaav 96,128,204\nDS 8,60,s77,2048\n" \
"Ra 160,5,8192\nMaav 100,132,204\nE 243,179\nMaav 68,132,200\nC 217,242,243\nRa 164,5,9216\nMaav 64,128,200\nE 244,180\n" \
"Maav 32,128,196\nE 245,181\nMaav 36,132,196\nC 218,244,245\nRa 168,5,10240\nMaav 40,136,196\nE 246,182\nRa 172,5,11264\n" \
"Maav 44,140,196\nDS 8,60,s77,3072\nMaav 76,140,200\nE 247,183\ns_add_u32 m0, s32, 114688\nMaav 72,136,200\nC 219,246,247\nE 240,184\n" \
"D 18,68,0,0\nMaav 104,136,204\nRa 176,5,12288\nRa 180,5,13312\nMaav 108,140,204\nE 241,185\nD 18,68,s89,1024\nMaav 112,144,204\n" \
"C 220,240,241\nRa 184,5,14336\nRa 188,5,15360\nMaav 116,148,204\nE 242,186\nD 19,68,s90,2048\nMaav 84,148,200\nE 243,187\n" \
"Maav 80,144,200\nC 221,242,243\nD 19,68,s91,3072\nMaav 48,144,196\nE 244,188\nMaav 52,148,196\ns_ff1_i32_b64 s78, s[8:9]\ns_bitset0_b64 s[8:9], s78\n" \
"Maav 56,152,196\nE 245,189\nMaav 60,156,196\nC 222,244,245\nE 246,190\nMaav 92,156,200\ns_xor_b32 s82, s78, s10\ns_and_b32 s80, s82, 31\n" \
"Maav 88,152,200\nE 247,191\ns_lshl_b32 s80, s80, 18\ns_cmp_lt_u32 s78, 32\ns_cselect_b64 s[68:69], s[14:15], s[12:13]\ns_cselect_b32 s70, 0x800000, s92\nv_add_u32 v18, s80, v16\nv_xor_b32 v19, 128, v18\n" \
"Maav 120,152,204\nC 223,246,247\ns_waitcnt lgkmcnt(0)\nMaav 124,156,204\ns_mov_b32 s76, s73\ns_mov_b32 s73, s74\ns_mov_b32 s74, s75\ns_mov_b32 s75, s76\n" \
"s_add_u32 s77, s77, 0x4000\nQvav 176,160,64,120\nE 240,144\ns_add_u32 m0, s32, 131072\nRa 128,6,57344\nMvav 176,164,68\nD 18,68,0,0\nMvav 176,168,72\n" \
"E 241,145\nMvav 176,172,76\nC 200,240,241\nRa 132,6,58368\nRa 136,6,59392\nQvav 184,160,80,12\nD 18,68,s89,1024\nMvav 184,164,84\n" \
"E 242,146\nMvav 184,168,88\nRa 140,6,60416\nRa 144,6,61440\nRa 148,6,62464\nMvav 184,172,92\nE 243,147\nQvav 180,176,64,124\n" \
"C 201,242,243\nRa 152,6,63488\nMvav 180,180,68\nD 19,68,s90,2048\nRa 156,6,64512\nMvav 180,184,72\nE 244,148\nMvav 180,188,76\n" \
"D 19,68,s91,3072\nQvav 188,176,80,12\nE 245,149\nMvav 188,180,84\nC 202,244,245\nv_add_u32 v18, 131072, v18\nMvav 188,184,88\nv_add_u32 v19, 131072, v19\n" \
"v_mov_b32 v6, v5\nMvav 188,188,92\nE 246,150\nMvvv 96,108,212\nv_add_u32 v5, s74, v9\nv_smfmac_f32_16x16x64_bf16 v[100:103], v[104:107], v[216:223], v7\ns_waitcnt vmcnt(20) lgkmcnt(0)\ns_barrier\n" \
"Maav 96,128,220\nE 247,151\nMaav 100,132,220\nC 203,246,247\nRa 160,5,0\nMaav 68,132,216\nRa 164,5,1024\nMaav 64,128,216\n" \
"E 240,152\nMaav 32,128,212\nRa 168,5,2048\nMaav 36,132,212\nE 241,153\nMaav 40,136,212\nC 204,240,241\nMaav 44,140,212\n" \
"Ra 172,5,3072\nv_add_f32 v196, v96, v97\nv_add_f32 v197, v98, v99\nMaav 76,140,216\nE 242,154\nv_subrev_u32 v198, 0xf800000, v196\nMaav 72,136,216\nRa 176,5,4096\n" \
"v_subrev_u32 v199, 0xf800000, v197\nv_max_u32 v198, v198, v199\nMaav 104,136,220\nE 243,155\nv_cmp_lt_u32 vcc, 0x3c000000, v198\nMaav 108,140,220\nC 205,242,243\nMaav 112,144,220\n" \
"Ra 180,5,5120\nMaav 116,148,220\nE 244,156\nMaav 84,148,216\nRa 184,5,6144\nMaav 80,144,216\nE 245,157\nMaav 48,144,212\n" \
"C 206,244,245\nRa 188,5,7168\nMaav 52,148,212\nMaav 56,152,212\nE 246,158\nMaav 60,156,212\nMaav 92,156,216\nE 247,159\n" \
"Maav 88,152,216\nC 207,246,247\nMaav 120,152,220\ns_waitcnt lgkmcnt(0)\nMaav 124,156,220\ns_cbranch_vccnz zE3\ns_nop 7\nv_rcp_f32 v196, v196\n" \
"v_rcp_f32 v197, v197\ns_add_u32 s95, s83, 0\nAR 128,0\nAR 129,1\nAR 130,2\nAR 131,3\nAR 132,4\nAR 133,5\n" \
"AR 134,6\nAR 135,7\nv_pk_mul_f32 v[128:129], v[128:129], v[196:197] op_sel:[0,0] op_sel_hi:[1,0]\nv_pk_mul_f32 v[130:131], v[130:131], v[196:197] op_sel:[0,0] op_sel_hi:[1,0]\nv_pk_mul_f32 v[132:133], v[132:133], v[196:197] op_sel:[0,0] op_sel_hi:[1,0]\nv_pk_mul_f32 v[134:135], v[134:135], v[196:197] op_sel:[0,0] op_sel_hi:[1,0]\nC 32,128,129\nC 33,130,131\n" \
"C 34,132,133\nC 35,134,135\nbuffer_store_dwordx4 v[32:35], v0, s[60:63], s95 offen offset:0 sc1\nAR 136,8\nAR 137,9\nAR 138,10\nAR 139,11\nAR 140,12\n" \
"AR 141,13\nAR 142,14\nAR 143,15\nv_pk_mul_f32 v[136:137], v[136:137], v[196:197] op_sel:[0,0] op_sel_hi:[1,0]\nv_pk_mul_f32 v[138:139], v[138:139], v[196:197] op_sel:[0,0] op_sel_hi:[1,0]\nv_pk_mul_f32 v[140:141], v[140:141], v[196:197] op_sel:[0,0] op_sel_hi:[1,0]\nv_pk_mul_f32 v[142:143], v[142:143], v[196:197] op_sel:[0,0] op_sel_hi:[1,0]\nC 36,136,137\n" \
"C 37,138,139\nC 38,140,141\nC 39,142,143\nbuffer_store_dwordx4 v[36:39], v0, s[60:63], s95 offen offset:1024 sc1\ns_add_u32 s95, s83, 32768\nAR 128,16\nAR 129,17\nAR 130,18\n" \
"AR 131,19\nAR 132,20\nAR 133,21\nAR 134,22\nAR 135,23\nv_pk_mul_f32 v[128:129], v[128:129], v[196:197] op_sel:[0,0] op_sel_hi:[1,0]\nv_pk_mul_f32 v[130:131], v[130:131], v[196:197] op_sel:[0,0] op_sel_hi:[1,0]\nv_pk_mul_f32 v[132:133], v[132:133], v[196:197] op_sel:[0,0] op_sel_hi:[1,0]\n" \
"v_pk_mul_f32 v[134:135], v[134:135], v[196:197] op_sel:[0,0] op_sel_hi:[1,0]\nC 40,128,129\nC 41,130,131\nC 42,132,133\nC 43,134,135\nbuffer_store_dwordx4 v[40:43], v0, s[60:63], s95 offen offset:0 sc1\nAR 136,24\nAR 137,25\n" \
"AR 138,26\nAR 139,27\nAR 140,28\nAR 141,29\nAR 142,30\nAR 143,31\nv_pk_mul_f32 v[136:137], v[136:137], v[196:197] op_sel:[0,0] op_sel_hi:[1,0]\nv_pk_mul_f32 v[138:139], v[138:139], v[196:197] op_sel:[0,0] op_sel_hi:[1,0]\n" \
"v_pk_mul_f32 v[140:141], v[140:141], v[196:197] op_sel:[0,0] op_sel_hi:[1,0]\nv_pk_mul_f32 v[142:143], v[142:143], v[196:197] op_sel:[0,0] op_sel_hi:[1,0]\nC 44,136,137\nC 45,138,139\nC 46,140,141\nC 47,142,143\nbuffer_store_dwordx4 v[44:47], v0, s[60:63], s95 offen offset:1024 sc1\ns_add_u32 s95, s83, 8192\n" \
"AR 128,32\nAR 129,33\nAR 130,34\nAR 131,35\nAR 132,36\nAR 133,37\nAR 134,38\nAR 135,39\n" \
"v_pk_mul_f32 v[128:129], v[128:129], v[196:197] op_sel:[0,1] op_sel_hi:[1,1]\nv_pk_mul_f32 v[130:131], v[130:131], v[196:197] op_sel:[0,1] op_sel_hi:[1,1]\nv_pk_mul_f32 v[132:133], v[132:133], v[196:197] op_sel:[0,1] op_sel_hi:[1,1]\nv_pk_mul_f32 v[134:135], v[134:135], v[196:197] op_sel:[0,1] op_sel_hi:[1,1]\nC 48,128,129\nC 49,130,131\nC 50,132,133\nC 51,134,135\n" \
"buffer_store_dwordx4 v[48:51], v0, s[60:63], s95 offen offset:0 sc1\nAR 136,40\nAR 137,41\nAR 138,42\nAR 139,43\nAR 140,44\nAR 141,45\nAR 142,46\n" \
"AR 143,47\nv_pk_mul_f32 v[136:137], v[136:137], v[196:197] op_sel:[0,1] op_sel_hi:[1,1]\nv_pk_mul_f32 v[138:139], v[138:139], v[196:197] op_sel:[0,1] op_sel_hi:[1,1]\nv_pk_mul_f32 v[140:141], v[140:141], v[196:197] op_sel:[0,1] op_sel_hi:[1,1]\nv_pk_mul_f32 v[142:143], v[142:143], v[196:197] op_sel:[0,1] op_sel_hi:[1,1]\nC 52,136,137\nC 53,138,139\nC 54,140,141\n" \
"C 55,142,143\nbuffer_store_dwordx4 v[52:55], v0, s[60:63], s95 offen offset:1024 sc1\ns_add_u32 s95, s83, 40960\nAR 128,48\nAR 129,49\nAR 130,50\nAR 131,51\nAR 132,52\n" \
"AR 133,53\nAR 134,54\nAR 135,55\nv_pk_mul_f32 v[128:129], v[128:129], v[196:197] op_sel:[0,1] op_sel_hi:[1,1]\nv_pk_mul_f32 v[130:131], v[130:131], v[196:197] op_sel:[0,1] op_sel_hi:[1,1]\nv_pk_mul_f32 v[132:133], v[132:133], v[196:197] op_sel:[0,1] op_sel_hi:[1,1]\nv_pk_mul_f32 v[134:135], v[134:135], v[196:197] op_sel:[0,1] op_sel_hi:[1,1]\nC 56,128,129\n" \
"C 57,130,131\nC 58,132,133\nC 59,134,135\nbuffer_store_dwordx4 v[56:59], v0, s[60:63], s95 offen offset:0 sc1\nAR 136,56\nAR 137,57\nAR 138,58\nAR 139,59\n" \
"AR 140,60\nAR 141,61\nAR 142,62\nAR 143,63\nv_pk_mul_f32 v[136:137], v[136:137], v[196:197] op_sel:[0,1] op_sel_hi:[1,1]\nv_pk_mul_f32 v[138:139], v[138:139], v[196:197] op_sel:[0,1] op_sel_hi:[1,1]\nv_pk_mul_f32 v[140:141], v[140:141], v[196:197] op_sel:[0,1] op_sel_hi:[1,1]\nv_pk_mul_f32 v[142:143], v[142:143], v[196:197] op_sel:[0,1] op_sel_hi:[1,1]\n" \
"C 60,136,137\nC 61,138,139\nC 62,140,141\nC 63,142,143\nbuffer_store_dwordx4 v[60:63], v0, s[60:63], s95 offen offset:1024 sc1\ns_branch zE3\nzHs:\ns_add_u32 s96, s96, 2\n" \
"s_branch zH\nzL:\ns_waitcnt vmcnt(5)\ns_mov_b32 s96, s40\ns_mov_b32 s40, 0\nv_cmp_ne_u64 vcc, v[10:11], v[2:3]\ns_cbranch_vccz zO\ns_mov_b32 s94, 0x400000\n" \
"zt99:\ns_sleep 2\nbuffer_load_dwordx2 v[10:11], v1, s[60:63], 0 offen offset:64 sc1\ns_waitcnt vmcnt(0)\nv_cmp_ne_u64 vcc, v[10:11], v[2:3]\ns_cbranch_vccz zO\ns_sub_u32 s94, s94, 1\ns_cbranch_scc0 zt99\n" \
"s_branch zO\nzs101:\ns_mov_b32 s94, 0x400000\nzt101:\ns_sleep 2\nbuffer_load_dwordx2 v[10:11], v1, s[60:63], 0 offen offset:0 sc1\ns_waitcnt vmcnt(0)\nv_cmp_ne_u64 vcc, v[10:11], v[2:3]\n" \
"s_cbranch_vccz zr101\ns_sub_u32 s94, s94, 1\ns_cbranch_scc0 zt101\ns_branch zr101\nzs102:\ns_mov_b32 s94, 0x400000\nzt102:\ns_sleep 2\n" \
"buffer_load_dwordx2 v[10:11], v1, s[60:63], 0 offen offset:0 sc1\ns_waitcnt vmcnt(0)\nv_cmp_ne_u64 vcc, v[10:11], v[2:3]\ns_cbranch_vccz zr102\ns_sub_u32 s94, s94, 1\ns_cbranch_scc0 zt102\ns_branch zr102\nzs103:\n" \
"s_mov_b32 s94, 0x400000\nzt103:\ns_sleep 2\nbuffer_load_dwordx2 v[10:11], off, s[60:63], s49 sc1\ns_waitcnt vmcnt(0)\nv_cmp_ne_u64 vcc, v[10:11], v[2:3]\ns_cbranch_vccz zr103\ns_sub_u32 s94, s94, 1\n" \
"s_cbranch_scc0 zt103\ns_branch zr103\nzs104:\ns_mov_b32 s94, 0x400000\nzt104:\nbuffer_load_dwordx2 v[10:11], v1, s[60:63], 0 offen sc1\ns_waitcnt vmcnt(0)\nv_cmp_eq_u64 vcc, v[10:11], v[2:3]\n" \
"s_andn2_b32 s82, s7, vcc_hi\ns_cselect_b64 vcc, 0, vcc\ns_bitcmp1_b64 vcc, s79\ns_cbranch_scc1 zr104\ns_sleep 1\ns_sub_u32 s94, s94, 1\ns_cbranch_scc0 zt104\ns_branch zr104\n" \
"zs105:\ns_mov_b32 s94, 0x400000\nzt105:\nbuffer_load_dwordx2 v[10:11], v1, s[60:63], 0 offen sc1\ns_waitcnt vmcnt(0)\nv_cmp_eq_u64 vcc, v[10:11], v[2:3]\ns_bitcmp1_b64 vcc, s79\ns_cbranch_scc1 zr105\n" \
"s_sleep 1\ns_sub_u32 s94, s94, 1\ns_cbranch_scc0 zt105\ns_branch zr105\nzs106:\ns_mov_b32 s94, 0x400000\nzt106:\nbuffer_load_dwordx2 v[10:11], v7, s[60:63], 0 offen sc1\n" \
"s_waitcnt vmcnt(0)\nv_cmp_eq_u64 vcc, v[10:11], v[2:3]\ns_andn2_b32 s82, 10, vcc_lo\ns_cbranch_scc0 zr106\ns_sleep 1\ns_sub_u32 s94, s94, 1\ns_cbranch_scc0 zt106\ns_branch zr106\n" \
"zpl:\ns_mov_b32 s94, 0x200000\nzpm:\nbuffer_load_dwordx2 v[10:11], v7, s[60:63], 0 offen sc1\ns_waitcnt vmcnt(2)\nv_cmp_eq_u64 vcc, v[102:103], v[2:3]\ns_andn2_b32 s82, s6, vcc_lo\ns_cbranch_scc0 zpe\n" \
"buffer_load_dwordx2 v[102:103], v7, s[60:63], 0 offen sc1\ns_waitcnt vmcnt(2)\nv_cmp_eq_u64 vcc, v[120:121], v[2:3]\ns_andn2_b32 s82, s6, vcc_lo\ns_cbranch_scc0 zpe\nbuffer_load_dwordx2 v[120:121], v7, s[60:63], 0 offen sc1\ns_waitcnt vmcnt(2)\nv_cmp_eq_u64 vcc, v[10:11], v[2:3]\n" \
"s_andn2_b32 s82, s6, vcc_lo\ns_cbranch_scc0 zpe\ns_sub_u32 s94, s94, 1\ns_cbranch_scc0 zpm\ns_branch zpe\nzs107:\ns_mov_b32 s94, 0x400000\nzt107:\n" \
"buffer_load_dwordx2 v[10:11], v1, s[60:63], 0 offen sc1\ns_waitcnt vmcnt(0)\nv_cmp_eq_u64 vcc, v[10:11], v[2:3]\ns_bitcmp1_b64 vcc, s79\ns_cbranch_scc1 zr107\ns_sleep 1\ns_sub_u32 s94, s94, 1\ns_cbranch_scc0 zt107\n" \
"s_branch zr107\nzs108:\ns_mov_b32 s94, 0x400000\nzt108:\nbuffer_load_dwordx2 v[10:11], v1, s[60:63], 0 offen sc1\ns_waitcnt vmcnt(0)\nv_cmp_eq_u64 vcc, v[10:11], v[2:3]\ns_bitcmp1_b64 vcc, s79\n" \
"s_cbranch_scc1 zr108\ns_sleep 1\ns_sub_u32 s94, s94, 1\ns_cbranch_scc0 zt108\ns_branch zr108\nzs109:\ns_mov_b32 s94, 0x400000\nzt109:\n" \
"buffer_load_dwordx2 v[10:11], v1, s[60:63], 0 offen sc1\ns_waitcnt vmcnt(0)\nv_cmp_eq_u64 vcc, v[10:11], v[2:3]\ns_bitcmp1_b64 vcc, s79\ns_cbranch_scc1 zr109\ns_sleep 1\ns_sub_u32 s94, s94, 1\ns_cbranch_scc0 zt109\n" \
"s_branch zr109\nzep:\nv_lshrrev_b32 v28, 1, v0\ns_mov_b32 s94, 0x400000\nzepl:\nbuffer_load_dwordx2 v[96:97], v28, s[60:63], 0 offen offset:256 sc1\nbuffer_load_dwordx2 v[98:99], v28, s[60:63], 0 offen offset:768 sc1\nbuffer_load_dwordx2 v[100:101], v28, s[60:63], 0 offen offset:1280 sc1\n" \
"buffer_load_dwordx2 v[102:103], v28, s[60:63], 0 offen offset:1792 sc1\ns_waitcnt vmcnt(0)\nv_cmp_ne_u64 s[98:99], v[96:97], v[2:3]\nv_cmp_ne_u64 s[100:101], v[98:99], v[2:3]\nv_cmp_ne_u64 vcc, v[100:101], v[2:3]\ns_or_b64 s[98:99], s[98:99], s[100:101]\ns_or_b64 s[98:99], s[98:99], vcc\nv_cmp_ne_u64 vcc, v[102:103], v[2:3]\n" \
"s_or_b64 s[98:99], s[98:99], vcc\ns_cbranch_scc0 zepd\ns_sub_u32 s94, s94, 1\ns_cbranch_scc0 zepl\nzepd:\nbuffer_store_dwordx2 v[2:3], off, s[60:63], 0 sc1\ns_branch zeq\nzfb:\n" \
"v_cmp_lt_u32 vcc, 0x3c000000, v240\ns_cbranch_vccz zf0e\ns_add_u32 s96, s42, 2\ns_mov_b32 s77, s38\nv_mov_b32 v136, 0xff800000\nv_mov_b32 v138, 0\nv_add_u32 v140, 0x4000, v8\nv_accvgpr_write_b32 a0, 0\n" \
"v_accvgpr_write_b32 a1, 0\nv_accvgpr_write_b32 a2, 0\nv_accvgpr_write_b32 a3, 0\nv_accvgpr_write_b32 a4, 0\nv_accvgpr_write_b32 a5, 0\nv_accvgpr_write_b32 a6, 0\nv_accvgpr_write_b32 a7, 0\nv_accvgpr_write_b32 a8, 0\n" \
"v_accvgpr_write_b32 a9, 0\nv_accvgpr_write_b32 a10, 0\nv_accvgpr_write_b32 a11, 0\nv_accvgpr_write_b32 a12, 0\nv_accvgpr_write_b32 a13, 0\nv_accvgpr_write_b32 a14, 0\nv_accvgpr_write_b32 a15, 0\nv_accvgpr_write_b32 a16, 0\n" \
"v_accvgpr_write_b32 a17, 0\nv_accvgpr_write_b32 a18, 0\nv_accvgpr_write_b32 a19, 0\nv_accvgpr_write_b32 a20, 0\nv_accvgpr_write_b32 a21, 0\nv_accvgpr_write_b32 a22, 0\nv_accvgpr_write_b32 a23, 0\nv_accvgpr_write_b32 a24, 0\n" \
"v_accvgpr_write_b32 a25, 0\nv_accvgpr_write_b32 a26, 0\nv_accvgpr_write_b32 a27, 0\nv_accvgpr_write_b32 a28, 0\nv_accvgpr_write_b32 a29, 0\nv_accvgpr_write_b32 a30, 0\nv_accvgpr_write_b32 a31, 0\nzf0l:\n" \
"s_add_u32 s76, s77, 4096\nbuffer_load_dwordx4 a[160:163], v0, s[60:63], s77 offen offset:0 sc1\nbuffer_load_dwordx4 a[164:167], v0, s[60:63], s77 offen offset:1024 sc1\nbuffer_load_dwordx4 a[168:171], v0, s[60:63], s77 offen offset:2048 sc1\nbuffer_load_dwordx4 a[172:175], v0, s[60:63], s77 offen offset:3072 sc1\nbuffer_load_dwordx4 a[176:179], v0, s[60:63], s76 offen offset:0 sc1\nbuffer_load_dwordx4 a[180:183], v0, s[60:63], s76 offen offset:1024 sc1\nbuffer_load_dwordx4 a[184:187], v0, s[60:63], s76 offen offset:2048 sc1\n" \
"buffer_load_dwordx4 a[188:191], v0, s[60:63], s76 offen offset:3072 sc1\nbuffer_load_dwordx4 a[128:131], v140, s[60:63], s77 offen offset:0 sc1\nbuffer_load_dwordx4 a[132:135], v140, s[60:63], s77 offen offset:1024 sc1\nbuffer_load_dwordx4 a[136:139], v140, s[60:63], s77 offen offset:2048 sc1\nbuffer_load_dwordx4 a[140:143], v140, s[60:63], s77 offen offset:3072 sc1\nbuffer_load_dwordx4 a[144:147], v140, s[60:63], s76 offen offset:0 sc1\nbuffer_load_dwordx4 a[148:151], v140, s[60:63], s76 offen offset:1024 sc1\nbuffer_load_dwordx4 a[152:155], v140, s[60:63], s76 offen offset:2048 sc1\n" \
"buffer_load_dwordx4 a[156:159], v140, s[60:63], s76 offen offset:3072 sc1\nv_mov_b32 v144, v12\nv_mov_b32 v145, v12\nv_mov_b32 v146, v12\nv_mov_b32 v147, v12\nv_mov_b32 v148, v12\nv_mov_b32 v149, v12\nv_mov_b32 v150, v12\n" \
"v_mov_b32 v151, v12\ns_cmp_gt_u32 s96, 2\ns_cbranch_scc1 zf0m\nv_mov_b32 v144, v112\nv_mov_b32 v145, v113\nv_mov_b32 v146, v114\nv_mov_b32 v147, v115\nv_mov_b32 v148, v116\n" \
"v_mov_b32 v149, v117\nv_mov_b32 v150, v118\nv_mov_b32 v151, v119\ns_cmp_eq_u32 s96, 2\ns_cbranch_scc1 zf0m\nv_mov_b32 v144, v120\nv_mov_b32 v145, v121\nv_mov_b32 v146, v122\n" \
"v_mov_b32 v147, v123\nv_mov_b32 v148, v124\nv_mov_b32 v149, v125\nv_mov_b32 v150, v126\nv_mov_b32 v151, v127\nzf0m:\ns_waitcnt vmcnt(0)\nQvav 128,160,32,144\n" \
"Mvav 128,164,36\nMvav 128,168,40\nMvav 128,172,44\nQvav 132,176,32,148\nMvav 132,180,36\nMvav 132,184,40\nMvav 132,188,44\ns_nop 7\n" \
"v_max3_f32 v141, v128, v129, v130\nv_max3_f32 v142, v131, v132, v133\nv_max3_f32 v141, v141, v134, v135\nv_max3_f32 v141, v141, v142, v136\nv_mov_b32 v142, v141\ns_nop 1\nv_permlane16_swap_b32 v141, v142\ns_nop 0\n" \
"v_max_f32 v141, v141, v142\nv_mov_b32 v142, v141\ns_nop 1\nv_permlane32_swap_b32 v141, v142\ns_nop 0\nv_max_f32 v141, v141, v142\nv_sub_f32 v137, v136, v141\nE 137,137\n" \
"v_mov_b32 v136, v141\nv_sub_f32 v128, v128, v141\nv_sub_f32 v129, v129, v141\nv_sub_f32 v130, v130, v141\nv_sub_f32 v131, v131, v141\nv_sub_f32 v132, v132, v141\nv_sub_f32 v133, v133, v141\nv_sub_f32 v134, v134, v141\n" \
"v_sub_f32 v135, v135, v141\ns_branch 99f\n.p2align 12\nzp38:\ns_setpc_b64 s[98:99]\n99:\nE 128,128\nE 129,129\n" \
"E 130,130\nE 131,131\nE 132,132\nE 133,133\nE 134,134\nE 135,135\nv_mul_f32 v138, v138, v137\nC 192,128,129\n" \
"C 193,130,131\nC 194,132,133\nC 195,134,135\nv_add_f32 v138, v138, v128\nv_add_f32 v138, v138, v129\nv_add_f32 v138, v138, v130\nv_add_f32 v138, v138, v131\nv_add_f32 v138, v138, v132\n" \
"v_add_f32 v138, v138, v133\nv_add_f32 v138, v138, v134\nv_add_f32 v138, v138, v135\nAR 141,0\nv_mul_f32 v141, v141, v137\nAW 0,141\nAR 141,1\nv_mul_f32 v141, v141, v137\n" \
"AW 1,141\nAR 141,2\nv_mul_f32 v141, v141, v137\nAW 2,141\nAR 141,3\nv_mul_f32 v141, v141, v137\nAW 3,141\nAR 141,4\n" \
"v_mul_f32 v141, v141, v137\nAW 4,141\nAR 141,5\nv_mul_f32 v141, v141, v137\nAW 5,141\nAR 141,6\nv_mul_f32 v141, v141, v137\nAW 6,141\n" \
"AR 141,7\nv_mul_f32 v141, v141, v137\nAW 7,141\nAR 141,8\nv_mul_f32 v141, v141, v137\nAW 8,141\nAR 141,9\nv_mul_f32 v141, v141, v137\n" \
"AW 9,141\nAR 141,10\nv_mul_f32 v141, v141, v137\nAW 10,141\nAR 141,11\nv_mul_f32 v141, v141, v137\nAW 11,141\nAR 141,12\n" \
"v_mul_f32 v141, v141, v137\nAW 12,141\nAR 141,13\nv_mul_f32 v141, v141, v137\nAW 13,141\nAR 141,14\nv_mul_f32 v141, v141, v137\nAW 14,141\n" \
"AR 141,15\nv_mul_f32 v141, v141, v137\nAW 15,141\nAR 141,16\nv_mul_f32 v141, v141, v137\nAW 16,141\nAR 141,17\nv_mul_f32 v141, v141, v137\n" \
"AW 17,141\nAR 141,18\nv_mul_f32 v141, v141, v137\nAW 18,141\nAR 141,19\nv_mul_f32 v141, v141, v137\nAW 19,141\nAR 141,20\n" \
"v_mul_f32 v141, v141, v137\nAW 20,141\nAR 141,21\nv_mul_f32 v141, v141, v137\nAW 21,141\nAR 141,22\nv_mul_f32 v141, v141, v137\nAW 22,141\n" \
"AR 141,23\nv_mul_f32 v141, v141, v137\nAW 23,141\nAR 141,24\nv_mul_f32 v141, v141, v137\nAW 24,141\nAR 141,25\nv_mul_f32 v141, v141, v137\n" \
"AW 25,141\nAR 141,26\nv_mul_f32 v141, v141, v137\nAW 26,141\nAR 141,27\nv_mul_f32 v141, v141, v137\nAW 27,141\nAR 141,28\n" \
"v_mul_f32 v141, v141, v137\nAW 28,141\nAR 141,29\nv_mul_f32 v141, v141, v137\nAW 29,141\nAR 141,30\nv_mul_f32 v141, v141, v137\nAW 30,141\n" \
"AR 141,31\nv_mul_f32 v141, v141, v137\nAW 31,141\ns_nop 1\nMaav 0,128,192\nMaav 4,132,192\nMaav 8,136,192\nMaav 12,140,192\n" \
"Maav 16,144,192\nMaav 20,148,192\nMaav 24,152,192\nMaav 28,156,192\ns_add_u32 s77, s77, 8192\ns_sub_u32 s96, s96, 1\ns_cmp_lg_u32 s96, 0\ns_nop 7\n" \
"s_cbranch_scc1 zf0l\nv_mov_b32 v141, v138\ns_nop 1\nv_permlane16_swap_b32 v138, v141\ns_nop 0\nv_add_f32 v138, v138, v141\nv_mov_b32 v141, v138\ns_nop 1\n" \
"v_permlane32_swap_b32 v138, v141\ns_nop 0\nv_add_f32 v138, v138, v141\nv_mov_b32 v104, v138\nzf0e:\nv_cmp_lt_u32 vcc, 0x3c000000, v241\ns_cbranch_vccz zf1e\ns_add_u32 s96, s42, 4\n" \
"s_mov_b32 s77, s38\nv_mov_b32 v136, 0xff800000\nv_mov_b32 v138, 0\nv_add_u32 v140, 0x4000, v8\nv_accvgpr_write_b32 a32, 0\nv_accvgpr_write_b32 a33, 0\nv_accvgpr_write_b32 a34, 0\nv_accvgpr_write_b32 a35, 0\n" \
"v_accvgpr_write_b32 a36, 0\nv_accvgpr_write_b32 a37, 0\nv_accvgpr_write_b32 a38, 0\nv_accvgpr_write_b32 a39, 0\nv_accvgpr_write_b32 a40, 0\nv_accvgpr_write_b32 a41, 0\nv_accvgpr_write_b32 a42, 0\nv_accvgpr_write_b32 a43, 0\n" \
"v_accvgpr_write_b32 a44, 0\nv_accvgpr_write_b32 a45, 0\nv_accvgpr_write_b32 a46, 0\nv_accvgpr_write_b32 a47, 0\nv_accvgpr_write_b32 a48, 0\nv_accvgpr_write_b32 a49, 0\nv_accvgpr_write_b32 a50, 0\nv_accvgpr_write_b32 a51, 0\n" \
"v_accvgpr_write_b32 a52, 0\nv_accvgpr_write_b32 a53, 0\nv_accvgpr_write_b32 a54, 0\nv_accvgpr_write_b32 a55, 0\nv_accvgpr_write_b32 a56, 0\nv_accvgpr_write_b32 a57, 0\nv_accvgpr_write_b32 a58, 0\nv_accvgpr_write_b32 a59, 0\n" \
"v_accvgpr_write_b32 a60, 0\nv_accvgpr_write_b32 a61, 0\nv_accvgpr_write_b32 a62, 0\nv_accvgpr_write_b32 a63, 0\nzf1l:\ns_add_u32 s76, s77, 4096\nbuffer_load_dwordx4 a[160:163], v0, s[60:63], s77 offen offset:0 sc1\nbuffer_load_dwordx4 a[164:167], v0, s[60:63], s77 offen offset:1024 sc1\n" \
"buffer_load_dwordx4 a[168:171], v0, s[60:63], s77 offen offset:2048 sc1\nbuffer_load_dwordx4 a[172:175], v0, s[60:63], s77 offen offset:3072 sc1\nbuffer_load_dwordx4 a[176:179], v0, s[60:63], s76 offen offset:0 sc1\nbuffer_load_dwordx4 a[180:183], v0, s[60:63], s76 offen offset:1024 sc1\nbuffer_load_dwordx4 a[184:187], v0, s[60:63], s76 offen offset:2048 sc1\nbuffer_load_dwordx4 a[188:191], v0, s[60:63], s76 offen offset:3072 sc1\nbuffer_load_dwordx4 a[128:131], v140, s[60:63], s77 offen offset:0 sc1\nbuffer_load_dwordx4 a[132:135], v140, s[60:63], s77 offen offset:1024 sc1\n" \
"buffer_load_dwordx4 a[136:139], v140, s[60:63], s77 offen offset:2048 sc1\nbuffer_load_dwordx4 a[140:143], v140, s[60:63], s77 offen offset:3072 sc1\nbuffer_load_dwordx4 a[144:147], v140, s[60:63], s76 offen offset:0 sc1\nbuffer_load_dwordx4 a[148:151], v140, s[60:63], s76 offen offset:1024 sc1\nbuffer_load_dwordx4 a[152:155], v140, s[60:63], s76 offen offset:2048 sc1\nbuffer_load_dwordx4 a[156:159], v140, s[60:63], s76 offen offset:3072 sc1\nv_mov_b32 v144, v12\nv_mov_b32 v145, v12\n" \
"v_mov_b32 v146, v12\nv_mov_b32 v147, v12\nv_mov_b32 v148, v12\nv_mov_b32 v149, v12\nv_mov_b32 v150, v12\nv_mov_b32 v151, v12\ns_cmp_gt_u32 s96, 2\ns_cbranch_scc1 zf1m\n" \
"v_mov_b32 v144, v112\nv_mov_b32 v145, v113\nv_mov_b32 v146, v114\nv_mov_b32 v147, v115\nv_mov_b32 v148, v116\nv_mov_b32 v149, v117\nv_mov_b32 v150, v118\nv_mov_b32 v151, v119\n" \
"s_cmp_eq_u32 s96, 2\ns_cbranch_scc1 zf1m\nv_mov_b32 v144, v120\nv_mov_b32 v145, v121\nv_mov_b32 v146, v122\nv_mov_b32 v147, v123\nv_mov_b32 v148, v124\nv_mov_b32 v149, v125\n" \
"v_mov_b32 v150, v126\nv_mov_b32 v151, v127\nzf1m:\ns_waitcnt vmcnt(0)\nQvav 128,160,48,144\nMvav 128,164,52\nMvav 128,168,56\nMvav 128,172,60\n" \
"Qvav 132,176,48,148\nMvav 132,180,52\nMvav 132,184,56\nMvav 132,188,60\ns_nop 7\nv_max3_f32 v141, v128, v129, v130\nv_max3_f32 v142, v131, v132, v133\nv_max3_f32 v141, v141, v134, v135\n" \
"v_max3_f32 v141, v141, v142, v136\nv_mov_b32 v142, v141\ns_nop 1\nv_permlane16_swap_b32 v141, v142\ns_nop 0\nv_max_f32 v141, v141, v142\nv_mov_b32 v142, v141\ns_nop 1\n" \
"v_permlane32_swap_b32 v141, v142\ns_nop 0\nv_max_f32 v141, v141, v142\nv_sub_f32 v137, v136, v141\nE 137,137\nv_mov_b32 v136, v141\nv_sub_f32 v128, v128, v141\nv_sub_f32 v129, v129, v141\n" \
"v_sub_f32 v130, v130, v141\nv_sub_f32 v131, v131, v141\nv_sub_f32 v132, v132, v141\nv_sub_f32 v133, v133, v141\nv_sub_f32 v134, v134, v141\nv_sub_f32 v135, v135, v141\nE 128,128\nE 129,129\n" \
"E 130,130\nE 131,131\nE 132,132\nE 133,133\nE 134,134\nE 135,135\nv_mul_f32 v138, v138, v137\nC 192,128,129\n" \
"C 193,130,131\nC 194,132,133\nC 195,134,135\nv_add_f32 v138, v138, v128\nv_add_f32 v138, v138, v129\nv_add_f32 v138, v138, v130\nv_add_f32 v138, v138, v131\nv_add_f32 v138, v138, v132\n" \
"v_add_f32 v138, v138, v133\nv_add_f32 v138, v138, v134\nv_add_f32 v138, v138, v135\nAR 141,32\nv_mul_f32 v141, v141, v137\nAW 32,141\nAR 141,33\nv_mul_f32 v141, v141, v137\n" \
"AW 33,141\nAR 141,34\nv_mul_f32 v141, v141, v137\nAW 34,141\nAR 141,35\nv_mul_f32 v141, v141, v137\nAW 35,141\nAR 141,36\n" \
"v_mul_f32 v141, v141, v137\nAW 36,141\nAR 141,37\nv_mul_f32 v141, v141, v137\nAW 37,141\nAR 141,38\nv_mul_f32 v141, v141, v137\nAW 38,141\n" \
"AR 141,39\nv_mul_f32 v141, v141, v137\nAW 39,141\nAR 141,40\nv_mul_f32 v141, v141, v137\nAW 40,141\nAR 141,41\nv_mul_f32 v141, v141, v137\n" \
"AW 41,141\nAR 141,42\nv_mul_f32 v141, v141, v137\nAW 42,141\nAR 141,43\nv_mul_f32 v141, v141, v137\nAW 43,141\nAR 141,44\n" \
"v_mul_f32 v141, v141, v137\nAW 44,141\nAR 141,45\nv_mul_f32 v141, v141, v137\nAW 45,141\nAR 141,46\nv_mul_f32 v141, v141, v137\nAW 46,141\n" \
"AR 141,47\nv_mul_f32 v141, v141, v137\nAW 47,141\nAR 141,48\nv_mul_f32 v141, v141, v137\nAW 48,141\nAR 141,49\nv_mul_f32 v141, v141, v137\n" \
"AW 49,141\nAR 141,50\nv_mul_f32 v141, v141, v137\nAW 50,141\nAR 141,51\nv_mul_f32 v141, v141, v137\nAW 51,141\nAR 141,52\n" \
"v_mul_f32 v141, v141, v137\nAW 52,141\nAR 141,53\nv_mul_f32 v141, v141, v137\nAW 53,141\nAR 141,54\nv_mul_f32 v141, v141, v137\nAW 54,141\n" \
"AR 141,55\nv_mul_f32 v141, v141, v137\nAW 55,141\nAR 141,56\nv_mul_f32 v141, v141, v137\nAW 56,141\nAR 141,57\nv_mul_f32 v141, v141, v137\n" \
"AW 57,141\nAR 141,58\nv_mul_f32 v141, v141, v137\nAW 58,141\nAR 141,59\nv_mul_f32 v141, v141, v137\nAW 59,141\nAR 141,60\n" \
"v_mul_f32 v141, v141, v137\nAW 60,141\nAR 141,61\nv_mul_f32 v141, v141, v137\nAW 61,141\nAR 141,62\nv_mul_f32 v141, v141, v137\nAW 62,141\n" \
"AR 141,63\nv_mul_f32 v141, v141, v137\nAW 63,141\ns_nop 1\nMaav 32,128,192\nMaav 36,132,192\nMaav 40,136,192\nMaav 44,140,192\n" \
"Maav 48,144,192\nMaav 52,148,192\nMaav 56,152,192\nMaav 60,156,192\ns_add_u32 s77, s77, 8192\ns_sub_u32 s96, s96, 1\ns_cmp_lg_u32 s96, 0\ns_nop 7\n" \
"s_cbranch_scc1 zf1l\nv_mov_b32 v141, v138\ns_nop 1\nv_permlane16_swap_b32 v138, v141\ns_nop 0\nv_add_f32 v138, v138, v141\nv_mov_b32 v141, v138\ns_nop 1\n" \
"v_permlane32_swap_b32 v138, v141\ns_nop 0\nv_add_f32 v138, v138, v141\nv_mov_b32 v105, v138\nzf1e:\nv_cmp_lt_u32 vcc, 0x3c000000, v242\ns_cbranch_vccz zf2e\ns_add_u32 s96, s43, 2\n" \
"s_mov_b32 s77, s38\nv_mov_b32 v136, 0xff800000\nv_mov_b32 v138, 0\nv_add_u32 v140, 0x4000, v8\nv_accvgpr_write_b32 a64, 0\nv_accvgpr_write_b32 a65, 0\nv_accvgpr_write_b32 a66, 0\nv_accvgpr_write_b32 a67, 0\n" \
"v_accvgpr_write_b32 a68, 0\nv_accvgpr_write_b32 a69, 0\nv_accvgpr_write_b32 a70, 0\nv_accvgpr_write_b32 a71, 0\nv_accvgpr_write_b32 a72, 0\nv_accvgpr_write_b32 a73, 0\nv_accvgpr_write_b32 a74, 0\nv_accvgpr_write_b32 a75, 0\n" \
"v_accvgpr_write_b32 a76, 0\nv_accvgpr_write_b32 a77, 0\nv_accvgpr_write_b32 a78, 0\nv_accvgpr_write_b32 a79, 0\nv_accvgpr_write_b32 a80, 0\nv_accvgpr_write_b32 a81, 0\nv_accvgpr_write_b32 a82, 0\nv_accvgpr_write_b32 a83, 0\n" \
"v_accvgpr_write_b32 a84, 0\nv_accvgpr_write_b32 a85, 0\nv_accvgpr_write_b32 a86, 0\nv_accvgpr_write_b32 a87, 0\nv_accvgpr_write_b32 a88, 0\nv_accvgpr_write_b32 a89, 0\nv_accvgpr_write_b32 a90, 0\nv_accvgpr_write_b32 a91, 0\n" \
"v_accvgpr_write_b32 a92, 0\nv_accvgpr_write_b32 a93, 0\nv_accvgpr_write_b32 a94, 0\nv_accvgpr_write_b32 a95, 0\nzf2l:\ns_add_u32 s76, s77, 4096\nbuffer_load_dwordx4 a[160:163], v0, s[60:63], s77 offen offset:0 sc1\nbuffer_load_dwordx4 a[164:167], v0, s[60:63], s77 offen offset:1024 sc1\n" \
"buffer_load_dwordx4 a[168:171], v0, s[60:63], s77 offen offset:2048 sc1\nbuffer_load_dwordx4 a[172:175], v0, s[60:63], s77 offen offset:3072 sc1\nbuffer_load_dwordx4 a[176:179], v0, s[60:63], s76 offen offset:0 sc1\nbuffer_load_dwordx4 a[180:183], v0, s[60:63], s76 offen offset:1024 sc1\nbuffer_load_dwordx4 a[184:187], v0, s[60:63], s76 offen offset:2048 sc1\nbuffer_load_dwordx4 a[188:191], v0, s[60:63], s76 offen offset:3072 sc1\nbuffer_load_dwordx4 a[128:131], v140, s[60:63], s77 offen offset:0 sc1\nbuffer_load_dwordx4 a[132:135], v140, s[60:63], s77 offen offset:1024 sc1\n" \
"buffer_load_dwordx4 a[136:139], v140, s[60:63], s77 offen offset:2048 sc1\nbuffer_load_dwordx4 a[140:143], v140, s[60:63], s77 offen offset:3072 sc1\nbuffer_load_dwordx4 a[144:147], v140, s[60:63], s76 offen offset:0 sc1\nbuffer_load_dwordx4 a[148:151], v140, s[60:63], s76 offen offset:1024 sc1\nbuffer_load_dwordx4 a[152:155], v140, s[60:63], s76 offen offset:2048 sc1\nbuffer_load_dwordx4 a[156:159], v140, s[60:63], s76 offen offset:3072 sc1\nv_mov_b32 v144, v12\nv_mov_b32 v145, v12\n" \
"v_mov_b32 v146, v12\nv_mov_b32 v147, v12\nv_mov_b32 v148, v12\nv_mov_b32 v149, v12\nv_mov_b32 v150, v12\nv_mov_b32 v151, v12\ns_cmp_gt_u32 s96, 2\ns_cbranch_scc1 zf2m\n" \
"v_mov_b32 v144, v112\nv_mov_b32 v145, v113\nv_mov_b32 v146, v114\nv_mov_b32 v147, v115\nv_mov_b32 v148, v116\nv_mov_b32 v149, v117\nv_mov_b32 v150, v118\nv_mov_b32 v151, v119\n" \
"s_cmp_eq_u32 s96, 2\ns_cbranch_scc1 zf2m\nv_mov_b32 v144, v120\nv_mov_b32 v145, v121\nv_mov_b32 v146, v122\nv_mov_b32 v147, v123\nv_mov_b32 v148, v124\nv_mov_b32 v149, v125\n" \
"v_mov_b32 v150, v126\nv_mov_b32 v151, v127\nzf2m:\ns_waitcnt vmcnt(0)\nQvav 128,160,64,144\nMvav 128,164,68\nMvav 128,168,72\nMvav 128,172,76\n" \
"Qvav 132,176,64,148\nMvav 132,180,68\nMvav 132,184,72\nMvav 132,188,76\ns_nop 7\nv_max3_f32 v141, v128, v129, v130\nv_max3_f32 v142, v131, v132, v133\nv_max3_f32 v141, v141, v134, v135\n" \
"v_max3_f32 v141, v141, v142, v136\nv_mov_b32 v142, v141\ns_nop 1\nv_permlane16_swap_b32 v141, v142\ns_nop 0\nv_max_f32 v141, v141, v142\nv_mov_b32 v142, v141\ns_nop 1\n" \
"v_permlane32_swap_b32 v141, v142\ns_nop 0\nv_max_f32 v141, v141, v142\nv_sub_f32 v137, v136, v141\nE 137,137\nv_mov_b32 v136, v141\nv_sub_f32 v128, v128, v141\nv_sub_f32 v129, v129, v141\n" \
"v_sub_f32 v130, v130, v141\nv_sub_f32 v131, v131, v141\nv_sub_f32 v132, v132, v141\nv_sub_f32 v133, v133, v141\nv_sub_f32 v134, v134, v141\nv_sub_f32 v135, v135, v141\nE 128,128\nE 129,129\n" \
"E 130,130\nE 131,131\nE 132,132\nE 133,133\nE 134,134\nE 135,135\nv_mul_f32 v138, v138, v137\nC 192,128,129\n" \
"C 193,130,131\nC 194,132,133\nC 195,134,135\nv_add_f32 v138, v138, v128\nv_add_f32 v138, v138, v129\nv_add_f32 v138, v138, v130\nv_add_f32 v138, v138, v131\nv_add_f32 v138, v138, v132\n" \
"v_add_f32 v138, v138, v133\nv_add_f32 v138, v138, v134\nv_add_f32 v138, v138, v135\nAR 141,64\nv_mul_f32 v141, v141, v137\nAW 64,141\nAR 141,65\nv_mul_f32 v141, v141, v137\n" \
"AW 65,141\nAR 141,66\nv_mul_f32 v141, v141, v137\nAW 66,141\nAR 141,67\nv_mul_f32 v141, v141, v137\nAW 67,141\nAR 141,68\n" \
"v_mul_f32 v141, v141, v137\nAW 68,141\nAR 141,69\nv_mul_f32 v141, v141, v137\nAW 69,141\nAR 141,70\nv_mul_f32 v141, v141, v137\nAW 70,141\n" \
"AR 141,71\nv_mul_f32 v141, v141, v137\nAW 71,141\nAR 141,72\nv_mul_f32 v141, v141, v137\nAW 72,141\nAR 141,73\nv_mul_f32 v141, v141, v137\n" \
"AW 73,141\nAR 141,74\nv_mul_f32 v141, v141, v137\nAW 74,141\nAR 141,75\nv_mul_f32 v141, v141, v137\nAW 75,141\nAR 141,76\n" \
"v_mul_f32 v141, v141, v137\nAW 76,141\nAR 141,77\nv_mul_f32 v141, v141, v137\nAW 77,141\nAR 141,78\nv_mul_f32 v141, v141, v137\nAW 78,141\n" \
"AR 141,79\nv_mul_f32 v141, v141, v137\nAW 79,141\nAR 141,80\nv_mul_f32 v141, v141, v137\nAW 80,141\nAR 141,81\nv_mul_f32 v141, v141, v137\n" \
"AW 81,141\nAR 141,82\nv_mul_f32 v141, v141, v137\nAW 82,141\nAR 141,83\nv_mul_f32 v141, v141, v137\nAW 83,141\nAR 141,84\n" \
"v_mul_f32 v141, v141, v137\nAW 84,141\nAR 141,85\nv_mul_f32 v141, v141, v137\nAW 85,141\nAR 141,86\nv_mul_f32 v141, v141, v137\nAW 86,141\n" \
"AR 141,87\nv_mul_f32 v141, v141, v137\nAW 87,141\nAR 141,88\nv_mul_f32 v141, v141, v137\nAW 88,141\nAR 141,89\nv_mul_f32 v141, v141, v137\n" \
"AW 89,141\nAR 141,90\nv_mul_f32 v141, v141, v137\nAW 90,141\nAR 141,91\nv_mul_f32 v141, v141, v137\nAW 91,141\nAR 141,92\n" \
"v_mul_f32 v141, v141, v137\nAW 92,141\nAR 141,93\nv_mul_f32 v141, v141, v137\nAW 93,141\nAR 141,94\nv_mul_f32 v141, v141, v137\nAW 94,141\n" \
"AR 141,95\nv_mul_f32 v141, v141, v137\nAW 95,141\ns_nop 1\nMaav 64,128,192\nMaav 68,132,192\nMaav 72,136,192\nMaav 76,140,192\n" \
"Maav 80,144,192\nMaav 84,148,192\nMaav 88,152,192\nMaav 92,156,192\ns_add_u32 s77, s77, 8192\ns_sub_u32 s96, s96, 1\ns_cmp_lg_u32 s96, 0\ns_nop 7\n" \
"s_cbranch_scc1 zf2l\nv_mov_b32 v141, v138\ns_nop 1\nv_permlane16_swap_b32 v138, v141\ns_nop 0\nv_add_f32 v138, v138, v141\nv_mov_b32 v141, v138\ns_nop 1\n" \
"v_permlane32_swap_b32 v138, v141\ns_nop 0\nv_add_f32 v138, v138, v141\ns_branch 99f\n.p2align 12\nzp39:\ns_setpc_b64 s[98:99]\n99:\n" \
"v_mov_b32 v106, v138\nzf2e:\nv_cmp_lt_u32 vcc, 0x3c000000, v243\ns_cbranch_vccz zf3e\ns_add_u32 s96, s43, 4\ns_mov_b32 s77, s38\nv_mov_b32 v136, 0xff800000\nv_mov_b32 v138, 0\n" \
"v_add_u32 v140, 0x4000, v8\nv_accvgpr_write_b32 a96, 0\nv_accvgpr_write_b32 a97, 0\nv_accvgpr_write_b32 a98, 0\nv_accvgpr_write_b32 a99, 0\nv_accvgpr_write_b32 a100, 0\nv_accvgpr_write_b32 a101, 0\nv_accvgpr_write_b32 a102, 0\n" \
"v_accvgpr_write_b32 a103, 0\nv_accvgpr_write_b32 a104, 0\nv_accvgpr_write_b32 a105, 0\nv_accvgpr_write_b32 a106, 0\nv_accvgpr_write_b32 a107, 0\nv_accvgpr_write_b32 a108, 0\nv_accvgpr_write_b32 a109, 0\nv_accvgpr_write_b32 a110, 0\n" \
"v_accvgpr_write_b32 a111, 0\nv_accvgpr_write_b32 a112, 0\nv_accvgpr_write_b32 a113, 0\nv_accvgpr_write_b32 a114, 0\nv_accvgpr_write_b32 a115, 0\nv_accvgpr_write_b32 a116, 0\nv_accvgpr_write_b32 a117, 0\nv_accvgpr_write_b32 a118, 0\n" \
"v_accvgpr_write_b32 a119, 0\nv_accvgpr_write_b32 a120, 0\nv_accvgpr_write_b32 a121, 0\nv_accvgpr_write_b32 a122, 0\nv_accvgpr_write_b32 a123, 0\nv_accvgpr_write_b32 a124, 0\nv_accvgpr_write_b32 a125, 0\nv_accvgpr_write_b32 a126, 0\n" \
"v_accvgpr_write_b32 a127, 0\nzf3l:\ns_add_u32 s76, s77, 4096\nbuffer_load_dwordx4 a[160:163], v0, s[60:63], s77 offen offset:0 sc1\nbuffer_load_dwordx4 a[164:167], v0, s[60:63], s77 offen offset:1024 sc1\nbuffer_load_dwordx4 a[168:171], v0, s[60:63], s77 offen offset:2048 sc1\nbuffer_load_dwordx4 a[172:175], v0, s[60:63], s77 offen offset:3072 sc1\nbuffer_load_dwordx4 a[176:179], v0, s[60:63], s76 offen offset:0 sc1\n" \
"buffer_load_dwordx4 a[180:183], v0, s[60:63], s76 offen offset:1024 sc1\nbuffer_load_dwordx4 a[184:187], v0, s[60:63], s76 offen offset:2048 sc1\nbuffer_load_dwordx4 a[188:191], v0, s[60:63], s76 offen offset:3072 sc1\nbuffer_load_dwordx4 a[128:131], v140, s[60:63], s77 offen offset:0 sc1\nbuffer_load_dwordx4 a[132:135], v140, s[60:63], s77 offen offset:1024 sc1\nbuffer_load_dwordx4 a[136:139], v140, s[60:63], s77 offen offset:2048 sc1\nbuffer_load_dwordx4 a[140:143], v140, s[60:63], s77 offen offset:3072 sc1\nbuffer_load_dwordx4 a[144:147], v140, s[60:63], s76 offen offset:0 sc1\n" \
"buffer_load_dwordx4 a[148:151], v140, s[60:63], s76 offen offset:1024 sc1\nbuffer_load_dwordx4 a[152:155], v140, s[60:63], s76 offen offset:2048 sc1\nbuffer_load_dwordx4 a[156:159], v140, s[60:63], s76 offen offset:3072 sc1\nv_mov_b32 v144, v12\nv_mov_b32 v145, v12\nv_mov_b32 v146, v12\nv_mov_b32 v147, v12\nv_mov_b32 v148, v12\n" \
"v_mov_b32 v149, v12\nv_mov_b32 v150, v12\nv_mov_b32 v151, v12\ns_cmp_gt_u32 s96, 2\ns_cbranch_scc1 zf3m\nv_mov_b32 v144, v112\nv_mov_b32 v145, v113\nv_mov_b32 v146, v114\n" \
"v_mov_b32 v147, v115\nv_mov_b32 v148, v116\nv_mov_b32 v149, v117\nv_mov_b32 v150, v118\nv_mov_b32 v151, v119\ns_cmp_eq_u32 s96, 2\ns_cbranch_scc1 zf3m\nv_mov_b32 v144, v120\n" \
"v_mov_b32 v145, v121\nv_mov_b32 v146, v122\nv_mov_b32 v147, v123\nv_mov_b32 v148, v124\nv_mov_b32 v149, v125\nv_mov_b32 v150, v126\nv_mov_b32 v151, v127\nzf3m:\n" \
"s_waitcnt vmcnt(0)\nQvav 128,160,80,144\nMvav 128,164,84\nMvav 128,168,88\nMvav 128,172,92\nQvav 132,176,80,148\nMvav 132,180,84\nMvav 132,184,88\n" \
"Mvav 132,188,92\ns_nop 7\nv_max3_f32 v141, v128, v129, v130\nv_max3_f32 v142, v131, v132, v133\nv_max3_f32 v141, v141, v134, v135\nv_max3_f32 v141, v141, v142, v136\nv_mov_b32 v142, v141\ns_nop 1\n" \
"v_permlane16_swap_b32 v141, v142\ns_nop 0\nv_max_f32 v141, v141, v142\nv_mov_b32 v142, v141\ns_nop 1\nv_permlane32_swap_b32 v141, v142\ns_nop 0\nv_max_f32 v141, v141, v142\n" \
"v_sub_f32 v137, v136, v141\nE 137,137\nv_mov_b32 v136, v141\nv_sub_f32 v128, v128, v141\nv_sub_f32 v129, v129, v141\nv_sub_f32 v130, v130, v141\nv_sub_f32 v131, v131, v141\nv_sub_f32 v132, v132, v141\n" \
"v_sub_f32 v133, v133, v141\nv_sub_f32 v134, v134, v141\nv_sub_f32 v135, v135, v141\nE 128,128\nE 129,129\nE 130,130\nE 131,131\nE 132,132\n" \
"E 133,133\nE 134,134\nE 135,135\nv_mul_f32 v138, v138, v137\nC 192,128,129\nC 193,130,131\nC 194,132,133\nC 195,134,135\n" \
"v_add_f32 v138, v138, v128\nv_add_f32 v138, v138, v129\nv_add_f32 v138, v138, v130\nv_add_f32 v138, v138, v131\nv_add_f32 v138, v138, v132\nv_add_f32 v138, v138, v133\nv_add_f32 v138, v138, v134\nv_add_f32 v138, v138, v135\n" \
"AR 141,96\nv_mul_f32 v141, v141, v137\nAW 96,141\nAR 141,97\nv_mul_f32 v141, v141, v137\nAW 97,141\nAR 141,98\nv_mul_f32 v141, v141, v137\n" \
"AW 98,141\nAR 141,99\nv_mul_f32 v141, v141, v137\nAW 99,141\nAR 141,100\nv_mul_f32 v141, v141, v137\nAW 100,141\nAR 141,101\n" \
"v_mul_f32 v141, v141, v137\nAW 101,141\nAR 141,102\nv_mul_f32 v141, v141, v137\nAW 102,141\nAR 141,103\nv_mul_f32 v141, v141, v137\nAW 103,141\n" \
"AR 141,104\nv_mul_f32 v141, v141, v137\nAW 104,141\nAR 141,105\nv_mul_f32 v141, v141, v137\nAW 105,141\nAR 141,106\nv_mul_f32 v141, v141, v137\n" \
"AW 106,141\nAR 141,107\nv_mul_f32 v141, v141, v137\nAW 107,141\nAR 141,108\nv_mul_f32 v141, v141, v137\nAW 108,141\nAR 141,109\n" \
"v_mul_f32 v141, v141, v137\nAW 109,141\nAR 141,110\nv_mul_f32 v141, v141, v137\nAW 110,141\nAR 141,111\nv_mul_f32 v141, v141, v137\nAW 111,141\n" \
"AR 141,112\nv_mul_f32 v141, v141, v137\nAW 112,141\nAR 141,113\nv_mul_f32 v141, v141, v137\nAW 113,141\nAR 141,114\nv_mul_f32 v141, v141, v137\n" \
"AW 114,141\nAR 141,115\nv_mul_f32 v141, v141, v137\nAW 115,141\nAR 141,116\nv_mul_f32 v141, v141, v137\nAW 116,141\nAR 141,117\n" \
"v_mul_f32 v141, v141, v137\nAW 117,141\nAR 141,118\nv_mul_f32 v141, v141, v137\nAW 118,141\nAR 141,119\nv_mul_f32 v141, v141, v137\nAW 119,141\n" \
"AR 141,120\nv_mul_f32 v141, v141, v137\nAW 120,141\nAR 141,121\nv_mul_f32 v141, v141, v137\nAW 121,141\nAR 141,122\nv_mul_f32 v141, v141, v137\n" \
"AW 122,141\nAR 141,123\nv_mul_f32 v141, v141, v137\nAW 123,141\nAR 141,124\nv_mul_f32 v141, v141, v137\nAW 124,141\nAR 141,125\n" \
"v_mul_f32 v141, v141, v137\nAW 125,141\nAR 141,126\nv_mul_f32 v141, v141, v137\nAW 126,141\nAR 141,127\nv_mul_f32 v141, v141, v137\nAW 127,141\n" \
"s_nop 1\nMaav 96,128,192\nMaav 100,132,192\nMaav 104,136,192\nMaav 108,140,192\nMaav 112,144,192\nMaav 116,148,192\nMaav 120,152,192\n" \
"Maav 124,156,192\ns_add_u32 s77, s77, 8192\ns_sub_u32 s96, s96, 1\ns_cmp_lg_u32 s96, 0\ns_nop 7\ns_cbranch_scc1 zf3l\nv_mov_b32 v141, v138\ns_nop 1\n" \
"v_permlane16_swap_b32 v138, v141\ns_nop 0\nv_add_f32 v138, v138, v141\nv_mov_b32 v141, v138\ns_nop 1\nv_permlane32_swap_b32 v138, v141\ns_nop 0\nv_add_f32 v138, v138, v141\n" \
"v_mov_b32 v107, v138\nzf3e:\nv_rcp_f32 v196, v104\nv_rcp_f32 v197, v105\ns_nop 0\ns_add_u32 s95, s83, 0\nAR 128,0\nAR 129,1\n" \
"AR 130,2\nAR 131,3\nAR 132,4\nAR 133,5\nAR 134,6\nAR 135,7\nv_pk_mul_f32 v[128:129], v[128:129], v[196:197] op_sel:[0,0] op_sel_hi:[1,0]\nv_pk_mul_f32 v[130:131], v[130:131], v[196:197] op_sel:[0,0] op_sel_hi:[1,0]\n" \
"v_pk_mul_f32 v[132:133], v[132:133], v[196:197] op_sel:[0,0] op_sel_hi:[1,0]\nv_pk_mul_f32 v[134:135], v[134:135], v[196:197] op_sel:[0,0] op_sel_hi:[1,0]\nC 32,128,129\nC 33,130,131\nC 34,132,133\nC 35,134,135\nbuffer_store_dwordx4 v[32:35], v0, s[60:63], s95 offen offset:0 sc1\nAR 136,8\n" \
"AR 137,9\nAR 138,10\nAR 139,11\nAR 140,12\nAR 141,13\nAR 142,14\nAR 143,15\nv_pk_mul_f32 v[136:137], v[136:137], v[196:197] op_sel:[0,0] op_sel_hi:[1,0]\n" \
"v_pk_mul_f32 v[138:139], v[138:139], v[196:197] op_sel:[0,0] op_sel_hi:[1,0]\nv_pk_mul_f32 v[140:141], v[140:141], v[196:197] op_sel:[0,0] op_sel_hi:[1,0]\nv_pk_mul_f32 v[142:143], v[142:143], v[196:197] op_sel:[0,0] op_sel_hi:[1,0]\nC 36,136,137\nC 37,138,139\nC 38,140,141\nC 39,142,143\nbuffer_store_dwordx4 v[36:39], v0, s[60:63], s95 offen offset:1024 sc1\n" \
"s_add_u32 s95, s83, 32768\nAR 128,16\nAR 129,17\nAR 130,18\nAR 131,19\nAR 132,20\nAR 133,21\nAR 134,22\n" \
"AR 135,23\nv_pk_mul_f32 v[128:129], v[128:129], v[196:197] op_sel:[0,0] op_sel_hi:[1,0]\nv_pk_mul_f32 v[130:131], v[130:131], v[196:197] op_sel:[0,0] op_sel_hi:[1,0]\nv_pk_mul_f32 v[132:133], v[132:133], v[196:197] op_sel:[0,0] op_sel_hi:[1,0]\nv_pk_mul_f32 v[134:135], v[134:135], v[196:197] op_sel:[0,0] op_sel_hi:[1,0]\nC 40,128,129\nC 41,130,131\nC 42,132,133\n" \
"C 43,134,135\nbuffer_store_dwordx4 v[40:43], v0, s[60:63], s95 offen offset:0 sc1\nAR 136,24\nAR 137,25\nAR 138,26\nAR 139,27\nAR 140,28\nAR 141,29\n" \
"AR 142,30\nAR 143,31\nv_pk_mul_f32 v[136:137], v[136:137], v[196:197] op_sel:[0,0] op_sel_hi:[1,0]\nv_pk_mul_f32 v[138:139], v[138:139], v[196:197] op_sel:[0,0] op_sel_hi:[1,0]\nv_pk_mul_f32 v[140:141], v[140:141], v[196:197] op_sel:[0,0] op_sel_hi:[1,0]\nv_pk_mul_f32 v[142:143], v[142:143], v[196:197] op_sel:[0,0] op_sel_hi:[1,0]\nC 44,136,137\nC 45,138,139\n" \
"C 46,140,141\nC 47,142,143\nbuffer_store_dwordx4 v[44:47], v0, s[60:63], s95 offen offset:1024 sc1\ns_add_u32 s95, s83, 8192\nAR 128,32\nAR 129,33\nAR 130,34\nAR 131,35\n" \
"AR 132,36\nAR 133,37\nAR 134,38\nAR 135,39\nv_pk_mul_f32 v[128:129], v[128:129], v[196:197] op_sel:[0,1] op_sel_hi:[1,1]\nv_pk_mul_f32 v[130:131], v[130:131], v[196:197] op_sel:[0,1] op_sel_hi:[1,1]\nv_pk_mul_f32 v[132:133], v[132:133], v[196:197] op_sel:[0,1] op_sel_hi:[1,1]\nv_pk_mul_f32 v[134:135], v[134:135], v[196:197] op_sel:[0,1] op_sel_hi:[1,1]\n" \
"C 48,128,129\nC 49,130,131\nC 50,132,133\nC 51,134,135\nbuffer_store_dwordx4 v[48:51], v0, s[60:63], s95 offen offset:0 sc1\nAR 136,40\nAR 137,41\nAR 138,42\n" \
"AR 139,43\nAR 140,44\nAR 141,45\nAR 142,46\nAR 143,47\nv_pk_mul_f32 v[136:137], v[136:137], v[196:197] op_sel:[0,1] op_sel_hi:[1,1]\nv_pk_mul_f32 v[138:139], v[138:139], v[196:197] op_sel:[0,1] op_sel_hi:[1,1]\nv_pk_mul_f32 v[140:141], v[140:141], v[196:197] op_sel:[0,1] op_sel_hi:[1,1]\n" \
"v_pk_mul_f32 v[142:143], v[142:143], v[196:197] op_sel:[0,1] op_sel_hi:[1,1]\nC 52,136,137\nC 53,138,139\nC 54,140,141\nC 55,142,143\nbuffer_store_dwordx4 v[52:55], v0, s[60:63], s95 offen offset:1024 sc1\ns_add_u32 s95, s83, 40960\nAR 128,48\n" \
"AR 129,49\nAR 130,50\nAR 131,51\nAR 132,52\nAR 133,53\nAR 134,54\nAR 135,55\nv_pk_mul_f32 v[128:129], v[128:129], v[196:197] op_sel:[0,1] op_sel_hi:[1,1]\n" \
"v_pk_mul_f32 v[130:131], v[130:131], v[196:197] op_sel:[0,1] op_sel_hi:[1,1]\nv_pk_mul_f32 v[132:133], v[132:133], v[196:197] op_sel:[0,1] op_sel_hi:[1,1]\nv_pk_mul_f32 v[134:135], v[134:135], v[196:197] op_sel:[0,1] op_sel_hi:[1,1]\nC 56,128,129\nC 57,130,131\nC 58,132,133\nC 59,134,135\nbuffer_store_dwordx4 v[56:59], v0, s[60:63], s95 offen offset:0 sc1\n" \
"AR 136,56\nAR 137,57\nAR 138,58\nAR 139,59\nAR 140,60\nAR 141,61\nAR 142,62\nAR 143,63\n" \
"v_pk_mul_f32 v[136:137], v[136:137], v[196:197] op_sel:[0,1] op_sel_hi:[1,1]\nv_pk_mul_f32 v[138:139], v[138:139], v[196:197] op_sel:[0,1] op_sel_hi:[1,1]\nv_pk_mul_f32 v[140:141], v[140:141], v[196:197] op_sel:[0,1] op_sel_hi:[1,1]\nv_pk_mul_f32 v[142:143], v[142:143], v[196:197] op_sel:[0,1] op_sel_hi:[1,1]\nC 60,136,137\nC 61,138,139\nC 62,140,141\nC 63,142,143\n" \
"buffer_store_dwordx4 v[60:63], v0, s[60:63], s95 offen offset:1024 sc1\ns_branch zend\nzlast:\n.subsection 0\n.purgem D\n.purgem Ta\n.purgem Ra\n.purgem Zvaa\n" \
".purgem AR\n.purgem Zaaa\n.purgem Mvaa\n.purgem Maaa\n.purgem C\n.purgem AW\n.purgem DS\n.purgem Qvav\n" \
".purgem Mvav\n.purgem E\n.purgem Zaav\n.purgem Maav\n.purgem Mvvv\n.purgem Zvav\n.purgem Rv\n.purgem Mvva\n" \
".purgem Qvaa\n.purgem Zvvv\n.purgem Qvva\n" \
""
#define Z_STAMP 0
#define Z_NY 6
#define Z_NP 2
#define Z_CLOB "v16", "v17", "v18", "v19", "v20", "v21", "v22", "v23", "v24", "v25", "v26", "v27", "v28", "v29", "v30", "v31", "v32", "v33", "v34", "v35", "v36", "v37", "v38", "v39", "v40", "v41", "v42", "v43", "v44", "v45", "v46", "v47", "v48", "v49", "v50", "v51", "v52", "v53", "v54", "v55", "v56", "v57", "v58", "v59", "v60", "v61", "v62", "v63", "v64", "v65", "v66", "v67", "v68", "v69", "v70", "v71", "v72", "v73", "v74", "v75", "v76", "v77", "v78", "v79", "v80", "v81", "v82", "v83", "v84", "v85", "v86", "v87", "v88", "v89", "v90", "v91", "v92", "v93", "v94", "v95", "v96", "v97", "v98", "v99", "v100", "v101", "v102", "v103", "v104", "v105", "v106", "v107", "v108", "v109", "v110", "v111", "v112", "v113", "v114", "v115", "v116", "v117", "v118", "v119", "v120", "v121", "v122", "v123", "v124", "v125", "v126", "v127", "v128", "v129", "v130", "v131", "v132", "v133", "v134", "v135", "v136", "v137", "v138", "v139", "v140", "v141", "v142", "v143", "v144", "v145", "v146", "v147", "v148", "v149", "v150", "v151", "v152", "v153", "v154", "v155", "v156", "v157", "v158", "v159", "v160", "v161", "v162", "v163", "v164", "v165", "v166", "v167", "v168", "v169", "v170", "v171", "v172", "v173", "v174", "v175", "v176", "v177", "v178", "v179", "v180", "v181", "v182", "v183", "v184", "v185", "v186", "v187", "v188", "v189", "v190", "v191", "v192", "v193", "v194", "v195", "v196", "v197", "v198", "v199", "v200", "v201", "v202", "v203", "v204", "v205", "v206", "v207", "v208", "v209", "v210", "v211", "v212", "v213", "v214", "v215", "v216", "v217", "v218", "v219", "v220", "v221", "v222", "v223", "v224", "v225", "v226", "v227", "v228", "v229", "v230", "v231", "v232", "v233", "v234", "v235", "v236", "v237", "v238", "v239", "v240", "v241", "v242", "v243", "v244", "v245", "v246", "v247", "v248", "v249", "v250", "v251", "v252", "v253", "v254", "v255", "a0", "a1", "a2", "a3", "a4", "a5", "a6", "a7", "a8", "a9", "a10", "a11", "a12", "a13", "a14", "a15", "a16", "a17", "a18", "a19", "a20", "a21", "a22", "a23", "a24", "a25", "a26", "a27", "a28", "a29", "a30", "a31", "a32", "a33", "a34", "a35", "a36", "a37", "a38", "a39", "a40", "a41", "a42", "a43", "a44", "a45", "a46", "a47", "a48", "a49", "a50", "a51", "a52", "a53", "a54", "a55", "a56", "a57", "a58", "a59", "a60", "a61", "a62", "a63", "a64", "a65", "a66", "a67", "a68", "a69", "a70", "a71", "a72", "a73", "a74", "a75", "a76", "a77", "a78", "a79", "a80", "a81", "a82", "a83", "a84", "a85", "a86", "a87", "a88", "a89", "a90", "a91", "a92", "a93", "a94", "a95", "a96", "a97", "a98", "a99", "a100", "a101", "a102", "a103", "a104", "a105", "a106", "a107", "a108", "a109", "a110", "a111", "a112", "a113", "a114", "a115", "a116", "a117", "a118", "a119", "a120", "a121", "a122", "a123", "a124", "a125", "a126", "a127", "a128", "a129", "a130", "a131", "a132", "a133", "a134", "a135", "a136", "a137", "a138", "a139", "a140", "a141", "a142", "a143", "a144", "a145", "a146", "a147", "a148", "a149", "a150", "a151", "a152", "a153", "a154", "a155", "a156", "a157", "a158", "a159", "a160", "a161", "a162", "a163", "a164", "a165", "a166", "a167", "a168", "a169", "a170", "a171", "a172", "a173", "a174", "a175", "a176", "a177", "a178", "a179", "a180", "a181", "a182", "a183", "a184", "a185", "a186", "a187", "a188", "a189", "a190", "a191", "a192", "a193", "a194", "a195", "a196", "a197", "a198", "a199", "a200", "a201", "a202", "a203", "a204", "a205", "a206", "a207", "a208", "a209", "a210", "a211", "a212", "a213", "a214", "a215", "a216", "a217", "a218", "a219", "a220", "a221", "a222", "a223", "a224", "a225", "a226", "a227", "a228", "a229", "a230", "a231", "a232", "a233", "a234", "a235", "a236", "a237", "a238", "a239", "a240", "a241", "a242", "a243", "a244", "a245", "a246", "a247", "a248", "a249", "a250", "a251", "a252", "a253", "a254", "a255", "s2", "s4", "s5", "s6", "s7", "s8", "s9", "s10", "s11", "s12", "s13", "s14", "s15", "s16", "s17", "s18", "s19", "s20", "s21", "s22", "s23", "s24", "s25", "s26", "s27", "s28", "s29", "s30", "s31", "s72", "s73", "s74", "s75", "s76", "s77", "s78", "s79", "s88", "s89", "s90", "s91", "s92", "s93", "s94", "s95", "s96", "s97", "s98", "s99", "s100", "s101", "m0", "scc", "vcc", "memory"
#define Z_WS_CHECK static_assert(WS_OA == 50660352 && WS_CTX == 54854656 && WS_PART == 71631872 && FLG_LA == 8192 && FLG_C == 16384 && FLG_S == 32768 && WS_V - WS_K == 16810496, "layout")
#define Z_CLOB1 "v28", "a251", "a252", "a253", "s72", "s73", "s74", "s75", "s76", "s77", "s78", "s79", "s88", "s89", "s90", "s91", "s92", "s93", "s94", "s95", "m0", "scc", "memory"
typedef unsigned long long u64;
#define HPAD 1024
constexpr unsigned HS = 2048 * 256 + HPAD;
typedef float f32x32 __attribute__((ext_vector_type(32)));
typedef unsigned long long u64x4 __attribute__((ext_vector_type(4)));
typedef unsigned u32x16 __attribute__((ext_vector_type(16)));
typedef unsigned long long u64x2 __attribute__((ext_vector_type(2)));
#define XPAGE(name) asm volatile("s_branch 99f\n\t.p2align 12\n" name ":\n\ts_setpc_b64 s[68:69]\n99:")
#define TOUCH_CLOB "s64", "s65", "s66", "s67", "s68", "s69", "scc"
__device__ __forceinline__ unsigned lane_now() { unsigned l; asm volatile("v_mbcnt_lo_u32_b32 %0, -1, 0\n\tv_mbcnt_hi_u32_b32 %0, -1, %0" : "=v"(l)); return l; }
struct G1Args { u64 ckv, wuk, wuv, ws, Ax, Awqkv, Bx, wq, Awo, Bwo, out; };
constexpr unsigned PART_BYTES = 65536 + 8192 + 128;
constexpr unsigned WS_QKVA = 262144, WS_Q = WS_QKVA + 2048 * 6144, WS_K = WS_Q + 64 * 65536, WS_V = WS_K + 32 * HS + HPAD / 2, WS_OA = WS_V + 32 * HS + HPAD / 2,
                   WS_CTX = WS_OA + 2048 * 2048, WS_PART = WS_CTX + 2048 * 8192, WS_APART = WS_PART + 64 * 4 * 65536, WS_END = WS_APART + 64 * PART_BYTES, NOWHERE = 0x7fff0000u;
constexpr unsigned FLG_KV = 256, FLG_H = 4096, FLG_LA = 8192, FLG_C = 16384, FLG_S = 32768, WS_HALO = WS_QKVA;
Z_WS_CHECK;
typedef unsigned u32x4g __attribute__((ext_vector_type(4)));
#define STAMP(i)
#define RSTAMP(i)
#define STAMP_DECL
#define OARG
#define OPASS
#define STAMP_OUT()
namespace att {
typedef float    f32x4  __attribute__((ext_vector_type(4)));
typedef float    f32x16 __attribute__((ext_vector_type(16)));
typedef unsigned u32x4  __attribute__((ext_vector_type(4)));
#define VRING 0x8000u
#define QSTAGE 0x10000u
#define SRD_WORD3 0x00020000
#define DI __device__ __forceinline__
#define GAP() __builtin_amdgcn_sched_barrier(0)
DI unsigned rfl(unsigned x) { return __builtin_amdgcn_readfirstlane(x); }
DI unsigned lane_id() { return lane_now(); }
__global__ __launch_bounds__(256, 1)
void attn_kernel(G1Args kernel_args) {
  __shared__ __attribute__((aligned(1024))) char lds[163840];
  STAMP_DECL; STAMP(0); RSTAMP(14);
  u64x4 a4, b4;
  u64x2 c2;
  asm volatile("s_load_dwordx8 s[88:95], %3, 0x0\n\ts_getpc_b64 s[64:65]\n1:\n\ts_mov_b32 s66, zlast+256-1b\n\ts_mov_b32 s67, 0x20000\n\tbuffer_load_dword a254, %4, s[64:67], 0 offen\n\t"
               "s_load_dwordx8 s[80:87], %3, 0x20\n\ts_load_dwordx4 s[96:99], %3, 0x40"
               : "=&{s[88:95]}"(a4), "=&{s[80:87]}"(b4), "=&{s[96:99]}"(c2)
               : "s"(__builtin_amdgcn_kernarg_segment_ptr()), "v"(((blockIdx.x >> 3) * 64u + (threadIdx.x >> 6) * 16u + (threadIdx.x & 15u)) * 128u + ((threadIdx.x & 48u) << 24))
               : "s64", "s65", "s66", "s67", "a254");
  __amdgpu_buffer_rsrc_t srdG;
#define ROLES const unsigned xcd = b & 7u, jj = b >> 3, wk = 4096u * wid, head = 16u * (xcd >> 2) + 8u * (xcd & 1u) + 2u * (xcd & 2u) + (jj & 3u), slot = b >> 5
  {
  const unsigned wid = rfl(threadIdx.x >> 6), b = blockIdx.x;
  ROLES;
  const unsigned rt1 = 2u * (xcd & 1u) + (jj & 1u) + (jj < 16u ? 0u : 4u), ct1 = 4u * (xcd >> 1) + ((jj >> 1) & 3u), isv = (jj >> 3) & 1u;
  unsigned ln = lane_id();
  const unsigned l4 = ln >> 4, l15 = ln & 15u, swA = ((ln & 7u) ^ (ln >> 3)) << 4, cw = l15 ^ (l4 << 1);
  unsigned vA = (rt1 * 256u + 64u * wid + (ln >> 3)) * 1024u + swA, vW0 = (16u * wid + l4) * 8192u + ct1 * 512u + (cw << 4);
  asm volatile("s_waitcnt lgkmcnt(0)" : "+{s[88:95]}"(a4), "+{s[80:87]}"(b4), "+{s[96:99]}"(c2) : "v"(vA), "v"(vW0));
  srdG = __builtin_amdgcn_make_buffer_rsrc((void*)a4[3], (short)0, WS_END, SRD_WORD3);
  u64x4 dsc = {a4[0], (0x20000ull << 32) | (2048u * 1024u), isv ? a4[2] : a4[1], (0x20000ull << 32) | (512u * 8192u)};
  asm volatile("" :: "s"((__attribute__((address_space(3))) char*)lds));
  STAMP(22);
  asm volatile(Z_PRO1 : "+{v5}"(vA), "+{v6}"(vW0) : "{v29}"(((b * 4u + wid) * 16u + l15) * 128u + ((ln & 48u) << 24)), "{s32}"(wk), "{s[60:63]}"(srdG), "{s[64:71]}"(dsc) : Z_CLOB1);
  STAMP(23);
  const bool isX = slot < 4u;
  const unsigned lo = isX ? slot : slot + 4u, hi = (isX ? 7u : 23u) - lo;
  {
    const unsigned wr = wid >> 1, wc = wid & 1u, rta = 4u * (xcd & 1u) + (jj & 3u), cta = 4u * (xcd >> 1) + (slot & 3u), ta = rta * 16u + cta;
    const unsigned kvsig = FLG_KV + ((ct1 * 8u + rt1) * 2u + isv) * 8u;
    const u64 pAx = b4[0], pAw = b4[1], pBx = b4[2], pWq = b4[3];
    const unsigned gw = (b & 127u) * 4u + wid;
#define PF1 400u, 24u, 40u, 256u, 22u, 22u
    constexpr unsigned pf1[6] = {PF1};
    const unsigned nw1 = isX ? pf1[3] : pf1[0];
    const bool pw = gw < nw1;
    const unsigned pl = isX ? (pw ? pf1[4] : pf1[5]) : (pw ? pf1[1] : pf1[2]);
    const __amdgpu_buffer_rsrc_t srdP = __builtin_amdgcn_make_buffer_rsrc((void*)(isX ? (pw ? pAw : pAx) : (pw ? pWq : pBx + 1024u * 2048u)), (short)0,
                                                                          isX ? (pw ? 6291456u : 4194304u) : (pw ? 8388608u : 2097152u), SRD_WORD3);
    const unsigned vP = ((pw ? gw : gw - nw1) * pl + ln) * 128u + (ln < pl ? 0u : 0x40000000u), sP = (pw ? nw1 : 512u - nw1) * pl * 128u;
#define PF2 410u, 14u, 14u
    constexpr unsigned pf2[3] = {PF2};
    const bool k2 = gw < pf2[0];
    const unsigned pl2 = k2 ? pf2[1] : pf2[2], g2 = k2 ? gw : gw - pf2[0], n2 = k2 ? pf2[0] : 512u - pf2[0];
    const __amdgpu_buffer_rsrc_t srdP2 = __builtin_amdgcn_make_buffer_rsrc((void*)(k2 ? c2[1] : c2[0]), (short)0, isX ? 0u : k2 ? 8388608u : 2097152u, SRD_WORD3);
    const bool top = rta == 0u && wr == 0u;
    u32x16 sp = {wk, isv | (isX ? 2u : 0u) | (lo == 0u ? 4u : 0u) | (hi == lo + 1u ? 8u : 0u) | (b == 0u && wid == 3u ? 16u : 0u),
                 (isv ? WS_V : WS_K) + (2u * ct1 + wc) * HS + (rt1 * 2u + wr) * 32768u, kvsig, isX ? FLG_LA + ta * 8u : kvsig, (hi - lo) * 128u * 2048u, WS_K + head * HS,
                 isX ? 2u * lo - 1u : 12u - Z_NY / 2u, isX ? 0u : 2u * lo - 13u, 2u * (hi - lo) - 3u - Z_NP, 4u * lo, 4u * hi, sP, n2 * pl2 * 128u,
                 isX ? WS_HALO + (ta * 4u + wid) * 2048u : gw * 4096u, top ? NOWHERE : WS_HALO + ((wr ? ta : ta - 16u) * 4u + (wid ^ 2u)) * 2048u};
    u32x4 sq = {FLG_H + ta * 8u, FLG_H + (wr || top ? ta : ta - 16u) * 8u, WS_OA + rta * 524288u + cta * 32768u + wr * 16384u + wc * 1024u, top ? 1u : 0u};
    #pragma unroll
    for (int i = 0; i < 16; ++i) sp[i] = rfl(sp[i]);
    #pragma unroll
    for (int i = 0; i < 4; ++i) sq[i] = rfl(sq[i]);
    const unsigned q4 = l15 >> 2, p4 = ln & 3u, f4 = q4 | ((l4 & 1u) << 2), cv = cw ^ (wc << 3);
    u32x16 vp = {16u * ln, FLG_KV + (head >> 1) * 128u + 8u * (ln & 7u), 0u, 0u, vP, vA, vW0,
                 (wr * 128u + l15) * 128u + ((l4 ^ (ln & 7u)) << 4), 65536u + wc * 16384u + (8u * l4 + q4) * 256u + (((p4 >> 1) ^ (f4 << 1)) << 4) + (p4 & 1u) * 8u,
                 (rta * 256u + 64u * wid + (ln >> 3)) * 2048u + swA,
                 (16u * wid + l4) * 6144u + (cw >> 3) * 2048u + cta * 128u + (cw & 7u) * 16u, (16u * wid + l4) * 6144u + ((cw >> 3) ^ 1u) * 2048u + cta * 128u + (cw & 7u) * 16u,
                 (8u * wid + l4 + 32u * (cv >> 3)) * 6144u + 4096u + cta * 128u + (cv & 7u) * 16u,
                 (lo * 128u + 16u * wid + (ln >> 3)) * 2048u + swA, (16u * wid + l4) * 8192u + head * 256u + (cw << 4),
                 (g2 * pl2 + ln) * 128u + (ln < pl2 ? 0u : 0x40000000u)};
#define Z_STV
    asm volatile(Z_MAIN : "+{v[0:15]}"(vp), "+{s[32:47]}"(sp), "+{s[64:71]}"(dsc), "+{s[80:87]}"(b4) Z_STV
                 : "{s3}"(b), "{s[0:1]}"(__builtin_amdgcn_kernarg_segment_ptr()), "{s[48:51]}"(sq), "{s[52:55]}"(srdP), "{s[56:59]}"(srdP2), "{s[60:63]}"(srdG) : Z_CLOB);
  }
  }
  STAMP(13); RSTAMP(15); STAMP_OUT();
}
}

#define WSOFF 0
extern "C" void kernel_launch(void* const* d_in, const int* in_sizes, int n_in, void* d_out, int out_size,
                              void* d_ws, size_t ws_size, hipStream_t stream) {
  const bf16_t* A_x = (const bf16_t*)d_in[0];
  const bf16_t* A_wqkv = (const bf16_t*)d_in[1];
  const bf16_t* A_wo = (const bf16_t*)d_in[2];
  const bf16_t* B_x = (const bf16_t*)d_in[3];
  const bf16_t* B_ckv = (const bf16_t*)d_in[4];
  const bf16_t* B_wq = (const bf16_t*)d_in[5];
  const bf16_t* B_wuk = (const bf16_t*)d_in[6];
  const bf16_t* B_wuv = (const bf16_t*)d_in[7];
  const bf16_t* B_wo = (const bf16_t*)d_in[8];
    G1Args g1 = {(u64)B_ckv, (u64)B_wuk, (u64)B_wuv, (u64)d_ws + WSOFF, (u64)A_x, (u64)A_wqkv, (u64)B_x, (u64)B_wq, (u64)A_wo, (u64)B_wo, (u64)d_out};
  att::attn_kernel<<<256, 256, 0, stream>>>(g1);
}
```

```cpp
#include <hip/hip_runtime.h>
#include <hip/hip_bf16.h>
#include <stdint.h>
#include <utility>

typedef unsigned short bf16_t;
typedef short bf16x4 __attribute__((ext_vector_type(4)));
typedef float f32x4 __attribute__((ext_vector_type(4)));
typedef unsigned int u32x2 __attribute__((ext_vector_type(2)));

__device__ __forceinline__ unsigned pack_bf16(float a, float b) {
  typedef __bf16 bf2 __attribute__((ext_vector_type(2)));
  typedef float f2 __attribute__((ext_vector_type(2)));
  bf2 r = __builtin_convertvector((f2){a, b}, bf2);
  return __builtin_bit_cast(unsigned, r);
}
#define Z_PRO1 \
"s_lshl_b32 s88, s32, 1\nv_xor_b32 v28, 128, v6\ns_mov_b32 s73, 7168\ns_mov_b32 s74, 14336\ns_mov_b32 s75, 21504\ns_mov_b32 s76, 32768\ns_mov_b32 s77, 39936\ns_mov_b32 s78, 47104\n" \
"s_mov_b32 s79, 54272\ns_mov_b32 s89, 31744\ns_mov_b32 s90, 63488\ns_mov_b32 s91, 95232\ns_mov_b32 s92, 256\ns_mov_b32 s93, 32000\ns_mov_b32 s94, 63744\ns_mov_b32 s95, 95488\n" \
"s_mov_b32 s72, 524288\ns_waitcnt lgkmcnt(0)\ns_add_u32 m0, s88, 0\ns_load_dwordx4 s[12:15], s[0:1], 0x60\nbuffer_load_dwordx4 v5, s[64:67], 0 offen offset:0 lds\ns_load_dwordx2 s[16:17], s[0:1], 0x70\nv_mov_b32 v16, v6\nv_mov_b32 v17, v5\n" \
"buffer_load_dwordx4 v5, s[64:67], s73 offen offset:1024 lds\ns_cmp_lt_u32 s3, 128\ns_cselect_b32 s26, 2, 0\ns_cmp_lt_u32 s3, 32\ns_cselect_b32 s27, 4, 0\ns_or_b32 s33, s33, s26\nbuffer_load_dwordx4 v5, s[64:67], s74 offen offset:2048 lds\ns_and_b32 s26, s3, 96\n" \
"s_or_b32 s33, s33, s27\ns_cmp_eq_u32 s26, 96\ns_cselect_b32 s26, 8, 0\nbuffer_load_dwordx4 v5, s[64:67], s75 offen offset:3072 lds\ns_xor_b32 s27, s32, 0x3000\ns_or_b32 s33, s33, s26\ns_or_b32 s27, s27, s3\ns_cselect_b32 s27, 0, 16\n" \
"s_add_u32 m0, s88, 4096\ns_or_b32 s33, s33, s27\nbuffer_load_dwordx4 v5, s[64:67], s76 offen offset:0 lds\nv_xor_b32 v20, 32, v8\nv_xor_b32 v21, 64, v8\nv_xor_b32 v22, 96, v8\nbuffer_load_dwordx4 v5, s[64:67], s77 offen offset:1024 lds\nv_xor_b32 v23, 128, v8\n" \
"v_xor_b32 v24, 160, v8\nv_xor_b32 v25, 192, v8\nbuffer_load_dwordx4 v5, s[64:67], s78 offen offset:2048 lds\nv_xor_b32 v26, 224, v8\nv_xor_b32 v27, 64, v7\ns_and_b32 s26, s3, 127\nbuffer_load_dwordx4 v5, s[64:67], s79 offen offset:3072 lds\ns_lshr_b32 s27, s32, 12\n" \
"s_movk_i32 s28, 256\ns_lshl2_add_u32 s26, s26, s27\ns_add_u32 m0, s32, 65536\ns_mov_b32 s22, 6291456\nbuffer_load_dwordx4 v6, s[68:71], 0 offen offset:0 nt lds\ns_mov_b32 s23, 4194304\ns_cmp_lt_u32 s3, 128\ns_cselect_b32 s28, s28, 420\n" \
"s_cselect_b32 s29, 22, 18\ns_cselect_b32 s30, 19, 30\ns_cselect_b64 s[52:53], s[82:83], s[86:87]\ns_cmp_lt_u32 s3, 128\ns_cselect_b32 s54, s22, 8388608\ns_cselect_b64 s[56:57], s[80:81], s[84:85]\ns_cselect_b32 s22, 0, 2097152\ns_cselect_b32 s23, s23, 2097152\n" \
"buffer_load_dwordx4 v6, s[68:71], s89 offen offset:1024 nt lds\ns_add_u32 s56, s56, s22\ns_addc_u32 s57, s57, 0\ns_sub_u32 s27, 512, s28\ns_cmp_lt_u32 s26, s28\ns_cselect_b64 s[52:53], s[52:53], s[56:57]\ns_cselect_b32 s54, s54, s23\ns_cselect_b32 s29, s29, s30\n" \
"s_cselect_b32 s27, s28, s27\ns_cselect_b32 s28, 0, s28\nbuffer_load_dwordx4 v28, s[68:71], s90 offen offset:2048 nt lds\ns_mov_b32 s55, 0x20000\ns_sub_u32 s26, s26, s28\ns_lshl_b32 s30, s29, 4\nbuffer_load_dwordx4 v28, s[68:71], s91 offen offset:3072 nt lds\ns_lshl_b32 s29, s29, 7\n" \
"v_cmp_gt_u32 vcc, s30, v0\nv_lshlrev_b32 v18, 3, v0\ns_add_u32 m0, s32, 81920\ns_mul_i32 s26, s26, s29\nbuffer_load_dwordx4 v6, s[68:71], s92 offen offset:0 nt lds\ns_mul_i32 s44, s27, s29\nv_add_u32 v18, s26, v18\nv_cndmask_b32 v4, 2.0, v18, vcc\n" \
"buffer_load_dwordx4 v6, s[68:71], s93 offen offset:1024 nt lds\nbuffer_load_dwordx4 v28, s[68:71], s94 offen offset:2048 nt lds\nbuffer_load_dwordx4 v28, s[68:71], s95 offen offset:3072 nt lds\nbuffer_load_dwordx2 a[252:253], off, s[60:63], 0 sc1\nv_add_u32 v5, 128, v5\nv_add_u32 v6, s72, v6\nv_add_u32 v28, s72, v28\nbuffer_load_dword a251, v29, s[64:67], 0 offen sc1\n" \
""
#define Z_MAIN \
".macro D v,s,o,i\nbuffer_load_dwordx4 v\\v, s[\\s:\\s+3], \\o offen offset:\\i lds\n.endm\n.macro Ta d,v,o\nds_read_b64_tr_b16 a[\\d:\\d+1], v\\v offset:\\o\n.endm\n.macro Ra d,v,o\nds_read_b128 a[\\d:\\d+3], v\\v offset:\\o\n.endm\n.macro Zvaa d,a,b\nv_mfma_f32_16x16x32_bf16 v[\\d:\\d+3], a[\\a:\\a+3], a[\\b:\\b+3], 0\n.endm\n.macro AR d,a\nv_accvgpr_read_b32 v\\d, a\\a\n.endm\n.macro Zaaa d,a,b\nv_mfma_f32_16x16x32_bf16 a[\\d:\\d+3], a[\\a:\\a+3], a[\\b:\\b+3], 0\n.endm\n.macro Mvaa d,a,b\nv_mfma_f32_16x16x32_bf16 v[\\d:\\d+3], a[\\a:\\a+3], a[\\b:\\b+3], v[\\d:\\d+3]\n.endm\n.macro Maaa d,a,b\nv_mfma_f32_16x16x32_bf16 a[\\d:\\d+3], a[\\a:\\a+3], a[\\b:\\b+3], a[\\d:\\d+3]\n.endm\n" \
".macro C d,a,b\nv_cvt_pk_bf16_f32 v\\d, v\\a, v\\b\n.endm\n.macro AW d,a\nv_accvgpr_write_b32 a\\d, v\\a\n.endm\n.macro DS v,s,o,i\nbuffer_load_dwordx4 v\\v, s[\\s:\\s+3], \\o offen offset:\\i sc1 lds\n.endm\n.macro Qvav d,a,b,c\nv_mfma_f32_16x16x32_bf16 v[\\d:\\d+3], a[\\a:\\a+3], v[\\b:\\b+3], v[\\c:\\c+3]\n.endm\n.macro Mvav d,a,b\nv_mfma_f32_16x16x32_bf16 v[\\d:\\d+3], a[\\a:\\a+3], v[\\b:\\b+3], v[\\d:\\d+3]\n.endm\n.macro E d,a\nv_exp_f32 v\\d, v\\a\n.endm\n.macro Zaav d,a,b\nv_mfma_f32_16x16x32_bf16 a[\\d:\\d+3], a[\\a:\\a+3], v[\\b:\\b+3], 0\n.endm\n.macro Maav d,a,b\nv_mfma_f32_16x16x32_bf16 a[\\d:\\d+3], a[\\a:\\a+3], v[\\b:\\b+3], a[\\d:\\d+3]\n.endm\n" \
".macro Mvvv d,a,b\nv_mfma_f32_16x16x32_bf16 v[\\d:\\d+3], v[\\a:\\a+3], v[\\b:\\b+3], v[\\d:\\d+3]\n.endm\n.macro Zvav d,a,b\nv_mfma_f32_16x16x32_bf16 v[\\d:\\d+3], a[\\a:\\a+3], v[\\b:\\b+3], 0\n.endm\n.macro Rv d,v,o\nds_read_b128 v[\\d:\\d+3], v\\v offset:\\o\n.endm\n.macro Mvva d,a,b\nv_mfma_f32_16x16x32_bf16 v[\\d:\\d+3], v[\\a:\\a+3], a[\\b:\\b+3], v[\\d:\\d+3]\n.endm\n.macro Qvaa d,a,b,c\nv_mfma_f32_16x16x32_bf16 v[\\d:\\d+3], a[\\a:\\a+3], a[\\b:\\b+3], v[\\c:\\c+3]\n.endm\n.macro Zvvv d,a,b\nv_mfma_f32_16x16x32_bf16 v[\\d:\\d+3], v[\\a:\\a+3], v[\\b:\\b+3], 0\n.endm\n.macro Qvva d,a,b,c\nv_mfma_f32_16x16x32_bf16 v[\\d:\\d+3], v[\\a:\\a+3], a[\\b:\\b+3], v[\\c:\\c+3]\n.endm\ns_lshl_b32 s88, s32, 1\n" \
"v_xor_b32 v28, 128, v6\ns_mov_b32 s73, 7168\ns_mov_b32 s74, 14336\ns_mov_b32 s75, 21504\ns_mov_b32 s76, 32768\ns_mov_b32 s77, 39936\ns_mov_b32 s78, 47104\ns_mov_b32 s79, 54272\n" \
"s_mov_b32 s89, 31744\ns_mov_b32 s90, 63488\ns_mov_b32 s91, 95232\ns_mov_b32 s92, 256\ns_mov_b32 s93, 32000\ns_mov_b32 s94, 63744\ns_mov_b32 s95, 95488\ns_mov_b32 s72, 524288\n" \
"s_waitcnt lgkmcnt(0)\ns_add_u32 m0, s88, 0\ns_load_dwordx4 s[12:15], s[0:1], 0x60\nD 5,64,0,0\ns_load_dwordx2 s[16:17], s[0:1], 0x70\nv_mov_b32 v16, v6\nv_mov_b32 v17, v5\nD 5,64,s73,1024\n" \
"s_cmp_lt_u32 s3, 128\ns_cselect_b32 s26, 2, 0\ns_cmp_lt_u32 s3, 32\ns_cselect_b32 s27, 4, 0\ns_or_b32 s33, s33, s26\nD 5,64,s74,2048\ns_and_b32 s26, s3, 96\ns_or_b32 s33, s33, s27\n" \
"s_cmp_eq_u32 s26, 96\ns_cselect_b32 s26, 8, 0\nD 5,64,s75,3072\ns_xor_b32 s27, s32, 0x3000\ns_or_b32 s33, s33, s26\ns_or_b32 s27, s27, s3\ns_cselect_b32 s27, 0, 16\ns_add_u32 m0, s88, 4096\n" \
"s_or_b32 s33, s33, s27\nD 5,64,s76,0\nv_xor_b32 v20, 32, v8\nv_xor_b32 v21, 64, v8\nv_xor_b32 v22, 96, v8\nD 5,64,s77,1024\nv_xor_b32 v23, 128, v8\nv_xor_b32 v24, 160, v8\n" \
"v_xor_b32 v25, 192, v8\nD 5,64,s78,2048\nv_xor_b32 v26, 224, v8\nv_xor_b32 v27, 64, v7\ns_and_b32 s26, s3, 127\nD 5,64,s79,3072\ns_lshr_b32 s27, s32, 12\ns_movk_i32 s28, 256\n" \
"s_lshl2_add_u32 s26, s26, s27\ns_add_u32 m0, s32, 65536\ns_mov_b32 s22, 6291456\nbuffer_load_dwordx4 v6, s[68:71], 0 offen offset:0 nt lds\ns_mov_b32 s23, 4194304\ns_cmp_lt_u32 s3, 128\ns_cselect_b32 s28, s28, 420\ns_cselect_b32 s29, 22, 18\n" \
"s_cselect_b32 s30, 19, 30\ns_cselect_b64 s[52:53], s[82:83], s[86:87]\ns_cmp_lt_u32 s3, 128\ns_cselect_b32 s54, s22, 8388608\ns_cselect_b64 s[56:57], s[80:81], s[84:85]\ns_cselect_b32 s22, 0, 2097152\ns_cselect_b32 s23, s23, 2097152\nbuffer_load_dwordx4 v6, s[68:71], s89 offen offset:1024 nt lds\n" \
"s_add_u32 s56, s56, s22\ns_addc_u32 s57, s57, 0\ns_sub_u32 s27, 512, s28\ns_cmp_lt_u32 s26, s28\ns_cselect_b64 s[52:53], s[52:53], s[56:57]\ns_cselect_b32 s54, s54, s23\ns_cselect_b32 s29, s29, s30\ns_cselect_b32 s27, s28, s27\n" \
"s_cselect_b32 s28, 0, s28\nbuffer_load_dwordx4 v28, s[68:71], s90 offen offset:2048 nt lds\ns_mov_b32 s55, 0x20000\ns_sub_u32 s26, s26, s28\ns_lshl_b32 s30, s29, 4\nbuffer_load_dwordx4 v28, s[68:71], s91 offen offset:3072 nt lds\ns_lshl_b32 s29, s29, 7\nv_cmp_gt_u32 vcc, s30, v0\n" \
"v_lshlrev_b32 v18, 3, v0\ns_add_u32 m0, s32, 81920\ns_mul_i32 s26, s26, s29\nbuffer_load_dwordx4 v6, s[68:71], s92 offen offset:0 nt lds\ns_mul_i32 s44, s27, s29\nv_add_u32 v18, s26, v18\nv_cndmask_b32 v4, 2.0, v18, vcc\nbuffer_load_dwordx4 v6, s[68:71], s93 offen offset:1024 nt lds\n" \
"buffer_load_dwordx4 v28, s[68:71], s94 offen offset:2048 nt lds\nbuffer_load_dwordx4 v28, s[68:71], s95 offen offset:3072 nt lds\nbuffer_load_dwordx2 a[252:253], off, s[60:63], 0 sc1\nv_add_u32 v5, 128, v5\nv_add_u32 v6, s72, v6\nv_add_u32 v28, s72, v28\nbuffer_load_dword a251, v29, s[64:67], 0 offen sc1\ns_add_u32 m0, s88, 32768\n" \
"s_movk_i32 s2, 0x4000\nD 5,64,0,0\ns_movk_i32 s23, 0x1000\ns_getpc_b64 s[96:97]\n1:\ns_add_u32 s96, s96, zg0-1b\ns_addc_u32 s97, s97, 0\nD 5,64,s73,1024\n" \
"s_add_u32 s4, s32, zgv-zg0-4096\ns_add_u32 s5, s32, zgx-zg0-8192\nD 5,64,s74,2048\ns_add_u32 s6, s4, 4096\ns_cmp_lt_u32 s32, 0x2000\ns_cselect_b32 s5, s32, s5\nD 5,64,s75,3072\ns_cmp_eq_u32 s32, 0x3000\n" \
"s_cselect_b32 s6, s6, s4\ns_add_u32 s7, s32, zgt-zg0-0\ns_add_u32 m0, s88, 36864\ns_mov_b32 s10, zgx-zg0-0\nD 5,64,s76,0\ns_bitcmp1_b32 s33, 0\ns_cselect_b32 s5, s6, s5\ns_cselect_b32 s4, s4, s32\n" \
"s_cselect_b32 s10, s10, zga-zg0-0\ns_cselect_b32 s6, 0x3000, s2\ns_cselect_b32 s22, zgv-zg0+12288, s23\ns_bitcmp1_b32 s33, 1\ns_cselect_b32 s4, s5, s4\ns_cselect_b32 s6, s10, s6\ns_cselect_b32 s7, s7, s22\nD 5,64,s77,1024\n" \
"s_cmp_eq_u32 s32, 0\ns_cselect_b32 s4, 0, s4\ns_add_u32 s6, s6, s32\nD 5,64,s78,2048\ns_add_u32 s18, s96, s6\ns_addc_u32 s19, s97, 0\ns_add_u32 s20, s96, s7\ns_addc_u32 s21, s97, 0\n" \
"D 5,64,s79,3072\ns_add_u32 s24, s96, s4\ns_addc_u32 s25, s97, 0\ns_add_u32 m0, s32, 98304\ns_nop 0\nbuffer_load_dwordx4 v6, s[68:71], 0 offen offset:0 nt lds\nbuffer_load_dwordx4 v6, s[68:71], s89 offen offset:1024 nt lds\nbuffer_load_dwordx4 v28, s[68:71], s90 offen offset:2048 nt lds\n" \
"buffer_load_dwordx4 v28, s[68:71], s91 offen offset:3072 nt lds\ns_add_u32 m0, s32, 114688\ns_nop 0\nbuffer_load_dwordx4 v6, s[68:71], s92 offen offset:0 nt lds\nbuffer_load_dwordx4 v6, s[68:71], s93 offen offset:1024 nt lds\nbuffer_load_dwordx4 v28, s[68:71], s94 offen offset:2048 nt lds\nbuffer_load_dwordx4 v28, s[68:71], s95 offen offset:3072 nt lds\ns_swappc_b64 s[98:99], s[24:25]\n" \
"s_branch 99f\n.p2align 12\nzg0:\nzp0:\ns_setpc_b64 s[98:99]\n99:\ns_waitcnt vmcnt(17)\ns_barrier\n" \
"Ta 64,8,0\nTa 66,8,1024\nRa 0,7,0\nTa 68,20,0\nTa 70,20,1024\nRa 4,7,2048\nTa 72,21,0\nTa 74,21,1024\n" \
"Ra 8,7,4096\nTa 76,22,0\nTa 78,22,1024\nRa 12,7,6144\nTa 80,23,0\nTa 82,23,1024\nRa 16,7,8192\nTa 84,24,0\n" \
"Ta 86,24,1024\nRa 20,7,10240\nTa 88,25,0\nTa 90,25,1024\nRa 24,7,12288\nTa 92,26,0\nTa 94,26,1024\nRa 28,7,14336\n" \
"v_add_u32 v5, 128, v5\nv_add_u32 v6, s72, v6\nv_add_u32 v28, s72, v28\nbuffer_load_dword v30, v4, s[52:55], 0 offen sc1\nv_add_u32 v4, s44, v4\ns_bitcmp1_b32 s33, 0\ns_waitcnt lgkmcnt(0)\ns_cbranch_scc1 zV\n" \
"Zvaa 32,64,0\nTa 96,8,8192\ns_lshr_b32 s7, s3, 5\nZvaa 64,64,4\nTa 98,8,9216\ns_lshr_b32 s5, 170, s7\nZvaa 68,68,4\nRa 32,27,0\n" \
"s_and_b32 s5, s5, 1\nZvaa 36,68,0\nTa 100,20,8192\ns_lshl_b32 s7, s7, 2\nZvaa 40,72,0\nTa 102,20,9216\ns_lshr_b32 s4, 0x12124444, s7\nZvaa 72,72,4\n" \
"Ra 36,27,2048\ns_and_b32 s4, s4, 15\nZvaa 76,76,4\nTa 104,21,8192\ns_xor_b32 s6, s5, s3\nZvaa 44,76,0\nTa 106,21,9216\ns_lshr_b32 s7, 0xff00, s4\n" \
"Zvaa 48,80,0\nRa 40,27,4096\ns_and_b32 s7, s7, 0xff\nZvaa 80,80,4\nTa 108,22,8192\ns_xor_b32 s10, s7, 0xff\nZvaa 84,84,4\nTa 110,22,9216\n" \
"s_lshl_b32 s7, s7, 8\nZvaa 52,84,0\nRa 44,27,6144\ns_or_b32 s7, s7, 0xff\nZvaa 56,88,0\nTa 112,23,8192\ns_add_u32 s8, s4, 5\nZvaa 88,88,4\n" \
"Ta 114,23,9216\ns_sub_u32 s4, 262, s4\nZvaa 92,92,4\nRa 48,27,8192\ns_add_u32 s9, s4, 0x101\nZvaa 60,92,0\nTa 116,24,8192\nZvaa 124,92,8\n" \
"Ta 118,24,9216\nZvaa 156,92,12\nRa 52,27,10240\nZvaa 152,88,12\nTa 120,25,8192\nZvaa 120,88,8\nTa 122,25,9216\nZvaa 116,84,8\n" \
"Ra 56,27,12288\nZvaa 148,84,12\nTa 124,26,8192\nZvaa 144,80,12\nTa 126,26,9216\nZvaa 112,80,8\nRa 60,27,14336\nZvaa 108,76,8\n" \
"AR 2,252\nAR 3,253\nZvaa 140,76,12\nv_readfirstlane_b32 s96, v2\nv_readfirstlane_b32 s97, v3\nZvaa 136,72,12\ns_mul_i32 s97, s97, 0x7f4a7c15\ns_mul_hi_u32 s98, s96, 0x7f4a7c15\n" \
"Zvaa 104,72,8\ns_add_u32 s97, s97, s98\ns_mul_i32 s98, s96, 0x9e3779b9\nZvaa 100,68,8\ns_add_u32 s97, s97, s98\ns_mul_i32 s96, s96, 0x7f4a7c15\nZvaa 132,68,12\ns_add_u32 s96, s96, 0xd9b4e019\n" \
"s_addc_u32 s97, s97, 0x632be59b\nZvaa 128,64,12\nv_mov_b32 v2, s96\nv_mov_b32 v3, s97\nZvaa 96,64,8\ns_mov_b32 s96, 3\nZvaa 160,64,16\ns_waitcnt lgkmcnt(0)\n" \
"s_barrier\nZvaa 192,64,20\ns_add_u32 m0, s88, 0\nZvaa 196,68,20\nD 5,64,0,0\nZvaa 164,68,16\ns_cmp_lt_u32 s3, 128\ns_cselect_b32 s4, s9, s4\n" \
"Zvaa 168,72,16\ns_bitcmp1_b32 s6, 0\ns_cselect_b32 s9, s4, s8\ns_cselect_b32 s7, s10, s7\nZvaa 200,72,20\ns_and_b32 s6, s6, 1\ns_lshr_b32 s10, s3, 1\nZvaa 204,76,20\n" \
"D 5,64,s73,1024\nZvaa 172,76,16\ns_and_b32 s10, s10, 2\ns_or_b32 s6, s6, s10\nZvaa 176,80,16\ns_lshl_b32 s6, s6, 3\ns_lshl_b32 s8, s7, s6\nZvaa 208,80,20\n" \
"s_bfe_u32 s10, s3, 0x20003\ns_add_u32 s6, s6, s10\nZvaa 212,84,20\nD 5,64,s74,2048\nZvaa 180,84,16\ns_bitset0_b32 s8, s6\ns_lshl_b32 s5, s5, 16\nZvaa 184,88,16\n" \
"s_or_b32 s9, s9, s5\nZvaa 216,88,20\nZvaa 220,92,20\nD 5,64,s75,3072\nZvaa 188,92,16\nZvaa 252,92,24\nZaaa 156,92,28\ns_add_u32 m0, s88, 4096\n" \
"Zaaa 152,88,28\nD 5,64,s76,0\nZvaa 248,88,24\nZvaa 244,84,24\nZaaa 148,84,28\nZaaa 144,80,28\nD 5,64,s77,1024\nZvaa 240,80,24\n" \
"Zvaa 236,76,24\nZaaa 140,76,28\nZaaa 136,72,28\nD 5,64,s78,2048\nZvaa 232,72,24\nZvaa 228,68,24\nZaaa 132,68,28\nZaaa 128,64,28\n" \
"D 5,64,s79,3072\nZvaa 224,64,24\ns_branch 99f\n.p2align 12\nzp1:\ns_setpc_b64 s[98:99]\n99:\ns_nop 0\n" \
"zgK:\nMvaa 32,96,32\nMvaa 64,96,36\ns_add_u32 m0, s32, 65536\nMvaa 68,100,36\nbuffer_load_dwordx4 v6, s[68:71], 0 offen offset:0 nt lds\nMvaa 36,100,32\nMvaa 40,104,32\n" \
"Mvaa 72,104,36\nMvaa 76,108,36\nbuffer_load_dwordx4 v6, s[68:71], s89 offen offset:1024 nt lds\nMvaa 44,108,32\nMvaa 48,112,32\nMvaa 80,112,36\nMvaa 84,116,36\nbuffer_load_dwordx4 v28, s[68:71], s90 offen offset:2048 nt lds\n" \
"Mvaa 52,116,32\nMvaa 56,120,32\nMvaa 88,120,36\nMvaa 92,124,36\nbuffer_load_dwordx4 v28, s[68:71], s91 offen offset:3072 nt lds\nMvaa 60,124,32\nMvaa 124,124,40\nMvaa 156,124,44\n" \
"s_add_u32 m0, s32, 81920\nMvaa 152,120,44\nbuffer_load_dwordx4 v6, s[68:71], s92 offen offset:0 nt lds\nMvaa 120,120,40\nMvaa 116,116,40\nMvaa 148,116,44\nMvaa 144,112,44\nbuffer_load_dwordx4 v6, s[68:71], s93 offen offset:1024 nt lds\n" \
"Mvaa 112,112,40\nMvaa 108,108,40\nMvaa 140,108,44\nMvaa 136,104,44\nbuffer_load_dwordx4 v28, s[68:71], s94 offen offset:2048 nt lds\nMvaa 104,104,40\nMvaa 100,100,40\nMvaa 132,100,44\n" \
"s_swappc_b64 s[98:99], s[20:21]\nMvaa 128,96,44\nbuffer_load_dwordx4 v28, s[68:71], s95 offen offset:3072 nt lds\nMvaa 96,96,40\ns_waitcnt vmcnt(17)\ns_barrier\nMvaa 160,96,48\nbuffer_load_dword v30, v4, s[52:55], 0 offen sc1\n" \
"Ta 64,8,32768\nv_add_u32 v5, 128, v5\nMvaa 192,96,52\nTa 66,8,33792\nv_add_u32 v6, s72, v6\nMvaa 196,100,52\nRa 0,7,32768\nv_add_u32 v28, s72, v28\n" \
"Mvaa 164,100,48\nTa 68,20,32768\nMvaa 168,104,48\nTa 70,20,33792\nMvaa 200,104,52\nRa 4,7,34816\nMvaa 204,108,52\nv_add_u32 v4, s44, v4\n" \
"Ta 72,21,32768\nMvaa 172,108,48\nTa 74,21,33792\nMvaa 176,112,48\nRa 8,7,36864\nMvaa 208,112,52\nTa 76,22,32768\nMvaa 212,116,52\n" \
"Ta 78,22,33792\nMvaa 180,116,48\nRa 12,7,38912\nMvaa 184,120,48\nTa 80,23,32768\nMvaa 216,120,52\nTa 82,23,33792\nMvaa 220,124,52\n" \
"Ra 16,7,40960\nMvaa 188,124,48\nTa 84,24,32768\nMvaa 252,124,56\nTa 86,24,33792\nMaaa 156,124,60\nRa 20,7,43008\nMaaa 152,120,60\n" \
"Ta 88,25,32768\nMvaa 248,120,56\nTa 90,25,33792\nMvaa 244,116,56\nRa 24,7,45056\nMaaa 148,116,60\nTa 92,26,32768\nMaaa 144,112,60\n" \
"Ta 94,26,33792\nMvaa 240,112,56\nRa 28,7,47104\nMvaa 236,108,56\nMaaa 140,108,60\nMaaa 136,104,60\nMvaa 232,104,56\nMvaa 228,100,56\n" \
"Maaa 132,100,60\nMaaa 128,96,60\nMvaa 224,96,56\ns_waitcnt lgkmcnt(0)\nMvaa 32,64,0\nTa 96,8,40960\ns_and_b32 s4, s3, 4\nMvaa 64,64,4\n" \
"Ta 98,8,41984\ns_and_b32 s5, s3, 1\nMvaa 68,68,4\nRa 32,27,32768\ns_lshl_b32 s4, s4, 2\nMvaa 36,68,0\nTa 100,20,40960\ns_lshl3_add_u32 s4, s5, s4\n" \
"Mvaa 40,72,0\nTa 102,20,41984\ns_and_b32 s5, s3, 2\nMvaa 72,72,4\nRa 36,27,34816\ns_bfe_u32 s6, s3, 0x20003\nMvaa 76,76,4\nTa 104,21,40960\n" \
"s_lshl1_add_u32 s4, s5, s4\nMvaa 44,76,0\nTa 106,21,41984\ns_add_u32 s7, s4, s6\nMvaa 48,80,0\nRa 40,27,36864\ns_lshr_b32 s10, s11, 3\nMvaa 80,80,4\n" \
"Ta 108,22,40960\ns_and_b32 s22, s11, 7\nMvaa 84,84,4\nTa 110,22,41984\ns_lshl_b32 s4, s7, 8\nMvaa 52,84,0\nRa 44,27,38912\ns_lshl_b32 s5, s10, 9\n" \
"Mvaa 56,88,0\nTa 112,23,40960\ns_sub_u32 s4, s4, s5\nMvaa 88,88,4\nTa 114,23,41984\nv_add_u32 v14, s4, v16\nMvaa 92,92,4\nRa 48,27,40960\n" \
"s_mul_i32 s38, s7, 525312\nMvaa 60,92,0\nTa 116,24,40960\ns_add_u32 s38, s38, 17039360\nMvaa 124,92,8\nTa 118,24,41984\ns_lshr_b32 s4, s7, 1\nMvaa 156,92,12\n" \
"Ra 52,27,43008\ns_lshl_b32 s4, s4, 7\nMvaa 152,88,12\nTa 120,25,40960\nv_lshrrev_b32 v18, 1, v0\nMvaa 120,88,8\nTa 122,25,41984\ns_add_u32 s4, s4, 256\n" \
"Mvaa 116,84,8\nRa 56,27,45056\nv_and_b32 v18, 0x38, v18\nMvaa 148,84,12\nTa 124,26,40960\nv_add_u32 v1, s4, v18\nMvaa 144,80,12\nTa 126,26,41984\n" \
"s_lshr_b32 s23, s3, 5\nMvaa 112,80,8\nRa 60,27,47104\nMvaa 108,76,8\ns_cmp_lt_u32 s3, 128\ns_cselect_b32 s4, 0, 4\ns_cselect_b32 s5, 7, 23\nMvaa 140,76,12\n" \
"s_add_u32 s23, s23, s4\ns_sub_u32 s5, s5, s23\nMvaa 136,72,12\ns_lshl_b32 s42, s23, 2\ns_lshl_b32 s43, s5, 2\nMvaa 104,72,8\ns_sub_u32 s5, s5, s23\ns_lshl_b32 s37, s5, 18\n" \
"Mvaa 100,68,8\ns_lshl1_add_u32 s41, s5, -5\ns_lshl1_add_u32 s4, s23, -1\nMvaa 132,68,12\ns_lshl1_add_u32 s5, s23, -13\nMvaa 128,64,12\ns_cmp_lt_u32 s3, 128\ns_cselect_b32 s39, s4, 9\n" \
"s_cselect_b32 s40, 0, s5\nMvaa 96,64,8\ns_bfe_u32 s4, s32, 0x1000c\ns_lshr_b32 s5, s32, 13\nMvaa 160,64,16\ns_waitcnt lgkmcnt(0)\ns_barrier\nMvaa 192,64,20\n" \
"s_add_u32 m0, s88, 32768\ns_lshl1_add_u32 s4, s10, s4\nMvaa 196,68,20\nD 5,64,0,0\nMvaa 164,68,16\ns_lshl1_add_u32 s5, s22, s5\ns_mul_i32 s4, s4, 525312\nMvaa 168,72,16\n" \
"s_lshl_b32 s5, s5, 15\ns_add_u32 s4, s4, s5\nMvaa 200,72,20\ns_bitcmp1_b32 s33, 0\ns_cselect_b32 s5, 16810496, 0\ns_cselect_b32 s35, 8, 0\nMvaa 204,76,20\nD 5,64,s73,1024\n" \
"Mvaa 172,76,16\ns_add_u32 s4, s4, s5\ns_add_u32 s34, s4, 17039360\nMvaa 176,80,16\ns_lshl4_add_u32 s35, s11, s35\ns_add_u32 s35, s35, 256\nMvaa 208,80,20\ns_and_b32 s4, s3, 1\n" \
"s_lshl2_add_u32 s24, s4, s6\nMvaa 212,84,20\nD 5,64,s74,2048\nMvaa 180,84,16\ns_bfe_u32 s4, s3, 0x20001\ns_bfe_u32 s5, s3, 0x20005\nMvaa 184,88,16\ns_lshl2_add_u32 s25, s4, s5\n" \
"s_lshl4_add_u32 s26, s24, s25\nMvaa 216,88,20\nv_and_b32 v18, 0x70, v17\nv_lshlrev_b32 v19, 1, v17\nMvaa 220,92,20\nD 5,64,s75,3072\nMvaa 188,92,16\ns_sub_u32 s4, s24, s22\n" \
"v_sub_u32 v19, v19, v18\nMvaa 252,92,24\ns_lshl_b32 s4, s4, 19\nv_add_u32 v9, s4, v19\nMaaa 156,92,28\ns_add_u32 m0, s88, 36864\ns_lshl_b32 s4, s23, 18\nMaaa 152,88,28\n" \
"D 5,64,s76,0\nMvaa 248,88,24\ns_lshl_b32 s5, s22, 19\ns_sub_u32 s4, s4, s5\nMvaa 244,84,24\ns_mul_i32 s5, s32, 24\ns_sub_u32 s4, s4, s5\nMaaa 148,84,28\n" \
"v_add_u32 v13, s4, v19\nv_lshrrev_b32 v18, 13, v16\nMaaa 144,80,28\nD 5,64,s77,1024\nMvaa 240,80,24\nv_and_b32 v19, 0x70, v16\nv_mul_u32_u24 v18, 6144, v18\nMvaa 236,76,24\n" \
"s_lshl_b32 s4, s25, 7\nv_bfe_u32 v12, v16, 7, 1\nMaaa 140,76,28\nv_add3_u32 v18, v18, v19, s4\nv_xor_b32 v19, 1, v12\nMaaa 136,72,28\nD 5,64,s78,2048\nMvaa 232,72,24\n" \
"s_bfe_u32 s4, s32, 0x1000c\nv_lshl_add_u32 v10, v12, 11, v18\nMvaa 228,68,24\nv_lshl_add_u32 v11, v19, 11, v18\nv_xor_b32 v12, s4, v12\nMaaa 132,68,28\ns_mul_i32 s5, s32, 12\nv_mul_u32_u24 v12, 196608, v12\n" \
"Maaa 128,64,28\nD 5,64,s79,3072\nMvaa 224,64,24\ns_sub_u32 s5, 4096, s5\nv_add3_u32 v12, v18, v12, s5\nMvaa 32,96,32\ns_lshl3_add_u32 s48, s26, 4096\ns_add_u32 s4, s48, 4096\n" \
"Mvaa 64,96,36\ns_add_u32 m0, s32, 98304\ns_lshl_b32 s5, s26, 13\nMvaa 68,100,36\nbuffer_load_dwordx4 v6, s[68:71], 0 offen offset:0 nt lds\nMvaa 36,100,32\ns_lshr_b32 s6, s32, 1\ns_add_u32 s5, s5, s6\n" \
"Mvaa 40,104,32\ns_add_u32 s5, s5, 262144\ns_and_b32 s6, s3, 127\nMvaa 72,104,36\ns_lshl_b32 s6, s6, 14\ns_add_u32 s6, s6, s32\nMvaa 76,108,36\nbuffer_load_dwordx4 v6, s[68:71], s89 offen offset:1024 nt lds\n" \
"Mvaa 44,108,32\ns_cmp_lt_u32 s3, 128\ns_cselect_b32 s36, s4, s35\ns_cselect_b32 s46, s5, s6\nMvaa 48,112,32\ns_cmp_ge_u32 s32, 0x2000\ns_cselect_b32 s4, 0, 16\nMvaa 80,112,36\n" \
"s_sub_u32 s27, s26, s4\ns_xor_b32 s5, s32, 0x2000\nMvaa 84,116,36\nbuffer_load_dwordx4 v28, s[68:71], s90 offen offset:2048 nt lds\nMvaa 52,116,32\ns_lshl_b32 s4, s27, 13\ns_lshr_b32 s5, s5, 1\nMvaa 56,120,32\n" \
"s_add_u32 s4, s4, s5\ns_add_u32 s4, s4, 262144\nMvaa 88,120,36\ns_lshr_b32 s5, s32, 13\ns_or_b32 s5, s5, s24\nMvaa 92,124,36\nbuffer_load_dwordx4 v28, s[68:71], s91 offen offset:3072 nt lds\nMvaa 60,124,32\n" \
"s_cmp_eq_u32 s5, 0\ns_cselect_b32 s51, 1, 0\ns_cselect_b32 s47, 0x7fff0000, s4\ns_cselect_b32 s27, s26, s27\nMvaa 124,124,40\ns_lshl3_add_u32 s49, s27, 4096\ns_lshl_b32 s4, s26, 15\nMvaa 156,124,44\n" \
"s_add_u32 m0, s32, 114688\ns_and_b32 s5, s32, 0x2000\nMvaa 152,120,44\nbuffer_load_dwordx4 v6, s[68:71], s92 offen offset:0 nt lds\nMvaa 120,120,40\ns_lshl1_add_u32 s4, s5, s4\ns_bfe_u32 s5, s32, 0x1000c\nMvaa 116,116,40\n" \
"s_lshl_b32 s5, s5, 10\ns_add_u32 s4, s4, s5\nMvaa 148,116,44\ns_add_u32 s50, s4, 50660352\ns_lshr_b32 s6, s6, 12\nMvaa 144,112,44\nbuffer_load_dwordx4 v6, s[68:71], s93 offen offset:1024 nt lds\nMvaa 112,112,40\n" \
"s_mov_b32 s5, 182784\ns_mov_b32 s27, 2097152\nMvaa 108,108,40\nv_lshlrev_b32 v18, 3, v0\nv_cmp_gt_u32 vcc, 224, v0\nMvaa 140,108,44\ns_cmp_lt_u32 s6, 410\ns_cselect_b32 s4, 0, 410\n" \
"s_cselect_b32 s45, 734720, s5\ns_cselect_b64 s[56:57], s[14:15], s[12:13]\ns_cselect_b32 s27, 8388608, s27\nMvaa 136,104,44\nbuffer_load_dwordx4 v28, s[68:71], s94 offen offset:2048 nt lds\nMvaa 104,104,40\ns_sub_u32 s6, s6, s4\ns_mul_i32 s6, s6, 1792\n" \
"Mvaa 100,100,40\ns_cmp_lt_u32 s3, 128\ns_cselect_b32 s58, 0, s27\nMvaa 132,100,44\ns_swappc_b64 s[98:99], s[18:19]\ns_add_u32 s18, s18, 0x4000\ns_addc_u32 s19, s19, 0\nMvaa 128,96,44\n" \
"buffer_load_dwordx4 v28, s[68:71], s95 offen offset:3072 nt lds\nMvaa 96,96,40\ns_waitcnt vmcnt(17)\ns_barrier\nMvaa 160,96,48\nbuffer_load_dword v30, v4, s[52:55], 0 offen sc1\nTa 64,8,0\nv_add_u32 v5, 128, v5\n" \
"Mvaa 192,96,52\nTa 66,8,1024\nv_add_u32 v6, s72, v6\nMvaa 196,100,52\nRa 0,7,0\nv_add_u32 v28, s72, v28\nMvaa 164,100,48\nTa 68,20,0\n" \
"v_add_u32 v18, s6, v18\nMvaa 168,104,48\nTa 70,20,1024\ns_mov_b32 s59, 0x20000\nMvaa 200,104,52\nRa 4,7,2048\nv_cndmask_b32 v15, 2.0, v18, vcc\nMvaa 204,108,52\n" \
"v_add_u32 v4, s44, v4\nTa 72,21,0\nMvaa 172,108,48\nTa 74,21,1024\nMvaa 176,112,48\nRa 8,7,4096\nMvaa 208,112,52\nTa 76,22,0\n" \
"Mvaa 212,116,52\nTa 78,22,1024\nMvaa 180,116,48\nRa 12,7,6144\nMvaa 184,120,48\nTa 80,23,0\nMvaa 216,120,52\nTa 82,23,1024\n" \
"Mvaa 220,124,52\nRa 16,7,8192\nMvaa 188,124,48\nTa 84,24,0\nMvaa 252,124,56\nTa 86,24,1024\nMaaa 156,124,60\nRa 20,7,10240\n" \
"Maaa 152,120,60\nTa 88,25,0\nMvaa 248,120,56\nTa 90,25,1024\nMvaa 244,116,56\nRa 24,7,12288\nMaaa 148,116,60\nTa 92,26,0\n" \
"Maaa 144,112,60\nTa 94,26,1024\nMvaa 240,112,56\nRa 28,7,14336\nMvaa 236,108,56\nMaaa 140,108,60\nMaaa 136,104,60\nMvaa 232,104,56\n" \
"s_sub_u32 s96, s96, 1\nMvaa 228,100,56\ns_cmp_lg_u32 s96, 0\nMaaa 132,100,60\nMaaa 128,96,60\nMvaa 224,96,56\ns_waitcnt lgkmcnt(0)\ns_cbranch_scc0 zhK\n" \
"Mvaa 32,64,0\nTa 96,8,8192\nMvaa 64,64,4\nTa 98,8,9216\nMvaa 68,68,4\nRa 32,27,0\nMvaa 36,68,0\nTa 100,20,8192\n" \
"Mvaa 40,72,0\nTa 102,20,9216\nMvaa 72,72,4\nRa 36,27,2048\nMvaa 76,76,4\nTa 104,21,8192\nMvaa 44,76,0\nTa 106,21,9216\n" \
"Mvaa 48,80,0\nRa 40,27,4096\nMvaa 80,80,4\nTa 108,22,8192\nMvaa 84,84,4\nTa 110,22,9216\nMvaa 52,84,0\nRa 44,27,6144\n" \
"Mvaa 56,88,0\nTa 112,23,8192\nMvaa 88,88,4\nTa 114,23,9216\nMvaa 92,92,4\nRa 48,27,8192\nMvaa 60,92,0\nTa 116,24,8192\n" \
"Mvaa 124,92,8\nTa 118,24,9216\nMvaa 156,92,12\nRa 52,27,10240\nMvaa 152,88,12\nTa 120,25,8192\nMvaa 120,88,8\nTa 122,25,9216\n" \
"Mvaa 116,84,8\nRa 56,27,12288\nMvaa 148,84,12\nTa 124,26,8192\nMvaa 144,80,12\nTa 126,26,9216\nMvaa 112,80,8\nRa 60,27,14336\n" \
"Mvaa 108,76,8\nMvaa 140,76,12\nMvaa 136,72,12\nMvaa 104,72,8\nMvaa 100,68,8\nMvaa 132,68,12\nMvaa 128,64,12\nMvaa 96,64,8\n" \
"Mvaa 160,64,16\ns_waitcnt lgkmcnt(0)\ns_barrier\nMvaa 192,64,20\ns_add_u32 m0, s88, 0\nMvaa 196,68,20\nD 5,64,0,0\nMvaa 164,68,16\n" \
"Mvaa 168,72,16\nMvaa 200,72,20\nMvaa 204,76,20\nD 5,64,s73,1024\nMvaa 172,76,16\nMvaa 176,80,16\nMvaa 208,80,20\nMvaa 212,84,20\n" \
"D 5,64,s74,2048\nMvaa 180,84,16\nMvaa 184,88,16\nMvaa 216,88,20\nMvaa 220,92,20\nD 5,64,s75,3072\nMvaa 188,92,16\nMvaa 252,92,24\n" \
"Maaa 156,92,28\ns_add_u32 m0, s88, 4096\nMaaa 152,88,28\nD 5,64,s76,0\nMvaa 248,88,24\nMvaa 244,84,24\nMaaa 148,84,28\nMaaa 144,80,28\n" \
"D 5,64,s77,1024\nMvaa 240,80,24\nMvaa 236,76,24\nMaaa 140,76,28\nMaaa 136,72,28\nD 5,64,s78,2048\nMvaa 232,72,24\nMvaa 228,68,24\n" \
"Maaa 132,68,28\nMaaa 128,64,28\nD 5,64,s79,3072\nMvaa 224,64,24\ns_branch zgK\nzhK:\ns_bitcmp1_b32 s33, 1\ns_cbranch_scc1 zxK\n" \
"Mvaa 32,64,0\nTa 96,8,8192\ns_mov_b64 s[64:65], s[84:85]\nMvaa 64,64,4\nTa 98,8,9216\ns_mov_b32 s66, 4194304\nMvaa 68,68,4\nRa 32,27,0\n" \
"s_mov_b64 s[68:69], s[86:87]\nMvaa 36,68,0\nTa 100,20,8192\ns_branch 99f\n.p2align 12\nzp2:\ns_setpc_b64 s[98:99]\n99:\n" \
"s_mov_b32 s70, 8388608\nMvaa 40,72,0\nTa 102,20,9216\ns_mov_b32 s73, 15360\nMvaa 72,72,4\nRa 36,27,2048\ns_mov_b32 s74, 129024\nMvaa 76,76,4\n" \
"Ta 104,21,8192\ns_mov_b32 s75, 144384\nMvaa 44,76,0\nTa 106,21,9216\ns_add_u32 s76, s37, 0\nMvaa 48,80,0\nRa 40,27,4096\ns_add_u32 s77, s37, 15360\n" \
"Mvaa 80,80,4\nTa 108,22,8192\ns_add_u32 s78, s37, 129024\nMvaa 84,84,4\nTa 110,22,9216\ns_add_u32 s79, s37, 144384\nMvaa 52,84,0\nRa 44,27,6144\n" \
"s_mov_b32 s89, 31744\nMvaa 56,88,0\nTa 112,23,8192\ns_mov_b32 s90, 63488\nMvaa 88,88,4\nTa 114,23,9216\ns_mov_b32 s91, 95232\nMvaa 92,92,4\n" \
"Ra 48,27,8192\ns_mov_b32 s72, 524288\nMvaa 60,92,0\nTa 116,24,8192\nv_xor_b32 v28, 128, v14\nMvaa 124,92,8\nTa 118,24,9216\nMvaa 156,92,12\n" \
"Ra 52,27,10240\nMvaa 152,88,12\nTa 120,25,8192\nMvaa 120,88,8\nTa 122,25,9216\nMvaa 116,84,8\nRa 56,27,12288\nMvaa 148,84,12\n" \
"Ta 124,26,8192\nMvaa 144,80,12\nTa 126,26,9216\nMvaa 112,80,8\nRa 60,27,14336\nMvaa 108,76,8\nMvaa 140,76,12\nMvaa 136,72,12\n" \
"Mvaa 104,72,8\nMvaa 100,68,8\nMvaa 132,68,12\nMvaa 128,64,12\nMvaa 96,64,8\nMvaa 160,64,16\ns_waitcnt lgkmcnt(0)\ns_barrier\n" \
"Mvaa 192,64,20\ns_add_u32 m0, s88, 131072\ns_and_b32 s10, s3, 2\nMvaa 196,68,20\nD 13,64,0,0\nMvaa 164,68,16\ns_bfe_u32 s100, s3, 0x10002\ns_lshl_b32 s10, s10, 1\n" \
"Mvaa 168,72,16\ns_bfe_u32 s2, s9, 0x10010\ns_xor_b32 s99, s2, s3\nMvaa 200,72,20\ns_and_b32 s99, s99, 1\ns_lshl1_add_u32 s99, s100, s99\nMvaa 204,76,20\nD 13,64,s73,1024\n" \
"Mvaa 172,76,16\ns_bfe_u32 s101, s3, 0x20003\ns_lshl3_add_u32 s11, s99, s101\nMvaa 176,80,16\ns_add_u32 s101, s101, s10\ns_lshl3_add_u32 s10, s2, s10\nMvaa 208,80,20\ns_bfe_u32 s2, s3, 0x20005\n" \
"s_lshl1_add_u32 s2, s2, s100\nMvaa 212,84,20\nD 13,64,s74,2048\nMvaa 180,84,16\ns_bfe_u32 s23, s9, 0x80008\ns_and_b32 s22, s9, 255\nMvaa 184,88,16\ns_lshr_b32 s6, 4, s99\n" \
"s_lshr_b32 s25, s3, 5\nMvaa 216,88,20\ns_lshl_b32 s4, s25, 8\ns_lshl_b32 s18, s25, 21\nMvaa 220,92,20\nD 13,64,s75,3072\nMvaa 188,92,16\ns_lshl3_add_u32 s25, s25, s101\n" \
"s_lshl_b32 s26, s25, 18\nMvaa 252,92,24\ns_lshl_b32 s27, s25, 7\ns_add_u32 s4, s4, 16384\nMaaa 156,92,28\ns_add_u32 m0, s88, 135168\ns_xor_b32 s25, s11, s10\nMaaa 152,88,28\n" \
"D 13,64,s76,0\nMvaa 248,88,24\ns_lshl3_add_u32 s31, s25, s4\ns_lshl_b32 s5, s2, 7\nMvaa 244,84,24\ns_add_u32 s5, s5, 8192\ns_lshr_b32 s25, s32, 1\nMaaa 148,84,28\n" \
"s_add_u32 s18, s18, s25\ns_add_u32 s18, s18, 54854656\nMaaa 144,80,28\nD 13,64,s77,1024\nMvaa 240,80,24\ns_lshl_b32 s19, s2, 19\ns_add_u32 s19, s19, s25\nMvaa 236,76,24\n" \
"s_add_u32 s19, s19, 48563200\ns_lshr_b32 s25, s32, 7\nMaaa 140,76,28\ns_add_u32 s27, s27, s25\ns_lshl3_add_u32 s27, s99, s27\nMaaa 136,72,28\nD 13,64,s78,2048\nMvaa 232,72,24\n" \
"s_add_u32 s27, s27, 32768\ns_lshl1_add_u32 s26, s32, s26\nMvaa 228,68,24\ns_add_u32 s26, s26, 71631872\ns_lshl_b32 s28, s99, 15\nMaaa 132,68,28\ns_add_u32 s24, s26, s28\ns_sub_u32 s28, 0x10000, s28\n" \
"Maaa 128,64,28\nD 13,64,s79,3072\nMvaa 224,64,24\ns_lshl_b32 s25, s100, 17\ns_add_u32 s26, s26, s25\nMvaa 32,96,32\ns_cmp_eq_u32 s99, 0\ns_cselect_b32 s21, 128, 0\n" \
"Mvaa 64,96,36\ns_add_u32 m0, s32, 81920\ns_lshl_b32 s25, s21, 10\nMvaa 68,100,36\nD 14,68,0,0\nMvaa 36,100,32\ns_add_u32 s24, s24, s25\ns_add_u32 s25, s24, 0x20000\n" \
"Mvaa 40,104,32\ns_lshl_b32 s101, s101, 8\ns_and_b32 s20, s32, 0x2000\nMvaa 72,104,36\ns_lshl3_add_u32 s20, s20, s101\ns_and_b32 s99, s32, 0x1000\nMvaa 76,108,36\nD 14,68,s89,1024\n" \
"Mvaa 44,108,32\ns_lshl2_add_u32 s20, s99, s20\ns_lshl4_add_u32 s101, s32, s101\nMvaa 48,112,32\ns_lshl_b32 s2, s2, 20\ns_add_u32 s2, s2, s101\nMvaa 80,112,36\ns_lshl_b32 s100, s100, 7\n" \
"s_add_u32 s101, s101, s100\nMvaa 84,116,36\nD 28,68,s90,2048\nMvaa 52,116,32\ns_add_u32 s101, s101, 2048\ns_lshl_b32 s29, s42, 17\nMvaa 56,120,32\ns_lshl_b32 s30, s43, 17\n" \
"s_add_u32 s29, s29, s101\nMvaa 88,120,36\ns_add_u32 s30, s30, s101\ns_add_u32 s101, s2, 524288\nMvaa 92,124,36\nD 28,68,s91,3072\nMvaa 60,124,32\ns_cmp_eq_u32 s23, 2\n" \
"s_cselect_b32 s29, s2, s29\ns_cselect_b32 s30, s101, s30\ns_cselect_b32 s7, 0xffff, 0\ns_cselect_b32 s9, 0xff, 0\nMvaa 124,124,40\ns_and_b32 s101, s10, 8\ns_lshl_b32 s9, s9, s101\nMvaa 156,124,44\n" \
"s_add_u32 m0, s88, 0\nv_add_u32 v13, 128, v13\nMvaa 152,120,44\nD 13,64,0,0\nMvaa 120,120,40\ns_mov_b32 s2, zqS-zq0-8192\nMvaa 116,116,40\ns_cmp_eq_u32 s23, 1\n" \
"s_cselect_b32 s2, zqR-zq0-8192, s2\nMvaa 148,116,44\ns_cmp_eq_u32 s23, 0\ns_cselect_b32 s2, zqT-zq0-8192, s2\nMvaa 144,112,44\nD 13,64,s73,1024\nMvaa 112,112,40\ns_cmp_lt_u32 s32, 0x2000\n" \
"s_cselect_b32 s2, 0, s2\nMvaa 108,108,40\ns_add_u32 s2, s2, s32\nMvaa 140,108,44\nMvaa 136,104,44\nD 13,64,s74,2048\nMvaa 104,104,40\nMvaa 100,100,40\n" \
"Mvaa 132,100,44\nMvaa 128,96,44\nD 13,64,s75,3072\nMvaa 96,96,40\ns_waitcnt vmcnt(17)\ns_barrier\nMvaa 160,96,48\nbuffer_load_dword v30, v4, s[52:55], 0 offen sc1\n" \
"Ta 64,8,32768\nMvaa 192,96,52\nTa 66,8,33792\nMvaa 196,100,52\nRa 0,7,32768\nMvaa 164,100,48\nTa 68,20,32768\nMvaa 168,104,48\n" \
"Ta 70,20,33792\nMvaa 200,104,52\nRa 4,7,34816\nMvaa 204,108,52\nv_add_u32 v4, s44, v4\nTa 72,21,32768\nMvaa 172,108,48\nTa 74,21,33792\n" \
"Mvaa 176,112,48\nRa 8,7,36864\nMvaa 208,112,52\nTa 76,22,32768\nMvaa 212,116,52\nTa 78,22,33792\nMvaa 180,116,48\nRa 12,7,38912\n" \
"Mvaa 184,120,48\nTa 80,23,32768\nMvaa 216,120,52\nTa 82,23,33792\nMvaa 220,124,52\nRa 16,7,40960\nMvaa 188,124,48\nTa 84,24,32768\n" \
"Mvaa 252,124,56\nTa 86,24,33792\nMaaa 156,124,60\nRa 20,7,43008\nMaaa 152,120,60\nTa 88,25,32768\nMvaa 248,120,56\nTa 90,25,33792\n" \
"Mvaa 244,116,56\nRa 24,7,45056\nMaaa 148,116,60\nTa 92,26,32768\nMaaa 144,112,60\nTa 94,26,33792\nMvaa 240,112,56\nRa 28,7,47104\n" \
"Mvaa 236,108,56\nMaaa 140,108,60\nMaaa 136,104,60\nMvaa 232,104,56\nMvaa 228,100,56\nMaaa 132,100,60\nMaaa 128,96,60\nMvaa 224,96,56\n" \
"s_waitcnt lgkmcnt(0)\nMvaa 32,64,0\nTa 96,8,40960\nMvaa 64,64,4\nTa 98,8,41984\nMvaa 68,68,4\nRa 32,27,32768\nMvaa 36,68,0\n" \
"Ta 100,20,40960\nMvaa 40,72,0\nTa 102,20,41984\nMvaa 72,72,4\nRa 36,27,34816\nMvaa 76,76,4\nTa 104,21,40960\nMvaa 44,76,0\n" \
"Ta 106,21,41984\nMvaa 48,80,0\nRa 40,27,36864\nMvaa 80,80,4\nTa 108,22,40960\nMvaa 84,84,4\nTa 110,22,41984\nMvaa 52,84,0\n" \
"Ra 44,27,38912\nMvaa 56,88,0\nTa 112,23,40960\nMvaa 88,88,4\nTa 114,23,41984\nMvaa 92,92,4\nRa 48,27,40960\nMvaa 60,92,0\n" \
"Ta 116,24,40960\nMvaa 124,92,8\nTa 118,24,41984\nMvaa 156,92,12\nRa 52,27,43008\nMvaa 152,88,12\nTa 120,25,40960\nMvaa 120,88,8\n" \
"Ta 122,25,41984\nMvaa 116,84,8\nRa 56,27,45056\nMvaa 148,84,12\nTa 124,26,40960\nMvaa 144,80,12\nTa 126,26,41984\nMvaa 112,80,8\n" \
"Ra 60,27,47104\nMvaa 108,76,8\ns_and_b32 s96, s32, 0x1000\ns_lshl_b32 s97, s96, 1\ns_lshl_b32 s96, s96, 2\nMvaa 140,76,12\nv_add_u32 v16, s97, v7\nv_subrev_u32 v20, s96, v8\n" \
"v_xor_b32 v17, 64, v16\nMvaa 136,72,12\nv_add_u32 v18, 0x20000, v16\nv_add_u32 v19, 0x20000, v17\nv_xor_b32 v21, 32, v20\nMvaa 104,72,8\nv_xor_b32 v22, 64, v20\nv_xor_b32 v23, 96, v20\n" \
"v_xor_b32 v24, 128, v20\nMvaa 100,68,8\nv_xor_b32 v25, 160, v20\nv_xor_b32 v26, 192, v20\nv_xor_b32 v27, 224, v20\nMvaa 132,68,12\nMvaa 128,64,12\nMvaa 96,64,8\n" \
"Mvaa 160,64,16\ns_waitcnt vmcnt(5) lgkmcnt(0)\ns_barrier\nMvaa 192,64,20\ns_add_u32 m0, s88, 4096\nMvaa 196,68,20\nD 13,64,s76,0\nMvaa 164,68,16\n" \
"Ta 160,20,24576\nMvaa 168,72,16\nTa 162,20,25600\nMvaa 200,72,20\nD 13,64,s77,1024\nMvaa 204,76,20\nTa 164,21,24576\nMvaa 172,76,16\n" \
"Ta 166,21,25600\nMvaa 176,80,16\nD 13,64,s78,2048\nMvaa 208,80,20\nTa 168,22,24576\nMvaa 212,84,20\nTa 170,22,25600\nMvaa 180,84,16\n" \
"D 13,64,s79,3072\nMvaa 184,88,16\nTa 172,23,24576\nMvaa 216,88,20\ns_add_u32 m0, s32, 65536\nv_add_u32 v14, s72, v14\nv_add_u32 v28, s72, v28\nMvaa 220,92,20\n" \
"D 14,68,0,0\nMvaa 188,92,16\nTa 174,23,25600\nMvaa 252,92,24\nTa 176,24,24576\nMaaa 156,92,28\nD 14,68,s89,1024\nMaaa 152,88,28\n" \
"Ta 178,24,25600\nMvaa 248,88,24\nTa 180,25,24576\nMvaa 244,84,24\nD 28,68,s90,2048\nMaaa 148,84,28\nTa 182,25,25600\nMaaa 144,80,28\n" \
"Ta 184,26,24576\nMvaa 240,80,24\nD 28,68,s91,3072\nMvaa 236,76,24\nTa 186,26,25600\nMaaa 140,76,28\ns_add_u32 m0, s88, 32768\nv_add_u32 v13, 128, v13\n" \
"Maaa 136,72,28\nD 13,64,0,0\nMvaa 232,72,24\nTa 188,27,24576\nMvaa 228,68,24\nTa 190,27,25600\nMaaa 132,68,28\nD 13,64,s73,1024\n" \
"Maaa 128,64,28\nMvaa 224,64,24\nMvaa 32,96,32\nD 13,64,s74,2048\nMvaa 36,100,32\nTa 64,20,16384\nMvaa 40,104,32\nTa 66,20,17408\n" \
"Mvaa 44,108,32\nD 13,64,s75,3072\nMvaa 48,112,32\nMvaa 52,116,32\ns_add_u32 m0, s88, 36864\nMvaa 56,120,32\nD 13,64,s76,0\nMvaa 60,124,32\n" \
"Ra 0,18,0\nMvaa 64,96,36\nMvaa 68,100,36\nD 13,64,s77,1024\nMvaa 72,104,36\nRa 16,19,0\nC 32,32,33\nC 33,34,35\n" \
"Mvaa 76,108,36\nC 34,36,37\nC 35,38,39\nC 36,40,41\nMvaa 80,112,36\nD 13,64,s78,2048\nC 37,42,43\nMvaa 84,116,36\n" \
"Ta 68,21,16384\nC 38,44,45\nC 39,46,47\nMvaa 88,120,36\nC 40,48,49\nC 41,50,51\nC 42,52,53\nMvaa 92,124,36\n" \
"D 13,64,s79,3072\nC 43,54,55\nMvaa 96,96,40\nTa 70,21,17408\nC 44,56,57\nC 45,58,59\nMvaa 100,100,40\ns_add_u32 m0, s32, 98304\n" \
"v_add_u32 v14, s72, v14\nv_add_u32 v28, s72, v28\nMvaa 104,104,40\nD 14,68,0,0\nC 46,60,61\nMvaa 108,108,40\nC 47,62,63\nC 48,64,65\n" \
"C 49,66,67\nMvaa 112,112,40\nTa 72,22,16384\nC 50,68,69\nC 51,70,71\nMvaa 116,116,40\nD 14,68,s89,1024\nC 52,72,73\n" \
"Mvaa 120,120,40\nTa 74,22,17408\nC 53,74,75\nC 54,76,77\nMvaa 124,124,40\nC 55,78,79\nC 56,80,81\nC 57,82,83\n" \
"Mvaa 128,96,44\nD 28,68,s90,2048\nC 58,84,85\nMvaa 132,100,44\nTa 76,23,16384\nC 59,86,87\nC 60,88,89\nMvaa 136,104,44\n" \
"C 61,90,91\nC 62,92,93\nC 63,94,95\nMvaa 140,108,44\nD 28,68,s91,3072\nC 64,96,97\nMvaa 144,112,44\nTa 78,23,17408\n" \
"C 65,98,99\nC 66,100,101\nMvaa 148,116,44\nC 67,102,103\nC 68,104,105\nC 69,106,107\nMvaa 152,120,44\nbuffer_load_dword v30, v4, s[52:55], 0 offen sc1\n" \
"C 70,108,109\nMvaa 156,124,44\nTa 80,24,16384\nC 71,110,111\nC 72,112,113\nMaaa 128,96,60\nC 73,114,115\nC 74,116,117\n" \
"C 75,118,119\nMaaa 132,100,60\nTa 82,24,17408\nC 76,120,121\nC 77,122,123\nMaaa 136,104,60\nC 78,124,125\nC 79,126,127\n" \
"C 80,128,129\nMaaa 140,108,60\nTa 84,25,16384\nC 81,130,131\nC 82,132,133\nMaaa 144,112,60\nv_add_u32 v4, s44, v4\nC 83,134,135\n" \
"C 84,136,137\nMaaa 148,116,60\nTa 86,25,17408\nC 85,138,139\nC 86,140,141\ns_branch 99f\n.p2align 12\nzp3:\n" \
"s_setpc_b64 s[98:99]\n99:\nMaaa 152,120,60\nC 87,142,143\nC 88,144,145\nC 89,146,147\nMaaa 156,124,60\nTa 88,26,16384\n" \
"C 90,148,149\nC 91,150,151\nMvaa 160,96,48\nC 92,152,153\nC 93,154,155\nC 94,156,157\nMvaa 164,100,48\nTa 90,26,17408\n" \
"C 95,158,159\nMvaa 168,104,48\nAR 96,128\nAR 97,129\nAR 98,130\nMvaa 172,108,48\nTa 92,27,16384\nAR 99,131\n" \
"AR 100,132\nMvaa 176,112,48\nAR 101,133\nAR 102,134\nAR 103,135\nMvaa 180,116,48\nTa 94,27,17408\nC 96,96,97\n" \
"C 97,98,99\nMvaa 184,120,48\nC 98,100,101\nC 99,102,103\nAW 240,96\nMvaa 188,124,48\nRa 4,18,2048\nAW 241,97\n" \
"AW 242,98\nMvaa 192,96,52\nAW 243,99\nAR 104,136\nAR 105,137\nMvaa 196,100,52\nRa 20,19,2048\nAR 106,138\n" \
"AR 107,139\nMvaa 200,104,52\nAR 108,140\nAR 109,141\nAR 110,142\nMvaa 204,108,52\nRa 8,18,4096\nAR 111,143\n" \
"C 104,104,105\nMvaa 208,112,52\nC 105,106,107\nC 106,108,109\nC 107,110,111\nMvaa 212,116,52\nRa 24,19,4096\nAW 244,104\n" \
"AW 245,105\nMvaa 216,120,52\nAW 246,106\nAW 247,107\nAR 112,144\nMvaa 220,124,52\nRa 12,18,6144\nAR 113,145\n" \
"AR 114,146\nMvaa 224,96,56\nAR 115,147\nAR 116,148\nAR 117,149\nMvaa 228,100,56\nRa 28,19,6144\nAR 118,150\n" \
"AR 119,151\nMvaa 232,104,56\nC 112,112,113\nC 113,114,115\nC 114,116,117\nMvaa 236,108,56\nC 115,118,119\nAW 248,112\n" \
"AW 249,113\nMvaa 240,112,56\nAW 250,114\nAW 251,115\nAR 120,152\nMvaa 244,116,56\nAR 121,153\nAR 122,154\n" \
"AR 123,155\nMvaa 248,120,56\nAR 124,156\nAR 125,157\nAR 126,158\nMvaa 252,124,56\nAR 127,159\nC 120,120,121\n" \
"C 121,122,123\ns_waitcnt lgkmcnt(6)\nzY:\nZvaa 128,64,0\nv_add_u32 v13, 128, v13\nC 122,124,125\nC 123,126,127\nMvaa 128,160,16\n" \
"s_add_u32 m0, s88, 131072\nAW 252,120\nAW 253,121\nZvaa 132,164,16\nD 13,64,0,0\nAW 254,122\nMvaa 132,68,0\nv_add_u32 v14, s72, v14\n" \
"AW 255,123\nC 160,160,161\nZvaa 136,72,0\nv_add_u32 v28, s72, v28\nC 161,162,163\nC 162,164,165\nMvaa 136,168,16\nC 163,166,167\n" \
"AW 192,160\nAW 193,161\nv_mov_b32 v4, v15\nZvaa 140,172,16\nAW 194,162\nAW 195,163\nC 168,168,169\nMvaa 140,76,0\n" \
"s_waitcnt lgkmcnt(0)\nD 13,64,s73,1024\nZvaa 144,80,0\nC 169,170,171\nC 170,172,173\nC 171,174,175\nMvaa 144,176,16\nAW 196,168\n" \
"AW 197,169\nAW 198,170\nZvaa 148,180,16\nAW 199,171\nC 176,176,177\nC 177,178,179\nMvaa 148,84,0\nC 178,180,181\n" \
"C 179,182,183\nAW 200,176\nZvaa 152,88,0\nD 13,64,s74,2048\nAW 201,177\nMvaa 152,184,16\nAW 202,178\nAW 203,179\n" \
"C 184,184,185\nZvaa 156,188,16\nC 185,186,187\nC 186,188,189\nC 187,190,191\nAW 204,184\nAW 205,185\nAW 206,186\n" \
"AW 207,187\nMvaa 156,92,0\nC 192,192,193\nC 193,194,195\nC 194,196,197\nZvaa 188,92,4\ns_waitcnt vmcnt(16)\ns_barrier\n" \
"C 195,198,199\nMvaa 188,188,20\nTa 96,20,0\nTa 98,20,1024\nAW 208,192\nZvaa 184,184,20\nD 13,64,s75,3072\nAW 209,193\n" \
"Mvaa 184,88,4\nTa 128,20,8192\nAW 210,194\nAW 211,195\nZvaa 180,84,4\nTa 130,20,9216\nC 200,200,201\nC 201,202,203\n" \
"Mvaa 180,180,20\nRa 32,16,0\nRa 48,17,0\nC 202,204,205\nZvaa 176,176,20\ns_add_u32 m0, s88, 135168\nC 203,206,207\nAW 212,200\n" \
"Mvaa 176,80,4\nD 13,64,s76,0\nAW 213,201\nZvaa 172,76,4\nTa 100,21,0\nAW 214,202\nAW 215,203\nMvaa 172,172,20\n" \
"Ta 102,21,1024\nTa 132,21,8192\nC 208,208,209\nZvaa 168,168,20\nTa 134,21,9216\nC 209,210,211\nC 210,212,213\nMvaa 168,72,4\n" \
"Ta 104,22,0\nC 211,214,215\nAW 216,208\nZvaa 164,68,4\nD 13,64,s77,1024\nAW 217,209\nMvaa 164,164,20\nTa 106,22,1024\n" \
"Ta 136,22,8192\nAW 218,210\nZvaa 160,160,20\nTa 138,22,9216\nAW 219,211\nC 216,216,217\nC 217,218,219\nC 218,220,221\n" \
"C 219,222,223\nAW 220,216\nAW 221,217\nAW 222,218\nAW 223,219\nMvaa 160,64,4\nTa 108,23,0\nC 224,224,225\n" \
"C 225,226,227\nZvaa 192,64,8\nTa 110,23,1024\nC 226,228,229\nC 227,230,231\nMvaa 192,160,24\nD 13,64,s78,2048\nAW 224,224\n" \
"Zvaa 196,164,24\nTa 140,23,8192\nTa 142,23,9216\nAW 225,225\nMvaa 196,68,8\nTa 112,24,0\nAW 226,226\nAW 227,227\n" \
"Zvaa 200,72,8\nTa 114,24,1024\nC 232,232,233\nC 233,234,235\nMvaa 200,168,24\nTa 144,24,8192\nC 234,236,237\nC 235,238,239\n" \
"Zvaa 204,172,24\nD 13,64,s79,3072\nAW 228,232\nMvaa 204,76,8\nTa 146,24,9216\nTa 116,25,0\nAW 229,233\nZvaa 208,80,8\n" \
"Ta 118,25,1024\nAW 230,234\nAW 231,235\nMvaa 208,176,24\nv_add_u32 v13, 128, v13\nTa 148,25,8192\nC 240,240,241\nZvaa 212,180,24\n" \
"s_add_u32 m0, s32, 81920\nC 241,242,243\nC 242,244,245\nMvaa 212,84,8\nD 14,68,0,0\nC 243,246,247\nZvaa 216,88,8\nTa 150,25,9216\n" \
"Ta 120,26,0\nAW 232,240\nMvaa 216,184,24\nTa 122,26,1024\nAW 233,241\nAW 234,242\nZvaa 220,188,24\nTa 152,26,8192\n" \
"AW 235,243\nC 248,248,249\nC 249,250,251\nC 250,252,253\nC 251,254,255\nAW 236,248\nAW 237,249\nAW 238,250\n" \
"AW 239,251\nMvaa 220,92,8\nTa 154,26,9216\nZvaa 252,92,12\nD 14,68,s89,1024\nMvaa 252,188,28\nTa 124,27,0\nTa 126,27,1024\n" \
"Zvaa 248,184,28\nTa 156,27,8192\nMvaa 248,88,12\nTa 158,27,9216\nZvaa 244,84,12\nRa 36,16,2048\nMvaa 244,180,28\nD 28,68,s90,2048\n" \
"Zvaa 240,176,28\nRa 52,17,2048\nRa 40,16,4096\nMvaa 240,80,12\nRa 56,17,4096\nZvaa 236,76,12\nRa 44,16,6144\nMvaa 236,172,28\n" \
"Ra 60,17,6144\nZvaa 232,168,28\nD 28,68,s91,3072\nMvaa 232,72,12\ns_add_u32 s95, s34, 0\nZvaa 228,68,12\nbuffer_store_dwordx4 v[32:35], v0, s[60:63], s95 offen offset:0 sc1\nMvaa 228,164,28\n" \
"v_add_u32 v14, s72, v14\nZvaa 224,160,28\nv_add_u32 v28, s72, v28\nMvaa 224,64,12\ns_waitcnt lgkmcnt(6)\nMvaa 128,96,32\nMvaa 128,128,48\ns_add_u32 m0, s88, 0\n" \
"Mvaa 132,132,48\nD 13,64,0,0\nMvaa 132,100,32\nMvaa 136,104,32\nMvaa 136,136,48\nMvaa 140,140,48\nMvaa 140,108,32\ns_waitcnt lgkmcnt(0)\n" \
"D 13,64,s73,1024\nMvaa 144,112,32\nMvaa 144,144,48\nMvaa 148,148,48\nbuffer_store_dwordx4 v[36:39], v0, s[60:63], s95 offen offset:1024 sc1\nMvaa 148,116,32\nMvaa 152,120,32\nD 13,64,s74,2048\n" \
"Mvaa 152,152,48\nMvaa 156,156,48\nMvaa 156,124,32\nMvaa 188,124,36\ns_waitcnt vmcnt(18)\ns_barrier\nMvaa 188,156,52\nTa 64,20,32768\n" \
"Ta 66,20,33792\nMvaa 184,152,52\nD 13,64,s75,3072\nMvaa 184,120,36\nTa 160,20,40960\nMvaa 180,116,36\nTa 162,20,41984\nMvaa 180,148,52\n" \
"Ra 0,16,32768\nRa 16,17,32768\nMvaa 176,144,52\ns_add_u32 m0, s88, 4096\nMvaa 176,112,36\nD 13,64,s76,0\nMvaa 172,108,36\nTa 68,21,32768\n" \
"Mvaa 172,140,52\nTa 70,21,33792\nTa 164,21,40960\nMvaa 168,136,52\nTa 166,21,41984\nMvaa 168,104,36\nTa 72,22,32768\nMvaa 164,100,36\n" \
"D 13,64,s77,1024\nMvaa 164,132,52\nTa 74,22,33792\nTa 168,22,40960\nMvaa 160,128,52\nTa 170,22,41984\nMvaa 160,96,36\nTa 76,23,32768\n" \
"Mvaa 192,96,40\nTa 78,23,33792\nMvaa 192,128,56\nD 13,64,s78,2048\nMvaa 196,132,56\nTa 172,23,40960\nTa 174,23,41984\nMvaa 196,100,40\n" \
"Ta 80,24,32768\nMvaa 200,104,40\nTa 82,24,33792\nMvaa 200,136,56\nTa 176,24,40960\nMvaa 204,140,56\nD 13,64,s79,3072\nMvaa 204,108,40\n" \
"Ta 178,24,41984\nTa 84,25,32768\nMvaa 208,112,40\nTa 86,25,33792\nMvaa 208,144,56\nv_add_u32 v13, 128, v13\nTa 180,25,40960\nMvaa 212,148,56\n" \
"s_add_u32 m0, s32, 65536\nMvaa 212,116,40\nD 14,68,0,0\nMvaa 216,120,40\nTa 182,25,41984\nTa 88,26,32768\nMvaa 216,152,56\nTa 90,26,33792\n" \
"Mvaa 220,156,56\nTa 184,26,40960\nMvaa 220,124,40\nTa 186,26,41984\nMvaa 252,124,44\nD 14,68,s89,1024\nMvaa 252,156,60\nTa 92,27,32768\n" \
"Ta 94,27,33792\nMvaa 248,152,60\nTa 188,27,40960\nMvaa 248,120,44\nTa 190,27,41984\nMvaa 244,116,44\nRa 4,16,34816\nMvaa 244,148,60\n" \
"D 28,68,s90,2048\nMvaa 240,144,60\nRa 20,17,34816\nRa 8,16,36864\nMvaa 240,112,44\nRa 24,17,36864\nMvaa 236,108,44\nRa 12,16,38912\n" \
"Mvaa 236,140,60\nRa 28,17,38912\nMvaa 232,136,60\nD 28,68,s91,3072\nMvaa 232,104,44\nMvaa 228,100,44\nMvaa 228,132,60\nv_add_u32 v14, s72, v14\n" \
"Mvaa 224,128,60\nv_add_u32 v28, s72, v28\nMvaa 224,96,44\ns_waitcnt lgkmcnt(6)\nMvaa 128,64,0\nMvaa 128,160,16\ns_add_u32 m0, s88, 32768\nMvaa 132,164,16\n" \
"D 13,64,0,0\nMvaa 132,68,0\nMvaa 136,72,0\nMvaa 136,168,16\nMvaa 140,172,16\nMvaa 140,76,0\ns_waitcnt lgkmcnt(0)\nD 13,64,s73,1024\n" \
"Mvaa 144,80,0\nMvaa 144,176,16\nMvaa 148,180,16\nbuffer_store_dwordx4 v[40:43], v0, s[60:63], s95 offen offset:2048 sc1\nMvaa 148,84,0\nMvaa 152,88,0\nD 13,64,s74,2048\nMvaa 152,184,16\n" \
"Mvaa 156,188,16\nMvaa 156,92,0\nMvaa 188,92,4\ns_waitcnt vmcnt(18)\ns_barrier\nMvaa 188,188,20\nTa 96,20,16384\nTa 98,20,17408\n" \
"Mvaa 184,184,20\nD 13,64,s75,3072\nMvaa 184,88,4\nTa 128,20,24576\nMvaa 180,84,4\nTa 130,20,25600\nMvaa 180,180,20\nRa 32,18,0\n" \
"Ra 48,19,0\nMvaa 176,176,20\ns_add_u32 m0, s88, 36864\nMvaa 176,80,4\nD 13,64,s76,0\nMvaa 172,76,4\nTa 100,21,16384\nMvaa 172,172,20\n" \
"Ta 102,21,17408\nTa 132,21,24576\nMvaa 168,168,20\nTa 134,21,25600\nMvaa 168,72,4\nTa 104,22,16384\nMvaa 164,68,4\nD 13,64,s77,1024\n" \
"Mvaa 164,164,20\nTa 106,22,17408\nTa 136,22,24576\nMvaa 160,160,20\ns_branch 99f\n.p2align 12\nzp4:\ns_setpc_b64 s[98:99]\n" \
"99:\nTa 138,22,25600\nMvaa 160,64,4\nTa 108,23,16384\nMvaa 192,64,8\nTa 110,23,17408\nMvaa 192,160,24\nD 13,64,s78,2048\n" \
"Mvaa 196,164,24\nTa 140,23,24576\nTa 142,23,25600\nMvaa 196,68,8\nTa 112,24,16384\nMvaa 200,72,8\nTa 114,24,17408\nMvaa 200,168,24\n" \
"Ta 144,24,24576\nMvaa 204,172,24\nD 13,64,s79,3072\nMvaa 204,76,8\nTa 146,24,25600\nTa 116,25,16384\nMvaa 208,80,8\nTa 118,25,17408\n" \
"Mvaa 208,176,24\nv_add_u32 v13, 128, v13\nTa 148,25,24576\nMvaa 212,180,24\ns_add_u32 m0, s32, 98304\nMvaa 212,84,8\nD 14,68,0,0\nMvaa 216,88,8\n" \
"Ta 150,25,25600\nTa 120,26,16384\nMvaa 216,184,24\nTa 122,26,17408\nMvaa 220,188,24\nTa 152,26,24576\nMvaa 220,92,8\nTa 154,26,25600\n" \
"Mvaa 252,92,12\nD 14,68,s89,1024\nMvaa 252,188,28\nTa 124,27,16384\nTa 126,27,17408\nMvaa 248,184,28\nTa 156,27,24576\nMvaa 248,88,12\n" \
"Ta 158,27,25600\nMvaa 244,84,12\nRa 36,18,2048\nMvaa 244,180,28\nD 28,68,s90,2048\nMvaa 240,176,28\nRa 52,19,2048\nRa 40,18,4096\n" \
"Mvaa 240,80,12\nRa 56,19,4096\nMvaa 236,76,12\nRa 44,18,6144\nMvaa 236,172,28\nRa 60,19,6144\nMvaa 232,168,28\nD 28,68,s91,3072\n" \
"Mvaa 232,72,12\nMvaa 228,68,12\nMvaa 228,164,28\nv_add_u32 v14, s72, v14\nMvaa 224,160,28\nv_add_u32 v28, s72, v28\nMvaa 224,64,12\ns_waitcnt lgkmcnt(6)\n" \
"Mvaa 128,96,32\nMvaa 128,128,48\ns_add_u32 m0, s88, 131072\nMvaa 132,132,48\nD 13,64,0,0\nMvaa 132,100,32\nMvaa 136,104,32\nMvaa 136,136,48\n" \
"Mvaa 140,140,48\nMvaa 140,108,32\ns_waitcnt lgkmcnt(0)\nD 13,64,s73,1024\nMvaa 144,112,32\nMvaa 144,144,48\nMvaa 148,148,48\nbuffer_store_dwordx4 v[44:47], v0, s[60:63], s95 offen offset:3072 sc1\n" \
"Mvaa 148,116,32\nMvaa 152,120,32\nD 13,64,s74,2048\nMvaa 152,152,48\nMvaa 156,156,48\nMvaa 156,124,32\nMvaa 188,124,36\ns_waitcnt vmcnt(17)\n" \
"s_barrier\nMvaa 188,156,52\nTa 64,20,0\nTa 66,20,1024\nMvaa 184,152,52\nD 13,64,s75,3072\nMvaa 184,120,36\nTa 160,20,8192\n" \
"Mvaa 180,116,36\nTa 162,20,9216\nMvaa 180,148,52\nRa 0,16,0\nRa 16,17,0\nMvaa 176,144,52\ns_add_u32 m0, s88, 135168\nMvaa 176,112,36\n" \
"D 13,64,s76,0\nMvaa 172,108,36\nTa 68,21,0\nMvaa 172,140,52\nTa 70,21,1024\nTa 164,21,8192\nMvaa 168,136,52\nTa 166,21,9216\n" \
"Mvaa 168,104,36\nTa 72,22,0\nMvaa 164,100,36\nD 13,64,s77,1024\nMvaa 164,132,52\nTa 74,22,1024\nTa 168,22,8192\nMvaa 160,128,52\n" \
"Ta 170,22,9216\nMvaa 160,96,36\nTa 76,23,0\nMvaa 192,96,40\nTa 78,23,1024\nMvaa 192,128,56\nD 13,64,s78,2048\nMvaa 196,132,56\n" \
"Ta 172,23,8192\nTa 174,23,9216\nMvaa 196,100,40\nTa 80,24,0\nMvaa 200,104,40\nTa 82,24,1024\nMvaa 200,136,56\nTa 176,24,8192\n" \
"Mvaa 204,140,56\nD 13,64,s79,3072\nMvaa 204,108,40\nTa 178,24,9216\nTa 84,25,0\nMvaa 208,112,40\nTa 86,25,1024\nMvaa 208,144,56\n" \
"v_add_u32 v13, 128, v13\nTa 180,25,8192\nMvaa 212,148,56\ns_add_u32 m0, s32, 81920\nMvaa 212,116,40\nD 14,68,0,0\nMvaa 216,120,40\nTa 182,25,9216\n" \
"Ta 88,26,0\nMvaa 216,152,56\nTa 90,26,1024\nMvaa 220,156,56\nTa 184,26,8192\nMvaa 220,124,40\nTa 186,26,9216\nMvaa 252,124,44\n" \
"D 14,68,s89,1024\nMvaa 252,156,60\nTa 92,27,0\nTa 94,27,1024\nMvaa 248,152,60\nTa 188,27,8192\nMvaa 248,120,44\nTa 190,27,9216\n" \
"Mvaa 244,116,44\nRa 4,16,2048\nMvaa 244,148,60\nD 28,68,s90,2048\nMvaa 240,144,60\nRa 20,17,2048\nRa 8,16,4096\nMvaa 240,112,44\n" \
"Ra 24,17,4096\nMvaa 236,108,44\nRa 12,16,6144\nMvaa 236,140,60\nRa 28,17,6144\nMvaa 232,136,60\nD 28,68,s91,3072\nMvaa 232,104,44\n" \
"Mvaa 228,100,44\nMvaa 228,132,60\nv_add_u32 v14, s72, v14\nMvaa 224,128,60\nv_add_u32 v28, s72, v28\nMvaa 224,96,44\ns_waitcnt lgkmcnt(6)\nMvaa 128,64,0\n" \
"Mvaa 128,160,16\ns_add_u32 m0, s88, 0\nMvaa 132,164,16\nD 13,64,0,0\nMvaa 132,68,0\nMvaa 136,72,0\nMvaa 136,168,16\nMvaa 140,172,16\n" \
"Mvaa 140,76,0\ns_waitcnt lgkmcnt(0)\nD 13,64,s73,1024\nMvaa 144,80,0\nMvaa 144,176,16\ns_add_u32 s95, s34, 4096\nMvaa 148,180,16\nbuffer_store_dwordx4 v[48:51], v0, s[60:63], s95 offen offset:0 sc1\n" \
"Mvaa 148,84,0\nMvaa 152,88,0\nD 13,64,s74,2048\nMvaa 152,184,16\nMvaa 156,188,16\nMvaa 156,92,0\nMvaa 188,92,4\ns_waitcnt vmcnt(17)\n" \
"s_barrier\nMvaa 188,188,20\nTa 96,20,32768\nTa 98,20,33792\nMvaa 184,184,20\nD 13,64,s75,3072\nMvaa 184,88,4\nTa 128,20,40960\n" \
"Mvaa 180,84,4\nTa 130,20,41984\nMvaa 180,180,20\nRa 32,16,32768\nRa 48,17,32768\nMvaa 176,176,20\ns_add_u32 m0, s88, 4096\nMvaa 176,80,4\n" \
"D 13,64,s76,0\nMvaa 172,76,4\nTa 100,21,32768\nMvaa 172,172,20\nTa 102,21,33792\nTa 132,21,40960\nMvaa 168,168,20\nTa 134,21,41984\n" \
"Mvaa 168,72,4\nTa 104,22,32768\nMvaa 164,68,4\nD 13,64,s77,1024\nMvaa 164,164,20\nTa 106,22,33792\nTa 136,22,40960\nMvaa 160,160,20\n" \
"Ta 138,22,41984\nMvaa 160,64,4\nTa 108,23,32768\nMvaa 192,64,8\nTa 110,23,33792\nMvaa 192,160,24\nD 13,64,s78,2048\nMvaa 196,164,24\n" \
"Ta 140,23,40960\nTa 142,23,41984\nMvaa 196,68,8\nTa 112,24,32768\nMvaa 200,72,8\nTa 114,24,33792\nMvaa 200,168,24\nTa 144,24,40960\n" \
"Mvaa 204,172,24\nD 13,64,s79,3072\nMvaa 204,76,8\nTa 146,24,41984\nTa 116,25,32768\nMvaa 208,80,8\nTa 118,25,33792\nMvaa 208,176,24\n" \
"v_add_u32 v13, 128, v13\nTa 148,25,40960\nMvaa 212,180,24\ns_add_u32 m0, s32, 65536\nMvaa 212,84,8\nD 14,68,0,0\nMvaa 216,88,8\nTa 150,25,41984\n" \
"Ta 120,26,32768\nMvaa 216,184,24\nTa 122,26,33792\nMvaa 220,188,24\nTa 152,26,40960\nMvaa 220,92,8\nTa 154,26,41984\nMvaa 252,92,12\n" \
"D 14,68,s89,1024\nMvaa 252,188,28\nTa 124,27,32768\nTa 126,27,33792\nMvaa 248,184,28\nTa 156,27,40960\nMvaa 248,88,12\nTa 158,27,41984\n" \
"Mvaa 244,84,12\nRa 36,16,34816\nMvaa 244,180,28\nD 28,68,s90,2048\nMvaa 240,176,28\nRa 52,17,34816\nRa 40,16,36864\nMvaa 240,80,12\n" \
"Ra 56,17,36864\nMvaa 236,76,12\nRa 44,16,38912\nMvaa 236,172,28\nRa 60,17,38912\nMvaa 232,168,28\nD 28,68,s91,3072\nMvaa 232,72,12\n" \
"Mvaa 228,68,12\nMvaa 228,164,28\nv_add_u32 v14, s72, v14\nMvaa 224,160,28\nv_add_u32 v28, s72, v28\nMvaa 224,64,12\ns_waitcnt lgkmcnt(6)\nMvaa 128,96,32\n" \
"Mvaa 128,128,48\ns_add_u32 m0, s88, 32768\nMvaa 132,132,48\nD 13,64,0,0\nMvaa 132,100,32\nMvaa 136,104,32\nMvaa 136,136,48\nMvaa 140,140,48\n" \
"Mvaa 140,108,32\ns_waitcnt lgkmcnt(0)\nD 13,64,s73,1024\nMvaa 144,112,32\nMvaa 144,144,48\nMvaa 148,148,48\nbuffer_store_dwordx4 v[52:55], v0, s[60:63], s95 offen offset:1024 sc1\nMvaa 148,116,32\n" \
"Mvaa 152,120,32\nD 13,64,s74,2048\nMvaa 152,152,48\nMvaa 156,156,48\nMvaa 156,124,32\nMvaa 188,124,36\ns_waitcnt vmcnt(17)\ns_barrier\n" \
"Mvaa 188,156,52\nTa 64,20,16384\nTa 66,20,17408\nMvaa 184,152,52\nD 13,64,s75,3072\nMvaa 184,120,36\nTa 160,20,24576\nMvaa 180,116,36\n" \
"Ta 162,20,25600\nMvaa 180,148,52\nRa 0,18,0\nRa 16,19,0\nMvaa 176,144,52\ns_add_u32 m0, s88, 36864\nMvaa 176,112,36\nD 13,64,s76,0\n" \
"Mvaa 172,108,36\nTa 68,21,16384\nMvaa 172,140,52\nTa 70,21,17408\nTa 164,21,24576\nMvaa 168,136,52\nTa 166,21,25600\nMvaa 168,104,36\n" \
"Ta 72,22,16384\nMvaa 164,100,36\nD 13,64,s77,1024\nMvaa 164,132,52\nTa 74,22,17408\nTa 168,22,24576\nMvaa 160,128,52\nTa 170,22,25600\n" \
"Mvaa 160,96,36\nTa 76,23,16384\nMvaa 192,96,40\nTa 78,23,17408\nMvaa 192,128,56\nD 13,64,s78,2048\nMvaa 196,132,56\nTa 172,23,24576\n" \
"Ta 174,23,25600\nMvaa 196,100,40\nTa 80,24,16384\nMvaa 200,104,40\nTa 82,24,17408\nMvaa 200,136,56\nTa 176,24,24576\nMvaa 204,140,56\n" \
"D 13,64,s79,3072\nMvaa 204,108,40\nTa 178,24,25600\nTa 84,25,16384\nMvaa 208,112,40\nTa 86,25,17408\nMvaa 208,144,56\nv_add_u32 v13, 128, v13\n" \
"Ta 180,25,24576\nMvaa 212,148,56\ns_add_u32 m0, s32, 98304\nMvaa 212,116,40\nD 14,68,0,0\nMvaa 216,120,40\nTa 182,25,25600\nTa 88,26,16384\n" \
"Mvaa 216,152,56\nTa 90,26,17408\nMvaa 220,156,56\nTa 184,26,24576\nMvaa 220,124,40\nTa 186,26,25600\nMvaa 252,124,44\nD 14,68,s89,1024\n" \
"Mvaa 252,156,60\nTa 92,27,16384\nTa 94,27,17408\nMvaa 248,152,60\nTa 188,27,24576\nMvaa 248,120,44\nTa 190,27,25600\nMvaa 244,116,44\n" \
"Ra 4,18,2048\nMvaa 244,148,60\nD 28,68,s90,2048\nMvaa 240,144,60\nRa 20,19,2048\nRa 8,18,4096\nMvaa 240,112,44\nRa 24,19,4096\n" \
"Mvaa 236,108,44\nRa 12,18,6144\nMvaa 236,140,60\nRa 28,19,6144\nMvaa 232,136,60\nD 28,68,s91,3072\nMvaa 232,104,44\nMvaa 228,100,44\n" \
"Mvaa 228,132,60\nv_add_u32 v14, s72, v14\nMvaa 224,128,60\nv_add_u32 v28, s72, v28\nMvaa 224,96,44\ns_waitcnt lgkmcnt(6)\nMvaa 128,64,0\nMvaa 128,160,16\n" \
"s_add_u32 m0, s88, 131072\nMvaa 132,164,16\nD 13,64,0,0\nMvaa 132,68,0\nMvaa 136,72,0\nMvaa 136,168,16\nMvaa 140,172,16\nMvaa 140,76,0\n" \
"s_waitcnt lgkmcnt(0)\nD 13,64,s73,1024\nMvaa 144,80,0\nMvaa 144,176,16\nMvaa 148,180,16\nbuffer_store_dwordx4 v[56:59], v0, s[60:63], s95 offen offset:2048 sc1\nMvaa 148,84,0\nMvaa 152,88,0\n" \
"D 13,64,s74,2048\nMvaa 152,184,16\nMvaa 156,188,16\nMvaa 156,92,0\nMvaa 188,92,4\ns_waitcnt vmcnt(17)\ns_barrier\nMvaa 188,188,20\n" \
"Ta 96,20,0\nTa 98,20,1024\nMvaa 184,184,20\nD 13,64,s75,3072\nMvaa 184,88,4\nTa 128,20,8192\nMvaa 180,84,4\nTa 130,20,9216\n" \
"Mvaa 180,180,20\nRa 32,16,0\nRa 48,17,0\nMvaa 176,176,20\ns_add_u32 m0, s88, 135168\nMvaa 176,80,4\nD 13,64,s76,0\nMvaa 172,76,4\n" \
"Ta 100,21,0\nMvaa 172,172,20\nTa 102,21,1024\nTa 132,21,8192\nMvaa 168,168,20\nTa 134,21,9216\nMvaa 168,72,4\nTa 104,22,0\n" \
"Mvaa 164,68,4\nD 13,64,s77,1024\nMvaa 164,164,20\nTa 106,22,1024\nTa 136,22,8192\nMvaa 160,160,20\nTa 138,22,9216\nMvaa 160,64,4\n" \
"Ta 108,23,0\nMvaa 192,64,8\nTa 110,23,1024\nMvaa 192,160,24\nD 13,64,s78,2048\nMvaa 196,164,24\nTa 140,23,8192\nTa 142,23,9216\n" \
"Mvaa 196,68,8\nTa 112,24,0\nMvaa 200,72,8\nTa 114,24,1024\ns_branch 99f\n.p2align 12\nzp5:\ns_setpc_b64 s[98:99]\n" \
"99:\nMvaa 200,168,24\nTa 144,24,8192\nMvaa 204,172,24\nD 13,64,s79,3072\nMvaa 204,76,8\nTa 146,24,9216\nTa 116,25,0\n" \
"Mvaa 208,80,8\nTa 118,25,1024\nMvaa 208,176,24\nv_add_u32 v13, 128, v13\nTa 148,25,8192\nMvaa 212,180,24\ns_add_u32 m0, s32, 81920\nMvaa 212,84,8\n" \
"D 14,68,0,0\nMvaa 216,88,8\nTa 150,25,9216\nTa 120,26,0\nMvaa 216,184,24\nTa 122,26,1024\nMvaa 220,188,24\nTa 152,26,8192\n" \
"Mvaa 220,92,8\nTa 154,26,9216\nMvaa 252,92,12\nD 14,68,s89,1024\nMvaa 252,188,28\nTa 124,27,0\nTa 126,27,1024\nMvaa 248,184,28\n" \
"Ta 156,27,8192\nMvaa 248,88,12\nTa 158,27,9216\nMvaa 244,84,12\nRa 36,16,2048\nMvaa 244,180,28\nD 28,68,s90,2048\nMvaa 240,176,28\n" \
"Ra 52,17,2048\nRa 40,16,4096\nMvaa 240,80,12\nRa 56,17,4096\nMvaa 236,76,12\nRa 44,16,6144\nMvaa 236,172,28\nRa 60,17,6144\n" \
"Mvaa 232,168,28\nD 28,68,s91,3072\nMvaa 232,72,12\nMvaa 228,68,12\nMvaa 228,164,28\nv_add_u32 v14, s72, v14\nMvaa 224,160,28\nv_add_u32 v28, s72, v28\n" \
"Mvaa 224,64,12\ns_waitcnt lgkmcnt(6)\nMvaa 128,96,32\nMvaa 128,128,48\ns_add_u32 m0, s88, 0\nMvaa 132,132,48\nD 13,64,0,0\nMvaa 132,100,32\n" \
"Mvaa 136,104,32\nMvaa 136,136,48\nMvaa 140,140,48\nMvaa 140,108,32\ns_waitcnt lgkmcnt(0)\nD 13,64,s73,1024\nMvaa 144,112,32\nMvaa 144,144,48\n" \
"Mvaa 148,148,48\nbuffer_store_dwordx4 v[60:63], v0, s[60:63], s95 offen offset:3072 sc1\nMvaa 148,116,32\nMvaa 152,120,32\nD 13,64,s74,2048\nMvaa 152,152,48\nMvaa 156,156,48\nMvaa 156,124,32\n" \
"Mvaa 188,124,36\ns_waitcnt vmcnt(17)\ns_barrier\nMvaa 188,156,52\nTa 64,20,32768\nTa 66,20,33792\nMvaa 184,152,52\nD 13,64,s75,3072\n" \
"Mvaa 184,120,36\nTa 160,20,40960\nMvaa 180,116,36\nTa 162,20,41984\nMvaa 180,148,52\nRa 0,16,32768\nRa 16,17,32768\nMvaa 176,144,52\n" \
"s_add_u32 m0, s88, 4096\nMvaa 176,112,36\nD 13,64,s76,0\nMvaa 172,108,36\nTa 68,21,32768\nMvaa 172,140,52\nTa 70,21,33792\nTa 164,21,40960\n" \
"Mvaa 168,136,52\nTa 166,21,41984\nMvaa 168,104,36\nTa 72,22,32768\nMvaa 164,100,36\nD 13,64,s77,1024\nMvaa 164,132,52\nTa 74,22,33792\n" \
"Ta 168,22,40960\nMvaa 160,128,52\nTa 170,22,41984\nMvaa 160,96,36\nTa 76,23,32768\nMvaa 192,96,40\nTa 78,23,33792\nMvaa 192,128,56\n" \
"D 13,64,s78,2048\nMvaa 196,132,56\nTa 172,23,40960\nTa 174,23,41984\nMvaa 196,100,40\nTa 80,24,32768\nMvaa 200,104,40\nTa 82,24,33792\n" \
"Mvaa 200,136,56\nTa 176,24,40960\nMvaa 204,140,56\nD 13,64,s79,3072\nMvaa 204,108,40\nTa 178,24,41984\nTa 84,25,32768\nMvaa 208,112,40\n" \
"Ta 86,25,33792\nMvaa 208,144,56\nv_add_u32 v13, 128, v13\nTa 180,25,40960\nMvaa 212,148,56\ns_add_u32 m0, s32, 65536\nMvaa 212,116,40\nD 14,68,0,0\n" \
"Mvaa 216,120,40\nTa 182,25,41984\nTa 88,26,32768\nMvaa 216,152,56\nTa 90,26,33792\nMvaa 220,156,56\nTa 184,26,40960\nMvaa 220,124,40\n" \
"Ta 186,26,41984\nMvaa 252,124,44\nD 14,68,s89,1024\nMvaa 252,156,60\nTa 92,27,32768\nTa 94,27,33792\nMvaa 248,152,60\nTa 188,27,40960\n" \
"Mvaa 248,120,44\nTa 190,27,41984\nMvaa 244,116,44\nRa 4,16,34816\nMvaa 244,148,60\nD 28,68,s90,2048\nMvaa 240,144,60\nRa 20,17,34816\n" \
"Ra 8,16,36864\nMvaa 240,112,44\nRa 24,17,36864\nMvaa 236,108,44\nRa 12,16,38912\nMvaa 236,140,60\nRa 28,17,38912\nMvaa 232,136,60\n" \
"D 28,68,s91,3072\nMvaa 232,104,44\ns_add_u32 s95, s34, 8192\nMvaa 228,100,44\nbuffer_store_dwordx4 v[64:67], v0, s[60:63], s95 offen offset:0 sc1\nMvaa 228,132,60\nv_add_u32 v14, s72, v14\nMvaa 224,128,60\n" \
"v_add_u32 v28, s72, v28\nMvaa 224,96,44\ns_waitcnt lgkmcnt(6)\nMvaa 128,64,0\nMvaa 128,160,16\ns_add_u32 m0, s88, 32768\nMvaa 132,164,16\nD 13,64,0,0\n" \
"Mvaa 132,68,0\nMvaa 136,72,0\nMvaa 136,168,16\nMvaa 140,172,16\nMvaa 140,76,0\ns_waitcnt lgkmcnt(0)\nD 13,64,s73,1024\nMvaa 144,80,0\n" \
"Mvaa 144,176,16\nMvaa 148,180,16\nbuffer_store_dwordx4 v[68:71], v0, s[60:63], s95 offen offset:1024 sc1\nMvaa 148,84,0\nMvaa 152,88,0\nD 13,64,s74,2048\nMvaa 152,184,16\nMvaa 156,188,16\n" \
"Mvaa 156,92,0\nMvaa 188,92,4\ns_waitcnt vmcnt(18)\ns_barrier\nMvaa 188,188,20\nTa 96,20,16384\nTa 98,20,17408\nMvaa 184,184,20\n" \
"D 13,64,s75,3072\nMvaa 184,88,4\nTa 128,20,24576\nMvaa 180,84,4\nTa 130,20,25600\nMvaa 180,180,20\nRa 32,18,0\nRa 48,19,0\n" \
"Mvaa 176,176,20\ns_add_u32 m0, s88, 36864\nMvaa 176,80,4\nD 13,64,s76,0\nMvaa 172,76,4\nTa 100,21,16384\nMvaa 172,172,20\nTa 102,21,17408\n" \
"Ta 132,21,24576\nMvaa 168,168,20\nTa 134,21,25600\nMvaa 168,72,4\nTa 104,22,16384\nMvaa 164,68,4\nD 13,64,s77,1024\nMvaa 164,164,20\n" \
"Ta 106,22,17408\nTa 136,22,24576\nMvaa 160,160,20\nTa 138,22,25600\nMvaa 160,64,4\nTa 108,23,16384\nMvaa 192,64,8\nTa 110,23,17408\n" \
"Mvaa 192,160,24\nD 13,64,s78,2048\nMvaa 196,164,24\nTa 140,23,24576\nTa 142,23,25600\nMvaa 196,68,8\nTa 112,24,16384\nMvaa 200,72,8\n" \
"Ta 114,24,17408\nMvaa 200,168,24\nTa 144,24,24576\nMvaa 204,172,24\nD 13,64,s79,3072\nMvaa 204,76,8\nTa 146,24,25600\nTa 116,25,16384\n" \
"Mvaa 208,80,8\nTa 118,25,17408\nMvaa 208,176,24\nv_add_u32 v13, 128, v13\nTa 148,25,24576\nMvaa 212,180,24\ns_add_u32 m0, s32, 98304\nMvaa 212,84,8\n" \
"D 14,68,0,0\nMvaa 216,88,8\nTa 150,25,25600\nTa 120,26,16384\nMvaa 216,184,24\nTa 122,26,17408\nMvaa 220,188,24\nTa 152,26,24576\n" \
"Mvaa 220,92,8\nTa 154,26,25600\nMvaa 252,92,12\nD 14,68,s89,1024\nMvaa 252,188,28\nTa 124,27,16384\nTa 126,27,17408\nMvaa 248,184,28\n" \
"Ta 156,27,24576\nMvaa 248,88,12\nTa 158,27,25600\nMvaa 244,84,12\nRa 36,18,2048\nMvaa 244,180,28\nD 28,68,s90,2048\nMvaa 240,176,28\n" \
"Ra 52,19,2048\nRa 40,18,4096\nMvaa 240,80,12\nRa 56,19,4096\nMvaa 236,76,12\nRa 44,18,6144\nMvaa 236,172,28\nRa 60,19,6144\n" \
"Mvaa 232,168,28\nD 28,68,s91,3072\nMvaa 232,72,12\nMvaa 228,68,12\nMvaa 228,164,28\nv_add_u32 v14, s72, v14\nMvaa 224,160,28\nv_add_u32 v28, s72, v28\n" \
"Mvaa 224,64,12\ns_waitcnt lgkmcnt(6)\nMvaa 128,96,32\nMvaa 128,128,48\ns_add_u32 m0, s88, 131072\nMvaa 132,132,48\nD 13,64,0,0\nMvaa 132,100,32\n" \
"Mvaa 136,104,32\nMvaa 136,136,48\nMvaa 140,140,48\nMvaa 140,108,32\ns_waitcnt lgkmcnt(0)\nD 13,64,s73,1024\nMvaa 144,112,32\nMvaa 144,144,48\n" \
"Mvaa 148,148,48\nbuffer_store_dwordx4 v[72:75], v0, s[60:63], s95 offen offset:2048 sc1\nMvaa 148,116,32\nMvaa 152,120,32\nD 13,64,s74,2048\nMvaa 152,152,48\nMvaa 156,156,48\nMvaa 156,124,32\n" \
"Mvaa 188,124,36\ns_waitcnt vmcnt(18)\ns_barrier\nMvaa 188,156,52\nTa 64,20,0\nTa 66,20,1024\nMvaa 184,152,52\nD 13,64,s75,3072\n" \
"Mvaa 184,120,36\nTa 160,20,8192\nMvaa 180,116,36\nTa 162,20,9216\nMvaa 180,148,52\nRa 0,16,0\nRa 16,17,0\nMvaa 176,144,52\n" \
"s_add_u32 m0, s88, 135168\nMvaa 176,112,36\nD 13,64,s76,0\nMvaa 172,108,36\nTa 68,21,0\nMvaa 172,140,52\nTa 70,21,1024\nTa 164,21,8192\n" \
"Mvaa 168,136,52\nTa 166,21,9216\nMvaa 168,104,36\nTa 72,22,0\nMvaa 164,100,36\nD 13,64,s77,1024\nMvaa 164,132,52\nTa 74,22,1024\n" \
"Ta 168,22,8192\nMvaa 160,128,52\nTa 170,22,9216\nMvaa 160,96,36\nTa 76,23,0\nMvaa 192,96,40\nTa 78,23,1024\nMvaa 192,128,56\n" \
"D 13,64,s78,2048\nMvaa 196,132,56\nTa 172,23,8192\nTa 174,23,9216\nMvaa 196,100,40\nTa 80,24,0\nMvaa 200,104,40\nTa 82,24,1024\n" \
"Mvaa 200,136,56\nTa 176,24,8192\nMvaa 204,140,56\nD 13,64,s79,3072\nMvaa 204,108,40\nTa 178,24,9216\nTa 84,25,0\nMvaa 208,112,40\n" \
"Ta 86,25,1024\nMvaa 208,144,56\nv_add_u32 v13, 128, v13\nTa 180,25,8192\nMvaa 212,148,56\ns_add_u32 m0, s32, 81920\nMvaa 212,116,40\nD 14,68,0,0\n" \
"Mvaa 216,120,40\nTa 182,25,9216\nTa 88,26,0\nMvaa 216,152,56\nTa 90,26,1024\nMvaa 220,156,56\nTa 184,26,8192\nMvaa 220,124,40\n" \
"Ta 186,26,9216\nMvaa 252,124,44\nD 14,68,s89,1024\nMvaa 252,156,60\nTa 92,27,0\nTa 94,27,1024\nMvaa 248,152,60\nTa 188,27,8192\n" \
"Mvaa 248,120,44\nTa 190,27,9216\nMvaa 244,116,44\nRa 4,16,2048\nMvaa 244,148,60\nD 28,68,s90,2048\nMvaa 240,144,60\nRa 20,17,2048\n" \
"Ra 8,16,4096\nMvaa 240,112,44\nRa 24,17,4096\nMvaa 236,108,44\nRa 12,16,6144\nMvaa 236,140,60\nRa 28,17,6144\nMvaa 232,136,60\n" \
"D 28,68,s91,3072\nMvaa 232,104,44\nMvaa 228,100,44\nMvaa 228,132,60\nv_add_u32 v14, s72, v14\nMvaa 224,128,60\nv_add_u32 v28, s72, v28\nMvaa 224,96,44\n" \
"s_waitcnt lgkmcnt(6)\nMvaa 128,64,0\nMvaa 128,160,16\ns_add_u32 m0, s88, 0\nMvaa 132,164,16\nD 13,64,0,0\nMvaa 132,68,0\nMvaa 136,72,0\n" \
"Mvaa 136,168,16\nMvaa 140,172,16\nMvaa 140,76,0\ns_waitcnt lgkmcnt(0)\nD 13,64,s73,1024\nMvaa 144,80,0\nMvaa 144,176,16\nMvaa 148,180,16\n" \
"buffer_store_dwordx4 v[76:79], v0, s[60:63], s95 offen offset:3072 sc1\nMvaa 148,84,0\nMvaa 152,88,0\nD 13,64,s74,2048\nMvaa 152,184,16\nMvaa 156,188,16\nMvaa 156,92,0\nMvaa 188,92,4\n" \
"s_waitcnt vmcnt(17)\ns_barrier\nMvaa 188,188,20\nTa 96,20,32768\nTa 98,20,33792\nMvaa 184,184,20\nD 13,64,s75,3072\nMvaa 184,88,4\n" \
"Ta 128,20,40960\nMvaa 180,84,4\nTa 130,20,41984\nMvaa 180,180,20\nRa 32,16,32768\nRa 48,17,32768\nMvaa 176,176,20\ns_add_u32 m0, s88, 4096\n" \
"Mvaa 176,80,4\nD 13,64,s76,0\nMvaa 172,76,4\nTa 100,21,32768\nMvaa 172,172,20\nTa 102,21,33792\nTa 132,21,40960\nMvaa 168,168,20\n" \
"Ta 134,21,41984\nMvaa 168,72,4\nTa 104,22,32768\nMvaa 164,68,4\nD 13,64,s77,1024\nMvaa 164,164,20\nTa 106,22,33792\nTa 136,22,40960\n" \
"Mvaa 160,160,20\nTa 138,22,41984\nMvaa 160,64,4\nTa 108,23,32768\nMvaa 192,64,8\nTa 110,23,33792\nMvaa 192,160,24\nD 13,64,s78,2048\n" \
"Mvaa 196,164,24\nTa 140,23,40960\nTa 142,23,41984\nMvaa 196,68,8\nTa 112,24,32768\nMvaa 200,72,8\nTa 114,24,33792\nMvaa 200,168,24\n" \
"Ta 144,24,40960\nMvaa 204,172,24\nD 13,64,s79,3072\nMvaa 204,76,8\nTa 146,24,41984\nTa 116,25,32768\nMvaa 208,80,8\nTa 118,25,33792\n" \
"Mvaa 208,176,24\nv_add_u32 v13, 128, v13\nTa 148,25,40960\nMvaa 212,180,24\ns_add_u32 m0, s32, 65536\ns_branch 99f\n.p2align 12\nzp6:\n" \
"s_setpc_b64 s[98:99]\n99:\nMvaa 212,84,8\nD 14,68,0,0\nMvaa 216,88,8\nTa 150,25,41984\nTa 120,26,32768\nMvaa 216,184,24\n" \
"Ta 122,26,33792\nMvaa 220,188,24\nTa 152,26,40960\nMvaa 220,92,8\nTa 154,26,41984\nMvaa 252,92,12\nD 14,68,s89,1024\nMvaa 252,188,28\n" \
"Ta 124,27,32768\nTa 126,27,33792\nMvaa 248,184,28\nTa 156,27,40960\nMvaa 248,88,12\nTa 158,27,41984\nMvaa 244,84,12\nRa 36,16,34816\n" \
"Mvaa 244,180,28\nD 28,68,s90,2048\nMvaa 240,176,28\nRa 52,17,34816\nRa 40,16,36864\nMvaa 240,80,12\nRa 56,17,36864\nMvaa 236,76,12\n" \
"Ra 44,16,38912\nMvaa 236,172,28\nRa 60,17,38912\nMvaa 232,168,28\nD 28,68,s91,3072\nMvaa 232,72,12\nMvaa 228,68,12\nMvaa 228,164,28\n" \
"v_add_u32 v14, s72, v14\nMvaa 224,160,28\nv_add_u32 v28, s72, v28\nMvaa 224,64,12\ns_waitcnt lgkmcnt(6)\nMvaa 128,96,32\nMvaa 128,128,48\ns_add_u32 m0, s88, 32768\n" \
"Mvaa 132,132,48\nD 13,64,0,0\nMvaa 132,100,32\nMvaa 136,104,32\nMvaa 136,136,48\nMvaa 140,140,48\nMvaa 140,108,32\ns_waitcnt lgkmcnt(0)\n" \
"D 13,64,s73,1024\nMvaa 144,112,32\nMvaa 144,144,48\ns_add_u32 s95, s34, 12288\nMvaa 148,148,48\nbuffer_store_dwordx4 v[80:83], v0, s[60:63], s95 offen offset:0 sc1\nMvaa 148,116,32\nMvaa 152,120,32\n" \
"D 13,64,s74,2048\nMvaa 152,152,48\nMvaa 156,156,48\nMvaa 156,124,32\nMvaa 188,124,36\ns_waitcnt vmcnt(17)\ns_barrier\nMvaa 188,156,52\n" \
"Ta 64,20,16384\nTa 66,20,17408\nMvaa 184,152,52\nD 13,64,s75,3072\nMvaa 184,120,36\nTa 160,20,24576\nMvaa 180,116,36\nTa 162,20,25600\n" \
"Mvaa 180,148,52\nRa 0,18,0\nRa 16,19,0\nMvaa 176,144,52\ns_add_u32 m0, s88, 36864\nMvaa 176,112,36\nD 13,64,s76,0\nMvaa 172,108,36\n" \
"Ta 68,21,16384\nMvaa 172,140,52\nTa 70,21,17408\nTa 164,21,24576\nMvaa 168,136,52\nTa 166,21,25600\nMvaa 168,104,36\nTa 72,22,16384\n" \
"Mvaa 164,100,36\nD 13,64,s77,1024\nMvaa 164,132,52\nTa 74,22,17408\nTa 168,22,24576\nMvaa 160,128,52\nTa 170,22,25600\nMvaa 160,96,36\n" \
"Ta 76,23,16384\nMvaa 192,96,40\nTa 78,23,17408\nMvaa 192,128,56\nD 13,64,s78,2048\nMvaa 196,132,56\nTa 172,23,24576\nTa 174,23,25600\n" \
"Mvaa 196,100,40\nTa 80,24,16384\nMvaa 200,104,40\nTa 82,24,17408\nMvaa 200,136,56\nTa 176,24,24576\nMvaa 204,140,56\nD 13,64,s79,3072\n" \
"Mvaa 204,108,40\nTa 178,24,25600\nTa 84,25,16384\nMvaa 208,112,40\nTa 86,25,17408\nMvaa 208,144,56\nv_add_u32 v13, 128, v13\nTa 180,25,24576\n" \
"Mvaa 212,148,56\ns_add_u32 m0, s32, 98304\nMvaa 212,116,40\nD 14,68,0,0\nMvaa 216,120,40\nTa 182,25,25600\nTa 88,26,16384\nMvaa 216,152,56\n" \
"Ta 90,26,17408\nMvaa 220,156,56\nTa 184,26,24576\nMvaa 220,124,40\nTa 186,26,25600\nMvaa 252,124,44\nD 14,68,s89,1024\nMvaa 252,156,60\n" \
"Ta 92,27,16384\nTa 94,27,17408\nMvaa 248,152,60\nTa 188,27,24576\nMvaa 248,120,44\nTa 190,27,25600\nMvaa 244,116,44\nRa 4,18,2048\n" \
"Mvaa 244,148,60\nD 28,68,s90,2048\nMvaa 240,144,60\nRa 20,19,2048\nRa 8,18,4096\nMvaa 240,112,44\nRa 24,19,4096\nMvaa 236,108,44\n" \
"Ra 12,18,6144\nMvaa 236,140,60\nRa 28,19,6144\nMvaa 232,136,60\nD 28,68,s91,3072\nMvaa 232,104,44\nMvaa 228,100,44\nMvaa 228,132,60\n" \
"v_add_u32 v14, s72, v14\nMvaa 224,128,60\nv_add_u32 v28, s72, v28\nMvaa 224,96,44\ns_waitcnt lgkmcnt(6)\nMvaa 128,64,0\nMvaa 128,160,16\ns_add_u32 m0, s88, 131072\n" \
"Mvaa 132,164,16\nD 13,64,0,0\nMvaa 132,68,0\nMvaa 136,72,0\nMvaa 136,168,16\nMvaa 140,172,16\nMvaa 140,76,0\ns_waitcnt lgkmcnt(0)\n" \
"D 13,64,s73,1024\nMvaa 144,80,0\nMvaa 144,176,16\nMvaa 148,180,16\nbuffer_store_dwordx4 v[84:87], v0, s[60:63], s95 offen offset:1024 sc1\nMvaa 148,84,0\nMvaa 152,88,0\nD 13,64,s74,2048\n" \
"Mvaa 152,184,16\nMvaa 156,188,16\nMvaa 156,92,0\nMvaa 188,92,4\ns_waitcnt vmcnt(17)\ns_barrier\nMvaa 188,188,20\nTa 96,20,0\n" \
"Ta 98,20,1024\nMvaa 184,184,20\nD 13,64,s75,3072\nMvaa 184,88,4\nTa 128,20,8192\nMvaa 180,84,4\nTa 130,20,9216\nMvaa 180,180,20\n" \
"Ra 32,16,0\nRa 48,17,0\nMvaa 176,176,20\ns_add_u32 m0, s88, 135168\nMvaa 176,80,4\nD 13,64,s76,0\nMvaa 172,76,4\nTa 100,21,0\n" \
"Mvaa 172,172,20\nTa 102,21,1024\nTa 132,21,8192\nMvaa 168,168,20\nTa 134,21,9216\nMvaa 168,72,4\nTa 104,22,0\nMvaa 164,68,4\n" \
"D 13,64,s77,1024\nMvaa 164,164,20\nTa 106,22,1024\nTa 136,22,8192\nMvaa 160,160,20\nTa 138,22,9216\nMvaa 160,64,4\nTa 108,23,0\n" \
"Mvaa 192,64,8\nTa 110,23,1024\nMvaa 192,160,24\nD 13,64,s78,2048\nMvaa 196,164,24\nTa 140,23,8192\nTa 142,23,9216\nMvaa 196,68,8\n" \
"Ta 112,24,0\nMvaa 200,72,8\nTa 114,24,1024\nMvaa 200,168,24\nTa 144,24,8192\nMvaa 204,172,24\nD 13,64,s79,3072\nMvaa 204,76,8\n" \
"Ta 146,24,9216\nTa 116,25,0\nMvaa 208,80,8\nTa 118,25,1024\nMvaa 208,176,24\nv_add_u32 v13, 128, v13\nTa 148,25,8192\nMvaa 212,180,24\n" \
"s_add_u32 m0, s32, 81920\nMvaa 212,84,8\nD 14,68,0,0\nMvaa 216,88,8\nTa 150,25,9216\nTa 120,26,0\nMvaa 216,184,24\nTa 122,26,1024\n" \
"Mvaa 220,188,24\nTa 152,26,8192\nMvaa 220,92,8\nTa 154,26,9216\nMvaa 252,92,12\nD 14,68,s89,1024\nMvaa 252,188,28\nTa 124,27,0\n" \
"Ta 126,27,1024\nMvaa 248,184,28\nTa 156,27,8192\nMvaa 248,88,12\nTa 158,27,9216\nMvaa 244,84,12\nRa 36,16,2048\nMvaa 244,180,28\n" \
"D 28,68,s90,2048\nMvaa 240,176,28\nRa 52,17,2048\nRa 40,16,4096\nMvaa 240,80,12\nRa 56,17,4096\nMvaa 236,76,12\nRa 44,16,6144\n" \
"Mvaa 236,172,28\nRa 60,17,6144\nMvaa 232,168,28\nD 28,68,s91,3072\nMvaa 232,72,12\nMvaa 228,68,12\nMvaa 228,164,28\nv_add_u32 v14, s72, v14\n" \
"Mvaa 224,160,28\nv_add_u32 v28, s72, v28\nMvaa 224,64,12\ns_waitcnt lgkmcnt(6)\nMvaa 128,96,32\nMvaa 128,128,48\nbuffer_load_dwordx2 v[10:11], v1, s[60:63], 0 offen offset:0 sc1\nMvaa 132,132,48\n" \
"v_subrev_u32 v9, s32, v0\nv_add_u32 v8, 0x1004200, v0\nMvaa 132,100,32\ns_add_u32 s77, s38, s32\nv_mov_b32 v12, 0xc2200000\nMvaa 136,104,32\nv_mov_b32 v13, 0xc2200000\nv_mov_b32 v14, 0xc2200000\n" \
"Mvaa 136,136,48\nv_mov_b32 v15, 0xc2200000\nv_bfe_u32 v29, v0, 4, 4\nMvaa 140,140,48\nv_lshrrev_b32 v28, 8, v0\ns_lshr_b32 s96, s32, 8\nMvaa 140,108,32\ns_waitcnt lgkmcnt(0)\n" \
"v_lshlrev_b32 v6, 2, v28\nMvaa 144,112,32\nv_add_u32 v30, s96, v29\nv_sub_u32 v30, v30, v6\nMvaa 144,144,48\nv_mov_b32 v6, 0xff800000\nv_cmp_gt_i32 s[90:91], 0, v30\nMvaa 148,148,48\n" \
"buffer_store_dwordx4 v[88:91], v0, s[60:63], s95 offen offset:2048 sc1\nMvaa 148,116,32\nv_cmp_gt_i32 s[92:93], 1, v30\nv_cmp_gt_i32 s[74:75], 2, v30\nMvaa 152,120,32\nv_cmp_gt_i32 s[78:79], 3, v30\nv_cndmask_b32 v112, v12, v6, s[90:91]\nMvaa 152,152,48\n" \
"v_cndmask_b32 v113, v12, v6, s[92:93]\nv_cndmask_b32 v114, v12, v6, s[74:75]\nMvaa 156,156,48\nv_cndmask_b32 v115, v12, v6, s[78:79]\nv_cmp_gt_i32 s[90:91], 16, v30\nMvaa 156,124,32\nv_cmp_gt_i32 s[92:93], 17, v30\nv_cmp_gt_i32 s[74:75], 18, v30\n" \
"Mvaa 188,124,36\ns_waitcnt vmcnt(15)\ns_barrier\nMvaa 188,156,52\nTa 64,20,32768\nv_cmp_gt_i32 s[78:79], 19, v30\nMvaa 184,152,52\nTa 66,20,33792\n" \
"v_cndmask_b32 v116, v12, v6, s[90:91]\nMvaa 184,120,36\nTa 160,20,40960\nv_cndmask_b32 v117, v12, v6, s[92:93]\nMvaa 180,116,36\nTa 162,20,41984\nv_cndmask_b32 v118, v12, v6, s[74:75]\nMvaa 180,148,52\n" \
"Ra 0,16,32768\nv_cndmask_b32 v119, v12, v6, s[78:79]\nMvaa 176,144,52\nRa 16,17,32768\nv_cmp_gt_i32 s[90:91], 32, v30\nMvaa 176,112,36\nTa 68,21,32768\nv_cmp_gt_i32 s[92:93], 33, v30\n" \
"Mvaa 172,108,36\nTa 70,21,33792\nv_cmp_gt_i32 s[74:75], 34, v30\nMvaa 172,140,52\nTa 164,21,40960\nv_cmp_gt_i32 s[78:79], 35, v30\nMvaa 168,136,52\nTa 166,21,41984\n" \
"v_cndmask_b32 v120, v12, v6, s[90:91]\nMvaa 168,104,36\nTa 72,22,32768\nv_cndmask_b32 v121, v12, v6, s[92:93]\nMvaa 164,100,36\nTa 74,22,33792\nv_cndmask_b32 v122, v12, v6, s[74:75]\nMvaa 164,132,52\n" \
"Ta 168,22,40960\nv_cndmask_b32 v123, v12, v6, s[78:79]\nMvaa 160,128,52\nTa 170,22,41984\nv_cmp_gt_i32 s[90:91], 48, v30\nMvaa 160,96,36\nTa 76,23,32768\nv_cmp_gt_i32 s[92:93], 49, v30\n" \
"Mvaa 192,96,40\nTa 78,23,33792\nv_cmp_gt_i32 s[74:75], 50, v30\nMvaa 192,128,56\nTa 172,23,40960\nv_cmp_gt_i32 s[78:79], 51, v30\nMvaa 196,132,56\nTa 174,23,41984\n" \
"v_cndmask_b32 v124, v12, v6, s[90:91]\nMvaa 196,100,40\nTa 80,24,32768\nv_cndmask_b32 v125, v12, v6, s[92:93]\nMvaa 200,104,40\nTa 82,24,33792\nv_cndmask_b32 v126, v12, v6, s[74:75]\nMvaa 200,136,56\n" \
"v_cndmask_b32 v127, v12, v6, s[78:79]\nMvaa 204,140,56\nTa 176,24,40960\nMvaa 204,108,40\nTa 178,24,41984\nMvaa 208,112,40\nTa 84,25,32768\nMvaa 208,144,56\n" \
"Ta 86,25,33792\nMvaa 212,148,56\nTa 180,25,40960\nMvaa 212,116,40\nTa 182,25,41984\nMvaa 216,120,40\nTa 88,26,32768\nMvaa 216,152,56\n" \
"Ta 90,26,33792\nMvaa 220,156,56\nTa 184,26,40960\nMvaa 220,124,40\nTa 186,26,41984\nMvaa 252,124,44\nTa 92,27,32768\nMvaa 252,156,60\n" \
"Ta 94,27,33792\nMvaa 248,152,60\nTa 188,27,40960\nMvaa 248,120,44\nTa 190,27,41984\nMvaa 244,116,44\nRa 4,16,34816\nMvaa 244,148,60\n" \
"Ra 20,17,34816\nMvaa 240,144,60\nRa 8,16,36864\nMvaa 240,112,44\nRa 24,17,36864\nMvaa 236,108,44\nRa 12,16,38912\nMvaa 236,140,60\n" \
"Ra 28,17,38912\nMvaa 232,136,60\nMvaa 232,104,44\nMvaa 228,100,44\nMvaa 228,132,60\nMvaa 224,128,60\nMvaa 224,96,44\ns_waitcnt lgkmcnt(6)\n" \
"Mvaa 128,64,0\nv_and_b32 v30, 3, v29\nv_bfe_u32 v6, v29, 1, 1\nMvaa 128,160,16\nv_lshrrev_b32 v28, 1, v28\nv_cmp_eq_u32 s[90:91], 2, v30\nMvaa 132,164,16\nv_cmp_eq_u32 s[92:93], v6, v28\n" \
"v_and_b32 v30, 1, v29\nMvaa 132,68,0\nv_mov_b32 v6, 0x3f803f80\nv_cmp_eq_u32 s[78:79], 1, v30\nMvaa 136,72,0\nv_mov_b32 v7, 0x4444\nv_mov_b32 v30, 0xeeee\nMvaa 136,168,16\n" \
"v_cndmask_b32 v108, 0, v6, s[90:91]\nv_cndmask_b32 v109, 0, v6, s[90:91]\nMvaa 140,172,16\nv_cndmask_b32 v110, 0, v6, s[90:91]\nv_cndmask_b32 v111, 0, v6, s[90:91]\nMvaa 140,76,0\ns_waitcnt lgkmcnt(0)\nv_cndmask_b32 v104, 0, v6, s[92:93]\n" \
"Mvaa 144,80,0\nv_cndmask_b32 v105, 0, v6, s[92:93]\nv_cndmask_b32 v106, 0, v6, s[92:93]\nMvaa 144,176,16\nv_cndmask_b32 v107, 0, v6, s[92:93]\nv_cndmask_b32 v7, v7, v30, s[78:79]\nMvaa 148,180,16\nbuffer_store_dwordx4 v[92:95], v0, s[60:63], s95 offen offset:3072 sc1\n" \
"Mvaa 148,84,0\nv_mov_b32 v96, 0\nv_mov_b32 v97, 0\nMvaa 152,88,0\nv_mov_b32 v98, 0\nv_mov_b32 v99, 0\nMvaa 152,184,16\nv_mov_b32 v100, 0\n" \
"v_mov_b32 v101, 0\nMvaa 156,188,16\nv_mov_b32 v102, 0\nv_mov_b32 v103, 0\nMvaa 156,92,0\nv_mov_b32 v5, v0\ns_mov_b32 s73, s32\nMvaa 188,92,4\n" \
"s_waitcnt vmcnt(2)\ns_barrier\nv_cmp_ne_u64 vcc, v[10:11], v[2:3]\ns_cbranch_vccnz zs101\nzr101:\nMvaa 188,188,20\ns_mov_b32 m0, s32\nTa 96,20,16384\n" \
"Mvaa 184,184,20\nDS 0,60,s77,0\nMvaa 184,88,4\nTa 98,20,17408\nTa 128,20,24576\nMvaa 180,84,4\nTa 130,20,25600\ns_add_u32 s74, s32, 0x4000\n" \
"Mvaa 180,180,20\nRa 32,18,0\ns_add_u32 s75, s32, 0x8000\nMvaa 176,176,20\nDS 0,60,s77,1024\nMvaa 176,80,4\nRa 48,19,0\nTa 100,21,16384\n" \
"Mvaa 172,76,4\nTa 102,21,17408\ns_mov_b32 s97, 0x3e0293ee\nMvaa 172,172,20\nTa 132,21,24576\ns_mov_b32 s89, 31744\nMvaa 168,168,20\nDS 0,60,s77,2048\n" \
"s_branch 99f\n.p2align 12\nzp7:\ns_setpc_b64 s[98:99]\n99:\nMvaa 168,72,4\nTa 134,21,25600\nTa 104,22,16384\n" \
"Mvaa 164,68,4\nTa 106,22,17408\ns_mov_b32 s90, 6144\nMvaa 164,164,20\nTa 136,22,24576\nTa 138,22,25600\nMvaa 160,160,20\nDS 0,60,s77,3072\n" \
"Mvaa 160,64,4\nTa 108,23,16384\ns_mov_b32 s91, 37888\nMvaa 192,64,8\nTa 110,23,17408\ns_mov_b32 s92, 0x200000\nMvaa 192,160,24\ns_add_u32 m0, s32, 16384\n" \
"s_add_u32 s77, s77, 0x4000\nMvaa 196,164,24\nDS 0,60,s77,0\nMvaa 196,68,8\nTa 140,23,24576\nTa 142,23,25600\nMvaa 200,72,8\nTa 112,24,16384\n" \
"s_mov_b32 s78, s11\nMvaa 200,168,24\nTa 114,24,17408\ns_add_u32 s84, s32, 98304\nMvaa 204,172,24\nDS 0,60,s77,1024\nMvaa 204,76,8\nTa 144,24,24576\n" \
"Ta 146,24,25600\nMvaa 208,80,8\nTa 116,25,16384\ns_mov_b32 s85, 8192\nMvaa 208,176,24\nTa 118,25,17408\nTa 148,25,24576\nMvaa 212,180,24\n" \
"DS 0,60,s77,2048\nMvaa 212,84,8\nTa 150,25,25600\ns_mov_b32 s86, 16384\nMvaa 216,88,8\nTa 120,26,16384\ns_mov_b32 s87, 24576\nMvaa 216,184,24\n" \
"Ta 122,26,17408\nTa 152,26,24576\nMvaa 220,188,24\nDS 0,60,s77,3072\nMvaa 220,92,8\nTa 154,26,25600\ns_xor_b32 s82, s11, s10\nMvaa 252,92,12\n" \
"Ta 124,27,16384\nTa 126,27,17408\nMvaa 252,188,28\ns_add_u32 m0, s32, 49152\ns_lshl_b32 s83, s82, 16\nMvaa 248,184,28\nDS 8,60,s77,0\nMvaa 248,88,12\n" \
"Ta 156,27,24576\nTa 158,27,25600\nMvaa 244,84,12\nRa 36,18,2048\ns_add_u32 s83, s83, s18\nMvaa 244,180,28\nRa 52,19,2048\nMvaa 240,176,28\n" \
"DS 8,60,s77,1024\nMvaa 240,80,12\nRa 40,18,4096\nRa 56,19,4096\nMvaa 236,76,12\nRa 44,18,6144\nMvaa 236,172,28\nRa 60,19,6144\n" \
"Mvaa 232,168,28\nDS 8,60,s77,2048\nMvaa 232,72,12\nMvaa 228,68,12\nMvaa 228,164,28\nMvaa 224,160,28\nDS 8,60,s77,3072\nMvaa 224,64,12\n" \
"s_waitcnt lgkmcnt(6)\nMvaa 128,96,32\ns_add_u32 m0, s32, 32768\ns_add_u32 s77, s77, 0x4000\nMvaa 128,128,48\nDS 0,60,s77,0\nMvaa 132,132,48\nv_lshrrev_b32 v17, 8, v0\n" \
"v_and_b32 v16, 0xf0, v0\nMvaa 132,100,32\nDS 0,60,s77,1024\nMvaa 136,104,32\nv_lshlrev_b32 v18, 5, v17\nv_xor_b32 v16, v16, v18\nMvaa 136,136,48\nDS 0,60,s77,2048\n" \
"Mvaa 140,140,48\nv_lshl_add_u32 v16, v17, 11, v16\nv_add_u32 v16, s20, v16\nMvaa 140,108,32\ns_waitcnt lgkmcnt(0)\nDS 0,60,s77,3072\nMvaa 144,112,32\ns_add_u32 m0, s32, 65536\n" \
"s_xor_b32 s82, s78, s10\nMvaa 144,144,48\nDS 8,60,s77,0\nMvaa 148,148,48\ns_and_b32 s80, s82, 31\ns_lshl_b32 s80, s80, 18\nMvaa 148,116,32\nDS 8,60,s77,1024\n" \
"Mvaa 152,120,32\ns_cmp_lt_u32 s78, 32\ns_cselect_b64 s[68:69], s[14:15], s[12:13]\nMvaa 152,152,48\nDS 8,60,s77,2048\nMvaa 156,156,48\ns_cselect_b32 s70, 0x800000, s92\nv_add_u32 v18, s80, v16\n" \
"Mvaa 156,124,32\nDS 8,60,s77,3072\nv_lshlrev_b32 v29, 2, v0\nMvaa 160,96,36\ns_add_u32 s77, s77, 0x4000\nv_xor_b32 v19, 128, v18\nMvaa 160,128,52\nMvaa 164,132,52\n" \
"Mvaa 164,100,36\nbuffer_load_dword v30, v29, s[64:67], s46 offen sc1\nMvaa 168,104,36\nMvaa 168,136,52\nv_mul_f32 v128, s97, v128\nv_mul_f32 v129, s97, v129\nv_mul_f32 v130, s97, v130\nMvaa 172,140,52\n" \
"v_mul_f32 v131, s97, v131\nv_mul_f32 v132, s97, v132\nv_mul_f32 v133, s97, v133\nMvaa 172,108,36\nv_mul_f32 v134, s97, v134\nv_mul_f32 v135, s97, v135\nC 32,128,129\nMvaa 176,112,36\n" \
"C 33,130,131\nv_mul_f32 v136, s97, v136\nv_mul_f32 v137, s97, v137\nMvaa 176,144,52\nv_mul_f32 v138, s97, v138\nv_mul_f32 v139, s97, v139\nC 34,132,133\nMvaa 180,148,52\n" \
"C 35,134,135\nv_mul_f32 v140, s97, v140\nv_mul_f32 v141, s97, v141\nMvaa 180,116,36\nv_mul_f32 v142, s97, v142\nv_mul_f32 v143, s97, v143\nC 36,136,137\nMvaa 184,120,36\n" \
"C 37,138,139\nv_mul_f32 v144, s97, v144\nv_mul_f32 v145, s97, v145\nMvaa 184,152,52\nv_mul_f32 v146, s97, v146\nv_mul_f32 v147, s97, v147\nC 38,140,141\nMvaa 188,156,52\n" \
"C 39,142,143\nv_mul_f32 v148, s97, v148\nv_mul_f32 v149, s97, v149\nMvaa 188,124,36\nv_mul_f32 v150, s97, v150\nv_mul_f32 v151, s97, v151\nC 40,144,145\nMvaa 192,96,40\n" \
"C 41,146,147\nv_mul_f32 v152, s97, v152\nv_mul_f32 v153, s97, v153\nMvaa 192,128,56\nv_mul_f32 v154, s97, v154\nv_mul_f32 v155, s97, v155\nC 42,148,149\nMvaa 196,132,56\n" \
"C 43,150,151\nv_mul_f32 v156, s97, v156\nv_mul_f32 v157, s97, v157\nMvaa 196,100,40\nv_mul_f32 v158, s97, v158\nv_mul_f32 v159, s97, v159\nC 44,152,153\nMvaa 200,104,40\n" \
"C 45,154,155\nC 46,156,157\nC 47,158,159\nMvaa 200,136,56\nv_mul_f32 v160, s97, v160\nv_mul_f32 v161, s97, v161\nv_mul_f32 v162, s97, v162\nMvaa 204,140,56\n" \
"v_mul_f32 v163, s97, v163\nv_mul_f32 v164, s97, v164\nv_mul_f32 v165, s97, v165\nMvaa 204,108,40\nv_mul_f32 v166, s97, v166\nv_mul_f32 v167, s97, v167\nC 48,160,161\nMvaa 208,112,40\n" \
"s_waitcnt vmcnt(17)\ns_barrier\nC 49,162,163\nMvaa 208,144,56\nRa 160,5,0\nv_mul_f32 v168, s97, v168\nv_mul_f32 v169, s97, v169\nMvaa 212,148,56\n" \
"v_mul_f32 v170, s97, v170\nv_mul_f32 v171, s97, v171\nC 50,164,165\nMvaa 212,116,40\nRa 164,5,1024\nC 51,166,167\nv_mul_f32 v172, s97, v172\nMvaa 216,120,40\n" \
"v_mul_f32 v173, s97, v173\nv_mul_f32 v174, s97, v174\nv_mul_f32 v175, s97, v175\nMvaa 216,152,56\nRa 168,5,2048\nC 52,168,169\nC 53,170,171\nMvaa 220,156,56\n" \
"v_mul_f32 v176, s97, v176\nv_mul_f32 v177, s97, v177\nv_mul_f32 v178, s97, v178\nMvaa 220,124,40\nRa 172,5,3072\nv_mul_f32 v179, s97, v179\nC 54,172,173\nMvaa 224,96,44\n" \
"C 55,174,175\nv_mul_f32 v180, s97, v180\nv_mul_f32 v181, s97, v181\nMvaa 224,128,60\nRa 176,5,4096\nv_mul_f32 v182, s97, v182\nv_mul_f32 v183, s97, v183\nMvaa 228,132,60\n" \
"C 56,176,177\nC 57,178,179\nv_mul_f32 v184, s97, v184\nMvaa 228,100,44\nRa 180,5,5120\nv_mul_f32 v185, s97, v185\nv_mul_f32 v186, s97, v186\nMvaa 232,104,44\n" \
"v_mul_f32 v187, s97, v187\nC 58,180,181\nC 59,182,183\nMvaa 232,136,60\nRa 184,5,6144\nv_mul_f32 v188, s97, v188\nv_mul_f32 v189, s97, v189\nMvaa 236,140,60\n" \
"v_mul_f32 v190, s97, v190\nv_mul_f32 v191, s97, v191\nC 60,184,185\nMvaa 236,108,44\nRa 188,5,7168\nC 61,186,187\nC 62,188,189\nMvaa 240,112,44\n" \
"C 63,190,191\nv_mul_f32 v192, s97, v192\nv_mul_f32 v193, s97, v193\nMvaa 240,144,60\nv_mul_f32 v194, s97, v194\nv_mul_f32 v195, s97, v195\nv_mul_f32 v196, s97, v196\nMvaa 244,148,60\n" \
"v_mul_f32 v197, s97, v197\nv_mul_f32 v198, s97, v198\nv_mul_f32 v199, s97, v199\nMvaa 244,116,44\nC 64,192,193\nC 65,194,195\nv_mul_f32 v200, s97, v200\nMvaa 248,120,44\n" \
"s_bitcmp1_b32 s33, 2\nv_mul_f32 v201, s97, v201\nv_mul_f32 v202, s97, v202\nMvaa 248,152,60\nv_mul_f32 v203, s97, v203\nC 66,196,197\nC 67,198,199\nMvaa 252,156,60\n" \
"Mvaa 252,124,44\ns_waitcnt lgkmcnt(0)\nQvav 128,160,32,12\nRa 128,5,8192\nv_mul_f32 v204, s97, v204\nv_mul_f32 v205, s97, v205\nMvav 128,164,36\nv_mul_f32 v206, s97, v206\n" \
"v_mul_f32 v207, s97, v207\nC 68,200,201\nMvav 128,168,40\nRa 132,5,9216\nC 69,202,203\nv_mul_f32 v208, s97, v208\nMvav 128,172,44\nv_mul_f32 v209, s97, v209\n" \
"v_mul_f32 v210, s97, v210\nv_mul_f32 v211, s97, v211\nQvav 132,176,32,12\nRa 136,5,10240\nC 70,204,205\nC 71,206,207\nMvav 132,180,36\nv_mul_f32 v212, s97, v212\n" \
"v_mul_f32 v213, s97, v213\nv_mul_f32 v214, s97, v214\nMvav 132,184,40\nRa 140,5,11264\nv_mul_f32 v215, s97, v215\nC 72,208,209\nMvav 132,188,44\nC 73,210,211\n" \
"v_mul_f32 v216, s97, v216\nv_mul_f32 v217, s97, v217\nQvav 136,160,48,12\nRa 144,5,12288\nv_mul_f32 v218, s97, v218\nv_mul_f32 v219, s97, v219\nMvav 136,164,52\nC 74,212,213\n" \
"C 75,214,215\nv_mul_f32 v220, s97, v220\nMvav 136,168,56\nRa 148,5,13312\nv_mul_f32 v221, s97, v221\nv_mul_f32 v222, s97, v222\nMvav 136,172,60\nv_mul_f32 v223, s97, v223\n" \
"C 76,216,217\nC 77,218,219\nQvav 140,176,48,12\nRa 152,5,14336\nC 78,220,221\nC 79,222,223\nMvav 140,180,52\nv_mul_f32 v224, s97, v224\n" \
"v_mul_f32 v225, s97, v225\nv_mul_f32 v226, s97, v226\nMvav 140,184,56\nRa 156,5,15360\nv_mul_f32 v227, s97, v227\nv_mul_f32 v228, s97, v228\nMvav 140,188,60\nv_mul_f32 v229, s97, v229\n" \
"v_mul_f32 v230, s97, v230\nv_mul_f32 v231, s97, v231\nQvav 144,160,64,12\nC 80,224,225\nC 81,226,227\nv_mul_f32 v232, s97, v232\nMvav 144,164,68\nv_mul_f32 v233, s97, v233\n" \
"v_mul_f32 v234, s97, v234\nv_mul_f32 v235, s97, v235\nMvav 144,168,72\nC 82,228,229\nC 83,230,231\nv_mul_f32 v236, s97, v236\nMvav 144,172,76\nv_mul_f32 v237, s97, v237\n" \
"v_mul_f32 v238, s97, v238\nv_mul_f32 v239, s97, v239\nQvav 148,176,64,12\nC 84,232,233\nC 85,234,235\nv_mul_f32 v240, s97, v240\nMvav 148,180,68\nv_mul_f32 v241, s97, v241\n" \
"v_mul_f32 v242, s97, v242\nv_mul_f32 v243, s97, v243\nC 86,236,237\nC 87,238,239\nv_mul_f32 v244, s97, v244\nv_mul_f32 v245, s97, v245\nv_mul_f32 v246, s97, v246\nv_mul_f32 v247, s97, v247\n" \
"C 88,240,241\nC 89,242,243\nv_mul_f32 v248, s97, v248\nv_mul_f32 v249, s97, v249\nv_mul_f32 v250, s97, v250\nv_mul_f32 v251, s97, v251\nC 90,244,245\nC 91,246,247\n" \
"v_mul_f32 v252, s97, v252\nv_mul_f32 v253, s97, v253\nv_mul_f32 v254, s97, v254\nv_mul_f32 v255, s97, v255\nC 92,248,249\nC 93,250,251\nC 94,252,253\nC 95,254,255\n" \
"Mvav 148,184,72\nE 240,128\nMvav 148,188,76\nE 241,129\nQvav 152,160,80,12\nC 192,240,241\nE 242,130\nMvav 152,164,84\n" \
"E 243,131\nMvav 152,168,88\nC 193,242,243\nMvav 152,172,92\nE 244,132\nQvav 156,176,80,12\nE 245,133\nMvav 156,180,84\n" \
"C 194,244,245\nE 246,134\nMvav 156,184,88\nE 247,135\nMvav 156,188,92\ns_waitcnt lgkmcnt(0)\nC 195,246,247\nQvav 160,128,32,12\n" \
"E 240,136\nMvav 160,132,36\nE 241,137\nMvav 160,136,40\nC 196,240,241\nE 242,138\nMvav 160,140,44\nQvav 168,128,48,12\n" \
"v_mov_b32 v6, v5\nv_add_u32 v5, s74, v9\nE 243,139\nMvav 168,132,52\nC 197,242,243\nE 244,140\nMvav 168,136,56\nE 245,141\n" \
"Mvav 168,140,60\nC 198,244,245\nE 246,142\nQvav 176,128,64,12\ns_waitcnt vmcnt(9)\ns_barrier\nMvav 176,132,68\nRa 160,5,0\n" \
"E 247,143\nMvav 176,136,72\ns_mov_b32 m0, s73\nC 199,246,247\nE 240,144\nMvav 176,140,76\nRa 164,5,1024\nDS 0,60,s77,0\n" \
"E 241,145\nQvav 184,128,80,12\nC 200,240,241\nMvav 184,132,84\nRa 168,5,2048\nE 242,146\nMvav 184,136,88\nE 243,147\n" \
"Mvav 184,140,92\nRa 172,5,3072\nC 201,242,243\nE 244,148\nQvav 164,144,32,12\nDS 0,60,s77,1024\nE 245,149\nMvav 164,148,36\n" \
"Ra 176,5,4096\nC 202,244,245\nMvav 164,152,40\nE 246,150\nMvav 164,156,44\nRa 180,5,5120\nE 247,151\nQvav 172,144,48,12\n" \
"C 203,246,247\nE 240,152\nMvav 172,148,52\nRa 184,5,6144\nDS 0,60,s77,2048\nMvav 172,152,56\nE 241,153\nMvav 172,156,60\n" \
"Ra 188,5,7168\nC 204,240,241\nE 242,154\nQvav 180,144,64,12\nE 243,155\nMvav 180,148,68\nC 205,242,243\nE 244,156\n" \
"Mvav 180,152,72\nDS 0,60,s77,3072\nMvav 180,156,76\nE 245,157\nQvav 188,144,80,12\nC 206,244,245\nE 246,158\nMvav 188,148,84\n" \
"E 247,159\nMvav 188,152,88\nC 207,246,247\ns_mov_b32 s96, s39\nMvav 188,156,92\ns_waitcnt lgkmcnt(0)\nQvav 128,160,32,12\nE 240,160\n" \
"Mvav 128,164,36\nE 241,161\nMvav 128,168,40\nC 208,240,241\nRa 128,6,49152\nMvav 128,172,44\nE 242,162\ns_add_u32 m0, s75, 0xc000\n" \
"Qvav 136,160,48,12\nDS 8,60,s77,0\nMvav 136,164,52\nE 243,163\nMvav 136,168,56\nC 209,242,243\nRa 132,6,50176\nMvav 136,172,60\n" \
"E 244,164\nQvav 144,160,64,12\nDS 8,60,s77,1024\nMvav 144,164,68\nE 245,165\nMvav 144,168,72\nC 210,244,245\nRa 136,6,51200\n" \
"Mvav 144,172,76\nE 246,166\ns_add_u32 s95, s34, 16384\nQvav 152,160,80,12\nbuffer_store_dwordx4 a[192:195], v0, s[60:63], s95 offen offset:0 sc1\nMvav 152,164,84\nE 247,167\nMvav 152,168,88\n" \
"C 211,246,247\nRa 140,6,52224\nMvav 152,172,92\nE 240,168\nQvav 132,176,32,12\nDS 8,60,s77,2048\nMvav 132,180,36\nE 241,169\n" \
"Mvav 132,184,40\nC 212,240,241\nRa 144,6,53248\nMvav 132,188,44\nE 242,170\nQvav 140,176,48,12\nbuffer_store_dwordx4 a[196:199], v0, s[60:63], s95 offen offset:1024 sc1\nMvav 140,180,52\n" \
"E 243,171\nMvav 140,184,56\nC 213,242,243\nRa 148,6,54272\nMvav 140,188,60\nE 244,172\nQvav 148,176,64,12\nDS 8,60,s77,3072\n" \
"Mvav 148,180,68\nE 245,173\nMvav 148,184,72\nC 214,244,245\nRa 152,6,55296\nMvav 148,188,76\nE 246,174\nQvav 156,176,80,12\n" \
"Ra 156,6,56320\ns_mov_b32 s76, s73\nMvav 156,180,84\nE 247,175\nMvav 156,184,88\nC 215,246,247\ns_mov_b32 s73, s74\nMvav 156,188,92\n" \
"E 240,176\nv_smfmac_f32_16x16x64_bf16 v[96:99], v[104:107], v[192:199], v7\ns_mov_b32 s74, s75\nv_smfmac_f32_16x16x64_bf16 v[100:103], v[104:107], v[200:207], v7\ns_waitcnt lgkmcnt(0)\nZaav 96,128,204\nbuffer_store_dwordx4 a[200:203], v0, s[60:63], s95 offen offset:2048 sc1\nZaav 100,132,204\n" \
"E 241,177\nZaav 68,132,200\nC 216,240,241\nRa 160,5,8192\nZaav 64,128,200\nE 242,178\ns_branch 99f\n.p2align 12\n" \
"zp8:\ns_setpc_b64 s[98:99]\n99:\nZaav 32,128,196\nRa 164,5,9216\ns_mov_b32 s75, s76\nZaav 36,132,196\nE 243,179\n" \
"Zaav 4,132,192\nC 217,242,243\ns_add_u32 s77, s77, 0x4000\nZaav 0,128,192\nE 244,180\nZaav 8,136,192\nRa 168,5,10240\nZaav 12,140,192\n" \
"E 245,181\nZaav 44,140,196\nC 218,244,245\nRa 172,5,11264\nZaav 40,136,196\nE 246,182\nZaav 72,136,200\nRa 176,5,12288\n" \
"Zaav 76,140,200\nE 247,183\nZaav 108,140,204\nC 219,246,247\nZaav 104,136,204\nE 240,184\nZaav 112,144,204\nbuffer_store_dwordx4 a[204:207], v0, s[60:63], s95 offen offset:3072 sc1\n" \
"Zaav 116,148,204\nE 241,185\nZaav 84,148,200\nC 220,240,241\nRa 180,5,13312\nZaav 80,144,200\nE 242,186\nZaav 48,144,196\n" \
"Ra 184,5,14336\nZaav 52,148,196\nE 243,187\nZaav 20,148,192\nC 221,242,243\nZaav 16,144,192\nE 244,188\nZaav 24,152,192\n" \
"Ra 188,5,15360\nZaav 28,156,192\nE 245,189\nZaav 60,156,196\nC 222,244,245\nZaav 56,152,196\nE 246,190\nZaav 88,152,200\n" \
"Zaav 92,156,200\nE 247,191\nZaav 124,156,204\nC 223,246,247\ns_waitcnt lgkmcnt(0)\nZaav 120,152,204\nQvav 160,160,32,12\nE 240,128\n" \
"Mvav 160,164,36\nE 241,129\nMvav 160,168,40\nC 192,240,241\nRa 128,6,57344\nMvav 160,172,44\nE 242,130\ns_add_u32 s95, s34, 20480\n" \
"Qvav 168,160,48,12\nbuffer_store_dwordx4 a[208:211], v0, s[60:63], s95 offen offset:0 sc1\nMvav 168,164,52\nE 243,131\nMvav 168,168,56\nC 193,242,243\nMvav 168,172,60\nE 244,132\n" \
"Qvav 176,160,64,12\nRa 132,6,58368\nMvav 176,164,68\nE 245,133\nMvav 176,168,72\nC 194,244,245\nRa 136,6,59392\nMvav 176,172,76\n" \
"E 246,134\nQvav 184,160,80,12\nRa 140,6,60416\nMvav 184,164,84\nE 247,135\nMvav 184,168,88\nC 195,246,247\nMvav 184,172,92\n" \
"E 240,136\nQvav 164,176,32,12\nRa 144,6,61440\nMvav 164,180,36\nE 241,137\nMvav 164,184,40\nC 196,240,241\nRa 148,6,62464\n" \
"Mvav 164,188,44\nE 242,138\nQvav 172,176,48,12\nbuffer_store_dwordx4 a[212:215], v0, s[60:63], s95 offen offset:1024 sc1\nMvav 172,180,52\nE 243,139\nMvav 172,184,56\nC 197,242,243\n" \
"Mvav 172,188,60\nE 244,140\nQvav 180,176,64,12\nRa 152,6,63488\nMvav 180,180,68\nE 245,141\nMvav 180,184,72\nC 198,244,245\n" \
"Ra 156,6,64512\nMvav 180,188,76\nE 246,142\nQvav 188,176,80,12\nv_mov_b32 v6, v5\nv_add_u32 v5, s74, v9\nMvav 188,180,84\nE 247,143\n" \
"Mvav 188,184,88\nC 199,246,247\nMvav 188,188,92\nE 240,144\nv_smfmac_f32_16x16x64_bf16 v[96:99], v[104:107], v[208:215], v7\nv_smfmac_f32_16x16x64_bf16 v[100:103], v[104:107], v[216:223], v7\ns_waitcnt vmcnt(15) lgkmcnt(0)\ns_barrier\n" \
"Maav 96,128,220\nbuffer_store_dwordx4 a[216:219], v0, s[60:63], s95 offen offset:2048 sc1\nMaav 100,132,220\nE 241,145\nMaav 68,132,216\nC 200,240,241\nRa 160,5,0\nMaav 64,128,216\n" \
"E 242,146\ns_mov_b32 m0, s73\nMaav 32,128,212\nDS 0,60,s77,0\nMaav 36,132,212\nE 243,147\nMaav 4,132,208\nC 201,242,243\n" \
"Ra 164,5,1024\nMaav 0,128,208\nE 244,148\nMaav 8,136,208\nDS 0,60,s77,1024\nMaav 12,140,208\nE 245,149\nMaav 44,140,212\n" \
"C 202,244,245\nRa 168,5,2048\nMaav 40,136,212\nE 246,150\nMaav 72,136,216\nRa 172,5,3072\nRa 176,5,4096\nMaav 76,140,216\n" \
"E 247,151\nMaav 108,140,220\nC 203,246,247\nRa 180,5,5120\nMaav 104,136,220\nE 240,152\nMaav 112,144,220\nDS 0,60,s77,2048\n" \
"Maav 116,148,220\nE 241,153\nMaav 84,148,216\nC 204,240,241\nRa 184,5,6144\nMaav 80,144,216\nE 242,154\nMaav 48,144,212\n" \
"buffer_store_dwordx4 a[220:223], v0, s[60:63], s95 offen offset:3072 sc1\nMaav 52,148,212\nE 243,155\nMaav 20,148,208\nC 205,242,243\nRa 188,5,7168\nMaav 16,144,208\nE 244,156\n" \
"Maav 24,152,208\nDS 0,60,s77,3072\nMaav 28,156,208\nE 245,157\nMaav 60,156,212\nC 206,244,245\nMaav 56,152,212\nE 246,158\n" \
"Maav 88,152,216\nMaav 92,156,216\nE 247,159\nMaav 124,156,220\nC 207,246,247\ns_waitcnt lgkmcnt(0)\nMaav 120,152,220\nQvav 128,160,32,12\n" \
"E 240,160\nMvav 128,164,36\nE 241,161\nMvav 128,168,40\nC 208,240,241\nRa 128,6,49152\nMvav 128,172,44\nE 242,162\n" \
"s_add_u32 m0, s75, 0xc000\nQvav 136,160,48,12\nDS 8,60,s77,0\nMvav 136,164,52\nE 243,163\nMvav 136,168,56\nC 209,242,243\nRa 132,6,50176\n" \
"Mvav 136,172,60\nE 244,164\nQvav 144,160,64,12\nDS 8,60,s77,1024\nMvav 144,164,68\nE 245,165\nMvav 144,168,72\nC 210,244,245\n" \
"Ra 136,6,51200\nMvav 144,172,76\nE 246,166\ns_add_u32 s95, s34, 24576\nQvav 152,160,80,12\nbuffer_store_dwordx4 a[224:227], v0, s[60:63], s95 offen offset:0 sc1\nMvav 152,164,84\nE 247,167\n" \
"Mvav 152,168,88\nC 211,246,247\nRa 140,6,52224\nMvav 152,172,92\nE 240,168\nQvav 132,176,32,12\nDS 8,60,s77,2048\nMvav 132,180,36\n" \
"E 241,169\nMvav 132,184,40\nC 212,240,241\nRa 144,6,53248\nMvav 132,188,44\nE 242,170\nQvav 140,176,48,12\nbuffer_store_dwordx4 a[228:231], v0, s[60:63], s95 offen offset:1024 sc1\n" \
"Mvav 140,180,52\nE 243,171\nMvav 140,184,56\nC 213,242,243\nRa 148,6,54272\nMvav 140,188,60\nE 244,172\nQvav 148,176,64,12\n" \
"DS 8,60,s77,3072\nMvav 148,180,68\nE 245,173\nMvav 148,184,72\nC 214,244,245\nRa 152,6,55296\nMvav 148,188,76\nE 246,174\n" \
"Qvav 156,176,80,12\nRa 156,6,56320\ns_mov_b32 s76, s73\nMvav 156,180,84\nE 247,175\nMvav 156,184,88\nC 215,246,247\ns_mov_b32 s73, s74\n" \
"Mvav 156,188,92\nE 240,176\nv_smfmac_f32_16x16x64_bf16 v[96:99], v[104:107], v[192:199], v7\ns_mov_b32 s74, s75\nv_smfmac_f32_16x16x64_bf16 v[100:103], v[104:107], v[200:207], v7\ns_waitcnt lgkmcnt(0)\nMaav 96,128,204\nbuffer_store_dwordx4 a[232:235], v0, s[60:63], s95 offen offset:2048 sc1\n" \
"Maav 100,132,204\nE 241,177\nMaav 68,132,200\nC 216,240,241\nRa 160,5,8192\nMaav 64,128,200\nE 242,178\nMaav 32,128,196\n" \
"Ra 164,5,9216\ns_mov_b32 s75, s76\nMaav 36,132,196\nE 243,179\nMaav 4,132,192\nC 217,242,243\ns_add_u32 s77, s77, 0x4000\nMaav 0,128,192\n" \
"E 244,180\nMaav 8,136,192\nRa 168,5,10240\nMaav 12,140,192\nE 245,181\nMaav 44,140,196\nC 218,244,245\nRa 172,5,11264\n" \
"Maav 40,136,196\nE 246,182\nMaav 72,136,200\nRa 176,5,12288\nMaav 76,140,200\nE 247,183\nMaav 108,140,204\nC 219,246,247\n" \
"Maav 104,136,204\nE 240,184\nMaav 112,144,204\nbuffer_store_dwordx4 a[236:239], v0, s[60:63], s95 offen offset:3072 sc1\nMaav 116,148,204\nE 241,185\nMaav 84,148,200\nC 220,240,241\n" \
"Ra 180,5,13312\nMaav 80,144,200\nE 242,186\nMaav 48,144,196\nRa 184,5,14336\nMaav 52,148,196\nE 243,187\nMaav 20,148,192\n" \
"C 221,242,243\nMaav 16,144,192\nE 244,188\nMaav 24,152,192\nRa 188,5,15360\nMaav 28,156,192\nE 245,189\nMaav 60,156,196\n" \
"C 222,244,245\nMaav 56,152,196\nE 246,190\nMaav 88,152,200\nMaav 92,156,200\nE 247,191\nMaav 124,156,204\nC 223,246,247\n" \
"s_waitcnt lgkmcnt(0)\nMaav 120,152,204\nQvav 160,160,32,12\nE 240,128\nMvav 160,164,36\nE 241,129\nMvav 160,168,40\nC 192,240,241\n" \
"Ra 128,6,57344\nMvav 160,172,44\nE 242,130\ns_add_u32 s95, s34, 28672\nQvav 168,160,48,12\nbuffer_store_dwordx4 a[240:243], v0, s[60:63], s95 offen offset:0 sc1\nMvav 168,164,52\nE 243,131\n" \
"Mvav 168,168,56\nC 193,242,243\nMvav 168,172,60\nE 244,132\nQvav 176,160,64,12\nRa 132,6,58368\nMvav 176,164,68\nE 245,133\n" \
"Mvav 176,168,72\nC 194,244,245\nRa 136,6,59392\nMvav 176,172,76\nE 246,134\nQvav 184,160,80,12\nRa 140,6,60416\nMvav 184,164,84\n" \
"E 247,135\nMvav 184,168,88\nC 195,246,247\nMvav 184,172,92\nE 240,136\nQvav 164,176,32,12\nRa 144,6,61440\nMvav 164,180,36\n" \
"E 241,137\nMvav 164,184,40\nC 196,240,241\nRa 148,6,62464\nMvav 164,188,44\nE 242,138\nQvav 172,176,48,12\nbuffer_store_dwordx4 a[244:247], v0, s[60:63], s95 offen offset:1024 sc1\n" \
"Mvav 172,180,52\nE 243,139\nMvav 172,184,56\nC 197,242,243\nMvav 172,188,60\nE 244,140\nQvav 180,176,64,12\nRa 152,6,63488\n" \
"Mvav 180,180,68\nE 245,141\nMvav 180,184,72\nC 198,244,245\nRa 156,6,64512\nMvav 180,188,76\nE 246,142\nQvav 188,176,80,12\n" \
"v_mov_b32 v6, v5\nv_add_u32 v5, s74, v9\nMvav 188,180,84\nE 247,143\nMvav 188,184,88\nC 199,246,247\nMvav 188,188,92\nE 240,144\n" \
"v_smfmac_f32_16x16x64_bf16 v[96:99], v[104:107], v[208:215], v7\nv_smfmac_f32_16x16x64_bf16 v[100:103], v[104:107], v[216:223], v7\ns_waitcnt vmcnt(20) lgkmcnt(0)\ns_barrier\nMaav 96,128,220\nbuffer_store_dwordx4 a[248:251], v0, s[60:63], s95 offen offset:2048 sc1\nMaav 100,132,220\nE 241,145\n" \
"Maav 68,132,216\nC 200,240,241\nRa 160,5,0\nMaav 64,128,216\nE 242,146\ns_mov_b32 m0, s73\nMaav 32,128,212\nDS 0,60,s77,0\n" \
"Maav 36,132,212\nE 243,147\nMaav 4,132,208\nC 201,242,243\nRa 164,5,1024\nMaav 0,128,208\nE 244,148\nMaav 8,136,208\n" \
"DS 0,60,s77,1024\nMaav 12,140,208\nE 245,149\nMaav 44,140,212\nC 202,244,245\nRa 168,5,2048\nMaav 40,136,212\nE 246,150\n" \
"Maav 72,136,216\nRa 172,5,3072\nRa 176,5,4096\nMaav 76,140,216\nE 247,151\nMaav 108,140,220\nC 203,246,247\nRa 180,5,5120\n" \
"Maav 104,136,220\nE 240,152\nMaav 112,144,220\nDS 0,60,s77,2048\nMaav 116,148,220\nE 241,153\nMaav 84,148,216\nC 204,240,241\n" \
"Ra 184,5,6144\nMaav 80,144,216\nE 242,154\nMaav 48,144,212\nbuffer_store_dwordx4 a[252:255], v0, s[60:63], s95 offen offset:3072 sc1\nMaav 52,148,212\nE 243,155\nMaav 20,148,208\n" \
"C 205,242,243\nRa 188,5,7168\nMaav 16,144,208\nE 244,156\nMaav 24,152,208\nDS 0,60,s77,3072\nMaav 28,156,208\nE 245,157\n" \
"Maav 60,156,212\nC 206,244,245\nMaav 56,152,212\nE 246,158\nMaav 88,152,216\nMaav 92,156,216\nE 247,159\nMaav 124,156,220\n" \
"C 207,246,247\ns_waitcnt lgkmcnt(0)\nMaav 120,152,220\nQvav 128,160,32,12\nE 240,160\nMvav 128,164,36\nE 241,161\nMvav 128,168,40\n" \
"C 208,240,241\nRa 128,6,49152\nMvav 128,172,44\nE 242,162\ns_add_u32 m0, s75, 0xc000\nQvav 136,160,48,12\nDS 8,60,s77,0\nMvav 136,164,52\n" \
"E 243,163\nMvav 136,168,56\nC 209,242,243\nRa 132,6,50176\nMvav 136,172,60\nE 244,164\nQvav 144,160,64,12\nDS 8,60,s77,1024\n" \
"Mvav 144,164,68\nE 245,165\nMvav 144,168,72\nC 210,244,245\nMvav 144,172,76\nE 246,166\nQvav 152,160,80,12\nRa 136,6,51200\n" \
"Ra 140,6,52224\nMvav 152,164,84\nE 247,167\nMvav 152,168,88\nC 211,246,247\nMvav 152,172,92\nE 240,168\nQvav 132,176,32,12\n" \
"DS 8,60,s77,2048\nMvav 132,180,36\nE 241,169\nMvav 132,184,40\nC 212,240,241\nRa 144,6,53248\nMvav 132,188,44\nE 242,170\n" \
"Qvav 140,176,48,12\nRa 148,6,54272\nMvav 140,180,52\nE 243,171\nMvav 140,184,56\nC 213,242,243\nRa 152,6,55296\nMvav 140,188,60\n" \
"E 244,172\nQvav 148,176,64,12\nDS 8,60,s77,3072\nMvav 148,180,68\nE 245,173\nMvav 148,184,72\nC 214,244,245\ns_mov_b32 s76, s73\n" \
"Mvav 148,188,76\nE 246,174\nQvav 156,176,80,12\nRa 156,6,56320\ns_mov_b32 s73, s74\nMvav 156,180,84\nE 247,175\nMvav 156,184,88\n" \
"C 215,246,247\ns_mov_b32 s74, s75\nMvav 156,188,92\nE 240,176\nv_smfmac_f32_16x16x64_bf16 v[96:99], v[104:107], v[192:199], v7\ns_mov_b32 s75, s76\nv_smfmac_f32_16x16x64_bf16 v[100:103], v[104:107], v[200:207], v7\ns_waitcnt lgkmcnt(0)\n" \
"Maav 96,128,204\nRa 160,5,8192\ns_add_u32 s77, s77, 0x4000\nMaav 100,132,204\nE 241,177\nMaav 68,132,200\nC 216,240,241\nRa 164,5,9216\n" \
"Maav 64,128,200\nE 242,178\nMaav 32,128,196\nMaav 36,132,196\nE 243,179\nMaav 4,132,192\nC 217,242,243\ns_branch 99f\n" \
".p2align 12\nzp9:\ns_setpc_b64 s[98:99]\n99:\nRa 168,5,10240\nMaav 0,128,192\nE 244,180\nMaav 8,136,192\n" \
"Ra 172,5,11264\nMaav 12,140,192\nE 245,181\nMaav 44,140,196\nC 218,244,245\nMaav 40,136,196\nE 246,182\nMaav 72,136,200\n" \
"Ra 176,5,12288\nMaav 76,140,200\nE 247,183\nMaav 108,140,204\nC 219,246,247\nMaav 104,136,204\nE 240,184\nMaav 112,144,204\n" \
"Ra 180,5,13312\nMaav 116,148,204\nE 241,185\nMaav 84,148,200\nC 220,240,241\nMaav 80,144,200\nE 242,186\nMaav 48,144,196\n" \
"Ra 184,5,14336\nMaav 52,148,196\nE 243,187\nMaav 20,148,192\nC 221,242,243\nRa 188,5,15360\nMaav 16,144,192\nE 244,188\n" \
"Maav 24,152,192\nMaav 28,156,192\nE 245,189\nMaav 60,156,196\nC 222,244,245\nMaav 56,152,196\nE 246,190\nMaav 88,152,200\n" \
"Maav 92,156,200\nE 247,191\nMaav 124,156,204\nC 223,246,247\ns_waitcnt lgkmcnt(0)\nMaav 120,152,204\nQvav 160,160,32,12\nE 240,128\n" \
"Mvav 160,164,36\nE 241,129\nMvav 160,168,40\nC 192,240,241\nRa 128,6,57344\nMvav 160,172,44\nE 242,130\nQvav 168,160,48,12\n" \
"Ra 132,6,58368\nMvav 168,164,52\nE 243,131\nMvav 168,168,56\nC 193,242,243\nMvav 168,172,60\nE 244,132\nQvav 176,160,64,12\n" \
"Ra 136,6,59392\nMvav 176,164,68\nE 245,133\nMvav 176,168,72\nC 194,244,245\nMvav 176,172,76\nE 246,134\nQvav 184,160,80,12\n" \
"Ra 140,6,60416\nMvav 184,164,84\nE 247,135\nMvav 184,168,88\nC 195,246,247\nMvav 184,172,92\nE 240,136\nQvav 164,176,32,12\n" \
"Ra 144,6,61440\nMvav 164,180,36\nE 241,137\nMvav 164,184,40\nC 196,240,241\nMvav 164,188,44\nE 242,138\nQvav 172,176,48,12\n" \
"Ra 148,6,62464\nMvav 172,180,52\nE 243,139\nMvav 172,184,56\nC 197,242,243\nRa 152,6,63488\nMvav 172,188,60\nE 244,140\n" \
"Qvav 180,176,64,12\nMvav 180,180,68\nE 245,141\nMvav 180,184,72\nC 198,244,245\nRa 156,6,64512\nMvav 180,188,76\nE 246,142\n" \
"Qvav 188,176,80,12\nv_mov_b32 v6, v5\nv_add_u32 v5, s74, v9\nMvav 188,180,84\nE 247,143\nMvav 188,184,88\nC 199,246,247\nMvav 188,188,92\n" \
"E 240,144\nv_smfmac_f32_16x16x64_bf16 v[96:99], v[104:107], v[208:215], v7\nv_smfmac_f32_16x16x64_bf16 v[100:103], v[104:107], v[216:223], v7\ns_waitcnt vmcnt(5) lgkmcnt(0)\ns_barrier\nMaav 96,128,220\nbuffer_store_dwordx2 v[2:3], off, s[60:63], s36 sc1\nMaav 100,132,220\n" \
"E 241,145\nMaav 68,132,216\nC 200,240,241\nRa 160,5,0\nMaav 64,128,216\nE 242,146\ns_mov_b32 m0, s73\nMaav 32,128,212\n" \
"DS 0,60,s77,0\nMaav 36,132,212\nE 243,147\nMaav 4,132,208\nC 201,242,243\nRa 164,5,1024\nMaav 0,128,208\nE 244,148\n" \
"Maav 8,136,208\nDS 0,60,s77,1024\nMaav 12,140,208\nE 245,149\nMaav 44,140,212\nC 202,244,245\nRa 168,5,2048\nMaav 40,136,212\n" \
"E 246,150\nMaav 72,136,216\nRa 172,5,3072\nMaav 76,140,216\nE 247,151\nMaav 108,140,220\nC 203,246,247\nRa 176,5,4096\n" \
"Maav 104,136,220\nE 240,152\nMaav 112,144,220\nDS 0,60,s77,2048\nMaav 116,148,220\nE 241,153\nMaav 84,148,216\nC 204,240,241\n" \
"Ra 180,5,5120\nMaav 80,144,216\nE 242,154\nMaav 48,144,212\nRa 184,5,6144\nRa 188,5,7168\nMaav 52,148,212\nE 243,155\n" \
"Maav 20,148,208\nC 205,242,243\nMaav 16,144,208\nE 244,156\nMaav 24,152,208\nDS 0,60,s77,3072\nMaav 28,156,208\nE 245,157\n" \
"Maav 60,156,212\nC 206,244,245\nMaav 56,152,212\nE 246,158\nMaav 88,152,216\nMaav 92,156,216\nE 247,159\nMaav 124,156,220\n" \
"C 207,246,247\ns_waitcnt lgkmcnt(0)\nMaav 120,152,220\ns_branch 99f\n.p2align 12\nzgt:\nzp10:\ns_setpc_b64 s[98:99]\n" \
"99:\ns_nop 0\ns_nop 0\ns_nop 0\nzO:\nQvav 128,160,32,12\nE 240,160\nMvav 128,164,36\n" \
"E 241,161\nMvav 128,168,40\nC 208,240,241\nRa 128,6,49152\nMvav 128,172,44\nE 242,162\ns_add_u32 m0, s75, 0xc000\nQvav 136,160,48,12\n" \
"DS 8,60,s77,0\nMvav 136,164,52\nE 243,163\nMvav 136,168,56\nC 209,242,243\nRa 132,6,50176\nMvav 136,172,60\nE 244,164\n" \
"Qvav 144,160,64,12\nDS 8,60,s77,1024\nMvav 144,164,68\nE 245,165\nMvav 144,168,72\nC 210,244,245\nRa 136,6,51200\nMvav 144,172,76\n" \
"E 246,166\nQvav 152,160,80,12\nDS 8,60,s77,2048\nMvav 152,164,84\nE 247,167\nMvav 152,168,88\nC 211,246,247\nRa 140,6,52224\n" \
"Mvav 152,172,92\nE 240,168\nQvav 132,176,32,12\nDS 8,60,s77,3072\nMvav 132,180,36\nE 241,169\nMvav 132,184,40\nC 212,240,241\n" \
"Ra 144,6,53248\nMvav 132,188,44\nE 242,170\nQvav 140,176,48,12\nbuffer_load_dwordx2 v[10:11], v1, s[60:63], 0 offen offset:64 sc1\nMvav 140,180,52\nE 243,171\nMvav 140,184,56\n" \
"C 213,242,243\nRa 148,6,54272\nMvav 140,188,60\nE 244,172\nQvav 148,176,64,12\nbuffer_load_dword v30, v4, s[56:59], 0 offen sc1\nMvav 148,180,68\nE 245,173\n" \
"Mvav 148,184,72\nC 214,244,245\nRa 152,6,55296\nMvav 148,188,76\nE 246,174\nQvav 156,176,80,12\nRa 156,6,56320\ns_mov_b32 s76, s73\n" \
"Mvav 156,180,84\nE 247,175\nMvav 156,184,88\nC 215,246,247\ns_mov_b32 s73, s74\nMvav 156,188,92\nE 240,176\nv_smfmac_f32_16x16x64_bf16 v[96:99], v[104:107], v[192:199], v7\n" \
"s_mov_b32 s74, s75\nv_smfmac_f32_16x16x64_bf16 v[100:103], v[104:107], v[200:207], v7\ns_waitcnt lgkmcnt(0)\nMaav 96,128,204\nRa 160,5,8192\ns_mov_b32 s75, s76\nMaav 100,132,204\nE 241,177\n" \
"Maav 68,132,200\nC 216,240,241\nRa 164,5,9216\nMaav 64,128,200\nE 242,178\nMaav 32,128,196\ns_add_u32 s77, s77, 0x4000\nv_add_u32 v4, s45, v4\n" \
"Maav 36,132,196\nE 243,179\nMaav 4,132,192\nC 217,242,243\nRa 168,5,10240\nMaav 0,128,192\nE 244,180\nMaav 8,136,192\n" \
"Ra 172,5,11264\nMaav 12,140,192\nE 245,181\nMaav 44,140,196\nC 218,244,245\nMaav 40,136,196\nE 246,182\nMaav 72,136,200\n" \
"Ra 176,5,12288\nMaav 76,140,200\nE 247,183\nMaav 108,140,204\nC 219,246,247\nMaav 104,136,204\nE 240,184\nMaav 112,144,204\n" \
"Ra 180,5,13312\nMaav 116,148,204\nE 241,185\nMaav 84,148,200\nC 220,240,241\nMaav 80,144,200\nE 242,186\nMaav 48,144,196\n" \
"Ra 184,5,14336\nMaav 52,148,196\nE 243,187\nMaav 20,148,192\nC 221,242,243\nRa 188,5,15360\nMaav 16,144,192\nE 244,188\n" \
"Maav 24,152,192\nMaav 28,156,192\nE 245,189\nMaav 60,156,196\nC 222,244,245\nMaav 56,152,196\nE 246,190\nMaav 88,152,200\n" \
"Maav 92,156,200\nE 247,191\nMaav 124,156,204\nC 223,246,247\ns_waitcnt lgkmcnt(0)\nMaav 120,152,204\nzE:\nQvav 160,160,32,12\n" \
"E 240,128\nMvav 160,164,36\nE 241,129\nMvav 160,168,40\nC 192,240,241\nRa 128,6,57344\nMvav 160,172,44\nE 242,130\n" \
"Qvav 168,160,48,12\nRa 132,6,58368\nMvav 168,164,52\nE 243,131\nMvav 168,168,56\nC 193,242,243\nMvav 168,172,60\nE 244,132\n" \
"Qvav 176,160,64,12\nRa 136,6,59392\nMvav 176,164,68\nE 245,133\nMvav 176,168,72\nC 194,244,245\nMvav 176,172,76\nE 246,134\n" \
"Qvav 184,160,80,12\nRa 140,6,60416\nMvav 184,164,84\nE 247,135\nMvav 184,168,88\nC 195,246,247\nMvav 184,172,92\nE 240,136\n" \
"Qvav 164,176,32,12\nRa 144,6,61440\nMvav 164,180,36\nE 241,137\nMvav 164,184,40\nC 196,240,241\nMvav 164,188,44\nE 242,138\n" \
"Qvav 172,176,48,12\nRa 148,6,62464\nMvav 172,180,52\nE 243,139\nMvav 172,184,56\nC 197,242,243\nRa 152,6,63488\nMvav 172,188,60\n" \
"E 244,140\nQvav 180,176,64,12\nMvav 180,180,68\nE 245,141\nMvav 180,184,72\nC 198,244,245\nRa 156,6,64512\nMvav 180,188,76\n" \
"E 246,142\nQvav 188,176,80,12\nv_mov_b32 v6, v5\nv_add_u32 v5, s74, v9\nMvav 188,180,84\nE 247,143\nMvav 188,184,88\nC 199,246,247\n" \
"Mvav 188,188,92\nE 240,144\nv_smfmac_f32_16x16x64_bf16 v[96:99], v[104:107], v[208:215], v7\nv_smfmac_f32_16x16x64_bf16 v[100:103], v[104:107], v[216:223], v7\ns_waitcnt vmcnt(9) lgkmcnt(0)\ns_barrier\nMaav 96,128,220\nRa 160,5,0\n" \
"Ra 164,5,1024\nMaav 100,132,220\nE 241,145\nMaav 68,132,216\nC 200,240,241\nMaav 64,128,216\nE 242,146\ns_mov_b32 m0, s73\n" \
"Maav 32,128,212\nDS 0,60,s77,0\nMaav 36,132,212\nE 243,147\nMaav 4,132,208\nC 201,242,243\nRa 168,5,2048\nMaav 0,128,208\n" \
"E 244,148\nMaav 8,136,208\nDS 0,60,s77,1024\nMaav 12,140,208\nE 245,149\nMaav 44,140,212\nC 202,244,245\nRa 172,5,3072\n" \
"Maav 40,136,212\nE 246,150\nMaav 72,136,216\nRa 176,5,4096\nMaav 76,140,216\nE 247,151\nMaav 108,140,220\nC 203,246,247\n" \
"Ra 180,5,5120\nMaav 104,136,220\nE 240,152\nMaav 112,144,220\nDS 0,60,s77,2048\nMaav 116,148,220\nE 241,153\nMaav 84,148,216\n" \
"C 204,240,241\nMaav 80,144,216\nE 242,154\nMaav 48,144,212\nDS 0,60,s77,3072\nMaav 52,148,212\nE 243,155\nMaav 20,148,208\n" \
"C 205,242,243\nRa 184,5,6144\nMaav 16,144,208\nE 244,156\nMaav 24,152,208\nRa 188,5,7168\nMaav 28,156,208\nE 245,157\n" \
"Maav 60,156,212\nC 206,244,245\ns_sub_u32 s96, s96, 1\nMaav 56,152,212\nE 246,158\nMaav 88,152,216\ns_cmp_lg_u32 s96, 0\nMaav 92,156,216\n" \
"E 247,159\nMaav 124,156,220\nC 207,246,247\ns_waitcnt lgkmcnt(0)\nMaav 120,152,220\ns_cbranch_scc1 zO\ns_cmp_lg_u32 s40, 0\ns_cbranch_scc1 zL\n" \
"Qvav 128,160,32,112\nE 240,160\nMvav 128,164,36\nE 241,161\nMvav 128,168,40\nC 208,240,241\nRa 128,6,49152\nMvav 128,172,44\n" \
"E 242,162\ns_add_u32 m0, s75, 0xc000\nQvav 136,160,48,12\nDS 8,60,s77,0\nMvav 136,164,52\nE 243,163\nMvav 136,168,56\nC 209,242,243\n" \
"Ra 132,6,50176\nMvav 136,172,60\nE 244,164\nQvav 144,160,64,12\nDS 8,60,s77,1024\nMvav 144,164,68\nE 245,165\nMvav 144,168,72\n" \
"C 210,244,245\nMvav 144,172,76\nE 246,166\nQvav 152,160,80,12\nRa 136,6,51200\nRa 140,6,52224\nMvav 152,164,84\nE 247,167\n" \
"Mvav 152,168,88\nC 211,246,247\nMvav 152,172,92\nE 240,168\nQvav 132,176,32,116\nDS 8,60,s77,2048\nMvav 132,180,36\nE 241,169\n" \
"Mvav 132,184,40\nC 212,240,241\nRa 144,6,53248\nMvav 132,188,44\nE 242,170\nQvav 140,176,48,12\nRa 148,6,54272\nMvav 140,180,52\n" \
"E 243,171\nMvav 140,184,56\nC 213,242,243\nRa 152,6,55296\nMvav 140,188,60\nE 244,172\nQvav 148,176,64,12\nDS 8,60,s77,3072\n" \
"Mvav 148,180,68\nE 245,173\nMvav 148,184,72\nC 214,244,245\ns_mov_b32 s76, s73\nMvav 148,188,76\nE 246,174\nQvav 156,176,80,12\n" \
"Ra 156,6,56320\ns_mov_b32 s73, s74\nMvav 156,180,84\nE 247,175\nMvav 156,184,88\nC 215,246,247\ns_mov_b32 s74, s75\nMvav 156,188,92\n" \
"E 240,176\nv_smfmac_f32_16x16x64_bf16 v[96:99], v[104:107], v[192:199], v7\ns_mov_b32 s75, s76\nv_smfmac_f32_16x16x64_bf16 v[100:103], v[104:107], v[200:207], v7\ns_waitcnt lgkmcnt(0)\nMaav 96,128,204\nRa 160,5,8192\ns_add_u32 s77, s77, 0x4000\n" \
"Maav 100,132,204\nE 241,177\nMaav 68,132,200\nC 216,240,241\nRa 164,5,9216\nMaav 64,128,200\nE 242,178\nMaav 32,128,196\n" \
"Maav 36,132,196\nE 243,179\nMaav 4,132,192\nC 217,242,243\nRa 168,5,10240\nMaav 0,128,192\nE 244,180\nMaav 8,136,192\n" \
"Ra 172,5,11264\nMaav 12,140,192\nE 245,181\nMaav 44,140,196\nC 218,244,245\nMaav 40,136,196\nE 246,182\nMaav 72,136,200\n" \
"Ra 176,5,12288\nMaav 76,140,200\nE 247,183\nMaav 108,140,204\nC 219,246,247\nMaav 104,136,204\nE 240,184\nMaav 112,144,204\n" \
"Ra 180,5,13312\nMaav 116,148,204\nE 241,185\nMaav 84,148,200\nC 220,240,241\nMaav 80,144,200\nE 242,186\nMaav 48,144,196\n" \
"Ra 184,5,14336\nMaav 52,148,196\nE 243,187\nMaav 20,148,192\nC 221,242,243\nRa 188,5,15360\nMaav 16,144,192\nE 244,188\n" \
"Maav 24,152,192\nMaav 28,156,192\nE 245,189\nMaav 60,156,196\nC 222,244,245\nMaav 56,152,196\nE 246,190\nMaav 88,152,200\n" \
"Maav 92,156,200\nE 247,191\nMaav 124,156,204\nC 223,246,247\ns_waitcnt lgkmcnt(0)\nMaav 120,152,204\nQvav 160,160,32,120\nE 240,128\n" \
"Mvav 160,164,36\nE 241,129\nMvav 160,168,40\nC 192,240,241\nRa 128,6,57344\nMvav 160,172,44\nE 242,130\nQvav 168,160,48,12\n" \
"Ra 132,6,58368\nMvav 168,164,52\nE 243,131\nMvav 168,168,56\nC 193,242,243\nMvav 168,172,60\nE 244,132\nQvav 176,160,64,12\n" \
"Ra 136,6,59392\nMvav 176,164,68\nE 245,133\nMvav 176,168,72\nC 194,244,245\nMvav 176,172,76\nE 246,134\nQvav 184,160,80,12\n" \
"Ra 140,6,60416\nMvav 184,164,84\nE 247,135\nMvav 184,168,88\nC 195,246,247\nMvav 184,172,92\nE 240,136\nQvav 164,176,32,124\n" \
"Ra 144,6,61440\nMvav 164,180,36\nE 241,137\nMvav 164,184,40\nC 196,240,241\nMvav 164,188,44\nE 242,138\nQvav 172,176,48,12\n" \
"Ra 148,6,62464\nMvav 172,180,52\nE 243,139\nMvav 172,184,56\nC 197,242,243\nRa 152,6,63488\nMvav 172,188,60\nE 244,140\n" \
"Qvav 180,176,64,12\nMvav 180,180,68\nE 245,141\nMvav 180,184,72\nC 198,244,245\nRa 156,6,64512\nMvav 180,188,76\nE 246,142\n" \
"Qvav 188,176,80,12\nv_mov_b32 v6, v5\nv_add_u32 v5, s74, v9\nMvav 188,180,84\nE 247,143\nMvav 188,184,88\nC 199,246,247\nMvav 188,188,92\n" \
"E 240,144\nv_smfmac_f32_16x16x64_bf16 v[96:99], v[104:107], v[208:215], v7\nv_smfmac_f32_16x16x64_bf16 v[100:103], v[104:107], v[216:223], v7\ns_waitcnt vmcnt(8) lgkmcnt(0)\ns_barrier\nMaav 96,128,220\nRa 160,5,0\nRa 164,5,1024\n" \
"Maav 100,132,220\nE 241,145\nMaav 68,132,216\nC 200,240,241\nMaav 64,128,216\nE 242,146\ns_mov_b32 m0, s73\nMaav 32,128,212\n" \
"DS 0,60,s77,0\nMaav 36,132,212\nE 243,147\nMaav 4,132,208\nC 201,242,243\nRa 168,5,2048\nMaav 0,128,208\nE 244,148\n" \
"Maav 8,136,208\nDS 0,60,s77,1024\nMaav 12,140,208\nE 245,149\nMaav 44,140,212\nC 202,244,245\nRa 172,5,3072\nMaav 40,136,212\n" \
"E 246,150\nMaav 72,136,216\nRa 176,5,4096\nMaav 76,140,216\nE 247,151\nMaav 108,140,220\nC 203,246,247\nRa 180,5,5120\n" \
"Maav 104,136,220\nE 240,152\nMaav 112,144,220\nDS 0,60,s77,2048\nMaav 116,148,220\nE 241,153\nMaav 84,148,216\nC 204,240,241\n" \
"Maav 80,144,216\nE 242,154\nMaav 48,144,212\nRa 184,5,6144\nRa 188,5,7168\nMaav 52,148,212\nE 243,155\nMaav 20,148,208\n" \
"C 205,242,243\nMaav 16,144,208\nE 244,156\nMaav 24,152,208\nDS 0,60,s77,3072\nMaav 28,156,208\nE 245,157\nMaav 60,156,212\n" \
"C 206,244,245\nMaav 56,152,212\nE 246,158\nMaav 88,152,216\nMaav 92,156,216\nE 247,159\nMaav 124,156,220\nC 207,246,247\n" \
"s_waitcnt lgkmcnt(0)\nMaav 120,152,220\nQvav 136,160,48,112\nE 240,160\nRa 128,6,49152\nMvav 136,164,52\nE 241,161\nMvav 136,168,56\n" \
"C 208,240,241\nRa 132,6,50176\nMvav 136,172,60\nE 242,162\ns_add_u32 m0, s75, 0xc000\nQvav 144,160,64,12\nDS 8,60,s77,0\nMvav 144,164,68\n" \
"E 243,163\nRa 136,6,51200\nMvav 144,168,72\nC 209,242,243\nE 244,164\ns_branch 99f\n.p2align 12\nzp11:\n" \
"s_setpc_b64 s[98:99]\n99:\nMvav 144,172,76\nDS 8,60,s77,1024\nQvav 152,160,80,12\nE 245,165\nMvav 152,164,84\nC 210,244,245\n" \
"Ra 140,6,52224\nMvav 152,168,88\nE 246,166\nRa 144,6,53248\nMvav 152,172,92\nDS 8,60,s77,2048\nQvav 140,176,48,116\nE 247,167\n" \
"Mvav 140,180,52\nC 211,246,247\nE 240,168\nMvav 140,184,56\nDS 8,60,s77,3072\nMvav 140,188,60\nE 241,169\nRa 148,6,54272\n" \
"Qvav 148,176,64,12\nC 212,240,241\nRa 152,6,55296\nMvav 148,180,68\nE 242,170\nMvav 148,184,72\nE 243,171\nMvav 148,188,76\n" \
"C 213,242,243\nRa 156,6,56320\nQvav 156,176,80,12\nE 244,172\nMvav 156,180,84\ns_mov_b32 s76, s73\ns_mov_b32 s73, s74\nMvav 156,184,88\n" \
"E 245,173\nMvav 156,188,92\nC 214,244,245\ns_mov_b32 s74, s75\nv_smfmac_f32_16x16x64_bf16 v[96:99], v[104:107], v[192:199], v7\ns_mov_b32 s75, s76\nv_smfmac_f32_16x16x64_bf16 v[100:103], v[104:107], v[200:207], v7\ns_waitcnt lgkmcnt(0)\n" \
"Maav 96,128,204\nE 246,174\nMaav 100,132,204\nE 247,175\nMaav 68,132,200\nC 215,246,247\nRa 160,5,8192\nMaav 64,128,200\n" \
"E 240,176\nMaav 32,128,196\nRa 164,5,9216\ns_add_u32 s77, s77, 0x4000\nMaav 36,132,196\nE 241,177\nMaav 4,132,192\nC 216,240,241\n" \
"E 242,178\nMaav 0,128,192\nRa 168,5,10240\nMaav 8,136,192\nE 243,179\nMaav 12,140,192\nC 217,242,243\nRa 172,5,11264\n" \
"Maav 44,140,196\nE 244,180\nMaav 40,136,196\nRa 176,5,12288\nMaav 72,136,200\nE 245,181\nMaav 76,140,200\nC 218,244,245\n" \
"E 246,182\nMaav 108,140,204\nRa 180,5,13312\nMaav 104,136,204\nE 247,183\nMaav 112,144,204\nC 219,246,247\nMaav 116,148,204\n" \
"E 240,184\nMaav 84,148,200\nE 241,185\nMaav 80,144,200\nC 220,240,241\nRa 184,5,14336\nMaav 48,144,196\nE 242,186\n" \
"Maav 52,148,196\nRa 188,5,15360\nMaav 20,148,192\nE 243,187\nMaav 16,144,192\nC 221,242,243\nMaav 24,152,192\nE 244,188\n" \
"Maav 28,156,192\nE 245,189\nMaav 60,156,196\nC 222,244,245\nMaav 56,152,196\nE 246,190\nMaav 88,152,200\nMaav 92,156,200\n" \
"E 247,191\nMaav 124,156,204\nC 223,246,247\ns_waitcnt lgkmcnt(0)\nMaav 120,152,204\nzD4:\nQvav 168,160,48,120\nE 240,136\n" \
"s_add_u32 m0, s32, 98304\nMvav 168,164,52\nD 18,68,0,0\nMvav 168,168,56\nE 241,137\nMvav 168,172,60\nC 196,240,241\nRa 128,6,57344\n" \
"Qvav 176,160,64,12\nE 242,138\nMvav 176,164,68\nRa 132,6,58368\nRa 136,6,59392\nMvav 176,168,72\nE 243,139\nMvav 176,172,76\n" \
"C 197,242,243\nRa 140,6,60416\nQvav 184,160,80,12\nD 18,68,s89,1024\nMvav 184,164,84\nE 244,140\nMvav 184,168,88\nRa 144,6,61440\n" \
"Mvav 184,172,92\nE 245,141\nQvav 172,176,48,124\nC 198,244,245\nRa 148,6,62464\nMvav 172,180,52\nE 246,142\nMvav 172,184,56\n" \
"D 19,68,s90,2048\nMvav 172,188,60\nRa 152,6,63488\nRa 156,6,64512\nQvav 180,176,64,12\nE 247,143\nMvav 180,180,68\nC 199,246,247\n" \
"v_mov_b32 v6, v5\nMvav 180,184,72\nE 240,144\nMvav 180,188,76\nD 19,68,s91,3072\nQvav 188,176,80,12\nE 241,145\nMvav 188,180,84\n" \
"C 200,240,241\nv_add_u32 v18, 131072, v18\nMvav 188,184,88\nE 242,146\nMvav 188,188,92\nv_add_u32 v19, 131072, v19\nv_add_u32 v5, s74, v9\nv_smfmac_f32_16x16x64_bf16 v[96:99], v[104:107], v[208:215], v7\n" \
"v_smfmac_f32_16x16x64_bf16 v[100:103], v[104:107], v[216:223], v7\ns_waitcnt vmcnt(12) lgkmcnt(0)\ns_barrier\nMaav 96,128,220\nRa 160,5,0\nMaav 100,132,220\nE 243,147\nMaav 68,132,216\n" \
"C 201,242,243\nRa 164,5,1024\nMaav 64,128,216\nE 244,148\ns_mov_b32 m0, s73\nMaav 32,128,212\nDS 0,60,s77,0\ns_getpc_b64 s[100:101]\n" \
"7:\ns_add_u32 s100, s100, s2\ns_addc_u32 s101, s101, 0\nMaav 36,132,212\nE 245,149\ns_add_u32 s100, s100, zq0-7b\ns_addc_u32 s101, s101, 0\nMaav 4,132,208\n" \
"C 202,244,245\ns_swappc_b64 s[98:99], s[100:101]\nMaav 0,128,208\nRa 168,5,2048\nMaav 8,136,208\nE 246,150\nMaav 12,140,208\nDS 0,60,s77,1024\n" \
"Maav 44,140,212\nE 247,151\nMaav 40,136,212\nC 203,246,247\nRa 172,5,3072\nMaav 72,136,216\nE 240,152\nMaav 76,140,216\n" \
"Ra 176,5,4096\nMaav 108,140,220\nE 241,153\nMaav 104,136,220\nC 204,240,241\nMaav 112,144,220\nDS 0,60,s77,2048\nMaav 116,148,220\n" \
"E 242,154\nMaav 84,148,216\nRa 180,5,5120\nMaav 80,144,216\nE 243,155\nMaav 48,144,212\nC 205,242,243\nRa 184,5,6144\n" \
"Maav 52,148,212\nE 244,156\nMaav 20,148,208\nDS 0,60,s77,3072\nMaav 16,144,208\nRa 188,5,7168\nMaav 24,152,208\nE 245,157\n" \
"Maav 28,156,208\nC 206,244,245\nMaav 60,156,212\nE 246,158\nMaav 56,152,212\ns_mov_b32 s96, s41\ns_bitcmp1_b32 s33, 3\nMaav 88,152,216\n" \
"E 247,159\nMaav 92,156,216\nC 207,246,247\nMaav 124,156,220\ns_waitcnt lgkmcnt(0)\nMaav 120,152,220\ns_cbranch_scc1 zDa\nQvav 144,160,64,12\n" \
"E 240,168\nRa 128,6,49152\nMvav 144,164,68\nE 241,169\nMvav 144,168,72\nC 212,240,241\nRa 132,6,50176\nRa 136,6,51200\n" \
"Mvav 144,172,76\nE 242,170\ns_add_u32 m0, s75, 0xc000\nQvav 152,160,80,12\nDS 8,60,s77,0\nMvav 152,164,84\nE 243,171\nRa 140,6,52224\n" \
"Mvav 152,168,88\nC 213,242,243\nE 244,172\nMvav 152,172,92\nDS 8,60,s77,1024\nRa 144,6,53248\nQvav 148,176,64,12\nE 245,173\n" \
"Mvav 148,180,68\nC 214,244,245\nRa 148,6,54272\nMvav 148,184,72\nE 246,174\nRa 152,6,55296\nMvav 148,188,76\nE 247,175\n" \
"Qvav 156,176,80,12\nC 215,246,247\nRa 156,6,56320\nMvav 156,180,84\nE 240,176\nMvav 156,184,88\nMvav 156,188,92\nE 241,177\n" \
"Mvvv 96,108,196\nC 216,240,241\nE 242,178\nv_smfmac_f32_16x16x64_bf16 v[100:103], v[104:107], v[200:207], v7\ns_waitcnt lgkmcnt(0)\nMaav 96,128,204\nDS 8,60,s77,2048\nRa 160,5,8192\n" \
"Maav 100,132,204\nE 243,179\nMaav 68,132,200\nC 217,242,243\nRa 164,5,9216\nMaav 64,128,200\nE 244,180\nMaav 32,128,196\n" \
"E 245,181\nMaav 36,132,196\nC 218,244,245\nRa 168,5,10240\nMaav 40,136,196\nE 246,182\nRa 172,5,11264\nMaav 44,140,196\n" \
"DS 8,60,s77,3072\nMaav 76,140,200\nE 247,183\ns_add_u32 m0, s32, 114688\nMaav 72,136,200\nC 219,246,247\nE 240,184\nD 18,68,0,0\n" \
"Maav 104,136,204\nRa 176,5,12288\nRa 180,5,13312\nMaav 108,140,204\nE 241,185\nD 18,68,s89,1024\nMaav 112,144,204\nC 220,240,241\n" \
"Ra 184,5,14336\nRa 188,5,15360\nMaav 116,148,204\nE 242,186\nD 19,68,s90,2048\nMaav 84,148,200\nE 243,187\nMaav 80,144,200\n" \
"C 221,242,243\nD 19,68,s91,3072\nMaav 48,144,196\nE 244,188\nMaav 52,148,196\ns_ff1_i32_b64 s78, s[8:9]\ns_bitset0_b64 s[8:9], s78\nMaav 56,152,196\n" \
"E 245,189\nMaav 60,156,196\nC 222,244,245\nE 246,190\nMaav 92,156,200\ns_xor_b32 s82, s78, s10\ns_and_b32 s80, s82, 31\nMaav 88,152,200\n" \
"E 247,191\ns_lshl_b32 s80, s80, 18\ns_cmp_lt_u32 s78, 32\ns_cselect_b64 s[68:69], s[14:15], s[12:13]\ns_cselect_b32 s70, 0x800000, s92\nv_add_u32 v18, s80, v16\nv_xor_b32 v19, 128, v18\nMaav 120,152,204\n" \
"C 223,246,247\ns_waitcnt lgkmcnt(0)\nMaav 124,156,204\ns_mov_b32 s76, s73\ns_mov_b32 s73, s74\ns_mov_b32 s74, s75\ns_mov_b32 s75, s76\ns_add_u32 s77, s77, 0x4000\n" \
"Qvav 176,160,64,12\nE 240,144\ns_add_u32 m0, s32, 131072\nRa 128,6,57344\nMvav 176,164,68\nD 18,68,0,0\nMvav 176,168,72\nE 241,145\n" \
"Mvav 176,172,76\nC 200,240,241\nRa 132,6,58368\nRa 136,6,59392\nQvav 184,160,80,12\nD 18,68,s89,1024\nMvav 184,164,84\nE 242,146\n" \
"Mvav 184,168,88\nRa 140,6,60416\nRa 144,6,61440\nRa 148,6,62464\nMvav 184,172,92\nE 243,147\nQvav 180,176,64,12\nC 201,242,243\n" \
"Ra 152,6,63488\nMvav 180,180,68\nD 19,68,s90,2048\nRa 156,6,64512\nMvav 180,184,72\nE 244,148\nMvav 180,188,76\nD 19,68,s91,3072\n" \
"Qvav 188,176,80,12\nE 245,149\nMvav 188,180,84\nC 202,244,245\nv_add_u32 v18, 131072, v18\nMvav 188,184,88\nv_add_u32 v19, 131072, v19\nv_mov_b32 v6, v5\n" \
"Mvav 188,188,92\nE 246,150\nMvvv 96,108,212\nv_add_u32 v5, s74, v9\nv_smfmac_f32_16x16x64_bf16 v[100:103], v[104:107], v[216:223], v7\ns_waitcnt vmcnt(20) lgkmcnt(0)\ns_barrier\nMaav 96,128,220\n" \
"E 247,151\nMaav 100,132,220\nC 203,246,247\ns_mov_b32 m0, s73\nMaav 68,132,216\nDS 0,60,s77,0\nMaav 64,128,216\nE 240,152\n" \
"Maav 32,128,212\nRa 160,5,0\nRa 164,5,1024\nMaav 36,132,212\nE 241,153\nMaav 40,136,212\nC 204,240,241\nRa 168,5,2048\n" \
"Maav 44,140,212\nDS 0,60,s77,1024\nv_add_f32 v196, v96, v97\nMaav 76,140,216\nE 242,154\nv_add_f32 v197, v98, v99\nMaav 72,136,216\nRa 172,5,3072\n" \
"Ra 176,5,4096\nv_subrev_u32 v198, 0xf800000, v196\nMaav 104,136,220\nE 243,155\nv_subrev_u32 v199, 0xf800000, v197\nMaav 108,140,220\nC 205,242,243\nRa 180,5,5120\n" \
"v_max_u32 v198, v198, v199\nMaav 112,144,220\nDS 0,60,s77,2048\nv_cmp_lt_u32 vcc, 0x3c000000, v198\nMaav 116,148,220\nE 244,156\nMaav 84,148,216\nRa 184,5,6144\n" \
"Ra 188,5,7168\nMaav 80,144,216\nE 245,157\nMaav 48,144,212\nC 206,244,245\nMaav 52,148,212\nDS 0,60,s77,3072\nMaav 56,152,212\n" \
"E 246,158\nMaav 60,156,212\nMaav 92,156,216\nE 247,159\nMaav 88,152,216\nC 207,246,247\nMaav 120,152,220\ns_waitcnt lgkmcnt(0)\n" \
"Maav 124,156,220\ns_cbranch_vccnz zHs\nQvav 144,160,64,12\nE 240,176\nRa 128,6,49152\nMvav 144,164,68\nE 241,177\nMvav 144,168,72\n" \
"C 216,240,241\nRa 132,6,50176\nv_rcp_f32 v196, v196\nv_rcp_f32 v197, v197\nMvav 144,172,76\nE 242,178\ns_add_u32 m0, s75, 0xc000\ns_add_u32 s95, s83, 0\n" \
"AR 128,0\nQvav 152,160,80,12\nDS 8,60,s77,0\nRa 136,6,51200\nAR 129,1\nMvav 152,164,84\nE 243,179\nAR 130,2\n" \
"AR 131,3\nMvav 152,168,88\nC 217,242,243\nRa 140,6,52224\nAR 132,4\nAR 133,5\nMvav 152,172,92\nE 244,180\n" \
"AR 134,6\nQvav 148,176,64,12\nDS 8,60,s77,1024\nRa 144,6,53248\nAR 135,7\nv_mul_f32 v128, v128, v196\nMvav 148,180,68\nE 245,181\n" \
"v_mul_f32 v129, v129, v196\nMvav 148,184,72\nC 218,244,245\nRa 148,6,54272\nv_mul_f32 v130, v130, v196\nv_mul_f32 v131, v131, v196\nMvav 148,188,76\nE 246,182\n" \
"v_mul_f32 v132, v132, v196\nv_mul_f32 v133, v133, v196\nQvav 156,176,80,12\nRa 152,6,55296\nRa 156,6,56320\nv_mul_f32 v134, v134, v196\nMvav 156,180,84\nE 247,183\n" \
"v_mul_f32 v135, v135, v196\nC 32,128,129\nMvav 156,184,88\nC 219,246,247\nC 33,130,131\nMvav 156,188,92\nE 240,184\nC 34,132,133\n" \
"C 35,134,135\nv_smfmac_f32_16x16x64_bf16 v[100:103], v[104:107], v[200:207], v7\ns_waitcnt lgkmcnt(0)\nbuffer_store_dwordx4 v[32:35], v0, s[60:63], s95 offen offset:0 sc1\nAR 136,8\nMaav 96,128,204\nDS 8,60,s77,2048\nRa 160,5,8192\n" \
"AR 137,9\nMaav 100,132,204\nE 241,185\nAR 138,10\nAR 139,11\nMaav 68,132,200\nC 220,240,241\nRa 164,5,9216\n" \
"AR 140,12\nMaav 64,128,200\nE 242,186\nAR 141,13\nAR 142,14\nMaav 72,136,200\nDS 8,60,s77,3072\nRa 168,5,10240\n" \
"AR 143,15\nv_mul_f32 v136, v136, v196\nMaav 76,140,200\nE 243,187\nv_mul_f32 v137, v137, v196\nMaav 108,140,204\nC 221,242,243\nRa 172,5,11264\n" \
"v_mul_f32 v138, v138, v196\nv_mul_f32 v139, v139, v196\nMaav 104,136,204\nE 244,188\nv_mul_f32 v140, v140, v196\nMaav 112,144,204\nRa 176,5,12288\nRa 180,5,13312\n" \
"Ra 184,5,14336\nv_mul_f32 v141, v141, v196\nv_mul_f32 v142, v142, v196\nMaav 116,148,204\nE 245,189\nv_mul_f32 v143, v143, v196\nC 36,136,137\nMaav 84,148,200\n" \
"C 222,244,245\nRa 188,5,15360\nC 37,138,139\nMaav 80,144,200\nE 246,190\nC 38,140,141\nC 39,142,143\nMaav 88,152,200\n" \
"s_branch 99f\n.p2align 12\nzp12:\ns_setpc_b64 s[98:99]\n99:\ns_mov_b32 s76, s73\ns_mov_b32 s73, s74\nbuffer_store_dwordx4 v[36:39], v0, s[60:63], s95 offen offset:1024 sc1\n" \
"Maav 92,156,200\nE 247,191\nMaav 124,156,204\nC 223,246,247\ns_waitcnt lgkmcnt(0)\nMaav 120,152,204\ns_mov_b32 s74, s75\ns_mov_b32 s75, s76\n" \
"s_add_u32 s77, s77, 0x4000\nQvav 176,160,64,12\nE 240,144\nMvav 176,164,68\nE 241,145\nMvav 176,168,72\nC 200,240,241\nRa 128,6,57344\n" \
"s_add_u32 s95, s83, 32768\nAR 128,16\nMvav 176,172,76\nE 242,146\nAR 129,17\nAR 130,18\nQvav 184,160,80,12\nRa 132,6,58368\n" \
"Ra 136,6,59392\nAR 131,19\nMvav 184,164,84\nE 243,147\nAR 132,20\nAR 133,21\nMvav 184,168,88\nC 201,242,243\n" \
"Ra 140,6,60416\nAR 134,22\nMvav 184,172,92\nE 244,148\nAR 135,23\nv_mul_f32 v128, v128, v196\nQvav 180,176,64,12\nRa 144,6,61440\n" \
"Ra 148,6,62464\nv_mul_f32 v129, v129, v196\nMvav 180,180,68\nE 245,149\nv_mul_f32 v130, v130, v196\nv_mul_f32 v131, v131, v196\nMvav 180,184,72\nC 202,244,245\n" \
"Ra 152,6,63488\nv_mul_f32 v132, v132, v196\nMvav 180,188,76\nE 246,150\nv_mul_f32 v133, v133, v196\nv_mul_f32 v134, v134, v196\nQvav 188,176,80,12\nRa 156,6,64512\n" \
"v_mul_f32 v135, v135, v196\nMvav 188,180,84\nE 247,151\nC 40,128,129\nC 41,130,131\nMvav 188,184,88\nC 203,246,247\nv_mov_b32 v6, v5\n" \
"C 42,132,133\nMvav 188,188,92\nE 240,152\nC 43,134,135\nbuffer_store_dwordx4 v[40:43], v0, s[60:63], s95 offen offset:0 sc1\nv_smfmac_f32_16x16x64_bf16 v[100:103], v[104:107], v[216:223], v7\ns_waitcnt vmcnt(19) lgkmcnt(0)\ns_barrier\n" \
"v_add_u32 v5, s74, v9\nAR 136,24\nAR 137,25\nMaav 96,128,220\ns_mov_b32 m0, s73\nRa 160,5,0\nRa 164,5,1024\nAR 138,26\n" \
"Maav 100,132,220\nE 241,153\nDS 0,60,s77,0\nAR 139,27\nAR 140,28\nMaav 68,132,216\nC 204,240,241\nRa 168,5,2048\n" \
"AR 141,29\nMaav 64,128,216\nE 242,154\nAR 142,30\nAR 143,31\nMaav 72,136,216\nDS 0,60,s77,1024\nRa 172,5,3072\n" \
"v_mul_f32 v136, v136, v196\nMaav 76,140,216\nE 243,155\nv_mul_f32 v137, v137, v196\nv_mul_f32 v138, v138, v196\nMaav 108,140,220\nC 205,242,243\nRa 176,5,4096\n" \
"v_mul_f32 v139, v139, v196\nMaav 104,136,220\nE 244,156\nDS 0,60,s77,2048\nv_mul_f32 v140, v140, v196\nv_mul_f32 v141, v141, v196\nMaav 112,144,220\nRa 180,5,5120\n" \
"Ra 184,5,6144\nRa 188,5,7168\nv_mul_f32 v142, v142, v196\nMaav 116,148,220\nE 245,157\nv_mul_f32 v143, v143, v196\nC 44,136,137\nMaav 84,148,216\n" \
"C 206,244,245\nDS 0,60,s77,3072\nC 45,138,139\nMaav 80,144,216\nE 246,158\nC 46,140,141\nC 47,142,143\nMaav 88,152,216\n" \
"buffer_store_dwordx4 v[44:47], v0, s[60:63], s95 offen offset:1024 sc1\nMaav 92,156,216\nE 247,159\nMaav 124,156,220\nC 207,246,247\ns_waitcnt lgkmcnt(0)\nMaav 120,152,220\nQvav 144,160,64,12\n" \
"E 240,176\nRa 128,6,49152\nMvav 144,164,68\nE 241,177\nMvav 144,168,72\nC 216,240,241\nRa 132,6,50176\ns_add_u32 s95, s83, 8192\n" \
"AR 128,32\nMvav 144,172,76\nE 242,178\ns_add_u32 m0, s75, 0xc000\nAR 129,33\nAR 130,34\nQvav 152,160,80,12\nDS 8,60,s77,0\n" \
"Ra 136,6,51200\nAR 131,35\nMvav 152,164,84\nE 243,179\nAR 132,36\nAR 133,37\nMvav 152,168,88\nC 217,242,243\n" \
"Ra 140,6,52224\nAR 134,38\nMvav 152,172,92\nE 244,180\nAR 135,39\nv_mul_f32 v128, v128, v197\nQvav 148,176,64,12\nDS 8,60,s77,1024\n" \
"Ra 144,6,53248\nv_mul_f32 v129, v129, v197\nMvav 148,180,68\nE 245,181\nv_mul_f32 v130, v130, v197\nv_mul_f32 v131, v131, v197\nMvav 148,184,72\nC 218,244,245\n" \
"Ra 148,6,54272\nv_mul_f32 v132, v132, v197\nMvav 148,188,76\nE 246,182\nv_mul_f32 v133, v133, v197\nv_mul_f32 v134, v134, v197\nQvav 156,176,80,12\nRa 152,6,55296\n" \
"Ra 156,6,56320\nv_mul_f32 v135, v135, v197\nMvav 156,180,84\nE 247,183\nC 48,128,129\nC 49,130,131\nMvav 156,184,88\nC 219,246,247\n" \
"C 50,132,133\nMvav 156,188,92\nE 240,184\nC 51,134,135\nbuffer_store_dwordx4 v[48:51], v0, s[60:63], s95 offen offset:0 sc1\nv_smfmac_f32_16x16x64_bf16 v[100:103], v[104:107], v[200:207], v7\ns_waitcnt lgkmcnt(0)\nAR 136,40\n" \
"AR 137,41\nMaav 96,128,204\nDS 8,60,s77,2048\nRa 160,5,8192\nAR 138,42\nMaav 100,132,204\nE 241,185\nAR 139,43\n" \
"AR 140,44\nMaav 68,132,200\nC 220,240,241\nRa 164,5,9216\nAR 141,45\nMaav 64,128,200\nE 242,186\nAR 142,46\n" \
"AR 143,47\nMaav 72,136,200\nDS 8,60,s77,3072\nRa 168,5,10240\nv_mul_f32 v136, v136, v197\nMaav 76,140,200\nE 243,187\nv_mul_f32 v137, v137, v197\n" \
"v_mul_f32 v138, v138, v197\nMaav 108,140,204\nC 221,242,243\nRa 172,5,11264\nv_mul_f32 v139, v139, v197\nMaav 104,136,204\nE 244,188\nv_mul_f32 v140, v140, v197\n" \
"v_mul_f32 v141, v141, v197\nMaav 112,144,204\nRa 176,5,12288\nRa 180,5,13312\nRa 184,5,14336\nv_mul_f32 v142, v142, v197\nMaav 116,148,204\nE 245,189\n" \
"v_mul_f32 v143, v143, v197\nC 52,136,137\nMaav 84,148,200\nC 222,244,245\nRa 188,5,15360\nC 53,138,139\nMaav 80,144,200\nE 246,190\n" \
"C 54,140,141\nC 55,142,143\nMaav 88,152,200\ns_mov_b32 s76, s73\ns_mov_b32 s73, s74\nbuffer_store_dwordx4 v[52:55], v0, s[60:63], s95 offen offset:1024 sc1\nMaav 92,156,200\nE 247,191\n" \
"Maav 124,156,204\nC 223,246,247\ns_waitcnt lgkmcnt(0)\nMaav 120,152,204\ns_mov_b32 s74, s75\ns_mov_b32 s75, s76\ns_add_u32 s77, s77, 0x4000\nQvav 176,160,64,12\n" \
"E 240,144\nMvav 176,164,68\nE 241,145\nMvav 176,168,72\nC 200,240,241\nRa 128,6,57344\ns_add_u32 s95, s83, 40960\nAR 128,48\n" \
"Mvav 176,172,76\nE 242,146\nAR 129,49\nAR 130,50\nQvav 184,160,80,12\nRa 132,6,58368\nRa 136,6,59392\nAR 131,51\n" \
"Mvav 184,164,84\nE 243,147\nAR 132,52\nAR 133,53\nMvav 184,168,88\nC 201,242,243\nRa 140,6,60416\nAR 134,54\n" \
"Mvav 184,172,92\nE 244,148\nAR 135,55\nv_mul_f32 v128, v128, v197\nQvav 180,176,64,12\nRa 144,6,61440\nRa 148,6,62464\nv_mul_f32 v129, v129, v197\n" \
"Mvav 180,180,68\nE 245,149\nv_mul_f32 v130, v130, v197\nv_mul_f32 v131, v131, v197\nMvav 180,184,72\nC 202,244,245\nRa 152,6,63488\nv_mul_f32 v132, v132, v197\n" \
"Mvav 180,188,76\nE 246,150\nv_mul_f32 v133, v133, v197\nv_mul_f32 v134, v134, v197\nQvav 188,176,80,12\nRa 156,6,64512\nv_mul_f32 v135, v135, v197\nMvav 188,180,84\n" \
"E 247,151\nC 56,128,129\nC 57,130,131\nMvav 188,184,88\nC 203,246,247\nv_mov_b32 v6, v5\nC 58,132,133\nMvav 188,188,92\n" \
"E 240,152\nC 59,134,135\nbuffer_store_dwordx4 v[56:59], v0, s[60:63], s95 offen offset:0 sc1\nv_smfmac_f32_16x16x64_bf16 v[100:103], v[104:107], v[216:223], v7\ns_waitcnt vmcnt(14) lgkmcnt(0)\ns_barrier\nv_add_u32 v5, s74, v9\nAR 136,56\n" \
"AR 137,57\nMaav 96,128,220\ns_mov_b32 m0, s73\nRa 160,5,0\nRa 164,5,1024\nAR 138,58\nMaav 100,132,220\nE 241,153\n" \
"DS 0,60,s77,0\nAR 139,59\nAR 140,60\nMaav 68,132,216\nC 204,240,241\nRa 168,5,2048\nAR 141,61\nMaav 64,128,216\n" \
"E 242,154\nAR 142,62\nAR 143,63\nMaav 72,136,216\nDS 0,60,s77,1024\nRa 172,5,3072\nv_mul_f32 v136, v136, v197\nMaav 76,140,216\n" \
"E 243,155\nv_mul_f32 v137, v137, v197\nv_mul_f32 v138, v138, v197\nMaav 108,140,220\nC 205,242,243\nRa 176,5,4096\nv_mul_f32 v139, v139, v197\nMaav 104,136,220\n" \
"E 244,156\nDS 0,60,s77,2048\nv_mul_f32 v140, v140, v197\nv_mul_f32 v141, v141, v197\nMaav 112,144,220\nRa 180,5,5120\nRa 184,5,6144\nRa 188,5,7168\n" \
"v_mul_f32 v142, v142, v197\nMaav 116,148,220\nE 245,157\nv_mul_f32 v143, v143, v197\nC 60,136,137\nMaav 84,148,216\nC 206,244,245\nDS 0,60,s77,3072\n" \
"C 61,138,139\nMaav 80,144,216\nE 246,158\nC 62,140,141\nC 63,142,143\nMaav 88,152,216\nbuffer_store_dwordx4 v[60:63], v0, s[60:63], s95 offen offset:1024 sc1\nMaav 92,156,216\n" \
"E 247,159\nMaav 124,156,220\nC 207,246,247\ns_waitcnt lgkmcnt(0)\nMaav 120,152,220\ns_branch 99f\n.p2align 12\nzp13:\n" \
"s_setpc_b64 s[98:99]\n99:\nzH:\nQvav 144,160,64,12\nE 240,176\nRa 128,6,49152\nMvav 144,164,68\nE 241,177\n" \
"Mvav 144,168,72\nC 216,240,241\nRa 132,6,50176\nMvav 144,172,76\nE 242,178\ns_add_u32 m0, s75, 0xc000\nQvav 152,160,80,12\nDS 8,60,s77,0\n" \
"Ra 136,6,51200\nMvav 152,164,84\nE 243,179\nMvav 152,168,88\nC 217,242,243\nRa 140,6,52224\nMvav 152,172,92\nE 244,180\n" \
"Qvav 148,176,64,12\nDS 8,60,s77,1024\nRa 144,6,53248\nMvav 148,180,68\nE 245,181\nMvav 148,184,72\nC 218,244,245\nRa 148,6,54272\n" \
"Mvav 148,188,76\nE 246,182\nQvav 156,176,80,12\nRa 152,6,55296\nRa 156,6,56320\nMvav 156,180,84\nE 247,183\nMvav 156,184,88\n" \
"C 219,246,247\nMvav 156,188,92\nE 240,184\nv_smfmac_f32_16x16x64_bf16 v[100:103], v[104:107], v[200:207], v7\ns_waitcnt lgkmcnt(0)\nMaav 96,128,204\nDS 8,60,s77,2048\nRa 160,5,8192\n" \
"Maav 100,132,204\nE 241,185\nMaav 68,132,200\nC 220,240,241\nRa 164,5,9216\nMaav 64,128,200\nE 242,186\nMaav 72,136,200\n" \
"DS 8,60,s77,3072\nRa 168,5,10240\nMaav 76,140,200\nE 243,187\nMaav 108,140,204\nC 221,242,243\nRa 172,5,11264\nMaav 104,136,204\n" \
"E 244,188\nMaav 112,144,204\nRa 176,5,12288\nRa 180,5,13312\nRa 184,5,14336\nMaav 116,148,204\nE 245,189\nMaav 84,148,200\n" \
"C 222,244,245\nRa 188,5,15360\nMaav 80,144,200\nE 246,190\nMaav 88,152,200\ns_mov_b32 s76, s73\ns_mov_b32 s73, s74\nMaav 92,156,200\n" \
"E 247,191\nMaav 124,156,204\nC 223,246,247\ns_waitcnt lgkmcnt(0)\nMaav 120,152,204\ns_mov_b32 s74, s75\ns_mov_b32 s75, s76\ns_add_u32 s77, s77, 0x4000\n" \
"Qvav 176,160,64,12\nE 240,144\nMvav 176,164,68\nE 241,145\nMvav 176,168,72\nC 200,240,241\nRa 128,6,57344\nMvav 176,172,76\n" \
"E 242,146\nQvav 184,160,80,12\nRa 132,6,58368\nRa 136,6,59392\nMvav 184,164,84\nE 243,147\nMvav 184,168,88\nC 201,242,243\n" \
"Ra 140,6,60416\nMvav 184,172,92\nE 244,148\nQvav 180,176,64,12\nRa 144,6,61440\nRa 148,6,62464\nMvav 180,180,68\nE 245,149\n" \
"Mvav 180,184,72\nC 202,244,245\nRa 152,6,63488\nMvav 180,188,76\nE 246,150\nQvav 188,176,80,12\nRa 156,6,64512\nMvav 188,180,84\n" \
"E 247,151\nMvav 188,184,88\nC 203,246,247\nv_mov_b32 v6, v5\nMvav 188,188,92\nE 240,152\nv_smfmac_f32_16x16x64_bf16 v[100:103], v[104:107], v[216:223], v7\ns_waitcnt vmcnt(8) lgkmcnt(0)\n" \
"s_barrier\nv_add_u32 v5, s74, v9\nMaav 96,128,220\ns_mov_b32 m0, s73\nRa 160,5,0\nRa 164,5,1024\nMaav 100,132,220\nE 241,153\n" \
"DS 0,60,s77,0\nMaav 68,132,216\nC 204,240,241\nRa 168,5,2048\nMaav 64,128,216\nE 242,154\nMaav 72,136,216\nDS 0,60,s77,1024\n" \
"Ra 172,5,3072\nMaav 76,140,216\nE 243,155\nMaav 108,140,220\nC 205,242,243\nRa 176,5,4096\nMaav 104,136,220\nE 244,156\n" \
"DS 0,60,s77,2048\nMaav 112,144,220\nRa 180,5,5120\nRa 184,5,6144\nRa 188,5,7168\nMaav 116,148,220\nE 245,157\nMaav 84,148,216\n" \
"C 206,244,245\ns_sub_u32 s96, s96, 1\nDS 0,60,s77,3072\nMaav 80,144,216\nE 246,158\nMaav 88,152,216\ns_cmp_lg_u32 s96, 0\nMaav 92,156,216\n" \
"E 247,159\nMaav 124,156,220\nC 207,246,247\ns_waitcnt lgkmcnt(0)\nMaav 120,152,220\ns_cbranch_scc1 zH\nQvav 144,160,64,112\nE 240,176\n" \
"Ra 128,6,49152\nMvav 144,164,68\nE 241,177\nMvav 144,168,72\nC 216,240,241\nRa 132,6,50176\nMvav 144,172,76\nE 242,178\n" \
"s_add_u32 m0, s75, 0xc000\nQvav 152,160,80,12\nDS 8,60,s77,0\nRa 136,6,51200\nMvav 152,164,84\nE 243,179\nMvav 152,168,88\nC 217,242,243\n" \
"Ra 140,6,52224\nMvav 152,172,92\nE 244,180\nQvav 148,176,64,116\nDS 8,60,s77,1024\nRa 144,6,53248\nMvav 148,180,68\nE 245,181\n" \
"Mvav 148,184,72\nC 218,244,245\nRa 148,6,54272\nMvav 148,188,76\nE 246,182\nQvav 156,176,80,12\nRa 152,6,55296\nRa 156,6,56320\n" \
"Mvav 156,180,84\nE 247,183\nMvav 156,184,88\nC 219,246,247\nMvav 156,188,92\nE 240,184\nv_smfmac_f32_16x16x64_bf16 v[100:103], v[104:107], v[200:207], v7\ns_waitcnt lgkmcnt(0)\n" \
"Maav 96,128,204\nDS 8,60,s77,2048\nRa 160,5,8192\nMaav 100,132,204\nE 241,185\nMaav 68,132,200\nC 220,240,241\nRa 164,5,9216\n" \
"Maav 64,128,200\nE 242,186\nMaav 72,136,200\nDS 8,60,s77,3072\nRa 168,5,10240\nMaav 76,140,200\nE 243,187\nMaav 108,140,204\n" \
"C 221,242,243\nRa 172,5,11264\nMaav 104,136,204\nE 244,188\nMaav 112,144,204\nRa 176,5,12288\nRa 180,5,13312\nRa 184,5,14336\n" \
"Maav 116,148,204\nE 245,189\nMaav 84,148,200\nC 222,244,245\nRa 188,5,15360\nMaav 80,144,200\nE 246,190\nMaav 88,152,200\n" \
"s_mov_b32 s76, s73\ns_mov_b32 s73, s74\nMaav 92,156,200\nE 247,191\nMaav 124,156,204\nC 223,246,247\ns_waitcnt lgkmcnt(0)\nMaav 120,152,204\n" \
"s_mov_b32 s74, s75\ns_mov_b32 s75, s76\ns_add_u32 s77, s77, 0x4000\nQvav 176,160,64,120\nE 240,144\nMvav 176,164,68\nE 241,145\nMvav 176,168,72\n" \
"C 200,240,241\nRa 128,6,57344\nMvav 176,172,76\nE 242,146\nQvav 184,160,80,12\nRa 132,6,58368\nRa 136,6,59392\nMvav 184,164,84\n" \
"E 243,147\nMvav 184,168,88\nC 201,242,243\nRa 140,6,60416\nMvav 184,172,92\nE 244,148\nQvav 180,176,64,124\nRa 144,6,61440\n" \
"Ra 148,6,62464\nMvav 180,180,68\nE 245,149\nMvav 180,184,72\nC 202,244,245\nRa 152,6,63488\nMvav 180,188,76\nE 246,150\n" \
"Qvav 188,176,80,12\nRa 156,6,64512\nMvav 188,180,84\nE 247,151\nMvav 188,184,88\nC 203,246,247\nv_mov_b32 v6, v5\nMvav 188,188,92\n" \
"E 240,152\nv_smfmac_f32_16x16x64_bf16 v[100:103], v[104:107], v[216:223], v7\ns_waitcnt vmcnt(8) lgkmcnt(0)\ns_barrier\nv_add_u32 v5, s74, v9\nMaav 96,128,220\nRa 160,5,0\nRa 164,5,1024\n" \
"Maav 100,132,220\nE 241,153\nMaav 68,132,216\nC 204,240,241\nRa 168,5,2048\nMaav 64,128,216\nE 242,154\nMaav 72,136,216\n" \
"Ra 172,5,3072\nRa 176,5,4096\nMaav 76,140,216\nE 243,155\nMaav 108,140,220\nC 205,242,243\nRa 180,5,5120\nMaav 104,136,220\n" \
"E 244,156\nMaav 112,144,220\nRa 184,5,6144\nRa 188,5,7168\nMaav 116,148,220\nE 245,157\nMaav 84,148,216\nC 206,244,245\n" \
"Maav 80,144,216\nE 246,158\nMaav 88,152,216\nMaav 92,156,216\nE 247,159\nMaav 124,156,220\nC 207,246,247\ns_waitcnt lgkmcnt(0)\n" \
"Maav 120,152,220\nzE3:\nQvav 152,160,80,112\nE 240,176\nRa 128,6,49152\nRa 132,6,50176\nMvav 152,164,84\nE 241,177\n" \
"s_add_u32 m0, s32, 147456\nRa 136,6,51200\nMvav 152,168,88\nC 216,240,241\nE 242,178\nD 18,68,0,0\nRa 140,6,52224\nMvav 152,172,92\n" \
"Ra 144,6,53248\nRa 148,6,54272\nRa 152,6,55296\nQvav 156,176,80,116\nE 243,179\nRa 156,6,56320\nMvav 156,180,84\nC 217,242,243\n" \
"E 244,180\nMvav 156,184,88\nD 18,68,s89,1024\nMvav 156,188,92\nE 245,181\nv_smfmac_f32_16x16x64_bf16 v[100:103], v[104:107], v[200:207], v7\nC 218,244,245\ns_waitcnt lgkmcnt(0)\n" \
"Maav 96,128,204\nE 246,182\nRa 160,5,8192\nMaav 100,132,204\nD 19,68,s90,2048\nMaav 68,132,200\nE 247,183\nRa 164,5,9216\n" \
"Maav 64,128,200\nC 219,246,247\nE 240,184\nMaav 72,136,200\nE 241,185\nRa 168,5,10240\nMaav 76,140,200\nC 220,240,241\n" \
"D 19,68,s91,3072\nMaav 108,140,204\nE 242,186\nRa 172,5,11264\nMaav 104,136,204\nE 243,187\nMaav 112,144,204\nC 221,242,243\n" \
"Ra 176,5,12288\nRa 180,5,13312\nMaav 116,148,204\nE 244,188\nMaav 84,148,200\nE 245,189\nRa 184,5,14336\nMaav 80,144,200\n" \
"C 222,244,245\nRa 188,5,15360\nMaav 88,152,200\nE 246,190\nMaav 92,156,200\nE 247,191\nMaav 124,156,204\nC 223,246,247\n" \
"s_waitcnt lgkmcnt(0)\nMaav 120,152,204\nQvav 184,160,80,120\nRa 128,6,57344\nMvav 184,164,84\nRa 132,6,58368\nMvav 184,168,88\nRa 136,6,59392\n" \
"Mvav 184,172,92\nRa 140,6,60416\nQvav 188,176,80,124\nRa 144,6,61440\nMvav 188,180,84\nRa 148,6,62464\nMvav 188,184,88\nRa 152,6,63488\n" \
"Mvav 188,188,92\nRa 156,6,64512\nv_smfmac_f32_16x16x64_bf16 v[100:103], v[104:107], v[216:223], v7\ns_waitcnt vmcnt(4) lgkmcnt(0)\ns_barrier\nv_mov_b32 v6, v5\nMaav 96,128,220\nRa 160,6,49152\n" \
"Maav 100,132,220\nE 240,152\nE 241,153\nMaav 68,132,216\nRa 164,6,50176\nC 204,240,241\nMaav 64,128,216\nE 242,154\n" \
"Maav 72,136,216\nRa 168,6,51200\nMaav 76,140,216\nE 243,155\nMaav 108,140,220\nRa 172,6,52224\nC 205,242,243\nMaav 104,136,220\n" \
"E 244,156\nMaav 112,144,220\nRa 176,6,53248\nMaav 116,148,220\nE 245,157\nMaav 84,148,216\nRa 180,6,54272\nC 206,244,245\n" \
"Maav 80,144,216\nE 246,158\nMaav 88,152,216\nRa 184,6,55296\nMaav 92,156,216\nE 247,159\nMaav 124,156,220\nRa 188,6,56320\n" \
"C 207,246,247\nMaav 120,152,220\ns_waitcnt lgkmcnt(0)\nE 240,184\nMvvv 100,108,204\nRa 128,6,57344\nE 241,185\nMaav 96,160,204\n" \
"Ra 132,6,58368\nC 192,240,241\nE 242,186\nMaav 100,164,204\nRa 136,6,59392\nE 243,187\nMaav 104,168,204\nRa 140,6,60416\n" \
"C 193,242,243\nE 244,188\nMaav 108,172,204\nRa 144,6,61440\nE 245,189\nMaav 112,176,204\nRa 148,6,62464\nC 194,244,245\n" \
"E 246,190\nMaav 116,180,204\nRa 152,6,63488\nE 247,191\nMaav 120,184,204\nRa 156,6,64512\nC 195,246,247\nMaav 124,188,204\n" \
"s_nop 0\ns_waitcnt lgkmcnt(0)\nMvvv 100,108,192\nMaav 96,128,192\nMaav 100,132,192\nMaav 104,136,192\nMaav 108,140,192\nMaav 112,144,192\n" \
"Maav 116,148,192\nMaav 120,152,192\nv_add_f32 v104, v96, v97\nv_add_f32 v105, v98, v99\nv_add_f32 v106, v100, v101\nv_add_f32 v107, v102, v103\nMaav 124,156,192\nv_subrev_u32 v240, 0xf800000, v104\n" \
"v_subrev_u32 v241, 0xf800000, v105\nv_subrev_u32 v242, 0xf800000, v106\nv_subrev_u32 v243, 0xf800000, v107\nv_max3_u32 v244, v240, v241, v242\nv_max_u32 v244, v244, v243\nv_cmp_lt_u32 vcc, 0x3c000000, v244\ns_cbranch_vccnz zfb\nzend:\n" \
"s_nop 4\nv_rcp_f32 v110, v106\nv_rcp_f32 v111, v107\nv_xor_b32 v20, s21, v20\nv_xor_b32 v21, s21, v21\nv_xor_b32 v22, s21, v22\nv_xor_b32 v23, s21, v23\nv_xor_b32 v24, s21, v24\n" \
"v_xor_b32 v25, s21, v25\nv_xor_b32 v26, s21, v26\nv_xor_b32 v27, s21, v27\ns_add_u32 s95, s83, 16384\nAR 112,64\nAR 113,65\nAR 114,66\nAR 115,67\n" \
"AR 116,68\nAR 117,69\nAR 118,70\nAR 119,71\nv_pk_mul_f32 v[112:113], v[112:113], v[110:111] op_sel:[0,0] op_sel_hi:[1,0]\nv_pk_mul_f32 v[114:115], v[114:115], v[110:111] op_sel:[0,0] op_sel_hi:[1,0]\nv_pk_mul_f32 v[116:117], v[116:117], v[110:111] op_sel:[0,0] op_sel_hi:[1,0]\nv_pk_mul_f32 v[118:119], v[118:119], v[110:111] op_sel:[0,0] op_sel_hi:[1,0]\n" \
"C 64,112,113\nC 65,114,115\nC 66,116,117\nC 67,118,119\nTa 192,20,32768\nTa 194,20,33792\nTa 224,20,40960\nTa 226,20,41984\n" \
"buffer_store_dwordx4 v[64:67], v0, s[60:63], s95 offen offset:0 sc1\nAR 120,72\nAR 121,73\nAR 122,74\nAR 123,75\nAR 124,76\nAR 125,77\nAR 126,78\n" \
"AR 127,79\nv_pk_mul_f32 v[120:121], v[120:121], v[110:111] op_sel:[0,0] op_sel_hi:[1,0]\nv_pk_mul_f32 v[122:123], v[122:123], v[110:111] op_sel:[0,0] op_sel_hi:[1,0]\nv_pk_mul_f32 v[124:125], v[124:125], v[110:111] op_sel:[0,0] op_sel_hi:[1,0]\nv_pk_mul_f32 v[126:127], v[126:127], v[110:111] op_sel:[0,0] op_sel_hi:[1,0]\nC 68,120,121\nC 69,122,123\nC 70,124,125\n" \
"C 71,126,127\nTa 196,21,32768\nTa 198,21,33792\nTa 228,21,40960\nTa 230,21,41984\nbuffer_store_dwordx4 v[68:71], v0, s[60:63], s95 offen offset:1024 sc1\ns_add_u32 s95, s83, 24576\nAR 112,96\n" \
"AR 113,97\nAR 114,98\nAR 115,99\nAR 116,100\nAR 117,101\nAR 118,102\nAR 119,103\nv_pk_mul_f32 v[112:113], v[112:113], v[110:111] op_sel:[0,1] op_sel_hi:[1,1]\n" \
"v_pk_mul_f32 v[114:115], v[114:115], v[110:111] op_sel:[0,1] op_sel_hi:[1,1]\nv_pk_mul_f32 v[116:117], v[116:117], v[110:111] op_sel:[0,1] op_sel_hi:[1,1]\nv_pk_mul_f32 v[118:119], v[118:119], v[110:111] op_sel:[0,1] op_sel_hi:[1,1]\nC 80,112,113\nC 81,114,115\ns_branch 99f\n.p2align 12\nzq0:\n" \
"zp14:\ns_setpc_b64 s[98:99]\n99:\nC 82,116,117\nC 83,118,119\nTa 200,22,32768\nTa 202,22,33792\nTa 232,22,40960\n" \
"Ta 234,22,41984\nbuffer_store_dwordx4 v[80:83], v0, s[60:63], s95 offen offset:0 sc1\nAR 120,104\nAR 121,105\nAR 122,106\nAR 123,107\nAR 124,108\nAR 125,109\n" \
"AR 126,110\nAR 127,111\nv_pk_mul_f32 v[120:121], v[120:121], v[110:111] op_sel:[0,1] op_sel_hi:[1,1]\nv_pk_mul_f32 v[122:123], v[122:123], v[110:111] op_sel:[0,1] op_sel_hi:[1,1]\nv_pk_mul_f32 v[124:125], v[124:125], v[110:111] op_sel:[0,1] op_sel_hi:[1,1]\nv_pk_mul_f32 v[126:127], v[126:127], v[110:111] op_sel:[0,1] op_sel_hi:[1,1]\nC 84,120,121\nC 85,122,123\n" \
"C 86,124,125\nC 87,126,127\nTa 204,23,32768\nTa 206,23,33792\nTa 236,23,40960\nTa 238,23,41984\nbuffer_store_dwordx4 v[84:87], v0, s[60:63], s95 offen offset:1024 sc1\ns_add_u32 s95, s83, 49152\n" \
"AR 112,80\nAR 113,81\nAR 114,82\nAR 115,83\nAR 116,84\nAR 117,85\nAR 118,86\nAR 119,87\n" \
"v_pk_mul_f32 v[112:113], v[112:113], v[110:111] op_sel:[0,0] op_sel_hi:[1,0]\nv_pk_mul_f32 v[114:115], v[114:115], v[110:111] op_sel:[0,0] op_sel_hi:[1,0]\nv_pk_mul_f32 v[116:117], v[116:117], v[110:111] op_sel:[0,0] op_sel_hi:[1,0]\nv_pk_mul_f32 v[118:119], v[118:119], v[110:111] op_sel:[0,0] op_sel_hi:[1,0]\nC 72,112,113\nC 73,114,115\nC 74,116,117\nC 75,118,119\n" \
"Ta 208,24,32768\nTa 210,24,33792\nTa 240,24,40960\nTa 242,24,41984\nbuffer_store_dwordx4 v[72:75], v0, s[60:63], s95 offen offset:0 sc1\nAR 120,88\nAR 121,89\nAR 122,90\n" \
"AR 123,91\nAR 124,92\nAR 125,93\nAR 126,94\nAR 127,95\nv_pk_mul_f32 v[120:121], v[120:121], v[110:111] op_sel:[0,0] op_sel_hi:[1,0]\nv_pk_mul_f32 v[122:123], v[122:123], v[110:111] op_sel:[0,0] op_sel_hi:[1,0]\nv_pk_mul_f32 v[124:125], v[124:125], v[110:111] op_sel:[0,0] op_sel_hi:[1,0]\n" \
"v_pk_mul_f32 v[126:127], v[126:127], v[110:111] op_sel:[0,0] op_sel_hi:[1,0]\nC 76,120,121\nC 77,122,123\nC 78,124,125\nC 79,126,127\nTa 212,25,32768\nTa 214,25,33792\nTa 244,25,40960\n" \
"Ta 246,25,41984\nbuffer_store_dwordx4 v[76:79], v0, s[60:63], s95 offen offset:1024 sc1\ns_add_u32 s95, s83, 57344\nAR 112,112\nAR 113,113\nAR 114,114\nAR 115,115\nAR 116,116\n" \
"AR 117,117\nAR 118,118\nAR 119,119\nv_pk_mul_f32 v[112:113], v[112:113], v[110:111] op_sel:[0,1] op_sel_hi:[1,1]\nv_pk_mul_f32 v[114:115], v[114:115], v[110:111] op_sel:[0,1] op_sel_hi:[1,1]\nv_pk_mul_f32 v[116:117], v[116:117], v[110:111] op_sel:[0,1] op_sel_hi:[1,1]\nv_pk_mul_f32 v[118:119], v[118:119], v[110:111] op_sel:[0,1] op_sel_hi:[1,1]\nC 88,112,113\n" \
"C 89,114,115\nC 90,116,117\nC 91,118,119\nTa 216,26,32768\nTa 218,26,33792\nTa 248,26,40960\nTa 250,26,41984\nbuffer_store_dwordx4 v[88:91], v0, s[60:63], s95 offen offset:0 sc1\n" \
"AR 120,120\nAR 121,121\nAR 122,122\nAR 123,123\nAR 124,124\nAR 125,125\nAR 126,126\nAR 127,127\n" \
"v_pk_mul_f32 v[120:121], v[120:121], v[110:111] op_sel:[0,1] op_sel_hi:[1,1]\nv_pk_mul_f32 v[122:123], v[122:123], v[110:111] op_sel:[0,1] op_sel_hi:[1,1]\nv_pk_mul_f32 v[124:125], v[124:125], v[110:111] op_sel:[0,1] op_sel_hi:[1,1]\nv_pk_mul_f32 v[126:127], v[126:127], v[110:111] op_sel:[0,1] op_sel_hi:[1,1]\nC 92,120,121\nC 93,122,123\nC 94,124,125\nC 95,126,127\n" \
"Ta 220,27,32768\nTa 222,27,33792\nTa 252,27,40960\nTa 254,27,41984\nbuffer_store_dwordx4 v[92:95], v0, s[60:63], s95 offen offset:1024 sc1\ns_waitcnt lgkmcnt(0)\nZvav 128,192,32\nv_lshrrev_b32 v28, 1, v0\n" \
"s_lshl_b32 s82, s10, 3\nMvav 128,224,36\nv_xor_b32 v29, s82, v28\nv_and_b32 v28, 0x78, v28\nZvav 132,228,36\nv_add_u32 v29, s4, v29\nv_add_u32 v28, s5, v28\nMvav 132,196,32\n" \
"v_cmp_gt_u32 vcc, 512, v0\ns_nop 0\nZvav 136,200,32\nv_cndmask_b32 v1, v28, v29, vcc\ns_mov_b32 s79, s78\nMvav 136,232,36\ns_xor_b32 s82, s79, s10\ns_lshl_b32 s81, s82, 16\n" \
"Zvav 140,236,36\ns_cmp_lt_u32 s79, 32\ns_cselect_b32 s82, s18, s19\nMvav 140,204,32\ns_add_u32 s81, s81, s82\nZvav 144,208,32\nMvav 144,240,36\nZvav 148,244,36\n" \
"Mvav 148,212,32\nZvav 152,216,32\nMvav 152,248,36\nZvav 156,252,36\nMvav 156,220,32\nZvav 188,220,48\nMvav 188,252,52\nZvav 184,248,52\n" \
"Mvav 184,216,48\nZvav 180,212,48\ns_ff1_i32_b64 s78, s[8:9]\ns_bitset0_b64 s[8:9], s78\nMvav 180,244,52\ns_xor_b32 s82, s78, s10\ns_and_b32 s80, s82, 31\nZvav 176,240,52\n" \
"s_lshl_b32 s80, s80, 18\ns_cmp_lt_u32 s78, 32\nMvav 176,208,48\ns_cselect_b64 s[68:69], s[14:15], s[12:13]\ns_cselect_b32 s70, 0x800000, s92\nZvav 172,204,48\nv_add_u32 v18, s80, v16\nv_xor_b32 v19, 128, v18\n" \
"Mvav 172,236,52\nZvav 168,232,52\ns_waitcnt vmcnt(24)\ns_barrier\nMvav 168,200,48\nTa 128,20,49152\nTa 130,20,50176\nZvav 164,196,48\n" \
"Ta 160,20,57344\nMvav 164,228,52\nTa 162,20,58368\nZvav 160,224,52\nTa 132,21,49152\nMvav 160,192,48\nTa 134,21,50176\nZvav 192,192,64\n" \
"Ta 164,21,57344\nTa 166,21,58368\nMvav 192,224,68\nTa 136,22,49152\nZvav 196,228,68\nTa 138,22,50176\nMvav 196,196,64\nTa 168,22,57344\n" \
"Zvav 200,200,64\nTa 170,22,58368\nMvav 200,232,68\nTa 140,23,49152\nTa 142,23,50176\nZvav 204,236,68\nTa 172,23,57344\nMvav 204,204,64\n" \
"Ta 174,23,58368\nZvav 208,208,64\ns_add_u32 m0, s84, 0\nMvav 208,240,68\nD 18,68,0,0\nZvav 212,244,68\nTa 144,24,49152\nTa 146,24,50176\n" \
"Mvav 212,212,64\nTa 176,24,57344\nZvav 216,216,64\nTa 178,24,58368\nMvav 216,248,68\nD 18,68,s89,1024\nZvav 220,252,68\nTa 148,25,49152\n" \
"Mvav 220,220,64\nTa 150,25,50176\nZvav 252,220,80\nTa 180,25,57344\nTa 182,25,58368\nMvav 252,252,84\nTa 152,26,49152\nZvav 248,248,84\n" \
"Ta 154,26,50176\nMvav 248,216,80\nTa 184,26,57344\nZvav 244,212,80\nTa 186,26,58368\nMvav 244,244,84\nTa 156,27,49152\nTa 158,27,50176\n" \
"Zvav 240,240,84\ns_cmp_lg_u32 s32, 0\nMvav 240,208,80\nTa 188,27,57344\nZvav 236,204,80\ns_waitcnt vmcnt(2)\ns_barrier\ns_cbranch_scc1 zsn\n" \
"buffer_store_dwordx2 v[2:3], off, s[60:63], s31 sc1\nzsn:\nMvav 236,236,84\nD 19,68,s90,2048\nZvav 232,232,84\nTa 190,27,58368\nMvav 232,200,80\nZvav 228,196,80\n" \
"Mvav 228,228,84\nD 19,68,s91,3072\nZvav 224,224,84\nv_add_u32 v18, 131072, v18\nMvav 224,192,80\nv_add_u32 v19, 131072, v19\ns_waitcnt lgkmcnt(0)\nMvav 128,128,40\n" \
"Mvav 128,160,44\nMvav 132,164,44\nMvav 132,132,40\nMvav 136,136,40\nMvav 136,168,44\nMvav 140,172,44\nMvav 140,140,40\nMvav 144,144,40\n" \
"v_xor_b32 v20, 0x8000, v20\nv_xor_b32 v21, 0x8000, v21\nMvav 144,176,44\nv_xor_b32 v22, 0x8000, v22\nv_xor_b32 v23, 0x8000, v23\nMvav 148,180,44\nv_xor_b32 v24, 0x8000, v24\nv_xor_b32 v25, 0x8000, v25\n" \
"Mvav 148,148,40\nv_xor_b32 v26, 0x8000, v26\nv_xor_b32 v27, 0x8000, v27\nMvav 152,152,40\nMvav 152,184,44\nMvav 156,188,44\nMvav 156,156,40\nMvav 188,156,56\n" \
"buffer_load_dwordx2 v[10:11], v1, s[60:63], 0 offen sc1\nMvav 188,188,60\nMvav 184,184,60\nMvav 184,152,56\nMvav 180,148,56\nMvav 180,180,60\nMvav 176,176,60\nMvav 176,144,56\n" \
"Mvav 172,140,56\nMvav 172,172,60\nMvav 168,168,60\ns_waitcnt vmcnt(25)\ns_barrier\nMvav 168,136,56\nTa 192,20,32768\nTa 194,20,33792\n" \
"Ta 224,20,40960\nMvav 164,132,56\nTa 226,20,41984\nTa 196,21,32768\nMvav 164,164,60\nTa 198,21,33792\nTa 228,21,40960\nMvav 160,160,60\n" \
"Ta 230,21,41984\nTa 200,22,32768\nMvav 160,128,56\nTa 202,22,33792\nTa 232,22,40960\nMvav 192,128,72\nv_add_u32 v9, s81, v0\nTa 234,22,41984\n" \
"Mvav 192,160,76\ns_cmp_eq_u32 s32, 0\nTa 204,23,32768\nMvav 196,164,76\nTa 206,23,33792\nTa 236,23,40960\nMvav 196,132,72\nTa 238,23,41984\n" \
"Ta 208,24,32768\nMvav 200,136,72\ns_cbranch_scc1 zsk\ns_waitcnt vmcnt(0)\nv_cmp_eq_u64 vcc, v[10:11], v[2:3]\ns_andn2_b32 s82, s7, vcc_hi\ns_cselect_b64 vcc, 0, vcc\ns_bitcmp1_b64 vcc, s79\n" \
"s_cbranch_scc0 zs104\nzr104:\nzsk:\ns_barrier\ns_bitcmp1_b32 s33, 4\nMvav 200,168,76\ns_cbranch_scc1 zep\nzeq:\n" \
"Ta 210,24,33792\nMvav 204,172,76\nbuffer_load_dwordx4 a[0:3], v9, s[60:63], 0 offen offset:0 sc1\nMvav 204,140,72\nbuffer_load_dwordx4 a[4:7], v9, s[60:63], 0 offen offset:1024 sc1\nMvav 208,144,72\ns_add_u32 m0, s84, 16384\nTa 240,24,40960\n" \
"Mvav 208,176,76\nD 18,68,0,0\nbuffer_load_dwordx2 v[10:11], v1, s[60:63], 0 offen sc1\nMvav 212,180,76\ns_xor_b32 s84, s84, 0x38000\nbuffer_load_dwordx4 a[8:11], v9, s[60:63], s85 offen offset:0 sc1\nMvav 212,148,72\nTa 242,24,41984\n" \
"Ta 212,25,32768\nMvav 216,152,72\nbuffer_load_dwordx4 a[12:15], v9, s[60:63], s85 offen offset:1024 sc1\nMvav 216,184,76\nD 18,68,s89,1024\nbuffer_load_dwordx4 a[16:19], v9, s[60:63], s86 offen offset:0 sc1\nMvav 220,188,76\nTa 214,25,33792\n" \
"Ta 244,25,40960\nMvav 220,156,72\nbuffer_load_dwordx4 a[20:23], v9, s[60:63], s86 offen offset:1024 sc1\nMvav 252,156,88\nbuffer_load_dwordx4 a[24:27], v9, s[60:63], s87 offen offset:0 sc1\nMvav 252,188,92\nTa 246,25,41984\nTa 216,26,32768\n" \
"Mvav 248,184,92\nbuffer_load_dwordx4 a[28:31], v9, s[60:63], s87 offen offset:1024 sc1\nMvav 248,152,88\nv_add_u32 v9, 0x8000, v9\nTa 218,26,33792\nMvav 244,148,88\nTa 248,26,40960\nTa 250,26,41984\n" \
"Mvav 244,180,92\nbuffer_load_dwordx4 a[32:35], v9, s[60:63], 0 offen offset:0 sc1\nMvav 240,176,92\nbuffer_load_dwordx4 a[36:39], v9, s[60:63], 0 offen offset:1024 sc1\nMvav 240,144,88\nTa 220,27,32768\nTa 222,27,33792\nMvav 236,140,88\n" \
"buffer_load_dwordx4 a[40:43], v9, s[60:63], s85 offen offset:0 sc1\ns_mov_b32 s96, s22\nMvav 236,172,92\nD 19,68,s90,2048\nbuffer_load_dwordx4 a[44:47], v9, s[60:63], s85 offen offset:1024 sc1\ns_cmp_eq_u32 s96, 0\nMvav 232,168,92\nTa 252,27,40960\n" \
"Ta 254,27,41984\nMvav 232,136,88\nbuffer_load_dwordx4 a[48:51], v9, s[60:63], s86 offen offset:0 sc1\nMvav 228,132,88\nbuffer_load_dwordx4 a[52:55], v9, s[60:63], s86 offen offset:1024 sc1\nMvav 228,164,92\nD 19,68,s91,3072\nMvav 224,160,92\n" \
"Mvav 224,128,88\ns_waitcnt vmcnt(17) lgkmcnt(0)\ns_cbranch_scc1 zox\ns_branch 99f\n.p2align 12\nzp15:\ns_setpc_b64 s[98:99]\n99:\n" \
"zol:\nMvaa 128,192,0\ns_waitcnt vmcnt(10)\nv_cmp_eq_u64 vcc, v[10:11], v[2:3]\ns_mov_b32 s79, s78\nMvaa 128,224,4\nbuffer_load_dwordx4 a[56:59], v9, s[60:63], s87 offen offset:0 sc1\ns_xor_b32 s82, s79, s10\n" \
"Mvaa 132,228,4\ns_lshl_b32 s81, s82, 16\ns_cmp_lt_u32 s79, 32\ns_cselect_b32 s82, s18, s19\nMvaa 132,196,0\ns_add_u32 s81, s81, s82\ns_bitcmp1_b64 vcc, s79\nMvaa 136,200,0\n" \
"Mvaa 136,232,4\nbuffer_load_dwordx4 a[60:63], v9, s[60:63], s87 offen offset:1024 sc1\nMvaa 140,236,4\nMvaa 140,204,0\nv_add_u32 v9, s81, v0\nMvaa 144,208,0\nMvaa 144,240,4\nMvaa 148,244,4\n" \
"s_cbranch_scc0 zs107\nzr107:\nMvaa 148,212,0\ns_ff1_i32_b64 s78, s[8:9]\ns_bitset0_b64 s[8:9], s78\nMvaa 152,216,0\ns_xor_b32 s82, s78, s10\ns_and_b32 s80, s82, 31\n" \
"Mvaa 152,248,4\ns_lshl_b32 s80, s80, 18\ns_cmp_lt_u32 s78, 32\nMvaa 156,252,4\ns_cselect_b64 s[68:69], s[14:15], s[12:13]\ns_cselect_b32 s70, 0x800000, s92\nMvaa 156,220,0\nv_add_u32 v18, s80, v16\n" \
"v_xor_b32 v19, 128, v18\ns_waitcnt vmcnt(15)\nMvaa 188,220,8\nMvaa 188,252,12\nbuffer_load_dwordx4 a[0:3], v9, s[60:63], 0 offen offset:0 sc1\nMvaa 184,248,12\nMvaa 184,216,8\nMvaa 180,212,8\n" \
"Mvaa 180,244,12\nbuffer_load_dwordx4 a[4:7], v9, s[60:63], 0 offen offset:1024 sc1\nMvaa 176,240,12\nMvaa 176,208,8\nMvaa 172,204,8\nMvaa 172,236,12\nMvaa 168,232,12\ns_waitcnt vmcnt(29)\n" \
"s_barrier\nMvaa 168,200,8\nTa 128,20,49152\nTa 130,20,50176\nMvaa 164,196,8\nTa 160,20,57344\nMvaa 164,228,12\nTa 162,20,58368\n" \
"Ta 132,21,49152\nMvaa 160,224,12\nTa 134,21,50176\nMvaa 160,192,8\ns_waitcnt vmcnt(14)\nTa 164,21,57344\nMvaa 192,192,16\nTa 166,21,58368\n" \
"Mvaa 192,224,20\nbuffer_load_dwordx4 a[8:11], v9, s[60:63], s85 offen offset:0 sc1\nMvaa 196,228,20\nTa 136,22,49152\nMvaa 196,196,16\nTa 138,22,50176\nTa 168,22,57344\nMvaa 200,200,16\n" \
"Ta 170,22,58368\nMvaa 200,232,20\nbuffer_load_dwordx4 a[12:15], v9, s[60:63], s85 offen offset:1024 sc1\nMvaa 204,236,20\nTa 140,23,49152\nMvaa 204,204,16\nTa 142,23,50176\nTa 172,23,57344\n" \
"Mvaa 208,208,16\ns_add_u32 m0, s84, 0\nMvaa 208,240,20\nD 18,68,0,0\nMvaa 212,244,20\nTa 174,23,58368\nTa 144,24,49152\nMvaa 212,212,16\n" \
"Ta 146,24,50176\nMvaa 216,216,16\nTa 176,24,57344\nTa 178,24,58368\nMvaa 216,248,20\nD 18,68,s89,1024\nMvaa 220,252,20\nTa 148,25,49152\n" \
"Mvaa 220,220,16\ns_waitcnt vmcnt(16)\nTa 150,25,50176\nMvaa 252,220,24\nTa 180,25,57344\nMvaa 252,252,28\nbuffer_load_dwordx4 a[16:19], v9, s[60:63], s86 offen offset:0 sc1\nMvaa 248,248,28\n" \
"Ta 182,25,58368\nMvaa 248,216,24\nTa 152,26,49152\nTa 154,26,50176\nMvaa 244,212,24\nTa 184,26,57344\nMvaa 244,244,28\nbuffer_load_dwordx4 a[20:23], v9, s[60:63], s86 offen offset:1024 sc1\n" \
"Mvaa 240,240,28\nTa 186,26,58368\nMvaa 240,208,24\nTa 156,27,49152\nTa 158,27,50176\nMvaa 236,204,24\nTa 188,27,57344\nMvaa 236,236,28\n" \
"D 19,68,s90,2048\nMvaa 232,232,28\nTa 190,27,58368\nMvaa 232,200,24\nMvaa 228,196,24\nMvaa 228,228,28\nD 19,68,s91,3072\nMvaa 224,224,28\n" \
"v_add_u32 v18, 131072, v18\nMvaa 224,192,24\nv_add_u32 v19, 131072, v19\ns_waitcnt vmcnt(18) lgkmcnt(0)\nMvaa 128,128,32\nMvaa 128,160,36\nbuffer_load_dwordx4 a[24:27], v9, s[60:63], s87 offen offset:0 sc1\nMvaa 132,164,36\n" \
"Mvaa 132,132,32\nMvaa 136,136,32\nMvaa 136,168,36\nbuffer_load_dwordx4 a[28:31], v9, s[60:63], s87 offen offset:1024 sc1\nMvaa 140,172,36\nMvaa 140,140,32\nv_add_u32 v9, 0x8000, v9\nMvaa 144,144,32\n" \
"v_xor_b32 v20, 0x8000, v20\nv_xor_b32 v21, 0x8000, v21\nMvaa 144,176,36\nv_xor_b32 v22, 0x8000, v22\nv_xor_b32 v23, 0x8000, v23\nMvaa 148,180,36\nv_xor_b32 v24, 0x8000, v24\nv_xor_b32 v25, 0x8000, v25\n" \
"Mvaa 148,148,32\nbuffer_load_dwordx2 v[10:11], v1, s[60:63], 0 offen sc1\nMvaa 152,152,32\nv_xor_b32 v26, 0x8000, v26\nv_xor_b32 v27, 0x8000, v27\nMvaa 152,184,36\nMvaa 156,188,36\nMvaa 156,156,32\n" \
"s_waitcnt vmcnt(18)\nMvaa 188,156,40\nMvaa 188,188,44\nbuffer_load_dwordx4 a[32:35], v9, s[60:63], 0 offen offset:0 sc1\nMvaa 184,184,44\nMvaa 184,152,40\nMvaa 180,148,40\nMvaa 180,180,44\n" \
"buffer_load_dwordx4 a[36:39], v9, s[60:63], 0 offen offset:1024 sc1\nMvaa 176,176,44\nMvaa 176,144,40\nMvaa 172,140,40\nMvaa 172,172,44\nMvaa 168,168,44\ns_waitcnt vmcnt(30)\ns_barrier\n" \
"Mvaa 168,136,40\nTa 192,20,32768\nTa 194,20,33792\nMvaa 164,132,40\nTa 224,20,40960\nMvaa 164,164,44\nTa 226,20,41984\nTa 196,21,32768\n" \
"Mvaa 160,160,44\nTa 198,21,33792\nMvaa 160,128,40\ns_waitcnt vmcnt(18)\nTa 228,21,40960\nMvaa 192,128,48\nTa 230,21,41984\nMvaa 192,160,52\n" \
"buffer_load_dwordx4 a[40:43], v9, s[60:63], s85 offen offset:0 sc1\nMvaa 196,164,52\nTa 200,22,32768\nTa 202,22,33792\nMvaa 196,132,48\nTa 232,22,40960\nMvaa 200,136,48\nTa 234,22,41984\n" \
"Ta 204,23,32768\nMvaa 200,168,52\nbuffer_load_dwordx4 a[44:47], v9, s[60:63], s85 offen offset:1024 sc1\nMvaa 204,172,52\nTa 206,23,33792\nMvaa 204,140,48\nTa 236,23,40960\nTa 238,23,41984\n" \
"Mvaa 208,144,48\ns_add_u32 m0, s84, 16384\nMvaa 208,176,52\nD 18,68,0,0\nMvaa 212,180,52\ns_xor_b32 s84, s84, 0x38000\nTa 208,24,32768\nMvaa 212,148,48\n" \
"Ta 210,24,33792\nTa 240,24,40960\nMvaa 216,152,48\nTa 242,24,41984\nMvaa 216,184,52\nD 18,68,s89,1024\nMvaa 220,188,52\nTa 212,25,32768\n" \
"Ta 214,25,33792\nMvaa 220,156,48\ns_waitcnt vmcnt(19)\nMvaa 252,156,56\nTa 244,25,40960\nTa 246,25,41984\nMvaa 252,188,60\nbuffer_load_dwordx4 a[48:51], v9, s[60:63], s86 offen offset:0 sc1\n" \
"Mvaa 248,184,60\nTa 216,26,32768\nMvaa 248,152,56\nTa 218,26,33792\nTa 248,26,40960\nMvaa 244,148,56\nTa 250,26,41984\nMvaa 244,180,60\n" \
"buffer_load_dwordx4 a[52:55], v9, s[60:63], s86 offen offset:1024 sc1\nMvaa 240,176,60\nTa 220,27,32768\nTa 222,27,33792\nMvaa 240,144,56\nTa 252,27,40960\nMvaa 236,140,56\ns_sub_u32 s96, s96, 1\n" \
"Ta 254,27,41984\nMvaa 236,172,60\nD 19,68,s90,2048\nMvaa 232,168,60\ns_cmp_lg_u32 s96, 0\nMvaa 232,136,56\nMvaa 228,132,56\nMvaa 228,164,60\n" \
"D 19,68,s91,3072\nMvaa 224,160,60\nMvaa 224,128,56\ns_waitcnt vmcnt(21) lgkmcnt(0)\ns_cbranch_scc1 zol\nzox:\ns_cmp_eq_u32 s23, 0\ns_cbranch_scc1 zot\n" \
"s_cmp_eq_u32 s23, 1\ns_cbranch_scc1 zof\ns_cmp_eq_u32 s23, 3\ns_cbranch_scc1 zo1\ns_branch zos\ns_branch 99f\n.p2align 12\nzqR:\n" \
"zp16:\ns_setpc_b64 s[98:99]\n99:\nzof:\nMvaa 128,192,0\nMvaa 128,224,4\nbuffer_load_dwordx4 a[56:59], v9, s[60:63], s87 offen offset:0 sc1\nMvaa 132,228,4\n" \
"Mvaa 132,196,0\nMvaa 136,200,0\ns_waitcnt vmcnt(26)\ns_barrier\nMvaa 136,232,4\nbuffer_load_dwordx4 a[60:63], v9, s[60:63], s87 offen offset:1024 sc1\nMvaa 140,236,4\nTa 128,20,49152\n" \
"Ta 130,20,50176\nMvaa 140,204,0\nTa 160,20,57344\nTa 162,20,58368\nMvaa 144,208,0\nTa 132,21,49152\nMvaa 144,240,4\nTa 134,21,50176\n" \
"Ta 164,21,57344\nMvaa 148,244,4\nTa 166,21,58368\nMvaa 148,212,0\nTa 136,22,49152\nTa 138,22,50176\nMvaa 152,216,0\nTa 168,22,57344\n" \
"Mvaa 152,248,4\nTa 170,22,58368\nTa 140,23,49152\nMvaa 156,252,4\nTa 142,23,50176\nMvaa 156,220,0\nTa 172,23,57344\nTa 174,23,58368\n" \
"s_waitcnt vmcnt(15)\nMvaa 160,192,8\nTa 144,24,49152\nMvaa 160,224,12\nTa 146,24,50176\nTa 176,24,57344\nMvaa 164,228,12\nTa 178,24,58368\n" \
"Mvaa 164,196,8\nTa 148,25,49152\nTa 150,25,50176\nMvaa 168,200,8\nTa 180,25,57344\nMvaa 168,232,12\nTa 182,25,58368\nTa 152,26,49152\n" \
"Mvaa 172,236,12\nTa 154,26,50176\nMvaa 172,204,8\nTa 184,26,57344\nTa 186,26,58368\nMvaa 176,208,8\nTa 156,27,49152\nMvaa 176,240,12\n" \
"Ta 158,27,50176\nTa 188,27,57344\nMvaa 180,244,12\nTa 190,27,58368\nMvaa 180,212,8\nMvaa 184,216,8\nMvaa 184,248,12\nMvaa 188,252,12\n" \
"Mvaa 188,220,8\ns_waitcnt vmcnt(8) lgkmcnt(0)\nMvaa 128,128,32\nMvaa 128,160,36\nMvaa 132,164,36\nMvaa 132,132,32\nMvaa 136,136,32\nMvaa 136,168,36\n" \
"Mvaa 140,172,36\nMvaa 140,140,32\nMvaa 144,144,32\nMvaa 144,176,36\nMvaa 148,180,36\nMvaa 148,148,32\nMvaa 152,152,32\nMvaa 152,184,36\n" \
"Mvaa 156,188,36\nMvaa 156,156,32\ns_waitcnt vmcnt(5)\nMvaa 160,128,40\nMvaa 160,160,44\nMvaa 164,164,44\nMvaa 164,132,40\nC 32,128,129\n" \
"C 33,130,131\nMvaa 168,136,40\nC 34,132,133\nC 35,134,135\nMvaa 168,168,44\nC 36,136,137\nC 37,138,139\nMvaa 172,172,44\n" \
"C 38,140,141\nC 39,142,143\nMvaa 172,140,40\nC 40,144,145\nC 41,146,147\nMvaa 176,144,40\nC 42,148,149\nC 43,150,151\n" \
"Mvaa 176,176,44\nC 44,152,153\nC 45,154,155\nMvaa 180,180,44\nC 46,156,157\nC 47,158,159\nMvaa 180,148,40\ns_add_u32 s95, s24, 0\n" \
"Mvaa 184,152,40\nbuffer_store_dwordx4 v[32:35], v0, s[60:63], s95 offen offset:0 sc1\nMvaa 184,184,44\nMvaa 188,188,44\nbuffer_store_dwordx4 v[36:39], v0, s[60:63], s95 offen offset:1024 sc1\nMvaa 188,156,40\ns_waitcnt vmcnt(5)\nMvaa 192,192,16\n" \
"s_add_u32 s95, s25, 0\nMvaa 192,224,20\nbuffer_store_dwordx4 v[40:43], v0, s[60:63], s95 offen offset:0 sc1\nMvaa 192,128,48\nMvaa 192,160,52\nbuffer_store_dwordx4 v[44:47], v0, s[60:63], s95 offen offset:1024 sc1\nMvaa 196,228,20\nMvaa 196,196,16\n" \
"C 48,160,161\nC 49,162,163\nMvaa 196,164,52\nC 50,164,165\nC 51,166,167\nMvaa 196,132,48\nC 52,168,169\nC 53,170,171\n" \
"Mvaa 200,200,16\nC 54,172,173\nC 55,174,175\nMvaa 200,232,20\nC 56,176,177\nC 57,178,179\nMvaa 200,136,48\nC 58,180,181\n" \
"C 59,182,183\nMvaa 200,168,52\nC 60,184,185\nC 61,186,187\nMvaa 204,236,20\nC 62,188,189\nC 63,190,191\nMvaa 204,204,16\n" \
"s_add_u32 s95, s24, 2048\nMvaa 204,172,52\nbuffer_store_dwordx4 v[48:51], v0, s[60:63], s95 offen offset:0 sc1\nMvaa 204,140,48\nMvaa 208,208,16\nbuffer_store_dwordx4 v[52:55], v0, s[60:63], s95 offen offset:1024 sc1\nMvaa 208,240,20\nMvaa 208,144,48\n" \
"s_add_u32 s95, s25, 2048\nMvaa 208,176,52\nbuffer_store_dwordx4 v[56:59], v0, s[60:63], s95 offen offset:0 sc1\nMvaa 212,244,20\nMvaa 212,212,16\nbuffer_store_dwordx4 v[60:63], v0, s[60:63], s95 offen offset:1024 sc1\nMvaa 212,180,52\nMvaa 212,148,48\n" \
"Mvaa 216,216,16\nMvaa 216,248,20\nMvaa 216,152,48\nMvaa 216,184,52\nMvaa 220,252,20\nMvaa 220,220,16\nMvaa 220,188,52\nMvaa 220,156,48\n" \
"s_waitcnt vmcnt(8)\nMvaa 224,192,24\nMvaa 224,224,28\nMvaa 224,128,56\nMvaa 224,160,60\nC 64,192,193\nC 65,194,195\nMvaa 228,228,28\n" \
"C 66,196,197\nC 67,198,199\nMvaa 228,196,24\nC 68,200,201\nC 69,202,203\nMvaa 228,164,60\nC 70,204,205\nC 71,206,207\n" \
"Mvaa 228,132,56\nC 72,208,209\nC 73,210,211\nMvaa 232,200,24\nC 74,212,213\nC 75,214,215\nMvaa 232,232,28\nC 76,216,217\n" \
"C 77,218,219\nMvaa 232,136,56\nC 78,220,221\nC 79,222,223\nMvaa 232,168,60\ns_add_u32 s95, s24, 4096\nMvaa 236,236,28\nbuffer_store_dwordx4 v[64:67], v0, s[60:63], s95 offen offset:0 sc1\n" \
"Mvaa 236,204,24\nMvaa 236,172,60\nbuffer_store_dwordx4 v[68:71], v0, s[60:63], s95 offen offset:1024 sc1\nMvaa 236,140,56\nMvaa 240,208,24\ns_add_u32 s95, s25, 4096\nMvaa 240,240,28\nbuffer_store_dwordx4 v[72:75], v0, s[60:63], s95 offen offset:0 sc1\n" \
"Mvaa 240,144,56\nMvaa 240,176,60\nbuffer_store_dwordx4 v[76:79], v0, s[60:63], s95 offen offset:1024 sc1\nMvaa 244,244,28\nMvaa 244,212,24\nMvaa 244,180,60\nMvaa 244,148,56\nMvaa 248,216,24\n" \
"Mvaa 248,248,28\nMvaa 248,152,56\nMvaa 248,184,60\nMvaa 252,252,28\nMvaa 252,220,24\nMvaa 252,188,60\nMvaa 252,156,56\ns_nop 4\n" \
"C 80,224,225\nC 81,226,227\nC 82,228,229\nC 83,230,231\nC 84,232,233\nC 85,234,235\nC 86,236,237\nC 87,238,239\n" \
"C 88,240,241\nC 89,242,243\nC 90,244,245\nC 91,246,247\nC 92,248,249\nC 93,250,251\nC 94,252,253\nC 95,254,255\n" \
"s_add_u32 s95, s24, 6144\nbuffer_store_dwordx4 v[80:83], v0, s[60:63], s95 offen offset:0 sc1\nbuffer_store_dwordx4 v[84:87], v0, s[60:63], s95 offen offset:1024 sc1\ns_add_u32 s95, s25, 6144\nbuffer_store_dwordx4 v[88:91], v0, s[60:63], s95 offen offset:0 sc1\nbuffer_store_dwordx4 v[92:95], v0, s[60:63], s95 offen offset:1024 sc1\ns_waitcnt vmcnt(0)\nbuffer_store_dwordx2 v[2:3], off, s[60:63], s27 sc1\n" \
"s_branch zfin\ns_branch 99f\n.p2align 12\nzqS:\nzp17:\ns_setpc_b64 s[98:99]\n99:\nzos:\n" \
"C 32,128,129\nC 33,130,131\nC 34,132,133\nC 35,134,135\nC 36,136,137\nC 37,138,139\nC 38,140,141\nC 39,142,143\n" \
"C 40,144,145\nC 41,146,147\nC 42,148,149\nC 43,150,151\nC 44,152,153\nC 45,154,155\nC 46,156,157\nC 47,158,159\n" \
"Zvaa 128,192,0\ns_waitcnt vmcnt(10)\nv_cmp_eq_u64 vcc, v[10:11], v[2:3]\ns_mov_b32 s79, s78\nMvaa 128,224,4\nbuffer_load_dwordx4 a[56:59], v9, s[60:63], s87 offen offset:0 sc1\ns_xor_b32 s82, s79, s10\nZvaa 132,228,4\n" \
"s_lshl_b32 s81, s82, 16\ns_cmp_lt_u32 s79, 32\ns_cselect_b32 s82, s18, s19\nMvaa 132,196,0\ns_add_u32 s81, s81, s82\ns_bitcmp1_b64 vcc, s79\nC 48,160,161\nZvaa 136,200,0\n" \
"C 49,162,163\nC 50,164,165\nC 51,166,167\nMvaa 136,232,4\nbuffer_load_dwordx4 a[60:63], v9, s[60:63], s87 offen offset:1024 sc1\nC 52,168,169\nZvaa 140,236,4\nC 53,170,171\n" \
"C 54,172,173\nC 55,174,175\nMvaa 140,204,0\nv_add_u32 v9, s81, v0\nC 56,176,177\nC 57,178,179\nZvaa 144,208,0\nC 58,180,181\n" \
"C 59,182,183\nC 60,184,185\nMvaa 144,240,4\nC 61,186,187\nC 62,188,189\nC 63,190,191\nZvaa 148,244,4\ns_cbranch_scc0 zs108\n" \
"zr108:\ns_add_u32 s95, s24, 0\nbuffer_store_dwordx4 v[32:35], v0, s[60:63], s95 offen offset:0 sc1\nMvaa 148,212,0\ns_ff1_i32_b64 s78, s[8:9]\ns_bitset0_b64 s[8:9], s78\nZvaa 152,216,0\ns_xor_b32 s82, s78, s10\n" \
"s_and_b32 s80, s82, 31\nMvaa 152,248,4\ns_lshl_b32 s80, s80, 18\ns_cmp_lt_u32 s78, 32\nZvaa 156,252,4\ns_cselect_b64 s[68:69], s[14:15], s[12:13]\ns_cselect_b32 s70, 0x800000, s92\nMvaa 156,220,0\n" \
"v_add_u32 v18, s80, v16\nv_xor_b32 v19, 128, v18\ns_waitcnt vmcnt(16)\nZvaa 188,220,8\nC 64,192,193\nC 65,194,195\nC 66,196,197\nMvaa 188,252,12\n" \
"buffer_load_dwordx4 a[0:3], v9, s[60:63], 0 offen offset:0 sc1\nC 67,198,199\nZvaa 184,248,12\nC 68,200,201\nC 69,202,203\nC 70,204,205\nMvaa 184,216,8\nC 71,206,207\n" \
"C 72,208,209\nC 73,210,211\nZvaa 180,212,8\nC 74,212,213\nC 75,214,215\nC 76,216,217\nMvaa 180,244,12\nbuffer_load_dwordx4 a[4:7], v9, s[60:63], 0 offen offset:1024 sc1\n" \
"C 77,218,219\nZvaa 176,240,12\nC 78,220,221\nC 79,222,223\nMvaa 176,208,8\nZvaa 172,204,8\nbuffer_store_dwordx4 v[36:39], v0, s[60:63], s95 offen offset:1024 sc1\nMvaa 172,236,12\n" \
"Zvaa 168,232,12\ns_waitcnt vmcnt(31)\ns_barrier\nMvaa 168,200,8\nTa 128,20,49152\nTa 130,20,50176\nZvaa 164,196,8\nTa 160,20,57344\n" \
"s_add_u32 s95, s25, 0\nbuffer_store_dwordx4 v[40:43], v0, s[60:63], s95 offen offset:0 sc1\nMvaa 164,228,12\nTa 162,20,58368\nTa 132,21,49152\nZvaa 160,224,12\nTa 134,21,50176\nMvaa 160,192,8\n" \
"s_waitcnt vmcnt(17)\nTa 164,21,57344\nZvaa 192,192,16\nTa 166,21,58368\nC 80,224,225\nC 81,226,227\nMvaa 192,224,20\nbuffer_load_dwordx4 a[8:11], v9, s[60:63], s85 offen offset:0 sc1\n" \
"C 82,228,229\nZvaa 196,228,20\nTa 136,22,49152\nC 83,230,231\nC 84,232,233\nMvaa 196,196,16\nTa 138,22,50176\nTa 168,22,57344\n" \
"C 85,234,235\nZvaa 200,200,16\nTa 170,22,58368\nC 86,236,237\nC 87,238,239\nMvaa 200,232,20\nbuffer_load_dwordx4 a[12:15], v9, s[60:63], s85 offen offset:1024 sc1\nC 88,240,241\n" \
"Zvaa 204,236,20\nTa 140,23,49152\nC 89,242,243\nC 90,244,245\nMvaa 204,204,16\nTa 142,23,50176\nTa 172,23,57344\nC 91,246,247\n" \
"Zvaa 208,208,16\ns_add_u32 m0, s84, 0\nC 92,248,249\nC 93,250,251\nMvaa 208,240,20\nD 18,68,0,0\nC 94,252,253\nZvaa 212,244,20\n" \
"Ta 174,23,58368\nTa 144,24,49152\nC 95,254,255\nMvaa 212,212,16\nTa 146,24,50176\nZvaa 216,216,16\nTa 176,24,57344\nTa 178,24,58368\n" \
"Mvaa 216,248,20\nD 18,68,s89,1024\nbuffer_store_dwordx4 v[44:47], v0, s[60:63], s95 offen offset:1024 sc1\nZvaa 220,252,20\nTa 148,25,49152\nMvaa 220,220,16\ns_waitcnt vmcnt(20)\nTa 150,25,50176\n" \
"Zvaa 252,220,24\nTa 180,25,57344\nMvaa 252,252,28\nbuffer_load_dwordx4 a[16:19], v9, s[60:63], s86 offen offset:0 sc1\nZvaa 248,248,28\nTa 182,25,58368\nMvaa 248,216,24\nTa 152,26,49152\n" \
"Ta 154,26,50176\nZvaa 244,212,24\nTa 184,26,57344\nMvaa 244,244,28\nbuffer_load_dwordx4 a[20:23], v9, s[60:63], s86 offen offset:1024 sc1\ns_add_u32 s95, s24, 2048\nbuffer_store_dwordx4 v[48:51], v0, s[60:63], s95 offen offset:0 sc1\nZvaa 240,240,28\n" \
"Ta 186,26,58368\nMvaa 240,208,24\nTa 156,27,49152\nTa 158,27,50176\nZvaa 236,204,24\nTa 188,27,57344\nMvaa 236,236,28\nD 19,68,s90,2048\n" \
"Zvaa 232,232,28\nTa 190,27,58368\nMvaa 232,200,24\nZvaa 228,196,24\nMvaa 228,228,28\nD 19,68,s91,3072\nZvaa 224,224,28\nv_add_u32 v18, 131072, v18\n" \
"Mvaa 224,192,24\nv_add_u32 v19, 131072, v19\ns_waitcnt vmcnt(23) lgkmcnt(0)\nMvaa 128,128,32\nbuffer_store_dwordx4 v[52:55], v0, s[60:63], s95 offen offset:1024 sc1\nMvaa 128,160,36\nbuffer_load_dwordx4 a[24:27], v9, s[60:63], s87 offen offset:0 sc1\nMvaa 132,164,36\n" \
"Mvaa 132,132,32\nMvaa 136,136,32\nMvaa 136,168,36\nbuffer_load_dwordx4 a[28:31], v9, s[60:63], s87 offen offset:1024 sc1\nMvaa 140,172,36\nMvaa 140,140,32\nv_add_u32 v9, 0x8000, v9\nMvaa 144,144,32\n" \
"v_xor_b32 v20, 0x8000, v20\nv_xor_b32 v21, 0x8000, v21\nMvaa 144,176,36\nv_xor_b32 v22, 0x8000, v22\nv_xor_b32 v23, 0x8000, v23\nMvaa 148,180,36\nv_xor_b32 v24, 0x8000, v24\nv_xor_b32 v25, 0x8000, v25\n" \
"Mvaa 148,148,32\nbuffer_load_dwordx2 v[10:11], v1, s[60:63], 0 offen sc1\nMvaa 152,152,32\nv_xor_b32 v26, 0x8000, v26\nv_xor_b32 v27, 0x8000, v27\nMvaa 152,184,36\nMvaa 156,188,36\ns_add_u32 s95, s25, 2048\n" \
"buffer_store_dwordx4 v[56:59], v0, s[60:63], s95 offen offset:0 sc1\nMvaa 156,156,32\ns_waitcnt vmcnt(25)\nMvaa 188,156,40\nMvaa 188,188,44\nbuffer_load_dwordx4 a[32:35], v9, s[60:63], 0 offen offset:0 sc1\nMvaa 184,184,44\nMvaa 184,152,40\n" \
"Mvaa 180,148,40\nMvaa 180,180,44\nbuffer_load_dwordx4 a[36:39], v9, s[60:63], 0 offen offset:1024 sc1\nMvaa 176,176,44\nMvaa 176,144,40\nMvaa 172,140,40\nbuffer_store_dwordx4 v[60:63], v0, s[60:63], s95 offen offset:1024 sc1\nMvaa 172,172,44\n" \
"Mvaa 168,168,44\ns_waitcnt vmcnt(38)\ns_barrier\nMvaa 168,136,40\nTa 192,20,32768\nTa 194,20,33792\nMvaa 164,132,40\nTa 224,20,40960\n" \
"Mvaa 164,164,44\nTa 226,20,41984\nTa 196,21,32768\nMvaa 160,160,44\nTa 198,21,33792\nMvaa 160,128,40\ns_waitcnt vmcnt(26)\nTa 228,21,40960\n" \
"Mvaa 192,128,48\nTa 230,21,41984\nMvaa 192,160,52\nbuffer_load_dwordx4 a[40:43], v9, s[60:63], s85 offen offset:0 sc1\nMvaa 196,164,52\nTa 200,22,32768\nTa 202,22,33792\nMvaa 196,132,48\n" \
"Ta 232,22,40960\nMvaa 200,136,48\nTa 234,22,41984\nMvaa 200,168,52\nbuffer_load_dwordx4 a[44:47], v9, s[60:63], s85 offen offset:1024 sc1\ns_add_u32 s95, s24, 4096\nbuffer_store_dwordx4 v[64:67], v0, s[60:63], s95 offen offset:0 sc1\nMvaa 204,172,52\n" \
"Ta 204,23,32768\nTa 206,23,33792\nMvaa 204,140,48\nTa 236,23,40960\nMvaa 208,144,48\ns_add_u32 m0, s84, 16384\nTa 238,23,41984\nMvaa 208,176,52\n" \
"D 18,68,0,0\nMvaa 212,180,52\ns_xor_b32 s84, s84, 0x38000\nMvaa 212,148,48\nTa 208,24,32768\nTa 210,24,33792\nMvaa 216,152,48\nTa 240,24,40960\n" \
"Mvaa 216,184,52\nD 18,68,s89,1024\nMvaa 220,188,52\nTa 242,24,41984\nTa 212,25,32768\nMvaa 220,156,48\ns_waitcnt vmcnt(28)\nMvaa 252,156,56\n" \
"Ta 214,25,33792\nTa 244,25,40960\nMvaa 252,188,60\nbuffer_load_dwordx4 a[48:51], v9, s[60:63], s86 offen offset:0 sc1\nbuffer_store_dwordx4 v[68:71], v0, s[60:63], s95 offen offset:1024 sc1\nMvaa 248,184,60\nTa 246,25,41984\nMvaa 248,152,56\n" \
"Ta 216,26,32768\nTa 218,26,33792\nMvaa 244,148,56\nTa 248,26,40960\nMvaa 244,180,60\nbuffer_load_dwordx4 a[52:55], v9, s[60:63], s86 offen offset:1024 sc1\nMvaa 240,176,60\nTa 250,26,41984\n" \
"Mvaa 240,144,56\nTa 220,27,32768\nTa 222,27,33792\nMvaa 236,140,56\nTa 252,27,40960\nMvaa 236,172,60\nD 19,68,s90,2048\nMvaa 232,168,60\n" \
"Ta 254,27,41984\nMvaa 232,136,56\ns_add_u32 s95, s25, 4096\nbuffer_store_dwordx4 v[72:75], v0, s[60:63], s95 offen offset:0 sc1\nMvaa 228,132,56\nMvaa 228,164,60\nD 19,68,s91,3072\nMvaa 224,160,60\n" \
"Mvaa 224,128,56\ns_waitcnt vmcnt(31) lgkmcnt(0)\nMvaa 128,192,0\ns_waitcnt vmcnt(15)\nv_cmp_eq_u64 vcc, v[10:11], v[2:3]\ns_mov_b32 s79, s78\nMvaa 128,224,4\nbuffer_load_dwordx4 a[56:59], v9, s[60:63], s87 offen offset:0 sc1\n" \
"s_xor_b32 s82, s79, s10\nMvaa 132,228,4\ns_lshl_b32 s81, s82, 16\ns_cmp_lt_u32 s79, 32\ns_cselect_b32 s82, s18, s19\nMvaa 132,196,0\ns_add_u32 s81, s81, s82\ns_bitcmp1_b64 vcc, s79\n" \
"Mvaa 136,200,0\nMvaa 136,232,4\nbuffer_load_dwordx4 a[60:63], v9, s[60:63], s87 offen offset:1024 sc1\nMvaa 140,236,4\nMvaa 140,204,0\nv_add_u32 v9, s81, v0\nMvaa 144,208,0\nbuffer_store_dwordx4 v[76:79], v0, s[60:63], s95 offen offset:1024 sc1\n" \
"Mvaa 144,240,4\nMvaa 148,244,4\ns_cbranch_scc0 zs109\nzr109:\nMvaa 148,212,0\ns_ff1_i32_b64 s78, s[8:9]\ns_bitset0_b64 s[8:9], s78\nMvaa 152,216,0\n" \
"s_xor_b32 s82, s78, s10\ns_and_b32 s80, s82, 31\nMvaa 152,248,4\ns_lshl_b32 s80, s80, 18\ns_cmp_lt_u32 s78, 32\nMvaa 156,252,4\ns_cselect_b64 s[68:69], s[14:15], s[12:13]\ns_cselect_b32 s70, 0x800000, s92\n" \
"Mvaa 156,220,0\nv_add_u32 v18, s80, v16\nv_xor_b32 v19, 128, v18\ns_waitcnt vmcnt(30)\nMvaa 188,220,8\nMvaa 188,252,12\nbuffer_load_dwordx4 a[0:3], v9, s[60:63], 0 offen offset:0 sc1\nMvaa 184,248,12\n" \
"Mvaa 184,216,8\nMvaa 180,212,8\nMvaa 180,244,12\nbuffer_load_dwordx4 a[4:7], v9, s[60:63], 0 offen offset:1024 sc1\nMvaa 176,240,12\nMvaa 176,208,8\nMvaa 172,204,8\ns_add_u32 s95, s24, 6144\n" \
"buffer_store_dwordx4 v[80:83], v0, s[60:63], s95 offen offset:0 sc1\nMvaa 172,236,12\nMvaa 168,232,12\ns_waitcnt vmcnt(42)\ns_barrier\nMvaa 168,200,8\nTa 128,20,49152\nTa 130,20,50176\n" \
"Mvaa 164,196,8\nTa 160,20,57344\nMvaa 164,228,12\nTa 162,20,58368\nTa 132,21,49152\nMvaa 160,224,12\nTa 134,21,50176\nMvaa 160,192,8\n" \
"s_waitcnt vmcnt(28)\nTa 164,21,57344\nMvaa 192,192,16\nTa 166,21,58368\nMvaa 192,224,20\nbuffer_load_dwordx4 a[8:11], v9, s[60:63], s85 offen offset:0 sc1\nbuffer_store_dwordx4 v[84:87], v0, s[60:63], s95 offen offset:1024 sc1\nMvaa 196,228,20\n" \
"Ta 136,22,49152\nMvaa 196,196,16\nTa 138,22,50176\nTa 168,22,57344\nMvaa 200,200,16\nTa 170,22,58368\nMvaa 200,232,20\nbuffer_load_dwordx4 a[12:15], v9, s[60:63], s85 offen offset:1024 sc1\n" \
"Mvaa 204,236,20\nTa 140,23,49152\nMvaa 204,204,16\nTa 142,23,50176\nTa 172,23,57344\nMvaa 208,208,16\ns_add_u32 m0, s84, 0\nMvaa 208,240,20\n" \
"D 18,68,0,0\nMvaa 212,244,20\nTa 174,23,58368\nTa 144,24,49152\nMvaa 212,212,16\nTa 146,24,50176\nMvaa 216,216,16\nTa 176,24,57344\n" \
"Ta 178,24,58368\nMvaa 216,248,20\nD 18,68,s89,1024\ns_add_u32 s95, s25, 6144\nbuffer_store_dwordx4 v[88:91], v0, s[60:63], s95 offen offset:0 sc1\nMvaa 220,252,20\nTa 148,25,49152\nMvaa 220,220,16\n" \
"s_waitcnt vmcnt(28)\nTa 150,25,50176\nMvaa 252,220,24\nTa 180,25,57344\nMvaa 252,252,28\nbuffer_load_dwordx4 a[16:19], v9, s[60:63], s86 offen offset:0 sc1\nMvaa 248,248,28\nTa 182,25,58368\n" \
"Mvaa 248,216,24\nTa 152,26,49152\nTa 154,26,50176\nMvaa 244,212,24\nTa 184,26,57344\nMvaa 244,244,28\nbuffer_load_dwordx4 a[20:23], v9, s[60:63], s86 offen offset:1024 sc1\nMvaa 240,240,28\n" \
"Ta 186,26,58368\nMvaa 240,208,24\nTa 156,27,49152\nTa 158,27,50176\nMvaa 236,204,24\nTa 188,27,57344\nMvaa 236,236,28\nD 19,68,s90,2048\n" \
"buffer_store_dwordx4 v[92:95], v0, s[60:63], s95 offen offset:1024 sc1\nMvaa 232,232,28\nTa 190,27,58368\nMvaa 232,200,24\nMvaa 228,196,24\nMvaa 228,228,28\nD 19,68,s91,3072\nMvaa 224,224,28\n" \
"v_add_u32 v18, 131072, v18\nMvaa 224,192,24\nv_add_u32 v19, 131072, v19\ns_waitcnt vmcnt(29) lgkmcnt(0)\nMvaa 128,128,32\nMvaa 128,160,36\nbuffer_load_dwordx4 a[24:27], v9, s[60:63], s87 offen offset:0 sc1\nMvaa 132,164,36\n" \
"Mvaa 132,132,32\nMvaa 136,136,32\nMvaa 136,168,36\nbuffer_load_dwordx4 a[28:31], v9, s[60:63], s87 offen offset:1024 sc1\nMvaa 140,172,36\nMvaa 140,140,32\nv_add_u32 v9, 0x8000, v9\nMvaa 144,144,32\n" \
"v_xor_b32 v20, 0x8000, v20\nv_xor_b32 v21, 0x8000, v21\nMvaa 144,176,36\nv_xor_b32 v22, 0x8000, v22\nv_xor_b32 v23, 0x8000, v23\nMvaa 148,180,36\nv_xor_b32 v24, 0x8000, v24\nv_xor_b32 v25, 0x8000, v25\n" \
"Mvaa 148,148,32\nbuffer_load_dwordx2 v[10:11], v1, s[60:63], 0 offen sc1\nMvaa 152,152,32\nv_xor_b32 v26, 0x8000, v26\nv_xor_b32 v27, 0x8000, v27\nMvaa 152,184,36\nMvaa 156,188,36\nMvaa 156,156,32\n" \
"s_waitcnt vmcnt(29)\nMvaa 188,156,40\nMvaa 188,188,44\nbuffer_load_dwordx4 a[32:35], v9, s[60:63], 0 offen offset:0 sc1\nMvaa 184,184,44\nMvaa 184,152,40\nMvaa 180,148,40\nMvaa 180,180,44\n" \
"buffer_load_dwordx4 a[36:39], v9, s[60:63], 0 offen offset:1024 sc1\nMvaa 176,176,44\nMvaa 176,144,40\nMvaa 172,140,40\nMvaa 172,172,44\nMvaa 168,168,44\ns_waitcnt vmcnt(41)\ns_barrier\n" \
"Mvaa 168,136,40\nTa 192,20,32768\nTa 194,20,33792\nMvaa 164,132,40\nTa 224,20,40960\nMvaa 164,164,44\nTa 226,20,41984\nTa 196,21,32768\n" \
"s_branch 99f\n.p2align 12\nzp18:\ns_setpc_b64 s[98:99]\n99:\nMvaa 160,160,44\nTa 198,21,33792\nMvaa 160,128,40\n" \
"s_waitcnt vmcnt(25)\nTa 228,21,40960\nMvaa 192,128,48\nTa 230,21,41984\nMvaa 192,160,52\nbuffer_load_dwordx4 a[40:43], v9, s[60:63], s85 offen offset:0 sc1\nMvaa 196,164,52\nTa 200,22,32768\n" \
"Ta 202,22,33792\nMvaa 196,132,48\nTa 232,22,40960\nMvaa 200,136,48\nTa 234,22,41984\nMvaa 200,168,52\nbuffer_load_dwordx4 a[44:47], v9, s[60:63], s85 offen offset:1024 sc1\nMvaa 204,172,52\n" \
"Ta 204,23,32768\nTa 206,23,33792\nMvaa 204,140,48\nTa 236,23,40960\nMvaa 208,144,48\ns_add_u32 m0, s84, 16384\nTa 238,23,41984\nMvaa 208,176,52\n" \
"D 18,68,0,0\nMvaa 212,180,52\ns_xor_b32 s84, s84, 0x38000\nMvaa 212,148,48\nTa 208,24,32768\nTa 210,24,33792\nMvaa 216,152,48\nTa 240,24,40960\n" \
"Mvaa 216,184,52\nD 18,68,s89,1024\nMvaa 220,188,52\nTa 242,24,41984\nTa 212,25,32768\nMvaa 220,156,48\ns_waitcnt vmcnt(24)\nMvaa 252,156,56\n" \
"Ta 214,25,33792\nTa 244,25,40960\nMvaa 252,188,60\nbuffer_load_dwordx4 a[48:51], v9, s[60:63], s86 offen offset:0 sc1\nMvaa 248,184,60\nTa 246,25,41984\ns_mov_b32 s96, 5\ns_mov_b32 s23, 3\n" \
"Mvaa 248,152,56\nTa 216,26,32768\nTa 218,26,33792\nMvaa 244,148,56\nTa 248,26,40960\nMvaa 244,180,60\nbuffer_load_dwordx4 a[52:55], v9, s[60:63], s86 offen offset:1024 sc1\nMvaa 240,176,60\n" \
"Ta 250,26,41984\nMvaa 240,144,56\nTa 220,27,32768\nTa 222,27,33792\nMvaa 236,140,56\nTa 252,27,40960\nMvaa 236,172,60\nD 19,68,s90,2048\n" \
"Mvaa 232,168,60\nTa 254,27,41984\nMvaa 232,136,56\ns_waitcnt vmcnt(13)\nbuffer_store_dwordx2 v[2:3], off, s[60:63], s27 sc1\nMvaa 228,132,56\nMvaa 228,164,60\nD 19,68,s91,3072\n" \
"Mvaa 224,160,60\nMvaa 224,128,56\ns_waitcnt vmcnt(26) lgkmcnt(0)\ns_branch zol\nzo1:\nMvaa 128,192,0\nMvaa 128,224,4\nbuffer_load_dwordx4 a[56:59], v9, s[60:63], s87 offen offset:0 sc1\n" \
"Mvaa 132,228,4\nMvaa 132,196,0\nMvaa 136,200,0\ns_waitcnt vmcnt(26)\ns_barrier\nMvaa 136,232,4\nbuffer_load_dwordx4 a[60:63], v9, s[60:63], s87 offen offset:1024 sc1\nMvaa 140,236,4\n" \
"Ta 128,20,49152\nTa 130,20,50176\nMvaa 140,204,0\nTa 160,20,57344\nTa 162,20,58368\nMvaa 144,208,0\nTa 132,21,49152\nMvaa 144,240,4\n" \
"Ta 134,21,50176\nTa 164,21,57344\nMvaa 148,244,4\nTa 166,21,58368\nMvaa 148,212,0\nTa 136,22,49152\nTa 138,22,50176\nMvaa 152,216,0\n" \
"Ta 168,22,57344\nMvaa 152,248,4\nTa 170,22,58368\nTa 140,23,49152\nMvaa 156,252,4\nTa 142,23,50176\nMvaa 156,220,0\nTa 172,23,57344\n" \
"Ta 174,23,58368\ns_waitcnt vmcnt(21)\nMvaa 160,192,8\nTa 144,24,49152\nMvaa 160,224,12\nTa 146,24,50176\nTa 176,24,57344\nMvaa 164,228,12\n" \
"Ta 178,24,58368\nMvaa 164,196,8\nTa 148,25,49152\nTa 150,25,50176\nMvaa 168,200,8\nTa 180,25,57344\nMvaa 168,232,12\nTa 182,25,58368\n" \
"Ta 152,26,49152\nMvaa 172,236,12\nTa 154,26,50176\nMvaa 172,204,8\nTa 184,26,57344\nTa 186,26,58368\nMvaa 176,208,8\nTa 156,27,49152\n" \
"Mvaa 176,240,12\nTa 158,27,50176\nTa 188,27,57344\nMvaa 180,244,12\nTa 190,27,58368\nMvaa 180,212,8\nMvaa 184,216,8\ns_mov_b64 s[64:65], s[16:17]\n" \
"s_mov_b32 s66, 8388608\nMvaa 184,248,12\nv_bfe_u32 v28, v0, 4, 4\nv_lshrrev_b32 v29, 9, v0\nMvaa 188,252,12\nv_and_b32 v30, 7, v28\nv_xor_b32 v29, v29, v30\nMvaa 188,220,8\n" \
"s_waitcnt vmcnt(10) lgkmcnt(0)\ns_lshl_b32 s82, s32, 2\nMvaa 128,128,32\nv_lshrrev_b32 v30, 5, v0\nv_lshlrev_b32 v28, 7, v28\nMvaa 128,160,36\nv_and_b32 v30, 8, v30\nv_lshl_add_u32 v28, v29, 4, v28\n" \
"Mvaa 132,164,36\nv_add3_u32 v12, v28, v30, s82\nv_xor_b32 v13, 32, v12\nMvaa 132,132,32\nv_xor_b32 v14, 64, v12\nv_xor_b32 v15, 96, v12\nMvaa 136,136,32\nv_lshrrev_b32 v28, 7, v0\n" \
"v_lshlrev_b32 v29, 4, v28\nMvaa 136,168,36\nv_xor_b32 v29, v29, v0\nv_add_u32 v5, s82, v29\nMvaa 140,172,36\nv_and_b32 v29, 0x70, v0\nv_lshl_add_u32 v6, v28, 12, v29\nMvaa 140,140,32\n" \
"v_add_u32 v8, 0x8000, v6\nMvaa 144,144,32\nMvaa 144,176,36\nMvaa 148,180,36\nMvaa 148,148,32\nMvaa 152,152,32\nMvaa 152,184,36\nMvaa 156,188,36\n" \
"Mvaa 156,156,32\ns_waitcnt vmcnt(8)\nMvaa 160,128,40\nMvaa 160,160,44\nMvaa 164,164,44\nMvaa 164,132,40\nC 32,128,129\nC 33,130,131\n" \
"Mvaa 168,136,40\nC 34,132,133\nC 35,134,135\nMvaa 168,168,44\nds_write_b64 v12, v[32:33] offset:0\nC 36,136,137\nMvaa 172,172,44\nC 37,138,139\n" \
"ds_write_b64 v13, v[34:35] offset:0\nMvaa 172,140,40\nC 38,140,141\nC 39,142,143\nMvaa 176,144,40\nds_write_b64 v14, v[36:37] offset:0\nC 40,144,145\nMvaa 176,176,44\n" \
"C 41,146,147\nds_write_b64 v15, v[38:39] offset:0\nMvaa 180,180,44\nC 42,148,149\nC 43,150,151\nMvaa 180,148,40\nds_write_b64 v12, v[40:41] offset:2048\nC 44,152,153\n" \
"Mvaa 184,152,40\nC 45,154,155\nds_write_b64 v13, v[42:43] offset:2048\nMvaa 184,184,44\nC 46,156,157\nC 47,158,159\nMvaa 188,188,44\nds_write_b64 v14, v[44:45] offset:2048\n" \
"ds_write_b64 v15, v[46:47] offset:2048\nMvaa 188,156,40\ns_waitcnt vmcnt(4)\ns_add_u32 s95, s29, 0\nMvaa 192,192,16\nMvaa 192,224,20\ns_waitcnt lgkmcnt(0)\nRv 32,5,0\n" \
"Mvaa 192,128,48\nRv 36,5,1024\nRv 40,5,2048\nMvaa 192,160,52\nRv 44,5,3072\nMvaa 196,228,20\nMvaa 196,196,16\nMvaa 196,164,52\n" \
"Mvaa 196,132,48\nMvaa 200,200,16\ns_waitcnt lgkmcnt(0)\nMvaa 200,232,20\nbuffer_store_dwordx4 v[32:35], v6, s[64:67], s95 offen offset:0 sc1\nMvaa 200,136,48\nbuffer_store_dwordx4 v[36:39], v8, s[64:67], s95 offen offset:0 sc1\nMvaa 200,168,52\n" \
"buffer_store_dwordx4 v[40:43], v6, s[64:67], s95 offen offset:128 sc1\nMvaa 204,236,20\nbuffer_store_dwordx4 v[44:47], v8, s[64:67], s95 offen offset:128 sc1\nMvaa 204,204,16\nC 48,160,161\nC 49,162,163\nMvaa 204,172,52\nC 50,164,165\n" \
"C 51,166,167\nMvaa 204,140,48\nds_write_b64 v12, v[48:49] offset:4096\nC 52,168,169\nMvaa 208,208,16\nC 53,170,171\nds_write_b64 v13, v[50:51] offset:4096\nMvaa 208,240,20\n" \
"C 54,172,173\nC 55,174,175\nMvaa 208,144,48\nds_write_b64 v14, v[52:53] offset:4096\nC 56,176,177\nMvaa 208,176,52\nC 57,178,179\nds_write_b64 v15, v[54:55] offset:4096\n" \
"Mvaa 212,244,20\nC 58,180,181\nC 59,182,183\nMvaa 212,212,16\nds_write_b64 v12, v[56:57] offset:6144\nC 60,184,185\nMvaa 212,180,52\nC 61,186,187\n" \
"ds_write_b64 v13, v[58:59] offset:6144\nMvaa 212,148,48\nC 62,188,189\nC 63,190,191\nMvaa 216,216,16\nds_write_b64 v14, v[60:61] offset:6144\nds_write_b64 v15, v[62:63] offset:6144\nMvaa 216,248,20\n" \
"s_add_u32 s95, s29, 262144\nMvaa 216,152,48\nMvaa 216,184,52\ns_waitcnt lgkmcnt(0)\nRv 48,5,4096\nMvaa 220,252,20\nRv 52,5,5120\nRv 56,5,6144\n" \
"Mvaa 220,220,16\nRv 60,5,7168\nMvaa 220,188,52\nMvaa 220,156,48\ns_waitcnt vmcnt(4)\nMvaa 224,192,24\nMvaa 224,224,28\nMvaa 224,128,56\n" \
"s_waitcnt lgkmcnt(0)\nMvaa 224,160,60\nbuffer_store_dwordx4 v[48:51], v6, s[64:67], s95 offen offset:0 sc1\nMvaa 228,228,28\nbuffer_store_dwordx4 v[52:55], v8, s[64:67], s95 offen offset:0 sc1\nMvaa 228,196,24\nbuffer_store_dwordx4 v[56:59], v6, s[64:67], s95 offen offset:128 sc1\nMvaa 228,164,60\n" \
"buffer_store_dwordx4 v[60:63], v8, s[64:67], s95 offen offset:128 sc1\nMvaa 228,132,56\nC 64,192,193\nC 65,194,195\nMvaa 232,200,24\nC 66,196,197\nC 67,198,199\nMvaa 232,232,28\n" \
"ds_write_b64 v12, v[64:65] offset:8192\nC 68,200,201\nMvaa 232,136,56\nC 69,202,203\nds_write_b64 v13, v[66:67] offset:8192\nMvaa 232,168,60\nC 70,204,205\nC 71,206,207\n" \
"Mvaa 236,236,28\nds_write_b64 v14, v[68:69] offset:8192\nC 72,208,209\nMvaa 236,204,24\nC 73,210,211\nds_write_b64 v15, v[70:71] offset:8192\nMvaa 236,172,60\nC 74,212,213\n" \
"C 75,214,215\nMvaa 236,140,56\nds_write_b64 v12, v[72:73] offset:10240\nC 76,216,217\nMvaa 240,208,24\nC 77,218,219\nds_write_b64 v13, v[74:75] offset:10240\nMvaa 240,240,28\n" \
"C 78,220,221\nC 79,222,223\nMvaa 240,144,56\nds_write_b64 v14, v[76:77] offset:10240\nds_write_b64 v15, v[78:79] offset:10240\nMvaa 240,176,60\ns_add_u32 s95, s30, 0\nMvaa 244,244,28\n" \
"Mvaa 244,212,24\ns_waitcnt lgkmcnt(0)\nRv 64,5,8192\nMvaa 244,180,60\nRv 68,5,9216\nRv 72,5,10240\nMvaa 244,148,56\nRv 76,5,11264\n" \
"Mvaa 248,216,24\nMvaa 248,248,28\nMvaa 248,152,56\nMvaa 248,184,60\nMvaa 252,252,28\ns_waitcnt lgkmcnt(0)\nMvaa 252,220,24\nbuffer_store_dwordx4 v[64:67], v6, s[64:67], s95 offen offset:0 sc1\n" \
"Mvaa 252,188,60\nbuffer_store_dwordx4 v[68:71], v8, s[64:67], s95 offen offset:0 sc1\nMvaa 252,156,56\nbuffer_store_dwordx4 v[72:75], v6, s[64:67], s95 offen offset:128 sc1\nbuffer_store_dwordx4 v[76:79], v8, s[64:67], s95 offen offset:128 sc1\ns_nop 4\nC 80,224,225\nC 81,226,227\n" \
"C 82,228,229\nC 83,230,231\nds_write_b64 v12, v[80:81] offset:12288\nC 84,232,233\nC 85,234,235\nds_write_b64 v13, v[82:83] offset:12288\nC 86,236,237\nC 87,238,239\n" \
"ds_write_b64 v14, v[84:85] offset:12288\nC 88,240,241\nC 89,242,243\nds_write_b64 v15, v[86:87] offset:12288\nC 90,244,245\nC 91,246,247\nds_write_b64 v12, v[88:89] offset:14336\nC 92,248,249\n" \
"C 93,250,251\nds_write_b64 v13, v[90:91] offset:14336\nC 94,252,253\nC 95,254,255\nds_write_b64 v14, v[92:93] offset:14336\nds_write_b64 v15, v[94:95] offset:14336\ns_add_u32 s95, s30, 262144\ns_waitcnt lgkmcnt(0)\n" \
"Rv 80,5,12288\nRv 84,5,13312\nRv 88,5,14336\nRv 92,5,15360\ns_waitcnt lgkmcnt(0)\nbuffer_store_dwordx4 v[80:83], v6, s[64:67], s95 offen offset:0 sc1\nbuffer_store_dwordx4 v[84:87], v8, s[64:67], s95 offen offset:0 sc1\nbuffer_store_dwordx4 v[88:91], v6, s[64:67], s95 offen offset:128 sc1\n" \
"buffer_store_dwordx4 v[92:95], v8, s[64:67], s95 offen offset:128 sc1\ns_branch zfin\ns_branch 99f\n.p2align 12\nzqT:\nzp19:\ns_setpc_b64 s[98:99]\n99:\n" \
"zot:\nMvaa 128,192,0\ns_waitcnt vmcnt(10)\nv_cmp_eq_u64 vcc, v[10:11], v[2:3]\ns_mov_b32 s79, s78\nMvaa 128,224,4\nbuffer_load_dwordx4 a[56:59], v9, s[60:63], s87 offen offset:0 sc1\ns_xor_b32 s82, s79, s10\n" \
"Mvaa 132,228,4\ns_lshl_b32 s81, s82, 16\ns_cmp_lt_u32 s79, 32\ns_cselect_b32 s82, s18, s19\nMvaa 132,196,0\ns_add_u32 s81, s81, s82\ns_bitcmp1_b64 vcc, s79\nMvaa 136,200,0\n" \
"Mvaa 136,232,4\nbuffer_load_dwordx4 a[60:63], v9, s[60:63], s87 offen offset:1024 sc1\nMvaa 140,236,4\nMvaa 140,204,0\nv_add_u32 v9, s81, v0\ns_waitcnt vmcnt(15)\ns_waitcnt vmcnt(27)\ns_barrier\n" \
"Mvaa 172,204,8\nTa 128,20,49152\nTa 130,20,50176\nMvaa 172,236,12\nTa 160,20,57344\nMvaa 168,232,12\ns_cbranch_scc0 zs105\nzr105:\n" \
"Mvaa 168,200,8\nbuffer_load_dwordx4 a[64:67], v9, s[60:63], 0 offen offset:0 sc1\nMvaa 164,196,8\nTa 162,20,58368\nMvaa 164,228,12\nTa 132,21,49152\nMvaa 160,224,12\nTa 134,21,50176\n" \
"Ta 164,21,57344\nMvaa 160,192,8\nbuffer_load_dwordx4 a[68:71], v9, s[60:63], 0 offen offset:1024 sc1\ns_waitcnt vmcnt(14)\nMvaa 192,192,16\nTa 166,21,58368\nMvaa 192,224,20\nTa 136,22,49152\n" \
"Mvaa 196,228,20\nTa 138,22,50176\nMvaa 196,196,16\nbuffer_load_dwordx4 a[72:75], v9, s[60:63], s85 offen offset:0 sc1\nMvaa 200,200,16\nTa 168,22,57344\nMvaa 200,232,20\nTa 170,22,58368\n" \
"Mvaa 204,236,20\nTa 140,23,49152\nMvaa 204,204,16\nbuffer_load_dwordx4 a[76:79], v9, s[60:63], s85 offen offset:1024 sc1\ns_waitcnt vmcnt(14)\nMvaa 236,204,24\nTa 142,23,50176\nMvaa 236,236,28\n" \
"Ta 172,23,57344\nMvaa 232,232,28\nTa 174,23,58368\nMvaa 232,200,24\nbuffer_load_dwordx4 a[80:83], v9, s[60:63], s86 offen offset:0 sc1\nMvaa 228,196,24\nMvaa 228,228,28\nMvaa 224,224,28\n" \
"Mvaa 224,192,24\nbuffer_load_dwordx4 a[84:87], v9, s[60:63], s86 offen offset:1024 sc1\ns_waitcnt vmcnt(14) lgkmcnt(0)\nMvaa 128,128,32\nv_xor_b32 v20, 0x8000, v20\nv_xor_b32 v21, 0x8000, v21\nMvaa 128,160,36\nv_xor_b32 v22, 0x8000, v22\n" \
"v_xor_b32 v23, 0x8000, v23\nMvaa 132,164,36\nMvaa 132,132,32\nbuffer_load_dwordx4 a[88:91], v9, s[60:63], s87 offen offset:0 sc1\nMvaa 136,136,32\nMvaa 136,168,36\nMvaa 140,172,36\nMvaa 140,140,32\n" \
"buffer_load_dwordx4 a[92:95], v9, s[60:63], s87 offen offset:1024 sc1\ns_waitcnt vmcnt(13)\ns_waitcnt vmcnt(23)\ns_barrier\nMvaa 172,140,40\nTa 192,20,32768\nTa 194,20,33792\nMvaa 172,172,44\n" \
"Ta 224,20,40960\nMvaa 168,168,44\nTa 226,20,41984\nMvaa 168,136,40\nv_add_u32 v9, 0x8000, v9\nMvaa 164,132,40\nTa 196,21,32768\nMvaa 164,164,44\n" \
"Ta 198,21,33792\nMvaa 160,160,44\nTa 228,21,40960\nMvaa 160,128,40\nbuffer_load_dwordx4 a[96:99], v9, s[60:63], 0 offen offset:0 sc1\ns_waitcnt vmcnt(12)\nMvaa 192,128,48\nTa 230,21,41984\n" \
"Mvaa 192,160,52\nTa 200,22,32768\nMvaa 196,164,52\nTa 202,22,33792\nMvaa 196,132,48\nbuffer_load_dwordx4 a[100:103], v9, s[60:63], 0 offen offset:1024 sc1\nMvaa 200,136,48\nTa 232,22,40960\n" \
"Mvaa 200,168,52\nTa 234,22,41984\nMvaa 204,172,52\nTa 204,23,32768\nMvaa 204,140,48\nbuffer_load_dwordx4 a[104:107], v9, s[60:63], s85 offen offset:0 sc1\ns_waitcnt vmcnt(11)\nMvaa 236,140,56\n" \
"Ta 206,23,33792\nMvaa 236,172,60\nTa 236,23,40960\nMvaa 232,168,60\nTa 238,23,41984\nMvaa 232,136,56\nbuffer_load_dwordx4 a[108:111], v9, s[60:63], s85 offen offset:1024 sc1\nMvaa 228,132,56\n" \
"Mvaa 228,164,60\nMvaa 224,160,60\nMvaa 224,128,56\nbuffer_load_dwordx4 a[112:115], v9, s[60:63], s86 offen offset:0 sc1\ns_waitcnt vmcnt(11) lgkmcnt(0)\nMvaa 128,192,64\nv_bfe_u32 v28, v0, 4, 2\ns_and_b32 s82, s27, -32\n" \
"Mvaa 128,224,68\nv_lshl_add_u32 v7, v28, 3, s82\nv_bfe_u32 v28, v0, 6, 2\nMvaa 132,228,68\nv_lshrrev_b32 v29, 8, v0\nv_bfe_u32 v30, v0, 4, 2\nMvaa 132,196,64\nbuffer_load_dwordx4 a[116:119], v9, s[60:63], s86 offen offset:1024 sc1\n" \
"Mvaa 136,200,64\nv_cmp_eq_u32 vcc, v28, v29\nv_lshlrev_b32 v30, 4, v30\nMvaa 136,232,68\nv_mov_b32 v28, 0x3f80\nv_mov_b32 v29, 0\nMvaa 140,236,68\nv_cndmask_b32 v28, 0, v28, vcc\n" \
"v_lshlrev_b64 v[98:99], v30, v[28:29]\nMvaa 140,204,64\nbuffer_load_dwordx4 a[120:123], v9, s[60:63], s87 offen offset:0 sc1\ns_waitcnt vmcnt(11)\ns_waitcnt vmcnt(17)\ns_barrier\nMvaa 172,204,72\nTa 128,20,49152\n" \
"v_mov_b32 v96, 0\nMvaa 172,236,76\nTa 130,20,50176\nv_mov_b32 v97, 0\nMvaa 168,232,76\nTa 160,20,57344\nv_mov_b32 v100, 0\nMvaa 168,200,72\n" \
"buffer_load_dwordx4 a[124:127], v9, s[60:63], s87 offen offset:1024 sc1\nMvaa 164,196,72\nTa 162,20,58368\nv_mov_b32 v101, 0\nMvaa 164,228,76\nTa 132,21,49152\ns_mov_b64 s[64:65], s[16:17]\nMvaa 160,224,76\n" \
"Ta 134,21,50176\ns_mov_b32 s66, 8388608\nMvaa 160,192,72\ns_waitcnt vmcnt(10)\nTa 164,21,57344\nMvaa 192,192,80\nTa 166,21,58368\nv_bfe_u32 v28, v0, 4, 4\n" \
"Mvaa 192,224,84\nv_lshrrev_b32 v29, 9, v0\nv_and_b32 v30, 7, v28\nMvaa 196,228,84\nTa 136,22,49152\nv_xor_b32 v29, v29, v30\nMvaa 196,196,80\nTa 138,22,50176\n" \
"s_lshl_b32 s82, s32, 2\nMvaa 200,200,80\nTa 168,22,57344\nv_lshrrev_b32 v30, 5, v0\nMvaa 200,232,84\nTa 170,22,58368\nv_lshlrev_b32 v28, 7, v28\nMvaa 204,236,84\n" \
"Ta 140,23,49152\nv_and_b32 v30, 8, v30\nMvaa 204,204,80\ns_waitcnt vmcnt(8)\nTa 142,23,50176\nMvaa 236,204,88\nTa 172,23,57344\nv_lshl_add_u32 v28, v29, 4, v28\n" \
"Mvaa 236,236,92\nTa 174,23,58368\nv_add3_u32 v12, v28, v30, s82\nMvaa 232,232,92\nv_xor_b32 v13, 32, v12\nv_xor_b32 v14, 64, v12\nMvaa 232,200,88\nv_xor_b32 v15, 96, v12\n" \
"v_lshrrev_b32 v28, 7, v0\nMvaa 228,196,88\nv_lshlrev_b32 v29, 4, v28\nv_xor_b32 v29, v29, v0\nMvaa 228,228,92\nv_add_u32 v5, s82, v29\nv_and_b32 v29, 0x70, v0\nMvaa 224,224,92\n" \
"v_lshl_add_u32 v6, v28, 12, v29\nv_add_u32 v8, 0x8000, v6\nMvaa 224,192,88\ns_waitcnt vmcnt(6) lgkmcnt(0)\nMvaa 128,128,96\nMvaa 128,160,100\nMvaa 132,164,100\nMvaa 132,132,96\n" \
"Mvaa 136,136,96\nTa 144,24,49152\nMvaa 136,168,100\nTa 146,24,50176\nMvaa 140,172,100\nTa 176,24,57344\nbuffer_load_dwordx2 v[10:11], v7, s[60:63], 0 offen sc1\nMvaa 140,140,96\n" \
"s_waitcnt vmcnt(5)\nMvaa 172,140,104\nTa 178,24,58368\nMvaa 172,172,108\nTa 148,25,49152\nMvaa 168,168,108\nMvaa 168,136,104\nTa 150,25,50176\n" \
"Mvaa 164,132,104\nTa 180,25,57344\nC 104,128,129\nC 105,130,131\nMvaa 164,164,108\nTa 182,25,58368\nC 106,132,133\nC 107,134,135\n" \
"Mvaa 160,160,108\nC 108,136,137\nC 109,138,139\nC 110,140,141\nMvaa 160,128,104\ns_waitcnt vmcnt(3)\nTa 152,26,49152\nC 111,142,143\n" \
"Mvaa 192,128,112\ns_add_u32 s95, s24, 0\nbuffer_store_dwordx4 v[104:107], v0, s[60:63], s95 offen offset:0 sc1\nMvaa 192,160,116\nTa 154,26,50176\nbuffer_store_dwordx4 v[108:111], v0, s[60:63], s95 offen offset:1024 sc1\nMvaa 196,164,116\nTa 184,26,57344\n" \
"Mvaa 196,132,112\nTa 186,26,58368\nMvaa 200,136,112\nC 112,160,161\nC 113,162,163\nC 114,164,165\nMvaa 200,168,116\nTa 156,27,49152\n" \
"C 115,166,167\nC 116,168,169\nMvaa 204,172,116\nTa 158,27,50176\nC 117,170,171\nC 118,172,173\nMvaa 204,140,112\ns_waitcnt vmcnt(3)\n" \
"C 119,174,175\nMvaa 236,140,120\nTa 188,27,57344\nbuffer_store_dwordx4 v[112:115], v0, s[60:63], s95 offen offset:2048 sc1\nMvaa 236,172,124\nTa 190,27,58368\nbuffer_store_dwordx4 v[116:119], v0, s[60:63], s95 offen offset:3072 sc1\nMvaa 232,168,124\n" \
"Mvaa 232,136,120\nMvaa 228,132,120\nv_xor_b32 v24, 0x8000, v24\nv_xor_b32 v25, 0x8000, v25\nC 104,192,193\nMvaa 228,164,124\nv_xor_b32 v26, 0x8000, v26\nv_xor_b32 v27, 0x8000, v27\n" \
"C 105,194,195\nMvaa 224,160,124\nC 106,196,197\nC 107,198,199\nC 108,200,201\nMvaa 224,128,120\nC 109,202,203\nC 110,204,205\n" \
"C 111,206,207\nMvaa 144,208,0\ns_add_u32 s95, s24, 4096\nbuffer_store_dwordx4 v[104:107], v0, s[60:63], s95 offen offset:0 sc1\nMvaa 144,240,4\nbuffer_store_dwordx4 v[108:111], v0, s[60:63], s95 offen offset:1024 sc1\nMvaa 148,244,4\nMvaa 148,212,0\n" \
"Mvaa 152,216,0\nC 112,224,225\nC 113,226,227\nC 114,228,229\nMvaa 152,248,4\nC 115,230,231\nC 116,232,233\nC 117,234,235\n" \
"Mvaa 156,252,4\nC 118,236,237\nC 119,238,239\nMvaa 156,220,0\nbuffer_store_dwordx4 v[112:115], v0, s[60:63], s95 offen offset:2048 sc1\nMvaa 188,220,8\nbuffer_store_dwordx4 v[116:119], v0, s[60:63], s95 offen offset:3072 sc1\nMvaa 188,252,12\n" \
"s_waitcnt vmcnt(8)\nv_cmp_eq_u64 vcc, v[10:11], v[2:3]\ns_andn2_b32 s82, 10, vcc_lo\nMvaa 184,248,12\nMvaa 184,216,8\nMvaa 180,212,8\ns_cbranch_scc1 zs106\nzr106:\n" \
"Mvaa 180,244,12\ns_add_u32 s82, s26, 32768\ns_add_u32 s81, s82, 0\nMvaa 176,240,12\nbuffer_load_dwordx4 v[32:35], v0, s[60:63], s81 offen offset:0 sc1\nMvaa 176,208,8\nMvaa 208,208,16\nMvaa 208,240,20\n" \
"Mvaa 212,244,20\nbuffer_load_dwordx4 v[36:39], v0, s[60:63], s81 offen offset:1024 sc1\nMvaa 212,212,16\nMvaa 216,216,16\nMvaa 216,248,20\nMvaa 220,252,20\nbuffer_load_dwordx4 v[40:43], v0, s[60:63], s81 offen offset:2048 sc1\nMvaa 220,220,16\n" \
"Mvaa 252,220,24\nMvaa 252,252,28\nMvaa 248,248,28\nbuffer_load_dwordx4 v[44:47], v0, s[60:63], s81 offen offset:3072 sc1\nMvaa 248,216,24\nMvaa 244,212,24\nMvaa 244,244,28\nMvaa 240,240,28\n" \
"s_add_u32 s81, s82, 4096\nMvaa 240,208,24\ns_waitcnt lgkmcnt(0)\nMvaa 144,144,32\nbuffer_load_dwordx4 v[48:51], v0, s[60:63], s81 offen offset:0 sc1\nMvaa 144,176,36\nTa 208,24,32768\nMvaa 148,180,36\n" \
"Ta 210,24,33792\nMvaa 148,148,32\nMvaa 152,152,32\nTa 240,24,40960\nMvaa 152,184,36\nTa 242,24,41984\nMvaa 156,188,36\nbuffer_load_dwordx4 v[52:55], v0, s[60:63], s81 offen offset:1024 sc1\n" \
"Mvaa 156,156,32\nTa 212,25,32768\nMvaa 188,156,40\nTa 214,25,33792\nMvaa 188,188,44\nMvaa 184,184,44\nTa 244,25,40960\nMvaa 184,152,40\n" \
"Ta 246,25,41984\nMvaa 180,148,40\nbuffer_load_dwordx4 v[56:59], v0, s[60:63], s81 offen offset:2048 sc1\nMvaa 180,180,44\nTa 216,26,32768\nMvaa 176,176,44\nMvaa 176,144,40\nTa 218,26,33792\n" \
"Mvaa 208,144,48\nTa 248,26,40960\nMvaa 208,176,52\nbuffer_load_dwordx4 v[60:63], v0, s[60:63], s81 offen offset:3072 sc1\nMvaa 212,180,52\nTa 250,26,41984\nMvaa 212,148,48\nTa 220,27,32768\n" \
"Mvaa 216,152,48\nMvaa 216,184,52\nTa 222,27,33792\ns_add_u32 s82, s26, 98304\nMvaa 220,188,52\nTa 252,27,40960\ns_add_u32 s81, s82, 0\nMvaa 220,156,48\n" \
"buffer_load_dwordx4 v[64:67], v0, s[60:63], s81 offen offset:0 sc1\nMvaa 252,156,56\nTa 254,27,41984\nMvaa 252,188,60\nMvaa 248,184,60\nMvaa 248,152,56\nbuffer_load_dwordx4 v[68:71], v0, s[60:63], s81 offen offset:1024 sc1\nMvaa 244,148,56\n" \
"Mvaa 244,180,60\nMvaa 240,176,60\nMvaa 240,144,56\ns_waitcnt lgkmcnt(0)\nMvaa 144,208,64\nbuffer_load_dwordx4 v[72:75], v0, s[60:63], s81 offen offset:2048 sc1\nMvaa 144,240,68\nTa 144,24,49152\n" \
"Mvaa 148,244,68\nTa 146,24,50176\nMvaa 148,212,64\nMvaa 152,216,64\nTa 176,24,57344\ns_waitcnt vmcnt(11)\nbuffer_store_dwordx2 v[2:3], off, s[60:63], s27 sc1\nMvaa 152,248,68\n" \
"Ta 178,24,58368\nMvaa 156,252,68\nbuffer_load_dwordx4 v[76:79], v0, s[60:63], s81 offen offset:3072 sc1\nMvaa 156,220,64\nTa 148,25,49152\nMvaa 188,220,72\nTa 150,25,50176\nMvaa 188,252,76\n" \
"Mvaa 184,248,76\nTa 180,25,57344\ns_add_u32 s81, s82, 4096\nMvaa 184,216,72\nTa 182,25,58368\nMvaa 180,212,72\nbuffer_load_dwordx4 v[80:83], v0, s[60:63], s81 offen offset:0 sc1\nMvaa 180,244,76\n" \
"Ta 152,26,49152\nMvaa 176,240,76\nMvaa 176,208,72\nTa 154,26,50176\nMvaa 208,208,80\nTa 184,26,57344\nMvaa 208,240,84\nbuffer_load_dwordx4 v[84:87], v0, s[60:63], s81 offen offset:1024 sc1\n" \
"Mvaa 212,244,84\nTa 186,26,58368\nMvaa 212,212,80\nTa 156,27,49152\nMvaa 216,216,80\nMvaa 216,248,84\nTa 158,27,50176\nMvaa 220,252,84\n" \
"Ta 188,27,57344\nMvaa 220,220,80\nbuffer_load_dwordx4 v[88:91], v0, s[60:63], s81 offen offset:2048 sc1\nMvaa 252,220,88\nTa 190,27,58368\nMvaa 252,252,92\nMvaa 248,248,92\nMvaa 248,216,88\n" \
"buffer_load_dwordx4 v[92:95], v0, s[60:63], s81 offen offset:3072 sc1\nMvaa 244,212,88\nMvaa 244,244,92\nMvaa 240,240,92\nMvaa 240,208,88\ns_waitcnt lgkmcnt(0)\nMvaa 144,144,96\nbuffer_load_dwordx2 v[10:11], v7, s[60:63], 0 offen sc1\n" \
"Mvaa 144,176,100\nMvaa 148,180,100\nMvaa 148,148,96\nMvaa 152,152,96\nMvaa 152,184,100\nMvaa 156,188,100\nMvaa 156,156,96\nMvaa 188,156,104\n" \
"Mvaa 188,188,108\nMvaa 184,184,108\nMvaa 184,152,104\nMvaa 180,148,104\nMvaa 180,180,108\nMvaa 176,176,108\nMvaa 176,144,104\nMvaa 208,144,112\n" \
"buffer_load_dwordx2 v[102:103], v7, s[60:63], 0 offen sc1\nMvaa 208,176,116\nMvaa 212,180,116\nMvaa 212,148,112\nMvaa 216,152,112\nMvaa 216,184,116\nMvaa 220,188,116\nMvaa 220,156,112\n" \
"Mvaa 252,156,120\nMvaa 252,188,124\nMvaa 248,184,124\nMvaa 248,152,120\nMvaa 244,148,120\nMvaa 244,180,124\nMvaa 240,176,124\nMvaa 240,144,120\n" \
"buffer_load_dwordx2 v[120:121], v7, s[60:63], 0 offen sc1\ns_waitcnt vmcnt(2)\nv_cmp_eq_u64 vcc, v[10:11], v[2:3]\ns_andn2_b32 s82, s6, vcc_lo\ns_cbranch_scc1 zpl\nzpe:\ns_waitcnt vmcnt(18)\nMvvv 144,98,32\n" \
"s_add_u32 s82, s26, s28\ns_add_u32 s81, s82, 0\nbuffer_load_dwordx4 a[0:3], v0, s[60:63], s81 offen offset:0 sc1\nMvvv 148,96,32\nMvvv 152,98,36\nMvvv 156,96,36\nbuffer_load_dwordx4 a[4:7], v0, s[60:63], s81 offen offset:1024 sc1\ns_waitcnt vmcnt(18)\n" \
"Mvvv 176,98,40\nMvvv 180,96,40\nMvvv 184,98,44\nbuffer_load_dwordx4 a[8:11], v0, s[60:63], s81 offen offset:2048 sc1\nMvvv 188,96,44\ns_waitcnt vmcnt(17)\nMvvv 208,98,48\nMvvv 212,96,48\n" \
"s_branch 99f\n.p2align 12\nzp20:\ns_setpc_b64 s[98:99]\n99:\nbuffer_load_dwordx4 a[12:15], v0, s[60:63], s81 offen offset:3072 sc1\nMvvv 216,98,52\nMvvv 220,96,52\n" \
"s_waitcnt vmcnt(16)\nMvvv 240,98,56\ns_add_u32 s81, s82, 4096\nbuffer_load_dwordx4 a[16:19], v0, s[60:63], s81 offen offset:0 sc1\nMvvv 244,96,56\nMvvv 248,98,60\nMvvv 252,96,60\nbuffer_load_dwordx4 a[20:23], v0, s[60:63], s81 offen offset:1024 sc1\n" \
"s_waitcnt vmcnt(16)\nMvvv 144,98,64\nMvvv 148,96,64\nMvvv 152,98,68\nbuffer_load_dwordx4 a[24:27], v0, s[60:63], s81 offen offset:2048 sc1\nMvvv 156,96,68\ns_waitcnt vmcnt(14)\nMvvv 176,98,72\n" \
"Mvvv 180,96,72\nbuffer_load_dwordx4 a[28:31], v0, s[60:63], s81 offen offset:3072 sc1\nMvvv 184,98,76\nMvvv 188,96,76\ns_waitcnt vmcnt(13)\nMvvv 208,98,80\nMvvv 212,96,80\nMvvv 216,98,84\n" \
"Mvvv 220,96,84\ns_waitcnt vmcnt(11)\nMvvv 240,98,88\nMvvv 244,96,88\nMvvv 248,98,92\nMvvv 252,96,92\ns_waitcnt vmcnt(6)\nMvva 144,98,0\n" \
"Mvva 148,96,0\nMvva 152,98,4\nMvva 156,96,4\ns_waitcnt vmcnt(4)\nMvva 176,98,8\nMvva 180,96,8\nMvva 184,98,12\nMvva 188,96,12\n" \
"C 64,144,145\nC 65,146,147\nC 66,148,149\nC 67,150,151\nds_write_b64 v12, v[64:65] offset:0\nC 68,152,153\nC 69,154,155\nds_write_b64 v13, v[66:67] offset:0\n" \
"C 70,156,157\nC 71,158,159\nds_write_b64 v14, v[68:69] offset:0\nds_write_b64 v15, v[70:71] offset:0\ns_waitcnt vmcnt(2)\nMvva 208,98,16\nMvva 212,96,16\nMvva 216,98,20\n" \
"Mvva 220,96,20\ns_waitcnt lgkmcnt(0)\nRv 64,5,0\nRv 68,5,1024\nC 72,176,177\nC 73,178,179\nC 74,180,181\nC 75,182,183\n" \
"ds_write_b64 v12, v[72:73] offset:2048\nC 76,184,185\nC 77,186,187\nds_write_b64 v13, v[74:75] offset:2048\nC 78,188,189\nC 79,190,191\nds_write_b64 v14, v[76:77] offset:2048\nds_write_b64 v15, v[78:79] offset:2048\n" \
"s_waitcnt vmcnt(0)\nMvva 240,98,24\nMvva 244,96,24\nMvva 248,98,28\nMvva 252,96,28\ns_waitcnt lgkmcnt(0)\nRv 72,5,2048\nRv 76,5,3072\n" \
"C 80,208,209\nC 81,210,211\nC 82,212,213\nC 83,214,215\nds_write_b64 v12, v[80:81] offset:4096\nC 84,216,217\nC 85,218,219\nds_write_b64 v13, v[82:83] offset:4096\n" \
"C 86,220,221\nC 87,222,223\nds_write_b64 v14, v[84:85] offset:4096\nds_write_b64 v15, v[86:87] offset:4096\ns_add_u32 s95, s29, 0\ns_waitcnt lgkmcnt(10)\nbuffer_store_dwordx4 v[64:67], v6, s[64:67], s95 offen offset:0 sc1\nbuffer_store_dwordx4 v[68:71], v8, s[64:67], s95 offen offset:0 sc1\n" \
"s_nop 4\ns_waitcnt lgkmcnt(0)\nRv 80,5,4096\nRv 84,5,5120\nC 88,240,241\nC 89,242,243\nC 90,244,245\nC 91,246,247\n" \
"ds_write_b64 v12, v[88:89] offset:6144\nC 92,248,249\nC 93,250,251\nds_write_b64 v13, v[90:91] offset:6144\nC 94,252,253\nC 95,254,255\nds_write_b64 v14, v[92:93] offset:6144\nds_write_b64 v15, v[94:95] offset:6144\n" \
"s_add_u32 s95, s29, 262144\ns_waitcnt lgkmcnt(10)\nbuffer_store_dwordx4 v[72:75], v6, s[64:67], s95 offen offset:0 sc1\nbuffer_store_dwordx4 v[76:79], v8, s[64:67], s95 offen offset:0 sc1\ns_waitcnt lgkmcnt(0)\nRv 88,5,6144\nRv 92,5,7168\ns_add_u32 s95, s30, 0\n" \
"s_waitcnt lgkmcnt(6)\nbuffer_store_dwordx4 v[80:83], v6, s[64:67], s95 offen offset:0 sc1\nbuffer_store_dwordx4 v[84:87], v8, s[64:67], s95 offen offset:0 sc1\ns_add_u32 s95, s30, 262144\ns_waitcnt lgkmcnt(0)\nbuffer_store_dwordx4 v[88:91], v6, s[64:67], s95 offen offset:0 sc1\nbuffer_store_dwordx4 v[92:95], v8, s[64:67], s95 offen offset:0 sc1\nzfin:\n" \
".subsection 1\ns_branch 99f\n.p2align 12\nzgv:\nzp21:\ns_setpc_b64 s[98:99]\n99:\nzV:\n" \
"Zvaa 32,0,64\nTa 96,8,8192\ns_lshr_b32 s7, s3, 5\nZvaa 36,4,64\nTa 98,8,9216\ns_lshr_b32 s5, 170, s7\nZvaa 44,4,68\nRa 32,27,0\n" \
"s_and_b32 s5, s5, 1\nZvaa 40,0,68\nTa 100,20,8192\ns_lshl_b32 s7, s7, 2\nZvaa 48,0,72\nTa 102,20,9216\ns_lshr_b32 s4, 0x12124444, s7\nZvaa 52,4,72\n" \
"Ra 36,27,2048\ns_and_b32 s4, s4, 15\nZvaa 60,4,76\nTa 104,21,8192\ns_xor_b32 s6, s5, s3\nZvaa 56,0,76\nTa 106,21,9216\ns_lshr_b32 s7, 0xff00, s4\n" \
"Zvaa 64,0,80\nRa 40,27,4096\ns_and_b32 s7, s7, 0xff\nZvaa 68,4,80\nTa 108,22,8192\ns_xor_b32 s10, s7, 0xff\nZvaa 76,4,84\nTa 110,22,9216\n" \
"s_lshl_b32 s7, s7, 8\nZvaa 72,0,84\nRa 44,27,6144\ns_or_b32 s7, s7, 0xff\nZvaa 80,0,88\nTa 112,23,8192\ns_add_u32 s8, s4, 5\nZvaa 84,4,88\n" \
"Ta 114,23,9216\ns_sub_u32 s4, 262, s4\nZvaa 92,4,92\nRa 48,27,8192\ns_add_u32 s9, s4, 0x101\nZvaa 88,0,92\nTa 116,24,8192\nZvaa 152,8,92\n" \
"Ta 118,24,9216\nZvaa 156,12,92\nRa 52,27,10240\nZvaa 148,12,88\nTa 120,25,8192\nZvaa 144,8,88\nTa 122,25,9216\nZvaa 136,8,84\n" \
"Ra 56,27,12288\nZvaa 140,12,84\nTa 124,26,8192\nZvaa 132,12,80\nTa 126,26,9216\nZvaa 128,8,80\nRa 60,27,14336\nZvaa 120,8,76\n" \
"AR 2,252\nAR 3,253\nZvaa 124,12,76\nv_readfirstlane_b32 s96, v2\nv_readfirstlane_b32 s97, v3\nZvaa 116,12,72\ns_mul_i32 s97, s97, 0x7f4a7c15\ns_mul_hi_u32 s98, s96, 0x7f4a7c15\n" \
"Zvaa 112,8,72\ns_add_u32 s97, s97, s98\ns_mul_i32 s98, s96, 0x9e3779b9\nZvaa 104,8,68\ns_add_u32 s97, s97, s98\ns_mul_i32 s96, s96, 0x7f4a7c15\nZvaa 108,12,68\ns_add_u32 s96, s96, 0xd9b4e019\n" \
"s_addc_u32 s97, s97, 0x632be59b\nZvaa 100,12,64\nv_mov_b32 v2, s96\nv_mov_b32 v3, s97\nZvaa 96,8,64\ns_mov_b32 s96, 3\nZvaa 160,16,64\ns_waitcnt lgkmcnt(0)\n" \
"s_barrier\nZvaa 164,20,64\ns_add_u32 m0, s88, 0\nZvaa 172,20,68\nD 5,64,0,0\nZvaa 168,16,68\ns_cmp_lt_u32 s3, 128\ns_cselect_b32 s4, s9, s4\n" \
"Zvaa 176,16,72\ns_bitcmp1_b32 s6, 0\ns_cselect_b32 s9, s4, s8\ns_cselect_b32 s7, s10, s7\nZvaa 180,20,72\ns_and_b32 s6, s6, 1\ns_lshr_b32 s10, s3, 1\nZvaa 188,20,76\n" \
"D 5,64,s73,1024\nZvaa 184,16,76\ns_and_b32 s10, s10, 2\ns_or_b32 s6, s6, s10\nZvaa 192,16,80\ns_lshl_b32 s6, s6, 3\ns_lshl_b32 s8, s7, s6\nZvaa 196,20,80\n" \
"s_bfe_u32 s10, s3, 0x20003\ns_add_u32 s6, s6, s10\nZvaa 204,20,84\nD 5,64,s74,2048\nZvaa 200,16,84\ns_bitset0_b32 s8, s6\ns_lshl_b32 s5, s5, 16\nZvaa 208,16,88\n" \
"s_or_b32 s9, s9, s5\nZvaa 212,20,88\nZvaa 220,20,92\nD 5,64,s75,3072\nZvaa 216,16,92\nZaaa 152,24,92\nZaaa 156,28,92\ns_add_u32 m0, s88, 4096\n" \
"Zaaa 148,28,88\nD 5,64,s76,0\nZaaa 144,24,88\nZaaa 136,24,84\nZaaa 140,28,84\nZaaa 132,28,80\nD 5,64,s77,1024\nZaaa 128,24,80\n" \
"Zvaa 248,24,76\nZvaa 252,28,76\nZvaa 244,28,72\nD 5,64,s78,2048\nZvaa 240,24,72\nZvaa 232,24,68\nZvaa 236,28,68\nZvaa 228,28,64\n" \
"D 5,64,s79,3072\nZvaa 224,24,64\ns_branch 99f\n.p2align 12\nzp22:\ns_setpc_b64 s[98:99]\n99:\ns_nop 0\n" \
"zgV:\nMvaa 32,32,96\nMvaa 36,36,96\ns_add_u32 m0, s32, 65536\nMvaa 44,36,100\nbuffer_load_dwordx4 v6, s[68:71], 0 offen offset:0 nt lds\nMvaa 40,32,100\nMvaa 48,32,104\n" \
"Mvaa 52,36,104\nMvaa 60,36,108\nbuffer_load_dwordx4 v6, s[68:71], s89 offen offset:1024 nt lds\nMvaa 56,32,108\nMvaa 64,32,112\nMvaa 68,36,112\nMvaa 76,36,116\nbuffer_load_dwordx4 v28, s[68:71], s90 offen offset:2048 nt lds\n" \
"Mvaa 72,32,116\nMvaa 80,32,120\nMvaa 84,36,120\nMvaa 92,36,124\nbuffer_load_dwordx4 v28, s[68:71], s91 offen offset:3072 nt lds\nMvaa 88,32,124\nMvaa 152,40,124\nMvaa 156,44,124\n" \
"s_add_u32 m0, s32, 81920\nMvaa 148,44,120\nbuffer_load_dwordx4 v6, s[68:71], s92 offen offset:0 nt lds\nMvaa 144,40,120\nMvaa 136,40,116\nMvaa 140,44,116\nMvaa 132,44,112\nbuffer_load_dwordx4 v6, s[68:71], s93 offen offset:1024 nt lds\n" \
"Mvaa 128,40,112\nMvaa 120,40,108\nMvaa 124,44,108\nMvaa 116,44,104\nbuffer_load_dwordx4 v28, s[68:71], s94 offen offset:2048 nt lds\nMvaa 112,40,104\nMvaa 104,40,100\nMvaa 108,44,100\n" \
"s_swappc_b64 s[98:99], s[20:21]\nMvaa 100,44,96\nbuffer_load_dwordx4 v28, s[68:71], s95 offen offset:3072 nt lds\nMvaa 96,40,96\ns_waitcnt vmcnt(17)\ns_barrier\nMvaa 160,48,96\nbuffer_load_dword v30, v4, s[52:55], 0 offen sc1\n" \
"Ta 64,8,32768\nv_add_u32 v5, 128, v5\nMvaa 164,52,96\nTa 66,8,33792\nv_add_u32 v6, s72, v6\nMvaa 172,52,100\nRa 0,7,32768\nv_add_u32 v28, s72, v28\n" \
"Mvaa 168,48,100\nTa 68,20,32768\nMvaa 176,48,104\nTa 70,20,33792\nMvaa 180,52,104\nRa 4,7,34816\nMvaa 188,52,108\nv_add_u32 v4, s44, v4\n" \
"Ta 72,21,32768\nMvaa 184,48,108\nTa 74,21,33792\nMvaa 192,48,112\nRa 8,7,36864\nMvaa 196,52,112\nTa 76,22,32768\nMvaa 204,52,116\n" \
"Ta 78,22,33792\nMvaa 200,48,116\nRa 12,7,38912\nMvaa 208,48,120\nTa 80,23,32768\nMvaa 212,52,120\nTa 82,23,33792\nMvaa 220,52,124\n" \
"Ra 16,7,40960\nMvaa 216,48,124\nTa 84,24,32768\nMaaa 152,56,124\nTa 86,24,33792\nMaaa 156,60,124\nRa 20,7,43008\nMaaa 148,60,120\n" \
"Ta 88,25,32768\nMaaa 144,56,120\nTa 90,25,33792\nMaaa 136,56,116\nRa 24,7,45056\nMaaa 140,60,116\nTa 92,26,32768\nMaaa 132,60,112\n" \
"Ta 94,26,33792\nMaaa 128,56,112\nRa 28,7,47104\nMvaa 248,56,108\nMvaa 252,60,108\nMvaa 244,60,104\nMvaa 240,56,104\nMvaa 232,56,100\n" \
"Mvaa 236,60,100\nMvaa 228,60,96\nMvaa 224,56,96\ns_waitcnt lgkmcnt(0)\nMvaa 32,0,64\nTa 96,8,40960\ns_and_b32 s4, s3, 4\nMvaa 36,4,64\n" \
"Ta 98,8,41984\ns_and_b32 s5, s3, 1\nMvaa 44,4,68\nRa 32,27,32768\ns_lshl_b32 s4, s4, 2\nMvaa 40,0,68\nTa 100,20,40960\ns_lshl3_add_u32 s4, s5, s4\n" \
"Mvaa 48,0,72\nTa 102,20,41984\ns_and_b32 s5, s3, 2\nMvaa 52,4,72\nRa 36,27,34816\ns_bfe_u32 s6, s3, 0x20003\nMvaa 60,4,76\nTa 104,21,40960\n" \
"s_lshl1_add_u32 s4, s5, s4\nMvaa 56,0,76\nTa 106,21,41984\ns_add_u32 s7, s4, s6\nMvaa 64,0,80\nRa 40,27,36864\ns_lshr_b32 s10, s11, 3\nMvaa 68,4,80\n" \
"Ta 108,22,40960\ns_and_b32 s22, s11, 7\nMvaa 76,4,84\nTa 110,22,41984\ns_lshl_b32 s4, s7, 8\nMvaa 72,0,84\nRa 44,27,38912\ns_lshl_b32 s5, s10, 9\n" \
"Mvaa 80,0,88\nTa 112,23,40960\ns_sub_u32 s4, s4, s5\nMvaa 84,4,88\nTa 114,23,41984\nv_add_u32 v14, s4, v16\nMvaa 92,4,92\nRa 48,27,40960\n" \
"s_mul_i32 s38, s7, 525312\nMvaa 88,0,92\nTa 116,24,40960\ns_add_u32 s38, s38, 17039360\nMvaa 152,8,92\nTa 118,24,41984\ns_lshr_b32 s4, s7, 1\nMvaa 156,12,92\n" \
"Ra 52,27,43008\ns_lshl_b32 s4, s4, 7\nMvaa 148,12,88\nTa 120,25,40960\nv_lshrrev_b32 v18, 1, v0\nMvaa 144,8,88\nTa 122,25,41984\ns_add_u32 s4, s4, 256\n" \
"Mvaa 136,8,84\nRa 56,27,45056\nv_and_b32 v18, 0x38, v18\nMvaa 140,12,84\nTa 124,26,40960\nv_add_u32 v1, s4, v18\nMvaa 132,12,80\nTa 126,26,41984\n" \
"s_lshr_b32 s23, s3, 5\nMvaa 128,8,80\nRa 60,27,47104\nMvaa 120,8,76\ns_cmp_lt_u32 s3, 128\ns_cselect_b32 s4, 0, 4\ns_cselect_b32 s5, 7, 23\nMvaa 124,12,76\n" \
"s_add_u32 s23, s23, s4\ns_sub_u32 s5, s5, s23\nMvaa 116,12,72\ns_lshl_b32 s42, s23, 2\ns_lshl_b32 s43, s5, 2\nMvaa 112,8,72\ns_sub_u32 s5, s5, s23\ns_lshl_b32 s37, s5, 18\n" \
"Mvaa 104,8,68\ns_lshl1_add_u32 s41, s5, -5\ns_lshl1_add_u32 s4, s23, -1\nMvaa 108,12,68\ns_lshl1_add_u32 s5, s23, -13\nMvaa 100,12,64\ns_cmp_lt_u32 s3, 128\ns_cselect_b32 s39, s4, 9\n" \
"s_cselect_b32 s40, 0, s5\nMvaa 96,8,64\ns_bfe_u32 s4, s32, 0x1000c\ns_lshr_b32 s5, s32, 13\nMvaa 160,16,64\ns_waitcnt lgkmcnt(0)\ns_barrier\nMvaa 164,20,64\n" \
"s_add_u32 m0, s88, 32768\ns_lshl1_add_u32 s4, s10, s4\nMvaa 172,20,68\nD 5,64,0,0\nMvaa 168,16,68\ns_lshl1_add_u32 s5, s22, s5\ns_mul_i32 s4, s4, 525312\nMvaa 176,16,72\n" \
"s_lshl_b32 s5, s5, 15\ns_add_u32 s4, s4, s5\nMvaa 180,20,72\ns_bitcmp1_b32 s33, 0\ns_cselect_b32 s5, 16810496, 0\ns_cselect_b32 s35, 8, 0\nMvaa 188,20,76\nD 5,64,s73,1024\n" \
"Mvaa 184,16,76\ns_add_u32 s4, s4, s5\ns_add_u32 s34, s4, 17039360\nMvaa 192,16,80\ns_lshl4_add_u32 s35, s11, s35\ns_add_u32 s35, s35, 256\nMvaa 196,20,80\ns_and_b32 s4, s3, 1\n" \
"s_lshl2_add_u32 s24, s4, s6\nMvaa 204,20,84\nD 5,64,s74,2048\nMvaa 200,16,84\ns_bfe_u32 s4, s3, 0x20001\ns_bfe_u32 s5, s3, 0x20005\nMvaa 208,16,88\ns_lshl2_add_u32 s25, s4, s5\n" \
"s_lshl4_add_u32 s26, s24, s25\nMvaa 212,20,88\nv_and_b32 v18, 0x70, v17\nv_lshlrev_b32 v19, 1, v17\nMvaa 220,20,92\nD 5,64,s75,3072\nMvaa 216,16,92\ns_sub_u32 s4, s24, s22\n" \
"v_sub_u32 v19, v19, v18\nMaaa 152,24,92\ns_lshl_b32 s4, s4, 19\nv_add_u32 v9, s4, v19\nMaaa 156,28,92\ns_add_u32 m0, s88, 36864\ns_lshl_b32 s4, s23, 18\nMaaa 148,28,88\n" \
"D 5,64,s76,0\nMaaa 144,24,88\ns_lshl_b32 s5, s22, 19\ns_sub_u32 s4, s4, s5\nMaaa 136,24,84\ns_mul_i32 s5, s32, 24\ns_sub_u32 s4, s4, s5\nMaaa 140,28,84\n" \
"v_add_u32 v13, s4, v19\nv_lshrrev_b32 v18, 13, v16\nMaaa 132,28,80\nD 5,64,s77,1024\nMaaa 128,24,80\nv_and_b32 v19, 0x70, v16\nv_mul_u32_u24 v18, 6144, v18\nMvaa 248,24,76\n" \
"s_lshl_b32 s4, s25, 7\nv_bfe_u32 v12, v16, 7, 1\nMvaa 252,28,76\nv_add3_u32 v18, v18, v19, s4\nv_xor_b32 v19, 1, v12\nMvaa 244,28,72\nD 5,64,s78,2048\nMvaa 240,24,72\n" \
"s_bfe_u32 s4, s32, 0x1000c\nv_lshl_add_u32 v10, v12, 11, v18\nMvaa 232,24,68\nv_lshl_add_u32 v11, v19, 11, v18\nv_xor_b32 v12, s4, v12\nMvaa 236,28,68\ns_mul_i32 s5, s32, 12\nv_mul_u32_u24 v12, 196608, v12\n" \
"Mvaa 228,28,64\nD 5,64,s79,3072\nMvaa 224,24,64\ns_sub_u32 s5, 4096, s5\nv_add3_u32 v12, v18, v12, s5\nMvaa 32,32,96\ns_lshl3_add_u32 s48, s26, 4096\ns_add_u32 s4, s48, 4096\n" \
"Mvaa 36,36,96\ns_add_u32 m0, s32, 98304\ns_lshl_b32 s5, s26, 13\nMvaa 44,36,100\nbuffer_load_dwordx4 v6, s[68:71], 0 offen offset:0 nt lds\nMvaa 40,32,100\ns_lshr_b32 s6, s32, 1\ns_add_u32 s5, s5, s6\n" \
"Mvaa 48,32,104\ns_add_u32 s5, s5, 262144\ns_and_b32 s6, s3, 127\nMvaa 52,36,104\ns_lshl_b32 s6, s6, 14\ns_add_u32 s6, s6, s32\nMvaa 60,36,108\nbuffer_load_dwordx4 v6, s[68:71], s89 offen offset:1024 nt lds\n" \
"Mvaa 56,32,108\ns_cmp_lt_u32 s3, 128\ns_cselect_b32 s36, s4, s35\ns_cselect_b32 s46, s5, s6\nMvaa 64,32,112\ns_cmp_ge_u32 s32, 0x2000\ns_cselect_b32 s4, 0, 16\nMvaa 68,36,112\n" \
"s_sub_u32 s27, s26, s4\ns_xor_b32 s5, s32, 0x2000\nMvaa 76,36,116\nbuffer_load_dwordx4 v28, s[68:71], s90 offen offset:2048 nt lds\nMvaa 72,32,116\ns_lshl_b32 s4, s27, 13\ns_lshr_b32 s5, s5, 1\nMvaa 80,32,120\n" \
"s_add_u32 s4, s4, s5\ns_add_u32 s4, s4, 262144\nMvaa 84,36,120\ns_lshr_b32 s5, s32, 13\ns_or_b32 s5, s5, s24\nMvaa 92,36,124\nbuffer_load_dwordx4 v28, s[68:71], s91 offen offset:3072 nt lds\nMvaa 88,32,124\n" \
"s_cmp_eq_u32 s5, 0\ns_cselect_b32 s51, 1, 0\ns_cselect_b32 s47, 0x7fff0000, s4\ns_cselect_b32 s27, s26, s27\nMvaa 152,40,124\ns_lshl3_add_u32 s49, s27, 4096\ns_lshl_b32 s4, s26, 15\nMvaa 156,44,124\n" \
"s_add_u32 m0, s32, 114688\ns_and_b32 s5, s32, 0x2000\nMvaa 148,44,120\nbuffer_load_dwordx4 v6, s[68:71], s92 offen offset:0 nt lds\nMvaa 144,40,120\ns_lshl1_add_u32 s4, s5, s4\ns_bfe_u32 s5, s32, 0x1000c\nMvaa 136,40,116\n" \
"s_lshl_b32 s5, s5, 10\ns_add_u32 s4, s4, s5\nMvaa 140,44,116\ns_add_u32 s50, s4, 50660352\ns_lshr_b32 s6, s6, 12\nMvaa 132,44,112\nbuffer_load_dwordx4 v6, s[68:71], s93 offen offset:1024 nt lds\nMvaa 128,40,112\n" \
"s_mov_b32 s5, 182784\ns_mov_b32 s27, 2097152\nMvaa 120,40,108\nv_lshlrev_b32 v18, 3, v0\nv_cmp_gt_u32 vcc, 224, v0\nMvaa 124,44,108\ns_cmp_lt_u32 s6, 410\ns_cselect_b32 s4, 0, 410\n" \
"s_cselect_b32 s45, 734720, s5\ns_cselect_b64 s[56:57], s[14:15], s[12:13]\ns_cselect_b32 s27, 8388608, s27\nMvaa 116,44,104\nbuffer_load_dwordx4 v28, s[68:71], s94 offen offset:2048 nt lds\nMvaa 112,40,104\ns_sub_u32 s6, s6, s4\ns_mul_i32 s6, s6, 1792\n" \
"Mvaa 104,40,100\ns_cmp_lt_u32 s3, 128\ns_cselect_b32 s58, 0, s27\nMvaa 108,44,100\ns_swappc_b64 s[98:99], s[18:19]\ns_add_u32 s18, s18, 0x4000\ns_addc_u32 s19, s19, 0\nMvaa 100,44,96\n" \
"buffer_load_dwordx4 v28, s[68:71], s95 offen offset:3072 nt lds\nMvaa 96,40,96\ns_waitcnt vmcnt(17)\ns_barrier\nMvaa 160,48,96\nbuffer_load_dword v30, v4, s[52:55], 0 offen sc1\nTa 64,8,0\nv_add_u32 v5, 128, v5\n" \
"Mvaa 164,52,96\nTa 66,8,1024\nv_add_u32 v6, s72, v6\nMvaa 172,52,100\nRa 0,7,0\nv_add_u32 v28, s72, v28\nMvaa 168,48,100\nTa 68,20,0\n" \
"v_add_u32 v18, s6, v18\nMvaa 176,48,104\nTa 70,20,1024\ns_mov_b32 s59, 0x20000\nMvaa 180,52,104\nRa 4,7,2048\nv_cndmask_b32 v15, 2.0, v18, vcc\nMvaa 188,52,108\n" \
"v_add_u32 v4, s44, v4\nTa 72,21,0\nMvaa 184,48,108\nTa 74,21,1024\nMvaa 192,48,112\nRa 8,7,4096\nMvaa 196,52,112\nTa 76,22,0\n" \
"Mvaa 204,52,116\nTa 78,22,1024\nMvaa 200,48,116\nRa 12,7,6144\nMvaa 208,48,120\nTa 80,23,0\nMvaa 212,52,120\nTa 82,23,1024\n" \
"Mvaa 220,52,124\nRa 16,7,8192\nMvaa 216,48,124\nTa 84,24,0\nMaaa 152,56,124\nTa 86,24,1024\nMaaa 156,60,124\nRa 20,7,10240\n" \
"Maaa 148,60,120\nTa 88,25,0\nMaaa 144,56,120\nTa 90,25,1024\nMaaa 136,56,116\nRa 24,7,12288\nMaaa 140,60,116\nTa 92,26,0\n" \
"Maaa 132,60,112\nTa 94,26,1024\nMaaa 128,56,112\nRa 28,7,14336\nMvaa 248,56,108\nMvaa 252,60,108\nMvaa 244,60,104\nMvaa 240,56,104\n" \
"s_sub_u32 s96, s96, 1\nMvaa 232,56,100\ns_cmp_lg_u32 s96, 0\nMvaa 236,60,100\nMvaa 228,60,96\nMvaa 224,56,96\ns_waitcnt lgkmcnt(0)\ns_cbranch_scc0 zhV\n" \
"Mvaa 32,0,64\nTa 96,8,8192\nMvaa 36,4,64\nTa 98,8,9216\nMvaa 44,4,68\nRa 32,27,0\nMvaa 40,0,68\nTa 100,20,8192\n" \
"Mvaa 48,0,72\nTa 102,20,9216\nMvaa 52,4,72\nRa 36,27,2048\nMvaa 60,4,76\nTa 104,21,8192\nMvaa 56,0,76\nTa 106,21,9216\n" \
"Mvaa 64,0,80\nRa 40,27,4096\nMvaa 68,4,80\nTa 108,22,8192\nMvaa 76,4,84\nTa 110,22,9216\nMvaa 72,0,84\nRa 44,27,6144\n" \
"Mvaa 80,0,88\nTa 112,23,8192\nMvaa 84,4,88\nTa 114,23,9216\nMvaa 92,4,92\nRa 48,27,8192\nMvaa 88,0,92\nTa 116,24,8192\n" \
"Mvaa 152,8,92\nTa 118,24,9216\nMvaa 156,12,92\nRa 52,27,10240\nMvaa 148,12,88\nTa 120,25,8192\nMvaa 144,8,88\nTa 122,25,9216\n" \
"Mvaa 136,8,84\nRa 56,27,12288\nMvaa 140,12,84\nTa 124,26,8192\nMvaa 132,12,80\nTa 126,26,9216\nMvaa 128,8,80\nRa 60,27,14336\n" \
"Mvaa 120,8,76\nMvaa 124,12,76\nMvaa 116,12,72\nMvaa 112,8,72\nMvaa 104,8,68\nMvaa 108,12,68\nMvaa 100,12,64\nMvaa 96,8,64\n" \
"Mvaa 160,16,64\ns_waitcnt lgkmcnt(0)\ns_barrier\nMvaa 164,20,64\ns_add_u32 m0, s88, 0\nMvaa 172,20,68\nD 5,64,0,0\nMvaa 168,16,68\n" \
"Mvaa 176,16,72\nMvaa 180,20,72\nMvaa 188,20,76\nD 5,64,s73,1024\nMvaa 184,16,76\nMvaa 192,16,80\nMvaa 196,20,80\nMvaa 204,20,84\n" \
"D 5,64,s74,2048\nMvaa 200,16,84\nMvaa 208,16,88\nMvaa 212,20,88\nMvaa 220,20,92\nD 5,64,s75,3072\nMvaa 216,16,92\nMaaa 152,24,92\n" \
"Maaa 156,28,92\ns_add_u32 m0, s88, 4096\nMaaa 148,28,88\nD 5,64,s76,0\nMaaa 144,24,88\nMaaa 136,24,84\nMaaa 140,28,84\nMaaa 132,28,80\n" \
"D 5,64,s77,1024\nMaaa 128,24,80\nMvaa 248,24,76\nMvaa 252,28,76\nMvaa 244,28,72\nD 5,64,s78,2048\nMvaa 240,24,72\nMvaa 232,24,68\n" \
"Mvaa 236,28,68\nMvaa 228,28,64\nD 5,64,s79,3072\nMvaa 224,24,64\ns_branch zgV\nzhV:\ns_bitcmp1_b32 s33, 1\ns_cbranch_scc1 zxV\n" \
"Mvaa 32,0,64\nTa 96,8,8192\ns_mov_b64 s[64:65], s[84:85]\nMvaa 36,4,64\nTa 98,8,9216\ns_mov_b32 s66, 4194304\nMvaa 44,4,68\nRa 32,27,0\n" \
"s_mov_b64 s[68:69], s[86:87]\nMvaa 40,0,68\nTa 100,20,8192\ns_branch 99f\n.p2align 12\nzp23:\ns_setpc_b64 s[98:99]\n99:\n" \
"s_mov_b32 s70, 8388608\nMvaa 48,0,72\nTa 102,20,9216\ns_mov_b32 s73, 15360\nMvaa 52,4,72\nRa 36,27,2048\ns_mov_b32 s74, 129024\nMvaa 60,4,76\n" \
"Ta 104,21,8192\ns_mov_b32 s75, 144384\nMvaa 56,0,76\nTa 106,21,9216\ns_add_u32 s76, s37, 0\nMvaa 64,0,80\nRa 40,27,4096\ns_add_u32 s77, s37, 15360\n" \
"Mvaa 68,4,80\nTa 108,22,8192\ns_add_u32 s78, s37, 129024\nMvaa 76,4,84\nTa 110,22,9216\ns_add_u32 s79, s37, 144384\nMvaa 72,0,84\nRa 44,27,6144\n" \
"s_mov_b32 s89, 31744\nMvaa 80,0,88\nTa 112,23,8192\ns_mov_b32 s90, 63488\nMvaa 84,4,88\nTa 114,23,9216\ns_mov_b32 s91, 95232\nMvaa 92,4,92\n" \
"Ra 48,27,8192\ns_mov_b32 s72, 524288\nMvaa 88,0,92\nTa 116,24,8192\nv_xor_b32 v28, 128, v14\nMvaa 152,8,92\nTa 118,24,9216\nMvaa 156,12,92\n" \
"Ra 52,27,10240\nMvaa 148,12,88\nTa 120,25,8192\nMvaa 144,8,88\nTa 122,25,9216\nMvaa 136,8,84\nRa 56,27,12288\nMvaa 140,12,84\n" \
"Ta 124,26,8192\nMvaa 132,12,80\nTa 126,26,9216\nMvaa 128,8,80\nRa 60,27,14336\nMvaa 120,8,76\nMvaa 124,12,76\nMvaa 116,12,72\n" \
"Mvaa 112,8,72\nMvaa 104,8,68\nMvaa 108,12,68\nMvaa 100,12,64\nMvaa 96,8,64\nMvaa 160,16,64\ns_waitcnt lgkmcnt(0)\ns_barrier\n" \
"Mvaa 164,20,64\ns_add_u32 m0, s88, 131072\ns_and_b32 s10, s3, 2\nMvaa 172,20,68\nD 13,64,0,0\nMvaa 168,16,68\ns_bfe_u32 s100, s3, 0x10002\ns_lshl_b32 s10, s10, 1\n" \
"Mvaa 176,16,72\ns_bfe_u32 s2, s9, 0x10010\ns_xor_b32 s99, s2, s3\nMvaa 180,20,72\ns_and_b32 s99, s99, 1\ns_lshl1_add_u32 s99, s100, s99\nMvaa 188,20,76\nD 13,64,s73,1024\n" \
"Mvaa 184,16,76\ns_bfe_u32 s101, s3, 0x20003\ns_lshl3_add_u32 s11, s99, s101\nMvaa 192,16,80\ns_add_u32 s101, s101, s10\ns_lshl3_add_u32 s10, s2, s10\nMvaa 196,20,80\ns_bfe_u32 s2, s3, 0x20005\n" \
"s_lshl1_add_u32 s2, s2, s100\nMvaa 204,20,84\nD 13,64,s74,2048\nMvaa 200,16,84\ns_bfe_u32 s23, s9, 0x80008\ns_and_b32 s22, s9, 255\nMvaa 208,16,88\ns_lshr_b32 s6, 4, s99\n" \
"s_lshr_b32 s25, s3, 5\nMvaa 212,20,88\ns_lshl_b32 s4, s25, 8\ns_lshl_b32 s18, s25, 21\nMvaa 220,20,92\nD 13,64,s75,3072\nMvaa 216,16,92\ns_lshl3_add_u32 s25, s25, s101\n" \
"s_lshl_b32 s26, s25, 18\nMaaa 152,24,92\ns_lshl_b32 s27, s25, 7\ns_add_u32 s4, s4, 16384\nMaaa 156,28,92\ns_add_u32 m0, s88, 135168\ns_xor_b32 s25, s11, s10\nMaaa 148,28,88\n" \
"D 13,64,s76,0\nMaaa 144,24,88\ns_lshl3_add_u32 s31, s25, s4\ns_lshl_b32 s5, s2, 7\nMaaa 136,24,84\ns_add_u32 s5, s5, 8192\ns_lshr_b32 s25, s32, 1\nMaaa 140,28,84\n" \
"s_add_u32 s18, s18, s25\ns_add_u32 s18, s18, 54854656\nMaaa 132,28,80\nD 13,64,s77,1024\nMaaa 128,24,80\ns_lshl_b32 s19, s2, 19\ns_add_u32 s19, s19, s25\nMvaa 248,24,76\n" \
"s_add_u32 s19, s19, 48563200\ns_lshr_b32 s25, s32, 7\nMvaa 252,28,76\ns_add_u32 s27, s27, s25\ns_lshl3_add_u32 s27, s99, s27\nMvaa 244,28,72\nD 13,64,s78,2048\nMvaa 240,24,72\n" \
"s_add_u32 s27, s27, 32768\ns_lshl1_add_u32 s26, s32, s26\nMvaa 232,24,68\ns_add_u32 s26, s26, 71631872\ns_lshl_b32 s28, s99, 15\nMvaa 236,28,68\ns_add_u32 s24, s26, s28\ns_sub_u32 s28, 0x10000, s28\n" \
"Mvaa 228,28,64\nD 13,64,s79,3072\nMvaa 224,24,64\ns_lshl_b32 s25, s100, 17\ns_add_u32 s26, s26, s25\nMvaa 32,32,96\ns_cmp_eq_u32 s99, 0\ns_cselect_b32 s21, 128, 0\n" \
"Mvaa 36,36,96\ns_add_u32 m0, s32, 81920\ns_lshl_b32 s25, s21, 10\nMvaa 44,36,100\nD 14,68,0,0\nMvaa 40,32,100\ns_add_u32 s24, s24, s25\ns_add_u32 s25, s24, 0x20000\n" \
"Mvaa 48,32,104\ns_lshl_b32 s101, s101, 8\ns_and_b32 s20, s32, 0x2000\nMvaa 52,36,104\ns_lshl3_add_u32 s20, s20, s101\ns_and_b32 s99, s32, 0x1000\nMvaa 60,36,108\nD 14,68,s89,1024\n" \
"Mvaa 56,32,108\ns_lshl2_add_u32 s20, s99, s20\ns_lshl4_add_u32 s101, s32, s101\nMvaa 64,32,112\ns_lshl_b32 s2, s2, 20\ns_add_u32 s2, s2, s101\nMvaa 68,36,112\ns_lshl_b32 s100, s100, 7\n" \
"s_add_u32 s101, s101, s100\nMvaa 76,36,116\nD 28,68,s90,2048\nMvaa 72,32,116\ns_add_u32 s101, s101, 2048\ns_lshl_b32 s29, s42, 17\nMvaa 80,32,120\ns_lshl_b32 s30, s43, 17\n" \
"s_add_u32 s29, s29, s101\nMvaa 84,36,120\ns_add_u32 s30, s30, s101\ns_add_u32 s101, s2, 524288\nMvaa 92,36,124\nD 28,68,s91,3072\nMvaa 88,32,124\ns_cmp_eq_u32 s23, 2\n" \
"s_cselect_b32 s29, s2, s29\ns_cselect_b32 s30, s101, s30\ns_cselect_b32 s7, 0xffff, 0\ns_cselect_b32 s9, 0xff, 0\nMvaa 152,40,124\ns_and_b32 s101, s10, 8\ns_lshl_b32 s9, s9, s101\nMvaa 156,44,124\n" \
"s_add_u32 m0, s88, 0\nv_add_u32 v13, 128, v13\nMvaa 148,44,120\nD 13,64,0,0\nMvaa 144,40,120\ns_mov_b32 s2, zqS-zq0-8192\nMvaa 136,40,116\ns_cmp_eq_u32 s23, 1\n" \
"s_cselect_b32 s2, zqR-zq0-8192, s2\nMvaa 140,44,116\ns_cmp_eq_u32 s23, 0\ns_cselect_b32 s2, zqT-zq0-8192, s2\nMvaa 132,44,112\nD 13,64,s73,1024\nMvaa 128,40,112\ns_cmp_lt_u32 s32, 0x2000\n" \
"s_cselect_b32 s2, 0, s2\nMvaa 120,40,108\ns_add_u32 s2, s2, s32\nMvaa 124,44,108\nMvaa 116,44,104\nD 13,64,s74,2048\nMvaa 112,40,104\nMvaa 104,40,100\n" \
"Mvaa 108,44,100\nMvaa 100,44,96\nD 13,64,s75,3072\nMvaa 96,40,96\ns_waitcnt vmcnt(17)\ns_barrier\nMvaa 160,48,96\nbuffer_load_dword v30, v4, s[52:55], 0 offen sc1\n" \
"Ta 64,8,32768\nMvaa 164,52,96\nTa 66,8,33792\nMvaa 172,52,100\nRa 0,7,32768\nMvaa 168,48,100\nTa 68,20,32768\nMvaa 176,48,104\n" \
"Ta 70,20,33792\nMvaa 180,52,104\nRa 4,7,34816\nMvaa 188,52,108\nv_add_u32 v4, s44, v4\nTa 72,21,32768\nMvaa 184,48,108\nTa 74,21,33792\n" \
"Mvaa 192,48,112\nRa 8,7,36864\nMvaa 196,52,112\nTa 76,22,32768\nMvaa 204,52,116\nTa 78,22,33792\nMvaa 200,48,116\nRa 12,7,38912\n" \
"Mvaa 208,48,120\nTa 80,23,32768\nMvaa 212,52,120\nTa 82,23,33792\nMvaa 220,52,124\nRa 16,7,40960\nMvaa 216,48,124\nTa 84,24,32768\n" \
"Maaa 152,56,124\nTa 86,24,33792\nMaaa 156,60,124\nRa 20,7,43008\nMaaa 148,60,120\nTa 88,25,32768\nMaaa 144,56,120\nTa 90,25,33792\n" \
"Maaa 136,56,116\nRa 24,7,45056\nMaaa 140,60,116\nTa 92,26,32768\nMaaa 132,60,112\nTa 94,26,33792\nMaaa 128,56,112\nRa 28,7,47104\n" \
"Mvaa 248,56,108\nMvaa 252,60,108\nMvaa 244,60,104\nMvaa 240,56,104\nMvaa 232,56,100\nMvaa 236,60,100\nMvaa 228,60,96\nMvaa 224,56,96\n" \
"s_waitcnt lgkmcnt(0)\nMvaa 32,0,64\nTa 96,8,40960\nMvaa 36,4,64\nTa 98,8,41984\nMvaa 44,4,68\nRa 32,27,32768\nMvaa 40,0,68\n" \
"Ta 100,20,40960\nMvaa 48,0,72\nTa 102,20,41984\nMvaa 52,4,72\nRa 36,27,34816\nMvaa 60,4,76\nTa 104,21,40960\nMvaa 56,0,76\n" \
"Ta 106,21,41984\nMvaa 64,0,80\nRa 40,27,36864\nMvaa 68,4,80\nTa 108,22,40960\nMvaa 76,4,84\nTa 110,22,41984\nMvaa 72,0,84\n" \
"Ra 44,27,38912\nMvaa 80,0,88\nTa 112,23,40960\nMvaa 84,4,88\nTa 114,23,41984\nMvaa 92,4,92\nRa 48,27,40960\nMvaa 88,0,92\n" \
"Ta 116,24,40960\nMvaa 152,8,92\nTa 118,24,41984\nMvaa 156,12,92\nRa 52,27,43008\nMvaa 148,12,88\nTa 120,25,40960\nMvaa 144,8,88\n" \
"Ta 122,25,41984\nMvaa 136,8,84\nRa 56,27,45056\nMvaa 140,12,84\nTa 124,26,40960\nMvaa 132,12,80\nTa 126,26,41984\nMvaa 128,8,80\n" \
"Ra 60,27,47104\nMvaa 120,8,76\ns_and_b32 s96, s32, 0x1000\ns_lshl_b32 s97, s96, 1\ns_lshl_b32 s96, s96, 2\nMvaa 124,12,76\nv_add_u32 v16, s97, v7\nv_subrev_u32 v20, s96, v8\n" \
"v_xor_b32 v17, 64, v16\nMvaa 116,12,72\nv_add_u32 v18, 0x20000, v16\nv_add_u32 v19, 0x20000, v17\nv_xor_b32 v21, 32, v20\nMvaa 112,8,72\nv_xor_b32 v22, 64, v20\nv_xor_b32 v23, 96, v20\n" \
"v_xor_b32 v24, 128, v20\nMvaa 104,8,68\nv_xor_b32 v25, 160, v20\nv_xor_b32 v26, 192, v20\nv_xor_b32 v27, 224, v20\nMvaa 108,12,68\nMvaa 100,12,64\nMvaa 96,8,64\n" \
"Mvaa 160,16,64\ns_waitcnt vmcnt(5) lgkmcnt(0)\ns_barrier\nMvaa 164,20,64\ns_add_u32 m0, s88, 4096\nMvaa 172,20,68\nD 13,64,s76,0\nMvaa 168,16,68\n" \
"Ta 160,20,24576\nMvaa 176,16,72\nTa 162,20,25600\nMvaa 180,20,72\nD 13,64,s77,1024\nMvaa 188,20,76\nTa 164,21,24576\nMvaa 184,16,76\n" \
"Ta 166,21,25600\nMvaa 192,16,80\nD 13,64,s78,2048\nMvaa 196,20,80\nTa 168,22,24576\nMvaa 204,20,84\nTa 170,22,25600\nMvaa 200,16,84\n" \
"D 13,64,s79,3072\nMvaa 208,16,88\nTa 172,23,24576\nMvaa 212,20,88\ns_add_u32 m0, s32, 65536\nv_add_u32 v14, s72, v14\nv_add_u32 v28, s72, v28\nMvaa 220,20,92\n" \
"D 14,68,0,0\nMvaa 216,16,92\nTa 174,23,25600\nMaaa 152,24,92\nTa 176,24,24576\nMaaa 156,28,92\nD 14,68,s89,1024\nMaaa 148,28,88\n" \
"Ta 178,24,25600\nMaaa 144,24,88\nTa 180,25,24576\nMaaa 136,24,84\nD 28,68,s90,2048\nMaaa 140,28,84\nTa 182,25,25600\nMaaa 132,28,80\n" \
"Ta 184,26,24576\nMaaa 128,24,80\nD 28,68,s91,3072\nMvaa 248,24,76\nTa 186,26,25600\nMvaa 252,28,76\ns_add_u32 m0, s88, 32768\nv_add_u32 v13, 128, v13\n" \
"Mvaa 244,28,72\nD 13,64,0,0\nMvaa 240,24,72\nTa 188,27,24576\nMvaa 232,24,68\nTa 190,27,25600\nMvaa 236,28,68\nD 13,64,s73,1024\n" \
"Mvaa 228,28,64\nMvaa 224,24,64\nMvaa 32,32,96\nD 13,64,s74,2048\nMvaa 36,36,96\nTa 64,20,16384\nMvaa 40,32,100\nTa 66,20,17408\n" \
"Mvaa 44,36,100\nD 13,64,s75,3072\nMvaa 48,32,104\nMvaa 52,36,104\ns_add_u32 m0, s88, 36864\nMvaa 56,32,108\nD 13,64,s76,0\nMvaa 60,36,108\n" \
"Ra 0,18,0\nMvaa 64,32,112\nMvaa 68,36,112\nD 13,64,s77,1024\nMvaa 72,32,116\nRa 16,19,0\nC 32,32,33\nC 33,34,35\n" \
"Mvaa 76,36,116\nC 34,36,37\nC 35,38,39\nC 36,40,41\nMvaa 80,32,120\nD 13,64,s78,2048\nC 37,42,43\nMvaa 84,36,120\n" \
"Ta 68,21,16384\nC 38,44,45\nC 39,46,47\nMvaa 88,32,124\nC 40,48,49\nC 41,50,51\nC 42,52,53\nMvaa 92,36,124\n" \
"D 13,64,s79,3072\nC 43,54,55\nMvaa 96,40,96\nTa 70,21,17408\nC 44,56,57\nC 45,58,59\nMvaa 100,44,96\ns_add_u32 m0, s32, 98304\n" \
"v_add_u32 v14, s72, v14\nv_add_u32 v28, s72, v28\nMvaa 104,40,100\nD 14,68,0,0\nC 46,60,61\nMvaa 108,44,100\nC 47,62,63\nC 48,64,65\n" \
"C 49,66,67\nMvaa 112,40,104\nTa 72,22,16384\nC 50,68,69\nC 51,70,71\nMvaa 116,44,104\nD 14,68,s89,1024\nC 52,72,73\n" \
"Mvaa 120,40,108\nTa 74,22,17408\nC 53,74,75\nC 54,76,77\nMvaa 124,44,108\nC 55,78,79\nC 56,80,81\nC 57,82,83\n" \
"Mvaa 128,40,112\nD 28,68,s90,2048\nC 58,84,85\nMvaa 132,44,112\nTa 76,23,16384\nC 59,86,87\nC 60,88,89\nMvaa 136,40,116\n" \
"C 61,90,91\nC 62,92,93\nC 63,94,95\nMvaa 140,44,116\nD 28,68,s91,3072\nC 64,96,97\nMvaa 144,40,120\nTa 78,23,17408\n" \
"C 65,98,99\nC 66,100,101\nMvaa 148,44,120\nC 67,102,103\nC 68,104,105\nC 69,106,107\nMvaa 152,40,124\nbuffer_load_dword v30, v4, s[52:55], 0 offen sc1\n" \
"C 70,108,109\nMvaa 156,44,124\nTa 80,24,16384\nC 71,110,111\nC 72,112,113\nMaaa 128,56,112\nC 73,114,115\nC 74,116,117\n" \
"C 75,118,119\nMaaa 132,60,112\nTa 82,24,17408\nC 76,120,121\nC 77,122,123\nMaaa 136,56,116\nC 78,124,125\nC 79,126,127\n" \
"C 80,128,129\nMaaa 140,60,116\nTa 84,25,16384\nC 81,130,131\nC 82,132,133\nMaaa 144,56,120\nv_add_u32 v4, s44, v4\nC 83,134,135\n" \
"C 84,136,137\nMaaa 148,60,120\nTa 86,25,17408\nC 85,138,139\nC 86,140,141\ns_branch 99f\n.p2align 12\nzp24:\n" \
"s_setpc_b64 s[98:99]\n99:\nMaaa 152,56,124\nC 87,142,143\nC 88,144,145\nC 89,146,147\nMaaa 156,60,124\nTa 88,26,16384\n" \
"C 90,148,149\nC 91,150,151\nMvaa 160,48,96\nC 92,152,153\nC 93,154,155\nC 94,156,157\nMvaa 164,52,96\nTa 90,26,17408\n" \
"C 95,158,159\nMvaa 168,48,100\nAR 96,128\nAR 97,129\nAR 98,130\nMvaa 172,52,100\nTa 92,27,16384\nAR 99,131\n" \
"AR 100,132\nMvaa 176,48,104\nAR 101,133\nAR 102,134\nAR 103,135\nMvaa 180,52,104\nTa 94,27,17408\nC 96,96,97\n" \
"C 97,98,99\nMvaa 184,48,108\nC 98,100,101\nC 99,102,103\nAW 240,96\nMvaa 188,52,108\nRa 4,18,2048\nAW 241,97\n" \
"AW 242,98\nMvaa 192,48,112\nAW 243,99\nAR 104,136\nAR 105,137\nMvaa 196,52,112\nRa 20,19,2048\nAR 106,138\n" \
"AR 107,139\nMvaa 200,48,116\nAR 108,140\nAR 109,141\nAR 110,142\nMvaa 204,52,116\nRa 8,18,4096\nAR 111,143\n" \
"C 104,104,105\nMvaa 208,48,120\nC 105,106,107\nC 106,108,109\nC 107,110,111\nMvaa 212,52,120\nRa 24,19,4096\nAW 244,104\n" \
"AW 245,105\nMvaa 216,48,124\nAW 246,106\nAW 247,107\nAR 112,144\nMvaa 220,52,124\nRa 12,18,6144\nAR 113,145\n" \
"AR 114,146\nMvaa 224,56,96\nAR 115,147\nAR 116,148\nAR 117,149\nMvaa 228,60,96\nRa 28,19,6144\nAR 118,150\n" \
"AR 119,151\nMvaa 232,56,100\nC 112,112,113\nC 113,114,115\nC 114,116,117\nMvaa 236,60,100\nC 115,118,119\nAW 248,112\n" \
"AW 249,113\nMvaa 240,56,104\nAW 250,114\nAW 251,115\nAR 120,152\nMvaa 244,60,104\nAR 121,153\nAR 122,154\n" \
"AR 123,155\nMvaa 248,56,108\nAR 124,156\nAR 125,157\nAR 126,158\nMvaa 252,60,108\nAR 127,159\nC 120,120,121\n" \
"C 121,122,123\ns_waitcnt lgkmcnt(6)\ns_branch zY\nzxV:\nMvaa 32,0,64\nTa 96,8,8192\ns_mov_b64 s[64:65], s[80:81]\nMvaa 36,4,64\n" \
"Ta 98,8,9216\ns_mov_b32 s66, 4194304\nMvaa 44,4,68\nRa 32,27,0\ns_mov_b64 s[68:69], s[82:83]\nMvaa 40,0,68\nTa 100,20,8192\ns_mov_b32 s70, 6291456\n" \
"Mvaa 48,0,72\nTa 102,20,9216\ns_mov_b32 s73, 15360\nMvaa 52,4,72\nRa 36,27,2048\ns_mov_b32 s74, 30720\nMvaa 60,4,76\nTa 104,21,8192\n" \
"s_mov_b32 s75, 46080\nMvaa 56,0,76\nTa 106,21,9216\ns_mov_b32 s76, 65536\nMvaa 64,0,80\nRa 40,27,4096\ns_mov_b32 s77, 80896\nMvaa 68,4,80\n" \
"Ta 108,22,8192\ns_mov_b32 s78, 96256\nMvaa 76,4,84\nTa 110,22,9216\ns_mov_b32 s79, 111616\nMvaa 72,0,84\nRa 44,27,6144\ns_mov_b32 s89, 23552\n" \
"Mvaa 80,0,88\nTa 112,23,8192\ns_mov_b32 s90, 47104\nMvaa 84,4,88\nTa 114,23,9216\ns_mov_b32 s91, 70656\nMvaa 92,4,92\nRa 48,27,8192\n" \
"s_mov_b32 s72, 393216\nMvaa 88,0,92\nTa 116,24,8192\ns_lshr_b32 s92, s32, 1\nMvaa 152,8,92\nTa 118,24,9216\nMvaa 156,12,92\nRa 52,27,10240\n" \
"Mvaa 148,12,88\nTa 120,25,8192\nMvaa 144,8,88\nTa 122,25,9216\nMvaa 136,8,84\nRa 56,27,12288\nMvaa 140,12,84\nTa 124,26,8192\n" \
"Mvaa 132,12,80\nTa 126,26,9216\nMvaa 128,8,80\nRa 60,27,14336\nMvaa 120,8,76\nMvaa 124,12,76\nMvaa 116,12,72\nMvaa 112,8,72\n" \
"Mvaa 104,8,68\nMvaa 108,12,68\nMvaa 100,12,64\nMvaa 96,8,64\nMvaa 160,16,64\ns_waitcnt lgkmcnt(0)\ns_barrier\nMvaa 164,20,64\n" \
"s_add_u32 m0, s88, 0\ns_and_b32 s10, s3, 2\nMvaa 172,20,68\nD 9,64,0,0\nMvaa 168,16,68\ns_bfe_u32 s100, s3, 0x10002\ns_lshl_b32 s10, s10, 1\nMvaa 176,16,72\n" \
"s_bfe_u32 s2, s9, 0x10010\ns_xor_b32 s99, s2, s3\nMvaa 180,20,72\ns_and_b32 s99, s99, 1\ns_lshl1_add_u32 s99, s100, s99\nMvaa 188,20,76\nD 9,64,s73,1024\nMvaa 184,16,76\n" \
"s_bfe_u32 s101, s3, 0x20003\ns_lshl3_add_u32 s11, s99, s101\nMvaa 192,16,80\ns_add_u32 s101, s101, s10\ns_lshl3_add_u32 s10, s2, s10\nMvaa 196,20,80\ns_bfe_u32 s2, s3, 0x20005\ns_lshl1_add_u32 s2, s2, s100\n" \
"Mvaa 204,20,84\nD 9,64,s74,2048\nMvaa 200,16,84\ns_bfe_u32 s23, s9, 0x80008\ns_and_b32 s22, s9, 255\nMvaa 208,16,88\ns_lshr_b32 s6, 4, s99\ns_lshr_b32 s25, s3, 5\n" \
"Mvaa 212,20,88\ns_lshl_b32 s4, s25, 8\ns_lshl_b32 s18, s25, 21\nMvaa 220,20,92\nD 9,64,s75,3072\nMvaa 216,16,92\ns_lshl3_add_u32 s25, s25, s101\ns_lshl_b32 s26, s25, 18\n" \
"Maaa 152,24,92\ns_lshl_b32 s27, s25, 7\ns_add_u32 s4, s4, 16384\nMaaa 156,28,92\ns_add_u32 m0, s88, 4096\ns_xor_b32 s25, s11, s10\nMaaa 148,28,88\nD 9,64,s76,0\n" \
"Maaa 144,24,88\ns_lshl3_add_u32 s31, s25, s4\ns_lshl_b32 s5, s2, 7\nMaaa 136,24,84\ns_add_u32 s5, s5, 8192\ns_lshr_b32 s25, s32, 1\nMaaa 140,28,84\ns_add_u32 s18, s18, s25\n" \
"s_add_u32 s18, s18, 54854656\nMaaa 132,28,80\nD 9,64,s77,1024\nMaaa 128,24,80\ns_lshl_b32 s19, s2, 19\ns_add_u32 s19, s19, s25\nMvaa 248,24,76\ns_add_u32 s19, s19, 48563200\n" \
"s_lshr_b32 s25, s32, 7\nMvaa 252,28,76\ns_add_u32 s27, s27, s25\ns_lshl3_add_u32 s27, s99, s27\nMvaa 244,28,72\nD 9,64,s78,2048\nMvaa 240,24,72\ns_add_u32 s27, s27, 32768\n" \
"s_lshl1_add_u32 s26, s32, s26\nMvaa 232,24,68\ns_add_u32 s26, s26, 71631872\ns_lshl_b32 s28, s99, 15\nMvaa 236,28,68\ns_add_u32 s24, s26, s28\ns_sub_u32 s28, 0x10000, s28\nMvaa 228,28,64\n" \
"D 9,64,s79,3072\nMvaa 224,24,64\ns_lshl_b32 s25, s100, 17\ns_add_u32 s26, s26, s25\nMvaa 32,32,96\ns_cmp_eq_u32 s99, 0\ns_cselect_b32 s21, 128, 0\nMvaa 36,36,96\n" \
"s_add_u32 m0, s32, 65536\ns_lshl_b32 s25, s21, 10\nMvaa 44,36,100\nD 10,68,0,0\nMvaa 40,32,100\ns_add_u32 s24, s24, s25\ns_add_u32 s25, s24, 0x20000\nMvaa 48,32,104\n" \
"s_lshl_b32 s101, s101, 8\ns_and_b32 s20, s32, 0x2000\nMvaa 52,36,104\ns_lshl3_add_u32 s20, s20, s101\ns_and_b32 s99, s32, 0x1000\nMvaa 60,36,108\nD 10,68,s89,1024\nMvaa 56,32,108\n" \
"s_lshl2_add_u32 s20, s99, s20\ns_lshl4_add_u32 s101, s32, s101\nMvaa 64,32,112\ns_lshl_b32 s2, s2, 20\ns_add_u32 s2, s2, s101\nMvaa 68,36,112\ns_lshl_b32 s100, s100, 7\ns_add_u32 s101, s101, s100\n" \
"Mvaa 76,36,116\nD 11,68,s90,2048\nMvaa 72,32,116\ns_add_u32 s101, s101, 2048\ns_lshl_b32 s29, s42, 17\nMvaa 80,32,120\ns_lshl_b32 s30, s43, 17\ns_add_u32 s29, s29, s101\n" \
"Mvaa 84,36,120\ns_add_u32 s30, s30, s101\ns_add_u32 s101, s2, 524288\nMvaa 92,36,124\nD 11,68,s91,3072\nMvaa 88,32,124\ns_cmp_eq_u32 s23, 2\ns_cselect_b32 s29, s2, s29\n" \
"s_cselect_b32 s30, s101, s30\ns_cselect_b32 s7, 0xffff, 0\ns_cselect_b32 s9, 0xff, 0\nMvaa 152,40,124\ns_and_b32 s101, s10, 8\ns_lshl_b32 s9, s9, s101\nMvaa 156,44,124\ns_add_u32 m0, s92, 81920\n" \
"s_mov_b32 s2, zqS-zq0-8192\nMvaa 148,44,120\nD 12,68,0,0\nMvaa 144,40,120\ns_cmp_eq_u32 s23, 1\ns_cselect_b32 s2, zqR-zq0-8192, s2\nMvaa 136,40,116\ns_cmp_eq_u32 s23, 0\n" \
"s_cselect_b32 s2, zqT-zq0-8192, s2\nMvaa 140,44,116\ns_cmp_lt_u32 s32, 0x2000\ns_cselect_b32 s2, 0, s2\nMvaa 132,44,112\nD 12,68,s89,1024\nMvaa 128,40,112\ns_add_u32 s2, s2, s32\n" \
"Mvaa 120,40,108\nMvaa 124,44,108\nMvaa 116,44,104\nMvaa 112,40,104\nMvaa 104,40,100\nMvaa 108,44,100\nMvaa 100,44,96\nbuffer_load_dword v30, v4, s[52:55], 0 offen sc1\n" \
"Mvaa 96,40,96\ns_waitcnt vmcnt(16)\ns_barrier\nMvaa 160,48,96\nTa 64,8,32768\nv_add_u32 v9, 128, v9\nMvaa 164,52,96\nTa 66,8,33792\n" \
"v_add_u32 v10, s72, v10\nMvaa 172,52,100\nRa 0,7,32768\nv_add_u32 v11, s72, v11\nMvaa 168,48,100\nTa 68,20,32768\nv_add_u32 v12, s72, v12\nMvaa 176,48,104\n" \
"v_add_u32 v4, s44, v4\nTa 70,20,33792\nMvaa 180,52,104\nRa 4,7,34816\nMvaa 188,52,108\nTa 72,21,32768\nMvaa 184,48,108\nTa 74,21,33792\n" \
"Mvaa 192,48,112\nRa 8,7,36864\nMvaa 196,52,112\nTa 76,22,32768\nMvaa 204,52,116\nTa 78,22,33792\nMvaa 200,48,116\nRa 12,7,38912\n" \
"Mvaa 208,48,120\nTa 80,23,32768\nMvaa 212,52,120\nTa 82,23,33792\nMvaa 220,52,124\nRa 16,7,40960\nMvaa 216,48,124\nTa 84,24,32768\n" \
"Maaa 152,56,124\nTa 86,24,33792\nMaaa 156,60,124\nRa 20,7,43008\nMaaa 148,60,120\nTa 88,25,32768\nMaaa 144,56,120\nTa 90,25,33792\n" \
"Maaa 136,56,116\nRa 24,7,45056\nMaaa 140,60,116\nTa 92,26,32768\nMaaa 132,60,112\nTa 94,26,33792\nMaaa 128,56,112\nRa 28,7,47104\n" \
"Mvaa 248,56,108\nMvaa 252,60,108\nMvaa 244,60,104\nMvaa 240,56,104\nMvaa 232,56,100\nMvaa 236,60,100\nMvaa 228,60,96\nMvaa 224,56,96\n" \
"s_waitcnt lgkmcnt(0)\nMvaa 32,0,64\nTa 96,8,40960\nMvaa 36,4,64\nTa 98,8,41984\nMvaa 44,4,68\nRa 32,27,32768\nMvaa 40,0,68\n" \
"Ta 100,20,40960\nMvaa 48,0,72\nTa 102,20,41984\nMvaa 52,4,72\nRa 36,27,34816\nMvaa 60,4,76\nTa 104,21,40960\nMvaa 56,0,76\n" \
"Ta 106,21,41984\nMvaa 64,0,80\nRa 40,27,36864\nMvaa 68,4,80\nTa 108,22,40960\nMvaa 76,4,84\nTa 110,22,41984\nMvaa 72,0,84\n" \
"Ra 44,27,38912\nMvaa 80,0,88\nTa 112,23,40960\nMvaa 84,4,88\nTa 114,23,41984\nMvaa 92,4,92\nRa 48,27,40960\nMvaa 88,0,92\n" \
"Ta 116,24,40960\nMvaa 152,8,92\nTa 118,24,41984\nMvaa 156,12,92\nRa 52,27,43008\nMvaa 148,12,88\nTa 120,25,40960\nMvaa 144,8,88\n" \
"Ta 122,25,41984\nMvaa 136,8,84\nRa 56,27,45056\nMvaa 140,12,84\nTa 124,26,40960\nMvaa 132,12,80\nTa 126,26,41984\nMvaa 128,8,80\n" \
"Ra 60,27,47104\nMvaa 120,8,76\ns_and_b32 s96, s32, 0x1000\ns_lshr_b32 s97, s96, 6\nMvaa 124,12,76\ns_lshl_b32 s96, s96, 2\nv_subrev_u32 v20, s96, v8\nMvaa 116,12,72\n" \
"v_xor_b32 v20, s97, v20\nv_xor_b32 v21, 32, v20\nMvaa 112,8,72\nv_xor_b32 v22, 0x80, v20\nv_xor_b32 v23, 0xa0, v20\nMvaa 104,8,68\nMvaa 108,12,68\nMvaa 100,12,64\n" \
"Mvaa 96,8,64\nMvaa 160,16,64\ns_waitcnt lgkmcnt(0)\ns_barrier\nMvaa 164,20,64\ns_add_u32 m0, s88, 32768\nMvaa 172,20,68\nD 9,64,0,0\n" \
"Mvaa 168,16,68\nMvaa 176,16,72\nMvaa 180,20,72\nMvaa 188,20,76\nD 9,64,s73,1024\nMvaa 184,16,76\nMvaa 192,16,80\nMvaa 196,20,80\n" \
"Mvaa 204,20,84\nD 9,64,s74,2048\nMvaa 200,16,84\nMvaa 208,16,88\nMvaa 212,20,88\nMvaa 220,20,92\nD 9,64,s75,3072\nMvaa 216,16,92\n" \
"Maaa 152,24,92\nMaaa 156,28,92\ns_add_u32 m0, s88, 36864\nMaaa 148,28,88\nD 9,64,s76,0\nMaaa 144,24,88\nMaaa 136,24,84\nMaaa 140,28,84\n" \
"Maaa 132,28,80\nD 9,64,s77,1024\nMaaa 128,24,80\nMvaa 248,24,76\nMvaa 252,28,76\nMvaa 244,28,72\nD 9,64,s78,2048\nMvaa 240,24,72\n" \
"Mvaa 232,24,68\nMvaa 236,28,68\nMvaa 228,28,64\nD 9,64,s79,3072\nMvaa 224,24,64\nMvaa 32,32,96\nMvaa 36,36,96\ns_add_u32 m0, s32, 98304\n" \
"Mvaa 40,32,100\nD 10,68,0,0\nMvaa 44,36,100\nMvaa 48,32,104\nMvaa 52,36,104\nMvaa 56,32,108\nD 10,68,s89,1024\nMvaa 60,36,108\n" \
"Mvaa 64,32,112\nMvaa 68,36,112\nMvaa 72,32,116\nD 11,68,s90,2048\nC 32,32,33\nMvaa 76,36,116\nC 33,34,35\nC 34,36,37\n" \
"C 35,38,39\nMvaa 80,32,120\nC 36,40,41\nC 37,42,43\nC 38,44,45\nMvaa 84,36,120\nC 39,46,47\nC 40,48,49\n" \
"C 41,50,51\nMvaa 88,32,124\nD 11,68,s91,3072\nC 42,52,53\nMvaa 92,36,124\nC 43,54,55\nC 44,56,57\nC 45,58,59\n" \
"s_branch 99f\n.p2align 12\nzgx:\nzp25:\ns_setpc_b64 s[98:99]\n99:\nMvaa 96,40,96\nC 46,60,61\n" \
"C 47,62,63\nMvaa 100,44,96\ns_add_u32 m0, s92, 114688\nMvaa 104,40,100\nD 12,68,0,0\nC 48,64,65\nMvaa 108,44,100\nC 49,66,67\n" \
"C 50,68,69\nC 51,70,71\nMvaa 112,40,104\nC 52,72,73\nC 53,74,75\nC 54,76,77\nMvaa 116,44,104\nC 55,78,79\n" \
"C 56,80,81\nC 57,82,83\nMvaa 120,40,108\nD 12,68,s89,1024\nC 58,84,85\nMvaa 124,44,108\nC 59,86,87\nC 60,88,89\n" \
"C 61,90,91\nMvaa 128,40,112\nC 62,92,93\nC 63,94,95\nMvaa 132,44,112\nMvaa 136,40,116\nC 96,96,97\nC 97,98,99\n" \
"C 98,100,101\nMvaa 140,44,116\nC 99,102,103\nAW 160,96\nAW 161,97\nMvaa 144,40,120\nAW 162,98\nAW 163,99\n" \
"C 104,104,105\nMvaa 148,44,120\nC 105,106,107\nC 106,108,109\nC 107,110,111\nMvaa 152,40,124\nbuffer_load_dword v30, v4, s[52:55], 0 offen sc1\nAW 164,104\n" \
"Mvaa 156,44,124\ns_waitcnt vmcnt(16)\ns_barrier\nAW 165,105\nMvaa 160,48,96\nTa 64,20,0\nv_add_u32 v9, 128, v9\nAW 166,106\n" \
"Mvaa 164,52,96\nTa 66,20,1024\nv_add_u32 v10, s72, v10\nAW 167,107\nMvaa 168,48,100\nRa 0,7,0\nv_add_u32 v11, s72, v11\nC 112,112,113\n" \
"Mvaa 172,52,100\nTa 68,21,0\nv_add_u32 v12, s72, v12\nC 113,114,115\nMvaa 176,48,104\nv_add_u32 v4, s44, v4\nTa 70,21,1024\nC 114,116,117\n" \
"Mvaa 180,52,104\nRa 4,7,2048\nC 115,118,119\nAW 168,112\nMvaa 184,48,108\nTa 72,22,0\nAW 169,113\nAW 170,114\n" \
"Mvaa 188,52,108\nTa 74,22,1024\nAW 171,115\nC 120,120,121\nMvaa 192,48,112\nRa 8,7,4096\nC 121,122,123\nC 122,124,125\n" \
"Mvaa 196,52,112\nTa 76,23,0\nC 123,126,127\nAW 172,120\nMvaa 200,48,116\nTa 78,23,1024\nAW 173,121\nAW 174,122\n" \
"Mvaa 204,52,116\nRa 12,7,6144\nAW 175,123\nC 128,128,129\nMvaa 208,48,120\nTa 80,20,16384\nC 129,130,131\nC 130,132,133\n" \
"Mvaa 212,52,120\nTa 82,20,17408\nC 131,134,135\nAW 176,128\nMvaa 216,48,124\nRa 16,7,8192\nAW 177,129\nAW 178,130\n" \
"Mvaa 220,52,124\nTa 84,21,16384\nAW 179,131\nC 136,136,137\nMvaa 224,56,96\nTa 86,21,17408\nC 137,138,139\nC 138,140,141\n" \
"Mvaa 228,60,96\nRa 20,7,10240\nC 139,142,143\nAW 180,136\nMvaa 232,56,100\nRa 24,7,12288\nAW 181,137\nAW 182,138\n" \
"Mvaa 236,60,100\nRa 28,7,14336\nAW 183,139\nC 144,144,145\nMvaa 240,56,104\nC 145,146,147\nC 146,148,149\nC 147,150,151\n" \
"Mvaa 244,60,104\nAW 184,144\nAW 185,145\nAW 186,146\nMvaa 248,56,108\nAW 187,147\nC 152,152,153\nC 153,154,155\n" \
"Mvaa 252,60,108\nC 154,156,157\nC 155,158,159\nAW 188,152\nMaaa 128,56,112\nAW 189,153\nAW 190,154\nAW 191,155\n" \
"Maaa 132,60,112\nC 160,160,161\nC 161,162,163\nC 162,164,165\nMaaa 136,56,116\nC 163,166,167\nAW 192,160\nAW 193,161\n" \
"Maaa 140,60,116\nAW 194,162\nAW 195,163\nC 168,168,169\nMaaa 144,56,120\nC 169,170,171\nC 170,172,173\nC 171,174,175\n" \
"Maaa 148,60,120\nAW 196,168\nAW 197,169\nAW 198,170\nMaaa 152,56,124\nAW 199,171\nC 176,176,177\nC 177,178,179\n" \
"Maaa 156,60,124\nC 178,180,181\nC 179,182,183\nAW 200,176\ns_waitcnt lgkmcnt(0)\ns_branch zA\nzxK:\nMvaa 32,64,0\n" \
"Ta 96,8,8192\ns_mov_b64 s[64:65], s[80:81]\nMvaa 64,64,4\nTa 98,8,9216\ns_mov_b32 s66, 4194304\nMvaa 68,68,4\nRa 32,27,0\ns_mov_b64 s[68:69], s[82:83]\n" \
"Mvaa 36,68,0\nTa 100,20,8192\ns_mov_b32 s70, 6291456\nMvaa 40,72,0\nTa 102,20,9216\ns_mov_b32 s73, 15360\nMvaa 72,72,4\nRa 36,27,2048\n" \
"s_mov_b32 s74, 30720\nMvaa 76,76,4\nTa 104,21,8192\ns_mov_b32 s75, 46080\nMvaa 44,76,0\nTa 106,21,9216\ns_mov_b32 s76, 65536\nMvaa 48,80,0\n" \
"Ra 40,27,4096\ns_mov_b32 s77, 80896\nMvaa 80,80,4\nTa 108,22,8192\ns_mov_b32 s78, 96256\nMvaa 84,84,4\nTa 110,22,9216\ns_mov_b32 s79, 111616\n" \
"Mvaa 52,84,0\nRa 44,27,6144\ns_mov_b32 s89, 23552\nMvaa 56,88,0\nTa 112,23,8192\ns_mov_b32 s90, 47104\nMvaa 88,88,4\nTa 114,23,9216\n" \
"s_mov_b32 s91, 70656\nMvaa 92,92,4\nRa 48,27,8192\ns_mov_b32 s72, 393216\nMvaa 60,92,0\nTa 116,24,8192\ns_lshr_b32 s92, s32, 1\nMvaa 124,92,8\n" \
"Ta 118,24,9216\nMvaa 156,92,12\nRa 52,27,10240\nMvaa 152,88,12\nTa 120,25,8192\nMvaa 120,88,8\nTa 122,25,9216\nMvaa 116,84,8\n" \
"Ra 56,27,12288\nMvaa 148,84,12\nTa 124,26,8192\nMvaa 144,80,12\nTa 126,26,9216\nMvaa 112,80,8\nRa 60,27,14336\nMvaa 108,76,8\n" \
"Mvaa 140,76,12\nMvaa 136,72,12\nMvaa 104,72,8\nMvaa 100,68,8\nMvaa 132,68,12\nMvaa 128,64,12\nMvaa 96,64,8\nMvaa 160,64,16\n" \
"s_waitcnt lgkmcnt(0)\ns_barrier\nMvaa 192,64,20\ns_add_u32 m0, s88, 0\ns_and_b32 s10, s3, 2\nMvaa 196,68,20\nD 9,64,0,0\nMvaa 164,68,16\n" \
"s_bfe_u32 s100, s3, 0x10002\ns_lshl_b32 s10, s10, 1\nMvaa 168,72,16\ns_bfe_u32 s2, s9, 0x10010\ns_xor_b32 s99, s2, s3\nMvaa 200,72,20\ns_and_b32 s99, s99, 1\ns_lshl1_add_u32 s99, s100, s99\n" \
"Mvaa 204,76,20\nD 9,64,s73,1024\nMvaa 172,76,16\ns_bfe_u32 s101, s3, 0x20003\ns_lshl3_add_u32 s11, s99, s101\nMvaa 176,80,16\ns_add_u32 s101, s101, s10\ns_lshl3_add_u32 s10, s2, s10\n" \
"Mvaa 208,80,20\ns_bfe_u32 s2, s3, 0x20005\ns_lshl1_add_u32 s2, s2, s100\nMvaa 212,84,20\nD 9,64,s74,2048\nMvaa 180,84,16\ns_bfe_u32 s23, s9, 0x80008\ns_and_b32 s22, s9, 255\n" \
"Mvaa 184,88,16\ns_lshr_b32 s6, 4, s99\ns_lshr_b32 s25, s3, 5\nMvaa 216,88,20\ns_lshl_b32 s4, s25, 8\ns_lshl_b32 s18, s25, 21\nMvaa 220,92,20\nD 9,64,s75,3072\n" \
"Mvaa 188,92,16\ns_lshl3_add_u32 s25, s25, s101\ns_lshl_b32 s26, s25, 18\nMvaa 252,92,24\ns_lshl_b32 s27, s25, 7\ns_add_u32 s4, s4, 16384\nMaaa 156,92,28\ns_add_u32 m0, s88, 4096\n" \
"s_xor_b32 s25, s11, s10\nMaaa 152,88,28\nD 9,64,s76,0\nMvaa 248,88,24\ns_lshl3_add_u32 s31, s25, s4\ns_lshl_b32 s5, s2, 7\nMvaa 244,84,24\ns_add_u32 s5, s5, 8192\n" \
"s_lshr_b32 s25, s32, 1\nMaaa 148,84,28\ns_add_u32 s18, s18, s25\ns_add_u32 s18, s18, 54854656\nMaaa 144,80,28\nD 9,64,s77,1024\nMvaa 240,80,24\ns_lshl_b32 s19, s2, 19\n" \
"s_add_u32 s19, s19, s25\nMvaa 236,76,24\ns_add_u32 s19, s19, 48563200\ns_lshr_b32 s25, s32, 7\nMaaa 140,76,28\ns_add_u32 s27, s27, s25\ns_lshl3_add_u32 s27, s99, s27\nMaaa 136,72,28\n" \
"D 9,64,s78,2048\nMvaa 232,72,24\ns_add_u32 s27, s27, 32768\ns_lshl1_add_u32 s26, s32, s26\nMvaa 228,68,24\ns_add_u32 s26, s26, 71631872\ns_lshl_b32 s28, s99, 15\nMaaa 132,68,28\n" \
"s_add_u32 s24, s26, s28\ns_sub_u32 s28, 0x10000, s28\nMaaa 128,64,28\nD 9,64,s79,3072\nMvaa 224,64,24\ns_lshl_b32 s25, s100, 17\ns_add_u32 s26, s26, s25\nMvaa 32,96,32\n" \
"s_cmp_eq_u32 s99, 0\ns_cselect_b32 s21, 128, 0\nMvaa 64,96,36\ns_add_u32 m0, s32, 65536\ns_lshl_b32 s25, s21, 10\nMvaa 68,100,36\nD 10,68,0,0\nMvaa 36,100,32\n" \
"s_add_u32 s24, s24, s25\ns_add_u32 s25, s24, 0x20000\nMvaa 40,104,32\ns_lshl_b32 s101, s101, 8\ns_and_b32 s20, s32, 0x2000\nMvaa 72,104,36\ns_lshl3_add_u32 s20, s20, s101\ns_and_b32 s99, s32, 0x1000\n" \
"Mvaa 76,108,36\nD 10,68,s89,1024\nMvaa 44,108,32\ns_lshl2_add_u32 s20, s99, s20\ns_lshl4_add_u32 s101, s32, s101\nMvaa 48,112,32\ns_lshl_b32 s2, s2, 20\ns_add_u32 s2, s2, s101\n" \
"Mvaa 80,112,36\ns_lshl_b32 s100, s100, 7\ns_add_u32 s101, s101, s100\nMvaa 84,116,36\nD 11,68,s90,2048\nMvaa 52,116,32\ns_add_u32 s101, s101, 2048\ns_lshl_b32 s29, s42, 17\n" \
"Mvaa 56,120,32\ns_lshl_b32 s30, s43, 17\ns_add_u32 s29, s29, s101\nMvaa 88,120,36\ns_add_u32 s30, s30, s101\ns_add_u32 s101, s2, 524288\nMvaa 92,124,36\nD 11,68,s91,3072\n" \
"Mvaa 60,124,32\ns_cmp_eq_u32 s23, 2\ns_cselect_b32 s29, s2, s29\ns_cselect_b32 s30, s101, s30\ns_cselect_b32 s7, 0xffff, 0\ns_cselect_b32 s9, 0xff, 0\nMvaa 124,124,40\ns_and_b32 s101, s10, 8\n" \
"s_lshl_b32 s9, s9, s101\nMvaa 156,124,44\ns_add_u32 m0, s92, 81920\ns_mov_b32 s2, zqS-zq0-8192\nMvaa 152,120,44\nD 12,68,0,0\nMvaa 120,120,40\ns_cmp_eq_u32 s23, 1\n" \
"s_cselect_b32 s2, zqR-zq0-8192, s2\nMvaa 116,116,40\ns_cmp_eq_u32 s23, 0\ns_cselect_b32 s2, zqT-zq0-8192, s2\nMvaa 148,116,44\ns_cmp_lt_u32 s32, 0x2000\ns_cselect_b32 s2, 0, s2\nMvaa 144,112,44\n" \
"D 12,68,s89,1024\nMvaa 112,112,40\ns_add_u32 s2, s2, s32\nMvaa 108,108,40\nMvaa 140,108,44\nMvaa 136,104,44\nMvaa 104,104,40\nMvaa 100,100,40\n" \
"Mvaa 132,100,44\nMvaa 128,96,44\nbuffer_load_dword v30, v4, s[52:55], 0 offen sc1\nMvaa 96,96,40\ns_waitcnt vmcnt(16)\ns_barrier\nMvaa 160,96,48\nTa 64,8,32768\n" \
"v_add_u32 v9, 128, v9\nMvaa 192,96,52\nTa 66,8,33792\nv_add_u32 v10, s72, v10\nMvaa 196,100,52\nRa 0,7,32768\nv_add_u32 v11, s72, v11\nMvaa 164,100,48\n" \
"Ta 68,20,32768\nv_add_u32 v12, s72, v12\nMvaa 168,104,48\nv_add_u32 v4, s44, v4\nTa 70,20,33792\nMvaa 200,104,52\nRa 4,7,34816\nMvaa 204,108,52\n" \
"Ta 72,21,32768\nMvaa 172,108,48\nTa 74,21,33792\nMvaa 176,112,48\nRa 8,7,36864\nMvaa 208,112,52\nTa 76,22,32768\nMvaa 212,116,52\n" \
"Ta 78,22,33792\nMvaa 180,116,48\nRa 12,7,38912\nMvaa 184,120,48\nTa 80,23,32768\nMvaa 216,120,52\nTa 82,23,33792\nMvaa 220,124,52\n" \
"Ra 16,7,40960\nMvaa 188,124,48\nTa 84,24,32768\nMvaa 252,124,56\nTa 86,24,33792\nMaaa 156,124,60\nRa 20,7,43008\nMaaa 152,120,60\n" \
"Ta 88,25,32768\nMvaa 248,120,56\nTa 90,25,33792\nMvaa 244,116,56\nRa 24,7,45056\nMaaa 148,116,60\nTa 92,26,32768\nMaaa 144,112,60\n" \
"Ta 94,26,33792\nMvaa 240,112,56\nRa 28,7,47104\nMvaa 236,108,56\nMaaa 140,108,60\nMaaa 136,104,60\nMvaa 232,104,56\nMvaa 228,100,56\n" \
"Maaa 132,100,60\nMaaa 128,96,60\nMvaa 224,96,56\ns_waitcnt lgkmcnt(0)\nMvaa 32,64,0\nTa 96,8,40960\nMvaa 64,64,4\nTa 98,8,41984\n" \
"Mvaa 68,68,4\nRa 32,27,32768\nMvaa 36,68,0\nTa 100,20,40960\nMvaa 40,72,0\nTa 102,20,41984\nMvaa 72,72,4\nRa 36,27,34816\n" \
"Mvaa 76,76,4\nTa 104,21,40960\nMvaa 44,76,0\nTa 106,21,41984\nMvaa 48,80,0\nRa 40,27,36864\nMvaa 80,80,4\nTa 108,22,40960\n" \
"Mvaa 84,84,4\nTa 110,22,41984\nMvaa 52,84,0\nRa 44,27,38912\nMvaa 56,88,0\nTa 112,23,40960\nMvaa 88,88,4\nTa 114,23,41984\n" \
"Mvaa 92,92,4\nRa 48,27,40960\nMvaa 60,92,0\nTa 116,24,40960\nMvaa 124,92,8\nTa 118,24,41984\nMvaa 156,92,12\nRa 52,27,43008\n" \
"Mvaa 152,88,12\nTa 120,25,40960\nMvaa 120,88,8\nTa 122,25,41984\nMvaa 116,84,8\nRa 56,27,45056\nMvaa 148,84,12\nTa 124,26,40960\n" \
"Mvaa 144,80,12\nTa 126,26,41984\nMvaa 112,80,8\nRa 60,27,47104\nMvaa 108,76,8\ns_and_b32 s96, s32, 0x1000\ns_lshr_b32 s97, s96, 6\nMvaa 140,76,12\n" \
"s_lshl_b32 s96, s96, 2\nv_subrev_u32 v20, s96, v8\nMvaa 136,72,12\nv_xor_b32 v20, s97, v20\nv_xor_b32 v21, 32, v20\nMvaa 104,72,8\nv_xor_b32 v22, 0x80, v20\nv_xor_b32 v23, 0xa0, v20\n" \
"Mvaa 100,68,8\nMvaa 132,68,12\nMvaa 128,64,12\nMvaa 96,64,8\nMvaa 160,64,16\ns_waitcnt lgkmcnt(0)\ns_barrier\ns_branch 99f\n" \
".p2align 12\nzp26:\ns_setpc_b64 s[98:99]\n99:\nMvaa 192,64,20\ns_add_u32 m0, s88, 32768\nMvaa 196,68,20\nD 9,64,0,0\n" \
"Mvaa 164,68,16\nMvaa 168,72,16\nMvaa 200,72,20\nMvaa 204,76,20\nD 9,64,s73,1024\nMvaa 172,76,16\nMvaa 176,80,16\nMvaa 208,80,20\n" \
"Mvaa 212,84,20\nD 9,64,s74,2048\nMvaa 180,84,16\nMvaa 184,88,16\nMvaa 216,88,20\nMvaa 220,92,20\nD 9,64,s75,3072\nMvaa 188,92,16\n" \
"Mvaa 252,92,24\nMaaa 156,92,28\ns_add_u32 m0, s88, 36864\nMaaa 152,88,28\nD 9,64,s76,0\nMvaa 248,88,24\nMvaa 244,84,24\nMaaa 148,84,28\n" \
"Maaa 144,80,28\nD 9,64,s77,1024\nMvaa 240,80,24\nMvaa 236,76,24\nMaaa 140,76,28\nMaaa 136,72,28\nD 9,64,s78,2048\nMvaa 232,72,24\n" \
"Mvaa 228,68,24\nMaaa 132,68,28\nMaaa 128,64,28\nD 9,64,s79,3072\nMvaa 224,64,24\nMvaa 32,96,32\nMvaa 36,100,32\ns_add_u32 m0, s32, 98304\n" \
"Mvaa 40,104,32\nD 10,68,0,0\nMvaa 44,108,32\nMvaa 48,112,32\nMvaa 52,116,32\nMvaa 56,120,32\nD 10,68,s89,1024\nMvaa 60,124,32\n" \
"Mvaa 64,96,36\nMvaa 68,100,36\nMvaa 72,104,36\nD 11,68,s90,2048\nC 32,32,33\nMvaa 76,108,36\nC 33,34,35\nC 34,36,37\n" \
"C 35,38,39\nMvaa 80,112,36\nC 36,40,41\nC 37,42,43\nC 38,44,45\nMvaa 84,116,36\nC 39,46,47\nC 40,48,49\n" \
"C 41,50,51\nMvaa 88,120,36\nD 11,68,s91,3072\nC 42,52,53\nMvaa 92,124,36\nC 43,54,55\nC 44,56,57\nC 45,58,59\n" \
"Mvaa 96,96,40\nC 46,60,61\nC 47,62,63\nMvaa 100,100,40\ns_add_u32 m0, s92, 114688\nMvaa 104,104,40\nD 12,68,0,0\nC 48,64,65\n" \
"Mvaa 108,108,40\nC 49,66,67\nC 50,68,69\nC 51,70,71\nMvaa 112,112,40\nC 52,72,73\nC 53,74,75\nC 54,76,77\n" \
"Mvaa 116,116,40\nC 55,78,79\nC 56,80,81\nC 57,82,83\nMvaa 120,120,40\nD 12,68,s89,1024\nC 58,84,85\nMvaa 124,124,40\n" \
"C 59,86,87\nC 60,88,89\nC 61,90,91\nMvaa 128,96,44\nC 62,92,93\nC 63,94,95\nMvaa 132,100,44\nMvaa 136,104,44\n" \
"C 96,96,97\nC 97,98,99\nC 98,100,101\nMvaa 140,108,44\nC 99,102,103\nAW 160,96\nAW 161,97\nMvaa 144,112,44\n" \
"AW 162,98\nAW 163,99\nC 104,104,105\nMvaa 148,116,44\nC 105,106,107\nC 106,108,109\nC 107,110,111\nMvaa 152,120,44\n" \
"buffer_load_dword v30, v4, s[52:55], 0 offen sc1\nAW 164,104\nMvaa 156,124,44\ns_waitcnt vmcnt(16)\ns_barrier\nAW 165,105\nMvaa 160,96,48\nTa 64,20,0\n" \
"v_add_u32 v9, 128, v9\nAW 166,106\nMvaa 164,100,48\nTa 66,20,1024\nv_add_u32 v10, s72, v10\nAW 167,107\nMvaa 168,104,48\nRa 0,7,0\n" \
"v_add_u32 v11, s72, v11\nC 112,112,113\nMvaa 172,108,48\nTa 68,21,0\nv_add_u32 v12, s72, v12\nC 113,114,115\nMvaa 176,112,48\nv_add_u32 v4, s44, v4\n" \
"Ta 70,21,1024\nC 114,116,117\nMvaa 180,116,48\nRa 4,7,2048\nC 115,118,119\nAW 168,112\nMvaa 184,120,48\nTa 72,22,0\n" \
"AW 169,113\nAW 170,114\nMvaa 188,124,48\nTa 74,22,1024\nAW 171,115\nC 120,120,121\nMvaa 192,96,52\nRa 8,7,4096\n" \
"C 121,122,123\nC 122,124,125\nMvaa 196,100,52\nTa 76,23,0\nC 123,126,127\nAW 172,120\nMvaa 200,104,52\nTa 78,23,1024\n" \
"AW 173,121\nAW 174,122\nMvaa 204,108,52\nRa 12,7,6144\nAW 175,123\nC 128,128,129\nMvaa 208,112,52\nTa 80,20,16384\n" \
"C 129,130,131\nC 130,132,133\nMvaa 212,116,52\nTa 82,20,17408\nC 131,134,135\nAW 176,128\nMvaa 216,120,52\nRa 16,7,8192\n" \
"AW 177,129\nAW 178,130\nMvaa 220,124,52\nTa 84,21,16384\nAW 179,131\nC 136,136,137\nMvaa 224,96,56\nTa 86,21,17408\n" \
"C 137,138,139\nC 138,140,141\nMvaa 228,100,56\nRa 20,7,10240\nC 139,142,143\nAW 180,136\nMvaa 232,104,56\nRa 24,7,12288\n" \
"AW 181,137\nAW 182,138\nMvaa 236,108,56\nRa 28,7,14336\nAW 183,139\nC 144,144,145\nMvaa 240,112,56\nC 145,146,147\n" \
"C 146,148,149\nC 147,150,151\nMvaa 244,116,56\nAW 184,144\nAW 185,145\nAW 186,146\nMvaa 248,120,56\nAW 187,147\n" \
"C 152,152,153\nC 153,154,155\nMvaa 252,124,56\nC 154,156,157\nC 155,158,159\nAW 188,152\nMaaa 128,96,60\nAW 189,153\n" \
"AW 190,154\nAW 191,155\nMaaa 132,100,60\nC 160,160,161\nC 161,162,163\nC 162,164,165\nMaaa 136,104,60\nC 163,166,167\n" \
"AW 192,160\nAW 193,161\nMaaa 140,108,60\nAW 194,162\nAW 195,163\nC 168,168,169\nMaaa 144,112,60\nC 169,170,171\n" \
"C 170,172,173\nC 171,174,175\nMaaa 148,116,60\nAW 196,168\nAW 197,169\nAW 198,170\nMaaa 152,120,60\nAW 199,171\n" \
"C 176,176,177\nC 177,178,179\nMaaa 156,124,60\nC 178,180,181\nC 179,182,183\nAW 200,176\ns_waitcnt lgkmcnt(0)\nzA:\n" \
"Zvaa 64,64,0\nTa 96,20,8192\nAW 201,177\nAW 202,178\nZvaa 88,64,4\nTa 98,20,9216\nAW 203,179\nC 184,184,185\n" \
"Zvaa 92,68,4\nRa 32,27,0\nC 185,186,187\nC 186,188,189\nZvaa 68,68,0\nTa 100,21,8192\nC 187,190,191\nAW 204,184\n" \
"Zvaa 72,72,0\nTa 102,21,9216\nAW 205,185\nAW 206,186\nZvaa 96,72,4\nRa 36,27,2048\nAW 207,187\nC 192,192,193\n" \
"Zvaa 100,76,4\nTa 104,22,8192\nC 193,194,195\nC 194,196,197\nZvaa 76,76,0\nTa 106,22,9216\nC 195,198,199\nAW 208,192\n" \
"Zvaa 80,0,80\nRa 40,27,4096\nAW 209,193\nAW 210,194\nZvaa 104,4,80\nTa 108,23,8192\nAW 211,195\nC 200,200,201\n" \
"Zvaa 108,4,84\nTa 110,23,9216\nC 201,202,203\nC 202,204,205\nZvaa 84,0,84\nRa 44,27,6144\nC 203,206,207\nAW 212,200\n" \
"Zvaa 132,8,84\nTa 112,22,16384\nAW 213,201\nAW 214,202\nZvaa 156,12,84\nTa 114,22,17408\nAW 215,203\nC 208,208,209\n" \
"Zvaa 152,12,80\nRa 48,27,8192\nC 209,210,211\nC 210,212,213\nZvaa 128,8,80\nTa 116,23,16384\nC 211,214,215\nAW 216,208\n" \
"Zvaa 124,76,8\nTa 118,23,17408\nAW 217,209\nAW 218,210\nZvaa 148,76,12\nRa 52,27,10240\nAW 219,211\nC 216,216,217\n" \
"Zvaa 144,72,12\nRa 56,27,12288\nC 217,218,219\nC 218,220,221\nZvaa 120,72,8\nRa 60,27,14336\nC 219,222,223\nAW 220,216\n" \
"Zvaa 116,68,8\nAW 221,217\nAW 222,218\nAW 223,219\nZvaa 140,68,12\nC 224,224,225\nC 225,226,227\nC 226,228,229\n" \
"Zvaa 136,64,12\nC 227,230,231\nAW 224,224\nAW 225,225\nZvaa 112,64,8\nAW 226,226\nAW 227,227\nC 232,232,233\n" \
"Zvaa 160,64,16\nC 233,234,235\nC 234,236,237\nC 235,238,239\nZvaa 184,64,20\nAW 228,232\nAW 229,233\nAW 230,234\n" \
"Zvaa 188,68,20\ns_waitcnt lgkmcnt(0)\ns_barrier\nAW 231,235\nZvaa 164,68,16\ns_add_u32 m0, s88, 0\nC 240,240,241\nC 241,242,243\n" \
"Zvaa 168,72,16\nD 9,64,0,0\nC 242,244,245\nZvaa 192,72,20\nC 243,246,247\nAW 232,240\nAW 233,241\nZvaa 196,76,20\n" \
"AW 234,242\nAW 235,243\nC 248,248,249\nZvaa 172,76,16\nC 249,250,251\nC 250,252,253\nC 251,254,255\nZvaa 176,16,80\n" \
"D 9,64,s73,1024\nAW 236,248\nZvaa 200,20,80\nAW 237,249\nAW 238,250\nAW 239,251\nZvaa 204,20,84\nZvaa 180,16,84\n" \
"Zvaa 228,24,84\nD 9,64,s74,2048\nZvaa 252,28,84\ns_add_u32 s95, s34, 0\nZvaa 248,28,80\nbuffer_store_dwordx4 v[32:35], v0, s[60:63], s95 offen offset:0 sc1\nZvaa 224,24,80\nZvaa 220,76,24\n" \
"D 9,64,s75,3072\nZvaa 244,76,28\nZvaa 240,72,28\nZvaa 216,72,24\ns_add_u32 m0, s88, 4096\nZvaa 212,68,24\nD 9,64,s76,0\nZvaa 236,68,28\n" \
"Zvaa 232,64,28\nZvaa 208,64,24\nMvaa 64,96,32\nD 9,64,s77,1024\nMvaa 88,96,36\nMvaa 92,100,36\nbuffer_store_dwordx4 v[36:39], v0, s[60:63], s95 offen offset:1024 sc1\nMvaa 68,100,32\n" \
"Mvaa 72,104,32\nD 9,64,s78,2048\nMvaa 96,104,36\nMvaa 100,108,36\nMvaa 76,108,32\nMvaa 80,32,112\nD 9,64,s79,3072\nMvaa 104,36,112\n" \
"Mvaa 108,36,116\nMvaa 84,32,116\ns_add_u32 m0, s32, 65536\nMvaa 132,40,116\nD 10,68,0,0\nMvaa 156,44,116\nMvaa 152,44,112\nbuffer_store_dwordx4 v[40:43], v0, s[60:63], s95 offen offset:2048 sc1\n" \
"Mvaa 128,40,112\nMvaa 124,108,40\nD 10,68,s89,1024\nMvaa 148,108,44\nMvaa 144,104,44\nMvaa 120,104,40\nMvaa 116,100,40\nD 11,68,s90,2048\n" \
"Mvaa 140,100,44\nMvaa 136,96,44\ns_waitcnt vmcnt(15)\ns_barrier\nMvaa 112,96,40\nTa 64,20,32768\nMvaa 160,96,48\nD 11,68,s91,3072\n" \
"Ta 66,20,33792\nMvaa 184,96,52\nRa 0,7,32768\nMvaa 188,100,52\nTa 68,21,32768\nMvaa 164,100,48\ns_add_u32 m0, s92, 81920\nTa 70,21,33792\n" \
"Mvaa 168,104,48\nD 12,68,0,0\nRa 4,7,34816\nMvaa 192,104,52\nTa 72,22,32768\nMvaa 196,108,52\nTa 74,22,33792\nMvaa 172,108,48\n" \
"Ra 8,7,36864\nMvaa 176,48,112\nD 12,68,s89,1024\nTa 76,23,32768\nMvaa 200,52,112\nTa 78,23,33792\nv_add_u32 v9, 128, v9\nMvaa 204,52,116\n" \
"Ra 12,7,38912\nv_add_u32 v10, s72, v10\nMvaa 180,48,116\nTa 80,20,49152\nv_add_u32 v11, s72, v11\nMvaa 228,56,116\nTa 82,20,50176\nv_add_u32 v12, s72, v12\n" \
"Mvaa 252,60,116\nRa 16,7,40960\nMvaa 248,60,112\nTa 84,21,49152\nMvaa 224,56,112\nTa 86,21,50176\nMvaa 220,108,56\nRa 20,7,43008\n" \
"Mvaa 244,108,60\nRa 24,7,45056\nMvaa 240,104,60\nRa 28,7,47104\nMvaa 216,104,56\nbuffer_store_dwordx4 v[44:47], v0, s[60:63], s95 offen offset:3072 sc1\nMvaa 212,100,56\ns_add_u32 s95, s34, 4096\n" \
"Mvaa 236,100,60\nbuffer_store_dwordx4 v[48:51], v0, s[60:63], s95 offen offset:0 sc1\nMvaa 232,96,60\nMvaa 208,96,56\ns_waitcnt lgkmcnt(0)\nMvaa 64,64,0\nTa 96,20,40960\nMvaa 88,64,4\n" \
"Ta 98,20,41984\nMvaa 92,68,4\nRa 32,27,32768\nMvaa 68,68,0\nTa 100,21,40960\nMvaa 72,72,0\nTa 102,21,41984\nMvaa 96,72,4\n" \
"Ra 36,27,34816\nMvaa 100,76,4\nTa 104,22,40960\nMvaa 76,76,0\ns_branch 99f\n.p2align 12\nzga:\nzp27:\n" \
"s_setpc_b64 s[98:99]\n99:\nTa 106,22,41984\nMvaa 80,0,80\nRa 40,27,36864\nMvaa 104,4,80\nTa 108,23,40960\nMvaa 108,4,84\n" \
"Ta 110,23,41984\nMvaa 84,0,84\nRa 44,27,38912\nMvaa 132,8,84\nTa 112,22,49152\nMvaa 156,12,84\nTa 114,22,50176\nMvaa 152,12,80\n" \
"Ra 48,27,40960\nMvaa 128,8,80\nTa 116,23,49152\nMvaa 124,76,8\nTa 118,23,50176\nMvaa 148,76,12\nRa 52,27,43008\nMvaa 144,72,12\n" \
"Ra 56,27,45056\nMvaa 120,72,8\nRa 60,27,47104\nMvaa 116,68,8\nbuffer_store_dwordx4 v[52:55], v0, s[60:63], s95 offen offset:1024 sc1\nMvaa 140,68,12\nMvaa 136,64,12\nbuffer_store_dwordx4 v[56:59], v0, s[60:63], s95 offen offset:2048 sc1\n" \
"Mvaa 112,64,8\nMvaa 160,64,16\nMvaa 184,64,20\nMvaa 188,68,20\ns_waitcnt lgkmcnt(0)\ns_barrier\nMvaa 164,68,16\ns_add_u32 m0, s88, 32768\n" \
"Mvaa 168,72,16\nD 9,64,0,0\nMvaa 192,72,20\nMvaa 196,76,20\nMvaa 172,76,16\nMvaa 176,16,80\nD 9,64,s73,1024\nMvaa 200,20,80\n" \
"Mvaa 204,20,84\nbuffer_store_dwordx4 v[60:63], v0, s[60:63], s95 offen offset:3072 sc1\nMvaa 180,16,84\nMvaa 228,24,84\nD 9,64,s74,2048\nMvaa 252,28,84\nMvaa 248,28,80\nMvaa 224,24,80\n" \
"Mvaa 220,76,24\nD 9,64,s75,3072\nMvaa 244,76,28\nMvaa 240,72,28\nMvaa 216,72,24\ns_add_u32 m0, s88, 36864\nMvaa 212,68,24\nD 9,64,s76,0\n" \
"Mvaa 236,68,28\nMvaa 232,64,28\ns_add_u32 s95, s34, 8192\nMvaa 208,64,24\nbuffer_store_dwordx4 a[160:163], v0, s[60:63], s95 offen offset:0 sc1\nMvaa 64,96,32\nD 9,64,s77,1024\nMvaa 88,96,36\n" \
"AR 48,128\nAR 49,129\nMvaa 92,100,36\nAR 50,130\nAR 51,131\nMvaa 68,100,32\nAR 52,132\nAR 53,133\n" \
"Mvaa 72,104,32\nD 9,64,s78,2048\nMvaa 96,104,36\nAR 54,134\nAR 55,135\nMvaa 100,108,36\nC 32,48,49\nC 33,50,51\n" \
"Mvaa 76,108,32\nC 34,52,53\nC 35,54,55\nMvaa 80,32,112\nD 9,64,s79,3072\nMvaa 104,36,112\nAR 56,136\nAR 57,137\n" \
"Mvaa 108,36,116\nAR 58,138\nAR 59,139\nMvaa 84,32,116\ns_add_u32 m0, s32, 98304\nAR 60,140\nMvaa 132,40,116\nD 10,68,0,0\n" \
"Mvaa 156,44,116\nAR 61,141\nAR 62,142\nMvaa 152,44,112\nAR 63,143\nC 36,56,57\nMvaa 128,40,112\nbuffer_store_dwordx4 a[164:167], v0, s[60:63], s95 offen offset:1024 sc1\n" \
"Mvaa 124,108,40\nD 10,68,s89,1024\nMvaa 148,108,44\nC 37,58,59\nC 38,60,61\nMvaa 144,104,44\nC 39,62,63\nAR 48,144\n" \
"Mvaa 120,104,40\nAR 49,145\nAR 50,146\nMvaa 116,100,40\nD 11,68,s90,2048\nMvaa 140,100,44\nAR 51,147\nAR 52,148\n" \
"Mvaa 136,96,44\ns_waitcnt vmcnt(18)\ns_barrier\nMvaa 112,96,40\nTa 64,20,0\nAR 53,149\nMvaa 160,96,48\nD 11,68,s91,3072\n" \
"Ta 66,20,1024\nMvaa 184,96,52\nRa 0,7,0\nAR 54,150\nMvaa 188,100,52\nTa 68,21,0\nAR 55,151\nMvaa 164,100,48\n" \
"s_add_u32 m0, s92, 114688\nTa 70,21,1024\nMvaa 168,104,48\nD 12,68,0,0\nRa 4,7,2048\nMvaa 192,104,52\nTa 72,22,0\nC 40,48,49\n" \
"Mvaa 196,108,52\nTa 74,22,1024\nC 41,50,51\nMvaa 172,108,48\nRa 8,7,4096\nC 42,52,53\nMvaa 176,48,112\nD 12,68,s89,1024\n" \
"Ta 76,23,0\nMvaa 200,52,112\nTa 78,23,1024\nv_add_u32 v9, 128, v9\nMvaa 204,52,116\nRa 12,7,6144\nv_add_u32 v10, s72, v10\nMvaa 180,48,116\n" \
"Ta 80,20,16384\nv_add_u32 v11, s72, v11\nMvaa 228,56,116\nTa 82,20,17408\nv_add_u32 v12, s72, v12\nMvaa 252,60,116\nRa 16,7,8192\nC 43,54,55\n" \
"Mvaa 248,60,112\nTa 84,21,16384\nAR 56,152\nMvaa 224,56,112\nTa 86,21,17408\nAR 57,153\nMvaa 220,108,56\nRa 20,7,10240\n" \
"AR 58,154\nMvaa 244,108,60\nRa 24,7,12288\nAR 59,155\nMvaa 240,104,60\nRa 28,7,14336\nAR 60,156\nMvaa 216,104,56\n" \
"buffer_store_dwordx4 a[168:171], v0, s[60:63], s95 offen offset:2048 sc1\nMvaa 212,100,56\nAR 61,157\nAR 62,158\nMvaa 236,100,60\nAR 63,159\nC 44,56,57\nMvaa 232,96,60\n" \
"C 45,58,59\nC 46,60,61\nMvaa 208,96,56\ns_waitcnt lgkmcnt(0)\nC 47,62,63\nMvaa 64,64,0\nTa 96,20,8192\nMvaa 88,64,4\n" \
"Ta 98,20,9216\nMvaa 92,68,4\nRa 32,27,0\nMvaa 68,68,0\nTa 100,21,8192\nMvaa 72,72,0\nTa 102,21,9216\nMvaa 96,72,4\n" \
"Ra 36,27,2048\nMvaa 100,76,4\nTa 104,22,8192\nMvaa 76,76,0\nTa 106,22,9216\nMvaa 80,0,80\nRa 40,27,4096\nMvaa 104,4,80\n" \
"Ta 108,23,8192\nMvaa 108,4,84\nTa 110,23,9216\nMvaa 84,0,84\nRa 44,27,6144\nMvaa 132,8,84\nTa 112,22,16384\nMvaa 156,12,84\n" \
"Ta 114,22,17408\nMvaa 152,12,80\nRa 48,27,8192\nMvaa 128,8,80\nTa 116,23,16384\nMvaa 124,76,8\nTa 118,23,17408\nMvaa 148,76,12\n" \
"Ra 52,27,10240\nMvaa 144,72,12\nRa 56,27,12288\nMvaa 120,72,8\nRa 60,27,14336\nMvaa 116,68,8\nbuffer_store_dwordx4 a[172:175], v0, s[60:63], s95 offen offset:3072 sc1\nMvaa 140,68,12\n" \
"s_add_u32 s95, s34, 12288\nMvaa 136,64,12\nbuffer_store_dwordx4 a[176:179], v0, s[60:63], s95 offen offset:0 sc1\nMvaa 112,64,8\nMvaa 160,64,16\nMvaa 184,64,20\nMvaa 188,68,20\ns_waitcnt lgkmcnt(0)\n" \
"s_barrier\nMvaa 164,68,16\ns_add_u32 m0, s88, 0\nMvaa 168,72,16\nD 9,64,0,0\nMvaa 192,72,20\nMvaa 196,76,20\nMvaa 172,76,16\n" \
"Mvaa 176,16,80\nD 9,64,s73,1024\nMvaa 200,20,80\nMvaa 204,20,84\nbuffer_store_dwordx4 a[180:183], v0, s[60:63], s95 offen offset:1024 sc1\nMvaa 180,16,84\nMvaa 228,24,84\nD 9,64,s74,2048\n" \
"Mvaa 252,28,84\nMvaa 248,28,80\nMvaa 224,24,80\nMvaa 220,76,24\nD 9,64,s75,3072\nMvaa 244,76,28\nMvaa 240,72,28\nMvaa 216,72,24\n" \
"s_add_u32 m0, s88, 4096\nMvaa 212,68,24\nD 9,64,s76,0\nMvaa 236,68,28\nMvaa 232,64,28\nMvaa 208,64,24\nbuffer_store_dwordx4 a[184:187], v0, s[60:63], s95 offen offset:2048 sc1\nMvaa 64,96,32\n" \
"D 9,64,s77,1024\nMvaa 88,96,36\nMvaa 92,100,36\nMvaa 68,100,32\nMvaa 72,104,32\nD 9,64,s78,2048\nMvaa 96,104,36\nMvaa 100,108,36\n" \
"Mvaa 76,108,32\nMvaa 80,32,112\nD 9,64,s79,3072\nMvaa 104,36,112\nMvaa 108,36,116\nMvaa 84,32,116\ns_add_u32 m0, s32, 65536\nMvaa 132,40,116\n" \
"D 10,68,0,0\nMvaa 156,44,116\nMvaa 152,44,112\nMvaa 128,40,112\nbuffer_store_dwordx4 a[188:191], v0, s[60:63], s95 offen offset:3072 sc1\nMvaa 124,108,40\nD 10,68,s89,1024\nMvaa 148,108,44\n" \
"Mvaa 144,104,44\nMvaa 120,104,40\nMvaa 116,100,40\nD 11,68,s90,2048\nMvaa 140,100,44\nMvaa 136,96,44\ns_waitcnt vmcnt(17)\ns_barrier\n" \
"Mvaa 112,96,40\nTa 64,20,32768\nMvaa 160,96,48\nD 11,68,s91,3072\nTa 66,20,33792\nMvaa 184,96,52\nRa 0,7,32768\nMvaa 188,100,52\n" \
"Ta 68,21,32768\nMvaa 164,100,48\ns_add_u32 m0, s92, 81920\nTa 70,21,33792\nMvaa 168,104,48\nD 12,68,0,0\nRa 4,7,34816\nMvaa 192,104,52\n" \
"Ta 72,22,32768\nMvaa 196,108,52\nTa 74,22,33792\nMvaa 172,108,48\nRa 8,7,36864\nMvaa 176,48,112\nD 12,68,s89,1024\nTa 76,23,32768\n" \
"Mvaa 200,52,112\nTa 78,23,33792\nv_add_u32 v9, 128, v9\nMvaa 204,52,116\nRa 12,7,38912\nv_add_u32 v10, s72, v10\nMvaa 180,48,116\nTa 80,20,49152\n" \
"v_add_u32 v11, s72, v11\nMvaa 228,56,116\nTa 82,20,50176\nv_add_u32 v12, s72, v12\nMvaa 252,60,116\nRa 16,7,40960\nMvaa 248,60,112\nTa 84,21,49152\n" \
"Mvaa 224,56,112\nTa 86,21,50176\nMvaa 220,108,56\nRa 20,7,43008\nMvaa 244,108,60\nRa 24,7,45056\nMvaa 240,104,60\nRa 28,7,47104\n" \
"s_add_u32 s95, s34, 16384\nMvaa 216,104,56\nbuffer_store_dwordx4 a[192:195], v0, s[60:63], s95 offen offset:0 sc1\nMvaa 212,100,56\nMvaa 236,100,60\nMvaa 232,96,60\nMvaa 208,96,56\ns_waitcnt lgkmcnt(0)\n" \
"Mvaa 64,64,0\nTa 96,20,40960\nMvaa 88,64,4\nTa 98,20,41984\nMvaa 92,68,4\nRa 32,27,32768\nMvaa 68,68,0\nTa 100,21,40960\n" \
"Mvaa 72,72,0\nTa 102,21,41984\nMvaa 96,72,4\nRa 36,27,34816\nMvaa 100,76,4\nTa 104,22,40960\nMvaa 76,76,0\nTa 106,22,41984\n" \
"Mvaa 80,0,80\nRa 40,27,36864\nMvaa 104,4,80\nTa 108,23,40960\nMvaa 108,4,84\nTa 110,23,41984\nMvaa 84,0,84\nRa 44,27,38912\n" \
"Mvaa 132,8,84\nTa 112,22,49152\nMvaa 156,12,84\nTa 114,22,50176\nMvaa 152,12,80\nRa 48,27,40960\nMvaa 128,8,80\nTa 116,23,49152\n" \
"Mvaa 124,76,8\nTa 118,23,50176\nMvaa 148,76,12\nRa 52,27,43008\nMvaa 144,72,12\nRa 56,27,45056\nMvaa 120,72,8\nRa 60,27,47104\n" \
"Mvaa 116,68,8\nbuffer_store_dwordx4 a[196:199], v0, s[60:63], s95 offen offset:1024 sc1\nMvaa 140,68,12\nMvaa 136,64,12\nbuffer_store_dwordx4 a[200:203], v0, s[60:63], s95 offen offset:2048 sc1\nMvaa 112,64,8\nMvaa 160,64,16\nMvaa 184,64,20\n" \
"Mvaa 188,68,20\ns_waitcnt lgkmcnt(0)\ns_barrier\nMvaa 164,68,16\ns_add_u32 m0, s88, 32768\nMvaa 168,72,16\nD 9,64,0,0\nMvaa 192,72,20\n" \
"Mvaa 196,76,20\nMvaa 172,76,16\nMvaa 176,16,80\nD 9,64,s73,1024\nMvaa 200,20,80\nMvaa 204,20,84\nbuffer_store_dwordx4 a[204:207], v0, s[60:63], s95 offen offset:3072 sc1\nMvaa 180,16,84\n" \
"Mvaa 228,24,84\nD 9,64,s74,2048\nMvaa 252,28,84\nMvaa 248,28,80\nMvaa 224,24,80\nMvaa 220,76,24\nD 9,64,s75,3072\nMvaa 244,76,28\n" \
"Mvaa 240,72,28\nMvaa 216,72,24\ns_add_u32 m0, s88, 36864\nMvaa 212,68,24\nD 9,64,s76,0\nMvaa 236,68,28\nMvaa 232,64,28\ns_add_u32 s95, s34, 20480\n" \
"Mvaa 208,64,24\nbuffer_store_dwordx4 a[208:211], v0, s[60:63], s95 offen offset:0 sc1\nMvaa 64,96,32\nD 9,64,s77,1024\nMvaa 88,96,36\nMvaa 92,100,36\nMvaa 68,100,32\nMvaa 72,104,32\n" \
"D 9,64,s78,2048\nMvaa 96,104,36\nMvaa 100,108,36\nMvaa 76,108,32\nMvaa 80,32,112\nD 9,64,s79,3072\nMvaa 104,36,112\nMvaa 108,36,116\n" \
"Mvaa 84,32,116\ns_add_u32 m0, s32, 98304\nMvaa 132,40,116\nD 10,68,0,0\nMvaa 156,44,116\nMvaa 152,44,112\nMvaa 128,40,112\nbuffer_store_dwordx4 a[212:215], v0, s[60:63], s95 offen offset:1024 sc1\n" \
"Mvaa 124,108,40\nD 10,68,s89,1024\nMvaa 148,108,44\nMvaa 144,104,44\nMvaa 120,104,40\nMvaa 116,100,40\nD 11,68,s90,2048\nMvaa 140,100,44\n" \
"Mvaa 136,96,44\ns_waitcnt vmcnt(17)\ns_barrier\nMvaa 112,96,40\nTa 64,20,0\nMvaa 160,96,48\nD 11,68,s91,3072\nTa 66,20,1024\n" \
"Mvaa 184,96,52\nRa 0,7,0\nMvaa 188,100,52\nTa 68,21,0\nMvaa 164,100,48\ns_add_u32 m0, s92, 114688\nTa 70,21,1024\nMvaa 168,104,48\n" \
"D 12,68,0,0\nRa 4,7,2048\nMvaa 192,104,52\nTa 72,22,0\nMvaa 196,108,52\nTa 74,22,1024\nMvaa 172,108,48\nRa 8,7,4096\n" \
"Mvaa 176,48,112\nD 12,68,s89,1024\nTa 76,23,0\nMvaa 200,52,112\nTa 78,23,1024\nv_add_u32 v9, 128, v9\nMvaa 204,52,116\nRa 12,7,6144\n" \
"v_add_u32 v10, s72, v10\nMvaa 180,48,116\nTa 80,20,16384\nv_add_u32 v11, s72, v11\ns_branch 99f\n.p2align 12\nzp28:\ns_setpc_b64 s[98:99]\n" \
"99:\nMvaa 228,56,116\nTa 82,20,17408\nv_add_u32 v12, s72, v12\nMvaa 252,60,116\nRa 16,7,8192\nMvaa 248,60,112\nTa 84,21,16384\n" \
"Mvaa 224,56,112\nTa 86,21,17408\nMvaa 220,108,56\nRa 20,7,10240\nMvaa 244,108,60\nRa 24,7,12288\nMvaa 240,104,60\nRa 28,7,14336\n" \
"Mvaa 216,104,56\nbuffer_store_dwordx4 a[216:219], v0, s[60:63], s95 offen offset:2048 sc1\nMvaa 212,100,56\nMvaa 236,100,60\nMvaa 232,96,60\nMvaa 208,96,56\ns_waitcnt lgkmcnt(0)\nMvaa 64,64,0\n" \
"Ta 96,20,8192\nMvaa 88,64,4\nTa 98,20,9216\nMvaa 92,68,4\nRa 32,27,0\nMvaa 68,68,0\nTa 100,21,8192\nMvaa 72,72,0\n" \
"Ta 102,21,9216\nMvaa 96,72,4\nRa 36,27,2048\nMvaa 100,76,4\nTa 104,22,8192\nMvaa 76,76,0\nTa 106,22,9216\nMvaa 80,0,80\n" \
"Ra 40,27,4096\nMvaa 104,4,80\nTa 108,23,8192\nMvaa 108,4,84\nTa 110,23,9216\nMvaa 84,0,84\nRa 44,27,6144\nMvaa 132,8,84\n" \
"Ta 112,22,16384\nMvaa 156,12,84\nTa 114,22,17408\nMvaa 152,12,80\nRa 48,27,8192\nMvaa 128,8,80\nTa 116,23,16384\nMvaa 124,76,8\n" \
"Ta 118,23,17408\nMvaa 148,76,12\nRa 52,27,10240\nMvaa 144,72,12\nRa 56,27,12288\nMvaa 120,72,8\nRa 60,27,14336\nMvaa 116,68,8\n" \
"buffer_store_dwordx4 a[220:223], v0, s[60:63], s95 offen offset:3072 sc1\nMvaa 140,68,12\ns_add_u32 s95, s34, 24576\nMvaa 136,64,12\nbuffer_store_dwordx4 a[224:227], v0, s[60:63], s95 offen offset:0 sc1\nMvaa 112,64,8\nMvaa 160,64,16\nMvaa 184,64,20\n" \
"Mvaa 188,68,20\ns_waitcnt lgkmcnt(0)\ns_barrier\nMvaa 164,68,16\ns_add_u32 m0, s88, 0\nMvaa 168,72,16\nD 9,64,0,0\nMvaa 192,72,20\n" \
"Mvaa 196,76,20\nMvaa 172,76,16\nMvaa 176,16,80\nD 9,64,s73,1024\nMvaa 200,20,80\nMvaa 204,20,84\nbuffer_store_dwordx4 a[228:231], v0, s[60:63], s95 offen offset:1024 sc1\nMvaa 180,16,84\n" \
"Mvaa 228,24,84\nD 9,64,s74,2048\nMvaa 252,28,84\nMvaa 248,28,80\nMvaa 224,24,80\nMvaa 220,76,24\nD 9,64,s75,3072\nMvaa 244,76,28\n" \
"Mvaa 240,72,28\nMvaa 216,72,24\ns_add_u32 m0, s88, 4096\nMvaa 212,68,24\nD 9,64,s76,0\nMvaa 236,68,28\nMvaa 232,64,28\nMvaa 208,64,24\n" \
"buffer_store_dwordx4 a[232:235], v0, s[60:63], s95 offen offset:2048 sc1\nMvaa 64,96,32\nD 9,64,s77,1024\nMvaa 88,96,36\nMvaa 92,100,36\nMvaa 68,100,32\nMvaa 72,104,32\nD 9,64,s78,2048\n" \
"Mvaa 96,104,36\nMvaa 100,108,36\nMvaa 76,108,32\nMvaa 80,32,112\nD 9,64,s79,3072\nMvaa 104,36,112\nMvaa 108,36,116\nMvaa 84,32,116\n" \
"s_add_u32 m0, s32, 65536\nMvaa 132,40,116\nD 10,68,0,0\nMvaa 156,44,116\nMvaa 152,44,112\nMvaa 128,40,112\nbuffer_store_dwordx4 a[236:239], v0, s[60:63], s95 offen offset:3072 sc1\nMvaa 124,108,40\n" \
"D 10,68,s89,1024\nMvaa 148,108,44\nMvaa 144,104,44\nMvaa 120,104,40\nMvaa 116,100,40\nD 11,68,s90,2048\nMvaa 140,100,44\nMvaa 136,96,44\n" \
"s_waitcnt vmcnt(17)\ns_barrier\nMvaa 112,96,40\nTa 64,20,32768\nMvaa 160,96,48\nD 11,68,s91,3072\nTa 66,20,33792\nMvaa 184,96,52\n" \
"Ra 0,7,32768\nMvaa 188,100,52\nTa 68,21,32768\nMvaa 164,100,48\ns_add_u32 m0, s92, 81920\nTa 70,21,33792\nMvaa 168,104,48\nD 12,68,0,0\n" \
"Ra 4,7,34816\nMvaa 192,104,52\nTa 72,22,32768\nMvaa 196,108,52\nTa 74,22,33792\nMvaa 172,108,48\nRa 8,7,36864\nMvaa 176,48,112\n" \
"D 12,68,s89,1024\nTa 76,23,32768\nMvaa 200,52,112\nTa 78,23,33792\nv_add_u32 v9, 128, v9\nMvaa 204,52,116\nRa 12,7,38912\nv_add_u32 v10, s72, v10\n" \
"Mvaa 180,48,116\nTa 80,20,49152\nv_add_u32 v11, s72, v11\nMvaa 228,56,116\nTa 82,20,50176\nv_add_u32 v12, s72, v12\nMvaa 252,60,116\nRa 16,7,40960\n" \
"Mvaa 248,60,112\nTa 84,21,49152\nMvaa 224,56,112\nTa 86,21,50176\nMvaa 220,108,56\nRa 20,7,43008\nMvaa 244,108,60\nRa 24,7,45056\n" \
"Mvaa 240,104,60\nRa 28,7,47104\ns_add_u32 s95, s34, 28672\nMvaa 216,104,56\nbuffer_store_dwordx4 v[32:35], v0, s[60:63], s95 offen offset:0 sc1\nMvaa 212,100,56\nMvaa 236,100,60\nMvaa 232,96,60\n" \
"Mvaa 208,96,56\ns_waitcnt lgkmcnt(0)\nMvaa 64,64,0\nTa 96,20,40960\nMvaa 88,64,4\nTa 98,20,41984\nMvaa 92,68,4\nRa 32,27,32768\n" \
"Mvaa 68,68,0\nTa 100,21,40960\nMvaa 72,72,0\nTa 102,21,41984\nMvaa 96,72,4\nRa 36,27,34816\nMvaa 100,76,4\nTa 104,22,40960\n" \
"Mvaa 76,76,0\nTa 106,22,41984\nMvaa 80,0,80\nRa 40,27,36864\nMvaa 104,4,80\nTa 108,23,40960\nMvaa 108,4,84\nTa 110,23,41984\n" \
"Mvaa 84,0,84\nRa 44,27,38912\nMvaa 132,8,84\nTa 112,22,49152\nMvaa 156,12,84\nTa 114,22,50176\nMvaa 152,12,80\nRa 48,27,40960\n" \
"Mvaa 128,8,80\nTa 116,23,49152\nMvaa 124,76,8\nTa 118,23,50176\nMvaa 148,76,12\nRa 52,27,43008\nMvaa 144,72,12\nRa 56,27,45056\n" \
"Mvaa 120,72,8\nRa 60,27,47104\nMvaa 116,68,8\nbuffer_store_dwordx4 v[36:39], v0, s[60:63], s95 offen offset:1024 sc1\nMvaa 140,68,12\nMvaa 136,64,12\nbuffer_store_dwordx4 v[40:43], v0, s[60:63], s95 offen offset:2048 sc1\nMvaa 112,64,8\n" \
"Mvaa 160,64,16\nMvaa 184,64,20\nMvaa 188,68,20\ns_waitcnt lgkmcnt(0)\ns_barrier\nMvaa 164,68,16\ns_add_u32 m0, s88, 32768\nMvaa 168,72,16\n" \
"D 9,64,0,0\nMvaa 192,72,20\nMvaa 196,76,20\nbuffer_store_dwordx4 v[44:47], v0, s[60:63], s95 offen offset:3072 sc1\nMvaa 172,76,16\nMvaa 176,16,80\nD 9,64,s73,1024\nMvaa 200,20,80\n" \
"Mvaa 204,20,84\nMvaa 180,16,84\nMvaa 228,24,84\nD 9,64,s74,2048\nMvaa 252,28,84\nMvaa 248,28,80\nMvaa 224,24,80\nMvaa 220,76,24\n" \
"D 9,64,s75,3072\nMvaa 244,76,28\nMvaa 240,72,28\nMvaa 216,72,24\ns_add_u32 m0, s88, 36864\nMvaa 212,68,24\nD 9,64,s76,0\nMvaa 236,68,28\n" \
"Mvaa 232,64,28\nMvaa 208,64,24\nMvaa 64,96,32\nD 9,64,s77,1024\nMvaa 88,96,36\nMvaa 92,100,36\ns_mov_b32 s96, 4\nMvaa 68,100,32\n" \
"Mvaa 72,104,32\nD 9,64,s78,2048\nMvaa 96,104,36\nMvaa 100,108,36\nMvaa 76,108,32\nMvaa 80,32,112\nD 9,64,s79,3072\nMvaa 104,36,112\n" \
"Mvaa 108,36,116\nMvaa 84,32,116\ns_add_u32 m0, s32, 98304\nMvaa 132,40,116\nD 10,68,0,0\nMvaa 156,44,116\nMvaa 152,44,112\nMvaa 128,40,112\n" \
"Mvaa 124,108,40\nD 10,68,s89,1024\nMvaa 148,108,44\nMvaa 144,104,44\nMvaa 120,104,40\nMvaa 116,100,40\nD 11,68,s90,2048\nMvaa 140,100,44\n" \
"Mvaa 136,96,44\ns_waitcnt vmcnt(10)\ns_barrier\nMvaa 112,96,40\nTa 64,20,0\nMvaa 160,96,48\nD 11,68,s91,3072\nTa 66,20,1024\n" \
"Mvaa 184,96,52\nRa 0,7,0\nbuffer_store_dwordx2 v[2:3], off, s[60:63], s35 sc1\nMvaa 188,100,52\nTa 68,21,0\nMvaa 164,100,48\ns_add_u32 m0, s92, 114688\nTa 70,21,1024\n" \
"Mvaa 168,104,48\nD 12,68,0,0\nRa 4,7,2048\nMvaa 192,104,52\nTa 72,22,0\nMvaa 196,108,52\nTa 74,22,1024\nMvaa 172,108,48\n" \
"Ra 8,7,4096\nMvaa 176,48,112\nD 12,68,s89,1024\nTa 76,23,0\nMvaa 200,52,112\nTa 78,23,1024\nv_add_u32 v9, 128, v9\nMvaa 204,52,116\n" \
"Ra 12,7,6144\nv_add_u32 v10, s72, v10\nMvaa 180,48,116\nTa 80,20,16384\nv_add_u32 v11, s72, v11\nMvaa 228,56,116\nTa 82,20,17408\nv_add_u32 v12, s72, v12\n" \
"Mvaa 252,60,116\nRa 16,7,8192\nMvaa 248,60,112\nTa 84,21,16384\nMvaa 224,56,112\nTa 86,21,17408\nMvaa 220,108,56\nRa 20,7,10240\n" \
"Mvaa 244,108,60\nRa 24,7,12288\nMvaa 240,104,60\nRa 28,7,14336\nMvaa 216,104,56\nMvaa 212,100,56\nMvaa 236,100,60\nMvaa 232,96,60\n" \
"Mvaa 208,96,56\ns_waitcnt lgkmcnt(0)\ns_branch 99f\n.p2align 12\nzp29:\ns_setpc_b64 s[98:99]\n99:\ns_nop 0\n" \
"zal:\nMvaa 64,64,0\nTa 96,20,8192\nMvaa 88,64,4\nTa 98,20,9216\nMvaa 92,68,4\nRa 32,27,0\nMvaa 68,68,0\n" \
"Ta 100,21,8192\nMvaa 72,72,0\nTa 102,21,9216\nMvaa 96,72,4\nRa 36,27,2048\nMvaa 100,76,4\nTa 104,22,8192\nMvaa 76,76,0\n" \
"Ta 106,22,9216\nMvaa 80,0,80\nRa 40,27,4096\nMvaa 104,4,80\nTa 108,23,8192\nMvaa 108,4,84\nTa 110,23,9216\nMvaa 84,0,84\n" \
"Ra 44,27,6144\nMvaa 132,8,84\nTa 112,22,16384\nMvaa 156,12,84\nTa 114,22,17408\nMvaa 152,12,80\nRa 48,27,8192\nMvaa 128,8,80\n" \
"Ta 116,23,16384\nMvaa 124,76,8\nTa 118,23,17408\nMvaa 148,76,12\nRa 52,27,10240\nMvaa 144,72,12\nRa 56,27,12288\nMvaa 120,72,8\n" \
"Ra 60,27,14336\nMvaa 116,68,8\nMvaa 140,68,12\nMvaa 136,64,12\nMvaa 112,64,8\nMvaa 160,64,16\nMvaa 184,64,20\nMvaa 188,68,20\n" \
"s_waitcnt lgkmcnt(0)\ns_barrier\nMvaa 164,68,16\ns_add_u32 m0, s88, 0\nMvaa 168,72,16\nD 9,64,0,0\nMvaa 192,72,20\nMvaa 196,76,20\n" \
"Mvaa 172,76,16\nMvaa 176,16,80\nD 9,64,s73,1024\nMvaa 200,20,80\nMvaa 204,20,84\nMvaa 180,16,84\nMvaa 228,24,84\nD 9,64,s74,2048\n" \
"Mvaa 252,28,84\nMvaa 248,28,80\nMvaa 224,24,80\nMvaa 220,76,24\nD 9,64,s75,3072\nMvaa 244,76,28\nMvaa 240,72,28\nMvaa 216,72,24\n" \
"s_add_u32 m0, s88, 4096\nMvaa 212,68,24\nD 9,64,s76,0\nMvaa 236,68,28\nMvaa 232,64,28\nMvaa 208,64,24\nMvaa 64,96,32\nD 9,64,s77,1024\n" \
"Mvaa 88,96,36\nMvaa 92,100,36\nMvaa 68,100,32\nMvaa 72,104,32\nD 9,64,s78,2048\nMvaa 96,104,36\nMvaa 100,108,36\nMvaa 76,108,32\n" \
"Mvaa 80,32,112\nD 9,64,s79,3072\nMvaa 104,36,112\nMvaa 108,36,116\nMvaa 84,32,116\ns_add_u32 m0, s32, 65536\nMvaa 132,40,116\nD 10,68,0,0\n" \
"Mvaa 156,44,116\nMvaa 152,44,112\nMvaa 128,40,112\nMvaa 124,108,40\nD 10,68,s89,1024\nMvaa 148,108,44\nMvaa 144,104,44\nMvaa 120,104,40\n" \
"Mvaa 116,100,40\nD 11,68,s90,2048\nMvaa 140,100,44\nMvaa 136,96,44\ns_waitcnt vmcnt(11)\ns_barrier\nMvaa 112,96,40\nTa 64,20,32768\n" \
"Mvaa 160,96,48\nD 11,68,s91,3072\nTa 66,20,33792\nMvaa 184,96,52\nRa 0,7,32768\nMvaa 188,100,52\nTa 68,21,32768\nMvaa 164,100,48\n" \
"s_add_u32 m0, s92, 81920\nTa 70,21,33792\nMvaa 168,104,48\nD 12,68,0,0\nRa 4,7,34816\nMvaa 192,104,52\nTa 72,22,32768\nMvaa 196,108,52\n" \
"Ta 74,22,33792\nMvaa 172,108,48\nRa 8,7,36864\nMvaa 176,48,112\nD 12,68,s89,1024\nTa 76,23,32768\nMvaa 200,52,112\nTa 78,23,33792\n" \
"v_add_u32 v9, 128, v9\nMvaa 204,52,116\nRa 12,7,38912\nv_add_u32 v10, s72, v10\nMvaa 180,48,116\nTa 80,20,49152\nv_add_u32 v11, s72, v11\nMvaa 228,56,116\n" \
"Ta 82,20,50176\nv_add_u32 v12, s72, v12\nMvaa 252,60,116\nRa 16,7,40960\nMvaa 248,60,112\nTa 84,21,49152\nMvaa 224,56,112\nTa 86,21,50176\n" \
"Mvaa 220,108,56\nRa 20,7,43008\nMvaa 244,108,60\nRa 24,7,45056\nMvaa 240,104,60\nRa 28,7,47104\nMvaa 216,104,56\nMvaa 212,100,56\n" \
"Mvaa 236,100,60\nMvaa 232,96,60\nMvaa 208,96,56\ns_waitcnt lgkmcnt(0)\nMvaa 64,64,0\nTa 96,20,40960\nMvaa 88,64,4\nTa 98,20,41984\n" \
"Mvaa 92,68,4\nRa 32,27,32768\nMvaa 68,68,0\nTa 100,21,40960\nMvaa 72,72,0\nTa 102,21,41984\nMvaa 96,72,4\nRa 36,27,34816\n" \
"Mvaa 100,76,4\nTa 104,22,40960\nMvaa 76,76,0\nTa 106,22,41984\nMvaa 80,0,80\nRa 40,27,36864\nMvaa 104,4,80\nTa 108,23,40960\n" \
"Mvaa 108,4,84\nTa 110,23,41984\nMvaa 84,0,84\nRa 44,27,38912\nMvaa 132,8,84\nTa 112,22,49152\nMvaa 156,12,84\nTa 114,22,50176\n" \
"Mvaa 152,12,80\nRa 48,27,40960\nMvaa 128,8,80\nTa 116,23,49152\nMvaa 124,76,8\nTa 118,23,50176\nMvaa 148,76,12\nRa 52,27,43008\n" \
"Mvaa 144,72,12\nRa 56,27,45056\nMvaa 120,72,8\nRa 60,27,47104\nMvaa 116,68,8\nMvaa 140,68,12\nMvaa 136,64,12\nMvaa 112,64,8\n" \
"Mvaa 160,64,16\nMvaa 184,64,20\nMvaa 188,68,20\ns_waitcnt lgkmcnt(0)\ns_barrier\nMvaa 164,68,16\ns_add_u32 m0, s88, 32768\nMvaa 168,72,16\n" \
"D 9,64,0,0\nMvaa 192,72,20\nMvaa 196,76,20\nMvaa 172,76,16\nMvaa 176,16,80\nD 9,64,s73,1024\nMvaa 200,20,80\nMvaa 204,20,84\n" \
"Mvaa 180,16,84\nMvaa 228,24,84\nD 9,64,s74,2048\nMvaa 252,28,84\nMvaa 248,28,80\nMvaa 224,24,80\nMvaa 220,76,24\nD 9,64,s75,3072\n" \
"Mvaa 244,76,28\nMvaa 240,72,28\nMvaa 216,72,24\ns_add_u32 m0, s88, 36864\nMvaa 212,68,24\nD 9,64,s76,0\nMvaa 236,68,28\nMvaa 232,64,28\n" \
"Mvaa 208,64,24\nMvaa 64,96,32\nD 9,64,s77,1024\nMvaa 88,96,36\nMvaa 92,100,36\nMvaa 68,100,32\nMvaa 72,104,32\nD 9,64,s78,2048\n" \
"Mvaa 96,104,36\nMvaa 100,108,36\nMvaa 76,108,32\nMvaa 80,32,112\nD 9,64,s79,3072\nMvaa 104,36,112\nMvaa 108,36,116\nMvaa 84,32,116\n" \
"s_add_u32 m0, s32, 98304\nMvaa 132,40,116\nD 10,68,0,0\nMvaa 156,44,116\nMvaa 152,44,112\nMvaa 128,40,112\nMvaa 124,108,40\nD 10,68,s89,1024\n" \
"Mvaa 148,108,44\nMvaa 144,104,44\nMvaa 120,104,40\nMvaa 116,100,40\nD 11,68,s90,2048\nMvaa 140,100,44\nMvaa 136,96,44\ns_waitcnt vmcnt(11)\n" \
"s_barrier\nMvaa 112,96,40\nTa 64,20,0\nMvaa 160,96,48\nD 11,68,s91,3072\nTa 66,20,1024\nMvaa 184,96,52\nRa 0,7,0\n" \
"Mvaa 188,100,52\nTa 68,21,0\nMvaa 164,100,48\ns_add_u32 m0, s92, 114688\nTa 70,21,1024\nMvaa 168,104,48\nD 12,68,0,0\nRa 4,7,2048\n" \
"Mvaa 192,104,52\nTa 72,22,0\nMvaa 196,108,52\nTa 74,22,1024\nMvaa 172,108,48\nRa 8,7,4096\nMvaa 176,48,112\nD 12,68,s89,1024\n" \
"Ta 76,23,0\nMvaa 200,52,112\nTa 78,23,1024\nv_add_u32 v9, 128, v9\nMvaa 204,52,116\nRa 12,7,6144\nv_add_u32 v10, s72, v10\nMvaa 180,48,116\n" \
"Ta 80,20,16384\nv_add_u32 v11, s72, v11\nMvaa 228,56,116\nTa 82,20,17408\nv_add_u32 v12, s72, v12\nMvaa 252,60,116\nRa 16,7,8192\nMvaa 248,60,112\n" \
"Ta 84,21,16384\nMvaa 224,56,112\nTa 86,21,17408\nMvaa 220,108,56\nRa 20,7,10240\nMvaa 244,108,60\nRa 24,7,12288\nMvaa 240,104,60\n" \
"Ra 28,7,14336\nMvaa 216,104,56\ns_sub_u32 s96, s96, 1\nMvaa 212,100,56\ns_cmp_lg_u32 s96, 0\nMvaa 236,100,60\nMvaa 232,96,60\nMvaa 208,96,56\n" \
"s_waitcnt lgkmcnt(0)\ns_cbranch_scc1 zal\nMvaa 64,64,0\nTa 96,20,8192\ns_mov_b64 s[64:65], s[84:85]\ns_mov_b32 s66, 4194304\nMvaa 88,64,4\nTa 98,20,9216\n" \
"s_mov_b64 s[68:69], s[86:87]\ns_mov_b32 s70, 8388608\nMvaa 92,68,4\nRa 32,27,0\ns_mov_b32 s73, 15360\ns_mov_b32 s74, 129024\nMvaa 68,68,0\nTa 100,21,8192\n" \
"s_mov_b32 s75, 144384\ns_add_u32 s76, s37, 0\nMvaa 72,72,0\nTa 102,21,9216\ns_add_u32 s77, s37, 15360\ns_add_u32 s78, s37, 129024\nMvaa 96,72,4\nRa 36,27,2048\n" \
"s_add_u32 s79, s37, 144384\ns_mov_b32 s89, 31744\nMvaa 100,76,4\nTa 104,22,8192\ns_mov_b32 s90, 63488\ns_mov_b32 s91, 95232\nMvaa 76,76,0\nTa 106,22,9216\n" \
"s_mov_b32 s72, 524288\nv_xor_b32 v28, 128, v14\nMvaa 80,0,80\nRa 40,27,4096\nMvaa 104,4,80\nTa 108,23,8192\nMvaa 108,4,84\nTa 110,23,9216\n" \
"Mvaa 84,0,84\nRa 44,27,6144\nMvaa 132,8,84\nTa 112,22,16384\nMvaa 156,12,84\nTa 114,22,17408\nMvaa 152,12,80\nRa 48,27,8192\n" \
"Mvaa 128,8,80\nTa 116,23,16384\nMvaa 124,76,8\nTa 118,23,17408\nMvaa 148,76,12\nRa 52,27,10240\nMvaa 144,72,12\nRa 56,27,12288\n" \
"Mvaa 120,72,8\nRa 60,27,14336\nMvaa 116,68,8\nMvaa 140,68,12\nMvaa 136,64,12\nMvaa 112,64,8\nMvaa 160,64,16\nMvaa 184,64,20\n" \
"Mvaa 188,68,20\ns_waitcnt lgkmcnt(0)\ns_barrier\nMvaa 164,68,16\ns_add_u32 m0, s88, 131072\nMvaa 168,72,16\nD 13,64,0,0\nMvaa 192,72,20\n" \
"Mvaa 196,76,20\nMvaa 172,76,16\nMvaa 176,16,80\nD 13,64,s73,1024\nMvaa 200,20,80\nMvaa 204,20,84\nMvaa 180,16,84\nMvaa 228,24,84\n" \
"D 13,64,s74,2048\nMvaa 252,28,84\nMvaa 248,28,80\nMvaa 224,24,80\nMvaa 220,76,24\nD 13,64,s75,3072\nMvaa 244,76,28\nMvaa 240,72,28\n" \
"Mvaa 216,72,24\ns_add_u32 m0, s88, 135168\nMvaa 212,68,24\nD 13,64,s76,0\nMvaa 236,68,28\nMvaa 232,64,28\nMvaa 208,64,24\nMvaa 64,96,32\n" \
"D 13,64,s77,1024\nMvaa 88,96,36\nMvaa 92,100,36\nMvaa 68,100,32\nMvaa 72,104,32\nD 13,64,s78,2048\nMvaa 96,104,36\nMvaa 100,108,36\n" \
"Mvaa 76,108,32\nMvaa 80,32,112\nD 13,64,s79,3072\nMvaa 104,36,112\nMvaa 108,36,116\nMvaa 84,32,116\ns_add_u32 m0, s32, 81920\nMvaa 132,40,116\n" \
"D 14,68,0,0\nMvaa 156,44,116\nMvaa 152,44,112\nMvaa 128,40,112\nMvaa 124,108,40\nD 14,68,s89,1024\nMvaa 148,108,44\nMvaa 144,104,44\n" \
"Mvaa 120,104,40\nMvaa 116,100,40\nD 28,68,s90,2048\nMvaa 140,100,44\nMvaa 136,96,44\ns_waitcnt vmcnt(11)\ns_barrier\nMvaa 112,96,40\n" \
"Ta 64,20,32768\nMvaa 160,96,48\nD 28,68,s91,3072\nTa 66,20,33792\nMvaa 184,96,52\nRa 0,7,32768\nMvaa 188,100,52\nTa 68,21,32768\n" \
"Mvaa 164,100,48\ns_add_u32 m0, s88, 0\nv_add_u32 v13, 128, v13\nTa 70,21,33792\nMvaa 168,104,48\nD 13,64,0,0\nRa 4,7,34816\nMvaa 192,104,52\n" \
"Ta 72,22,32768\nMvaa 196,108,52\nTa 74,22,33792\nMvaa 172,108,48\nRa 8,7,36864\nMvaa 176,48,112\nD 13,64,s73,1024\nTa 76,23,32768\n" \
"Mvaa 200,52,112\nTa 78,23,33792\nMvaa 204,52,116\nRa 12,7,38912\nMvaa 180,48,116\nTa 80,20,49152\nMvaa 228,56,116\nD 13,64,s74,2048\n" \
"Ta 82,20,50176\nMvaa 252,60,116\nRa 16,7,40960\nMvaa 248,60,112\nTa 84,21,49152\nMvaa 224,56,112\nTa 86,21,50176\nMvaa 220,108,56\n" \
"D 13,64,s75,3072\nRa 20,7,43008\nMvaa 244,108,60\nRa 24,7,45056\nMvaa 240,104,60\nRa 28,7,47104\nMvaa 216,104,56\nMvaa 212,100,56\n" \
"Mvaa 236,100,60\nMvaa 232,96,60\nMvaa 208,96,56\ns_waitcnt lgkmcnt(0)\nMvaa 64,64,0\nTa 96,20,40960\nMvaa 88,64,4\nTa 98,20,41984\n" \
"Mvaa 92,68,4\nRa 32,27,32768\nMvaa 68,68,0\nTa 100,21,40960\nMvaa 72,72,0\nTa 102,21,41984\nMvaa 96,72,4\nRa 36,27,34816\n" \
"Mvaa 100,76,4\nTa 104,22,40960\nMvaa 76,76,0\nTa 106,22,41984\nMvaa 80,0,80\nRa 40,27,36864\ns_branch 99f\n.p2align 12\n" \
"zp30:\ns_setpc_b64 s[98:99]\n99:\nMvaa 104,4,80\nTa 108,23,40960\nMvaa 108,4,84\nTa 110,23,41984\nMvaa 84,0,84\n" \
"Ra 44,27,38912\nMvaa 132,8,84\nTa 112,22,49152\nMvaa 156,12,84\nTa 114,22,50176\nMvaa 152,12,80\nRa 48,27,40960\nMvaa 128,8,80\n" \
"Ta 116,23,49152\nMvaa 124,76,8\nTa 118,23,50176\nMvaa 148,76,12\nRa 52,27,43008\nMvaa 144,72,12\nRa 56,27,45056\ns_and_b32 s96, s32, 0x1000\n" \
"s_lshl_b32 s97, s96, 1\nMvaa 120,72,8\nRa 60,27,47104\ns_lshl_b32 s96, s96, 2\nv_add_u32 v16, s97, v7\nMvaa 116,68,8\nv_subrev_u32 v20, s96, v8\nv_xor_b32 v17, 64, v16\n" \
"v_add_u32 v18, 0x20000, v16\nMvaa 140,68,12\nv_add_u32 v19, 0x20000, v17\nv_xor_b32 v21, 32, v20\nv_xor_b32 v22, 64, v20\nMvaa 136,64,12\nv_xor_b32 v23, 96, v20\nv_xor_b32 v24, 128, v20\n" \
"v_xor_b32 v25, 160, v20\nMvaa 112,64,8\nv_xor_b32 v26, 192, v20\nv_xor_b32 v27, 224, v20\nMvaa 160,64,16\nMvaa 184,64,20\nMvaa 188,68,20\ns_waitcnt vmcnt(4) lgkmcnt(0)\n" \
"s_barrier\nMvaa 164,68,16\ns_add_u32 m0, s88, 4096\nMvaa 168,72,16\nD 13,64,s76,0\nMvaa 192,72,20\nTa 160,20,24576\nTa 162,20,25600\n" \
"Mvaa 196,76,20\nTa 164,21,24576\nMvaa 172,76,16\nD 13,64,s77,1024\nMvaa 176,16,80\nTa 166,21,25600\nTa 168,22,24576\nMvaa 200,20,80\n" \
"Ta 170,22,25600\nMvaa 204,20,84\nD 13,64,s78,2048\nMvaa 180,16,84\nTa 172,23,24576\nTa 174,23,25600\nMvaa 228,24,84\nTa 176,24,24576\n" \
"Mvaa 252,28,84\nD 13,64,s79,3072\nMvaa 248,28,80\nTa 178,24,25600\nTa 180,25,24576\nMvaa 224,24,80\ns_add_u32 m0, s32, 65536\nv_add_u32 v14, s72, v14\n" \
"v_add_u32 v28, s72, v28\nMvaa 220,76,24\nD 14,68,0,0\nMvaa 244,76,28\nTa 182,25,25600\nMvaa 240,72,28\nTa 184,26,24576\nTa 186,26,25600\n" \
"Mvaa 216,72,24\nD 14,68,s89,1024\nMvaa 212,68,24\nTa 188,27,24576\nMvaa 236,68,28\nTa 190,27,25600\nMvaa 232,64,28\nD 28,68,s90,2048\n" \
"Mvaa 208,64,24\nMvaa 112,96,40\nTa 64,20,16384\nMvaa 116,100,40\nD 28,68,s91,3072\nMvaa 120,104,40\nTa 66,20,17408\nMvaa 124,108,40\n" \
"s_add_u32 m0, s88, 32768\nv_add_u32 v13, 128, v13\nMvaa 128,40,112\nD 13,64,0,0\nMvaa 132,40,116\nRa 0,18,0\nMvaa 136,96,44\nRa 16,19,0\n" \
"Mvaa 140,100,44\nD 13,64,s73,1024\nMvaa 144,104,44\nTa 68,21,16384\nMvaa 148,108,44\nTa 70,21,17408\nC 112,112,113\nC 113,114,115\n" \
"Mvaa 152,44,112\nD 13,64,s74,2048\nC 114,116,117\nMvaa 156,44,116\nTa 72,22,16384\nC 115,118,119\nC 120,120,121\nMvaa 160,96,48\n" \
"C 121,122,123\nC 122,124,125\nC 123,126,127\nMvaa 164,100,48\nD 13,64,s75,3072\nC 38,128,129\nMvaa 168,104,48\nTa 74,22,17408\n" \
"C 39,130,131\nC 56,132,133\nMvaa 172,108,48\ns_add_u32 m0, s88, 36864\nTa 76,23,16384\nC 57,134,135\nMvaa 176,48,112\nD 13,64,s76,0\n" \
"AW 200,112\nMvaa 180,48,116\nTa 78,23,17408\nAW 201,113\nAW 202,114\nMvaa 184,96,52\nTa 80,24,16384\nAW 203,115\n" \
"AW 232,120\nMvaa 188,100,52\nD 13,64,s77,1024\nAW 233,121\nMvaa 192,104,52\nAW 234,122\nAW 235,123\nC 136,136,137\n" \
"Mvaa 196,108,52\nTa 82,24,17408\nC 137,138,139\nC 138,140,141\nMvaa 200,52,112\nD 13,64,s78,2048\nC 139,142,143\nMvaa 204,52,116\n" \
"Ta 84,25,16384\nC 144,144,145\nC 145,146,147\nMvaa 64,96,32\nTa 86,25,17408\nC 146,148,149\nC 147,150,151\nMvaa 68,100,32\n" \
"D 13,64,s79,3072\nC 40,152,153\nMvaa 72,104,32\nTa 88,26,16384\nC 41,154,155\nC 58,156,157\nMvaa 76,108,32\ns_add_u32 m0, s32, 98304\n" \
"v_add_u32 v14, s72, v14\nv_add_u32 v28, s72, v28\nMvaa 80,32,112\nD 14,68,0,0\nC 59,158,159\nMvaa 84,32,116\nTa 90,26,17408\nAW 204,136\n" \
"AW 205,137\nMvaa 208,96,56\nTa 92,27,16384\nAW 206,138\nAW 207,139\nMvaa 212,100,56\nD 14,68,s89,1024\nAW 236,144\n" \
"Mvaa 216,104,56\nTa 94,27,17408\nAW 237,145\nAW 238,146\nMvaa 220,108,56\nAW 239,147\nC 160,160,161\nC 161,162,163\n" \
"Mvaa 224,56,112\nD 28,68,s90,2048\nC 162,164,165\nMvaa 228,56,116\nRa 4,18,2048\nC 163,166,167\nC 168,168,169\nMvaa 232,96,60\n" \
"Ra 20,19,2048\nC 169,170,171\nC 170,172,173\nMvaa 236,100,60\nD 28,68,s91,3072\nC 171,174,175\nMvaa 240,104,60\nRa 8,18,4096\n" \
"C 42,176,177\nC 43,178,179\nMvaa 244,108,60\nRa 24,19,4096\nC 60,180,181\nC 61,182,183\nMvaa 248,60,112\nRa 12,18,6144\n" \
"AW 208,160\nAW 209,161\nMvaa 252,60,116\nRa 28,19,6144\nAW 210,162\nAW 211,163\nMvaa 88,96,36\nAW 240,168\n" \
"AW 241,169\nAW 242,170\nMvaa 92,100,36\nAW 243,171\nC 184,184,185\nC 185,186,187\nMvaa 96,104,36\nC 186,188,189\n" \
"C 187,190,191\nC 192,192,193\nMvaa 100,108,36\nC 193,194,195\nC 194,196,197\nC 195,198,199\nMvaa 104,36,112\nC 44,200,201\n" \
"C 45,202,203\nC 62,204,205\nMvaa 108,36,116\nC 63,206,207\nAW 212,184\nAW 213,185\ns_waitcnt lgkmcnt(6)\nZvaa 128,64,0\n" \
"v_add_u32 v13, 128, v13\nAW 214,186\nAW 215,187\nMvaa 128,160,16\ns_add_u32 m0, s88, 131072\nAW 244,192\nAW 245,193\nZvaa 132,164,16\n" \
"D 13,64,0,0\nAW 246,194\nMvaa 132,68,0\nv_add_u32 v14, s72, v14\nAW 247,195\nC 64,64,65\nZvaa 136,72,0\nv_add_u32 v28, s72, v28\n" \
"C 65,66,67\nC 66,68,69\nMvaa 136,168,16\nC 67,70,71\nC 72,72,73\nC 73,74,75\nZvaa 140,172,16\nC 74,76,77\n" \
"C 75,78,79\nC 34,80,81\nMvaa 140,76,0\ns_waitcnt lgkmcnt(0)\nD 13,64,s73,1024\nZvaa 144,80,0\nC 35,82,83\nC 52,84,85\n" \
"C 53,86,87\nMvaa 144,176,16\nAW 192,64\nAW 193,65\nAW 194,66\nZvaa 148,180,16\nAW 195,67\nAW 224,72\n" \
"AW 225,73\nMvaa 148,84,0\nAW 226,74\nAW 227,75\nC 208,208,209\nZvaa 152,88,0\nD 13,64,s74,2048\nC 209,210,211\n" \
"Mvaa 152,184,16\nC 210,212,213\nC 211,214,215\nC 216,216,217\nZvaa 156,188,16\nC 217,218,219\nC 218,220,221\nC 219,222,223\n" \
"Mvaa 156,92,0\nC 46,224,225\nC 47,226,227\nC 64,228,229\nZvaa 188,92,4\ns_waitcnt vmcnt(15)\ns_barrier\nC 65,230,231\n" \
"Mvaa 188,188,20\nTa 96,20,0\nTa 98,20,1024\nAW 216,208\nZvaa 184,184,20\nD 13,64,s75,3072\nAW 217,209\nMvaa 184,88,4\n" \
"Ta 128,20,8192\nTa 130,20,9216\nAW 218,210\nZvaa 180,84,4\nAW 219,211\nAW 248,216\nAW 249,217\nMvaa 180,180,20\n" \
"Ra 32,16,0\nRa 48,17,0\nAW 250,218\nZvaa 176,176,20\ns_add_u32 m0, s88, 135168\nTa 100,21,0\nAW 251,219\nMvaa 176,80,4\n" \
"D 13,64,s76,0\nC 232,232,233\nZvaa 172,76,4\nTa 102,21,1024\nTa 132,21,8192\nC 233,234,235\nMvaa 172,172,20\nC 234,236,237\n" \
"C 235,238,239\nC 240,240,241\nZvaa 168,168,20\nTa 134,21,9216\nTa 104,22,0\nC 241,242,243\nMvaa 168,72,4\nTa 106,22,1024\n" \
"Ta 136,22,8192\nC 242,244,245\nZvaa 164,68,4\nD 13,64,s77,1024\nC 243,246,247\nMvaa 164,164,20\nC 48,248,249\nC 49,250,251\n" \
"C 66,252,253\nZvaa 160,160,20\nTa 138,22,9216\nTa 108,23,0\nC 67,254,255\nMvaa 160,64,4\nAW 220,232\nAW 221,233\n" \
"AW 222,234\nZvaa 192,64,8\nTa 110,23,1024\nTa 140,23,8192\nAW 223,235\nMvaa 192,160,24\nD 13,64,s78,2048\nAW 252,240\n" \
"Zvaa 196,164,24\nTa 142,23,9216\nTa 112,24,0\nAW 253,241\nMvaa 196,68,8\nAW 254,242\nAW 255,243\nC 88,88,89\n" \
"Zvaa 200,72,8\nTa 114,24,1024\nTa 144,24,8192\nC 89,90,91\nMvaa 200,168,24\nTa 146,24,9216\nTa 116,25,0\nC 90,92,93\n" \
"Zvaa 204,172,24\nD 13,64,s79,3072\nC 91,94,95\nMvaa 204,76,8\nC 96,96,97\nC 97,98,99\nC 98,100,101\nZvaa 208,80,8\n" \
"Ta 118,25,1024\nTa 148,25,8192\nC 99,102,103\nMvaa 208,176,24\nv_add_u32 v13, 128, v13\nTa 150,25,9216\nC 36,104,105\nZvaa 212,180,24\n" \
"s_add_u32 m0, s32, 81920\nTa 120,26,0\nC 37,106,107\nMvaa 212,84,8\nD 14,68,0,0\nC 54,108,109\nZvaa 216,88,8\nTa 122,26,1024\n" \
"Ta 152,26,8192\nC 55,110,111\nMvaa 216,184,24\nAW 196,88\nAW 197,89\nAW 198,90\nZvaa 220,188,24\nTa 154,26,9216\n" \
"Ta 124,27,0\nAW 199,91\nMvaa 220,92,8\nAW 228,96\nAW 229,97\nAW 230,98\nZvaa 252,92,12\nD 14,68,s89,1024\n" \
"AW 231,99\nMvaa 252,188,28\nTa 126,27,1024\nTa 156,27,8192\nZvaa 248,184,28\nTa 158,27,9216\nRa 36,16,2048\nMvaa 248,88,12\n" \
"Zvaa 244,84,12\nRa 52,17,2048\nRa 40,16,4096\nMvaa 244,180,28\nD 28,68,s90,2048\nZvaa 240,176,28\nRa 56,17,4096\nRa 44,16,6144\n" \
"Mvaa 240,80,12\nZvaa 236,76,12\nRa 60,17,6144\nMvaa 236,172,28\nZvaa 232,168,28\nD 28,68,s91,3072\nMvaa 232,72,12\nZvaa 228,68,12\n" \
"Mvaa 228,164,28\nv_add_u32 v14, s72, v14\nZvaa 224,160,28\nv_add_u32 v28, s72, v28\nMvaa 224,64,12\ns_waitcnt lgkmcnt(6)\nMvaa 128,96,32\nbuffer_store_dwordx4 a[252:255], v0, s[60:63], s46 offen sc1\n" \
"Mvaa 128,128,48\ns_add_u32 m0, s88, 0\nMvaa 132,132,48\nD 13,64,0,0\nMvaa 132,100,32\nbuffer_store_dwordx2 v[48:49], v0, s[60:63], s46 offen offset:1024 sc1\nMvaa 136,104,32\nbuffer_store_dwordx2 v[66:67], v0, s[60:63], s46 offen offset:1032 sc1\n" \
"Mvaa 136,136,48\nv_mov_b32 v100, 0\nv_bfe_u32 v104, v0, 4, 4\nMvaa 140,140,48\nv_lshrrev_b32 v105, 6, v0\nv_and_b32 v105, 12, v105\nMvaa 140,108,32\ns_waitcnt lgkmcnt(0)\n" \
"D 13,64,s73,1024\nMvaa 144,112,32\nv_sub_u32 v104, v104, v105\nv_mov_b32 v107, 0xff800000\nMvaa 144,144,48\ns_mov_b32 s93, 0x3e8293ee\nv_subrev_u32 v105, 0, v104\nMvaa 148,148,48\n" \
"v_and_b32 v106, 0x80000001, v105\nv_cmp_eq_u32 vcc, 0, v106\nMvaa 148,116,32\nv_mov_b32 v84, 0x3f803f80\ns_nop 0\nMvaa 152,120,32\nD 13,64,s74,2048\nMvaa 152,152,48\n" \
"v_cndmask_b32 v76, v107, v100, vcc\nv_cmp_eq_u32 vcc, 0x80000000, v106\nMvaa 156,156,48\ns_nop 1\nv_cndmask_b32 v72, v107, v100, vcc\nMvaa 156,124,32\nv_subrev_u32 v105, 1, v104\nv_and_b32 v106, 0x80000001, v105\n" \
"Mvaa 188,124,36\ns_waitcnt vmcnt(18)\ns_barrier\nMvaa 188,156,52\nTa 64,20,32768\nTa 66,20,33792\nMvaa 184,152,52\nD 13,64,s75,3072\n" \
"Mvaa 184,120,36\nTa 160,20,40960\nTa 162,20,41984\nMvaa 180,116,36\nv_cmp_eq_u32 vcc, 0, v106\ns_branch 99f\n.p2align 12\nzp31:\n" \
"s_setpc_b64 s[98:99]\n99:\nv_mov_b32 v85, 0x3f803f80\nMvaa 180,148,52\nRa 0,16,32768\nRa 16,17,32768\nMvaa 176,144,52\ns_add_u32 m0, s88, 4096\n" \
"Ta 68,21,32768\nMvaa 176,112,36\nD 13,64,s76,0\nMvaa 172,108,36\nTa 70,21,33792\nTa 164,21,40960\nMvaa 172,140,52\ns_nop 0\n" \
"v_cndmask_b32 v77, v107, v100, vcc\nMvaa 168,136,52\nTa 166,21,41984\nTa 72,22,32768\nMvaa 168,104,36\nTa 74,22,33792\nTa 168,22,40960\nMvaa 164,100,36\n" \
"D 13,64,s77,1024\nMvaa 164,132,52\nv_cmp_eq_u32 vcc, 0x80000000, v106\ns_nop 1\nMvaa 160,128,52\nTa 170,22,41984\nTa 76,23,32768\nMvaa 160,96,36\n" \
"v_cndmask_b32 v73, v107, v100, vcc\nv_subrev_u32 v105, 2, v104\nMvaa 192,96,40\nTa 78,23,33792\nTa 172,23,40960\nMvaa 192,128,56\nD 13,64,s78,2048\nMvaa 196,132,56\n" \
"Ta 174,23,41984\nTa 80,24,32768\nMvaa 196,100,40\nv_and_b32 v106, 0x80000001, v105\nv_cmp_eq_u32 vcc, 0, v106\nMvaa 200,104,40\nTa 82,24,33792\nTa 176,24,40960\n" \
"Mvaa 200,136,56\nTa 178,24,41984\nTa 84,25,32768\nMvaa 204,140,56\nD 13,64,s79,3072\nMvaa 204,108,40\nv_mov_b32 v86, 0x3f803f80\ns_nop 0\n" \
"Mvaa 208,112,40\nTa 86,25,33792\nTa 180,25,40960\nMvaa 208,144,56\nv_add_u32 v13, 128, v13\nTa 182,25,41984\nMvaa 212,148,56\ns_add_u32 m0, s32, 65536\n" \
"Ta 88,26,32768\nMvaa 212,116,40\nD 14,68,0,0\nMvaa 216,120,40\nTa 90,26,33792\nTa 184,26,40960\nMvaa 216,152,56\nv_cndmask_b32 v78, v107, v100, vcc\n" \
"v_cmp_eq_u32 vcc, 0x80000000, v106\nMvaa 220,156,56\nTa 186,26,41984\nTa 92,27,32768\nMvaa 220,124,40\ns_nop 1\nv_cndmask_b32 v74, v107, v100, vcc\nMvaa 252,124,44\n" \
"D 14,68,s89,1024\nMvaa 252,156,60\nTa 94,27,33792\nTa 188,27,40960\nMvaa 248,152,60\nTa 190,27,41984\nRa 4,16,34816\nMvaa 248,120,44\n" \
"v_subrev_u32 v105, 3, v104\nv_and_b32 v106, 0x80000001, v105\nMvaa 244,116,44\nRa 20,17,34816\nRa 8,16,36864\nMvaa 244,148,60\nD 28,68,s90,2048\nMvaa 240,144,60\n" \
"Ra 24,17,36864\nRa 12,16,38912\nMvaa 240,112,44\nv_cmp_eq_u32 vcc, 0, v106\nv_mov_b32 v87, 0x3f803f80\nMvaa 236,108,44\nRa 28,17,38912\ns_nop 0\n" \
"Mvaa 236,140,60\nv_cndmask_b32 v79, v107, v100, vcc\nv_cmp_eq_u32 vcc, 0x80000000, v106\nMvaa 232,136,60\nD 28,68,s91,3072\nMvaa 232,104,44\ns_nop 1\nv_cndmask_b32 v75, v107, v100, vcc\n" \
"Mvaa 228,100,44\ns_cmp_eq_u32 s51, 0\ns_cselect_b64 vcc, -1, 0\nMvaa 228,132,60\nv_add_u32 v14, s72, v14\nv_cndmask_b32 v80, v107, v72, vcc\nMvaa 224,128,60\nv_add_u32 v28, s72, v28\n" \
"v_cndmask_b32 v81, v107, v73, vcc\nMvaa 224,96,44\ns_waitcnt lgkmcnt(6)\nv_cndmask_b32 v82, v107, v74, vcc\nMvaa 128,64,0\nv_cndmask_b32 v83, v107, v75, vcc\nQvaa 88,224,196,72\nQvaa 92,228,196,76\n" \
"Qvaa 108,228,200,72\nQvaa 112,232,200,76\nMvaa 128,160,16\ns_add_u32 m0, s88, 32768\nMvaa 132,164,16\nD 13,64,0,0\nMvaa 132,68,0\nMvaa 136,72,0\n" \
"v_max3_f32 v104, v88, v89, v90\nv_max3_f32 v105, v91, v92, v93\nMvaa 136,168,16\nv_max3_f32 v104, v104, v94, v95\nv_max_f32 v104, v104, v105\nMvaa 140,172,16\nv_mov_b32 v105, v104\nv_max3_f32 v124, v108, v109, v110\n" \
"Mvaa 140,76,0\ns_waitcnt lgkmcnt(0)\nD 13,64,s73,1024\nMvaa 144,80,0\nv_max3_f32 v125, v111, v112, v113\nv_max3_f32 v124, v124, v114, v115\nMvaa 144,176,16\nv_max_f32 v124, v124, v125\n" \
"v_mov_b32 v125, v124\nMvaa 148,180,16\nv_permlane16_swap_b32 v104, v105\nv_permlane16_swap_b32 v124, v125\nMvaa 148,84,0\nv_max_f32 v104, v104, v105\nv_mov_b32 v105, v104\nMvaa 152,88,0\n" \
"D 13,64,s74,2048\nMvaa 152,184,16\nv_max_f32 v124, v124, v125\nv_mov_b32 v125, v124\nMvaa 156,188,16\nv_permlane32_swap_b32 v104, v105\nv_permlane32_swap_b32 v124, v125\nMvaa 156,92,0\n" \
"v_max_f32 v104, v104, v105\nv_mul_f32 v104, s93, v104\nMvaa 188,92,4\ns_waitcnt vmcnt(18)\ns_barrier\nMvaa 188,188,20\nTa 96,20,16384\nTa 98,20,17408\n" \
"Mvaa 184,184,20\nD 13,64,s75,3072\nMvaa 184,88,4\nTa 128,20,24576\nTa 130,20,25600\nMvaa 180,84,4\nv_fma_f32 v88, v88, s93, -v104\nv_fma_f32 v89, v89, s93, -v104\n" \
"Mvaa 180,180,20\nRa 32,18,0\nRa 48,19,0\nMvaa 176,176,20\ns_add_u32 m0, s88, 36864\nTa 100,21,16384\nMvaa 176,80,4\nD 13,64,s76,0\n" \
"Mvaa 172,76,4\nTa 102,21,17408\nTa 132,21,24576\nMvaa 172,172,20\nv_fma_f32 v90, v90, s93, -v104\nv_fma_f32 v91, v91, s93, -v104\nMvaa 168,168,20\nTa 134,21,25600\n" \
"Ta 104,22,16384\nMvaa 168,72,4\nTa 106,22,17408\nTa 136,22,24576\nMvaa 164,68,4\nD 13,64,s77,1024\nMvaa 164,164,20\nv_fma_f32 v92, v92, s93, -v104\n" \
"v_fma_f32 v93, v93, s93, -v104\nMvaa 160,160,20\nTa 138,22,25600\nTa 108,23,16384\nMvaa 160,64,4\nv_fma_f32 v94, v94, s93, -v104\nv_fma_f32 v95, v95, s93, -v104\nMvaa 192,64,8\n" \
"Ta 110,23,17408\nTa 140,23,24576\nMvaa 192,160,24\nD 13,64,s78,2048\nMvaa 196,164,24\nTa 142,23,25600\nTa 112,24,16384\nMvaa 196,68,8\n" \
"E 88,88\nMvaa 200,72,8\nTa 114,24,17408\nTa 144,24,24576\nMvaa 200,168,24\nTa 146,24,25600\nTa 116,25,16384\nMvaa 204,172,24\n" \
"D 13,64,s79,3072\nMvaa 204,76,8\nE 89,89\nMvaa 208,80,8\nTa 118,25,17408\nTa 148,25,24576\nMvaa 208,176,24\nv_add_u32 v13, 128, v13\n" \
"Ta 150,25,25600\nMvaa 212,180,24\ns_add_u32 m0, s32, 98304\nTa 120,26,16384\nMvaa 212,84,8\nD 14,68,0,0\nMvaa 216,88,8\nTa 122,26,17408\n" \
"Ta 152,26,24576\nMvaa 216,184,24\nE 90,90\nMvaa 220,188,24\nTa 154,26,25600\nTa 124,27,16384\nMvaa 220,92,8\nC 96,88,89\n" \
"Mvaa 252,92,12\nD 14,68,s89,1024\nMvaa 252,188,28\nTa 126,27,17408\nTa 156,27,24576\nMvaa 248,184,28\nTa 158,27,25600\nRa 36,18,2048\n" \
"Mvaa 248,88,12\nE 91,91\nMvaa 244,84,12\nRa 52,19,2048\nRa 40,18,4096\nMvaa 244,180,28\nD 28,68,s90,2048\nMvaa 240,176,28\n" \
"Ra 56,19,4096\nRa 44,18,6144\nMvaa 240,80,12\nE 92,92\nMvaa 236,76,12\nRa 60,19,6144\nC 97,90,91\nMvaa 236,172,28\n" \
"E 93,93\nMvaa 232,168,28\nD 28,68,s91,3072\nMvaa 232,72,12\nE 94,94\nMvaa 228,68,12\nC 98,92,93\nMvaa 228,164,28\n" \
"v_add_u32 v14, s72, v14\nMvaa 224,160,28\nv_add_u32 v28, s72, v28\nMvaa 224,64,12\ns_waitcnt lgkmcnt(6)\nMvaa 128,96,32\nE 95,95\nMvaa 128,128,48\n" \
"s_add_u32 m0, s88, 131072\nv_max_f32 v124, v124, v125\nMvaa 132,132,48\nD 13,64,0,0\nMvaa 132,100,32\nv_mul_f32 v124, s93, v124\nv_fma_f32 v108, v108, s93, -v124\nMvaa 136,104,32\n" \
"v_fma_f32 v109, v109, s93, -v124\nv_fma_f32 v110, v110, s93, -v124\nMvaa 136,136,48\nv_fma_f32 v111, v111, s93, -v124\nv_fma_f32 v112, v112, s93, -v124\nMvaa 140,140,48\nv_fma_f32 v113, v113, s93, -v124\nv_fma_f32 v114, v114, s93, -v124\n" \
"Mvaa 140,108,32\ns_waitcnt lgkmcnt(0)\nD 13,64,s73,1024\nMvaa 144,112,32\nv_fma_f32 v115, v115, s93, -v124\nMvaa 144,144,48\nE 108,108\nMvaa 148,148,48\n" \
"E 109,109\nMvaa 148,116,32\nE 110,110\nMvaa 152,120,32\nD 13,64,s74,2048\nMvaa 152,152,48\nC 116,108,109\nMvaa 156,156,48\n" \
"E 111,111\nMvaa 156,124,32\nE 112,112\nMvaa 188,124,36\ns_waitcnt vmcnt(15)\ns_barrier\nMvaa 188,156,52\nTa 64,20,0\n" \
"Ta 66,20,1024\nMvaa 184,152,52\nD 13,64,s75,3072\nMvaa 184,120,36\nTa 160,20,8192\nTa 162,20,9216\nbuffer_store_dwordx2 v[2:3], off, s[60:63], s48 sc1\nMvaa 180,116,36\n" \
"C 117,110,111\nMvaa 180,148,52\nRa 0,16,0\nRa 16,17,0\nMvaa 176,144,52\ns_add_u32 m0, s88, 135168\nTa 68,21,0\nMvaa 176,112,36\n" \
"D 13,64,s76,0\nMvaa 172,108,36\nTa 70,21,1024\nTa 164,21,8192\nMvaa 172,140,52\nE 113,113\nMvaa 168,136,52\nTa 166,21,9216\n" \
"Ta 72,22,0\nMvaa 168,104,36\nTa 74,22,1024\nTa 168,22,8192\nMvaa 164,100,36\nD 13,64,s77,1024\nMvaa 164,132,52\nE 114,114\n" \
"Mvaa 160,128,52\nTa 170,22,9216\nTa 76,23,0\nMvaa 160,96,36\nC 118,112,113\nMvaa 192,96,40\nTa 78,23,1024\nTa 172,23,8192\n" \
"Mvaa 192,128,56\nD 13,64,s78,2048\nMvaa 196,132,56\nTa 174,23,9216\nTa 80,24,0\nMvaa 196,100,40\nE 115,115\nMvaa 200,104,40\n" \
"Ta 82,24,1024\nTa 176,24,8192\nMvaa 200,136,56\nTa 178,24,9216\nTa 84,25,0\nMvaa 204,140,56\nD 13,64,s79,3072\nMvaa 204,108,40\n" \
"C 99,94,95\nC 119,114,115\nMvaa 208,112,40\nTa 86,25,1024\nTa 180,25,8192\nZvvv 100,84,96\nZvvv 88,34,96\nZvvv 92,52,96\n" \
"Zvvv 120,84,116\nZvvv 108,36,116\nZvvv 112,54,116\nMvaa 208,144,56\nv_add_u32 v13, 128, v13\nTa 182,25,9216\nMvaa 212,148,56\ns_add_u32 m0, s32, 81920\n" \
"Ta 88,26,0\nMvaa 212,116,40\nD 14,68,0,0\nMvaa 216,120,40\nTa 90,26,1024\nTa 184,26,8192\nMvaa 216,152,56\nv_rcp_f32 v104, v100\n" \
"Mvaa 220,156,56\nTa 186,26,9216\nTa 92,27,0\nMvaa 220,124,40\nv_rcp_f32 v124, v120\nMvaa 252,124,44\nD 14,68,s89,1024\nMvaa 252,156,60\n" \
"Ta 94,27,1024\nTa 188,27,8192\nMvaa 248,152,60\nTa 190,27,9216\nRa 4,16,2048\nMvaa 248,120,44\nv_mul_f32 v88, v88, v104\nv_mul_f32 v89, v89, v104\n" \
"Mvaa 244,116,44\nRa 20,17,2048\nRa 8,16,4096\nMvaa 244,148,60\nD 28,68,s90,2048\nMvaa 240,144,60\nRa 24,17,4096\nRa 12,16,6144\n" \
"Mvaa 240,112,44\nv_mul_f32 v90, v90, v104\nv_mul_f32 v91, v91, v104\nMvaa 236,108,44\nRa 28,17,6144\nv_mul_f32 v92, v92, v104\nMvaa 236,140,60\nv_mul_f32 v93, v93, v104\n" \
"v_mul_f32 v94, v94, v104\nMvaa 232,136,60\nD 28,68,s91,3072\nMvaa 232,104,44\nv_mul_f32 v95, v95, v104\nC 96,88,89\nMvaa 228,100,44\nC 97,90,91\n" \
"C 98,92,93\nMvaa 228,132,60\nv_add_u32 v14, s72, v14\nC 99,94,95\nMvaa 224,128,60\nv_add_u32 v28, s72, v28\nMvaa 224,96,44\ns_waitcnt lgkmcnt(6)\n" \
"Mvaa 128,64,0\ns_add_u32 s95, s50, 2048\nbuffer_store_dwordx4 v[96:99], v0, s[60:63], s95 offen sc1\nMvaa 128,160,16\ns_add_u32 m0, s88, 0\nv_mul_f32 v108, v108, v124\nMvaa 132,164,16\nD 13,64,0,0\n" \
"Mvaa 132,68,0\nv_mul_f32 v109, v109, v124\nv_mul_f32 v110, v110, v124\nMvaa 136,72,0\nv_mul_f32 v111, v111, v124\nv_mul_f32 v112, v112, v124\nMvaa 136,168,16\nv_mul_f32 v113, v113, v124\n" \
"v_mul_f32 v114, v114, v124\nMvaa 140,172,16\nv_mul_f32 v115, v115, v124\nC 116,108,109\nMvaa 140,76,0\ns_waitcnt lgkmcnt(0)\nD 13,64,s73,1024\nMvaa 144,80,0\n" \
"C 117,110,111\nC 118,112,113\nMvaa 144,176,16\nC 119,114,115\nMvaa 148,180,16\ns_add_u32 s95, s50, 4096\nbuffer_store_dwordx4 v[116:119], v0, s[60:63], s95 offen sc1\nMvaa 148,84,0\n" \
"Qvaa 88,232,204,72\nQvaa 92,236,204,76\nQvaa 108,236,208,72\nQvaa 112,240,208,76\nMvaa 152,88,0\nD 13,64,s74,2048\nMvaa 152,184,16\nMvaa 156,188,16\n" \
"Mvaa 156,92,0\nMvaa 188,92,4\ns_waitcnt vmcnt(18)\ns_barrier\nMvaa 188,188,20\nTa 96,20,32768\nTa 98,20,33792\nMvaa 184,184,20\n" \
"D 13,64,s75,3072\nMvaa 184,88,4\nTa 128,20,40960\nTa 130,20,41984\nMvaa 180,84,4\nv_max3_f32 v104, v88, v89, v90\nv_max3_f32 v105, v91, v92, v93\nMvaa 180,180,20\n" \
"Ra 32,16,32768\nRa 48,17,32768\nMvaa 176,176,20\ns_add_u32 m0, s88, 4096\nTa 100,21,32768\nMvaa 176,80,4\nD 13,64,s76,0\nMvaa 172,76,4\n" \
"Ta 102,21,33792\nTa 132,21,40960\nMvaa 172,172,20\nv_max3_f32 v104, v104, v94, v95\nv_max_f32 v104, v104, v105\nMvaa 168,168,20\nTa 134,21,41984\nTa 104,22,32768\n" \
"Mvaa 168,72,4\nTa 106,22,33792\nTa 136,22,40960\nMvaa 164,68,4\nD 13,64,s77,1024\nMvaa 164,164,20\nv_mov_b32 v105, v104\nv_max3_f32 v124, v108, v109, v110\n" \
"Mvaa 160,160,20\nTa 138,22,41984\nTa 108,23,32768\nMvaa 160,64,4\nv_max3_f32 v125, v111, v112, v113\nv_max3_f32 v124, v124, v114, v115\nMvaa 192,64,8\nTa 110,23,33792\n" \
"Ta 140,23,40960\nMvaa 192,160,24\nD 13,64,s78,2048\nMvaa 196,164,24\nTa 142,23,41984\nTa 112,24,32768\nMvaa 196,68,8\nv_max_f32 v124, v124, v125\n" \
"s_branch 99f\n.p2align 12\nzp32:\ns_setpc_b64 s[98:99]\n99:\nv_mov_b32 v125, v124\nMvaa 200,72,8\nTa 114,24,33792\n" \
"Ta 144,24,40960\nMvaa 200,168,24\nTa 146,24,41984\nTa 116,25,32768\nMvaa 204,172,24\nD 13,64,s79,3072\nMvaa 204,76,8\nv_permlane16_swap_b32 v104, v105\n" \
"v_permlane16_swap_b32 v124, v125\nMvaa 208,80,8\nTa 118,25,33792\nTa 148,25,40960\nMvaa 208,176,24\nv_add_u32 v13, 128, v13\nTa 150,25,41984\nMvaa 212,180,24\n" \
"s_add_u32 m0, s32, 65536\nTa 120,26,32768\nMvaa 212,84,8\nD 14,68,0,0\nMvaa 216,88,8\nTa 122,26,33792\nTa 152,26,40960\nMvaa 216,184,24\n" \
"v_max_f32 v104, v104, v105\nv_mov_b32 v105, v104\nMvaa 220,188,24\nTa 154,26,41984\nTa 124,27,32768\nMvaa 220,92,8\nv_max_f32 v124, v124, v125\nv_mov_b32 v125, v124\n" \
"Mvaa 252,92,12\nD 14,68,s89,1024\nMvaa 252,188,28\nTa 126,27,33792\nTa 156,27,40960\nMvaa 248,184,28\nTa 158,27,41984\nRa 36,16,34816\n" \
"Mvaa 248,88,12\nv_permlane32_swap_b32 v104, v105\nv_permlane32_swap_b32 v124, v125\nMvaa 244,84,12\nRa 52,17,34816\nRa 40,16,36864\nMvaa 244,180,28\nD 28,68,s90,2048\n" \
"Mvaa 240,176,28\nRa 56,17,36864\nRa 44,16,38912\nMvaa 240,80,12\nv_max_f32 v104, v104, v105\nv_mul_f32 v104, s93, v104\nMvaa 236,76,12\nRa 60,17,38912\n" \
"v_fma_f32 v88, v88, s93, -v104\nMvaa 236,172,28\nv_fma_f32 v89, v89, s93, -v104\nv_fma_f32 v90, v90, s93, -v104\nMvaa 232,168,28\nD 28,68,s91,3072\nMvaa 232,72,12\nv_fma_f32 v91, v91, s93, -v104\n" \
"v_fma_f32 v92, v92, s93, -v104\nMvaa 228,68,12\nv_fma_f32 v93, v93, s93, -v104\nv_fma_f32 v94, v94, s93, -v104\nMvaa 228,164,28\nv_add_u32 v14, s72, v14\nv_fma_f32 v95, v95, s93, -v104\nMvaa 224,160,28\n" \
"v_add_u32 v28, s72, v28\nMvaa 224,64,12\ns_waitcnt lgkmcnt(6)\nMvaa 128,96,32\nE 88,88\nMvaa 128,128,48\ns_add_u32 m0, s88, 32768\nMvaa 132,132,48\n" \
"D 13,64,0,0\nMvaa 132,100,32\nE 89,89\nMvaa 136,104,32\nE 90,90\nMvaa 136,136,48\nC 96,88,89\nMvaa 140,140,48\n" \
"E 91,91\nMvaa 140,108,32\ns_waitcnt lgkmcnt(0)\nD 13,64,s73,1024\nMvaa 144,112,32\nE 92,92\nMvaa 144,144,48\nC 97,90,91\n" \
"Mvaa 148,148,48\nE 93,93\nMvaa 148,116,32\nE 94,94\nMvaa 152,120,32\nD 13,64,s74,2048\nMvaa 152,152,48\nC 98,92,93\n" \
"Mvaa 156,156,48\nE 95,95\nMvaa 156,124,32\nv_max_f32 v124, v124, v125\nv_mul_f32 v124, s93, v124\nMvaa 188,124,36\ns_waitcnt vmcnt(17)\ns_barrier\n" \
"Mvaa 188,156,52\nTa 64,20,16384\nTa 66,20,17408\nMvaa 184,152,52\nD 13,64,s75,3072\nMvaa 184,120,36\nTa 160,20,24576\nTa 162,20,25600\n" \
"Mvaa 180,116,36\nv_fma_f32 v108, v108, s93, -v124\nv_fma_f32 v109, v109, s93, -v124\nMvaa 180,148,52\nRa 0,18,0\nRa 16,19,0\nMvaa 176,144,52\ns_add_u32 m0, s88, 36864\n" \
"Ta 68,21,16384\nMvaa 176,112,36\nD 13,64,s76,0\nMvaa 172,108,36\nTa 70,21,17408\nTa 164,21,24576\nMvaa 172,140,52\nv_fma_f32 v110, v110, s93, -v124\n" \
"v_fma_f32 v111, v111, s93, -v124\nMvaa 168,136,52\nTa 166,21,25600\nTa 72,22,16384\nMvaa 168,104,36\nTa 74,22,17408\nTa 168,22,24576\nMvaa 164,100,36\n" \
"D 13,64,s77,1024\nMvaa 164,132,52\nv_fma_f32 v112, v112, s93, -v124\nv_fma_f32 v113, v113, s93, -v124\nMvaa 160,128,52\nTa 170,22,25600\nTa 76,23,16384\nMvaa 160,96,36\n" \
"v_fma_f32 v114, v114, s93, -v124\nv_fma_f32 v115, v115, s93, -v124\nMvaa 192,96,40\nTa 78,23,17408\nTa 172,23,24576\nMvaa 192,128,56\nD 13,64,s78,2048\nMvaa 196,132,56\n" \
"Ta 174,23,25600\nTa 80,24,16384\nMvaa 196,100,40\nE 108,108\nMvaa 200,104,40\nTa 82,24,17408\nTa 176,24,24576\nMvaa 200,136,56\n" \
"Ta 178,24,25600\nTa 84,25,16384\nMvaa 204,140,56\nD 13,64,s79,3072\nMvaa 204,108,40\nE 109,109\nMvaa 208,112,40\nTa 86,25,17408\n" \
"Ta 180,25,24576\nMvaa 208,144,56\nv_add_u32 v13, 128, v13\nTa 182,25,25600\nMvaa 212,148,56\ns_add_u32 m0, s32, 98304\nTa 88,26,16384\nMvaa 212,116,40\n" \
"D 14,68,0,0\nMvaa 216,120,40\nTa 90,26,17408\nTa 184,26,24576\nMvaa 216,152,56\nE 110,110\nMvaa 220,156,56\nTa 186,26,25600\n" \
"Ta 92,27,16384\nMvaa 220,124,40\nC 116,108,109\nMvaa 252,124,44\nD 14,68,s89,1024\nMvaa 252,156,60\nTa 94,27,17408\nTa 188,27,24576\n" \
"Mvaa 248,152,60\nTa 190,27,25600\nRa 4,18,2048\nMvaa 248,120,44\nE 111,111\nMvaa 244,116,44\nRa 20,19,2048\nRa 8,18,4096\n" \
"Mvaa 244,148,60\nD 28,68,s90,2048\nMvaa 240,144,60\nRa 24,19,4096\nRa 12,18,6144\nMvaa 240,112,44\nE 112,112\nMvaa 236,108,44\n" \
"Ra 28,19,6144\nC 117,110,111\nMvaa 236,140,60\nE 113,113\nMvaa 232,136,60\nD 28,68,s91,3072\nMvaa 232,104,44\nE 114,114\n" \
"Mvaa 228,100,44\nC 118,112,113\nMvaa 228,132,60\nv_add_u32 v14, s72, v14\nMvaa 224,128,60\nv_add_u32 v28, s72, v28\nMvaa 224,96,44\ns_waitcnt lgkmcnt(6)\n" \
"Mvaa 128,64,0\nE 115,115\nMvaa 128,160,16\ns_add_u32 m0, s88, 131072\nC 99,94,95\nMvaa 132,164,16\nD 13,64,0,0\nMvaa 132,68,0\n" \
"C 119,114,115\nZvvv 100,84,96\nZvvv 88,38,96\nZvvv 92,56,96\nZvvv 120,84,116\nZvvv 108,40,116\nZvvv 112,58,116\nMvaa 136,72,0\n" \
"Mvaa 136,168,16\nMvaa 140,172,16\nv_rcp_f32 v104, v100\nMvaa 140,76,0\ns_waitcnt lgkmcnt(0)\nD 13,64,s73,1024\nMvaa 144,80,0\nv_rcp_f32 v124, v120\n" \
"Mvaa 144,176,16\nv_mul_f32 v88, v88, v104\nv_mul_f32 v89, v89, v104\nMvaa 148,180,16\nv_mul_f32 v90, v90, v104\nv_mul_f32 v91, v91, v104\nMvaa 148,84,0\nv_mul_f32 v92, v92, v104\n" \
"v_mul_f32 v93, v93, v104\nMvaa 152,88,0\nD 13,64,s74,2048\nMvaa 152,184,16\nv_mul_f32 v94, v94, v104\nv_mul_f32 v95, v95, v104\nMvaa 156,188,16\nC 96,88,89\n" \
"C 97,90,91\nMvaa 156,92,0\nC 98,92,93\nC 99,94,95\nMvaa 188,92,4\ns_waitcnt vmcnt(15)\ns_barrier\nMvaa 188,188,20\n" \
"Ta 96,20,0\nTa 98,20,1024\nMvaa 184,184,20\nD 13,64,s75,3072\nMvaa 184,88,4\nTa 128,20,8192\nTa 130,20,9216\nMvaa 180,84,4\n" \
"s_add_u32 s95, s50, 6144\nbuffer_store_dwordx4 v[96:99], v0, s[60:63], s95 offen sc1\nMvaa 180,180,20\nRa 32,16,0\nRa 48,17,0\nMvaa 176,176,20\ns_add_u32 m0, s88, 135168\nTa 100,21,0\n" \
"Mvaa 176,80,4\nD 13,64,s76,0\nMvaa 172,76,4\nTa 102,21,1024\nTa 132,21,8192\nMvaa 172,172,20\nv_mul_f32 v108, v108, v124\nv_mul_f32 v109, v109, v124\n" \
"Mvaa 168,168,20\nTa 134,21,9216\nTa 104,22,0\nMvaa 168,72,4\nTa 106,22,1024\nTa 136,22,8192\nMvaa 164,68,4\nD 13,64,s77,1024\n" \
"Mvaa 164,164,20\nv_mul_f32 v110, v110, v124\nv_mul_f32 v111, v111, v124\nMvaa 160,160,20\nTa 138,22,9216\nTa 108,23,0\nMvaa 160,64,4\nv_mul_f32 v112, v112, v124\n" \
"v_mul_f32 v113, v113, v124\nMvaa 192,64,8\nTa 110,23,1024\nTa 140,23,8192\nMvaa 192,160,24\nD 13,64,s78,2048\nMvaa 196,164,24\nTa 142,23,9216\n" \
"Ta 112,24,0\nMvaa 196,68,8\nv_mul_f32 v114, v114, v124\nv_mul_f32 v115, v115, v124\nMvaa 200,72,8\nTa 114,24,1024\nTa 144,24,8192\nMvaa 200,168,24\n" \
"Ta 146,24,9216\nTa 116,25,0\nMvaa 204,172,24\nD 13,64,s79,3072\nMvaa 204,76,8\nC 116,108,109\nC 117,110,111\nMvaa 208,80,8\n" \
"Ta 118,25,1024\nTa 148,25,8192\nMvaa 208,176,24\nv_add_u32 v13, 128, v13\nTa 150,25,9216\nMvaa 212,180,24\ns_add_u32 m0, s32, 81920\nTa 120,26,0\n" \
"Mvaa 212,84,8\nD 14,68,0,0\nMvaa 216,88,8\nTa 122,26,1024\nTa 152,26,8192\nMvaa 216,184,24\nC 118,112,113\nC 119,114,115\n" \
"Mvaa 220,188,24\nTa 154,26,9216\nTa 124,27,0\nMvaa 220,92,8\ns_add_u32 s95, s50, 8192\nbuffer_store_dwordx4 v[116:119], v0, s[60:63], s95 offen sc1\nMvaa 252,92,12\nD 14,68,s89,1024\n" \
"Qvaa 88,240,212,72\nQvaa 92,244,212,76\nQvaa 108,244,216,72\nQvaa 112,248,216,76\nMvaa 252,188,28\nTa 126,27,1024\nTa 156,27,8192\nMvaa 248,184,28\n" \
"Ta 158,27,9216\nRa 36,16,2048\nMvaa 248,88,12\nv_max3_f32 v104, v88, v89, v90\nv_max3_f32 v105, v91, v92, v93\nMvaa 244,84,12\nRa 52,17,2048\nRa 40,16,4096\n" \
"Mvaa 244,180,28\nD 28,68,s90,2048\nMvaa 240,176,28\nRa 56,17,4096\nRa 44,16,6144\nMvaa 240,80,12\nv_max3_f32 v104, v104, v94, v95\nv_max_f32 v104, v104, v105\n" \
"Mvaa 236,76,12\nRa 60,17,6144\nv_mov_b32 v105, v104\nMvaa 236,172,28\nv_max3_f32 v124, v108, v109, v110\nv_max3_f32 v125, v111, v112, v113\nMvaa 232,168,28\nD 28,68,s91,3072\n" \
"Mvaa 232,72,12\nv_max3_f32 v124, v124, v114, v115\nv_max_f32 v124, v124, v125\nMvaa 228,68,12\nv_mov_b32 v125, v124\nv_permlane16_swap_b32 v104, v105\nMvaa 228,164,28\nv_add_u32 v14, s72, v14\n" \
"v_permlane16_swap_b32 v124, v125\nMvaa 224,160,28\nv_add_u32 v28, s72, v28\nv_max_f32 v104, v104, v105\nMvaa 224,64,12\ns_waitcnt lgkmcnt(6)\nv_mov_b32 v105, v104\nMvaa 128,96,32\n" \
"v_max_f32 v124, v124, v125\nv_mov_b32 v125, v124\nMvaa 128,128,48\ns_add_u32 m0, s88, 0\nv_permlane32_swap_b32 v104, v105\nMvaa 132,132,48\nD 13,64,0,0\nMvaa 132,100,32\n" \
"v_permlane32_swap_b32 v124, v125\nv_max_f32 v104, v104, v105\nMvaa 136,104,32\nbuffer_load_dwordx2 v[10:11], off, s[60:63], s49 sc1\nMvaa 136,136,48\nv_mul_f32 v104, s93, v104\nv_fma_f32 v88, v88, s93, -v104\nMvaa 140,140,48\n" \
"v_fma_f32 v89, v89, s93, -v104\nv_fma_f32 v90, v90, s93, -v104\nMvaa 140,108,32\ns_waitcnt lgkmcnt(0)\nD 13,64,s73,1024\nMvaa 144,112,32\nv_fma_f32 v91, v91, s93, -v104\nv_fma_f32 v92, v92, s93, -v104\n" \
"Mvaa 144,144,48\nv_fma_f32 v93, v93, s93, -v104\nv_fma_f32 v94, v94, s93, -v104\nMvaa 148,148,48\nv_fma_f32 v95, v95, s93, -v104\nMvaa 148,116,32\nE 88,88\nMvaa 152,120,32\n" \
"D 13,64,s74,2048\nMvaa 152,152,48\nE 89,89\nMvaa 156,156,48\nE 90,90\nMvaa 156,124,32\nC 96,88,89\nMvaa 188,124,36\n" \
"s_waitcnt vmcnt(18)\ns_barrier\nMvaa 188,156,52\nTa 64,20,32768\nTa 66,20,33792\nMvaa 184,152,52\nD 13,64,s75,3072\nMvaa 184,120,36\n" \
"Ta 160,20,40960\nTa 162,20,41984\nMvaa 180,116,36\nE 91,91\nMvaa 180,148,52\nRa 0,16,32768\nRa 16,17,32768\nMvaa 176,144,52\n" \
"s_add_u32 m0, s88, 4096\nTa 68,21,32768\nMvaa 176,112,36\nD 13,64,s76,0\nMvaa 172,108,36\nTa 70,21,33792\nTa 164,21,40960\nMvaa 172,140,52\n" \
"E 92,92\nMvaa 168,136,52\nTa 166,21,41984\nTa 72,22,32768\nMvaa 168,104,36\nTa 74,22,33792\nTa 168,22,40960\nMvaa 164,100,36\n" \
"D 13,64,s77,1024\nMvaa 164,132,52\nC 97,90,91\nMvaa 160,128,52\nTa 170,22,41984\nTa 76,23,32768\nMvaa 160,96,36\nE 93,93\n" \
"Mvaa 192,96,40\nTa 78,23,33792\nTa 172,23,40960\nMvaa 192,128,56\nD 13,64,s78,2048\nMvaa 196,132,56\nTa 174,23,41984\nTa 80,24,32768\n" \
"Mvaa 196,100,40\nE 94,94\nMvaa 200,104,40\nTa 82,24,33792\nTa 176,24,40960\nMvaa 200,136,56\nTa 178,24,41984\nTa 84,25,32768\n" \
"Mvaa 204,140,56\nD 13,64,s79,3072\nMvaa 204,108,40\nC 98,92,93\nMvaa 208,112,40\nTa 86,25,33792\nTa 180,25,40960\nMvaa 208,144,56\n" \
"v_add_u32 v13, 128, v13\nTa 182,25,41984\nMvaa 212,148,56\ns_add_u32 m0, s32, 65536\nTa 88,26,32768\nMvaa 212,116,40\nD 14,68,0,0\nMvaa 216,120,40\n" \
"Ta 90,26,33792\nTa 184,26,40960\nMvaa 216,152,56\nE 95,95\nMvaa 220,156,56\nTa 186,26,41984\nTa 92,27,32768\nMvaa 220,124,40\n" \
"v_max_f32 v124, v124, v125\nv_mul_f32 v124, s93, v124\nMvaa 252,124,44\nD 14,68,s89,1024\nMvaa 252,156,60\nTa 94,27,33792\nTa 188,27,40960\nMvaa 248,152,60\n" \
"Ta 190,27,41984\nRa 4,16,34816\nMvaa 248,120,44\nv_fma_f32 v108, v108, s93, -v124\nv_fma_f32 v109, v109, s93, -v124\nMvaa 244,116,44\nRa 20,17,34816\nRa 8,16,36864\n" \
"Mvaa 244,148,60\ns_branch 99f\n.p2align 12\nzp33:\ns_setpc_b64 s[98:99]\n99:\nD 28,68,s90,2048\nMvaa 240,144,60\n" \
"Ra 24,17,36864\nRa 12,16,38912\nMvaa 240,112,44\nv_fma_f32 v110, v110, s93, -v124\nv_fma_f32 v111, v111, s93, -v124\nMvaa 236,108,44\nRa 28,17,38912\nv_fma_f32 v112, v112, s93, -v124\n" \
"Mvaa 236,140,60\nv_fma_f32 v113, v113, s93, -v124\nv_fma_f32 v114, v114, s93, -v124\nMvaa 232,136,60\nD 28,68,s91,3072\nMvaa 232,104,44\nv_fma_f32 v115, v115, s93, -v124\nMvaa 228,100,44\n" \
"E 108,108\nMvaa 228,132,60\nv_add_u32 v14, s72, v14\nMvaa 224,128,60\nv_add_u32 v28, s72, v28\nMvaa 224,96,44\ns_waitcnt lgkmcnt(6)\nMvaa 128,64,0\n" \
"E 109,109\nMvaa 128,160,16\ns_add_u32 m0, s88, 32768\nMvaa 132,164,16\nD 13,64,0,0\nMvaa 132,68,0\nE 110,110\nMvaa 136,72,0\n" \
"s_waitcnt vmcnt(12)\nv_cmp_ne_u64 vcc, v[10:11], v[2:3]\ns_cbranch_vccnz zs103\nzr103:\nMvaa 136,168,16\nbuffer_load_dwordx4 v[68:71], v0, s[60:63], s47 offen sc1\nMvaa 140,172,16\nbuffer_load_dwordx2 v[32:33], v0, s[60:63], s47 offen offset:1024 sc1\n" \
"Mvaa 140,76,0\ns_waitcnt lgkmcnt(0)\nD 13,64,s73,1024\nbuffer_load_dwordx2 v[50:51], v0, s[60:63], s47 offen offset:1032 sc1\nMvaa 144,80,0\nC 116,108,109\nMvaa 144,176,16\nE 111,111\n" \
"Mvaa 148,180,16\nE 112,112\nMvaa 148,84,0\nC 117,110,111\nMvaa 152,88,0\nD 13,64,s74,2048\nMvaa 152,184,16\nE 113,113\n" \
"Mvaa 156,188,16\nE 114,114\nMvaa 156,92,0\nC 118,112,113\nMvaa 188,92,4\ns_waitcnt vmcnt(19)\ns_barrier\nMvaa 188,188,20\n" \
"Ta 96,20,16384\nTa 98,20,17408\nMvaa 184,184,20\nD 13,64,s75,3072\nMvaa 184,88,4\nTa 128,20,24576\nTa 130,20,25600\nMvaa 180,84,4\n" \
"E 115,115\nMvaa 180,180,20\nRa 32,18,0\nRa 48,19,0\nMvaa 176,176,20\ns_add_u32 m0, s88, 36864\nTa 100,21,16384\nMvaa 176,80,4\n" \
"D 13,64,s76,0\nMvaa 172,76,4\nTa 102,21,17408\nTa 132,21,24576\nMvaa 172,172,20\nC 99,94,95\nC 119,114,115\nMvaa 168,168,20\n" \
"Ta 134,21,25600\nTa 104,22,16384\nZvvv 100,84,96\nZvvv 88,42,96\nZvvv 92,60,96\nZvvv 120,84,116\nZvvv 108,44,116\nZvvv 112,62,116\n" \
"Mvaa 168,72,4\nTa 106,22,17408\nTa 136,22,24576\nMvaa 164,68,4\nD 13,64,s77,1024\nMvaa 164,164,20\nv_rcp_f32 v104, v100\nMvaa 160,160,20\n" \
"Ta 138,22,25600\nTa 108,23,16384\nMvaa 160,64,4\nv_rcp_f32 v124, v120\nMvaa 192,64,8\nTa 110,23,17408\nTa 140,23,24576\nMvaa 192,160,24\n" \
"D 13,64,s78,2048\nMvaa 196,164,24\nTa 142,23,25600\nTa 112,24,16384\nMvaa 196,68,8\nv_mul_f32 v88, v88, v104\nv_mul_f32 v89, v89, v104\nMvaa 200,72,8\n" \
"Ta 114,24,17408\nTa 144,24,24576\nMvaa 200,168,24\nTa 146,24,25600\nTa 116,25,16384\nMvaa 204,172,24\nD 13,64,s79,3072\nMvaa 204,76,8\n" \
"v_mul_f32 v90, v90, v104\nv_mul_f32 v91, v91, v104\nMvaa 208,80,8\nTa 118,25,17408\nTa 148,25,24576\nMvaa 208,176,24\nv_add_u32 v13, 128, v13\nTa 150,25,25600\n" \
"Mvaa 212,180,24\ns_add_u32 m0, s32, 98304\nTa 120,26,16384\nMvaa 212,84,8\nD 14,68,0,0\nMvaa 216,88,8\nTa 122,26,17408\nTa 152,26,24576\n" \
"Mvaa 216,184,24\nv_mul_f32 v92, v92, v104\nv_mul_f32 v93, v93, v104\nMvaa 220,188,24\nTa 154,26,25600\nTa 124,27,16384\nMvaa 220,92,8\nv_mul_f32 v94, v94, v104\n" \
"v_mul_f32 v95, v95, v104\nMvaa 252,92,12\nD 14,68,s89,1024\nMvaa 252,188,28\nTa 126,27,17408\nTa 156,27,24576\nMvaa 248,184,28\nTa 158,27,25600\n" \
"Ra 36,18,2048\nMvaa 248,88,12\nC 96,88,89\nC 97,90,91\nMvaa 244,84,12\nRa 52,19,2048\nRa 40,18,4096\nMvaa 244,180,28\n" \
"D 28,68,s90,2048\nMvaa 240,176,28\nRa 56,19,4096\nRa 44,18,6144\nMvaa 240,80,12\nC 98,92,93\nC 99,94,95\nMvaa 236,76,12\n" \
"Ra 60,19,6144\nMvaa 236,172,28\ns_add_u32 s95, s50, 10240\nbuffer_store_dwordx4 v[96:99], v0, s[60:63], s95 offen sc1\nMvaa 232,168,28\nD 28,68,s91,3072\nMvaa 232,72,12\nv_mul_f32 v108, v108, v124\n" \
"v_mul_f32 v109, v109, v124\nMvaa 228,68,12\nv_mul_f32 v110, v110, v124\nv_mul_f32 v111, v111, v124\nMvaa 228,164,28\nv_add_u32 v14, s72, v14\nv_mul_f32 v112, v112, v124\nMvaa 224,160,28\n" \
"v_add_u32 v28, s72, v28\nv_mul_f32 v113, v113, v124\nMvaa 224,64,12\ns_waitcnt lgkmcnt(6)\nv_mul_f32 v114, v114, v124\nMvaa 128,96,32\nv_mul_f32 v115, v115, v124\nC 116,108,109\n" \
"Mvaa 128,128,48\ns_add_u32 m0, s88, 131072\nC 117,110,111\nMvaa 132,132,48\nD 13,64,0,0\nMvaa 132,100,32\nC 118,112,113\nC 119,114,115\n" \
"Mvaa 136,104,32\ns_add_u32 s95, s50, 12288\nbuffer_store_dwordx4 v[116:119], v0, s[60:63], s95 offen sc1\nMvaa 136,136,48\nMvaa 140,140,48\nMvaa 140,108,32\ns_waitcnt lgkmcnt(0)\nD 13,64,s73,1024\n" \
"Mvaa 144,112,32\nMvaa 144,144,48\nMvaa 148,148,48\nMvaa 148,116,32\nMvaa 152,120,32\nD 13,64,s74,2048\nMvaa 152,152,48\nMvaa 156,156,48\n" \
"Mvaa 156,124,32\nMvaa 188,124,36\ns_waitcnt vmcnt(20)\ns_barrier\nMvaa 188,156,52\nTa 64,20,0\nTa 66,20,1024\nMvaa 184,152,52\n" \
"D 13,64,s75,3072\nMvaa 184,120,36\nTa 160,20,8192\nTa 162,20,9216\nMvaa 180,116,36\ns_waitcnt vmcnt(16)\nQvaa 88,248,220,72\nQvaa 92,252,220,76\n" \
"Qvva 108,68,192,80\nQvaa 112,224,192,76\nMvaa 180,148,52\nRa 0,16,0\nRa 16,17,0\nMvaa 176,144,52\ns_add_u32 m0, s88, 135168\nTa 68,21,0\n" \
"Mvaa 176,112,36\nD 13,64,s76,0\nMvaa 172,108,36\nTa 70,21,1024\nTa 164,21,8192\nMvaa 172,140,52\nv_max3_f32 v104, v88, v89, v90\nv_max3_f32 v105, v91, v92, v93\n" \
"Mvaa 168,136,52\nTa 166,21,9216\nTa 72,22,0\nMvaa 168,104,36\nTa 74,22,1024\nTa 168,22,8192\nMvaa 164,100,36\nD 13,64,s77,1024\n" \
"Mvaa 164,132,52\nv_max3_f32 v104, v104, v94, v95\nv_max_f32 v104, v104, v105\nMvaa 160,128,52\nTa 170,22,9216\nTa 76,23,0\nMvaa 160,96,36\nv_mov_b32 v105, v104\n" \
"v_max3_f32 v124, v108, v109, v110\nMvaa 192,96,40\nTa 78,23,1024\nTa 172,23,8192\nMvaa 192,128,56\nD 13,64,s78,2048\nMvaa 196,132,56\nTa 174,23,9216\n" \
"Ta 80,24,0\nMvaa 196,100,40\nv_max3_f32 v125, v111, v112, v113\nv_max3_f32 v124, v124, v114, v115\nMvaa 200,104,40\nTa 82,24,1024\nTa 176,24,8192\nMvaa 200,136,56\n" \
"Ta 178,24,9216\nTa 84,25,0\nMvaa 204,140,56\nD 13,64,s79,3072\nMvaa 204,108,40\nv_max_f32 v124, v124, v125\nv_mov_b32 v125, v124\nMvaa 208,112,40\n" \
"Ta 86,25,1024\nTa 180,25,8192\nMvaa 208,144,56\nv_add_u32 v13, 128, v13\nTa 182,25,9216\nMvaa 212,148,56\ns_add_u32 m0, s32, 81920\nTa 88,26,0\n" \
"Mvaa 212,116,40\nD 14,68,0,0\nMvaa 216,120,40\nTa 90,26,1024\nTa 184,26,8192\nMvaa 216,152,56\nv_permlane16_swap_b32 v104, v105\nv_permlane16_swap_b32 v124, v125\n" \
"Mvaa 220,156,56\nTa 186,26,9216\nTa 92,27,0\nMvaa 220,124,40\nv_max_f32 v104, v104, v105\nv_mov_b32 v105, v104\nMvaa 252,124,44\nD 14,68,s89,1024\n" \
"Mvaa 252,156,60\nTa 94,27,1024\nTa 188,27,8192\nMvaa 248,152,60\nTa 190,27,9216\nRa 4,16,2048\nMvaa 248,120,44\nv_max_f32 v124, v124, v125\n" \
"v_mov_b32 v125, v124\nMvaa 244,116,44\nRa 20,17,2048\nRa 8,16,4096\nMvaa 244,148,60\nD 28,68,s90,2048\nMvaa 240,144,60\nRa 24,17,4096\n" \
"Ra 12,16,6144\nMvaa 240,112,44\nv_permlane32_swap_b32 v104, v105\nv_permlane32_swap_b32 v124, v125\nMvaa 236,108,44\nRa 28,17,6144\nv_max_f32 v104, v104, v105\nMvaa 236,140,60\n" \
"v_mul_f32 v104, s93, v104\nv_fma_f32 v88, v88, s93, -v104\nMvaa 232,136,60\nD 28,68,s91,3072\nMvaa 232,104,44\nv_fma_f32 v89, v89, s93, -v104\nv_fma_f32 v90, v90, s93, -v104\nMvaa 228,100,44\n" \
"v_fma_f32 v91, v91, s93, -v104\nv_fma_f32 v92, v92, s93, -v104\nMvaa 228,132,60\nv_add_u32 v14, s72, v14\nv_fma_f32 v93, v93, s93, -v104\nMvaa 224,128,60\nv_add_u32 v28, s72, v28\nv_fma_f32 v94, v94, s93, -v104\n" \
"Mvaa 224,96,44\ns_waitcnt lgkmcnt(6)\nv_fma_f32 v95, v95, s93, -v104\nMvaa 128,64,0\nE 88,88\nMvaa 128,160,16\ns_add_u32 m0, s88, 0\nMvaa 132,164,16\n" \
"D 13,64,0,0\nMvaa 132,68,0\nE 89,89\nMvaa 136,72,0\nE 90,90\nMvaa 136,168,16\nC 96,88,89\nMvaa 140,172,16\n" \
"E 91,91\nMvaa 140,76,0\ns_waitcnt lgkmcnt(0)\nD 13,64,s73,1024\nMvaa 144,80,0\nE 92,92\nMvaa 144,176,16\nC 97,90,91\n" \
"Mvaa 148,180,16\nE 93,93\nMvaa 148,84,0\nE 94,94\nMvaa 152,88,0\nD 13,64,s74,2048\nMvaa 152,184,16\nC 98,92,93\n" \
"Mvaa 156,188,16\nE 95,95\nMvaa 156,92,0\nv_max_f32 v124, v124, v125\nv_mul_f32 v124, s93, v124\nMvaa 188,92,4\ns_waitcnt vmcnt(16)\ns_barrier\n" \
"Mvaa 188,188,20\nTa 96,20,32768\nTa 98,20,33792\nMvaa 184,184,20\nD 13,64,s75,3072\nMvaa 184,88,4\nTa 128,20,40960\nTa 130,20,41984\n" \
"Mvaa 180,84,4\nv_fma_f32 v108, v108, s93, -v124\nv_fma_f32 v109, v109, s93, -v124\nMvaa 180,180,20\nRa 32,16,32768\nRa 48,17,32768\nMvaa 176,176,20\ns_add_u32 m0, s88, 4096\n" \
"Ta 100,21,32768\nMvaa 176,80,4\nD 13,64,s76,0\nMvaa 172,76,4\nTa 102,21,33792\nTa 132,21,40960\nMvaa 172,172,20\nv_fma_f32 v110, v110, s93, -v124\n" \
"v_fma_f32 v111, v111, s93, -v124\nMvaa 168,168,20\nTa 134,21,41984\nTa 104,22,32768\nMvaa 168,72,4\nTa 106,22,33792\nTa 136,22,40960\nMvaa 164,68,4\n" \
"D 13,64,s77,1024\nMvaa 164,164,20\nv_fma_f32 v112, v112, s93, -v124\nv_fma_f32 v113, v113, s93, -v124\nMvaa 160,160,20\nTa 138,22,41984\nTa 108,23,32768\nMvaa 160,64,4\n" \
"v_fma_f32 v114, v114, s93, -v124\nv_fma_f32 v115, v115, s93, -v124\nMvaa 192,64,8\nTa 110,23,33792\nTa 140,23,40960\nMvaa 192,160,24\nD 13,64,s78,2048\nMvaa 196,164,24\n" \
"Ta 142,23,41984\nTa 112,24,32768\nMvaa 196,68,8\nE 108,108\nMvaa 200,72,8\nTa 114,24,33792\nTa 144,24,40960\nMvaa 200,168,24\n" \
"Ta 146,24,41984\nTa 116,25,32768\nMvaa 204,172,24\nD 13,64,s79,3072\nMvaa 204,76,8\nE 109,109\nMvaa 208,80,8\nTa 118,25,33792\n" \
"Ta 148,25,40960\nMvaa 208,176,24\nv_add_u32 v13, 128, v13\nTa 150,25,41984\nMvaa 212,180,24\ns_add_u32 m0, s32, 65536\nTa 120,26,32768\nMvaa 212,84,8\n" \
"D 14,68,0,0\nMvaa 216,88,8\nTa 122,26,33792\nTa 152,26,40960\nMvaa 216,184,24\nE 110,110\nMvaa 220,188,24\nTa 154,26,41984\n" \
"Ta 124,27,32768\nMvaa 220,92,8\nC 116,108,109\nMvaa 252,92,12\nD 14,68,s89,1024\nMvaa 252,188,28\nTa 126,27,33792\nTa 156,27,40960\n" \
"Mvaa 248,184,28\nTa 158,27,41984\nRa 36,16,34816\nMvaa 248,88,12\nE 111,111\nMvaa 244,84,12\nRa 52,17,34816\nRa 40,16,36864\n" \
"Mvaa 244,180,28\nD 28,68,s90,2048\nMvaa 240,176,28\nRa 56,17,36864\nRa 44,16,38912\nMvaa 240,80,12\nE 112,112\nMvaa 236,76,12\n" \
"Ra 60,17,38912\nC 117,110,111\nMvaa 236,172,28\nE 113,113\nMvaa 232,168,28\nD 28,68,s91,3072\nMvaa 232,72,12\nE 114,114\n" \
"Mvaa 228,68,12\nC 118,112,113\nMvaa 228,164,28\nv_add_u32 v14, s72, v14\nMvaa 224,160,28\nv_add_u32 v28, s72, v28\nMvaa 224,64,12\ns_waitcnt lgkmcnt(6)\n" \
"Mvaa 128,96,32\nE 115,115\nMvaa 128,128,48\ns_add_u32 m0, s88, 32768\nC 99,94,95\nMvaa 132,132,48\nD 13,64,0,0\nMvaa 132,100,32\n" \
"C 119,114,115\nZvvv 100,84,96\nZvvv 88,46,96\nZvvv 92,64,96\nZvvv 120,84,116\nZvvv 108,32,116\nZvvv 112,50,116\nMvaa 136,104,32\n" \
"Mvaa 136,136,48\nMvaa 140,140,48\nv_rcp_f32 v104, v100\nMvaa 140,108,32\ns_waitcnt lgkmcnt(0)\nD 13,64,s73,1024\nMvaa 144,112,32\nv_rcp_f32 v124, v120\n" \
"Mvaa 144,144,48\nv_mul_f32 v88, v88, v104\nv_mul_f32 v89, v89, v104\nMvaa 148,148,48\nv_mul_f32 v90, v90, v104\nv_mul_f32 v91, v91, v104\nMvaa 148,116,32\nv_mul_f32 v92, v92, v104\n" \
"v_mul_f32 v93, v93, v104\nMvaa 152,120,32\nD 13,64,s74,2048\nMvaa 152,152,48\nv_mul_f32 v94, v94, v104\ns_branch 99f\n.p2align 12\nzp34:\n" \
"s_setpc_b64 s[98:99]\n99:\nv_mul_f32 v95, v95, v104\nMvaa 156,156,48\nC 96,88,89\nC 97,90,91\nMvaa 156,124,32\nC 98,92,93\n" \
"C 99,94,95\nMvaa 188,124,36\ns_waitcnt vmcnt(15)\ns_barrier\nMvaa 188,156,52\nTa 64,20,16384\nTa 66,20,17408\nMvaa 184,152,52\n" \
"D 13,64,s75,3072\nMvaa 184,120,36\nTa 160,20,24576\nTa 162,20,25600\nMvaa 180,116,36\ns_add_u32 s95, s50, 14336\nbuffer_store_dwordx4 v[96:99], v0, s[60:63], s95 offen sc1\nMvaa 180,148,52\n" \
"Ra 0,18,0\nRa 16,19,0\nMvaa 176,144,52\ns_add_u32 m0, s88, 36864\nTa 68,21,16384\nMvaa 176,112,36\nD 13,64,s76,0\nMvaa 172,108,36\n" \
"Ta 70,21,17408\nTa 164,21,24576\nMvaa 172,140,52\nv_mul_f32 v108, v108, v124\nv_mul_f32 v109, v109, v124\nMvaa 168,136,52\nTa 166,21,25600\nTa 72,22,16384\n" \
"Mvaa 168,104,36\nTa 74,22,17408\nTa 168,22,24576\nMvaa 164,100,36\nD 13,64,s77,1024\nMvaa 164,132,52\nv_mul_f32 v110, v110, v124\nv_mul_f32 v111, v111, v124\n" \
"Mvaa 160,128,52\nTa 170,22,25600\nTa 76,23,16384\nMvaa 160,96,36\nv_mul_f32 v112, v112, v124\nv_mul_f32 v113, v113, v124\nMvaa 192,96,40\nTa 78,23,17408\n" \
"Ta 172,23,24576\nMvaa 192,128,56\nD 13,64,s78,2048\nMvaa 196,132,56\nTa 174,23,25600\nTa 80,24,16384\nMvaa 196,100,40\nv_mul_f32 v114, v114, v124\n" \
"v_mul_f32 v115, v115, v124\nMvaa 200,104,40\nTa 82,24,17408\nTa 176,24,24576\nMvaa 200,136,56\nTa 178,24,25600\nTa 84,25,16384\nMvaa 204,140,56\n" \
"D 13,64,s79,3072\nMvaa 204,108,40\nC 116,108,109\nC 117,110,111\nMvaa 208,112,40\nTa 86,25,17408\nTa 180,25,24576\nMvaa 208,144,56\n" \
"v_add_u32 v13, 128, v13\nTa 182,25,25600\nMvaa 212,148,56\ns_add_u32 m0, s32, 98304\nTa 88,26,16384\nMvaa 212,116,40\nD 14,68,0,0\nMvaa 216,120,40\n" \
"Ta 90,26,17408\nTa 184,26,24576\nMvaa 216,152,56\nC 118,112,113\nC 119,114,115\nMvaa 220,156,56\nTa 186,26,25600\nTa 92,27,16384\n" \
"Mvaa 220,124,40\ns_add_u32 s95, s50, 0\nbuffer_store_dwordx4 v[116:119], v0, s[60:63], s95 offen sc1\nMvaa 252,124,44\nD 14,68,s89,1024\nMvaa 252,156,60\nTa 94,27,17408\nTa 188,27,24576\n" \
"Mvaa 248,152,60\nTa 190,27,25600\nRa 4,18,2048\nMvaa 248,120,44\nMvaa 244,116,44\nRa 20,19,2048\nRa 8,18,4096\nMvaa 244,148,60\n" \
"D 28,68,s90,2048\nMvaa 240,144,60\nRa 24,19,4096\nRa 12,18,6144\nMvaa 240,112,44\nMvaa 236,108,44\nRa 28,19,6144\nMvaa 236,140,60\n" \
"Mvaa 232,136,60\nD 28,68,s91,3072\nMvaa 232,104,44\nMvaa 228,100,44\nMvaa 228,132,60\nv_add_u32 v14, s72, v14\nMvaa 224,128,60\nv_add_u32 v28, s72, v28\n" \
"Mvaa 224,96,44\ns_waitcnt lgkmcnt(6)\nMvaa 128,64,0\nMvaa 128,160,16\ns_add_u32 m0, s88, 131072\nMvaa 132,164,16\nD 13,64,0,0\nMvaa 132,68,0\n" \
"Mvaa 136,72,0\nMvaa 136,168,16\nMvaa 140,172,16\nMvaa 140,76,0\ns_waitcnt lgkmcnt(0)\nD 13,64,s73,1024\nMvaa 144,80,0\nMvaa 144,176,16\n" \
"Mvaa 148,180,16\nMvaa 148,84,0\nMvaa 152,88,0\nD 13,64,s74,2048\nMvaa 152,184,16\nMvaa 156,188,16\nMvaa 156,92,0\nMvaa 188,92,4\n" \
"s_waitcnt vmcnt(17)\ns_barrier\nMvaa 188,188,20\nTa 96,20,0\nTa 98,20,1024\nMvaa 184,184,20\nD 13,64,s75,3072\nMvaa 184,88,4\n" \
"Ta 128,20,8192\nTa 130,20,9216\nMvaa 180,84,4\nMvaa 180,180,20\nRa 32,16,0\nRa 48,17,0\nMvaa 176,176,20\ns_add_u32 m0, s88, 135168\n" \
"Ta 100,21,0\nMvaa 176,80,4\nD 13,64,s76,0\nMvaa 172,76,4\nTa 102,21,1024\nTa 132,21,8192\nMvaa 172,172,20\nMvaa 168,168,20\n" \
"Ta 134,21,9216\nTa 104,22,0\nMvaa 168,72,4\nTa 106,22,1024\nTa 136,22,8192\nMvaa 164,68,4\nD 13,64,s77,1024\nMvaa 164,164,20\n" \
"Mvaa 160,160,20\nTa 138,22,9216\nTa 108,23,0\nMvaa 160,64,4\nMvaa 192,64,8\nTa 110,23,1024\nTa 140,23,8192\nMvaa 192,160,24\n" \
"D 13,64,s78,2048\nMvaa 196,164,24\nTa 142,23,9216\nTa 112,24,0\nMvaa 196,68,8\nMvaa 200,72,8\nTa 114,24,1024\nTa 144,24,8192\n" \
"Mvaa 200,168,24\nTa 146,24,9216\nTa 116,25,0\nMvaa 204,172,24\nD 13,64,s79,3072\nMvaa 204,76,8\nMvaa 208,80,8\nTa 118,25,1024\n" \
"Ta 148,25,8192\nMvaa 208,176,24\nv_add_u32 v13, 128, v13\nTa 150,25,9216\nMvaa 212,180,24\ns_add_u32 m0, s32, 81920\nTa 120,26,0\nMvaa 212,84,8\n" \
"D 14,68,0,0\nMvaa 216,88,8\nTa 122,26,1024\nTa 152,26,8192\nMvaa 216,184,24\nMvaa 220,188,24\nTa 154,26,9216\nTa 124,27,0\n" \
"Mvaa 220,92,8\nMvaa 252,92,12\nD 14,68,s89,1024\nMvaa 252,188,28\nTa 126,27,1024\nTa 156,27,8192\nMvaa 248,184,28\nTa 158,27,9216\n" \
"Ra 36,16,2048\nMvaa 248,88,12\nMvaa 244,84,12\nRa 52,17,2048\nRa 40,16,4096\nMvaa 244,180,28\nD 28,68,s90,2048\nMvaa 240,176,28\n" \
"Ra 56,17,4096\nRa 44,16,6144\nMvaa 240,80,12\nMvaa 236,76,12\nRa 60,17,6144\nMvaa 236,172,28\nMvaa 232,168,28\nD 28,68,s91,3072\n" \
"Mvaa 232,72,12\nMvaa 228,68,12\nMvaa 228,164,28\nv_add_u32 v14, s72, v14\nMvaa 224,160,28\nv_add_u32 v28, s72, v28\nMvaa 224,64,12\ns_waitcnt lgkmcnt(6)\n" \
"Mvaa 128,96,32\nMvaa 128,128,48\nbuffer_load_dwordx2 v[10:11], v1, s[60:63], 0 offen offset:0 sc1\nMvaa 132,132,48\nv_subrev_u32 v9, s32, v0\nv_add_u32 v8, 0x1004200, v0\nMvaa 132,100,32\ns_add_u32 s77, s38, s32\n" \
"v_mov_b32 v12, 0xc2200000\nMvaa 136,104,32\nv_mov_b32 v13, 0xc2200000\nv_mov_b32 v14, 0xc2200000\nMvaa 136,136,48\nv_mov_b32 v15, 0xc2200000\nv_bfe_u32 v29, v0, 4, 4\nMvaa 140,140,48\n" \
"v_lshrrev_b32 v28, 8, v0\ns_lshr_b32 s96, s32, 8\nMvaa 140,108,32\ns_waitcnt lgkmcnt(0)\nv_lshlrev_b32 v6, 2, v28\nMvaa 144,112,32\nv_add_u32 v30, s96, v29\nv_sub_u32 v30, v30, v6\n" \
"Mvaa 144,144,48\nv_mov_b32 v6, 0xff800000\nv_cmp_gt_i32 s[90:91], 0, v30\nMvaa 148,148,48\nv_cmp_gt_i32 s[92:93], 1, v30\nv_cmp_gt_i32 s[74:75], 2, v30\nMvaa 148,116,32\nv_cmp_gt_i32 s[78:79], 3, v30\n" \
"v_cndmask_b32 v112, v12, v6, s[90:91]\nMvaa 152,120,32\nv_cndmask_b32 v113, v12, v6, s[92:93]\nv_cndmask_b32 v114, v12, v6, s[74:75]\nMvaa 152,152,48\nv_cndmask_b32 v115, v12, v6, s[78:79]\nv_cmp_gt_i32 s[90:91], 16, v30\nMvaa 156,156,48\n" \
"v_cmp_gt_i32 s[92:93], 17, v30\nv_cmp_gt_i32 s[74:75], 18, v30\nMvaa 156,124,32\nv_cmp_gt_i32 s[78:79], 19, v30\nv_cndmask_b32 v116, v12, v6, s[90:91]\nMvaa 188,124,36\ns_waitcnt vmcnt(13)\ns_barrier\n" \
"Mvaa 188,156,52\nTa 64,20,32768\nv_cndmask_b32 v117, v12, v6, s[92:93]\nMvaa 184,152,52\nTa 66,20,33792\nv_cndmask_b32 v118, v12, v6, s[74:75]\nMvaa 184,120,36\nTa 160,20,40960\n" \
"v_cndmask_b32 v119, v12, v6, s[78:79]\nMvaa 180,116,36\nTa 162,20,41984\nv_cmp_gt_i32 s[90:91], 32, v30\nMvaa 180,148,52\nRa 0,16,32768\nv_cmp_gt_i32 s[92:93], 33, v30\nMvaa 176,144,52\n" \
"Ra 16,17,32768\nv_cmp_gt_i32 s[74:75], 34, v30\nMvaa 176,112,36\nTa 68,21,32768\nv_cmp_gt_i32 s[78:79], 35, v30\nMvaa 172,108,36\nTa 70,21,33792\nv_cndmask_b32 v120, v12, v6, s[90:91]\n" \
"Mvaa 172,140,52\nTa 164,21,40960\nv_cndmask_b32 v121, v12, v6, s[92:93]\nMvaa 168,136,52\nTa 166,21,41984\nv_cndmask_b32 v122, v12, v6, s[74:75]\nMvaa 168,104,36\nTa 72,22,32768\n" \
"v_cndmask_b32 v123, v12, v6, s[78:79]\nMvaa 164,100,36\nTa 74,22,33792\nv_cmp_gt_i32 s[90:91], 48, v30\nMvaa 164,132,52\nTa 168,22,40960\nv_cmp_gt_i32 s[92:93], 49, v30\nMvaa 160,128,52\n" \
"Ta 170,22,41984\nv_cmp_gt_i32 s[74:75], 50, v30\nMvaa 160,96,36\nTa 76,23,32768\nv_cmp_gt_i32 s[78:79], 51, v30\nMvaa 192,96,40\nTa 78,23,33792\nv_cndmask_b32 v124, v12, v6, s[90:91]\n" \
"Mvaa 192,128,56\nTa 172,23,40960\nv_cndmask_b32 v125, v12, v6, s[92:93]\nMvaa 196,132,56\nTa 174,23,41984\nv_cndmask_b32 v126, v12, v6, s[74:75]\nMvaa 196,100,40\nTa 80,24,32768\n" \
"v_cndmask_b32 v127, v12, v6, s[78:79]\nMvaa 200,104,40\nTa 82,24,33792\nMvaa 200,136,56\nMvaa 204,140,56\nTa 176,24,40960\nMvaa 204,108,40\nTa 178,24,41984\n" \
"Mvaa 208,112,40\nTa 84,25,32768\nMvaa 208,144,56\nTa 86,25,33792\nMvaa 212,148,56\nTa 180,25,40960\nMvaa 212,116,40\nTa 182,25,41984\n" \
"Mvaa 216,120,40\nTa 88,26,32768\nMvaa 216,152,56\nTa 90,26,33792\nMvaa 220,156,56\nTa 184,26,40960\nMvaa 220,124,40\nTa 186,26,41984\n" \
"Mvaa 252,124,44\nTa 92,27,32768\nMvaa 252,156,60\nTa 94,27,33792\nMvaa 248,152,60\nTa 188,27,40960\nMvaa 248,120,44\nTa 190,27,41984\n" \
"Mvaa 244,116,44\nRa 4,16,34816\nMvaa 244,148,60\nRa 20,17,34816\nMvaa 240,144,60\nRa 8,16,36864\nMvaa 240,112,44\nRa 24,17,36864\n" \
"Mvaa 236,108,44\nRa 12,16,38912\nMvaa 236,140,60\nRa 28,17,38912\nMvaa 232,136,60\nMvaa 232,104,44\nMvaa 228,100,44\nMvaa 228,132,60\n" \
"Mvaa 224,128,60\nMvaa 224,96,44\ns_waitcnt lgkmcnt(6)\nMvaa 128,64,0\nv_and_b32 v30, 3, v29\nv_bfe_u32 v6, v29, 1, 1\nMvaa 128,160,16\nv_lshrrev_b32 v28, 1, v28\n" \
"v_cmp_eq_u32 s[90:91], 2, v30\nMvaa 132,164,16\nv_cmp_eq_u32 s[92:93], v6, v28\nv_and_b32 v30, 1, v29\nMvaa 132,68,0\nv_mov_b32 v6, 0x3f803f80\nv_cmp_eq_u32 s[78:79], 1, v30\nMvaa 136,72,0\n" \
"v_mov_b32 v7, 0x4444\nv_mov_b32 v30, 0xeeee\nMvaa 136,168,16\nv_cndmask_b32 v108, 0, v6, s[90:91]\nv_cndmask_b32 v109, 0, v6, s[90:91]\nMvaa 140,172,16\nv_cndmask_b32 v110, 0, v6, s[90:91]\nv_cndmask_b32 v111, 0, v6, s[90:91]\n" \
"Mvaa 140,76,0\ns_waitcnt lgkmcnt(0)\nv_cndmask_b32 v104, 0, v6, s[92:93]\nMvaa 144,80,0\nv_cndmask_b32 v105, 0, v6, s[92:93]\nv_cndmask_b32 v106, 0, v6, s[92:93]\nMvaa 144,176,16\nv_cndmask_b32 v107, 0, v6, s[92:93]\n" \
"v_cndmask_b32 v7, v7, v30, s[78:79]\nMvaa 148,180,16\nv_mov_b32 v96, 0\nv_mov_b32 v97, 0\nMvaa 148,84,0\nv_mov_b32 v98, 0\nv_mov_b32 v99, 0\nMvaa 152,88,0\n" \
"v_mov_b32 v100, 0\nv_mov_b32 v101, 0\nMvaa 152,184,16\nv_mov_b32 v102, 0\nv_mov_b32 v103, 0\nMvaa 156,188,16\nv_mov_b32 v5, v0\ns_mov_b32 s73, s32\n" \
"Mvaa 156,92,0\ns_add_u32 s74, s32, 0x4000\ns_add_u32 s75, s32, 0x8000\nMvaa 188,92,4\ns_waitcnt vmcnt(0)\ns_barrier\nv_cmp_ne_u64 vcc, v[10:11], v[2:3]\ns_cbranch_vccnz zs102\n" \
"zr102:\nMvaa 188,188,20\ns_mov_b32 m0, s32\nTa 96,20,16384\nMvaa 184,184,20\nDS 0,60,s77,0\nMvaa 184,88,4\nTa 98,20,17408\n" \
"Ta 128,20,24576\nMvaa 180,84,4\nTa 130,20,25600\ns_mov_b32 s97, 0x3e0293ee\nMvaa 180,180,20\nRa 32,18,0\ns_mov_b32 s89, 31744\nMvaa 176,176,20\n" \
"DS 0,60,s77,1024\nMvaa 176,80,4\nRa 48,19,0\nTa 100,21,16384\nMvaa 172,76,4\nTa 102,21,17408\ns_mov_b32 s90, 6144\nMvaa 172,172,20\n" \
"Ta 132,21,24576\ns_mov_b32 s91, 37888\nMvaa 168,168,20\nDS 0,60,s77,2048\nMvaa 168,72,4\nTa 134,21,25600\nTa 104,22,16384\nMvaa 164,68,4\n" \
"Ta 106,22,17408\ns_mov_b32 s92, 0x200000\nMvaa 164,164,20\nTa 136,22,24576\nTa 138,22,25600\nMvaa 160,160,20\nDS 0,60,s77,3072\nMvaa 160,64,4\n" \
"Ta 108,23,16384\ns_mov_b32 s78, s11\nMvaa 192,64,8\nTa 110,23,17408\ns_add_u32 s84, s32, 98304\nMvaa 192,160,24\ns_add_u32 m0, s32, 16384\ns_add_u32 s77, s77, 0x4000\n" \
"Mvaa 196,164,24\nDS 0,60,s77,0\nMvaa 196,68,8\nTa 140,23,24576\nTa 142,23,25600\nMvaa 200,72,8\nTa 112,24,16384\ns_mov_b32 s85, 8192\n" \
"Mvaa 200,168,24\nTa 114,24,17408\ns_mov_b32 s86, 16384\nMvaa 204,172,24\nDS 0,60,s77,1024\nMvaa 204,76,8\nTa 144,24,24576\nTa 146,24,25600\n" \
"Mvaa 208,80,8\nTa 116,25,16384\ns_mov_b32 s87, 24576\nMvaa 208,176,24\nTa 118,25,17408\nTa 148,25,24576\nMvaa 212,180,24\nDS 0,60,s77,2048\n" \
"Mvaa 212,84,8\nTa 150,25,25600\ns_xor_b32 s82, s11, s10\nMvaa 216,88,8\ns_branch 99f\n.p2align 12\nzp35:\ns_setpc_b64 s[98:99]\n" \
"99:\nTa 120,26,16384\ns_lshl_b32 s83, s82, 16\nMvaa 216,184,24\nTa 122,26,17408\nTa 152,26,24576\nMvaa 220,188,24\nDS 0,60,s77,3072\n" \
"Mvaa 220,92,8\nTa 154,26,25600\ns_add_u32 s83, s83, s18\nMvaa 252,92,12\nTa 124,27,16384\nTa 126,27,17408\nMvaa 252,188,28\ns_add_u32 m0, s32, 49152\n" \
"Mvaa 248,184,28\nDS 8,60,s77,0\nMvaa 248,88,12\nTa 156,27,24576\nTa 158,27,25600\nMvaa 244,84,12\nRa 36,18,2048\nMvaa 244,180,28\n" \
"Ra 52,19,2048\nMvaa 240,176,28\nDS 8,60,s77,1024\nMvaa 240,80,12\nRa 40,18,4096\nRa 56,19,4096\nMvaa 236,76,12\nRa 44,18,6144\n" \
"Mvaa 236,172,28\nRa 60,19,6144\nMvaa 232,168,28\nDS 8,60,s77,2048\nMvaa 232,72,12\nMvaa 228,68,12\nMvaa 228,164,28\nMvaa 224,160,28\n" \
"DS 8,60,s77,3072\nMvaa 224,64,12\ns_waitcnt lgkmcnt(6)\nMvaa 128,96,32\ns_add_u32 m0, s32, 32768\ns_add_u32 s77, s77, 0x4000\nMvaa 128,128,48\nDS 0,60,s77,0\n" \
"Mvaa 132,132,48\nv_lshrrev_b32 v17, 8, v0\nv_and_b32 v16, 0xf0, v0\nMvaa 132,100,32\nDS 0,60,s77,1024\nMvaa 136,104,32\nv_lshlrev_b32 v18, 5, v17\nv_xor_b32 v16, v16, v18\n" \
"Mvaa 136,136,48\nDS 0,60,s77,2048\nMvaa 140,140,48\nv_lshl_add_u32 v16, v17, 11, v16\nv_add_u32 v16, s20, v16\nMvaa 140,108,32\ns_waitcnt lgkmcnt(0)\nDS 0,60,s77,3072\n" \
"Mvaa 144,112,32\ns_add_u32 m0, s32, 65536\ns_xor_b32 s82, s78, s10\nMvaa 144,144,48\nDS 8,60,s77,0\nMvaa 148,148,48\ns_and_b32 s80, s82, 31\ns_lshl_b32 s80, s80, 18\n" \
"Mvaa 148,116,32\nDS 8,60,s77,1024\nMvaa 152,120,32\ns_cmp_lt_u32 s78, 32\ns_cselect_b64 s[68:69], s[14:15], s[12:13]\nMvaa 152,152,48\nDS 8,60,s77,2048\nMvaa 156,156,48\n" \
"s_cselect_b32 s70, 0x800000, s92\nv_add_u32 v18, s80, v16\nMvaa 156,124,32\nDS 8,60,s77,3072\nMvaa 160,96,36\ns_add_u32 s77, s77, 0x4000\nv_xor_b32 v19, 128, v18\nMvaa 160,128,52\n" \
"Mvaa 164,132,52\nMvaa 164,100,36\nMvaa 168,104,36\nMvaa 168,136,52\nv_mul_f32 v128, s97, v128\nv_mul_f32 v129, s97, v129\nv_mul_f32 v130, s97, v130\nMvaa 172,140,52\n" \
"v_mul_f32 v131, s97, v131\nv_mul_f32 v132, s97, v132\nv_mul_f32 v133, s97, v133\nMvaa 172,108,36\nv_mul_f32 v134, s97, v134\nv_mul_f32 v135, s97, v135\nC 32,128,129\nMvaa 176,112,36\n" \
"C 33,130,131\nv_mul_f32 v136, s97, v136\nv_mul_f32 v137, s97, v137\nMvaa 176,144,52\nv_mul_f32 v138, s97, v138\nv_mul_f32 v139, s97, v139\nC 34,132,133\nMvaa 180,148,52\n" \
"C 35,134,135\nv_mul_f32 v140, s97, v140\nv_mul_f32 v141, s97, v141\nMvaa 180,116,36\nv_mul_f32 v142, s97, v142\nv_mul_f32 v143, s97, v143\nC 36,136,137\nMvaa 184,120,36\n" \
"C 37,138,139\nv_mul_f32 v144, s97, v144\nv_mul_f32 v145, s97, v145\nMvaa 184,152,52\nv_mul_f32 v146, s97, v146\nv_mul_f32 v147, s97, v147\nC 38,140,141\nMvaa 188,156,52\n" \
"C 39,142,143\nv_mul_f32 v148, s97, v148\nv_mul_f32 v149, s97, v149\nMvaa 188,124,36\nv_mul_f32 v150, s97, v150\nv_mul_f32 v151, s97, v151\nC 40,144,145\nMvaa 192,96,40\n" \
"C 41,146,147\nv_mul_f32 v152, s97, v152\nv_mul_f32 v153, s97, v153\nMvaa 192,128,56\nv_mul_f32 v154, s97, v154\nv_mul_f32 v155, s97, v155\nC 42,148,149\nMvaa 196,132,56\n" \
"C 43,150,151\nv_mul_f32 v156, s97, v156\nv_mul_f32 v157, s97, v157\nMvaa 196,100,40\nv_mul_f32 v158, s97, v158\nv_mul_f32 v159, s97, v159\nC 44,152,153\nMvaa 200,104,40\n" \
"C 45,154,155\nC 46,156,157\nC 47,158,159\nMvaa 200,136,56\nv_mul_f32 v160, s97, v160\nv_mul_f32 v161, s97, v161\nv_mul_f32 v162, s97, v162\nMvaa 204,140,56\n" \
"v_mul_f32 v163, s97, v163\nv_mul_f32 v164, s97, v164\nv_mul_f32 v165, s97, v165\nMvaa 204,108,40\nv_mul_f32 v166, s97, v166\nv_mul_f32 v167, s97, v167\nC 48,160,161\nMvaa 208,112,40\n" \
"s_waitcnt vmcnt(16)\ns_barrier\nC 49,162,163\nMvaa 208,144,56\nRa 160,5,0\nv_mul_f32 v168, s97, v168\nv_mul_f32 v169, s97, v169\nMvaa 212,148,56\n" \
"v_mul_f32 v170, s97, v170\nv_mul_f32 v171, s97, v171\nC 50,164,165\nMvaa 212,116,40\nRa 164,5,1024\nC 51,166,167\nv_mul_f32 v172, s97, v172\nMvaa 216,120,40\n" \
"v_mul_f32 v173, s97, v173\nv_mul_f32 v174, s97, v174\nv_mul_f32 v175, s97, v175\nMvaa 216,152,56\nRa 168,5,2048\nC 52,168,169\nC 53,170,171\nMvaa 220,156,56\n" \
"v_mul_f32 v176, s97, v176\nv_mul_f32 v177, s97, v177\nv_mul_f32 v178, s97, v178\nMvaa 220,124,40\nRa 172,5,3072\nv_mul_f32 v179, s97, v179\nC 54,172,173\nMvaa 224,96,44\n" \
"C 55,174,175\nv_mul_f32 v180, s97, v180\nv_mul_f32 v181, s97, v181\nMvaa 224,128,60\nRa 176,5,4096\nv_mul_f32 v182, s97, v182\nv_mul_f32 v183, s97, v183\nMvaa 228,132,60\n" \
"C 56,176,177\nC 57,178,179\nv_mul_f32 v184, s97, v184\nMvaa 228,100,44\nRa 180,5,5120\nv_mul_f32 v185, s97, v185\nv_mul_f32 v186, s97, v186\nMvaa 232,104,44\n" \
"v_mul_f32 v187, s97, v187\nC 58,180,181\nC 59,182,183\nMvaa 232,136,60\nRa 184,5,6144\nv_mul_f32 v188, s97, v188\nv_mul_f32 v189, s97, v189\nMvaa 236,140,60\n" \
"v_mul_f32 v190, s97, v190\nv_mul_f32 v191, s97, v191\nC 60,184,185\nMvaa 236,108,44\nRa 188,5,7168\nC 61,186,187\nC 62,188,189\nMvaa 240,112,44\n" \
"C 63,190,191\nv_mul_f32 v192, s97, v192\nv_mul_f32 v193, s97, v193\nMvaa 240,144,60\nv_mul_f32 v194, s97, v194\nv_mul_f32 v195, s97, v195\nv_mul_f32 v196, s97, v196\nMvaa 244,148,60\n" \
"v_mul_f32 v197, s97, v197\nv_mul_f32 v198, s97, v198\nv_mul_f32 v199, s97, v199\nMvaa 244,116,44\nC 64,192,193\nC 65,194,195\nv_mul_f32 v200, s97, v200\nMvaa 248,120,44\n" \
"s_bitcmp1_b32 s33, 2\nv_mul_f32 v201, s97, v201\nv_mul_f32 v202, s97, v202\nMvaa 248,152,60\nv_mul_f32 v203, s97, v203\nC 66,196,197\nC 67,198,199\nMvaa 252,156,60\n" \
"Mvaa 252,124,44\ns_waitcnt lgkmcnt(0)\ns_cbranch_scc1 zZ\nQvav 128,160,32,12\nRa 128,5,8192\nv_mul_f32 v204, s97, v204\nv_mul_f32 v205, s97, v205\nMvav 128,164,36\n" \
"v_mul_f32 v206, s97, v206\nv_mul_f32 v207, s97, v207\nC 68,200,201\nMvav 128,168,40\nRa 132,5,9216\nC 69,202,203\nv_mul_f32 v208, s97, v208\nMvav 128,172,44\n" \
"v_mul_f32 v209, s97, v209\nv_mul_f32 v210, s97, v210\nv_mul_f32 v211, s97, v211\nQvav 132,176,32,12\nRa 136,5,10240\nC 70,204,205\nC 71,206,207\nMvav 132,180,36\n" \
"v_mul_f32 v212, s97, v212\nv_mul_f32 v213, s97, v213\nv_mul_f32 v214, s97, v214\nMvav 132,184,40\nRa 140,5,11264\nv_mul_f32 v215, s97, v215\nC 72,208,209\nMvav 132,188,44\n" \
"C 73,210,211\nv_mul_f32 v216, s97, v216\nv_mul_f32 v217, s97, v217\nQvav 136,160,48,12\nRa 144,5,12288\nv_mul_f32 v218, s97, v218\nv_mul_f32 v219, s97, v219\nMvav 136,164,52\n" \
"C 74,212,213\nC 75,214,215\nv_mul_f32 v220, s97, v220\nMvav 136,168,56\nRa 148,5,13312\nv_mul_f32 v221, s97, v221\nv_mul_f32 v222, s97, v222\nMvav 136,172,60\n" \
"v_mul_f32 v223, s97, v223\nC 76,216,217\nC 77,218,219\nQvav 140,176,48,12\nRa 152,5,14336\nC 78,220,221\nC 79,222,223\nMvav 140,180,52\n" \
"v_mul_f32 v224, s97, v224\nv_mul_f32 v225, s97, v225\nv_mul_f32 v226, s97, v226\nMvav 140,184,56\nRa 156,5,15360\nv_mul_f32 v227, s97, v227\nv_mul_f32 v228, s97, v228\nMvav 140,188,60\n" \
"v_mul_f32 v229, s97, v229\nv_mul_f32 v230, s97, v230\nv_mul_f32 v231, s97, v231\nQvav 144,160,64,12\nC 80,224,225\nC 81,226,227\nv_mul_f32 v232, s97, v232\nMvav 144,164,68\n" \
"v_mul_f32 v233, s97, v233\nv_mul_f32 v234, s97, v234\nv_mul_f32 v235, s97, v235\nMvav 144,168,72\nC 82,228,229\nC 83,230,231\nv_mul_f32 v236, s97, v236\nMvav 144,172,76\n" \
"v_mul_f32 v237, s97, v237\nv_mul_f32 v238, s97, v238\nv_mul_f32 v239, s97, v239\nQvav 148,176,64,12\nC 84,232,233\nC 85,234,235\nv_mul_f32 v240, s97, v240\nMvav 148,180,68\n" \
"v_mul_f32 v241, s97, v241\nv_mul_f32 v242, s97, v242\nv_mul_f32 v243, s97, v243\nC 86,236,237\nC 87,238,239\nv_mul_f32 v244, s97, v244\nv_mul_f32 v245, s97, v245\nv_mul_f32 v246, s97, v246\n" \
"v_mul_f32 v247, s97, v247\nC 88,240,241\nC 89,242,243\nv_mul_f32 v248, s97, v248\nv_mul_f32 v249, s97, v249\nv_mul_f32 v250, s97, v250\nv_mul_f32 v251, s97, v251\nC 90,244,245\n" \
"C 91,246,247\nv_mul_f32 v252, s97, v252\nv_mul_f32 v253, s97, v253\nv_mul_f32 v254, s97, v254\nv_mul_f32 v255, s97, v255\nC 92,248,249\nC 93,250,251\nC 94,252,253\n" \
"C 95,254,255\nMvav 148,184,72\nE 240,128\nMvav 148,188,76\nE 241,129\nQvav 152,160,80,12\nC 192,240,241\nE 242,130\n" \
"Mvav 152,164,84\nE 243,131\nMvav 152,168,88\nC 193,242,243\nMvav 152,172,92\nE 244,132\nQvav 156,176,80,12\nE 245,133\n" \
"Mvav 156,180,84\nC 194,244,245\nE 246,134\nMvav 156,184,88\nE 247,135\nMvav 156,188,92\ns_waitcnt lgkmcnt(0)\nC 195,246,247\n" \
"Qvav 160,128,32,12\nE 240,136\nMvav 160,132,36\nE 241,137\nMvav 160,136,40\nC 196,240,241\nE 242,138\nMvav 160,140,44\n" \
"Qvav 168,128,48,12\nv_mov_b32 v6, v5\nv_add_u32 v5, s74, v9\nE 243,139\nMvav 168,132,52\nC 197,242,243\nE 244,140\nMvav 168,136,56\n" \
"E 245,141\nMvav 168,140,60\nC 198,244,245\nE 246,142\nQvav 176,128,64,12\ns_waitcnt vmcnt(8)\ns_barrier\nbuffer_store_dwordx2 v[2:3], off, s[60:63], s36 sc1\n" \
"Mvav 176,132,68\nRa 160,5,0\nE 247,143\nMvav 176,136,72\ns_mov_b32 m0, s73\nC 199,246,247\nE 240,144\nMvav 176,140,76\n" \
"Ra 164,5,1024\nDS 0,60,s77,0\nE 241,145\nQvav 184,128,80,12\nC 200,240,241\nMvav 184,132,84\nRa 168,5,2048\nE 242,146\n" \
"Mvav 184,136,88\nE 243,147\nMvav 184,140,92\nRa 172,5,3072\nC 201,242,243\nE 244,148\nQvav 164,144,32,12\nDS 0,60,s77,1024\n" \
"E 245,149\nMvav 164,148,36\nRa 176,5,4096\nC 202,244,245\nMvav 164,152,40\nE 246,150\nMvav 164,156,44\nRa 180,5,5120\n" \
"E 247,151\nQvav 172,144,48,12\nC 203,246,247\nE 240,152\nMvav 172,148,52\nRa 184,5,6144\nDS 0,60,s77,2048\nMvav 172,152,56\n" \
"E 241,153\nMvav 172,156,60\nRa 188,5,7168\nC 204,240,241\nE 242,154\nQvav 180,144,64,12\nE 243,155\nMvav 180,148,68\n" \
"C 205,242,243\nE 244,156\nMvav 180,152,72\nDS 0,60,s77,3072\nMvav 180,156,76\nE 245,157\nQvav 188,144,80,12\nC 206,244,245\n" \
"E 246,158\nMvav 188,148,84\nE 247,159\nMvav 188,152,88\nC 207,246,247\ns_mov_b32 s96, s39\nMvav 188,156,92\ns_waitcnt lgkmcnt(0)\n" \
"Qvav 128,160,32,12\nE 240,160\nMvav 128,164,36\nE 241,161\nMvav 128,168,40\nC 208,240,241\nRa 128,6,49152\nMvav 128,172,44\n" \
"E 242,162\ns_add_u32 m0, s75, 0xc000\nQvav 136,160,48,12\nDS 8,60,s77,0\nMvav 136,164,52\nE 243,163\nMvav 136,168,56\nC 209,242,243\n" \
"Ra 132,6,50176\nMvav 136,172,60\nE 244,164\nQvav 144,160,64,12\nDS 8,60,s77,1024\nMvav 144,164,68\nE 245,165\nMvav 144,168,72\n" \
"C 210,244,245\nMvav 144,172,76\nE 246,166\nQvav 152,160,80,12\nRa 136,6,51200\nRa 140,6,52224\nMvav 152,164,84\nE 247,167\n" \
"Mvav 152,168,88\nC 211,246,247\nMvav 152,172,92\nE 240,168\nQvav 132,176,32,12\nDS 8,60,s77,2048\nMvav 132,180,36\nE 241,169\n" \
"Mvav 132,184,40\nC 212,240,241\nRa 144,6,53248\nMvav 132,188,44\nE 242,170\nQvav 140,176,48,12\nRa 148,6,54272\nMvav 140,180,52\n" \
"E 243,171\nMvav 140,184,56\nC 213,242,243\nRa 152,6,55296\nMvav 140,188,60\nE 244,172\nQvav 148,176,64,12\nDS 8,60,s77,3072\n" \
"Mvav 148,180,68\nE 245,173\nMvav 148,184,72\nC 214,244,245\ns_mov_b32 s76, s73\nMvav 148,188,76\nE 246,174\nQvav 156,176,80,12\n" \
"Ra 156,6,56320\ns_mov_b32 s73, s74\nMvav 156,180,84\nE 247,175\nMvav 156,184,88\nC 215,246,247\ns_mov_b32 s74, s75\nMvav 156,188,92\n" \
"E 240,176\nv_smfmac_f32_16x16x64_bf16 v[96:99], v[104:107], v[192:199], v7\ns_mov_b32 s75, s76\nv_smfmac_f32_16x16x64_bf16 v[100:103], v[104:107], v[200:207], v7\ns_waitcnt lgkmcnt(0)\nZaav 96,128,204\nRa 160,5,8192\ns_add_u32 s77, s77, 0x4000\n" \
"Zaav 100,132,204\nE 241,177\nZaav 68,132,200\nC 216,240,241\nRa 164,5,9216\nZaav 64,128,200\nE 242,178\nZaav 32,128,196\n" \
"Zaav 36,132,196\nE 243,179\nZaav 4,132,192\nC 217,242,243\nRa 168,5,10240\nZaav 0,128,192\nE 244,180\nZaav 8,136,192\n" \
"Ra 172,5,11264\nZaav 12,140,192\nE 245,181\nZaav 44,140,196\nC 218,244,245\nZaav 40,136,196\nE 246,182\nZaav 72,136,200\n" \
"Ra 176,5,12288\nZaav 76,140,200\nE 247,183\nZaav 108,140,204\nC 219,246,247\nZaav 104,136,204\nE 240,184\nZaav 112,144,204\n" \
"Ra 180,5,13312\nZaav 116,148,204\nE 241,185\nZaav 84,148,200\nC 220,240,241\nZaav 80,144,200\nE 242,186\nZaav 48,144,196\n" \
"Ra 184,5,14336\nZaav 52,148,196\nE 243,187\nZaav 20,148,192\nC 221,242,243\nRa 188,5,15360\nZaav 16,144,192\nE 244,188\n" \
"Zaav 24,152,192\nZaav 28,156,192\nE 245,189\nZaav 60,156,196\nC 222,244,245\nZaav 56,152,196\nE 246,190\nZaav 88,152,200\n" \
"Zaav 92,156,200\nE 247,191\nZaav 124,156,204\nC 223,246,247\ns_waitcnt lgkmcnt(0)\nZaav 120,152,204\ns_branch zE\nzZ:\n" \
"Qvav 128,160,32,112\nRa 128,5,8192\ns_branch 99f\n.p2align 12\nzp36:\ns_setpc_b64 s[98:99]\n99:\nv_mul_f32 v204, s97, v204\n" \
"v_mul_f32 v205, s97, v205\nMvav 128,164,36\nv_mul_f32 v206, s97, v206\nv_mul_f32 v207, s97, v207\nC 68,200,201\nMvav 128,168,40\nRa 132,5,9216\nC 69,202,203\n" \
"v_mul_f32 v208, s97, v208\nMvav 128,172,44\nv_mul_f32 v209, s97, v209\nv_mul_f32 v210, s97, v210\nv_mul_f32 v211, s97, v211\nQvav 132,176,32,116\nRa 136,5,10240\nC 70,204,205\n" \
"C 71,206,207\nMvav 132,180,36\nv_mul_f32 v212, s97, v212\nv_mul_f32 v213, s97, v213\nv_mul_f32 v214, s97, v214\nMvav 132,184,40\nRa 140,5,11264\nv_mul_f32 v215, s97, v215\n" \
"C 72,208,209\nMvav 132,188,44\nC 73,210,211\nv_mul_f32 v216, s97, v216\nv_mul_f32 v217, s97, v217\nQvav 136,160,48,12\nRa 144,5,12288\nv_mul_f32 v218, s97, v218\n" \
"v_mul_f32 v219, s97, v219\nMvav 136,164,52\nC 74,212,213\nC 75,214,215\nv_mul_f32 v220, s97, v220\nMvav 136,168,56\nRa 148,5,13312\nv_mul_f32 v221, s97, v221\n" \
"v_mul_f32 v222, s97, v222\nMvav 136,172,60\nv_mul_f32 v223, s97, v223\nC 76,216,217\nC 77,218,219\nQvav 140,176,48,12\nRa 152,5,14336\nC 78,220,221\n" \
"C 79,222,223\nMvav 140,180,52\nv_mul_f32 v224, s97, v224\nv_mul_f32 v225, s97, v225\nv_mul_f32 v226, s97, v226\nMvav 140,184,56\nRa 156,5,15360\nv_mul_f32 v227, s97, v227\n" \
"v_mul_f32 v228, s97, v228\nMvav 140,188,60\nv_mul_f32 v229, s97, v229\nv_mul_f32 v230, s97, v230\nv_mul_f32 v231, s97, v231\nQvav 144,160,64,12\nC 80,224,225\nC 81,226,227\n" \
"v_mul_f32 v232, s97, v232\nMvav 144,164,68\nv_mul_f32 v233, s97, v233\nv_mul_f32 v234, s97, v234\nv_mul_f32 v235, s97, v235\nMvav 144,168,72\nC 82,228,229\nC 83,230,231\n" \
"v_mul_f32 v236, s97, v236\nMvav 144,172,76\nv_mul_f32 v237, s97, v237\nv_mul_f32 v238, s97, v238\nv_mul_f32 v239, s97, v239\nQvav 148,176,64,12\nC 84,232,233\nC 85,234,235\n" \
"v_mul_f32 v240, s97, v240\nMvav 148,180,68\nv_mul_f32 v241, s97, v241\nv_mul_f32 v242, s97, v242\nv_mul_f32 v243, s97, v243\nC 86,236,237\nC 87,238,239\nv_mul_f32 v244, s97, v244\n" \
"v_mul_f32 v245, s97, v245\nv_mul_f32 v246, s97, v246\nv_mul_f32 v247, s97, v247\nC 88,240,241\nC 89,242,243\nv_mul_f32 v248, s97, v248\nv_mul_f32 v249, s97, v249\nv_mul_f32 v250, s97, v250\n" \
"v_mul_f32 v251, s97, v251\nC 90,244,245\nC 91,246,247\nv_mul_f32 v252, s97, v252\nv_mul_f32 v253, s97, v253\nv_mul_f32 v254, s97, v254\nv_mul_f32 v255, s97, v255\nC 92,248,249\n" \
"C 93,250,251\nC 94,252,253\nC 95,254,255\nMvav 148,184,72\nE 240,128\nMvav 148,188,76\nE 241,129\nQvav 152,160,80,12\n" \
"C 192,240,241\nE 242,130\nMvav 152,164,84\nE 243,131\nMvav 152,168,88\nC 193,242,243\nMvav 152,172,92\nE 244,132\n" \
"Qvav 156,176,80,12\nE 245,133\nMvav 156,180,84\nC 194,244,245\nE 246,134\nMvav 156,184,88\nE 247,135\nMvav 156,188,92\n" \
"s_waitcnt lgkmcnt(0)\nC 195,246,247\nQvav 160,128,32,120\nE 240,136\nMvav 160,132,36\nE 241,137\nMvav 160,136,40\nC 196,240,241\n" \
"E 242,138\nMvav 160,140,44\nQvav 168,128,48,12\nv_mov_b32 v6, v5\nv_add_u32 v5, s74, v9\nE 243,139\nMvav 168,132,52\nC 197,242,243\n" \
"E 244,140\nMvav 168,136,56\nE 245,141\nMvav 168,140,60\nC 198,244,245\nE 246,142\nQvav 176,128,64,12\ns_waitcnt vmcnt(8)\n" \
"s_barrier\nbuffer_store_dwordx2 v[2:3], off, s[60:63], s36 sc1\nMvav 176,132,68\nRa 160,5,0\nE 247,143\nMvav 176,136,72\ns_mov_b32 m0, s73\nC 199,246,247\n" \
"E 240,144\nMvav 176,140,76\nRa 164,5,1024\nDS 0,60,s77,0\nE 241,145\nQvav 184,128,80,12\nC 200,240,241\nMvav 184,132,84\n" \
"Ra 168,5,2048\nE 242,146\nMvav 184,136,88\nE 243,147\nMvav 184,140,92\nRa 172,5,3072\nC 201,242,243\nE 244,148\n" \
"Qvav 164,144,32,124\nDS 0,60,s77,1024\nE 245,149\nMvav 164,148,36\nRa 176,5,4096\nC 202,244,245\nMvav 164,152,40\nE 246,150\n" \
"Mvav 164,156,44\nRa 180,5,5120\nE 247,151\nQvav 172,144,48,12\nC 203,246,247\nE 240,152\nMvav 172,148,52\nRa 184,5,6144\n" \
"DS 0,60,s77,2048\nMvav 172,152,56\nE 241,153\nMvav 172,156,60\nRa 188,5,7168\nC 204,240,241\nE 242,154\nQvav 180,144,64,12\n" \
"E 243,155\nMvav 180,148,68\nC 205,242,243\nE 244,156\nMvav 180,152,72\nDS 0,60,s77,3072\nMvav 180,156,76\nE 245,157\n" \
"Qvav 188,144,80,12\nC 206,244,245\nE 246,158\nMvav 188,148,84\nE 247,159\nMvav 188,152,88\nC 207,246,247\ns_mov_b32 s96, s39\n" \
"Mvav 188,156,92\ns_waitcnt lgkmcnt(0)\nQvav 136,160,48,112\nE 240,160\nRa 128,6,49152\nMvav 136,164,52\nE 241,161\nMvav 136,168,56\n" \
"C 208,240,241\nRa 132,6,50176\nMvav 136,172,60\nE 242,162\ns_add_u32 m0, s75, 0xc000\nQvav 144,160,64,12\nDS 8,60,s77,0\nMvav 144,164,68\n" \
"E 243,163\nRa 136,6,51200\nMvav 144,168,72\nC 209,242,243\nE 244,164\nMvav 144,172,76\nDS 8,60,s77,1024\nQvav 152,160,80,12\n" \
"E 245,165\nMvav 152,164,84\nC 210,244,245\nRa 140,6,52224\nMvav 152,168,88\nE 246,166\nRa 144,6,53248\nMvav 152,172,92\n" \
"DS 8,60,s77,2048\nQvav 140,176,48,116\nE 247,167\nMvav 140,180,52\nC 211,246,247\nE 240,168\nMvav 140,184,56\nDS 8,60,s77,3072\n" \
"Mvav 140,188,60\nE 241,169\nRa 148,6,54272\nQvav 148,176,64,12\nC 212,240,241\nRa 152,6,55296\nMvav 148,180,68\nE 242,170\n" \
"Mvav 148,184,72\nE 243,171\nMvav 148,188,76\nC 213,242,243\nRa 156,6,56320\nQvav 156,176,80,12\nE 244,172\nMvav 156,180,84\n" \
"s_mov_b32 s76, s73\ns_mov_b32 s73, s74\nMvav 156,184,88\nE 245,173\nMvav 156,188,92\nC 214,244,245\ns_mov_b32 s74, s75\nv_smfmac_f32_16x16x64_bf16 v[96:99], v[104:107], v[192:199], v7\n" \
"s_mov_b32 s75, s76\nv_smfmac_f32_16x16x64_bf16 v[100:103], v[104:107], v[200:207], v7\ns_waitcnt lgkmcnt(0)\nZaav 96,128,204\nE 246,174\nZaav 100,132,204\nE 247,175\nZaav 68,132,200\n" \
"C 215,246,247\nRa 160,5,8192\nZaav 64,128,200\nE 240,176\nZaav 32,128,196\nRa 164,5,9216\ns_add_u32 s77, s77, 0x4000\nZaav 36,132,196\n" \
"E 241,177\nZaav 4,132,192\nC 216,240,241\nE 242,178\nZaav 0,128,192\nRa 168,5,10240\nZaav 8,136,192\nE 243,179\n" \
"Zaav 12,140,192\nC 217,242,243\nRa 172,5,11264\nZaav 44,140,196\nE 244,180\nZaav 40,136,196\nRa 176,5,12288\nZaav 72,136,200\n" \
"E 245,181\nZaav 76,140,200\nC 218,244,245\nE 246,182\nZaav 108,140,204\nRa 180,5,13312\nZaav 104,136,204\nE 247,183\n" \
"Zaav 112,144,204\nC 219,246,247\nZaav 116,148,204\nE 240,184\nZaav 84,148,200\nE 241,185\nZaav 80,144,200\nC 220,240,241\n" \
"Ra 184,5,14336\nZaav 48,144,196\nE 242,186\nZaav 52,148,196\nRa 188,5,15360\nZaav 20,148,192\nE 243,187\nZaav 16,144,192\n" \
"C 221,242,243\nZaav 24,152,192\nE 244,188\nZaav 28,156,192\nE 245,189\nZaav 60,156,196\nC 222,244,245\nZaav 56,152,196\n" \
"E 246,190\nZaav 88,152,200\nZaav 92,156,200\nE 247,191\nZaav 124,156,204\nC 223,246,247\ns_waitcnt lgkmcnt(0)\nZaav 120,152,204\n" \
"s_branch zD4\ns_branch 99f\n.p2align 12\nzp37:\ns_setpc_b64 s[98:99]\n99:\nzDa:\nQvav 144,160,64,112\n" \
"E 240,168\nRa 128,6,49152\nMvav 144,164,68\nE 241,169\nMvav 144,168,72\nC 212,240,241\nRa 132,6,50176\nRa 136,6,51200\n" \
"Mvav 144,172,76\nE 242,170\ns_add_u32 m0, s75, 0xc000\nQvav 152,160,80,12\nDS 8,60,s77,0\nMvav 152,164,84\nE 243,171\nRa 140,6,52224\n" \
"Mvav 152,168,88\nC 213,242,243\nE 244,172\nMvav 152,172,92\nDS 8,60,s77,1024\nRa 144,6,53248\nQvav 148,176,64,116\nE 245,173\n" \
"Mvav 148,180,68\nC 214,244,245\nRa 148,6,54272\nMvav 148,184,72\nE 246,174\nRa 152,6,55296\nMvav 148,188,76\nE 247,175\n" \
"Qvav 156,176,80,12\nC 215,246,247\nRa 156,6,56320\nMvav 156,180,84\nE 240,176\nMvav 156,184,88\nMvav 156,188,92\nE 241,177\n" \
"Mvvv 96,108,196\nC 216,240,241\nE 242,178\nv_smfmac_f32_16x16x64_bf16 v[100:103], v[104:107], v[200:207], v7\ns_waitcnt lgkmcnt(0)\nMaav 96,128,204\nDS 8,60,s77,2048\nRa 160,5,8192\n" \
"Maav 100,132,204\nE 243,179\nMaav 68,132,200\nC 217,242,243\nRa 164,5,9216\nMaav 64,128,200\nE 244,180\nMaav 32,128,196\n" \
"E 245,181\nMaav 36,132,196\nC 218,244,245\nRa 168,5,10240\nMaav 40,136,196\nE 246,182\nRa 172,5,11264\nMaav 44,140,196\n" \
"DS 8,60,s77,3072\nMaav 76,140,200\nE 247,183\ns_add_u32 m0, s32, 114688\nMaav 72,136,200\nC 219,246,247\nE 240,184\nD 18,68,0,0\n" \
"Maav 104,136,204\nRa 176,5,12288\nRa 180,5,13312\nMaav 108,140,204\nE 241,185\nD 18,68,s89,1024\nMaav 112,144,204\nC 220,240,241\n" \
"Ra 184,5,14336\nRa 188,5,15360\nMaav 116,148,204\nE 242,186\nD 19,68,s90,2048\nMaav 84,148,200\nE 243,187\nMaav 80,144,200\n" \
"C 221,242,243\nD 19,68,s91,3072\nMaav 48,144,196\nE 244,188\nMaav 52,148,196\ns_ff1_i32_b64 s78, s[8:9]\ns_bitset0_b64 s[8:9], s78\nMaav 56,152,196\n" \
"E 245,189\nMaav 60,156,196\nC 222,244,245\nE 246,190\nMaav 92,156,200\ns_xor_b32 s82, s78, s10\ns_and_b32 s80, s82, 31\nMaav 88,152,200\n" \
"E 247,191\ns_lshl_b32 s80, s80, 18\ns_cmp_lt_u32 s78, 32\ns_cselect_b64 s[68:69], s[14:15], s[12:13]\ns_cselect_b32 s70, 0x800000, s92\nv_add_u32 v18, s80, v16\nv_xor_b32 v19, 128, v18\nMaav 120,152,204\n" \
"C 223,246,247\ns_waitcnt lgkmcnt(0)\nMaav 124,156,204\ns_mov_b32 s76, s73\ns_mov_b32 s73, s74\ns_mov_b32 s74, s75\ns_mov_b32 s75, s76\ns_add_u32 s77, s77, 0x4000\n" \
"Qvav 176,160,64,120\nE 240,144\ns_add_u32 m0, s32, 131072\nRa 128,6,57344\nMvav 176,164,68\nD 18,68,0,0\nMvav 176,168,72\nE 241,145\n" \
"Mvav 176,172,76\nC 200,240,241\nRa 132,6,58368\nRa 136,6,59392\nQvav 184,160,80,12\nD 18,68,s89,1024\nMvav 184,164,84\nE 242,146\n" \
"Mvav 184,168,88\nRa 140,6,60416\nRa 144,6,61440\nRa 148,6,62464\nMvav 184,172,92\nE 243,147\nQvav 180,176,64,124\nC 201,242,243\n" \
"Ra 152,6,63488\nMvav 180,180,68\nD 19,68,s90,2048\nRa 156,6,64512\nMvav 180,184,72\nE 244,148\nMvav 180,188,76\nD 19,68,s91,3072\n" \
"Qvav 188,176,80,12\nE 245,149\nMvav 188,180,84\nC 202,244,245\nv_add_u32 v18, 131072, v18\nMvav 188,184,88\nv_add_u32 v19, 131072, v19\nv_mov_b32 v6, v5\n" \
"Mvav 188,188,92\nE 246,150\nMvvv 96,108,212\nv_add_u32 v5, s74, v9\nv_smfmac_f32_16x16x64_bf16 v[100:103], v[104:107], v[216:223], v7\ns_waitcnt vmcnt(20) lgkmcnt(0)\ns_barrier\nMaav 96,128,220\n" \
"E 247,151\nMaav 100,132,220\nC 203,246,247\nRa 160,5,0\nMaav 68,132,216\nRa 164,5,1024\nMaav 64,128,216\nE 240,152\n" \
"Maav 32,128,212\nRa 168,5,2048\nMaav 36,132,212\nE 241,153\nMaav 40,136,212\nC 204,240,241\nMaav 44,140,212\nRa 172,5,3072\n" \
"v_add_f32 v196, v96, v97\nv_add_f32 v197, v98, v99\nMaav 76,140,216\nE 242,154\nv_subrev_u32 v198, 0xf800000, v196\nMaav 72,136,216\nRa 176,5,4096\nv_subrev_u32 v199, 0xf800000, v197\n" \
"v_max_u32 v198, v198, v199\nMaav 104,136,220\nE 243,155\nv_cmp_lt_u32 vcc, 0x3c000000, v198\nMaav 108,140,220\nC 205,242,243\nMaav 112,144,220\nRa 180,5,5120\n" \
"Maav 116,148,220\nE 244,156\nMaav 84,148,216\nRa 184,5,6144\nMaav 80,144,216\nE 245,157\nMaav 48,144,212\nC 206,244,245\n" \
"Ra 188,5,7168\nMaav 52,148,212\nMaav 56,152,212\nE 246,158\nMaav 60,156,212\nMaav 92,156,216\nE 247,159\nMaav 88,152,216\n" \
"C 207,246,247\nMaav 120,152,220\ns_waitcnt lgkmcnt(0)\nMaav 124,156,220\ns_cbranch_vccnz zE3\ns_nop 7\nv_rcp_f32 v196, v196\nv_rcp_f32 v197, v197\n" \
"s_add_u32 s95, s83, 0\nAR 128,0\nAR 129,1\nAR 130,2\nAR 131,3\nAR 132,4\nAR 133,5\nAR 134,6\n" \
"AR 135,7\nv_pk_mul_f32 v[128:129], v[128:129], v[196:197] op_sel:[0,0] op_sel_hi:[1,0]\nv_pk_mul_f32 v[130:131], v[130:131], v[196:197] op_sel:[0,0] op_sel_hi:[1,0]\nv_pk_mul_f32 v[132:133], v[132:133], v[196:197] op_sel:[0,0] op_sel_hi:[1,0]\nv_pk_mul_f32 v[134:135], v[134:135], v[196:197] op_sel:[0,0] op_sel_hi:[1,0]\nC 32,128,129\nC 33,130,131\nC 34,132,133\n" \
"C 35,134,135\nbuffer_store_dwordx4 v[32:35], v0, s[60:63], s95 offen offset:0 sc1\nAR 136,8\nAR 137,9\nAR 138,10\nAR 139,11\nAR 140,12\nAR 141,13\n" \
"AR 142,14\nAR 143,15\nv_pk_mul_f32 v[136:137], v[136:137], v[196:197] op_sel:[0,0] op_sel_hi:[1,0]\nv_pk_mul_f32 v[138:139], v[138:139], v[196:197] op_sel:[0,0] op_sel_hi:[1,0]\nv_pk_mul_f32 v[140:141], v[140:141], v[196:197] op_sel:[0,0] op_sel_hi:[1,0]\nv_pk_mul_f32 v[142:143], v[142:143], v[196:197] op_sel:[0,0] op_sel_hi:[1,0]\nC 36,136,137\nC 37,138,139\n" \
"C 38,140,141\nC 39,142,143\nbuffer_store_dwordx4 v[36:39], v0, s[60:63], s95 offen offset:1024 sc1\ns_add_u32 s95, s83, 32768\nAR 128,16\nAR 129,17\nAR 130,18\nAR 131,19\n" \
"AR 132,20\nAR 133,21\nAR 134,22\nAR 135,23\nv_pk_mul_f32 v[128:129], v[128:129], v[196:197] op_sel:[0,0] op_sel_hi:[1,0]\nv_pk_mul_f32 v[130:131], v[130:131], v[196:197] op_sel:[0,0] op_sel_hi:[1,0]\nv_pk_mul_f32 v[132:133], v[132:133], v[196:197] op_sel:[0,0] op_sel_hi:[1,0]\nv_pk_mul_f32 v[134:135], v[134:135], v[196:197] op_sel:[0,0] op_sel_hi:[1,0]\n" \
"C 40,128,129\nC 41,130,131\nC 42,132,133\nC 43,134,135\nbuffer_store_dwordx4 v[40:43], v0, s[60:63], s95 offen offset:0 sc1\nAR 136,24\nAR 137,25\nAR 138,26\n" \
"AR 139,27\nAR 140,28\nAR 141,29\nAR 142,30\nAR 143,31\nv_pk_mul_f32 v[136:137], v[136:137], v[196:197] op_sel:[0,0] op_sel_hi:[1,0]\nv_pk_mul_f32 v[138:139], v[138:139], v[196:197] op_sel:[0,0] op_sel_hi:[1,0]\nv_pk_mul_f32 v[140:141], v[140:141], v[196:197] op_sel:[0,0] op_sel_hi:[1,0]\n" \
"v_pk_mul_f32 v[142:143], v[142:143], v[196:197] op_sel:[0,0] op_sel_hi:[1,0]\nC 44,136,137\nC 45,138,139\nC 46,140,141\nC 47,142,143\nbuffer_store_dwordx4 v[44:47], v0, s[60:63], s95 offen offset:1024 sc1\ns_add_u32 s95, s83, 8192\nAR 128,32\n" \
"AR 129,33\nAR 130,34\nAR 131,35\nAR 132,36\nAR 133,37\nAR 134,38\nAR 135,39\nv_pk_mul_f32 v[128:129], v[128:129], v[196:197] op_sel:[0,1] op_sel_hi:[1,1]\n" \
"v_pk_mul_f32 v[130:131], v[130:131], v[196:197] op_sel:[0,1] op_sel_hi:[1,1]\nv_pk_mul_f32 v[132:133], v[132:133], v[196:197] op_sel:[0,1] op_sel_hi:[1,1]\nv_pk_mul_f32 v[134:135], v[134:135], v[196:197] op_sel:[0,1] op_sel_hi:[1,1]\nC 48,128,129\nC 49,130,131\nC 50,132,133\nC 51,134,135\nbuffer_store_dwordx4 v[48:51], v0, s[60:63], s95 offen offset:0 sc1\n" \
"AR 136,40\nAR 137,41\nAR 138,42\nAR 139,43\nAR 140,44\nAR 141,45\nAR 142,46\nAR 143,47\n" \
"v_pk_mul_f32 v[136:137], v[136:137], v[196:197] op_sel:[0,1] op_sel_hi:[1,1]\nv_pk_mul_f32 v[138:139], v[138:139], v[196:197] op_sel:[0,1] op_sel_hi:[1,1]\nv_pk_mul_f32 v[140:141], v[140:141], v[196:197] op_sel:[0,1] op_sel_hi:[1,1]\nv_pk_mul_f32 v[142:143], v[142:143], v[196:197] op_sel:[0,1] op_sel_hi:[1,1]\nC 52,136,137\nC 53,138,139\nC 54,140,141\nC 55,142,143\n" \
"buffer_store_dwordx4 v[52:55], v0, s[60:63], s95 offen offset:1024 sc1\ns_add_u32 s95, s83, 40960\nAR 128,48\nAR 129,49\nAR 130,50\nAR 131,51\nAR 132,52\nAR 133,53\n" \
"AR 134,54\nAR 135,55\nv_pk_mul_f32 v[128:129], v[128:129], v[196:197] op_sel:[0,1] op_sel_hi:[1,1]\nv_pk_mul_f32 v[130:131], v[130:131], v[196:197] op_sel:[0,1] op_sel_hi:[1,1]\nv_pk_mul_f32 v[132:133], v[132:133], v[196:197] op_sel:[0,1] op_sel_hi:[1,1]\nv_pk_mul_f32 v[134:135], v[134:135], v[196:197] op_sel:[0,1] op_sel_hi:[1,1]\nC 56,128,129\nC 57,130,131\n" \
"C 58,132,133\nC 59,134,135\nbuffer_store_dwordx4 v[56:59], v0, s[60:63], s95 offen offset:0 sc1\nAR 136,56\nAR 137,57\nAR 138,58\nAR 139,59\nAR 140,60\n" \
"AR 141,61\nAR 142,62\nAR 143,63\nv_pk_mul_f32 v[136:137], v[136:137], v[196:197] op_sel:[0,1] op_sel_hi:[1,1]\nv_pk_mul_f32 v[138:139], v[138:139], v[196:197] op_sel:[0,1] op_sel_hi:[1,1]\nv_pk_mul_f32 v[140:141], v[140:141], v[196:197] op_sel:[0,1] op_sel_hi:[1,1]\nv_pk_mul_f32 v[142:143], v[142:143], v[196:197] op_sel:[0,1] op_sel_hi:[1,1]\nC 60,136,137\n" \
"C 61,138,139\nC 62,140,141\nC 63,142,143\nbuffer_store_dwordx4 v[60:63], v0, s[60:63], s95 offen offset:1024 sc1\ns_branch zE3\nzHs:\ns_add_u32 s96, s96, 2\ns_branch zH\n" \
"zL:\ns_waitcnt vmcnt(5)\ns_mov_b32 s96, s40\ns_mov_b32 s40, 0\nv_cmp_ne_u64 vcc, v[10:11], v[2:3]\ns_cbranch_vccz zO\ns_mov_b32 s94, 0x400000\nzt99:\n" \
"s_sleep 2\nbuffer_load_dwordx2 v[10:11], v1, s[60:63], 0 offen offset:64 sc1\ns_waitcnt vmcnt(0)\nv_cmp_ne_u64 vcc, v[10:11], v[2:3]\ns_cbranch_vccz zO\ns_sub_u32 s94, s94, 1\ns_cbranch_scc0 zt99\ns_branch zO\n" \
"zs101:\ns_mov_b32 s94, 0x400000\nzt101:\ns_sleep 2\nbuffer_load_dwordx2 v[10:11], v1, s[60:63], 0 offen offset:0 sc1\ns_waitcnt vmcnt(0)\nv_cmp_ne_u64 vcc, v[10:11], v[2:3]\ns_cbranch_vccz zr101\n" \
"s_sub_u32 s94, s94, 1\ns_cbranch_scc0 zt101\ns_branch zr101\nzs102:\ns_mov_b32 s94, 0x400000\nzt102:\ns_sleep 2\nbuffer_load_dwordx2 v[10:11], v1, s[60:63], 0 offen offset:0 sc1\n" \
"s_waitcnt vmcnt(0)\nv_cmp_ne_u64 vcc, v[10:11], v[2:3]\ns_cbranch_vccz zr102\ns_sub_u32 s94, s94, 1\ns_cbranch_scc0 zt102\ns_branch zr102\nzs103:\ns_mov_b32 s94, 0x400000\n" \
"zt103:\ns_sleep 2\nbuffer_load_dwordx2 v[10:11], off, s[60:63], s49 sc1\ns_waitcnt vmcnt(0)\nv_cmp_ne_u64 vcc, v[10:11], v[2:3]\ns_cbranch_vccz zr103\ns_sub_u32 s94, s94, 1\ns_cbranch_scc0 zt103\n" \
"s_branch zr103\nzs104:\ns_mov_b32 s94, 0x400000\nzt104:\nbuffer_load_dwordx2 v[10:11], v1, s[60:63], 0 offen sc1\ns_waitcnt vmcnt(0)\nv_cmp_eq_u64 vcc, v[10:11], v[2:3]\ns_andn2_b32 s82, s7, vcc_hi\n" \
"s_cselect_b64 vcc, 0, vcc\ns_bitcmp1_b64 vcc, s79\ns_cbranch_scc1 zr104\ns_sleep 1\ns_sub_u32 s94, s94, 1\ns_cbranch_scc0 zt104\ns_branch zr104\nzs105:\n" \
"s_mov_b32 s94, 0x400000\nzt105:\nbuffer_load_dwordx2 v[10:11], v1, s[60:63], 0 offen sc1\ns_waitcnt vmcnt(0)\nv_cmp_eq_u64 vcc, v[10:11], v[2:3]\ns_bitcmp1_b64 vcc, s79\ns_cbranch_scc1 zr105\ns_sleep 1\n" \
"s_sub_u32 s94, s94, 1\ns_cbranch_scc0 zt105\ns_branch zr105\nzs106:\ns_mov_b32 s94, 0x400000\nzt106:\nbuffer_load_dwordx2 v[10:11], v7, s[60:63], 0 offen sc1\ns_waitcnt vmcnt(0)\n" \
"v_cmp_eq_u64 vcc, v[10:11], v[2:3]\ns_andn2_b32 s82, 10, vcc_lo\ns_cbranch_scc0 zr106\ns_sleep 1\ns_sub_u32 s94, s94, 1\ns_cbranch_scc0 zt106\ns_branch zr106\nzpl:\n" \
"s_mov_b32 s94, 0x200000\nzpm:\nbuffer_load_dwordx2 v[10:11], v7, s[60:63], 0 offen sc1\ns_waitcnt vmcnt(2)\nv_cmp_eq_u64 vcc, v[102:103], v[2:3]\ns_andn2_b32 s82, s6, vcc_lo\ns_cbranch_scc0 zpe\nbuffer_load_dwordx2 v[102:103], v7, s[60:63], 0 offen sc1\n" \
"s_waitcnt vmcnt(2)\nv_cmp_eq_u64 vcc, v[120:121], v[2:3]\ns_andn2_b32 s82, s6, vcc_lo\ns_cbranch_scc0 zpe\nbuffer_load_dwordx2 v[120:121], v7, s[60:63], 0 offen sc1\ns_waitcnt vmcnt(2)\nv_cmp_eq_u64 vcc, v[10:11], v[2:3]\ns_andn2_b32 s82, s6, vcc_lo\n" \
"s_cbranch_scc0 zpe\ns_sub_u32 s94, s94, 1\ns_cbranch_scc0 zpm\ns_branch zpe\nzs107:\ns_mov_b32 s94, 0x400000\nzt107:\nbuffer_load_dwordx2 v[10:11], v1, s[60:63], 0 offen sc1\n" \
"s_waitcnt vmcnt(0)\nv_cmp_eq_u64 vcc, v[10:11], v[2:3]\ns_bitcmp1_b64 vcc, s79\ns_cbranch_scc1 zr107\ns_sleep 1\ns_sub_u32 s94, s94, 1\ns_cbranch_scc0 zt107\ns_branch zr107\n" \
"zs108:\ns_mov_b32 s94, 0x400000\nzt108:\nbuffer_load_dwordx2 v[10:11], v1, s[60:63], 0 offen sc1\ns_waitcnt vmcnt(0)\nv_cmp_eq_u64 vcc, v[10:11], v[2:3]\ns_bitcmp1_b64 vcc, s79\ns_cbranch_scc1 zr108\n" \
"s_sleep 1\ns_sub_u32 s94, s94, 1\ns_cbranch_scc0 zt108\ns_branch zr108\nzs109:\ns_mov_b32 s94, 0x400000\nzt109:\nbuffer_load_dwordx2 v[10:11], v1, s[60:63], 0 offen sc1\n" \
"s_waitcnt vmcnt(0)\nv_cmp_eq_u64 vcc, v[10:11], v[2:3]\ns_bitcmp1_b64 vcc, s79\ns_cbranch_scc1 zr109\ns_sleep 1\ns_sub_u32 s94, s94, 1\ns_cbranch_scc0 zt109\ns_branch zr109\n" \
"zep:\nv_lshrrev_b32 v28, 1, v0\ns_mov_b32 s94, 0x400000\nzepl:\nbuffer_load_dwordx2 v[96:97], v28, s[60:63], 0 offen offset:256 sc1\nbuffer_load_dwordx2 v[98:99], v28, s[60:63], 0 offen offset:768 sc1\nbuffer_load_dwordx2 v[100:101], v28, s[60:63], 0 offen offset:1280 sc1\nbuffer_load_dwordx2 v[102:103], v28, s[60:63], 0 offen offset:1792 sc1\n" \
"s_waitcnt vmcnt(0)\nv_cmp_ne_u64 s[98:99], v[96:97], v[2:3]\nv_cmp_ne_u64 s[100:101], v[98:99], v[2:3]\nv_cmp_ne_u64 vcc, v[100:101], v[2:3]\ns_or_b64 s[98:99], s[98:99], s[100:101]\ns_or_b64 s[98:99], s[98:99], vcc\nv_cmp_ne_u64 vcc, v[102:103], v[2:3]\ns_or_b64 s[98:99], s[98:99], vcc\n" \
"s_cbranch_scc0 zepd\ns_sub_u32 s94, s94, 1\ns_cbranch_scc0 zepl\nzepd:\nbuffer_store_dwordx2 v[2:3], off, s[60:63], 0 sc1\ns_branch zeq\nzfb:\nv_cmp_lt_u32 vcc, 0x3c000000, v240\n" \
"s_cbranch_vccz zf0e\ns_add_u32 s96, s42, 2\ns_mov_b32 s77, s38\nv_mov_b32 v136, 0xff800000\nv_mov_b32 v138, 0\nv_add_u32 v140, 0x4000, v8\nv_accvgpr_write_b32 a0, 0\nv_accvgpr_write_b32 a1, 0\n" \
"v_accvgpr_write_b32 a2, 0\nv_accvgpr_write_b32 a3, 0\nv_accvgpr_write_b32 a4, 0\nv_accvgpr_write_b32 a5, 0\nv_accvgpr_write_b32 a6, 0\nv_accvgpr_write_b32 a7, 0\nv_accvgpr_write_b32 a8, 0\nv_accvgpr_write_b32 a9, 0\n" \
"v_accvgpr_write_b32 a10, 0\nv_accvgpr_write_b32 a11, 0\nv_accvgpr_write_b32 a12, 0\nv_accvgpr_write_b32 a13, 0\nv_accvgpr_write_b32 a14, 0\nv_accvgpr_write_b32 a15, 0\nv_accvgpr_write_b32 a16, 0\nv_accvgpr_write_b32 a17, 0\n" \
"v_accvgpr_write_b32 a18, 0\nv_accvgpr_write_b32 a19, 0\nv_accvgpr_write_b32 a20, 0\nv_accvgpr_write_b32 a21, 0\nv_accvgpr_write_b32 a22, 0\nv_accvgpr_write_b32 a23, 0\nv_accvgpr_write_b32 a24, 0\nv_accvgpr_write_b32 a25, 0\n" \
"v_accvgpr_write_b32 a26, 0\nv_accvgpr_write_b32 a27, 0\nv_accvgpr_write_b32 a28, 0\nv_accvgpr_write_b32 a29, 0\nv_accvgpr_write_b32 a30, 0\nv_accvgpr_write_b32 a31, 0\nzf0l:\ns_add_u32 s76, s77, 4096\n" \
"buffer_load_dwordx4 a[160:163], v0, s[60:63], s77 offen offset:0 sc1\nbuffer_load_dwordx4 a[164:167], v0, s[60:63], s77 offen offset:1024 sc1\nbuffer_load_dwordx4 a[168:171], v0, s[60:63], s77 offen offset:2048 sc1\nbuffer_load_dwordx4 a[172:175], v0, s[60:63], s77 offen offset:3072 sc1\nbuffer_load_dwordx4 a[176:179], v0, s[60:63], s76 offen offset:0 sc1\nbuffer_load_dwordx4 a[180:183], v0, s[60:63], s76 offen offset:1024 sc1\nbuffer_load_dwordx4 a[184:187], v0, s[60:63], s76 offen offset:2048 sc1\nbuffer_load_dwordx4 a[188:191], v0, s[60:63], s76 offen offset:3072 sc1\n" \
"buffer_load_dwordx4 a[128:131], v140, s[60:63], s77 offen offset:0 sc1\nbuffer_load_dwordx4 a[132:135], v140, s[60:63], s77 offen offset:1024 sc1\nbuffer_load_dwordx4 a[136:139], v140, s[60:63], s77 offen offset:2048 sc1\nbuffer_load_dwordx4 a[140:143], v140, s[60:63], s77 offen offset:3072 sc1\nbuffer_load_dwordx4 a[144:147], v140, s[60:63], s76 offen offset:0 sc1\nbuffer_load_dwordx4 a[148:151], v140, s[60:63], s76 offen offset:1024 sc1\nbuffer_load_dwordx4 a[152:155], v140, s[60:63], s76 offen offset:2048 sc1\nbuffer_load_dwordx4 a[156:159], v140, s[60:63], s76 offen offset:3072 sc1\n" \
"v_mov_b32 v144, v12\nv_mov_b32 v145, v12\nv_mov_b32 v146, v12\nv_mov_b32 v147, v12\nv_mov_b32 v148, v12\nv_mov_b32 v149, v12\nv_mov_b32 v150, v12\nv_mov_b32 v151, v12\n" \
"s_cmp_gt_u32 s96, 2\ns_cbranch_scc1 zf0m\nv_mov_b32 v144, v112\nv_mov_b32 v145, v113\nv_mov_b32 v146, v114\nv_mov_b32 v147, v115\nv_mov_b32 v148, v116\nv_mov_b32 v149, v117\n" \
"v_mov_b32 v150, v118\nv_mov_b32 v151, v119\ns_cmp_eq_u32 s96, 2\ns_cbranch_scc1 zf0m\nv_mov_b32 v144, v120\nv_mov_b32 v145, v121\nv_mov_b32 v146, v122\nv_mov_b32 v147, v123\n" \
"v_mov_b32 v148, v124\nv_mov_b32 v149, v125\nv_mov_b32 v150, v126\nv_mov_b32 v151, v127\nzf0m:\ns_waitcnt vmcnt(0)\nQvav 128,160,32,144\nMvav 128,164,36\n" \
"Mvav 128,168,40\nMvav 128,172,44\nQvav 132,176,32,148\nMvav 132,180,36\nMvav 132,184,40\nMvav 132,188,44\ns_nop 7\nv_max3_f32 v141, v128, v129, v130\n" \
"v_max3_f32 v142, v131, v132, v133\nv_max3_f32 v141, v141, v134, v135\nv_max3_f32 v141, v141, v142, v136\nv_mov_b32 v142, v141\ns_nop 1\nv_permlane16_swap_b32 v141, v142\ns_nop 0\nv_max_f32 v141, v141, v142\n" \
"v_mov_b32 v142, v141\ns_nop 1\nv_permlane32_swap_b32 v141, v142\ns_nop 0\nv_max_f32 v141, v141, v142\nv_sub_f32 v137, v136, v141\nE 137,137\nv_mov_b32 v136, v141\n" \
"v_sub_f32 v128, v128, v141\nv_sub_f32 v129, v129, v141\nv_sub_f32 v130, v130, v141\nv_sub_f32 v131, v131, v141\nv_sub_f32 v132, v132, v141\nv_sub_f32 v133, v133, v141\nv_sub_f32 v134, v134, v141\nv_sub_f32 v135, v135, v141\n" \
"s_branch 99f\n.p2align 12\nzp38:\ns_setpc_b64 s[98:99]\n99:\nE 128,128\nE 129,129\nE 130,130\n" \
"E 131,131\nE 132,132\nE 133,133\nE 134,134\nE 135,135\nv_mul_f32 v138, v138, v137\nC 192,128,129\nC 193,130,131\n" \
"C 194,132,133\nC 195,134,135\nv_add_f32 v138, v138, v128\nv_add_f32 v138, v138, v129\nv_add_f32 v138, v138, v130\nv_add_f32 v138, v138, v131\nv_add_f32 v138, v138, v132\nv_add_f32 v138, v138, v133\n" \
"v_add_f32 v138, v138, v134\nv_add_f32 v138, v138, v135\nAR 141,0\nv_mul_f32 v141, v141, v137\nAW 0,141\nAR 141,1\nv_mul_f32 v141, v141, v137\nAW 1,141\n" \
"AR 141,2\nv_mul_f32 v141, v141, v137\nAW 2,141\nAR 141,3\nv_mul_f32 v141, v141, v137\nAW 3,141\nAR 141,4\nv_mul_f32 v141, v141, v137\n" \
"AW 4,141\nAR 141,5\nv_mul_f32 v141, v141, v137\nAW 5,141\nAR 141,6\nv_mul_f32 v141, v141, v137\nAW 6,141\nAR 141,7\n" \
"v_mul_f32 v141, v141, v137\nAW 7,141\nAR 141,8\nv_mul_f32 v141, v141, v137\nAW 8,141\nAR 141,9\nv_mul_f32 v141, v141, v137\nAW 9,141\n" \
"AR 141,10\nv_mul_f32 v141, v141, v137\nAW 10,141\nAR 141,11\nv_mul_f32 v141, v141, v137\nAW 11,141\nAR 141,12\nv_mul_f32 v141, v141, v137\n" \
"AW 12,141\nAR 141,13\nv_mul_f32 v141, v141, v137\nAW 13,141\nAR 141,14\nv_mul_f32 v141, v141, v137\nAW 14,141\nAR 141,15\n" \
"v_mul_f32 v141, v141, v137\nAW 15,141\nAR 141,16\nv_mul_f32 v141, v141, v137\nAW 16,141\nAR 141,17\nv_mul_f32 v141, v141, v137\nAW 17,141\n" \
"AR 141,18\nv_mul_f32 v141, v141, v137\nAW 18,141\nAR 141,19\nv_mul_f32 v141, v141, v137\nAW 19,141\nAR 141,20\nv_mul_f32 v141, v141, v137\n" \
"AW 20,141\nAR 141,21\nv_mul_f32 v141, v141, v137\nAW 21,141\nAR 141,22\nv_mul_f32 v141, v141, v137\nAW 22,141\nAR 141,23\n" \
"v_mul_f32 v141, v141, v137\nAW 23,141\nAR 141,24\nv_mul_f32 v141, v141, v137\nAW 24,141\nAR 141,25\nv_mul_f32 v141, v141, v137\nAW 25,141\n" \
"AR 141,26\nv_mul_f32 v141, v141, v137\nAW 26,141\nAR 141,27\nv_mul_f32 v141, v141, v137\nAW 27,141\nAR 141,28\nv_mul_f32 v141, v141, v137\n" \
"AW 28,141\nAR 141,29\nv_mul_f32 v141, v141, v137\nAW 29,141\nAR 141,30\nv_mul_f32 v141, v141, v137\nAW 30,141\nAR 141,31\n" \
"v_mul_f32 v141, v141, v137\nAW 31,141\ns_nop 1\nMaav 0,128,192\nMaav 4,132,192\nMaav 8,136,192\nMaav 12,140,192\nMaav 16,144,192\n" \
"Maav 20,148,192\nMaav 24,152,192\nMaav 28,156,192\ns_add_u32 s77, s77, 8192\ns_sub_u32 s96, s96, 1\ns_cmp_lg_u32 s96, 0\ns_nop 7\ns_cbranch_scc1 zf0l\n" \
"v_mov_b32 v141, v138\ns_nop 1\nv_permlane16_swap_b32 v138, v141\ns_nop 0\nv_add_f32 v138, v138, v141\nv_mov_b32 v141, v138\ns_nop 1\nv_permlane32_swap_b32 v138, v141\n" \
"s_nop 0\nv_add_f32 v138, v138, v141\nv_mov_b32 v104, v138\nzf0e:\nv_cmp_lt_u32 vcc, 0x3c000000, v241\ns_cbranch_vccz zf1e\ns_add_u32 s96, s42, 4\ns_mov_b32 s77, s38\n" \
"v_mov_b32 v136, 0xff800000\nv_mov_b32 v138, 0\nv_add_u32 v140, 0x4000, v8\nv_accvgpr_write_b32 a32, 0\nv_accvgpr_write_b32 a33, 0\nv_accvgpr_write_b32 a34, 0\nv_accvgpr_write_b32 a35, 0\nv_accvgpr_write_b32 a36, 0\n" \
"v_accvgpr_write_b32 a37, 0\nv_accvgpr_write_b32 a38, 0\nv_accvgpr_write_b32 a39, 0\nv_accvgpr_write_b32 a40, 0\nv_accvgpr_write_b32 a41, 0\nv_accvgpr_write_b32 a42, 0\nv_accvgpr_write_b32 a43, 0\nv_accvgpr_write_b32 a44, 0\n" \
"v_accvgpr_write_b32 a45, 0\nv_accvgpr_write_b32 a46, 0\nv_accvgpr_write_b32 a47, 0\nv_accvgpr_write_b32 a48, 0\nv_accvgpr_write_b32 a49, 0\nv_accvgpr_write_b32 a50, 0\nv_accvgpr_write_b32 a51, 0\nv_accvgpr_write_b32 a52, 0\n" \
"v_accvgpr_write_b32 a53, 0\nv_accvgpr_write_b32 a54, 0\nv_accvgpr_write_b32 a55, 0\nv_accvgpr_write_b32 a56, 0\nv_accvgpr_write_b32 a57, 0\nv_accvgpr_write_b32 a58, 0\nv_accvgpr_write_b32 a59, 0\nv_accvgpr_write_b32 a60, 0\n" \
"v_accvgpr_write_b32 a61, 0\nv_accvgpr_write_b32 a62, 0\nv_accvgpr_write_b32 a63, 0\nzf1l:\ns_add_u32 s76, s77, 4096\nbuffer_load_dwordx4 a[160:163], v0, s[60:63], s77 offen offset:0 sc1\nbuffer_load_dwordx4 a[164:167], v0, s[60:63], s77 offen offset:1024 sc1\nbuffer_load_dwordx4 a[168:171], v0, s[60:63], s77 offen offset:2048 sc1\n" \
"buffer_load_dwordx4 a[172:175], v0, s[60:63], s77 offen offset:3072 sc1\nbuffer_load_dwordx4 a[176:179], v0, s[60:63], s76 offen offset:0 sc1\nbuffer_load_dwordx4 a[180:183], v0, s[60:63], s76 offen offset:1024 sc1\nbuffer_load_dwordx4 a[184:187], v0, s[60:63], s76 offen offset:2048 sc1\nbuffer_load_dwordx4 a[188:191], v0, s[60:63], s76 offen offset:3072 sc1\nbuffer_load_dwordx4 a[128:131], v140, s[60:63], s77 offen offset:0 sc1\nbuffer_load_dwordx4 a[132:135], v140, s[60:63], s77 offen offset:1024 sc1\nbuffer_load_dwordx4 a[136:139], v140, s[60:63], s77 offen offset:2048 sc1\n" \
"buffer_load_dwordx4 a[140:143], v140, s[60:63], s77 offen offset:3072 sc1\nbuffer_load_dwordx4 a[144:147], v140, s[60:63], s76 offen offset:0 sc1\nbuffer_load_dwordx4 a[148:151], v140, s[60:63], s76 offen offset:1024 sc1\nbuffer_load_dwordx4 a[152:155], v140, s[60:63], s76 offen offset:2048 sc1\nbuffer_load_dwordx4 a[156:159], v140, s[60:63], s76 offen offset:3072 sc1\nv_mov_b32 v144, v12\nv_mov_b32 v145, v12\nv_mov_b32 v146, v12\n" \
"v_mov_b32 v147, v12\nv_mov_b32 v148, v12\nv_mov_b32 v149, v12\nv_mov_b32 v150, v12\nv_mov_b32 v151, v12\ns_cmp_gt_u32 s96, 2\ns_cbranch_scc1 zf1m\nv_mov_b32 v144, v112\n" \
"v_mov_b32 v145, v113\nv_mov_b32 v146, v114\nv_mov_b32 v147, v115\nv_mov_b32 v148, v116\nv_mov_b32 v149, v117\nv_mov_b32 v150, v118\nv_mov_b32 v151, v119\ns_cmp_eq_u32 s96, 2\n" \
"s_cbranch_scc1 zf1m\nv_mov_b32 v144, v120\nv_mov_b32 v145, v121\nv_mov_b32 v146, v122\nv_mov_b32 v147, v123\nv_mov_b32 v148, v124\nv_mov_b32 v149, v125\nv_mov_b32 v150, v126\n" \
"v_mov_b32 v151, v127\nzf1m:\ns_waitcnt vmcnt(0)\nQvav 128,160,48,144\nMvav 128,164,52\nMvav 128,168,56\nMvav 128,172,60\nQvav 132,176,48,148\n" \
"Mvav 132,180,52\nMvav 132,184,56\nMvav 132,188,60\ns_nop 7\nv_max3_f32 v141, v128, v129, v130\nv_max3_f32 v142, v131, v132, v133\nv_max3_f32 v141, v141, v134, v135\nv_max3_f32 v141, v141, v142, v136\n" \
"v_mov_b32 v142, v141\ns_nop 1\nv_permlane16_swap_b32 v141, v142\ns_nop 0\nv_max_f32 v141, v141, v142\nv_mov_b32 v142, v141\ns_nop 1\nv_permlane32_swap_b32 v141, v142\n" \
"s_nop 0\nv_max_f32 v141, v141, v142\nv_sub_f32 v137, v136, v141\nE 137,137\nv_mov_b32 v136, v141\nv_sub_f32 v128, v128, v141\nv_sub_f32 v129, v129, v141\nv_sub_f32 v130, v130, v141\n" \
"v_sub_f32 v131, v131, v141\nv_sub_f32 v132, v132, v141\nv_sub_f32 v133, v133, v141\nv_sub_f32 v134, v134, v141\nv_sub_f32 v135, v135, v141\nE 128,128\nE 129,129\nE 130,130\n" \
"E 131,131\nE 132,132\nE 133,133\nE 134,134\nE 135,135\nv_mul_f32 v138, v138, v137\nC 192,128,129\nC 193,130,131\n" \
"C 194,132,133\nC 195,134,135\nv_add_f32 v138, v138, v128\nv_add_f32 v138, v138, v129\nv_add_f32 v138, v138, v130\nv_add_f32 v138, v138, v131\nv_add_f32 v138, v138, v132\nv_add_f32 v138, v138, v133\n" \
"v_add_f32 v138, v138, v134\nv_add_f32 v138, v138, v135\nAR 141,32\nv_mul_f32 v141, v141, v137\nAW 32,141\nAR 141,33\nv_mul_f32 v141, v141, v137\nAW 33,141\n" \
"AR 141,34\nv_mul_f32 v141, v141, v137\nAW 34,141\nAR 141,35\nv_mul_f32 v141, v141, v137\nAW 35,141\nAR 141,36\nv_mul_f32 v141, v141, v137\n" \
"AW 36,141\nAR 141,37\nv_mul_f32 v141, v141, v137\nAW 37,141\nAR 141,38\nv_mul_f32 v141, v141, v137\nAW 38,141\nAR 141,39\n" \
"v_mul_f32 v141, v141, v137\nAW 39,141\nAR 141,40\nv_mul_f32 v141, v141, v137\nAW 40,141\nAR 141,41\nv_mul_f32 v141, v141, v137\nAW 41,141\n" \
"AR 141,42\nv_mul_f32 v141, v141, v137\nAW 42,141\nAR 141,43\nv_mul_f32 v141, v141, v137\nAW 43,141\nAR 141,44\nv_mul_f32 v141, v141, v137\n" \
"AW 44,141\nAR 141,45\nv_mul_f32 v141, v141, v137\nAW 45,141\nAR 141,46\nv_mul_f32 v141, v141, v137\nAW 46,141\nAR 141,47\n" \
"v_mul_f32 v141, v141, v137\nAW 47,141\nAR 141,48\nv_mul_f32 v141, v141, v137\nAW 48,141\nAR 141,49\nv_mul_f32 v141, v141, v137\nAW 49,141\n" \
"AR 141,50\nv_mul_f32 v141, v141, v137\nAW 50,141\nAR 141,51\nv_mul_f32 v141, v141, v137\nAW 51,141\nAR 141,52\nv_mul_f32 v141, v141, v137\n" \
"AW 52,141\nAR 141,53\nv_mul_f32 v141, v141, v137\nAW 53,141\nAR 141,54\nv_mul_f32 v141, v141, v137\nAW 54,141\nAR 141,55\n" \
"v_mul_f32 v141, v141, v137\nAW 55,141\nAR 141,56\nv_mul_f32 v141, v141, v137\nAW 56,141\nAR 141,57\nv_mul_f32 v141, v141, v137\nAW 57,141\n" \
"AR 141,58\nv_mul_f32 v141, v141, v137\nAW 58,141\nAR 141,59\nv_mul_f32 v141, v141, v137\nAW 59,141\nAR 141,60\nv_mul_f32 v141, v141, v137\n" \
"AW 60,141\nAR 141,61\nv_mul_f32 v141, v141, v137\nAW 61,141\nAR 141,62\nv_mul_f32 v141, v141, v137\nAW 62,141\nAR 141,63\n" \
"v_mul_f32 v141, v141, v137\nAW 63,141\ns_nop 1\nMaav 32,128,192\nMaav 36,132,192\nMaav 40,136,192\nMaav 44,140,192\nMaav 48,144,192\n" \
"Maav 52,148,192\nMaav 56,152,192\nMaav 60,156,192\ns_add_u32 s77, s77, 8192\ns_sub_u32 s96, s96, 1\ns_cmp_lg_u32 s96, 0\ns_nop 7\ns_cbranch_scc1 zf1l\n" \
"v_mov_b32 v141, v138\ns_nop 1\nv_permlane16_swap_b32 v138, v141\ns_nop 0\nv_add_f32 v138, v138, v141\nv_mov_b32 v141, v138\ns_nop 1\nv_permlane32_swap_b32 v138, v141\n" \
"s_nop 0\nv_add_f32 v138, v138, v141\nv_mov_b32 v105, v138\nzf1e:\nv_cmp_lt_u32 vcc, 0x3c000000, v242\ns_cbranch_vccz zf2e\ns_add_u32 s96, s43, 2\ns_mov_b32 s77, s38\n" \
"v_mov_b32 v136, 0xff800000\nv_mov_b32 v138, 0\nv_add_u32 v140, 0x4000, v8\nv_accvgpr_write_b32 a64, 0\nv_accvgpr_write_b32 a65, 0\nv_accvgpr_write_b32 a66, 0\nv_accvgpr_write_b32 a67, 0\nv_accvgpr_write_b32 a68, 0\n" \
"v_accvgpr_write_b32 a69, 0\nv_accvgpr_write_b32 a70, 0\nv_accvgpr_write_b32 a71, 0\nv_accvgpr_write_b32 a72, 0\nv_accvgpr_write_b32 a73, 0\nv_accvgpr_write_b32 a74, 0\nv_accvgpr_write_b32 a75, 0\nv_accvgpr_write_b32 a76, 0\n" \
"v_accvgpr_write_b32 a77, 0\nv_accvgpr_write_b32 a78, 0\nv_accvgpr_write_b32 a79, 0\nv_accvgpr_write_b32 a80, 0\nv_accvgpr_write_b32 a81, 0\nv_accvgpr_write_b32 a82, 0\nv_accvgpr_write_b32 a83, 0\nv_accvgpr_write_b32 a84, 0\n" \
"v_accvgpr_write_b32 a85, 0\nv_accvgpr_write_b32 a86, 0\nv_accvgpr_write_b32 a87, 0\nv_accvgpr_write_b32 a88, 0\nv_accvgpr_write_b32 a89, 0\nv_accvgpr_write_b32 a90, 0\nv_accvgpr_write_b32 a91, 0\nv_accvgpr_write_b32 a92, 0\n" \
"v_accvgpr_write_b32 a93, 0\nv_accvgpr_write_b32 a94, 0\nv_accvgpr_write_b32 a95, 0\nzf2l:\ns_add_u32 s76, s77, 4096\nbuffer_load_dwordx4 a[160:163], v0, s[60:63], s77 offen offset:0 sc1\nbuffer_load_dwordx4 a[164:167], v0, s[60:63], s77 offen offset:1024 sc1\nbuffer_load_dwordx4 a[168:171], v0, s[60:63], s77 offen offset:2048 sc1\n" \
"buffer_load_dwordx4 a[172:175], v0, s[60:63], s77 offen offset:3072 sc1\nbuffer_load_dwordx4 a[176:179], v0, s[60:63], s76 offen offset:0 sc1\nbuffer_load_dwordx4 a[180:183], v0, s[60:63], s76 offen offset:1024 sc1\nbuffer_load_dwordx4 a[184:187], v0, s[60:63], s76 offen offset:2048 sc1\nbuffer_load_dwordx4 a[188:191], v0, s[60:63], s76 offen offset:3072 sc1\nbuffer_load_dwordx4 a[128:131], v140, s[60:63], s77 offen offset:0 sc1\nbuffer_load_dwordx4 a[132:135], v140, s[60:63], s77 offen offset:1024 sc1\nbuffer_load_dwordx4 a[136:139], v140, s[60:63], s77 offen offset:2048 sc1\n" \
"buffer_load_dwordx4 a[140:143], v140, s[60:63], s77 offen offset:3072 sc1\nbuffer_load_dwordx4 a[144:147], v140, s[60:63], s76 offen offset:0 sc1\nbuffer_load_dwordx4 a[148:151], v140, s[60:63], s76 offen offset:1024 sc1\nbuffer_load_dwordx4 a[152:155], v140, s[60:63], s76 offen offset:2048 sc1\nbuffer_load_dwordx4 a[156:159], v140, s[60:63], s76 offen offset:3072 sc1\nv_mov_b32 v144, v12\nv_mov_b32 v145, v12\nv_mov_b32 v146, v12\n" \
"v_mov_b32 v147, v12\nv_mov_b32 v148, v12\nv_mov_b32 v149, v12\nv_mov_b32 v150, v12\nv_mov_b32 v151, v12\ns_cmp_gt_u32 s96, 2\ns_cbranch_scc1 zf2m\nv_mov_b32 v144, v112\n" \
"v_mov_b32 v145, v113\nv_mov_b32 v146, v114\nv_mov_b32 v147, v115\nv_mov_b32 v148, v116\nv_mov_b32 v149, v117\nv_mov_b32 v150, v118\nv_mov_b32 v151, v119\ns_cmp_eq_u32 s96, 2\n" \
"s_cbranch_scc1 zf2m\nv_mov_b32 v144, v120\nv_mov_b32 v145, v121\nv_mov_b32 v146, v122\nv_mov_b32 v147, v123\nv_mov_b32 v148, v124\nv_mov_b32 v149, v125\nv_mov_b32 v150, v126\n" \
"v_mov_b32 v151, v127\nzf2m:\ns_waitcnt vmcnt(0)\nQvav 128,160,64,144\nMvav 128,164,68\nMvav 128,168,72\nMvav 128,172,76\nQvav 132,176,64,148\n" \
"Mvav 132,180,68\nMvav 132,184,72\nMvav 132,188,76\ns_nop 7\nv_max3_f32 v141, v128, v129, v130\nv_max3_f32 v142, v131, v132, v133\nv_max3_f32 v141, v141, v134, v135\nv_max3_f32 v141, v141, v142, v136\n" \
"v_mov_b32 v142, v141\ns_nop 1\nv_permlane16_swap_b32 v141, v142\ns_nop 0\nv_max_f32 v141, v141, v142\nv_mov_b32 v142, v141\ns_nop 1\nv_permlane32_swap_b32 v141, v142\n" \
"s_nop 0\nv_max_f32 v141, v141, v142\nv_sub_f32 v137, v136, v141\nE 137,137\nv_mov_b32 v136, v141\nv_sub_f32 v128, v128, v141\nv_sub_f32 v129, v129, v141\nv_sub_f32 v130, v130, v141\n" \
"v_sub_f32 v131, v131, v141\nv_sub_f32 v132, v132, v141\nv_sub_f32 v133, v133, v141\nv_sub_f32 v134, v134, v141\nv_sub_f32 v135, v135, v141\nE 128,128\nE 129,129\nE 130,130\n" \
"E 131,131\nE 132,132\nE 133,133\nE 134,134\nE 135,135\nv_mul_f32 v138, v138, v137\nC 192,128,129\nC 193,130,131\n" \
"C 194,132,133\nC 195,134,135\nv_add_f32 v138, v138, v128\nv_add_f32 v138, v138, v129\nv_add_f32 v138, v138, v130\nv_add_f32 v138, v138, v131\nv_add_f32 v138, v138, v132\nv_add_f32 v138, v138, v133\n" \
"v_add_f32 v138, v138, v134\nv_add_f32 v138, v138, v135\nAR 141,64\nv_mul_f32 v141, v141, v137\nAW 64,141\nAR 141,65\nv_mul_f32 v141, v141, v137\nAW 65,141\n" \
"AR 141,66\nv_mul_f32 v141, v141, v137\nAW 66,141\nAR 141,67\nv_mul_f32 v141, v141, v137\nAW 67,141\nAR 141,68\nv_mul_f32 v141, v141, v137\n" \
"AW 68,141\nAR 141,69\nv_mul_f32 v141, v141, v137\nAW 69,141\nAR 141,70\nv_mul_f32 v141, v141, v137\nAW 70,141\nAR 141,71\n" \
"v_mul_f32 v141, v141, v137\nAW 71,141\nAR 141,72\nv_mul_f32 v141, v141, v137\nAW 72,141\nAR 141,73\nv_mul_f32 v141, v141, v137\nAW 73,141\n" \
"AR 141,74\nv_mul_f32 v141, v141, v137\nAW 74,141\nAR 141,75\nv_mul_f32 v141, v141, v137\nAW 75,141\nAR 141,76\nv_mul_f32 v141, v141, v137\n" \
"AW 76,141\nAR 141,77\nv_mul_f32 v141, v141, v137\nAW 77,141\nAR 141,78\nv_mul_f32 v141, v141, v137\nAW 78,141\nAR 141,79\n" \
"v_mul_f32 v141, v141, v137\nAW 79,141\nAR 141,80\nv_mul_f32 v141, v141, v137\nAW 80,141\nAR 141,81\nv_mul_f32 v141, v141, v137\nAW 81,141\n" \
"AR 141,82\nv_mul_f32 v141, v141, v137\nAW 82,141\nAR 141,83\nv_mul_f32 v141, v141, v137\nAW 83,141\nAR 141,84\nv_mul_f32 v141, v141, v137\n" \
"AW 84,141\nAR 141,85\nv_mul_f32 v141, v141, v137\nAW 85,141\nAR 141,86\nv_mul_f32 v141, v141, v137\nAW 86,141\nAR 141,87\n" \
"v_mul_f32 v141, v141, v137\nAW 87,141\nAR 141,88\nv_mul_f32 v141, v141, v137\nAW 88,141\nAR 141,89\nv_mul_f32 v141, v141, v137\nAW 89,141\n" \
"AR 141,90\nv_mul_f32 v141, v141, v137\nAW 90,141\nAR 141,91\nv_mul_f32 v141, v141, v137\nAW 91,141\nAR 141,92\nv_mul_f32 v141, v141, v137\n" \
"AW 92,141\nAR 141,93\nv_mul_f32 v141, v141, v137\nAW 93,141\nAR 141,94\nv_mul_f32 v141, v141, v137\nAW 94,141\nAR 141,95\n" \
"v_mul_f32 v141, v141, v137\nAW 95,141\ns_nop 1\nMaav 64,128,192\nMaav 68,132,192\nMaav 72,136,192\nMaav 76,140,192\nMaav 80,144,192\n" \
"Maav 84,148,192\nMaav 88,152,192\nMaav 92,156,192\ns_add_u32 s77, s77, 8192\ns_sub_u32 s96, s96, 1\ns_cmp_lg_u32 s96, 0\ns_nop 7\ns_cbranch_scc1 zf2l\n" \
"v_mov_b32 v141, v138\ns_nop 1\nv_permlane16_swap_b32 v138, v141\ns_nop 0\nv_add_f32 v138, v138, v141\nv_mov_b32 v141, v138\ns_nop 1\nv_permlane32_swap_b32 v138, v141\n" \
"s_nop 0\nv_add_f32 v138, v138, v141\ns_branch 99f\n.p2align 12\nzp39:\ns_setpc_b64 s[98:99]\n99:\nv_mov_b32 v106, v138\n" \
"zf2e:\nv_cmp_lt_u32 vcc, 0x3c000000, v243\ns_cbranch_vccz zf3e\ns_add_u32 s96, s43, 4\ns_mov_b32 s77, s38\nv_mov_b32 v136, 0xff800000\nv_mov_b32 v138, 0\nv_add_u32 v140, 0x4000, v8\n" \
"v_accvgpr_write_b32 a96, 0\nv_accvgpr_write_b32 a97, 0\nv_accvgpr_write_b32 a98, 0\nv_accvgpr_write_b32 a99, 0\nv_accvgpr_write_b32 a100, 0\nv_accvgpr_write_b32 a101, 0\nv_accvgpr_write_b32 a102, 0\nv_accvgpr_write_b32 a103, 0\n" \
"v_accvgpr_write_b32 a104, 0\nv_accvgpr_write_b32 a105, 0\nv_accvgpr_write_b32 a106, 0\nv_accvgpr_write_b32 a107, 0\nv_accvgpr_write_b32 a108, 0\nv_accvgpr_write_b32 a109, 0\nv_accvgpr_write_b32 a110, 0\nv_accvgpr_write_b32 a111, 0\n" \
"v_accvgpr_write_b32 a112, 0\nv_accvgpr_write_b32 a113, 0\nv_accvgpr_write_b32 a114, 0\nv_accvgpr_write_b32 a115, 0\nv_accvgpr_write_b32 a116, 0\nv_accvgpr_write_b32 a117, 0\nv_accvgpr_write_b32 a118, 0\nv_accvgpr_write_b32 a119, 0\n" \
"v_accvgpr_write_b32 a120, 0\nv_accvgpr_write_b32 a121, 0\nv_accvgpr_write_b32 a122, 0\nv_accvgpr_write_b32 a123, 0\nv_accvgpr_write_b32 a124, 0\nv_accvgpr_write_b32 a125, 0\nv_accvgpr_write_b32 a126, 0\nv_accvgpr_write_b32 a127, 0\n" \
"zf3l:\ns_add_u32 s76, s77, 4096\nbuffer_load_dwordx4 a[160:163], v0, s[60:63], s77 offen offset:0 sc1\nbuffer_load_dwordx4 a[164:167], v0, s[60:63], s77 offen offset:1024 sc1\nbuffer_load_dwordx4 a[168:171], v0, s[60:63], s77 offen offset:2048 sc1\nbuffer_load_dwordx4 a[172:175], v0, s[60:63], s77 offen offset:3072 sc1\nbuffer_load_dwordx4 a[176:179], v0, s[60:63], s76 offen offset:0 sc1\nbuffer_load_dwordx4 a[180:183], v0, s[60:63], s76 offen offset:1024 sc1\n" \
"buffer_load_dwordx4 a[184:187], v0, s[60:63], s76 offen offset:2048 sc1\nbuffer_load_dwordx4 a[188:191], v0, s[60:63], s76 offen offset:3072 sc1\nbuffer_load_dwordx4 a[128:131], v140, s[60:63], s77 offen offset:0 sc1\nbuffer_load_dwordx4 a[132:135], v140, s[60:63], s77 offen offset:1024 sc1\nbuffer_load_dwordx4 a[136:139], v140, s[60:63], s77 offen offset:2048 sc1\nbuffer_load_dwordx4 a[140:143], v140, s[60:63], s77 offen offset:3072 sc1\nbuffer_load_dwordx4 a[144:147], v140, s[60:63], s76 offen offset:0 sc1\nbuffer_load_dwordx4 a[148:151], v140, s[60:63], s76 offen offset:1024 sc1\n" \
"buffer_load_dwordx4 a[152:155], v140, s[60:63], s76 offen offset:2048 sc1\nbuffer_load_dwordx4 a[156:159], v140, s[60:63], s76 offen offset:3072 sc1\nv_mov_b32 v144, v12\nv_mov_b32 v145, v12\nv_mov_b32 v146, v12\nv_mov_b32 v147, v12\nv_mov_b32 v148, v12\nv_mov_b32 v149, v12\n" \
"v_mov_b32 v150, v12\nv_mov_b32 v151, v12\ns_cmp_gt_u32 s96, 2\ns_cbranch_scc1 zf3m\nv_mov_b32 v144, v112\nv_mov_b32 v145, v113\nv_mov_b32 v146, v114\nv_mov_b32 v147, v115\n" \
"v_mov_b32 v148, v116\nv_mov_b32 v149, v117\nv_mov_b32 v150, v118\nv_mov_b32 v151, v119\ns_cmp_eq_u32 s96, 2\ns_cbranch_scc1 zf3m\nv_mov_b32 v144, v120\nv_mov_b32 v145, v121\n" \
"v_mov_b32 v146, v122\nv_mov_b32 v147, v123\nv_mov_b32 v148, v124\nv_mov_b32 v149, v125\nv_mov_b32 v150, v126\nv_mov_b32 v151, v127\nzf3m:\ns_waitcnt vmcnt(0)\n" \
"Qvav 128,160,80,144\nMvav 128,164,84\nMvav 128,168,88\nMvav 128,172,92\nQvav 132,176,80,148\nMvav 132,180,84\nMvav 132,184,88\nMvav 132,188,92\n" \
"s_nop 7\nv_max3_f32 v141, v128, v129, v130\nv_max3_f32 v142, v131, v132, v133\nv_max3_f32 v141, v141, v134, v135\nv_max3_f32 v141, v141, v142, v136\nv_mov_b32 v142, v141\ns_nop 1\nv_permlane16_swap_b32 v141, v142\n" \
"s_nop 0\nv_max_f32 v141, v141, v142\nv_mov_b32 v142, v141\ns_nop 1\nv_permlane32_swap_b32 v141, v142\ns_nop 0\nv_max_f32 v141, v141, v142\nv_sub_f32 v137, v136, v141\n" \
"E 137,137\nv_mov_b32 v136, v141\nv_sub_f32 v128, v128, v141\nv_sub_f32 v129, v129, v141\nv_sub_f32 v130, v130, v141\nv_sub_f32 v131, v131, v141\nv_sub_f32 v132, v132, v141\nv_sub_f32 v133, v133, v141\n" \
"v_sub_f32 v134, v134, v141\nv_sub_f32 v135, v135, v141\nE 128,128\nE 129,129\nE 130,130\nE 131,131\nE 132,132\nE 133,133\n" \
"E 134,134\nE 135,135\nv_mul_f32 v138, v138, v137\nC 192,128,129\nC 193,130,131\nC 194,132,133\nC 195,134,135\nv_add_f32 v138, v138, v128\n" \
"v_add_f32 v138, v138, v129\nv_add_f32 v138, v138, v130\nv_add_f32 v138, v138, v131\nv_add_f32 v138, v138, v132\nv_add_f32 v138, v138, v133\nv_add_f32 v138, v138, v134\nv_add_f32 v138, v138, v135\nAR 141,96\n" \
"v_mul_f32 v141, v141, v137\nAW 96,141\nAR 141,97\nv_mul_f32 v141, v141, v137\nAW 97,141\nAR 141,98\nv_mul_f32 v141, v141, v137\nAW 98,141\n" \
"AR 141,99\nv_mul_f32 v141, v141, v137\nAW 99,141\nAR 141,100\nv_mul_f32 v141, v141, v137\nAW 100,141\nAR 141,101\nv_mul_f32 v141, v141, v137\n" \
"AW 101,141\nAR 141,102\nv_mul_f32 v141, v141, v137\nAW 102,141\nAR 141,103\nv_mul_f32 v141, v141, v137\nAW 103,141\nAR 141,104\n" \
"v_mul_f32 v141, v141, v137\nAW 104,141\nAR 141,105\nv_mul_f32 v141, v141, v137\nAW 105,141\nAR 141,106\nv_mul_f32 v141, v141, v137\nAW 106,141\n" \
"AR 141,107\nv_mul_f32 v141, v141, v137\nAW 107,141\nAR 141,108\nv_mul_f32 v141, v141, v137\nAW 108,141\nAR 141,109\nv_mul_f32 v141, v141, v137\n" \
"AW 109,141\nAR 141,110\nv_mul_f32 v141, v141, v137\nAW 110,141\nAR 141,111\nv_mul_f32 v141, v141, v137\nAW 111,141\nAR 141,112\n" \
"v_mul_f32 v141, v141, v137\nAW 112,141\nAR 141,113\nv_mul_f32 v141, v141, v137\nAW 113,141\nAR 141,114\nv_mul_f32 v141, v141, v137\nAW 114,141\n" \
"AR 141,115\nv_mul_f32 v141, v141, v137\nAW 115,141\nAR 141,116\nv_mul_f32 v141, v141, v137\nAW 116,141\nAR 141,117\nv_mul_f32 v141, v141, v137\n" \
"AW 117,141\nAR 141,118\nv_mul_f32 v141, v141, v137\nAW 118,141\nAR 141,119\nv_mul_f32 v141, v141, v137\nAW 119,141\nAR 141,120\n" \
"v_mul_f32 v141, v141, v137\nAW 120,141\nAR 141,121\nv_mul_f32 v141, v141, v137\nAW 121,141\nAR 141,122\nv_mul_f32 v141, v141, v137\nAW 122,141\n" \
"AR 141,123\nv_mul_f32 v141, v141, v137\nAW 123,141\nAR 141,124\nv_mul_f32 v141, v141, v137\nAW 124,141\nAR 141,125\nv_mul_f32 v141, v141, v137\n" \
"AW 125,141\nAR 141,126\nv_mul_f32 v141, v141, v137\nAW 126,141\nAR 141,127\nv_mul_f32 v141, v141, v137\nAW 127,141\ns_nop 1\n" \
"Maav 96,128,192\nMaav 100,132,192\nMaav 104,136,192\nMaav 108,140,192\nMaav 112,144,192\nMaav 116,148,192\nMaav 120,152,192\nMaav 124,156,192\n" \
"s_add_u32 s77, s77, 8192\ns_sub_u32 s96, s96, 1\ns_cmp_lg_u32 s96, 0\ns_nop 7\ns_cbranch_scc1 zf3l\nv_mov_b32 v141, v138\ns_nop 1\nv_permlane16_swap_b32 v138, v141\n" \
"s_nop 0\nv_add_f32 v138, v138, v141\nv_mov_b32 v141, v138\ns_nop 1\nv_permlane32_swap_b32 v138, v141\ns_nop 0\nv_add_f32 v138, v138, v141\nv_mov_b32 v107, v138\n" \
"zf3e:\nv_rcp_f32 v196, v104\nv_rcp_f32 v197, v105\ns_nop 0\ns_add_u32 s95, s83, 0\nAR 128,0\nAR 129,1\nAR 130,2\n" \
"AR 131,3\nAR 132,4\nAR 133,5\nAR 134,6\nAR 135,7\nv_pk_mul_f32 v[128:129], v[128:129], v[196:197] op_sel:[0,0] op_sel_hi:[1,0]\nv_pk_mul_f32 v[130:131], v[130:131], v[196:197] op_sel:[0,0] op_sel_hi:[1,0]\nv_pk_mul_f32 v[132:133], v[132:133], v[196:197] op_sel:[0,0] op_sel_hi:[1,0]\n" \
"v_pk_mul_f32 v[134:135], v[134:135], v[196:197] op_sel:[0,0] op_sel_hi:[1,0]\nC 32,128,129\nC 33,130,131\nC 34,132,133\nC 35,134,135\nbuffer_store_dwordx4 v[32:35], v0, s[60:63], s95 offen offset:0 sc1\nAR 136,8\nAR 137,9\n" \
"AR 138,10\nAR 139,11\nAR 140,12\nAR 141,13\nAR 142,14\nAR 143,15\nv_pk_mul_f32 v[136:137], v[136:137], v[196:197] op_sel:[0,0] op_sel_hi:[1,0]\nv_pk_mul_f32 v[138:139], v[138:139], v[196:197] op_sel:[0,0] op_sel_hi:[1,0]\n" \
"v_pk_mul_f32 v[140:141], v[140:141], v[196:197] op_sel:[0,0] op_sel_hi:[1,0]\nv_pk_mul_f32 v[142:143], v[142:143], v[196:197] op_sel:[0,0] op_sel_hi:[1,0]\nC 36,136,137\nC 37,138,139\nC 38,140,141\nC 39,142,143\nbuffer_store_dwordx4 v[36:39], v0, s[60:63], s95 offen offset:1024 sc1\ns_add_u32 s95, s83, 32768\n" \
"AR 128,16\nAR 129,17\nAR 130,18\nAR 131,19\nAR 132,20\nAR 133,21\nAR 134,22\nAR 135,23\n" \
"v_pk_mul_f32 v[128:129], v[128:129], v[196:197] op_sel:[0,0] op_sel_hi:[1,0]\nv_pk_mul_f32 v[130:131], v[130:131], v[196:197] op_sel:[0,0] op_sel_hi:[1,0]\nv_pk_mul_f32 v[132:133], v[132:133], v[196:197] op_sel:[0,0] op_sel_hi:[1,0]\nv_pk_mul_f32 v[134:135], v[134:135], v[196:197] op_sel:[0,0] op_sel_hi:[1,0]\nC 40,128,129\nC 41,130,131\nC 42,132,133\nC 43,134,135\n" \
"buffer_store_dwordx4 v[40:43], v0, s[60:63], s95 offen offset:0 sc1\nAR 136,24\nAR 137,25\nAR 138,26\nAR 139,27\nAR 140,28\nAR 141,29\nAR 142,30\n" \
"AR 143,31\nv_pk_mul_f32 v[136:137], v[136:137], v[196:197] op_sel:[0,0] op_sel_hi:[1,0]\nv_pk_mul_f32 v[138:139], v[138:139], v[196:197] op_sel:[0,0] op_sel_hi:[1,0]\nv_pk_mul_f32 v[140:141], v[140:141], v[196:197] op_sel:[0,0] op_sel_hi:[1,0]\nv_pk_mul_f32 v[142:143], v[142:143], v[196:197] op_sel:[0,0] op_sel_hi:[1,0]\nC 44,136,137\nC 45,138,139\nC 46,140,141\n" \
"C 47,142,143\nbuffer_store_dwordx4 v[44:47], v0, s[60:63], s95 offen offset:1024 sc1\ns_add_u32 s95, s83, 8192\nAR 128,32\nAR 129,33\nAR 130,34\nAR 131,35\nAR 132,36\n" \
"AR 133,37\nAR 134,38\nAR 135,39\nv_pk_mul_f32 v[128:129], v[128:129], v[196:197] op_sel:[0,1] op_sel_hi:[1,1]\nv_pk_mul_f32 v[130:131], v[130:131], v[196:197] op_sel:[0,1] op_sel_hi:[1,1]\nv_pk_mul_f32 v[132:133], v[132:133], v[196:197] op_sel:[0,1] op_sel_hi:[1,1]\nv_pk_mul_f32 v[134:135], v[134:135], v[196:197] op_sel:[0,1] op_sel_hi:[1,1]\nC 48,128,129\n" \
"C 49,130,131\nC 50,132,133\nC 51,134,135\nbuffer_store_dwordx4 v[48:51], v0, s[60:63], s95 offen offset:0 sc1\nAR 136,40\nAR 137,41\nAR 138,42\nAR 139,43\n" \
"AR 140,44\nAR 141,45\nAR 142,46\nAR 143,47\nv_pk_mul_f32 v[136:137], v[136:137], v[196:197] op_sel:[0,1] op_sel_hi:[1,1]\nv_pk_mul_f32 v[138:139], v[138:139], v[196:197] op_sel:[0,1] op_sel_hi:[1,1]\nv_pk_mul_f32 v[140:141], v[140:141], v[196:197] op_sel:[0,1] op_sel_hi:[1,1]\nv_pk_mul_f32 v[142:143], v[142:143], v[196:197] op_sel:[0,1] op_sel_hi:[1,1]\n" \
"C 52,136,137\nC 53,138,139\nC 54,140,141\nC 55,142,143\nbuffer_store_dwordx4 v[52:55], v0, s[60:63], s95 offen offset:1024 sc1\ns_add_u32 s95, s83, 40960\nAR 128,48\nAR 129,49\n" \
"AR 130,50\nAR 131,51\nAR 132,52\nAR 133,53\nAR 134,54\nAR 135,55\nv_pk_mul_f32 v[128:129], v[128:129], v[196:197] op_sel:[0,1] op_sel_hi:[1,1]\nv_pk_mul_f32 v[130:131], v[130:131], v[196:197] op_sel:[0,1] op_sel_hi:[1,1]\n" \
"v_pk_mul_f32 v[132:133], v[132:133], v[196:197] op_sel:[0,1] op_sel_hi:[1,1]\nv_pk_mul_f32 v[134:135], v[134:135], v[196:197] op_sel:[0,1] op_sel_hi:[1,1]\nC 56,128,129\nC 57,130,131\nC 58,132,133\nC 59,134,135\nbuffer_store_dwordx4 v[56:59], v0, s[60:63], s95 offen offset:0 sc1\nAR 136,56\n" \
"AR 137,57\nAR 138,58\nAR 139,59\nAR 140,60\nAR 141,61\nAR 142,62\nAR 143,63\nv_pk_mul_f32 v[136:137], v[136:137], v[196:197] op_sel:[0,1] op_sel_hi:[1,1]\n" \
"v_pk_mul_f32 v[138:139], v[138:139], v[196:197] op_sel:[0,1] op_sel_hi:[1,1]\nv_pk_mul_f32 v[140:141], v[140:141], v[196:197] op_sel:[0,1] op_sel_hi:[1,1]\nv_pk_mul_f32 v[142:143], v[142:143], v[196:197] op_sel:[0,1] op_sel_hi:[1,1]\nC 60,136,137\nC 61,138,139\nC 62,140,141\nC 63,142,143\nbuffer_store_dwordx4 v[60:63], v0, s[60:63], s95 offen offset:1024 sc1\n" \
"s_branch zend\nzlast:\n.subsection 0\n.purgem D\n.purgem Ta\n.purgem Ra\n.purgem Zvaa\n.purgem AR\n" \
".purgem Zaaa\n.purgem Mvaa\n.purgem Maaa\n.purgem C\n.purgem AW\n.purgem DS\n.purgem Qvav\n.purgem Mvav\n" \
".purgem E\n.purgem Zaav\n.purgem Maav\n.purgem Mvvv\n.purgem Zvav\n.purgem Rv\n.purgem Mvva\n.purgem Qvaa\n" \
".purgem Zvvv\n.purgem Qvva\n" \
""
#define Z_STAMP 0
#define Z_PAD 0
#define Z_NTP 0
#define Z_NY 6
#define Z_NP 2
#define Z_PA 1
#define Z_CLOBA "v1", "v2", "v3", "v4", "v9", "v10", "v11", "v12", "v13", "v14", "v15", "v16", "v17", "v18", "v19", "v20", "v21", "v22", "v23", "v24", "v25", "v26", "v27", "v28", "v30", "v31", "v32", "v33", "v34", "v35", "v36", "v37", "v38", "v39", "v40", "v41", "v42", "v43", "v44", "v45", "v46", "v47", "v48", "v49", "v50", "v51", "v52", "v53", "v54", "v55", "v56", "v57", "v58", "v59", "v60", "v61", "v62", "v63", "v64", "v65", "v66", "v67", "v68", "v69", "v70", "v71", "v72", "v73", "v74", "v75", "v76", "v77", "v78", "v79", "v80", "v81", "v82", "v83", "v84", "v85", "v86", "v87", "v88", "v89", "v90", "v91", "v92", "v93", "v94", "v95", "v96", "v97", "v98", "v99", "v100", "v101", "v102", "v103", "v104", "v105", "v106", "v107", "v108", "v109", "v110", "v111", "v112", "v113", "v114", "v115", "v116", "v117", "v118", "v119", "v120", "v121", "v122", "v123", "v124", "v125", "v126", "v127", "v128", "v129", "v130", "v131", "v132", "v133", "v134", "v135", "v136", "v137", "v138", "v139", "v140", "v141", "v142", "v143", "v144", "v145", "v146", "v147", "v148", "v149", "v150", "v151", "v152", "v153", "v154", "v155", "v156", "v157", "v158", "v159", "v160", "v161", "v162", "v163", "v164", "v165", "v166", "v167", "v168", "v169", "v170", "v171", "v172", "v173", "v174", "v175", "v176", "v177", "v178", "v179", "v180", "v181", "v182", "v183", "v184", "v185", "v186", "v187", "v188", "v189", "v190", "v191", "v192", "v193", "v194", "v195", "v196", "v197", "v198", "v199", "v200", "v201", "v202", "v203", "v204", "v205", "v206", "v207", "v208", "v209", "v210", "v211", "v212", "v213", "v214", "v215", "v216", "v217", "v218", "v219", "v220", "v221", "v222", "v223", "v224", "v225", "v226", "v227", "v228", "v229", "v230", "v231", "v232", "v233", "v234", "v235", "v236", "v237", "v238", "v239", "v240", "v241", "v242", "v243", "v244", "v245", "v246", "v247", "v248", "v249", "v250", "v251", "v252", "v253", "v254", "v255", "a0", "a1", "a2", "a3", "a4", "a5", "a6", "a7", "a8", "a9", "a10", "a11", "a12", "a13", "a14", "a15", "a16", "a17", "a18", "a19", "a20", "a21", "a22", "a23", "a24", "a25", "a26", "a27", "a28", "a29", "a30", "a31", "a32", "a33", "a34", "a35", "a36", "a37", "a38", "a39", "a40", "a41", "a42", "a43", "a44", "a45", "a46", "a47", "a48", "a49", "a50", "a51", "a52", "a53", "a54", "a55", "a56", "a57", "a58", "a59", "a60", "a61", "a62", "a63", "a64", "a65", "a66", "a67", "a68", "a69", "a70", "a71", "a72", "a73", "a74", "a75", "a76", "a77", "a78", "a79", "a80", "a81", "a82", "a83", "a84", "a85", "a86", "a87", "a88", "a89", "a90", "a91", "a92", "a93", "a94", "a95", "a96", "a97", "a98", "a99", "a100", "a101", "a102", "a103", "a104", "a105", "a106", "a107", "a108", "a109", "a110", "a111", "a112", "a113", "a114", "a115", "a116", "a117", "a118", "a119", "a120", "a121", "a122", "a123", "a124", "a125", "a126", "a127", "a128", "a129", "a130", "a131", "a132", "a133", "a134", "a135", "a136", "a137", "a138", "a139", "a140", "a141", "a142", "a143", "a144", "a145", "a146", "a147", "a148", "a149", "a150", "a151", "a152", "a153", "a154", "a155", "a156", "a157", "a158", "a159", "a160", "a161", "a162", "a163", "a164", "a165", "a166", "a167", "a168", "a169", "a170", "a171", "a172", "a173", "a174", "a175", "a176", "a177", "a178", "a179", "a180", "a181", "a182", "a183", "a184", "a185", "a186", "a187", "a188", "a189", "a190", "a191", "a192", "a193", "a194", "a195", "a196", "a197", "a198", "a199", "a200", "a201", "a202", "a203", "a204", "a205", "a206", "a207", "a208", "a209", "a210", "a211", "a212", "a213", "a214", "a215", "a216", "a217", "a218", "a219", "a220", "a221", "a222", "a223", "a224", "a225", "a226", "a227", "a228", "a229", "a230", "a231", "a232", "a233", "a234", "a235", "a236", "a237", "a238", "a239", "a240", "a241", "a242", "a243", "a244", "a245", "a246", "a247", "a248", "a249", "a250", "a251", "a252", "a253", "a254", "a255", "s2", "s4", "s5", "s6", "s7", "s8", "s9", "s10", "s12", "s13", "s14", "s15", "s16", "s17", "s18", "s19", "s20", "s21", "s22", "s23", "s24", "s25", "s26", "s27", "s28", "s29", "s30", "s31", "s34", "s35", "s36", "s37", "s38", "s39", "s40", "s41", "s42", "s43", "s44", "s45", "s46", "s47", "s48", "s49", "s50", "s51", "s52", "s53", "s54", "s55", "s56", "s57", "s58", "s59", "s72", "s73", "s74", "s75", "s76", "s77", "s78", "s79", "s88", "s89", "s90", "s91", "s92", "s93", "s94", "s95", "s96", "s97", "s98", "s99", "s100", "s101", "m0", "scc", "vcc", "memory"
#define Z_CLOB "v16", "v17", "v18", "v19", "v20", "v21", "v22", "v23", "v24", "v25", "v26", "v27", "v28", "v29", "v30", "v31", "v32", "v33", "v34", "v35", "v36", "v37", "v38", "v39", "v40", "v41", "v42", "v43", "v44", "v45", "v46", "v47", "v48", "v49", "v50", "v51", "v52", "v53", "v54", "v55", "v56", "v57", "v58", "v59", "v60", "v61", "v62", "v63", "v64", "v65", "v66", "v67", "v68", "v69", "v70", "v71", "v72", "v73", "v74", "v75", "v76", "v77", "v78", "v79", "v80", "v81", "v82", "v83", "v84", "v85", "v86", "v87", "v88", "v89", "v90", "v91", "v92", "v93", "v94", "v95", "v96", "v97", "v98", "v99", "v100", "v101", "v102", "v103", "v104", "v105", "v106", "v107", "v108", "v109", "v110", "v111", "v112", "v113", "v114", "v115", "v116", "v117", "v118", "v119", "v120", "v121", "v122", "v123", "v124", "v125", "v126", "v127", "v128", "v129", "v130", "v131", "v132", "v133", "v134", "v135", "v136", "v137", "v138", "v139", "v140", "v141", "v142", "v143", "v144", "v145", "v146", "v147", "v148", "v149", "v150", "v151", "v152", "v153", "v154", "v155", "v156", "v157", "v158", "v159", "v160", "v161", "v162", "v163", "v164", "v165", "v166", "v167", "v168", "v169", "v170", "v171", "v172", "v173", "v174", "v175", "v176", "v177", "v178", "v179", "v180", "v181", "v182", "v183", "v184", "v185", "v186", "v187", "v188", "v189", "v190", "v191", "v192", "v193", "v194", "v195", "v196", "v197", "v198", "v199", "v200", "v201", "v202", "v203", "v204", "v205", "v206", "v207", "v208", "v209", "v210", "v211", "v212", "v213", "v214", "v215", "v216", "v217", "v218", "v219", "v220", "v221", "v222", "v223", "v224", "v225", "v226", "v227", "v228", "v229", "v230", "v231", "v232", "v233", "v234", "v235", "v236", "v237", "v238", "v239", "v240", "v241", "v242", "v243", "v244", "v245", "v246", "v247", "v248", "v249", "v250", "v251", "v252", "v253", "v254", "v255", "a0", "a1", "a2", "a3", "a4", "a5", "a6", "a7", "a8", "a9", "a10", "a11", "a12", "a13", "a14", "a15", "a16", "a17", "a18", "a19", "a20", "a21", "a22", "a23", "a24", "a25", "a26", "a27", "a28", "a29", "a30", "a31", "a32", "a33", "a34", "a35", "a36", "a37", "a38", "a39", "a40", "a41", "a42", "a43", "a44", "a45", "a46", "a47", "a48", "a49", "a50", "a51", "a52", "a53", "a54", "a55", "a56", "a57", "a58", "a59", "a60", "a61", "a62", "a63", "a64", "a65", "a66", "a67", "a68", "a69", "a70", "a71", "a72", "a73", "a74", "a75", "a76", "a77", "a78", "a79", "a80", "a81", "a82", "a83", "a84", "a85", "a86", "a87", "a88", "a89", "a90", "a91", "a92", "a93", "a94", "a95", "a96", "a97", "a98", "a99", "a100", "a101", "a102", "a103", "a104", "a105", "a106", "a107", "a108", "a109", "a110", "a111", "a112", "a113", "a114", "a115", "a116", "a117", "a118", "a119", "a120", "a121", "a122", "a123", "a124", "a125", "a126", "a127", "a128", "a129", "a130", "a131", "a132", "a133", "a134", "a135", "a136", "a137", "a138", "a139", "a140", "a141", "a142", "a143", "a144", "a145", "a146", "a147", "a148", "a149", "a150", "a151", "a152", "a153", "a154", "a155", "a156", "a157", "a158", "a159", "a160", "a161", "a162", "a163", "a164", "a165", "a166", "a167", "a168", "a169", "a170", "a171", "a172", "a173", "a174", "a175", "a176", "a177", "a178", "a179", "a180", "a181", "a182", "a183", "a184", "a185", "a186", "a187", "a188", "a189", "a190", "a191", "a192", "a193", "a194", "a195", "a196", "a197", "a198", "a199", "a200", "a201", "a202", "a203", "a204", "a205", "a206", "a207", "a208", "a209", "a210", "a211", "a212", "a213", "a214", "a215", "a216", "a217", "a218", "a219", "a220", "a221", "a222", "a223", "a224", "a225", "a226", "a227", "a228", "a229", "a230", "a231", "a232", "a233", "a234", "a235", "a236", "a237", "a238", "a239", "a240", "a241", "a242", "a243", "a244", "a245", "a246", "a247", "a248", "a249", "a250", "a251", "a252", "a253", "a254", "a255", "s2", "s4", "s5", "s6", "s7", "s8", "s9", "s10", "s11", "s12", "s13", "s14", "s15", "s16", "s17", "s18", "s19", "s20", "s21", "s22", "s23", "s24", "s25", "s26", "s27", "s28", "s29", "s30", "s31", "s72", "s73", "s74", "s75", "s76", "s77", "s78", "s79", "s88", "s89", "s90", "s91", "s92", "s93", "s94", "s95", "s96", "s97", "s98", "s99", "s100", "s101", "m0", "scc", "vcc", "memory"
#define Z_WS_CHECK static_assert(WS_OA == 50660352 && WS_CTX == 54854656 && WS_PART == 71631872 && FLG_LA == 8192 && FLG_C == 16384 && FLG_S == 32768 && WS_V - WS_K == 16810496, "layout")
#define Z_CLOB1 "v28", "a251", "a252", "a253", "s72", "s73", "s74", "s75", "s76", "s77", "s78", "s79", "s88", "s89", "s90", "s91", "s92", "s93", "s94", "s95", "m0", "scc", "memory"
typedef unsigned long long u64;
#define HPAD 1024
constexpr unsigned HS = 2048 * 256 + HPAD;
typedef float f32x32 __attribute__((ext_vector_type(32)));
typedef unsigned long long u64x4 __attribute__((ext_vector_type(4)));
typedef unsigned u32x16 __attribute__((ext_vector_type(16)));
typedef unsigned long long u64x2 __attribute__((ext_vector_type(2)));
#define XPAGE(name) asm volatile("s_branch 99f\n\t.p2align 12\n" name ":\n\ts_setpc_b64 s[68:69]\n99:")
#define TOUCH_CLOB "s64", "s65", "s66", "s67", "s68", "s69", "scc"
__device__ __forceinline__ unsigned lane_now() { unsigned l; asm volatile("v_mbcnt_lo_u32_b32 %0, -1, 0\n\tv_mbcnt_hi_u32_b32 %0, -1, %0" : "=v"(l)); return l; }
struct G1Desc { u64 p; unsigned n, w; };
struct G1Args { G1Desc ckv, wuk, wuv, ws; u64 Ax, Awqkv, Bx, wq, Awo, Bwo, out; };
constexpr unsigned PART_BYTES = 65536 + 8192 + 128;
constexpr unsigned WS_QKVA = 262144, WS_Q = WS_QKVA + 2048 * 6144, WS_K = WS_Q + 64 * 65536, WS_V = WS_K + 32 * HS + HPAD / 2, WS_OA = WS_V + 32 * HS + HPAD / 2,
                   WS_CTX = WS_OA + 2048 * 2048, WS_PART = WS_CTX + 2048 * 8192, WS_APART = WS_PART + 64 * 4 * 65536, WS_END = WS_APART + 64 * PART_BYTES, NOWHERE = 0x7fff0000u;
constexpr unsigned FLG_KV = 256, FLG_H = 4096, FLG_LA = 8192, FLG_C = 16384, FLG_S = 32768, WS_HALO = WS_QKVA;
Z_WS_CHECK;
typedef unsigned u32x4g __attribute__((ext_vector_type(4)));
#define STAMP(i)
#define RSTAMP(i)
#define STAMP_DECL
#define OARG
#define OPASS
#define STAMP_OUT()
namespace att {
typedef float    f32x4  __attribute__((ext_vector_type(4)));
typedef float    f32x16 __attribute__((ext_vector_type(16)));
typedef unsigned u32x4  __attribute__((ext_vector_type(4)));
#define VRING 0x8000u
#define QSTAGE 0x10000u
#define SRD_WORD3 0x00020000
#define DI __device__ __forceinline__
#define GAP() __builtin_amdgcn_sched_barrier(0)
DI unsigned rfl(unsigned x) { return __builtin_amdgcn_readfirstlane(x); }
DI unsigned lane_id() { return lane_now(); }
__global__ __launch_bounds__(256, 1)
void attn_kernel(G1Args kernel_args) {
  __shared__ __attribute__((aligned(1024))) char lds[163840];
  STAMP_DECL; STAMP(0); RSTAMP(14);
  u32x4 dA, dW, dG;
  u64x4 b4;
  unsigned b = blockIdx.x, tid = threadIdx.x;
  asm volatile("s_load_dwordx4 s[64:67], %6, 0x0\n\ts_lshr_b32 s56, %4, 2\n\ts_load_dwordx4 s[60:63], %6, 0x30\n\ts_and_b32 s56, s56, 16\n\ts_add_u32 s56, s56, 16\n\t"
               "s_load_dwordx4 s[68:71], %6, s56\n\ts_load_dwordx8 s[80:87], %6, 0x40"
               : "=&{s[64:67]}"(dA), "=&{s[68:71]}"(dW), "=&{s[60:63]}"(dG), "=&{s[80:87]}"(b4), "+{s3}"(b), "+v"(tid) : "s"(__builtin_amdgcn_kernarg_segment_ptr()) : "s56", "scc");
  asm volatile("s_getpc_b64 s[56:57]\n1:\n\ts_mov_b32 s58, zlast+256-1b\n\ts_mov_b32 s59, 0x20000\n\tbuffer_load_dword a254, %0, s[56:59], 0 offen"
               :: "v"(((b >> 3) * 64u + (tid >> 6) * 16u + (tid & 15u)) * 128u + ((tid & 48u) << 24)) : "s56", "s57", "s58", "s59", "a254");
  {
  const unsigned wid = rfl(tid >> 6), xcd = b & 7u, jj = b >> 3, wr = wid >> 1, wc = wid & 1u;
  unsigned wk = 4096u * wid;
  const unsigned rt1 = 2u * (xcd & 1u) + (jj & 1u) + ((b >> 5) & 4u), ct1 = 4u * (xcd >> 1) + ((jj >> 1) & 3u);
  const unsigned ln = tid & 63u;
  const unsigned l4 = ln >> 4, l15 = ln & 15u, swA = ((ln & 7u) ^ (ln >> 3)) << 4, cw = l15 ^ (l4 << 1);
  unsigned vA = (rt1 * 256u + 64u * wid + (ln >> 3)) * 1024u + swA, vW0 = (16u * wid + l4) * 8192u + ct1 * 512u + (cw << 4);
  unsigned vC = ((b * 4u + wid) * 16u + l15) * 128u + ((ln & 48u) << 24);
  const unsigned q4 = l15 >> 2, p4 = ln & 3u, f4 = q4 | ((l4 & 1u) << 2);
  unsigned rdA = (wr * 128u + l15) * 128u + ((l4 ^ (ln & 7u)) << 4), rdW = 65536u + wc * 16384u + (8u * l4 + q4) * 256u + (((p4 >> 1) ^ (f4 << 1)) << 4) + (p4 & 1u) * 8u;
  unsigned vL = 16u * ln, kvt = ct1 * 8u + rt1, role = (jj >> 3) & 1u;
  asm volatile("" :: "s"((__attribute__((address_space(3))) char*)lds));
#define Z_STV
  asm volatile(Z_MAIN : "+{v0}"(vL), "+{v5}"(vA), "+{v6}"(vW0), "+{v7}"(rdA), "+{v8}"(rdW), "+{v29}"(vC), "+{s11}"(kvt), "+{s33}"(role), "+{s[60:63]}"(dG), "+{s[64:67]}"(dA), "+{s[68:71]}"(dW), "+{s[80:87]}"(b4), "+{s3}"(b), "+{s32}"(wk) Z_STV
               : "{s[0:1]}"(__builtin_amdgcn_kernarg_segment_ptr()) : Z_CLOBA);
  }
  STAMP(13); RSTAMP(15); STAMP_OUT();
}
}

#define WSOFF 0
extern "C" void kernel_launch(void* const* d_in, const int* in_sizes, int n_in, void* d_out, int out_size,
                              void* d_ws, size_t ws_size, hipStream_t stream) {
  const bf16_t* A_x = (const bf16_t*)d_in[0];
  const bf16_t* A_wqkv = (const bf16_t*)d_in[1];
  const bf16_t* A_wo = (const bf16_t*)d_in[2];
  const bf16_t* B_x = (const bf16_t*)d_in[3];
  const bf16_t* B_ckv = (const bf16_t*)d_in[4];
  const bf16_t* B_wq = (const bf16_t*)d_in[5];
  const bf16_t* B_wuk = (const bf16_t*)d_in[6];
  const bf16_t* B_wuv = (const bf16_t*)d_in[7];
  const bf16_t* B_wo = (const bf16_t*)d_in[8];
    G1Args g1 = {{(u64)B_ckv, 2048u * 1024u, 0x20000u}, {(u64)B_wuk, 512u * 8192u, 0x20000u}, {(u64)B_wuv, 512u * 8192u, 0x20000u}, {(u64)d_ws + WSOFF, WS_END, 0x20000u}, (u64)A_x, (u64)A_wqkv, (u64)B_x, (u64)B_wq, (u64)A_wo, (u64)B_wo, (u64)d_out};
  att::attn_kernel<<<256, 256, 0, stream>>>(g1);
}
```

```cpp
#include <hip/hip_runtime.h>
#include <hip/hip_bf16.h>
#include <stdint.h>
#include <utility>

typedef unsigned short bf16_t;
typedef short bf16x4 __attribute__((ext_vector_type(4)));
typedef float f32x4 __attribute__((ext_vector_type(4)));
typedef unsigned int u32x2 __attribute__((ext_vector_type(2)));

__device__ __forceinline__ unsigned pack_bf16(float a, float b) {
  typedef __bf16 bf2 __attribute__((ext_vector_type(2)));
  typedef float f2 __attribute__((ext_vector_type(2)));
  bf2 r = __builtin_convertvector((f2){a, b}, bf2);
  return __builtin_bit_cast(unsigned, r);
}
#define Z_PRO1 \
"s_lshl_b32 s88, s32, 1\nv_xor_b32 v28, 128, v6\ns_mov_b32 s73, 7168\ns_mov_b32 s74, 14336\ns_mov_b32 s75, 21504\ns_mov_b32 s76, 32768\ns_mov_b32 s77, 39936\ns_mov_b32 s78, 47104\n" \
"s_mov_b32 s79, 54272\ns_mov_b32 s89, 31744\ns_mov_b32 s90, 63488\ns_mov_b32 s91, 95232\ns_mov_b32 s92, 256\ns_mov_b32 s93, 32000\ns_mov_b32 s94, 63744\ns_mov_b32 s95, 95488\n" \
"s_mov_b32 s72, 524288\ns_waitcnt lgkmcnt(0)\ns_add_u32 m0, s88, 0\ns_load_dwordx4 s[12:15], s[0:1], 0x60\nbuffer_load_dwordx4 v5, s[64:67], 0 offen offset:0 lds\ns_load_dwordx2 s[16:17], s[0:1], 0x70\nv_mov_b32 v16, v6\nv_mov_b32 v17, v5\n" \
"buffer_load_dwordx4 v5, s[64:67], s73 offen offset:1024 lds\ns_cmp_lt_u32 s3, 128\ns_cselect_b32 s26, 2, 0\ns_cmp_lt_u32 s3, 32\ns_cselect_b32 s27, 4, 0\ns_or_b32 s33, s33, s26\nbuffer_load_dwordx4 v5, s[64:67], s74 offen offset:2048 lds\ns_and_b32 s26, s3, 96\n" \
"s_or_b32 s33, s33, s27\ns_cmp_eq_u32 s26, 96\ns_cselect_b32 s26, 8, 0\nbuffer_load_dwordx4 v5, s[64:67], s75 offen offset:3072 lds\ns_xor_b32 s27, s32, 0x3000\ns_or_b32 s33, s33, s26\ns_or_b32 s27, s27, s3\ns_cselect_b32 s27, 0, 16\n" \
"s_add_u32 m0, s88, 4096\ns_or_b32 s33, s33, s27\nbuffer_load_dwordx4 v5, s[64:67], s76 offen offset:0 lds\nv_xor_b32 v20, 32, v8\nv_xor_b32 v21, 64, v8\nv_xor_b32 v22, 96, v8\nbuffer_load_dwordx4 v5, s[64:67], s77 offen offset:1024 lds\nv_xor_b32 v23, 128, v8\n" \
"v_xor_b32 v24, 160, v8\nv_xor_b32 v25, 192, v8\nbuffer_load_dwordx4 v5, s[64:67], s78 offen offset:2048 lds\nv_xor_b32 v26, 224, v8\nv_xor_b32 v27, 64, v7\ns_and_b32 s26, s3, 127\nbuffer_load_dwordx4 v5, s[64:67], s79 offen offset:3072 lds\ns_lshr_b32 s27, s32, 12\n" \
"s_movk_i32 s28, 256\ns_lshl2_add_u32 s26, s26, s27\ns_add_u32 m0, s32, 65536\ns_mov_b32 s22, 6291456\nbuffer_load_dwordx4 v6, s[68:71], 0 offen offset:0 nt lds\ns_mov_b32 s23, 4194304\ns_cmp_lt_u32 s3, 128\ns_cselect_b32 s28, s28, 420\n" \
"s_cselect_b32 s29, 22, 18\ns_cselect_b32 s30, 19, 30\ns_cselect_b64 s[52:53], s[82:83], s[86:87]\ns_cmp_lt_u32 s3, 128\ns_cselect_b32 s54, s22, 8388608\ns_cselect_b64 s[56:57], s[80:81], s[84:85]\ns_cselect_b32 s22, 0, 2097152\ns_cselect_b32 s23, s23, 2097152\n" \
"buffer_load_dwordx4 v6, s[68:71], s89 offen offset:1024 nt lds\ns_add_u32 s56, s56, s22\ns_addc_u32 s57, s57, 0\ns_sub_u32 s27, 512, s28\ns_cmp_lt_u32 s26, s28\ns_cselect_b64 s[52:53], s[52:53], s[56:57]\ns_cselect_b32 s54, s54, s23\ns_cselect_b32 s29, s29, s30\n" \
"s_cselect_b32 s27, s28, s27\ns_cselect_b32 s28, 0, s28\nbuffer_load_dwordx4 v28, s[68:71], s90 offen offset:2048 nt lds\ns_mov_b32 s55, 0x20000\ns_sub_u32 s26, s26, s28\ns_lshl_b32 s30, s29, 4\nbuffer_load_dwordx4 v28, s[68:71], s91 offen offset:3072 nt lds\ns_lshl_b32 s29, s29, 7\n" \
"v_cmp_gt_u32 vcc, s30, v0\nv_lshlrev_b32 v18, 3, v0\ns_add_u32 m0, s32, 81920\ns_mul_i32 s26, s26, s29\nbuffer_load_dwordx4 v6, s[68:71], s92 offen offset:0 nt lds\ns_mul_i32 s44, s27, s29\nv_add_u32 v18, s26, v18\nv_cndmask_b32 v4, 2.0, v18, vcc\n" \
"buffer_load_dwordx4 v6, s[68:71], s93 offen offset:1024 nt lds\nbuffer_load_dwordx4 v28, s[68:71], s94 offen offset:2048 nt lds\nbuffer_load_dwordx4 v28, s[68:71], s95 offen offset:3072 nt lds\nbuffer_load_dwordx2 a[252:253], off, s[60:63], 0 sc1\nv_add_u32 v5, 128, v5\nv_add_u32 v6, s72, v6\nv_add_u32 v28, s72, v28\nbuffer_load_dword a251, v29, s[64:67], 0 offen sc1\n" \
""
#define Z_MAIN \
".macro D v,s,o,i\nbuffer_load_dwordx4 v\\v, s[\\s:\\s+3], \\o offen offset:\\i lds\n.endm\n.macro Ta d,v,o\nds_read_b64_tr_b16 a[\\d:\\d+1], v\\v offset:\\o\n.endm\n.macro Ra d,v,o\nds_read_b128 a[\\d:\\d+3], v\\v offset:\\o\n.endm\n.macro Zvaa d,a,b\nv_mfma_f32_16x16x32_bf16 v[\\d:\\d+3], a[\\a:\\a+3], a[\\b:\\b+3], 0\n.endm\n.macro AR d,a\nv_accvgpr_read_b32 v\\d, a\\a\n.endm\n.macro Zaaa d,a,b\nv_mfma_f32_16x16x32_bf16 a[\\d:\\d+3], a[\\a:\\a+3], a[\\b:\\b+3], 0\n.endm\n.macro Mvaa d,a,b\nv_mfma_f32_16x16x32_bf16 v[\\d:\\d+3], a[\\a:\\a+3], a[\\b:\\b+3], v[\\d:\\d+3]\n.endm\n.macro Maaa d,a,b\nv_mfma_f32_16x16x32_bf16 a[\\d:\\d+3], a[\\a:\\a+3], a[\\b:\\b+3], a[\\d:\\d+3]\n.endm\n" \
".macro C d,a,b\nv_cvt_pk_bf16_f32 v\\d, v\\a, v\\b\n.endm\n.macro AW d,a\nv_accvgpr_write_b32 a\\d, v\\a\n.endm\n.macro DS v,s,o,i\nbuffer_load_dwordx4 v\\v, s[\\s:\\s+3], \\o offen offset:\\i sc1 lds\n.endm\n.macro Qvav d,a,b,c\nv_mfma_f32_16x16x32_bf16 v[\\d:\\d+3], a[\\a:\\a+3], v[\\b:\\b+3], v[\\c:\\c+3]\n.endm\n.macro Mvav d,a,b\nv_mfma_f32_16x16x32_bf16 v[\\d:\\d+3], a[\\a:\\a+3], v[\\b:\\b+3], v[\\d:\\d+3]\n.endm\n.macro E d,a\nv_exp_f32 v\\d, v\\a\n.endm\n.macro Zaav d,a,b\nv_mfma_f32_16x16x32_bf16 a[\\d:\\d+3], a[\\a:\\a+3], v[\\b:\\b+3], 0\n.endm\n.macro Maav d,a,b\nv_mfma_f32_16x16x32_bf16 a[\\d:\\d+3], a[\\a:\\a+3], v[\\b:\\b+3], a[\\d:\\d+3]\n.endm\n" \
".macro Mvvv d,a,b\nv_mfma_f32_16x16x32_bf16 v[\\d:\\d+3], v[\\a:\\a+3], v[\\b:\\b+3], v[\\d:\\d+3]\n.endm\n.macro Zvav d,a,b\nv_mfma_f32_16x16x32_bf16 v[\\d:\\d+3], a[\\a:\\a+3], v[\\b:\\b+3], 0\n.endm\n.macro Rv d,v,o\nds_read_b128 v[\\d:\\d+3], v\\v offset:\\o\n.endm\n.macro Mvva d,a,b\nv_mfma_f32_16x16x32_bf16 v[\\d:\\d+3], v[\\a:\\a+3], a[\\b:\\b+3], v[\\d:\\d+3]\n.endm\n.macro Qvaa d,a,b,c\nv_mfma_f32_16x16x32_bf16 v[\\d:\\d+3], a[\\a:\\a+3], a[\\b:\\b+3], v[\\c:\\c+3]\n.endm\n.macro Zvvv d,a,b\nv_mfma_f32_16x16x32_bf16 v[\\d:\\d+3], v[\\a:\\a+3], v[\\b:\\b+3], 0\n.endm\n.macro Qvva d,a,b,c\nv_mfma_f32_16x16x32_bf16 v[\\d:\\d+3], v[\\a:\\a+3], a[\\b:\\b+3], v[\\c:\\c+3]\n.endm\ns_lshl_b32 s88, s32, 1\n" \
"v_xor_b32 v28, 128, v6\ns_mov_b32 s73, 7168\ns_mov_b32 s74, 14336\ns_mov_b32 s75, 21504\ns_mov_b32 s76, 32768\ns_mov_b32 s77, 39936\ns_mov_b32 s78, 47104\ns_mov_b32 s79, 54272\n" \
"s_mov_b32 s89, 31744\ns_mov_b32 s90, 63488\ns_mov_b32 s91, 95232\ns_mov_b32 s92, 256\ns_mov_b32 s93, 32000\ns_mov_b32 s94, 63744\ns_mov_b32 s95, 95488\ns_mov_b32 s72, 524288\n" \
"s_waitcnt lgkmcnt(0)\ns_add_u32 m0, s88, 0\ns_load_dwordx4 s[12:15], s[0:1], 0x60\nD 5,64,0,0\ns_load_dwordx2 s[16:17], s[0:1], 0x70\nv_mov_b32 v16, v6\nv_mov_b32 v17, v5\nD 5,64,s73,1024\n" \
"s_cmp_lt_u32 s3, 128\ns_cselect_b32 s26, 2, 0\ns_cmp_lt_u32 s3, 32\ns_cselect_b32 s27, 4, 0\ns_or_b32 s33, s33, s26\nD 5,64,s74,2048\ns_and_b32 s26, s3, 96\ns_or_b32 s33, s33, s27\n" \
"s_cmp_eq_u32 s26, 96\ns_cselect_b32 s26, 8, 0\nD 5,64,s75,3072\ns_xor_b32 s27, s32, 0x3000\ns_or_b32 s33, s33, s26\ns_or_b32 s27, s27, s3\ns_cselect_b32 s27, 0, 16\ns_add_u32 m0, s88, 4096\n" \
"s_or_b32 s33, s33, s27\nD 5,64,s76,0\nv_xor_b32 v20, 32, v8\nv_xor_b32 v21, 64, v8\nv_xor_b32 v22, 96, v8\nD 5,64,s77,1024\nv_xor_b32 v23, 128, v8\nv_xor_b32 v24, 160, v8\n" \
"v_xor_b32 v25, 192, v8\nD 5,64,s78,2048\nv_xor_b32 v26, 224, v8\nv_xor_b32 v27, 64, v7\ns_and_b32 s26, s3, 127\nD 5,64,s79,3072\ns_lshr_b32 s27, s32, 12\ns_movk_i32 s28, 256\n" \
"s_lshl2_add_u32 s26, s26, s27\ns_add_u32 m0, s32, 65536\ns_mov_b32 s22, 6291456\nbuffer_load_dwordx4 v6, s[68:71], 0 offen offset:0 nt lds\ns_mov_b32 s23, 4194304\ns_cmp_lt_u32 s3, 128\ns_cselect_b32 s28, s28, 420\ns_cselect_b32 s29, 22, 18\n" \
"s_cselect_b32 s30, 19, 30\ns_cselect_b64 s[52:53], s[82:83], s[86:87]\ns_cmp_lt_u32 s3, 128\ns_cselect_b32 s54, s22, 8388608\ns_cselect_b64 s[56:57], s[80:81], s[84:85]\ns_cselect_b32 s22, 0, 2097152\ns_cselect_b32 s23, s23, 2097152\nbuffer_load_dwordx4 v6, s[68:71], s89 offen offset:1024 nt lds\n" \
"s_add_u32 s56, s56, s22\ns_addc_u32 s57, s57, 0\ns_sub_u32 s27, 512, s28\ns_cmp_lt_u32 s26, s28\ns_cselect_b64 s[52:53], s[52:53], s[56:57]\ns_cselect_b32 s54, s54, s23\ns_cselect_b32 s29, s29, s30\ns_cselect_b32 s27, s28, s27\n" \
"s_cselect_b32 s28, 0, s28\nbuffer_load_dwordx4 v28, s[68:71], s90 offen offset:2048 nt lds\ns_mov_b32 s55, 0x20000\ns_sub_u32 s26, s26, s28\ns_lshl_b32 s30, s29, 4\nbuffer_load_dwordx4 v28, s[68:71], s91 offen offset:3072 nt lds\ns_lshl_b32 s29, s29, 7\nv_cmp_gt_u32 vcc, s30, v0\n" \
"v_lshlrev_b32 v18, 3, v0\ns_add_u32 m0, s32, 81920\ns_mul_i32 s26, s26, s29\nbuffer_load_dwordx4 v6, s[68:71], s92 offen offset:0 nt lds\ns_mul_i32 s44, s27, s29\nv_add_u32 v18, s26, v18\nv_cndmask_b32 v4, 2.0, v18, vcc\nbuffer_load_dwordx4 v6, s[68:71], s93 offen offset:1024 nt lds\n" \
"buffer_load_dwordx4 v28, s[68:71], s94 offen offset:2048 nt lds\nbuffer_load_dwordx4 v28, s[68:71], s95 offen offset:3072 nt lds\nbuffer_load_dwordx2 a[252:253], off, s[60:63], 0 sc1\nv_add_u32 v5, 128, v5\nv_add_u32 v6, s72, v6\nv_add_u32 v28, s72, v28\nbuffer_load_dword a251, v29, s[64:67], 0 offen sc1\ns_add_u32 m0, s88, 32768\n" \
"s_movk_i32 s2, 0x4000\nD 5,64,0,0\ns_movk_i32 s23, 0x1000\ns_getpc_b64 s[96:97]\n1:\ns_add_u32 s96, s96, zg0-1b\ns_addc_u32 s97, s97, 0\nD 5,64,s73,1024\n" \
"s_add_u32 s4, s32, zgv-zg0-4096\ns_add_u32 s5, s32, zgx-zg0-8192\nD 5,64,s74,2048\ns_add_u32 s6, s4, 4096\ns_cmp_lt_u32 s32, 0x2000\ns_cselect_b32 s5, s32, s5\nD 5,64,s75,3072\ns_cmp_eq_u32 s32, 0x3000\n" \
"s_cselect_b32 s6, s6, s4\ns_add_u32 s7, s32, zgt-zg0-0\ns_add_u32 m0, s88, 36864\ns_mov_b32 s10, zgx-zg0-0\nD 5,64,s76,0\ns_bitcmp1_b32 s33, 0\ns_cselect_b32 s5, s6, s5\ns_cselect_b32 s4, s4, s32\n" \
"s_cselect_b32 s10, s10, zga-zg0-0\ns_cselect_b32 s6, 0x3000, s2\ns_cselect_b32 s22, zgv-zg0+12288, s23\ns_bitcmp1_b32 s33, 1\ns_cselect_b32 s4, s5, s4\ns_cselect_b32 s6, s10, s6\ns_cselect_b32 s7, s7, s22\nD 5,64,s77,1024\n" \
"s_cmp_eq_u32 s32, 0\ns_cselect_b32 s4, 0, s4\ns_add_u32 s6, s6, s32\nD 5,64,s78,2048\ns_add_u32 s18, s96, s6\ns_addc_u32 s19, s97, 0\ns_add_u32 s20, s96, s7\ns_addc_u32 s21, s97, 0\n" \
"D 5,64,s79,3072\ns_add_u32 s24, s96, s4\ns_addc_u32 s25, s97, 0\ns_add_u32 m0, s32, 98304\ns_nop 0\nbuffer_load_dwordx4 v6, s[68:71], 0 offen offset:0 nt lds\nbuffer_load_dwordx4 v6, s[68:71], s89 offen offset:1024 nt lds\nbuffer_load_dwordx4 v28, s[68:71], s90 offen offset:2048 nt lds\n" \
"buffer_load_dwordx4 v28, s[68:71], s91 offen offset:3072 nt lds\ns_add_u32 m0, s32, 114688\ns_nop 0\nbuffer_load_dwordx4 v6, s[68:71], s92 offen offset:0 nt lds\nbuffer_load_dwordx4 v6, s[68:71], s93 offen offset:1024 nt lds\nbuffer_load_dwordx4 v28, s[68:71], s94 offen offset:2048 nt lds\nbuffer_load_dwordx4 v28, s[68:71], s95 offen offset:3072 nt lds\ns_swappc_b64 s[98:99], s[24:25]\n" \
"s_branch 99f\n.p2align 12\nzg0:\nzp0:\ns_setpc_b64 s[98:99]\n99:\ns_waitcnt vmcnt(17)\ns_barrier\n" \
"Ta 64,8,0\nTa 66,8,1024\nRa 0,7,0\nTa 68,20,0\nTa 70,20,1024\nRa 4,7,2048\nTa 72,21,0\nTa 74,21,1024\n" \
"Ra 8,7,4096\nTa 76,22,0\nTa 78,22,1024\nRa 12,7,6144\nTa 80,23,0\nTa 82,23,1024\nRa 16,7,8192\nTa 84,24,0\n" \
"Ta 86,24,1024\nRa 20,7,10240\nTa 88,25,0\nTa 90,25,1024\nRa 24,7,12288\nTa 92,26,0\nTa 94,26,1024\nRa 28,7,14336\n" \
"v_add_u32 v5, 128, v5\nv_add_u32 v6, s72, v6\nv_add_u32 v28, s72, v28\nbuffer_load_dword v30, v4, s[52:55], 0 offen sc1\nv_add_u32 v4, s44, v4\ns_bitcmp1_b32 s33, 0\ns_waitcnt lgkmcnt(0)\ns_cbranch_scc1 zV\n" \
"Zvaa 32,64,0\nTa 96,8,8192\ns_lshr_b32 s7, s3, 5\nZvaa 64,64,4\nTa 98,8,9216\ns_lshr_b32 s5, 170, s7\nZvaa 68,68,4\nRa 32,27,0\n" \
"s_and_b32 s5, s5, 1\nZvaa 36,68,0\nTa 100,20,8192\ns_lshl_b32 s7, s7, 2\nZvaa 40,72,0\nTa 102,20,9216\ns_lshr_b32 s4, 0x12124444, s7\nZvaa 72,72,4\n" \
"Ra 36,27,2048\ns_and_b32 s4, s4, 15\nZvaa 76,76,4\nTa 104,21,8192\ns_xor_b32 s6, s5, s3\nZvaa 44,76,0\nTa 106,21,9216\ns_lshr_b32 s7, 0xff00, s4\n" \
"Zvaa 48,80,0\nRa 40,27,4096\ns_and_b32 s7, s7, 0xff\nZvaa 80,80,4\nTa 108,22,8192\ns_xor_b32 s10, s7, 0xff\nZvaa 84,84,4\nTa 110,22,9216\n" \
"s_lshl_b32 s7, s7, 8\nZvaa 52,84,0\nRa 44,27,6144\ns_or_b32 s7, s7, 0xff\nZvaa 56,88,0\nTa 112,23,8192\ns_add_u32 s8, s4, 5\nZvaa 88,88,4\n" \
"Ta 114,23,9216\ns_sub_u32 s4, 262, s4\nZvaa 92,92,4\nRa 48,27,8192\ns_add_u32 s9, s4, 0x101\nZvaa 60,92,0\nTa 116,24,8192\nZvaa 124,92,8\n" \
"Ta 118,24,9216\nZvaa 156,92,12\nRa 52,27,10240\nZvaa 152,88,12\nTa 120,25,8192\nZvaa 120,88,8\nTa 122,25,9216\nZvaa 116,84,8\n" \
"Ra 56,27,12288\nZvaa 148,84,12\nTa 124,26,8192\nZvaa 144,80,12\nTa 126,26,9216\nZvaa 112,80,8\nRa 60,27,14336\nZvaa 108,76,8\n" \
"AR 2,252\nAR 3,253\nZvaa 140,76,12\nv_readfirstlane_b32 s96, v2\nv_readfirstlane_b32 s97, v3\nZvaa 136,72,12\ns_mul_i32 s97, s97, 0x7f4a7c15\ns_mul_hi_u32 s98, s96, 0x7f4a7c15\n" \
"Zvaa 104,72,8\ns_add_u32 s97, s97, s98\ns_mul_i32 s98, s96, 0x9e3779b9\nZvaa 100,68,8\ns_add_u32 s97, s97, s98\ns_mul_i32 s96, s96, 0x7f4a7c15\nZvaa 132,68,12\ns_add_u32 s96, s96, 0xd9b4e019\n" \
"s_addc_u32 s97, s97, 0x632be59b\nZvaa 128,64,12\nv_mov_b32 v2, s96\nv_mov_b32 v3, s97\nZvaa 96,64,8\ns_mov_b32 s96, 3\nZvaa 160,64,16\ns_waitcnt lgkmcnt(0)\n" \
"s_barrier\nZvaa 192,64,20\ns_add_u32 m0, s88, 0\nZvaa 196,68,20\nD 5,64,0,0\nZvaa 164,68,16\ns_cmp_lt_u32 s3, 128\ns_cselect_b32 s4, s9, s4\n" \
"Zvaa 168,72,16\ns_bitcmp1_b32 s6, 0\ns_cselect_b32 s9, s4, s8\ns_cselect_b32 s7, s10, s7\nZvaa 200,72,20\ns_and_b32 s6, s6, 1\ns_lshr_b32 s10, s3, 1\nZvaa 204,76,20\n" \
"D 5,64,s73,1024\nZvaa 172,76,16\ns_and_b32 s10, s10, 2\ns_or_b32 s6, s6, s10\nZvaa 176,80,16\ns_lshl_b32 s6, s6, 3\ns_lshl_b32 s8, s7, s6\nZvaa 208,80,20\n" \
"s_bfe_u32 s10, s3, 0x20003\ns_add_u32 s6, s6, s10\nZvaa 212,84,20\nD 5,64,s74,2048\nZvaa 180,84,16\ns_bitset0_b32 s8, s6\ns_lshl_b32 s5, s5, 16\nZvaa 184,88,16\n" \
"s_or_b32 s9, s9, s5\nZvaa 216,88,20\nZvaa 220,92,20\nD 5,64,s75,3072\nZvaa 188,92,16\nZvaa 252,92,24\nZaaa 156,92,28\ns_add_u32 m0, s88, 4096\n" \
"Zaaa 152,88,28\nD 5,64,s76,0\nZvaa 248,88,24\nZvaa 244,84,24\nZaaa 148,84,28\nZaaa 144,80,28\nD 5,64,s77,1024\nZvaa 240,80,24\n" \
"Zvaa 236,76,24\nZaaa 140,76,28\nZaaa 136,72,28\nD 5,64,s78,2048\nZvaa 232,72,24\nZvaa 228,68,24\nZaaa 132,68,28\nZaaa 128,64,28\n" \
"D 5,64,s79,3072\nZvaa 224,64,24\ns_branch 99f\n.p2align 12\nzp1:\ns_setpc_b64 s[98:99]\n99:\ns_nop 0\n" \
"zgK:\nMvaa 32,96,32\nMvaa 64,96,36\ns_add_u32 m0, s32, 65536\nMvaa 68,100,36\nbuffer_load_dwordx4 v6, s[68:71], 0 offen offset:0 nt lds\nMvaa 36,100,32\nMvaa 40,104,32\n" \
"Mvaa 72,104,36\nMvaa 76,108,36\nbuffer_load_dwordx4 v6, s[68:71], s89 offen offset:1024 nt lds\nMvaa 44,108,32\nMvaa 48,112,32\nMvaa 80,112,36\nMvaa 84,116,36\nbuffer_load_dwordx4 v28, s[68:71], s90 offen offset:2048 nt lds\n" \
"Mvaa 52,116,32\nMvaa 56,120,32\nMvaa 88,120,36\nMvaa 92,124,36\nbuffer_load_dwordx4 v28, s[68:71], s91 offen offset:3072 nt lds\nMvaa 60,124,32\nMvaa 124,124,40\nMvaa 156,124,44\n" \
"s_add_u32 m0, s32, 81920\nMvaa 152,120,44\nbuffer_load_dwordx4 v6, s[68:71], s92 offen offset:0 nt lds\nMvaa 120,120,40\nMvaa 116,116,40\nMvaa 148,116,44\nMvaa 144,112,44\nbuffer_load_dwordx4 v6, s[68:71], s93 offen offset:1024 nt lds\n" \
"Mvaa 112,112,40\nMvaa 108,108,40\nMvaa 140,108,44\nMvaa 136,104,44\nbuffer_load_dwordx4 v28, s[68:71], s94 offen offset:2048 nt lds\nMvaa 104,104,40\nMvaa 100,100,40\nMvaa 132,100,44\n" \
"s_swappc_b64 s[98:99], s[20:21]\nMvaa 128,96,44\nbuffer_load_dwordx4 v28, s[68:71], s95 offen offset:3072 nt lds\nMvaa 96,96,40\ns_waitcnt vmcnt(17)\ns_barrier\nMvaa 160,96,48\nbuffer_load_dword v30, v4, s[52:55], 0 offen sc1\n" \
"Ta 64,8,32768\nv_add_u32 v5, 128, v5\nMvaa 192,96,52\nTa 66,8,33792\nv_add_u32 v6, s72, v6\nMvaa 196,100,52\nRa 0,7,32768\nv_add_u32 v28, s72, v28\n" \
"Mvaa 164,100,48\nTa 68,20,32768\nMvaa 168,104,48\nTa 70,20,33792\nMvaa 200,104,52\nRa 4,7,34816\nMvaa 204,108,52\nv_add_u32 v4, s44, v4\n" \
"Ta 72,21,32768\nMvaa 172,108,48\nTa 74,21,33792\nMvaa 176,112,48\nRa 8,7,36864\nMvaa 208,112,52\nTa 76,22,32768\nMvaa 212,116,52\n" \
"Ta 78,22,33792\nMvaa 180,116,48\nRa 12,7,38912\nMvaa 184,120,48\nTa 80,23,32768\nMvaa 216,120,52\nTa 82,23,33792\nMvaa 220,124,52\n" \
"Ra 16,7,40960\nMvaa 188,124,48\nTa 84,24,32768\nMvaa 252,124,56\nTa 86,24,33792\nMaaa 156,124,60\nRa 20,7,43008\nMaaa 152,120,60\n" \
"Ta 88,25,32768\nMvaa 248,120,56\nTa 90,25,33792\nMvaa 244,116,56\nRa 24,7,45056\nMaaa 148,116,60\nTa 92,26,32768\nMaaa 144,112,60\n" \
"Ta 94,26,33792\nMvaa 240,112,56\nRa 28,7,47104\nMvaa 236,108,56\nMaaa 140,108,60\nMaaa 136,104,60\nMvaa 232,104,56\nMvaa 228,100,56\n" \
"Maaa 132,100,60\nMaaa 128,96,60\nMvaa 224,96,56\ns_waitcnt lgkmcnt(0)\nMvaa 32,64,0\nTa 96,8,40960\ns_and_b32 s4, s3, 4\nMvaa 64,64,4\n" \
"Ta 98,8,41984\ns_and_b32 s5, s3, 1\nMvaa 68,68,4\nRa 32,27,32768\ns_lshl_b32 s4, s4, 2\nMvaa 36,68,0\nTa 100,20,40960\ns_lshl3_add_u32 s4, s5, s4\n" \
"Mvaa 40,72,0\nTa 102,20,41984\ns_and_b32 s5, s3, 2\nMvaa 72,72,4\nRa 36,27,34816\ns_bfe_u32 s6, s3, 0x20003\nMvaa 76,76,4\nTa 104,21,40960\n" \
"s_lshl1_add_u32 s4, s5, s4\nMvaa 44,76,0\nTa 106,21,41984\ns_add_u32 s7, s4, s6\nMvaa 48,80,0\nRa 40,27,36864\ns_lshr_b32 s10, s11, 3\nMvaa 80,80,4\n" \
"Ta 108,22,40960\ns_and_b32 s22, s11, 7\nMvaa 84,84,4\nTa 110,22,41984\ns_lshl_b32 s4, s7, 8\nMvaa 52,84,0\nRa 44,27,38912\ns_lshl_b32 s5, s10, 9\n" \
"Mvaa 56,88,0\nTa 112,23,40960\ns_sub_u32 s4, s4, s5\nMvaa 88,88,4\nTa 114,23,41984\nv_add_u32 v14, s4, v16\nMvaa 92,92,4\nRa 48,27,40960\n" \
"s_mul_i32 s38, s7, 525312\nMvaa 60,92,0\nTa 116,24,40960\ns_add_u32 s38, s38, 17039360\nMvaa 124,92,8\nTa 118,24,41984\ns_lshr_b32 s4, s7, 1\nMvaa 156,92,12\n" \
"Ra 52,27,43008\ns_lshl_b32 s4, s4, 7\nMvaa 152,88,12\nTa 120,25,40960\nv_lshrrev_b32 v18, 1, v0\nMvaa 120,88,8\nTa 122,25,41984\ns_add_u32 s4, s4, 256\n" \
"Mvaa 116,84,8\nRa 56,27,45056\nv_and_b32 v18, 0x38, v18\nMvaa 148,84,12\nTa 124,26,40960\nv_add_u32 v1, s4, v18\nMvaa 144,80,12\nTa 126,26,41984\n" \
"s_lshr_b32 s23, s3, 5\nMvaa 112,80,8\nRa 60,27,47104\nMvaa 108,76,8\ns_cmp_lt_u32 s3, 128\ns_cselect_b32 s4, 0, 4\ns_cselect_b32 s5, 7, 23\nMvaa 140,76,12\n" \
"s_add_u32 s23, s23, s4\ns_sub_u32 s5, s5, s23\nMvaa 136,72,12\ns_lshl_b32 s42, s23, 2\ns_lshl_b32 s43, s5, 2\nMvaa 104,72,8\ns_sub_u32 s5, s5, s23\ns_lshl_b32 s37, s5, 18\n" \
"Mvaa 100,68,8\ns_lshl1_add_u32 s41, s5, -5\ns_lshl1_add_u32 s4, s23, -1\nMvaa 132,68,12\ns_lshl1_add_u32 s5, s23, -13\nMvaa 128,64,12\ns_cmp_lt_u32 s3, 128\ns_cselect_b32 s39, s4, 9\n" \
"s_cselect_b32 s40, 0, s5\nMvaa 96,64,8\ns_bfe_u32 s4, s32, 0x1000c\ns_lshr_b32 s5, s32, 13\nMvaa 160,64,16\ns_waitcnt lgkmcnt(0)\ns_barrier\nMvaa 192,64,20\n" \
"s_add_u32 m0, s88, 32768\ns_lshl1_add_u32 s4, s10, s4\nMvaa 196,68,20\nD 5,64,0,0\nMvaa 164,68,16\ns_lshl1_add_u32 s5, s22, s5\ns_mul_i32 s4, s4, 525312\nMvaa 168,72,16\n" \
"s_lshl_b32 s5, s5, 15\ns_add_u32 s4, s4, s5\nMvaa 200,72,20\ns_bitcmp1_b32 s33, 0\ns_cselect_b32 s5, 16810496, 0\ns_cselect_b32 s35, 8, 0\nMvaa 204,76,20\nD 5,64,s73,1024\n" \
"Mvaa 172,76,16\ns_add_u32 s4, s4, s5\ns_add_u32 s34, s4, 17039360\nMvaa 176,80,16\ns_lshl4_add_u32 s35, s11, s35\ns_add_u32 s35, s35, 256\nMvaa 208,80,20\ns_and_b32 s4, s3, 1\n" \
"s_lshl2_add_u32 s24, s4, s6\nMvaa 212,84,20\nD 5,64,s74,2048\nMvaa 180,84,16\ns_bfe_u32 s4, s3, 0x20001\ns_bfe_u32 s5, s3, 0x20005\nMvaa 184,88,16\ns_lshl2_add_u32 s25, s4, s5\n" \
"s_lshl4_add_u32 s26, s24, s25\nMvaa 216,88,20\nv_and_b32 v18, 0x70, v17\nv_lshlrev_b32 v19, 1, v17\nMvaa 220,92,20\nD 5,64,s75,3072\nMvaa 188,92,16\ns_sub_u32 s4, s24, s22\n" \
"v_sub_u32 v19, v19, v18\nMvaa 252,92,24\ns_lshl_b32 s4, s4, 19\nv_add_u32 v9, s4, v19\nMaaa 156,92,28\ns_add_u32 m0, s88, 36864\ns_lshl_b32 s4, s23, 18\nMaaa 152,88,28\n" \
"D 5,64,s76,0\nMvaa 248,88,24\ns_lshl_b32 s5, s22, 19\ns_sub_u32 s4, s4, s5\nMvaa 244,84,24\ns_mul_i32 s5, s32, 24\ns_sub_u32 s4, s4, s5\nMaaa 148,84,28\n" \
"v_add_u32 v13, s4, v19\nv_lshrrev_b32 v18, 13, v16\nMaaa 144,80,28\nD 5,64,s77,1024\nMvaa 240,80,24\nv_and_b32 v19, 0x70, v16\nv_mul_u32_u24 v18, 6144, v18\nMvaa 236,76,24\n" \
"s_lshl_b32 s4, s25, 7\nv_bfe_u32 v12, v16, 7, 1\nMaaa 140,76,28\nv_add3_u32 v18, v18, v19, s4\nv_xor_b32 v19, 1, v12\nMaaa 136,72,28\nD 5,64,s78,2048\nMvaa 232,72,24\n" \
"s_bfe_u32 s4, s32, 0x1000c\nv_lshl_add_u32 v10, v12, 11, v18\nMvaa 228,68,24\nv_lshl_add_u32 v11, v19, 11, v18\nv_xor_b32 v12, s4, v12\nMaaa 132,68,28\ns_mul_i32 s5, s32, 12\nv_mul_u32_u24 v12, 196608, v12\n" \
"Maaa 128,64,28\nD 5,64,s79,3072\nMvaa 224,64,24\ns_sub_u32 s5, 4096, s5\nv_add3_u32 v12, v18, v12, s5\nMvaa 32,96,32\ns_lshl3_add_u32 s48, s26, 4096\ns_add_u32 s4, s48, 4096\n" \
"Mvaa 64,96,36\ns_add_u32 m0, s32, 98304\ns_lshl_b32 s5, s26, 13\nMvaa 68,100,36\nbuffer_load_dwordx4 v6, s[68:71], 0 offen offset:0 nt lds\nMvaa 36,100,32\ns_lshr_b32 s6, s32, 1\ns_add_u32 s5, s5, s6\n" \
"Mvaa 40,104,32\ns_add_u32 s5, s5, 262144\ns_and_b32 s6, s3, 127\nMvaa 72,104,36\ns_lshl_b32 s6, s6, 14\ns_add_u32 s6, s6, s32\nMvaa 76,108,36\nbuffer_load_dwordx4 v6, s[68:71], s89 offen offset:1024 nt lds\n" \
"Mvaa 44,108,32\ns_cmp_lt_u32 s3, 128\ns_cselect_b32 s36, s4, s35\ns_cselect_b32 s46, s5, s6\nMvaa 48,112,32\ns_cmp_ge_u32 s32, 0x2000\ns_cselect_b32 s4, 0, 16\nMvaa 80,112,36\n" \
"s_sub_u32 s27, s26, s4\ns_xor_b32 s5, s32, 0x2000\nMvaa 84,116,36\nbuffer_load_dwordx4 v28, s[68:71], s90 offen offset:2048 nt lds\nMvaa 52,116,32\ns_lshl_b32 s4, s27, 13\ns_lshr_b32 s5, s5, 1\nMvaa 56,120,32\n" \
"s_add_u32 s4, s4, s5\ns_add_u32 s4, s4, 262144\nMvaa 88,120,36\ns_lshr_b32 s5, s32, 13\ns_or_b32 s5, s5, s24\nMvaa 92,124,36\nbuffer_load_dwordx4 v28, s[68:71], s91 offen offset:3072 nt lds\nMvaa 60,124,32\n" \
"s_cmp_eq_u32 s5, 0\ns_cselect_b32 s51, 1, 0\ns_cselect_b32 s47, 0x7fff0000, s4\ns_cselect_b32 s27, s26, s27\nMvaa 124,124,40\ns_lshl3_add_u32 s49, s27, 4096\ns_lshl_b32 s4, s26, 15\nMvaa 156,124,44\n" \
"s_add_u32 m0, s32, 114688\ns_and_b32 s5, s32, 0x2000\nMvaa 152,120,44\nbuffer_load_dwordx4 v6, s[68:71], s92 offen offset:0 nt lds\nMvaa 120,120,40\ns_lshl1_add_u32 s4, s5, s4\ns_bfe_u32 s5, s32, 0x1000c\nMvaa 116,116,40\n" \
"s_lshl_b32 s5, s5, 10\ns_add_u32 s4, s4, s5\nMvaa 148,116,44\ns_add_u32 s50, s4, 50660352\ns_lshr_b32 s6, s6, 12\nMvaa 144,112,44\nbuffer_load_dwordx4 v6, s[68:71], s93 offen offset:1024 nt lds\nMvaa 112,112,40\n" \
"s_mov_b32 s5, 182784\ns_mov_b32 s27, 2097152\nMvaa 108,108,40\nv_lshlrev_b32 v18, 3, v0\nv_cmp_gt_u32 vcc, 224, v0\nMvaa 140,108,44\ns_cmp_lt_u32 s6, 410\ns_cselect_b32 s4, 0, 410\n" \
"s_cselect_b32 s45, 734720, s5\ns_cselect_b64 s[56:57], s[14:15], s[12:13]\ns_cselect_b32 s27, 8388608, s27\nMvaa 136,104,44\nbuffer_load_dwordx4 v28, s[68:71], s94 offen offset:2048 nt lds\nMvaa 104,104,40\ns_sub_u32 s6, s6, s4\ns_mul_i32 s6, s6, 1792\n" \
"Mvaa 100,100,40\ns_cmp_lt_u32 s3, 128\ns_cselect_b32 s58, 0, s27\nMvaa 132,100,44\ns_swappc_b64 s[98:99], s[18:19]\ns_add_u32 s18, s18, 0x4000\ns_addc_u32 s19, s19, 0\nMvaa 128,96,44\n" \
"buffer_load_dwordx4 v28, s[68:71], s95 offen offset:3072 nt lds\nMvaa 96,96,40\ns_waitcnt vmcnt(17)\ns_barrier\nMvaa 160,96,48\nbuffer_load_dword v30, v4, s[52:55], 0 offen sc1\nTa 64,8,0\nv_add_u32 v5, 128, v5\n" \
"Mvaa 192,96,52\nTa 66,8,1024\nv_add_u32 v6, s72, v6\nMvaa 196,100,52\nRa 0,7,0\nv_add_u32 v28, s72, v28\nMvaa 164,100,48\nTa 68,20,0\n" \
"v_add_u32 v18, s6, v18\nMvaa 168,104,48\nTa 70,20,1024\ns_mov_b32 s59, 0x20000\nMvaa 200,104,52\nRa 4,7,2048\nv_cndmask_b32 v15, 2.0, v18, vcc\nMvaa 204,108,52\n" \
"v_add_u32 v4, s44, v4\nTa 72,21,0\nMvaa 172,108,48\nTa 74,21,1024\nMvaa 176,112,48\nRa 8,7,4096\nMvaa 208,112,52\nTa 76,22,0\n" \
"Mvaa 212,116,52\nTa 78,22,1024\nMvaa 180,116,48\nRa 12,7,6144\nMvaa 184,120,48\nTa 80,23,0\nMvaa 216,120,52\nTa 82,23,1024\n" \
"Mvaa 220,124,52\nRa 16,7,8192\nMvaa 188,124,48\nTa 84,24,0\nMvaa 252,124,56\nTa 86,24,1024\nMaaa 156,124,60\nRa 20,7,10240\n" \
"Maaa 152,120,60\nTa 88,25,0\nMvaa 248,120,56\nTa 90,25,1024\nMvaa 244,116,56\nRa 24,7,12288\nMaaa 148,116,60\nTa 92,26,0\n" \
"Maaa 144,112,60\nTa 94,26,1024\nMvaa 240,112,56\nRa 28,7,14336\nMvaa 236,108,56\nMaaa 140,108,60\nMaaa 136,104,60\nMvaa 232,104,56\n" \
"s_sub_u32 s96, s96, 1\nMvaa 228,100,56\ns_cmp_lg_u32 s96, 0\nMaaa 132,100,60\nMaaa 128,96,60\nMvaa 224,96,56\ns_waitcnt lgkmcnt(0)\ns_cbranch_scc0 zhK\n" \
"Mvaa 32,64,0\nTa 96,8,8192\nMvaa 64,64,4\nTa 98,8,9216\nMvaa 68,68,4\nRa 32,27,0\nMvaa 36,68,0\nTa 100,20,8192\n" \
"Mvaa 40,72,0\nTa 102,20,9216\nMvaa 72,72,4\nRa 36,27,2048\nMvaa 76,76,4\nTa 104,21,8192\nMvaa 44,76,0\nTa 106,21,9216\n" \
"Mvaa 48,80,0\nRa 40,27,4096\nMvaa 80,80,4\nTa 108,22,8192\nMvaa 84,84,4\nTa 110,22,9216\nMvaa 52,84,0\nRa 44,27,6144\n" \
"Mvaa 56,88,0\nTa 112,23,8192\nMvaa 88,88,4\nTa 114,23,9216\nMvaa 92,92,4\nRa 48,27,8192\nMvaa 60,92,0\nTa 116,24,8192\n" \
"Mvaa 124,92,8\nTa 118,24,9216\nMvaa 156,92,12\nRa 52,27,10240\nMvaa 152,88,12\nTa 120,25,8192\nMvaa 120,88,8\nTa 122,25,9216\n" \
"Mvaa 116,84,8\nRa 56,27,12288\nMvaa 148,84,12\nTa 124,26,8192\nMvaa 144,80,12\nTa 126,26,9216\nMvaa 112,80,8\nRa 60,27,14336\n" \
"Mvaa 108,76,8\nMvaa 140,76,12\nMvaa 136,72,12\nMvaa 104,72,8\nMvaa 100,68,8\nMvaa 132,68,12\nMvaa 128,64,12\nMvaa 96,64,8\n" \
"Mvaa 160,64,16\ns_waitcnt lgkmcnt(0)\ns_barrier\nMvaa 192,64,20\ns_add_u32 m0, s88, 0\nMvaa 196,68,20\nD 5,64,0,0\nMvaa 164,68,16\n" \
"Mvaa 168,72,16\nMvaa 200,72,20\nMvaa 204,76,20\nD 5,64,s73,1024\nMvaa 172,76,16\nMvaa 176,80,16\nMvaa 208,80,20\nMvaa 212,84,20\n" \
"D 5,64,s74,2048\nMvaa 180,84,16\nMvaa 184,88,16\nMvaa 216,88,20\nMvaa 220,92,20\nD 5,64,s75,3072\nMvaa 188,92,16\nMvaa 252,92,24\n" \
"Maaa 156,92,28\ns_add_u32 m0, s88, 4096\nMaaa 152,88,28\nD 5,64,s76,0\nMvaa 248,88,24\nMvaa 244,84,24\nMaaa 148,84,28\nMaaa 144,80,28\n" \
"D 5,64,s77,1024\nMvaa 240,80,24\nMvaa 236,76,24\nMaaa 140,76,28\nMaaa 136,72,28\nD 5,64,s78,2048\nMvaa 232,72,24\nMvaa 228,68,24\n" \
"Maaa 132,68,28\nMaaa 128,64,28\nD 5,64,s79,3072\nMvaa 224,64,24\ns_branch zgK\nzhK:\ns_bitcmp1_b32 s33, 1\ns_cbranch_scc1 zxK\n" \
"Mvaa 32,64,0\nTa 96,8,8192\ns_mov_b64 s[64:65], s[84:85]\nMvaa 64,64,4\nTa 98,8,9216\ns_mov_b32 s66, 4194304\nMvaa 68,68,4\nRa 32,27,0\n" \
"s_mov_b64 s[68:69], s[86:87]\nMvaa 36,68,0\nTa 100,20,8192\ns_branch 99f\n.p2align 12\nzp2:\ns_setpc_b64 s[98:99]\n99:\n" \
"s_mov_b32 s70, 8388608\nMvaa 40,72,0\nTa 102,20,9216\ns_mov_b32 s73, 15360\nMvaa 72,72,4\nRa 36,27,2048\ns_mov_b32 s74, 129024\nMvaa 76,76,4\n" \
"Ta 104,21,8192\ns_mov_b32 s75, 144384\nMvaa 44,76,0\nTa 106,21,9216\ns_add_u32 s76, s37, 0\nMvaa 48,80,0\nRa 40,27,4096\ns_add_u32 s77, s37, 15360\n" \
"Mvaa 80,80,4\nTa 108,22,8192\ns_add_u32 s78, s37, 129024\nMvaa 84,84,4\nTa 110,22,9216\ns_add_u32 s79, s37, 144384\nMvaa 52,84,0\nRa 44,27,6144\n" \
"s_mov_b32 s89, 31744\nMvaa 56,88,0\nTa 112,23,8192\ns_mov_b32 s90, 63488\nMvaa 88,88,4\nTa 114,23,9216\ns_mov_b32 s91, 95232\nMvaa 92,92,4\n" \
"Ra 48,27,8192\ns_mov_b32 s72, 524288\nMvaa 60,92,0\nTa 116,24,8192\nv_xor_b32 v28, 128, v14\nMvaa 124,92,8\nTa 118,24,9216\nMvaa 156,92,12\n" \
"Ra 52,27,10240\nMvaa 152,88,12\nTa 120,25,8192\nMvaa 120,88,8\nTa 122,25,9216\nMvaa 116,84,8\nRa 56,27,12288\nMvaa 148,84,12\n" \
"Ta 124,26,8192\nMvaa 144,80,12\nTa 126,26,9216\nMvaa 112,80,8\nRa 60,27,14336\nMvaa 108,76,8\nMvaa 140,76,12\nMvaa 136,72,12\n" \
"Mvaa 104,72,8\nMvaa 100,68,8\nMvaa 132,68,12\nMvaa 128,64,12\nMvaa 96,64,8\nMvaa 160,64,16\ns_waitcnt lgkmcnt(0)\ns_barrier\n" \
"Mvaa 192,64,20\ns_add_u32 m0, s88, 131072\ns_and_b32 s10, s3, 2\nMvaa 196,68,20\nD 13,64,0,0\nMvaa 164,68,16\ns_bfe_u32 s100, s3, 0x10002\ns_lshl_b32 s10, s10, 1\n" \
"Mvaa 168,72,16\ns_bfe_u32 s2, s9, 0x10010\ns_xor_b32 s99, s2, s3\nMvaa 200,72,20\ns_and_b32 s99, s99, 1\ns_lshl1_add_u32 s99, s100, s99\nMvaa 204,76,20\nD 13,64,s73,1024\n" \
"Mvaa 172,76,16\ns_bfe_u32 s101, s3, 0x20003\ns_lshl3_add_u32 s11, s99, s101\nMvaa 176,80,16\ns_add_u32 s101, s101, s10\ns_lshl3_add_u32 s10, s2, s10\nMvaa 208,80,20\ns_bfe_u32 s2, s3, 0x20005\n" \
"s_lshl1_add_u32 s2, s2, s100\nMvaa 212,84,20\nD 13,64,s74,2048\nMvaa 180,84,16\ns_bfe_u32 s23, s9, 0x80008\ns_and_b32 s22, s9, 255\nMvaa 184,88,16\ns_lshr_b32 s6, 4, s99\n" \
"s_lshr_b32 s25, s3, 5\nMvaa 216,88,20\ns_lshl_b32 s4, s25, 8\ns_lshl_b32 s18, s25, 21\nMvaa 220,92,20\nD 13,64,s75,3072\nMvaa 188,92,16\ns_lshl3_add_u32 s25, s25, s101\n" \
"s_lshl_b32 s26, s25, 18\nMvaa 252,92,24\ns_lshl_b32 s27, s25, 7\ns_add_u32 s4, s4, 16384\nMaaa 156,92,28\ns_add_u32 m0, s88, 135168\ns_xor_b32 s25, s11, s10\nMaaa 152,88,28\n" \
"D 13,64,s76,0\nMvaa 248,88,24\ns_lshl3_add_u32 s31, s25, s4\ns_lshl_b32 s5, s2, 7\nMvaa 244,84,24\ns_add_u32 s5, s5, 8192\ns_lshr_b32 s25, s32, 1\nMaaa 148,84,28\n" \
"s_add_u32 s18, s18, s25\ns_add_u32 s18, s18, 54854656\nMaaa 144,80,28\nD 13,64,s77,1024\nMvaa 240,80,24\ns_lshl_b32 s19, s2, 19\ns_add_u32 s19, s19, s25\nMvaa 236,76,24\n" \
"s_add_u32 s19, s19, 48563200\ns_lshr_b32 s25, s32, 7\nMaaa 140,76,28\ns_add_u32 s27, s27, s25\ns_lshl3_add_u32 s27, s99, s27\nMaaa 136,72,28\nD 13,64,s78,2048\nMvaa 232,72,24\n" \
"s_add_u32 s27, s27, 32768\ns_lshl1_add_u32 s26, s32, s26\nMvaa 228,68,24\ns_add_u32 s26, s26, 71631872\ns_lshl_b32 s28, s99, 15\nMaaa 132,68,28\ns_add_u32 s24, s26, s28\ns_sub_u32 s28, 0x10000, s28\n" \
"Maaa 128,64,28\nD 13,64,s79,3072\nMvaa 224,64,24\ns_lshl_b32 s25, s100, 17\ns_add_u32 s26, s26, s25\nMvaa 32,96,32\ns_cmp_eq_u32 s99, 0\ns_cselect_b32 s21, 128, 0\n" \
"Mvaa 64,96,36\ns_add_u32 m0, s32, 81920\ns_lshl_b32 s25, s21, 10\nMvaa 68,100,36\nD 14,68,0,0\nMvaa 36,100,32\ns_add_u32 s24, s24, s25\ns_add_u32 s25, s24, 0x20000\n" \
"Mvaa 40,104,32\ns_lshl_b32 s101, s101, 8\ns_and_b32 s20, s32, 0x2000\nMvaa 72,104,36\ns_lshl3_add_u32 s20, s20, s101\ns_and_b32 s99, s32, 0x1000\nMvaa 76,108,36\nD 14,68,s89,1024\n" \
"Mvaa 44,108,32\ns_lshl2_add_u32 s20, s99, s20\ns_lshl4_add_u32 s101, s32, s101\nMvaa 48,112,32\ns_lshl_b32 s2, s2, 20\ns_add_u32 s2, s2, s101\nMvaa 80,112,36\ns_lshl_b32 s100, s100, 7\n" \
"s_add_u32 s101, s101, s100\nMvaa 84,116,36\nD 28,68,s90,2048\nMvaa 52,116,32\ns_add_u32 s101, s101, 2048\ns_lshl_b32 s29, s42, 17\nMvaa 56,120,32\ns_lshl_b32 s30, s43, 17\n" \
"s_add_u32 s29, s29, s101\nMvaa 88,120,36\ns_add_u32 s30, s30, s101\ns_add_u32 s101, s2, 524288\nMvaa 92,124,36\nD 28,68,s91,3072\nMvaa 60,124,32\ns_cmp_eq_u32 s23, 2\n" \
"s_cselect_b32 s29, s2, s29\ns_cselect_b32 s30, s101, s30\ns_cselect_b32 s7, 0xffff, 0\ns_cselect_b32 s9, 0xff, 0\nMvaa 124,124,40\ns_and_b32 s101, s10, 8\ns_lshl_b32 s9, s9, s101\nMvaa 156,124,44\n" \
"s_add_u32 m0, s88, 0\nv_add_u32 v13, 128, v13\nMvaa 152,120,44\nD 13,64,0,0\nMvaa 120,120,40\ns_mov_b32 s2, zqS-zq0-8192\nMvaa 116,116,40\ns_cmp_eq_u32 s23, 1\n" \
"s_cselect_b32 s2, zqR-zq0-8192, s2\nMvaa 148,116,44\ns_cmp_eq_u32 s23, 0\ns_cselect_b32 s2, zqT-zq0-8192, s2\nMvaa 144,112,44\nD 13,64,s73,1024\nMvaa 112,112,40\ns_cmp_lt_u32 s32, 0x2000\n" \
"s_cselect_b32 s2, 0, s2\nMvaa 108,108,40\ns_add_u32 s2, s2, s32\nMvaa 140,108,44\nMvaa 136,104,44\nD 13,64,s74,2048\nMvaa 104,104,40\nMvaa 100,100,40\n" \
"Mvaa 132,100,44\nMvaa 128,96,44\nD 13,64,s75,3072\nMvaa 96,96,40\ns_waitcnt vmcnt(17)\ns_barrier\nMvaa 160,96,48\nbuffer_load_dword v30, v4, s[52:55], 0 offen sc1\n" \
"Ta 64,8,32768\nMvaa 192,96,52\nTa 66,8,33792\nMvaa 196,100,52\nRa 0,7,32768\nMvaa 164,100,48\nTa 68,20,32768\nMvaa 168,104,48\n" \
"Ta 70,20,33792\nMvaa 200,104,52\nRa 4,7,34816\nMvaa 204,108,52\nv_add_u32 v4, s44, v4\nTa 72,21,32768\nMvaa 172,108,48\nTa 74,21,33792\n" \
"Mvaa 176,112,48\nRa 8,7,36864\nMvaa 208,112,52\nTa 76,22,32768\nMvaa 212,116,52\nTa 78,22,33792\nMvaa 180,116,48\nRa 12,7,38912\n" \
"Mvaa 184,120,48\nTa 80,23,32768\nMvaa 216,120,52\nTa 82,23,33792\nMvaa 220,124,52\nRa 16,7,40960\nMvaa 188,124,48\nTa 84,24,32768\n" \
"Mvaa 252,124,56\nTa 86,24,33792\nMaaa 156,124,60\nRa 20,7,43008\nMaaa 152,120,60\nTa 88,25,32768\nMvaa 248,120,56\nTa 90,25,33792\n" \
"Mvaa 244,116,56\nRa 24,7,45056\nMaaa 148,116,60\nTa 92,26,32768\nMaaa 144,112,60\nTa 94,26,33792\nMvaa 240,112,56\nRa 28,7,47104\n" \
"Mvaa 236,108,56\nMaaa 140,108,60\nMaaa 136,104,60\nMvaa 232,104,56\nMvaa 228,100,56\nMaaa 132,100,60\nMaaa 128,96,60\nMvaa 224,96,56\n" \
"s_waitcnt lgkmcnt(0)\nMvaa 32,64,0\nTa 96,8,40960\nMvaa 64,64,4\nTa 98,8,41984\nMvaa 68,68,4\nRa 32,27,32768\nMvaa 36,68,0\n" \
"Ta 100,20,40960\nMvaa 40,72,0\nTa 102,20,41984\nMvaa 72,72,4\nRa 36,27,34816\nMvaa 76,76,4\nTa 104,21,40960\nMvaa 44,76,0\n" \
"Ta 106,21,41984\nMvaa 48,80,0\nRa 40,27,36864\nMvaa 80,80,4\nTa 108,22,40960\nMvaa 84,84,4\nTa 110,22,41984\nMvaa 52,84,0\n" \
"Ra 44,27,38912\nMvaa 56,88,0\nTa 112,23,40960\nMvaa 88,88,4\nTa 114,23,41984\nMvaa 92,92,4\nRa 48,27,40960\nMvaa 60,92,0\n" \
"Ta 116,24,40960\nMvaa 124,92,8\nTa 118,24,41984\nMvaa 156,92,12\nRa 52,27,43008\nMvaa 152,88,12\nTa 120,25,40960\nMvaa 120,88,8\n" \
"Ta 122,25,41984\nMvaa 116,84,8\nRa 56,27,45056\nMvaa 148,84,12\nTa 124,26,40960\nMvaa 144,80,12\nTa 126,26,41984\nMvaa 112,80,8\n" \
"Ra 60,27,47104\nMvaa 108,76,8\ns_and_b32 s96, s32, 0x1000\ns_lshl_b32 s97, s96, 1\ns_lshl_b32 s96, s96, 2\nMvaa 140,76,12\nv_add_u32 v16, s97, v7\nv_subrev_u32 v20, s96, v8\n" \
"v_xor_b32 v17, 64, v16\nMvaa 136,72,12\nv_add_u32 v18, 0x20000, v16\nv_add_u32 v19, 0x20000, v17\nv_xor_b32 v21, 32, v20\nMvaa 104,72,8\nv_xor_b32 v22, 64, v20\nv_xor_b32 v23, 96, v20\n" \
"v_xor_b32 v24, 128, v20\nMvaa 100,68,8\nv_xor_b32 v25, 160, v20\nv_xor_b32 v26, 192, v20\nv_xor_b32 v27, 224, v20\nMvaa 132,68,12\nMvaa 128,64,12\nMvaa 96,64,8\n" \
"Mvaa 160,64,16\ns_waitcnt vmcnt(5) lgkmcnt(0)\ns_barrier\nMvaa 192,64,20\ns_add_u32 m0, s88, 4096\nMvaa 196,68,20\nD 13,64,s76,0\nMvaa 164,68,16\n" \
"Ta 160,20,24576\nMvaa 168,72,16\nTa 162,20,25600\nMvaa 200,72,20\nD 13,64,s77,1024\nMvaa 204,76,20\nTa 164,21,24576\nMvaa 172,76,16\n" \
"Ta 166,21,25600\nMvaa 176,80,16\nD 13,64,s78,2048\nMvaa 208,80,20\nTa 168,22,24576\nMvaa 212,84,20\nTa 170,22,25600\nMvaa 180,84,16\n" \
"D 13,64,s79,3072\nMvaa 184,88,16\nTa 172,23,24576\nMvaa 216,88,20\ns_add_u32 m0, s32, 65536\nv_add_u32 v14, s72, v14\nv_add_u32 v28, s72, v28\nMvaa 220,92,20\n" \
"D 14,68,0,0\nMvaa 188,92,16\nTa 174,23,25600\nMvaa 252,92,24\nTa 176,24,24576\nMaaa 156,92,28\nD 14,68,s89,1024\nMaaa 152,88,28\n" \
"Ta 178,24,25600\nMvaa 248,88,24\nTa 180,25,24576\nMvaa 244,84,24\nD 28,68,s90,2048\nMaaa 148,84,28\nTa 182,25,25600\nMaaa 144,80,28\n" \
"Ta 184,26,24576\nMvaa 240,80,24\nD 28,68,s91,3072\nMvaa 236,76,24\nTa 186,26,25600\nMaaa 140,76,28\ns_add_u32 m0, s88, 32768\nv_add_u32 v13, 128, v13\n" \
"Maaa 136,72,28\nD 13,64,0,0\nMvaa 232,72,24\nTa 188,27,24576\nMvaa 228,68,24\nTa 190,27,25600\nMaaa 132,68,28\nD 13,64,s73,1024\n" \
"Maaa 128,64,28\nMvaa 224,64,24\nMvaa 32,96,32\nD 13,64,s74,2048\nMvaa 36,100,32\nTa 64,20,16384\nMvaa 40,104,32\nTa 66,20,17408\n" \
"Mvaa 44,108,32\nD 13,64,s75,3072\nMvaa 48,112,32\nMvaa 52,116,32\ns_add_u32 m0, s88, 36864\nMvaa 56,120,32\nD 13,64,s76,0\nMvaa 60,124,32\n" \
"Ra 0,18,0\nMvaa 64,96,36\nMvaa 68,100,36\nD 13,64,s77,1024\nMvaa 72,104,36\nRa 16,19,0\nC 32,32,33\nC 33,34,35\n" \
"Mvaa 76,108,36\nC 34,36,37\nC 35,38,39\nC 36,40,41\nMvaa 80,112,36\nD 13,64,s78,2048\nC 37,42,43\nMvaa 84,116,36\n" \
"Ta 68,21,16384\nC 38,44,45\nC 39,46,47\nMvaa 88,120,36\nC 40,48,49\nC 41,50,51\nC 42,52,53\nMvaa 92,124,36\n" \
"D 13,64,s79,3072\nC 43,54,55\nMvaa 96,96,40\nTa 70,21,17408\nC 44,56,57\nC 45,58,59\nMvaa 100,100,40\ns_add_u32 m0, s32, 98304\n" \
"v_add_u32 v14, s72, v14\nv_add_u32 v28, s72, v28\nMvaa 104,104,40\nD 14,68,0,0\nC 46,60,61\nMvaa 108,108,40\nC 47,62,63\nC 48,64,65\n" \
"C 49,66,67\nMvaa 112,112,40\nTa 72,22,16384\nC 50,68,69\nC 51,70,71\nMvaa 116,116,40\nD 14,68,s89,1024\nC 52,72,73\n" \
"Mvaa 120,120,40\nTa 74,22,17408\nC 53,74,75\nC 54,76,77\nMvaa 124,124,40\nC 55,78,79\nC 56,80,81\nC 57,82,83\n" \
"Mvaa 128,96,44\nD 28,68,s90,2048\nC 58,84,85\nMvaa 132,100,44\nTa 76,23,16384\nC 59,86,87\nC 60,88,89\nMvaa 136,104,44\n" \
"C 61,90,91\nC 62,92,93\nC 63,94,95\nMvaa 140,108,44\nD 28,68,s91,3072\nC 64,96,97\nMvaa 144,112,44\nTa 78,23,17408\n" \
"C 65,98,99\nC 66,100,101\nMvaa 148,116,44\nC 67,102,103\nC 68,104,105\nC 69,106,107\nMvaa 152,120,44\nbuffer_load_dword v30, v4, s[52:55], 0 offen sc1\n" \
"C 70,108,109\nMvaa 156,124,44\nTa 80,24,16384\nC 71,110,111\nC 72,112,113\nMaaa 128,96,60\nC 73,114,115\nC 74,116,117\n" \
"C 75,118,119\nMaaa 132,100,60\nTa 82,24,17408\nC 76,120,121\nC 77,122,123\nMaaa 136,104,60\nC 78,124,125\nC 79,126,127\n" \
"C 80,128,129\nMaaa 140,108,60\nTa 84,25,16384\nC 81,130,131\nC 82,132,133\nMaaa 144,112,60\nv_add_u32 v4, s44, v4\nC 83,134,135\n" \
"C 84,136,137\nMaaa 148,116,60\nTa 86,25,17408\nC 85,138,139\nC 86,140,141\ns_branch 99f\n.p2align 12\nzp3:\n" \
"s_setpc_b64 s[98:99]\n99:\nMaaa 152,120,60\nC 87,142,143\nC 88,144,145\nC 89,146,147\nMaaa 156,124,60\nTa 88,26,16384\n" \
"C 90,148,149\nC 91,150,151\nMvaa 160,96,48\nC 92,152,153\nC 93,154,155\nC 94,156,157\nMvaa 164,100,48\nTa 90,26,17408\n" \
"C 95,158,159\nMvaa 168,104,48\nAR 96,128\nAR 97,129\nAR 98,130\nMvaa 172,108,48\nTa 92,27,16384\nAR 99,131\n" \
"AR 100,132\nMvaa 176,112,48\nAR 101,133\nAR 102,134\nAR 103,135\nMvaa 180,116,48\nTa 94,27,17408\nC 96,96,97\n" \
"C 97,98,99\nMvaa 184,120,48\nC 98,100,101\nC 99,102,103\nAW 240,96\nMvaa 188,124,48\nRa 4,18,2048\nAW 241,97\n" \
"AW 242,98\nMvaa 192,96,52\nAW 243,99\nAR 104,136\nAR 105,137\nMvaa 196,100,52\nRa 20,19,2048\nAR 106,138\n" \
"AR 107,139\nMvaa 200,104,52\nAR 108,140\nAR 109,141\nAR 110,142\nMvaa 204,108,52\nRa 8,18,4096\nAR 111,143\n" \
"C 104,104,105\nMvaa 208,112,52\nC 105,106,107\nC 106,108,109\nC 107,110,111\nMvaa 212,116,52\nRa 24,19,4096\nAW 244,104\n" \
"AW 245,105\nMvaa 216,120,52\nAW 246,106\nAW 247,107\nAR 112,144\nMvaa 220,124,52\nRa 12,18,6144\nAR 113,145\n" \
"AR 114,146\nMvaa 224,96,56\nAR 115,147\nAR 116,148\nAR 117,149\nMvaa 228,100,56\nRa 28,19,6144\nAR 118,150\n" \
"AR 119,151\nMvaa 232,104,56\nC 112,112,113\nC 113,114,115\nC 114,116,117\nMvaa 236,108,56\nC 115,118,119\nAW 248,112\n" \
"AW 249,113\nMvaa 240,112,56\nAW 250,114\nAW 251,115\nAR 120,152\nMvaa 244,116,56\nAR 121,153\nAR 122,154\n" \
"AR 123,155\nMvaa 248,120,56\nAR 124,156\nAR 125,157\nAR 126,158\nMvaa 252,124,56\nAR 127,159\nC 120,120,121\n" \
"C 121,122,123\ns_waitcnt lgkmcnt(6)\nzY:\nZvaa 128,64,0\nv_add_u32 v13, 128, v13\nC 122,124,125\nC 123,126,127\nMvaa 128,160,16\n" \
"s_add_u32 m0, s88, 131072\nAW 252,120\nAW 253,121\nZvaa 132,164,16\nD 13,64,0,0\nAW 254,122\nMvaa 132,68,0\nv_add_u32 v14, s72, v14\n" \
"AW 255,123\nC 160,160,161\nZvaa 136,72,0\nv_add_u32 v28, s72, v28\nC 161,162,163\nC 162,164,165\nMvaa 136,168,16\nC 163,166,167\n" \
"AW 192,160\nAW 193,161\nv_mov_b32 v4, v15\nZvaa 140,172,16\nAW 194,162\nAW 195,163\nC 168,168,169\nMvaa 140,76,0\n" \
"s_waitcnt lgkmcnt(0)\nD 13,64,s73,1024\nZvaa 144,80,0\nC 169,170,171\nC 170,172,173\nC 171,174,175\nMvaa 144,176,16\nAW 196,168\n" \
"AW 197,169\nAW 198,170\nZvaa 148,180,16\nAW 199,171\nC 176,176,177\nC 177,178,179\nMvaa 148,84,0\nC 178,180,181\n" \
"C 179,182,183\nAW 200,176\nZvaa 152,88,0\nD 13,64,s74,2048\nAW 201,177\nMvaa 152,184,16\nAW 202,178\nAW 203,179\n" \
"C 184,184,185\nZvaa 156,188,16\nC 185,186,187\nC 186,188,189\nC 187,190,191\nAW 204,184\nAW 205,185\nAW 206,186\n" \
"AW 207,187\nMvaa 156,92,0\nC 192,192,193\nC 193,194,195\nC 194,196,197\nZvaa 188,92,4\ns_waitcnt vmcnt(16)\ns_barrier\n" \
"C 195,198,199\nMvaa 188,188,20\nTa 96,20,0\nTa 98,20,1024\nAW 208,192\nZvaa 184,184,20\nD 13,64,s75,3072\nAW 209,193\n" \
"Mvaa 184,88,4\nTa 128,20,8192\nAW 210,194\nAW 211,195\nZvaa 180,84,4\nTa 130,20,9216\nC 200,200,201\nC 201,202,203\n" \
"Mvaa 180,180,20\nRa 32,16,0\nRa 48,17,0\nC 202,204,205\nZvaa 176,176,20\ns_add_u32 m0, s88, 135168\nC 203,206,207\nAW 212,200\n" \
"Mvaa 176,80,4\nD 13,64,s76,0\nAW 213,201\nZvaa 172,76,4\nTa 100,21,0\nAW 214,202\nAW 215,203\nMvaa 172,172,20\n" \
"Ta 102,21,1024\nTa 132,21,8192\nC 208,208,209\nZvaa 168,168,20\nTa 134,21,9216\nC 209,210,211\nC 210,212,213\nMvaa 168,72,4\n" \
"Ta 104,22,0\nC 211,214,215\nAW 216,208\nZvaa 164,68,4\nD 13,64,s77,1024\nAW 217,209\nMvaa 164,164,20\nTa 106,22,1024\n" \
"Ta 136,22,8192\nAW 218,210\nZvaa 160,160,20\nTa 138,22,9216\nAW 219,211\nC 216,216,217\nC 217,218,219\nC 218,220,221\n" \
"C 219,222,223\nAW 220,216\nAW 221,217\nAW 222,218\nAW 223,219\nMvaa 160,64,4\nTa 108,23,0\nC 224,224,225\n" \
"C 225,226,227\nZvaa 192,64,8\nTa 110,23,1024\nC 226,228,229\nC 227,230,231\nMvaa 192,160,24\nD 13,64,s78,2048\nAW 224,224\n" \
"Zvaa 196,164,24\nTa 140,23,8192\nTa 142,23,9216\nAW 225,225\nMvaa 196,68,8\nTa 112,24,0\nAW 226,226\nAW 227,227\n" \
"Zvaa 200,72,8\nTa 114,24,1024\nC 232,232,233\nC 233,234,235\nMvaa 200,168,24\nTa 144,24,8192\nC 234,236,237\nC 235,238,239\n" \
"Zvaa 204,172,24\nD 13,64,s79,3072\nAW 228,232\nMvaa 204,76,8\nTa 146,24,9216\nTa 116,25,0\nAW 229,233\nZvaa 208,80,8\n" \
"Ta 118,25,1024\nAW 230,234\nAW 231,235\nMvaa 208,176,24\nv_add_u32 v13, 128, v13\nTa 148,25,8192\nC 240,240,241\nZvaa 212,180,24\n" \
"s_add_u32 m0, s32, 81920\nC 241,242,243\nC 242,244,245\nMvaa 212,84,8\nD 14,68,0,0\nC 243,246,247\nZvaa 216,88,8\nTa 150,25,9216\n" \
"Ta 120,26,0\nAW 232,240\nMvaa 216,184,24\nTa 122,26,1024\nAW 233,241\nAW 234,242\nZvaa 220,188,24\nTa 152,26,8192\n" \
"AW 235,243\nC 248,248,249\nC 249,250,251\nC 250,252,253\nC 251,254,255\nAW 236,248\nAW 237,249\nAW 238,250\n" \
"AW 239,251\nMvaa 220,92,8\nTa 154,26,9216\nZvaa 252,92,12\nD 14,68,s89,1024\nMvaa 252,188,28\nTa 124,27,0\nTa 126,27,1024\n" \
"Zvaa 248,184,28\nTa 156,27,8192\nMvaa 248,88,12\nTa 158,27,9216\nZvaa 244,84,12\nRa 36,16,2048\nMvaa 244,180,28\nD 28,68,s90,2048\n" \
"Zvaa 240,176,28\nRa 52,17,2048\nRa 40,16,4096\nMvaa 240,80,12\nRa 56,17,4096\nZvaa 236,76,12\nRa 44,16,6144\nMvaa 236,172,28\n" \
"Ra 60,17,6144\nZvaa 232,168,28\nD 28,68,s91,3072\nMvaa 232,72,12\nZvaa 228,68,12\nMvaa 228,164,28\nv_add_u32 v14, s72, v14\nZvaa 224,160,28\n" \
"v_add_u32 v28, s72, v28\nMvaa 224,64,12\ns_waitcnt lgkmcnt(6)\nMvaa 128,96,32\nMvaa 128,128,48\ns_add_u32 m0, s88, 0\nMvaa 132,132,48\nD 13,64,0,0\n" \
"Mvaa 132,100,32\nMvaa 136,104,32\nMvaa 136,136,48\nMvaa 140,140,48\nMvaa 140,108,32\ns_waitcnt lgkmcnt(0)\nD 13,64,s73,1024\nMvaa 144,112,32\n" \
"Mvaa 144,144,48\nMvaa 148,148,48\nMvaa 148,116,32\nMvaa 152,120,32\nD 13,64,s74,2048\nMvaa 152,152,48\nMvaa 156,156,48\nMvaa 156,124,32\n" \
"Mvaa 188,124,36\ns_waitcnt vmcnt(16)\ns_barrier\nMvaa 188,156,52\nTa 64,20,32768\nTa 66,20,33792\nMvaa 184,152,52\nD 13,64,s75,3072\n" \
"Mvaa 184,120,36\nTa 160,20,40960\nMvaa 180,116,36\nTa 162,20,41984\nMvaa 180,148,52\nRa 0,16,32768\nRa 16,17,32768\nMvaa 176,144,52\n" \
"s_add_u32 m0, s88, 4096\nMvaa 176,112,36\nD 13,64,s76,0\nMvaa 172,108,36\nTa 68,21,32768\nMvaa 172,140,52\nTa 70,21,33792\nTa 164,21,40960\n" \
"Mvaa 168,136,52\nTa 166,21,41984\nMvaa 168,104,36\nTa 72,22,32768\nMvaa 164,100,36\nD 13,64,s77,1024\nMvaa 164,132,52\nTa 74,22,33792\n" \
"Ta 168,22,40960\nMvaa 160,128,52\nTa 170,22,41984\nMvaa 160,96,36\nTa 76,23,32768\nMvaa 192,96,40\nTa 78,23,33792\nMvaa 192,128,56\n" \
"D 13,64,s78,2048\nMvaa 196,132,56\nTa 172,23,40960\nTa 174,23,41984\nMvaa 196,100,40\nTa 80,24,32768\nMvaa 200,104,40\nTa 82,24,33792\n" \
"Mvaa 200,136,56\nTa 176,24,40960\nMvaa 204,140,56\nD 13,64,s79,3072\nMvaa 204,108,40\nTa 178,24,41984\nTa 84,25,32768\nMvaa 208,112,40\n" \
"Ta 86,25,33792\nMvaa 208,144,56\nv_add_u32 v13, 128, v13\nTa 180,25,40960\nMvaa 212,148,56\ns_add_u32 m0, s32, 65536\nMvaa 212,116,40\nD 14,68,0,0\n" \
"Mvaa 216,120,40\nTa 182,25,41984\nTa 88,26,32768\nMvaa 216,152,56\nTa 90,26,33792\nMvaa 220,156,56\nTa 184,26,40960\nMvaa 220,124,40\n" \
"Ta 186,26,41984\nMvaa 252,124,44\nD 14,68,s89,1024\nMvaa 252,156,60\nTa 92,27,32768\nTa 94,27,33792\nMvaa 248,152,60\nTa 188,27,40960\n" \
"Mvaa 248,120,44\nTa 190,27,41984\nMvaa 244,116,44\nRa 4,16,34816\nMvaa 244,148,60\nD 28,68,s90,2048\nMvaa 240,144,60\nRa 20,17,34816\n" \
"Ra 8,16,36864\nMvaa 240,112,44\nRa 24,17,36864\nMvaa 236,108,44\nRa 12,16,38912\nMvaa 236,140,60\nRa 28,17,38912\nMvaa 232,136,60\n" \
"D 28,68,s91,3072\nMvaa 232,104,44\nMvaa 228,100,44\nMvaa 228,132,60\nv_add_u32 v14, s72, v14\nMvaa 224,128,60\nv_add_u32 v28, s72, v28\nMvaa 224,96,44\n" \
"s_waitcnt lgkmcnt(6)\nMvaa 128,64,0\nMvaa 128,160,16\ns_add_u32 m0, s88, 32768\nMvaa 132,164,16\nD 13,64,0,0\nMvaa 132,68,0\nMvaa 136,72,0\n" \
"Mvaa 136,168,16\nMvaa 140,172,16\nMvaa 140,76,0\ns_waitcnt lgkmcnt(0)\nD 13,64,s73,1024\nMvaa 144,80,0\nMvaa 144,176,16\nMvaa 148,180,16\n" \
"Mvaa 148,84,0\nMvaa 152,88,0\nD 13,64,s74,2048\nMvaa 152,184,16\nMvaa 156,188,16\nMvaa 156,92,0\nMvaa 188,92,4\ns_waitcnt vmcnt(15)\n" \
"s_barrier\nMvaa 188,188,20\nTa 96,20,16384\nTa 98,20,17408\nMvaa 184,184,20\nD 13,64,s75,3072\nMvaa 184,88,4\nTa 128,20,24576\n" \
"Mvaa 180,84,4\nTa 130,20,25600\nMvaa 180,180,20\nRa 32,18,0\nRa 48,19,0\nMvaa 176,176,20\ns_add_u32 m0, s88, 36864\nMvaa 176,80,4\n" \
"D 13,64,s76,0\nMvaa 172,76,4\nTa 100,21,16384\nMvaa 172,172,20\nTa 102,21,17408\nTa 132,21,24576\nMvaa 168,168,20\nTa 134,21,25600\n" \
"Mvaa 168,72,4\nTa 104,22,16384\nMvaa 164,68,4\nD 13,64,s77,1024\nMvaa 164,164,20\nTa 106,22,17408\nTa 136,22,24576\nMvaa 160,160,20\n" \
"Ta 138,22,25600\nMvaa 160,64,4\nTa 108,23,16384\nMvaa 192,64,8\ns_branch 99f\n.p2align 12\nzp4:\ns_setpc_b64 s[98:99]\n" \
"99:\nTa 110,23,17408\nMvaa 192,160,24\nD 13,64,s78,2048\nMvaa 196,164,24\nTa 140,23,24576\nTa 142,23,25600\nMvaa 196,68,8\n" \
"Ta 112,24,16384\nMvaa 200,72,8\nTa 114,24,17408\nMvaa 200,168,24\nTa 144,24,24576\nMvaa 204,172,24\nD 13,64,s79,3072\nMvaa 204,76,8\n" \
"Ta 146,24,25600\nTa 116,25,16384\nMvaa 208,80,8\nTa 118,25,17408\nMvaa 208,176,24\nv_add_u32 v13, 128, v13\nTa 148,25,24576\nMvaa 212,180,24\n" \
"s_add_u32 m0, s32, 98304\nMvaa 212,84,8\nD 14,68,0,0\nMvaa 216,88,8\nTa 150,25,25600\nTa 120,26,16384\nMvaa 216,184,24\nTa 122,26,17408\n" \
"Mvaa 220,188,24\nTa 152,26,24576\nMvaa 220,92,8\nTa 154,26,25600\nMvaa 252,92,12\nD 14,68,s89,1024\nMvaa 252,188,28\nTa 124,27,16384\n" \
"Ta 126,27,17408\nMvaa 248,184,28\nTa 156,27,24576\nMvaa 248,88,12\nTa 158,27,25600\nMvaa 244,84,12\nRa 36,18,2048\nMvaa 244,180,28\n" \
"D 28,68,s90,2048\nMvaa 240,176,28\nRa 52,19,2048\nRa 40,18,4096\nMvaa 240,80,12\nRa 56,19,4096\nMvaa 236,76,12\nRa 44,18,6144\n" \
"Mvaa 236,172,28\nRa 60,19,6144\nMvaa 232,168,28\nD 28,68,s91,3072\nMvaa 232,72,12\nMvaa 228,68,12\nMvaa 228,164,28\nv_add_u32 v14, s72, v14\n" \
"Mvaa 224,160,28\nv_add_u32 v28, s72, v28\nMvaa 224,64,12\ns_waitcnt lgkmcnt(6)\nMvaa 128,96,32\nMvaa 128,128,48\ns_add_u32 m0, s88, 131072\nMvaa 132,132,48\n" \
"D 13,64,0,0\nMvaa 132,100,32\nMvaa 136,104,32\nMvaa 136,136,48\nMvaa 140,140,48\nMvaa 140,108,32\ns_waitcnt lgkmcnt(0)\nD 13,64,s73,1024\n" \
"Mvaa 144,112,32\nMvaa 144,144,48\nMvaa 148,148,48\nMvaa 148,116,32\nMvaa 152,120,32\nD 13,64,s74,2048\nMvaa 152,152,48\nMvaa 156,156,48\n" \
"Mvaa 156,124,32\nMvaa 188,124,36\ns_waitcnt vmcnt(15)\ns_barrier\nMvaa 188,156,52\nTa 64,20,0\nTa 66,20,1024\nMvaa 184,152,52\n" \
"D 13,64,s75,3072\nMvaa 184,120,36\nTa 160,20,8192\nMvaa 180,116,36\nTa 162,20,9216\nMvaa 180,148,52\nRa 0,16,0\nRa 16,17,0\n" \
"Mvaa 176,144,52\ns_add_u32 m0, s88, 135168\nMvaa 176,112,36\nD 13,64,s76,0\nMvaa 172,108,36\nTa 68,21,0\nMvaa 172,140,52\nTa 70,21,1024\n" \
"Ta 164,21,8192\nMvaa 168,136,52\nTa 166,21,9216\nMvaa 168,104,36\nTa 72,22,0\nMvaa 164,100,36\nD 13,64,s77,1024\nMvaa 164,132,52\n" \
"Ta 74,22,1024\nTa 168,22,8192\nMvaa 160,128,52\nTa 170,22,9216\nMvaa 160,96,36\nTa 76,23,0\nMvaa 192,96,40\nTa 78,23,1024\n" \
"Mvaa 192,128,56\nD 13,64,s78,2048\nMvaa 196,132,56\nTa 172,23,8192\nTa 174,23,9216\nMvaa 196,100,40\nTa 80,24,0\nMvaa 200,104,40\n" \
"Ta 82,24,1024\nMvaa 200,136,56\nTa 176,24,8192\nMvaa 204,140,56\nD 13,64,s79,3072\nMvaa 204,108,40\nTa 178,24,9216\nTa 84,25,0\n" \
"Mvaa 208,112,40\nTa 86,25,1024\nMvaa 208,144,56\nv_add_u32 v13, 128, v13\nTa 180,25,8192\nMvaa 212,148,56\ns_add_u32 m0, s32, 81920\nMvaa 212,116,40\n" \
"D 14,68,0,0\nMvaa 216,120,40\nTa 182,25,9216\nTa 88,26,0\nMvaa 216,152,56\nTa 90,26,1024\nMvaa 220,156,56\nTa 184,26,8192\n" \
"Mvaa 220,124,40\nTa 186,26,9216\nMvaa 252,124,44\nD 14,68,s89,1024\nMvaa 252,156,60\nTa 92,27,0\nTa 94,27,1024\nMvaa 248,152,60\n" \
"Ta 188,27,8192\nMvaa 248,120,44\nTa 190,27,9216\nMvaa 244,116,44\nRa 4,16,2048\nMvaa 244,148,60\nD 28,68,s90,2048\nMvaa 240,144,60\n" \
"Ra 20,17,2048\nRa 8,16,4096\nMvaa 240,112,44\nRa 24,17,4096\nMvaa 236,108,44\nRa 12,16,6144\nMvaa 236,140,60\nRa 28,17,6144\n" \
"Mvaa 232,136,60\nD 28,68,s91,3072\nMvaa 232,104,44\nMvaa 228,100,44\nMvaa 228,132,60\nv_add_u32 v14, s72, v14\nMvaa 224,128,60\nv_add_u32 v28, s72, v28\n" \
"Mvaa 224,96,44\ns_waitcnt lgkmcnt(6)\nMvaa 128,64,0\nMvaa 128,160,16\ns_add_u32 m0, s88, 0\nMvaa 132,164,16\nD 13,64,0,0\nMvaa 132,68,0\n" \
"Mvaa 136,72,0\nMvaa 136,168,16\nMvaa 140,172,16\nMvaa 140,76,0\ns_waitcnt lgkmcnt(0)\nD 13,64,s73,1024\nMvaa 144,80,0\nMvaa 144,176,16\n" \
"Mvaa 148,180,16\nMvaa 148,84,0\nMvaa 152,88,0\nD 13,64,s74,2048\nMvaa 152,184,16\nMvaa 156,188,16\nMvaa 156,92,0\nMvaa 188,92,4\n" \
"s_waitcnt vmcnt(15)\ns_barrier\nMvaa 188,188,20\nTa 96,20,32768\nTa 98,20,33792\nMvaa 184,184,20\nD 13,64,s75,3072\nMvaa 184,88,4\n" \
"Ta 128,20,40960\nMvaa 180,84,4\nTa 130,20,41984\nMvaa 180,180,20\nRa 32,16,32768\nRa 48,17,32768\nMvaa 176,176,20\ns_add_u32 m0, s88, 4096\n" \
"Mvaa 176,80,4\nD 13,64,s76,0\nMvaa 172,76,4\nTa 100,21,32768\nMvaa 172,172,20\nTa 102,21,33792\nTa 132,21,40960\nMvaa 168,168,20\n" \
"Ta 134,21,41984\nMvaa 168,72,4\nTa 104,22,32768\nMvaa 164,68,4\nD 13,64,s77,1024\nMvaa 164,164,20\nTa 106,22,33792\nTa 136,22,40960\n" \
"Mvaa 160,160,20\nTa 138,22,41984\nMvaa 160,64,4\nTa 108,23,32768\nMvaa 192,64,8\nTa 110,23,33792\nMvaa 192,160,24\nD 13,64,s78,2048\n" \
"Mvaa 196,164,24\nTa 140,23,40960\nTa 142,23,41984\nMvaa 196,68,8\nTa 112,24,32768\nMvaa 200,72,8\nTa 114,24,33792\nMvaa 200,168,24\n" \
"Ta 144,24,40960\nMvaa 204,172,24\nD 13,64,s79,3072\nMvaa 204,76,8\nTa 146,24,41984\nTa 116,25,32768\nMvaa 208,80,8\nTa 118,25,33792\n" \
"Mvaa 208,176,24\nv_add_u32 v13, 128, v13\nTa 148,25,40960\nMvaa 212,180,24\ns_add_u32 m0, s32, 65536\nMvaa 212,84,8\nD 14,68,0,0\nMvaa 216,88,8\n" \
"Ta 150,25,41984\nTa 120,26,32768\nMvaa 216,184,24\nTa 122,26,33792\nMvaa 220,188,24\nTa 152,26,40960\nMvaa 220,92,8\nTa 154,26,41984\n" \
"Mvaa 252,92,12\nD 14,68,s89,1024\nMvaa 252,188,28\nTa 124,27,32768\nTa 126,27,33792\nMvaa 248,184,28\nTa 156,27,40960\nMvaa 248,88,12\n" \
"Ta 158,27,41984\nMvaa 244,84,12\nRa 36,16,34816\nMvaa 244,180,28\nD 28,68,s90,2048\nMvaa 240,176,28\nRa 52,17,34816\nRa 40,16,36864\n" \
"Mvaa 240,80,12\nRa 56,17,36864\nMvaa 236,76,12\nRa 44,16,38912\nMvaa 236,172,28\nRa 60,17,38912\nMvaa 232,168,28\nD 28,68,s91,3072\n" \
"Mvaa 232,72,12\nMvaa 228,68,12\nMvaa 228,164,28\nv_add_u32 v14, s72, v14\nMvaa 224,160,28\nv_add_u32 v28, s72, v28\nMvaa 224,64,12\ns_waitcnt lgkmcnt(6)\n" \
"Mvaa 128,96,32\nMvaa 128,128,48\ns_add_u32 m0, s88, 32768\nMvaa 132,132,48\nD 13,64,0,0\nMvaa 132,100,32\nMvaa 136,104,32\nMvaa 136,136,48\n" \
"Mvaa 140,140,48\nMvaa 140,108,32\ns_waitcnt lgkmcnt(0)\nD 13,64,s73,1024\nMvaa 144,112,32\nMvaa 144,144,48\nMvaa 148,148,48\nMvaa 148,116,32\n" \
"Mvaa 152,120,32\nD 13,64,s74,2048\nMvaa 152,152,48\nMvaa 156,156,48\nMvaa 156,124,32\nMvaa 188,124,36\ns_waitcnt vmcnt(15)\ns_barrier\n" \
"Mvaa 188,156,52\nTa 64,20,16384\nTa 66,20,17408\nMvaa 184,152,52\nD 13,64,s75,3072\nMvaa 184,120,36\nTa 160,20,24576\nMvaa 180,116,36\n" \
"Ta 162,20,25600\nMvaa 180,148,52\nRa 0,18,0\nRa 16,19,0\nMvaa 176,144,52\ns_add_u32 m0, s88, 36864\nMvaa 176,112,36\nD 13,64,s76,0\n" \
"Mvaa 172,108,36\nTa 68,21,16384\nMvaa 172,140,52\nTa 70,21,17408\nTa 164,21,24576\nMvaa 168,136,52\nTa 166,21,25600\nMvaa 168,104,36\n" \
"Ta 72,22,16384\nMvaa 164,100,36\nD 13,64,s77,1024\nMvaa 164,132,52\nTa 74,22,17408\nTa 168,22,24576\nMvaa 160,128,52\nTa 170,22,25600\n" \
"Mvaa 160,96,36\nTa 76,23,16384\nMvaa 192,96,40\nTa 78,23,17408\nMvaa 192,128,56\nD 13,64,s78,2048\nMvaa 196,132,56\nTa 172,23,24576\n" \
"Ta 174,23,25600\nMvaa 196,100,40\nTa 80,24,16384\nMvaa 200,104,40\nTa 82,24,17408\nMvaa 200,136,56\nTa 176,24,24576\nMvaa 204,140,56\n" \
"D 13,64,s79,3072\nMvaa 204,108,40\nTa 178,24,25600\nTa 84,25,16384\nMvaa 208,112,40\nTa 86,25,17408\nMvaa 208,144,56\nv_add_u32 v13, 128, v13\n" \
"Ta 180,25,24576\nMvaa 212,148,56\ns_add_u32 m0, s32, 98304\nMvaa 212,116,40\nD 14,68,0,0\nMvaa 216,120,40\nTa 182,25,25600\nTa 88,26,16384\n" \
"Mvaa 216,152,56\nTa 90,26,17408\nMvaa 220,156,56\nTa 184,26,24576\nMvaa 220,124,40\nTa 186,26,25600\nMvaa 252,124,44\nD 14,68,s89,1024\n" \
"Mvaa 252,156,60\nTa 92,27,16384\nTa 94,27,17408\nMvaa 248,152,60\nTa 188,27,24576\nMvaa 248,120,44\nTa 190,27,25600\nMvaa 244,116,44\n" \
"Ra 4,18,2048\nMvaa 244,148,60\nD 28,68,s90,2048\nMvaa 240,144,60\nRa 20,19,2048\nRa 8,18,4096\nMvaa 240,112,44\nRa 24,19,4096\n" \
"Mvaa 236,108,44\nRa 12,18,6144\nMvaa 236,140,60\nRa 28,19,6144\nMvaa 232,136,60\nD 28,68,s91,3072\nMvaa 232,104,44\nMvaa 228,100,44\n" \
"Mvaa 228,132,60\nv_add_u32 v14, s72, v14\nMvaa 224,128,60\nv_add_u32 v28, s72, v28\nMvaa 224,96,44\ns_waitcnt lgkmcnt(6)\nMvaa 128,64,0\nMvaa 128,160,16\n" \
"s_add_u32 m0, s88, 131072\nMvaa 132,164,16\nD 13,64,0,0\nMvaa 132,68,0\nMvaa 136,72,0\nMvaa 136,168,16\nMvaa 140,172,16\nMvaa 140,76,0\n" \
"s_waitcnt lgkmcnt(0)\nD 13,64,s73,1024\nMvaa 144,80,0\nMvaa 144,176,16\ns_add_u32 s95, s34, 0\nMvaa 148,180,16\nbuffer_store_dwordx4 v[32:35], v0, s[60:63], s95 offen offset:0 sc1\nMvaa 148,84,0\n" \
"Mvaa 152,88,0\nD 13,64,s74,2048\nMvaa 152,184,16\nMvaa 156,188,16\nMvaa 156,92,0\nMvaa 188,92,4\ns_waitcnt vmcnt(16)\ns_barrier\n" \
"Mvaa 188,188,20\nTa 96,20,0\nTa 98,20,1024\nMvaa 184,184,20\nD 13,64,s75,3072\nMvaa 184,88,4\nTa 128,20,8192\nMvaa 180,84,4\n" \
"Ta 130,20,9216\nMvaa 180,180,20\nRa 32,16,0\nRa 48,17,0\nMvaa 176,176,20\ns_add_u32 m0, s88, 135168\nMvaa 176,80,4\nD 13,64,s76,0\n" \
"Mvaa 172,76,4\nTa 100,21,0\nMvaa 172,172,20\nTa 102,21,1024\nTa 132,21,8192\nMvaa 168,168,20\nTa 134,21,9216\nMvaa 168,72,4\n" \
"Ta 104,22,0\nMvaa 164,68,4\nD 13,64,s77,1024\nMvaa 164,164,20\nTa 106,22,1024\nTa 136,22,8192\nMvaa 160,160,20\nTa 138,22,9216\n" \
"Mvaa 160,64,4\nTa 108,23,0\nMvaa 192,64,8\nTa 110,23,1024\nMvaa 192,160,24\nD 13,64,s78,2048\nMvaa 196,164,24\nTa 140,23,8192\n" \
"Ta 142,23,9216\nMvaa 196,68,8\nTa 112,24,0\nMvaa 200,72,8\nTa 114,24,1024\nMvaa 200,168,24\nTa 144,24,8192\nMvaa 204,172,24\n" \
"D 13,64,s79,3072\nMvaa 204,76,8\nTa 146,24,9216\nTa 116,25,0\nMvaa 208,80,8\ns_branch 99f\n.p2align 12\nzp5:\n" \
"s_setpc_b64 s[98:99]\n99:\nTa 118,25,1024\nMvaa 208,176,24\nv_add_u32 v13, 128, v13\nTa 148,25,8192\nMvaa 212,180,24\ns_add_u32 m0, s32, 81920\n" \
"Mvaa 212,84,8\nD 14,68,0,0\nMvaa 216,88,8\nTa 150,25,9216\nTa 120,26,0\nMvaa 216,184,24\nTa 122,26,1024\nMvaa 220,188,24\n" \
"Ta 152,26,8192\nMvaa 220,92,8\nTa 154,26,9216\nMvaa 252,92,12\nD 14,68,s89,1024\nMvaa 252,188,28\nTa 124,27,0\nTa 126,27,1024\n" \
"Mvaa 248,184,28\nTa 156,27,8192\nMvaa 248,88,12\nTa 158,27,9216\nMvaa 244,84,12\nRa 36,16,2048\nMvaa 244,180,28\nD 28,68,s90,2048\n" \
"Mvaa 240,176,28\nRa 52,17,2048\nRa 40,16,4096\nMvaa 240,80,12\nRa 56,17,4096\nMvaa 236,76,12\nRa 44,16,6144\nMvaa 236,172,28\n" \
"Ra 60,17,6144\nMvaa 232,168,28\nD 28,68,s91,3072\nMvaa 232,72,12\nMvaa 228,68,12\nMvaa 228,164,28\nv_add_u32 v14, s72, v14\nMvaa 224,160,28\n" \
"v_add_u32 v28, s72, v28\nMvaa 224,64,12\ns_waitcnt lgkmcnt(6)\nMvaa 128,96,32\nMvaa 128,128,48\ns_add_u32 m0, s88, 0\nMvaa 132,132,48\nD 13,64,0,0\n" \
"Mvaa 132,100,32\nMvaa 136,104,32\nMvaa 136,136,48\nMvaa 140,140,48\nMvaa 140,108,32\ns_waitcnt lgkmcnt(0)\nD 13,64,s73,1024\nMvaa 144,112,32\n" \
"Mvaa 144,144,48\nMvaa 148,148,48\nbuffer_store_dwordx4 v[36:39], v0, s[60:63], s95 offen offset:1024 sc1\nMvaa 148,116,32\nMvaa 152,120,32\nD 13,64,s74,2048\nMvaa 152,152,48\nMvaa 156,156,48\n" \
"Mvaa 156,124,32\nMvaa 188,124,36\ns_waitcnt vmcnt(17)\ns_barrier\nMvaa 188,156,52\nTa 64,20,32768\nTa 66,20,33792\nMvaa 184,152,52\n" \
"D 13,64,s75,3072\nMvaa 184,120,36\nTa 160,20,40960\nMvaa 180,116,36\nTa 162,20,41984\nMvaa 180,148,52\nRa 0,16,32768\nRa 16,17,32768\n" \
"Mvaa 176,144,52\ns_add_u32 m0, s88, 4096\nMvaa 176,112,36\nD 13,64,s76,0\nMvaa 172,108,36\nTa 68,21,32768\nMvaa 172,140,52\nTa 70,21,33792\n" \
"Ta 164,21,40960\nMvaa 168,136,52\nTa 166,21,41984\nMvaa 168,104,36\nTa 72,22,32768\nMvaa 164,100,36\nD 13,64,s77,1024\nMvaa 164,132,52\n" \
"Ta 74,22,33792\nTa 168,22,40960\nMvaa 160,128,52\nTa 170,22,41984\nMvaa 160,96,36\nTa 76,23,32768\nMvaa 192,96,40\nTa 78,23,33792\n" \
"Mvaa 192,128,56\nD 13,64,s78,2048\nMvaa 196,132,56\nTa 172,23,40960\nTa 174,23,41984\nMvaa 196,100,40\nTa 80,24,32768\nMvaa 200,104,40\n" \
"Ta 82,24,33792\nMvaa 200,136,56\nTa 176,24,40960\nMvaa 204,140,56\nD 13,64,s79,3072\nMvaa 204,108,40\nTa 178,24,41984\nTa 84,25,32768\n" \
"Mvaa 208,112,40\nTa 86,25,33792\nMvaa 208,144,56\nv_add_u32 v13, 128, v13\nTa 180,25,40960\nMvaa 212,148,56\ns_add_u32 m0, s32, 65536\nMvaa 212,116,40\n" \
"D 14,68,0,0\nMvaa 216,120,40\nTa 182,25,41984\nTa 88,26,32768\nMvaa 216,152,56\nTa 90,26,33792\nMvaa 220,156,56\nTa 184,26,40960\n" \
"Mvaa 220,124,40\nTa 186,26,41984\nMvaa 252,124,44\nD 14,68,s89,1024\nMvaa 252,156,60\nTa 92,27,32768\nTa 94,27,33792\nMvaa 248,152,60\n" \
"Ta 188,27,40960\nMvaa 248,120,44\nTa 190,27,41984\nMvaa 244,116,44\nRa 4,16,34816\nMvaa 244,148,60\nD 28,68,s90,2048\nMvaa 240,144,60\n" \
"Ra 20,17,34816\nRa 8,16,36864\nMvaa 240,112,44\nRa 24,17,36864\nMvaa 236,108,44\nRa 12,16,38912\nMvaa 236,140,60\nRa 28,17,38912\n" \
"Mvaa 232,136,60\nD 28,68,s91,3072\nMvaa 232,104,44\nMvaa 228,100,44\nMvaa 228,132,60\nv_add_u32 v14, s72, v14\nMvaa 224,128,60\nv_add_u32 v28, s72, v28\n" \
"Mvaa 224,96,44\ns_waitcnt lgkmcnt(6)\nMvaa 128,64,0\nMvaa 128,160,16\ns_add_u32 m0, s88, 32768\nMvaa 132,164,16\nD 13,64,0,0\nMvaa 132,68,0\n" \
"Mvaa 136,72,0\nMvaa 136,168,16\nMvaa 140,172,16\nMvaa 140,76,0\ns_waitcnt lgkmcnt(0)\nD 13,64,s73,1024\nMvaa 144,80,0\nMvaa 144,176,16\n" \
"Mvaa 148,180,16\nbuffer_store_dwordx4 v[40:43], v0, s[60:63], s95 offen offset:2048 sc1\nMvaa 148,84,0\nMvaa 152,88,0\nD 13,64,s74,2048\nMvaa 152,184,16\nMvaa 156,188,16\nMvaa 156,92,0\n" \
"Mvaa 188,92,4\ns_waitcnt vmcnt(17)\ns_barrier\nMvaa 188,188,20\nTa 96,20,16384\nTa 98,20,17408\nMvaa 184,184,20\nD 13,64,s75,3072\n" \
"Mvaa 184,88,4\nTa 128,20,24576\nMvaa 180,84,4\nTa 130,20,25600\nMvaa 180,180,20\nRa 32,18,0\nRa 48,19,0\nMvaa 176,176,20\n" \
"s_add_u32 m0, s88, 36864\nMvaa 176,80,4\nD 13,64,s76,0\nMvaa 172,76,4\nTa 100,21,16384\nMvaa 172,172,20\nTa 102,21,17408\nTa 132,21,24576\n" \
"Mvaa 168,168,20\nTa 134,21,25600\nMvaa 168,72,4\nTa 104,22,16384\nMvaa 164,68,4\nD 13,64,s77,1024\nMvaa 164,164,20\nTa 106,22,17408\n" \
"Ta 136,22,24576\nMvaa 160,160,20\nTa 138,22,25600\nMvaa 160,64,4\nTa 108,23,16384\nMvaa 192,64,8\nTa 110,23,17408\nMvaa 192,160,24\n" \
"D 13,64,s78,2048\nMvaa 196,164,24\nTa 140,23,24576\nTa 142,23,25600\nMvaa 196,68,8\nTa 112,24,16384\nMvaa 200,72,8\nTa 114,24,17408\n" \
"Mvaa 200,168,24\nTa 144,24,24576\nMvaa 204,172,24\nD 13,64,s79,3072\nMvaa 204,76,8\nTa 146,24,25600\nTa 116,25,16384\nMvaa 208,80,8\n" \
"Ta 118,25,17408\nMvaa 208,176,24\nv_add_u32 v13, 128, v13\nTa 148,25,24576\nMvaa 212,180,24\ns_add_u32 m0, s32, 98304\nMvaa 212,84,8\nD 14,68,0,0\n" \
"Mvaa 216,88,8\nTa 150,25,25600\nTa 120,26,16384\nMvaa 216,184,24\nTa 122,26,17408\nMvaa 220,188,24\nTa 152,26,24576\nMvaa 220,92,8\n" \
"Ta 154,26,25600\nMvaa 252,92,12\nD 14,68,s89,1024\nMvaa 252,188,28\nTa 124,27,16384\nTa 126,27,17408\nMvaa 248,184,28\nTa 156,27,24576\n" \
"Mvaa 248,88,12\nTa 158,27,25600\nMvaa 244,84,12\nRa 36,18,2048\nMvaa 244,180,28\nD 28,68,s90,2048\nMvaa 240,176,28\nRa 52,19,2048\n" \
"Ra 40,18,4096\nMvaa 240,80,12\nRa 56,19,4096\nMvaa 236,76,12\nRa 44,18,6144\nMvaa 236,172,28\nRa 60,19,6144\nMvaa 232,168,28\n" \
"D 28,68,s91,3072\nMvaa 232,72,12\nMvaa 228,68,12\nbuffer_store_dwordx4 v[44:47], v0, s[60:63], s95 offen offset:3072 sc1\nMvaa 228,164,28\nv_add_u32 v14, s72, v14\nMvaa 224,160,28\nv_add_u32 v28, s72, v28\n" \
"Mvaa 224,64,12\ns_waitcnt lgkmcnt(6)\nMvaa 128,96,32\nMvaa 128,128,48\ns_add_u32 m0, s88, 131072\nMvaa 132,132,48\nD 13,64,0,0\nMvaa 132,100,32\n" \
"Mvaa 136,104,32\nMvaa 136,136,48\nMvaa 140,140,48\nMvaa 140,108,32\ns_waitcnt lgkmcnt(0)\nD 13,64,s73,1024\nMvaa 144,112,32\nMvaa 144,144,48\n" \
"s_add_u32 s95, s34, 4096\nMvaa 148,148,48\nbuffer_store_dwordx4 v[48:51], v0, s[60:63], s95 offen offset:0 sc1\nMvaa 148,116,32\nMvaa 152,120,32\nD 13,64,s74,2048\nMvaa 152,152,48\nMvaa 156,156,48\n" \
"Mvaa 156,124,32\nMvaa 188,124,36\ns_waitcnt vmcnt(18)\ns_barrier\nMvaa 188,156,52\nTa 64,20,0\nTa 66,20,1024\nMvaa 184,152,52\n" \
"D 13,64,s75,3072\nMvaa 184,120,36\nTa 160,20,8192\nMvaa 180,116,36\nTa 162,20,9216\nMvaa 180,148,52\nRa 0,16,0\nRa 16,17,0\n" \
"Mvaa 176,144,52\ns_add_u32 m0, s88, 135168\nMvaa 176,112,36\nD 13,64,s76,0\nMvaa 172,108,36\nTa 68,21,0\nMvaa 172,140,52\nTa 70,21,1024\n" \
"Ta 164,21,8192\nMvaa 168,136,52\nTa 166,21,9216\nMvaa 168,104,36\nTa 72,22,0\nMvaa 164,100,36\nD 13,64,s77,1024\nMvaa 164,132,52\n" \
"Ta 74,22,1024\nTa 168,22,8192\nMvaa 160,128,52\nTa 170,22,9216\nMvaa 160,96,36\nTa 76,23,0\nMvaa 192,96,40\nTa 78,23,1024\n" \
"Mvaa 192,128,56\nD 13,64,s78,2048\nMvaa 196,132,56\nTa 172,23,8192\nTa 174,23,9216\nMvaa 196,100,40\nTa 80,24,0\nMvaa 200,104,40\n" \
"Ta 82,24,1024\nMvaa 200,136,56\nTa 176,24,8192\nMvaa 204,140,56\nD 13,64,s79,3072\nMvaa 204,108,40\nTa 178,24,9216\nTa 84,25,0\n" \
"Mvaa 208,112,40\nTa 86,25,1024\nMvaa 208,144,56\nv_add_u32 v13, 128, v13\nTa 180,25,8192\nMvaa 212,148,56\ns_add_u32 m0, s32, 81920\nMvaa 212,116,40\n" \
"D 14,68,0,0\nMvaa 216,120,40\nTa 182,25,9216\nTa 88,26,0\nMvaa 216,152,56\nTa 90,26,1024\nMvaa 220,156,56\nTa 184,26,8192\n" \
"Mvaa 220,124,40\nTa 186,26,9216\nMvaa 252,124,44\nD 14,68,s89,1024\nMvaa 252,156,60\nTa 92,27,0\nTa 94,27,1024\nMvaa 248,152,60\n" \
"Ta 188,27,8192\nMvaa 248,120,44\nTa 190,27,9216\nMvaa 244,116,44\nRa 4,16,2048\nMvaa 244,148,60\nD 28,68,s90,2048\nMvaa 240,144,60\n" \
"Ra 20,17,2048\nRa 8,16,4096\nMvaa 240,112,44\nRa 24,17,4096\nMvaa 236,108,44\nRa 12,16,6144\nMvaa 236,140,60\nRa 28,17,6144\n" \
"Mvaa 232,136,60\nD 28,68,s91,3072\nMvaa 232,104,44\nMvaa 228,100,44\nbuffer_store_dwordx4 v[52:55], v0, s[60:63], s95 offen offset:1024 sc1\nMvaa 228,132,60\nv_add_u32 v14, s72, v14\nMvaa 224,128,60\n" \
"v_add_u32 v28, s72, v28\nMvaa 224,96,44\ns_waitcnt lgkmcnt(6)\nMvaa 128,64,0\nMvaa 128,160,16\ns_add_u32 m0, s88, 0\nMvaa 132,164,16\nD 13,64,0,0\n" \
"Mvaa 132,68,0\nMvaa 136,72,0\nMvaa 136,168,16\nMvaa 140,172,16\nMvaa 140,76,0\ns_waitcnt lgkmcnt(0)\nD 13,64,s73,1024\nMvaa 144,80,0\n" \
"Mvaa 144,176,16\nMvaa 148,180,16\nbuffer_store_dwordx4 v[56:59], v0, s[60:63], s95 offen offset:2048 sc1\nMvaa 148,84,0\nMvaa 152,88,0\nD 13,64,s74,2048\nMvaa 152,184,16\nMvaa 156,188,16\n" \
"Mvaa 156,92,0\nMvaa 188,92,4\ns_waitcnt vmcnt(19)\ns_barrier\nMvaa 188,188,20\nTa 96,20,32768\nTa 98,20,33792\nMvaa 184,184,20\n" \
"D 13,64,s75,3072\nMvaa 184,88,4\nTa 128,20,40960\nMvaa 180,84,4\nTa 130,20,41984\nMvaa 180,180,20\nRa 32,16,32768\nRa 48,17,32768\n" \
"Mvaa 176,176,20\ns_add_u32 m0, s88, 4096\nMvaa 176,80,4\nD 13,64,s76,0\nMvaa 172,76,4\nTa 100,21,32768\nMvaa 172,172,20\nTa 102,21,33792\n" \
"Ta 132,21,40960\nMvaa 168,168,20\nTa 134,21,41984\nMvaa 168,72,4\nTa 104,22,32768\nMvaa 164,68,4\nD 13,64,s77,1024\nMvaa 164,164,20\n" \
"Ta 106,22,33792\nTa 136,22,40960\nMvaa 160,160,20\nTa 138,22,41984\nMvaa 160,64,4\nTa 108,23,32768\nMvaa 192,64,8\nTa 110,23,33792\n" \
"Mvaa 192,160,24\nD 13,64,s78,2048\nMvaa 196,164,24\nTa 140,23,40960\nTa 142,23,41984\nMvaa 196,68,8\nTa 112,24,32768\nMvaa 200,72,8\n" \
"Ta 114,24,33792\nMvaa 200,168,24\nTa 144,24,40960\nMvaa 204,172,24\nD 13,64,s79,3072\nMvaa 204,76,8\nTa 146,24,41984\nTa 116,25,32768\n" \
"Mvaa 208,80,8\nTa 118,25,33792\nMvaa 208,176,24\nv_add_u32 v13, 128, v13\nTa 148,25,40960\nMvaa 212,180,24\ns_add_u32 m0, s32, 65536\nMvaa 212,84,8\n" \
"D 14,68,0,0\nMvaa 216,88,8\nTa 150,25,41984\nTa 120,26,32768\nMvaa 216,184,24\nTa 122,26,33792\ns_branch 99f\n.p2align 12\n" \
"zp6:\ns_setpc_b64 s[98:99]\n99:\nMvaa 220,188,24\nTa 152,26,40960\nMvaa 220,92,8\nTa 154,26,41984\nMvaa 252,92,12\n" \
"D 14,68,s89,1024\nMvaa 252,188,28\nTa 124,27,32768\nTa 126,27,33792\nMvaa 248,184,28\nTa 156,27,40960\nMvaa 248,88,12\nTa 158,27,41984\n" \
"Mvaa 244,84,12\nRa 36,16,34816\nMvaa 244,180,28\nD 28,68,s90,2048\nMvaa 240,176,28\nRa 52,17,34816\nRa 40,16,36864\nMvaa 240,80,12\n" \
"Ra 56,17,36864\nMvaa 236,76,12\nRa 44,16,38912\nMvaa 236,172,28\nRa 60,17,38912\nMvaa 232,168,28\nD 28,68,s91,3072\nMvaa 232,72,12\n" \
"Mvaa 228,68,12\nbuffer_store_dwordx4 v[60:63], v0, s[60:63], s95 offen offset:3072 sc1\nMvaa 228,164,28\nv_add_u32 v14, s72, v14\nMvaa 224,160,28\nv_add_u32 v28, s72, v28\nMvaa 224,64,12\ns_waitcnt lgkmcnt(6)\n" \
"Mvaa 128,96,32\nMvaa 128,128,48\ns_add_u32 m0, s88, 32768\nMvaa 132,132,48\nD 13,64,0,0\nMvaa 132,100,32\nMvaa 136,104,32\nMvaa 136,136,48\n" \
"Mvaa 140,140,48\nMvaa 140,108,32\ns_waitcnt lgkmcnt(0)\nD 13,64,s73,1024\nMvaa 144,112,32\nMvaa 144,144,48\ns_add_u32 s95, s34, 8192\nMvaa 148,148,48\n" \
"buffer_store_dwordx4 v[64:67], v0, s[60:63], s95 offen offset:0 sc1\nMvaa 148,116,32\nMvaa 152,120,32\nD 13,64,s74,2048\nMvaa 152,152,48\nMvaa 156,156,48\nMvaa 156,124,32\nMvaa 188,124,36\n" \
"s_waitcnt vmcnt(19)\ns_barrier\nMvaa 188,156,52\nTa 64,20,16384\nTa 66,20,17408\nMvaa 184,152,52\nD 13,64,s75,3072\nMvaa 184,120,36\n" \
"Ta 160,20,24576\nMvaa 180,116,36\nTa 162,20,25600\nMvaa 180,148,52\nRa 0,18,0\nRa 16,19,0\nMvaa 176,144,52\ns_add_u32 m0, s88, 36864\n" \
"Mvaa 176,112,36\nD 13,64,s76,0\nMvaa 172,108,36\nTa 68,21,16384\nMvaa 172,140,52\nTa 70,21,17408\nTa 164,21,24576\nMvaa 168,136,52\n" \
"Ta 166,21,25600\nMvaa 168,104,36\nTa 72,22,16384\nMvaa 164,100,36\nD 13,64,s77,1024\nMvaa 164,132,52\nTa 74,22,17408\nTa 168,22,24576\n" \
"Mvaa 160,128,52\nTa 170,22,25600\nMvaa 160,96,36\nTa 76,23,16384\nMvaa 192,96,40\nTa 78,23,17408\nMvaa 192,128,56\nD 13,64,s78,2048\n" \
"Mvaa 196,132,56\nTa 172,23,24576\nTa 174,23,25600\nMvaa 196,100,40\nTa 80,24,16384\nMvaa 200,104,40\nTa 82,24,17408\nMvaa 200,136,56\n" \
"Ta 176,24,24576\nMvaa 204,140,56\nD 13,64,s79,3072\nMvaa 204,108,40\nTa 178,24,25600\nTa 84,25,16384\nMvaa 208,112,40\nTa 86,25,17408\n" \
"Mvaa 208,144,56\nv_add_u32 v13, 128, v13\nTa 180,25,24576\nMvaa 212,148,56\ns_add_u32 m0, s32, 98304\nMvaa 212,116,40\nD 14,68,0,0\nMvaa 216,120,40\n" \
"Ta 182,25,25600\nTa 88,26,16384\nMvaa 216,152,56\nTa 90,26,17408\nMvaa 220,156,56\nTa 184,26,24576\nMvaa 220,124,40\nTa 186,26,25600\n" \
"Mvaa 252,124,44\nD 14,68,s89,1024\nMvaa 252,156,60\nTa 92,27,16384\nTa 94,27,17408\nMvaa 248,152,60\nTa 188,27,24576\nMvaa 248,120,44\n" \
"Ta 190,27,25600\nMvaa 244,116,44\nRa 4,18,2048\nMvaa 244,148,60\nD 28,68,s90,2048\nMvaa 240,144,60\nRa 20,19,2048\nRa 8,18,4096\n" \
"Mvaa 240,112,44\nRa 24,19,4096\nMvaa 236,108,44\nRa 12,18,6144\nMvaa 236,140,60\nRa 28,19,6144\nMvaa 232,136,60\nD 28,68,s91,3072\n" \
"Mvaa 232,104,44\nMvaa 228,100,44\nbuffer_store_dwordx4 v[68:71], v0, s[60:63], s95 offen offset:1024 sc1\nMvaa 228,132,60\nv_add_u32 v14, s72, v14\nMvaa 224,128,60\nv_add_u32 v28, s72, v28\nMvaa 224,96,44\n" \
"s_waitcnt lgkmcnt(6)\nMvaa 128,64,0\nMvaa 128,160,16\ns_add_u32 m0, s88, 131072\nMvaa 132,164,16\nD 13,64,0,0\nMvaa 132,68,0\nMvaa 136,72,0\n" \
"Mvaa 136,168,16\nMvaa 140,172,16\nMvaa 140,76,0\ns_waitcnt lgkmcnt(0)\nD 13,64,s73,1024\nMvaa 144,80,0\nMvaa 144,176,16\nMvaa 148,180,16\n" \
"buffer_store_dwordx4 v[72:75], v0, s[60:63], s95 offen offset:2048 sc1\nMvaa 148,84,0\nMvaa 152,88,0\nD 13,64,s74,2048\nMvaa 152,184,16\nMvaa 156,188,16\nMvaa 156,92,0\nMvaa 188,92,4\n" \
"s_waitcnt vmcnt(19)\ns_barrier\nMvaa 188,188,20\nTa 96,20,0\nTa 98,20,1024\nMvaa 184,184,20\nD 13,64,s75,3072\nMvaa 184,88,4\n" \
"Ta 128,20,8192\nMvaa 180,84,4\nTa 130,20,9216\nMvaa 180,180,20\nRa 32,16,0\nRa 48,17,0\nMvaa 176,176,20\ns_add_u32 m0, s88, 135168\n" \
"Mvaa 176,80,4\nD 13,64,s76,0\nMvaa 172,76,4\nTa 100,21,0\nMvaa 172,172,20\nTa 102,21,1024\nTa 132,21,8192\nMvaa 168,168,20\n" \
"Ta 134,21,9216\nMvaa 168,72,4\nTa 104,22,0\nMvaa 164,68,4\nD 13,64,s77,1024\nMvaa 164,164,20\nTa 106,22,1024\nTa 136,22,8192\n" \
"Mvaa 160,160,20\nTa 138,22,9216\nMvaa 160,64,4\nTa 108,23,0\nMvaa 192,64,8\nTa 110,23,1024\nMvaa 192,160,24\nD 13,64,s78,2048\n" \
"Mvaa 196,164,24\nTa 140,23,8192\nTa 142,23,9216\nMvaa 196,68,8\nTa 112,24,0\nMvaa 200,72,8\nTa 114,24,1024\nMvaa 200,168,24\n" \
"Ta 144,24,8192\nMvaa 204,172,24\nD 13,64,s79,3072\nMvaa 204,76,8\nTa 146,24,9216\nTa 116,25,0\nMvaa 208,80,8\nTa 118,25,1024\n" \
"Mvaa 208,176,24\nv_add_u32 v13, 128, v13\nTa 148,25,8192\nMvaa 212,180,24\ns_add_u32 m0, s32, 81920\nMvaa 212,84,8\nD 14,68,0,0\nMvaa 216,88,8\n" \
"Ta 150,25,9216\nTa 120,26,0\nMvaa 216,184,24\nTa 122,26,1024\nMvaa 220,188,24\nTa 152,26,8192\nMvaa 220,92,8\nTa 154,26,9216\n" \
"Mvaa 252,92,12\nD 14,68,s89,1024\nMvaa 252,188,28\nTa 124,27,0\nTa 126,27,1024\nMvaa 248,184,28\nTa 156,27,8192\nMvaa 248,88,12\n" \
"Ta 158,27,9216\nMvaa 244,84,12\nRa 36,16,2048\nMvaa 244,180,28\nD 28,68,s90,2048\nMvaa 240,176,28\nRa 52,17,2048\nRa 40,16,4096\n" \
"Mvaa 240,80,12\nRa 56,17,4096\nMvaa 236,76,12\nRa 44,16,6144\nMvaa 236,172,28\nRa 60,17,6144\nMvaa 232,168,28\nD 28,68,s91,3072\n" \
"Mvaa 232,72,12\nMvaa 228,68,12\nbuffer_store_dwordx4 v[76:79], v0, s[60:63], s95 offen offset:3072 sc1\nMvaa 228,164,28\nv_add_u32 v14, s72, v14\nMvaa 224,160,28\nv_add_u32 v28, s72, v28\nMvaa 224,64,12\n" \
"s_waitcnt lgkmcnt(6)\nMvaa 128,96,32\nMvaa 128,128,48\nbuffer_load_dwordx2 v[10:11], v1, s[60:63], 0 offen offset:0 sc1\nMvaa 132,132,48\nv_subrev_u32 v9, s32, v0\nv_add_u32 v8, 0x1004200, v0\nMvaa 132,100,32\n" \
"s_add_u32 s77, s38, s32\nv_mov_b32 v12, 0xc2200000\nMvaa 136,104,32\nv_mov_b32 v13, 0xc2200000\nv_mov_b32 v14, 0xc2200000\nMvaa 136,136,48\nv_mov_b32 v15, 0xc2200000\nv_bfe_u32 v29, v0, 4, 4\n" \
"Mvaa 140,140,48\nv_lshrrev_b32 v28, 8, v0\ns_lshr_b32 s96, s32, 8\nMvaa 140,108,32\ns_waitcnt lgkmcnt(0)\nv_lshlrev_b32 v6, 2, v28\nMvaa 144,112,32\nv_add_u32 v30, s96, v29\n" \
"v_sub_u32 v30, v30, v6\nMvaa 144,144,48\ns_add_u32 s95, s34, 12288\nv_mov_b32 v6, 0xff800000\nMvaa 148,148,48\nbuffer_store_dwordx4 v[80:83], v0, s[60:63], s95 offen offset:0 sc1\nMvaa 148,116,32\nv_cmp_gt_i32 s[90:91], 0, v30\n" \
"v_cmp_gt_i32 s[92:93], 1, v30\nMvaa 152,120,32\nv_cmp_gt_i32 s[74:75], 2, v30\nv_cmp_gt_i32 s[78:79], 3, v30\nMvaa 152,152,48\nv_cndmask_b32 v112, v12, v6, s[90:91]\nv_cndmask_b32 v113, v12, v6, s[92:93]\nMvaa 156,156,48\n" \
"v_cndmask_b32 v114, v12, v6, s[74:75]\nv_cndmask_b32 v115, v12, v6, s[78:79]\nMvaa 156,124,32\nv_cmp_gt_i32 s[90:91], 16, v30\nv_cmp_gt_i32 s[92:93], 17, v30\nMvaa 188,124,36\ns_waitcnt vmcnt(17)\ns_barrier\n" \
"Mvaa 188,156,52\nTa 64,20,32768\nv_cmp_gt_i32 s[74:75], 18, v30\nMvaa 184,152,52\nTa 66,20,33792\nv_cmp_gt_i32 s[78:79], 19, v30\nMvaa 184,120,36\nTa 160,20,40960\n" \
"v_cndmask_b32 v116, v12, v6, s[90:91]\nMvaa 180,116,36\nTa 162,20,41984\nv_cndmask_b32 v117, v12, v6, s[92:93]\nMvaa 180,148,52\nRa 0,16,32768\nv_cndmask_b32 v118, v12, v6, s[74:75]\nMvaa 176,144,52\n" \
"Ra 16,17,32768\nv_cndmask_b32 v119, v12, v6, s[78:79]\nMvaa 176,112,36\nTa 68,21,32768\nv_cmp_gt_i32 s[90:91], 32, v30\nMvaa 172,108,36\nTa 70,21,33792\nv_cmp_gt_i32 s[92:93], 33, v30\n" \
"Mvaa 172,140,52\nTa 164,21,40960\nv_cmp_gt_i32 s[74:75], 34, v30\nMvaa 168,136,52\nTa 166,21,41984\nv_cmp_gt_i32 s[78:79], 35, v30\nMvaa 168,104,36\nTa 72,22,32768\n" \
"v_cndmask_b32 v120, v12, v6, s[90:91]\nMvaa 164,100,36\nTa 74,22,33792\nv_cndmask_b32 v121, v12, v6, s[92:93]\nMvaa 164,132,52\nTa 168,22,40960\nv_cndmask_b32 v122, v12, v6, s[74:75]\nMvaa 160,128,52\n" \
"Ta 170,22,41984\nv_cndmask_b32 v123, v12, v6, s[78:79]\nMvaa 160,96,36\nTa 76,23,32768\nv_cmp_gt_i32 s[90:91], 48, v30\nMvaa 192,96,40\nTa 78,23,33792\nv_cmp_gt_i32 s[92:93], 49, v30\n" \
"Mvaa 192,128,56\nTa 172,23,40960\nv_cmp_gt_i32 s[74:75], 50, v30\nMvaa 196,132,56\nTa 174,23,41984\nv_cmp_gt_i32 s[78:79], 51, v30\nMvaa 196,100,40\nTa 80,24,32768\n" \
"v_cndmask_b32 v124, v12, v6, s[90:91]\nMvaa 200,104,40\nTa 82,24,33792\nv_cndmask_b32 v125, v12, v6, s[92:93]\nMvaa 200,136,56\nv_cndmask_b32 v126, v12, v6, s[74:75]\nv_cndmask_b32 v127, v12, v6, s[78:79]\nMvaa 204,140,56\n" \
"Ta 176,24,40960\nMvaa 204,108,40\nTa 178,24,41984\nMvaa 208,112,40\nTa 84,25,32768\nMvaa 208,144,56\nTa 86,25,33792\nMvaa 212,148,56\n" \
"Ta 180,25,40960\nMvaa 212,116,40\nTa 182,25,41984\nMvaa 216,120,40\nTa 88,26,32768\nMvaa 216,152,56\nTa 90,26,33792\nMvaa 220,156,56\n" \
"Ta 184,26,40960\nMvaa 220,124,40\nTa 186,26,41984\nMvaa 252,124,44\nTa 92,27,32768\nMvaa 252,156,60\nTa 94,27,33792\nMvaa 248,152,60\n" \
"Ta 188,27,40960\nMvaa 248,120,44\nTa 190,27,41984\nMvaa 244,116,44\nRa 4,16,34816\nMvaa 244,148,60\nRa 20,17,34816\nMvaa 240,144,60\n" \
"Ra 8,16,36864\nMvaa 240,112,44\nRa 24,17,36864\nMvaa 236,108,44\nRa 12,16,38912\nMvaa 236,140,60\nRa 28,17,38912\nMvaa 232,136,60\n" \
"buffer_store_dwordx4 v[84:87], v0, s[60:63], s95 offen offset:1024 sc1\nMvaa 232,104,44\nMvaa 228,100,44\nMvaa 228,132,60\nMvaa 224,128,60\nMvaa 224,96,44\ns_waitcnt lgkmcnt(6)\nMvaa 128,64,0\n" \
"v_and_b32 v30, 3, v29\nv_bfe_u32 v6, v29, 1, 1\nMvaa 128,160,16\nv_lshrrev_b32 v28, 1, v28\nv_cmp_eq_u32 s[90:91], 2, v30\nMvaa 132,164,16\nv_cmp_eq_u32 s[92:93], v6, v28\nv_and_b32 v30, 1, v29\n" \
"Mvaa 132,68,0\nv_mov_b32 v6, 0x3f803f80\nv_cmp_eq_u32 s[78:79], 1, v30\nMvaa 136,72,0\nv_mov_b32 v7, 0x4444\nv_mov_b32 v30, 0xeeee\nMvaa 136,168,16\nv_cndmask_b32 v108, 0, v6, s[90:91]\n" \
"v_cndmask_b32 v109, 0, v6, s[90:91]\nMvaa 140,172,16\nv_cndmask_b32 v110, 0, v6, s[90:91]\nv_cndmask_b32 v111, 0, v6, s[90:91]\nMvaa 140,76,0\ns_waitcnt lgkmcnt(0)\nv_cndmask_b32 v104, 0, v6, s[92:93]\nMvaa 144,80,0\n" \
"v_cndmask_b32 v105, 0, v6, s[92:93]\nv_cndmask_b32 v106, 0, v6, s[92:93]\nMvaa 144,176,16\nv_cndmask_b32 v107, 0, v6, s[92:93]\nv_cndmask_b32 v7, v7, v30, s[78:79]\nMvaa 148,180,16\nbuffer_store_dwordx4 v[88:91], v0, s[60:63], s95 offen offset:2048 sc1\nMvaa 148,84,0\n" \
"v_mov_b32 v96, 0\nv_mov_b32 v97, 0\nMvaa 152,88,0\nv_mov_b32 v98, 0\nv_mov_b32 v99, 0\nMvaa 152,184,16\nv_mov_b32 v100, 0\nv_mov_b32 v101, 0\n" \
"Mvaa 156,188,16\nv_mov_b32 v102, 0\nv_mov_b32 v103, 0\nMvaa 156,92,0\nv_mov_b32 v5, v0\ns_mov_b32 s73, s32\nMvaa 188,92,4\ns_waitcnt vmcnt(3)\n" \
"s_barrier\nv_cmp_ne_u64 vcc, v[10:11], v[2:3]\ns_cbranch_vccnz zs101\nzr101:\nMvaa 188,188,20\ns_mov_b32 m0, s32\nTa 96,20,16384\nMvaa 184,184,20\n" \
"DS 0,60,s77,0\nMvaa 184,88,4\nTa 98,20,17408\nTa 128,20,24576\nMvaa 180,84,4\nTa 130,20,25600\ns_add_u32 s74, s32, 0x4000\nMvaa 180,180,20\n" \
"Ra 32,18,0\ns_add_u32 s75, s32, 0x8000\nMvaa 176,176,20\nDS 0,60,s77,1024\nMvaa 176,80,4\nRa 48,19,0\nTa 100,21,16384\nMvaa 172,76,4\n" \
"Ta 102,21,17408\ns_mov_b32 s97, 0x3e0293ee\nMvaa 172,172,20\nTa 132,21,24576\ns_mov_b32 s89, 31744\nMvaa 168,168,20\nDS 0,60,s77,2048\nMvaa 168,72,4\n" \
"Ta 134,21,25600\ns_branch 99f\n.p2align 12\nzp7:\ns_setpc_b64 s[98:99]\n99:\nTa 104,22,16384\nMvaa 164,68,4\n" \
"Ta 106,22,17408\ns_mov_b32 s90, 6144\nMvaa 164,164,20\nTa 136,22,24576\nTa 138,22,25600\nMvaa 160,160,20\nDS 0,60,s77,3072\nMvaa 160,64,4\n" \
"Ta 108,23,16384\ns_mov_b32 s91, 37888\nMvaa 192,64,8\nTa 110,23,17408\ns_mov_b32 s92, 0x200000\nMvaa 192,160,24\ns_add_u32 m0, s32, 16384\ns_add_u32 s77, s77, 0x4000\n" \
"Mvaa 196,164,24\nDS 0,60,s77,0\nMvaa 196,68,8\nTa 140,23,24576\nTa 142,23,25600\nMvaa 200,72,8\nTa 112,24,16384\ns_mov_b32 s78, s11\n" \
"Mvaa 200,168,24\nTa 114,24,17408\ns_add_u32 s84, s32, 98304\nMvaa 204,172,24\nDS 0,60,s77,1024\nMvaa 204,76,8\nTa 144,24,24576\nTa 146,24,25600\n" \
"Mvaa 208,80,8\nTa 116,25,16384\ns_mov_b32 s85, 8192\nMvaa 208,176,24\nTa 118,25,17408\nTa 148,25,24576\nMvaa 212,180,24\nDS 0,60,s77,2048\n" \
"Mvaa 212,84,8\nTa 150,25,25600\ns_mov_b32 s86, 16384\nMvaa 216,88,8\nTa 120,26,16384\ns_mov_b32 s87, 24576\nMvaa 216,184,24\nTa 122,26,17408\n" \
"Ta 152,26,24576\nMvaa 220,188,24\nDS 0,60,s77,3072\nMvaa 220,92,8\nTa 154,26,25600\ns_xor_b32 s82, s11, s10\nMvaa 252,92,12\nTa 124,27,16384\n" \
"Ta 126,27,17408\nMvaa 252,188,28\ns_add_u32 m0, s32, 49152\ns_lshl_b32 s83, s82, 16\nMvaa 248,184,28\nDS 8,60,s77,0\nMvaa 248,88,12\nTa 156,27,24576\n" \
"Ta 158,27,25600\nMvaa 244,84,12\nRa 36,18,2048\ns_add_u32 s83, s83, s18\nMvaa 244,180,28\nRa 52,19,2048\nMvaa 240,176,28\nDS 8,60,s77,1024\n" \
"Mvaa 240,80,12\nRa 40,18,4096\nRa 56,19,4096\nMvaa 236,76,12\nRa 44,18,6144\nMvaa 236,172,28\nRa 60,19,6144\nMvaa 232,168,28\n" \
"DS 8,60,s77,2048\nMvaa 232,72,12\nMvaa 228,68,12\nbuffer_store_dwordx4 v[92:95], v0, s[60:63], s95 offen offset:3072 sc1\nMvaa 228,164,28\nMvaa 224,160,28\nDS 8,60,s77,3072\nMvaa 224,64,12\n" \
"s_waitcnt lgkmcnt(6)\nMvaa 128,96,32\ns_add_u32 m0, s32, 32768\ns_add_u32 s77, s77, 0x4000\nMvaa 128,128,48\nDS 0,60,s77,0\nMvaa 132,132,48\nv_lshrrev_b32 v17, 8, v0\n" \
"v_and_b32 v16, 0xf0, v0\nMvaa 132,100,32\nDS 0,60,s77,1024\nMvaa 136,104,32\nv_lshlrev_b32 v18, 5, v17\nv_xor_b32 v16, v16, v18\nMvaa 136,136,48\nDS 0,60,s77,2048\n" \
"Mvaa 140,140,48\nv_lshl_add_u32 v16, v17, 11, v16\nv_add_u32 v16, s20, v16\nMvaa 140,108,32\ns_waitcnt lgkmcnt(0)\nDS 0,60,s77,3072\nMvaa 144,112,32\ns_add_u32 m0, s32, 65536\n" \
"s_xor_b32 s82, s78, s10\nMvaa 144,144,48\nDS 8,60,s77,0\nMvaa 148,148,48\ns_and_b32 s80, s82, 31\ns_lshl_b32 s80, s80, 18\nMvaa 148,116,32\nDS 8,60,s77,1024\n" \
"Mvaa 152,120,32\ns_cmp_lt_u32 s78, 32\ns_cselect_b64 s[68:69], s[14:15], s[12:13]\nMvaa 152,152,48\nDS 8,60,s77,2048\nMvaa 156,156,48\ns_cselect_b32 s70, 0x800000, s92\nv_add_u32 v18, s80, v16\n" \
"Mvaa 156,124,32\nDS 8,60,s77,3072\nv_lshlrev_b32 v29, 2, v0\nMvaa 160,96,36\ns_add_u32 s77, s77, 0x4000\nv_xor_b32 v19, 128, v18\nMvaa 160,128,52\nMvaa 164,132,52\n" \
"Mvaa 164,100,36\nbuffer_load_dword v30, v29, s[64:67], s46 offen sc1\nMvaa 168,104,36\nMvaa 168,136,52\nv_mul_f32 v128, s97, v128\nv_mul_f32 v129, s97, v129\nv_mul_f32 v130, s97, v130\nMvaa 172,140,52\n" \
"v_mul_f32 v131, s97, v131\nv_mul_f32 v132, s97, v132\nv_mul_f32 v133, s97, v133\nMvaa 172,108,36\nv_mul_f32 v134, s97, v134\nv_mul_f32 v135, s97, v135\nC 32,128,129\nMvaa 176,112,36\n" \
"C 33,130,131\nv_mul_f32 v136, s97, v136\nv_mul_f32 v137, s97, v137\nMvaa 176,144,52\nv_mul_f32 v138, s97, v138\nv_mul_f32 v139, s97, v139\nC 34,132,133\nMvaa 180,148,52\n" \
"C 35,134,135\nv_mul_f32 v140, s97, v140\nv_mul_f32 v141, s97, v141\nMvaa 180,116,36\nv_mul_f32 v142, s97, v142\nv_mul_f32 v143, s97, v143\nC 36,136,137\nMvaa 184,120,36\n" \
"C 37,138,139\nv_mul_f32 v144, s97, v144\nv_mul_f32 v145, s97, v145\nMvaa 184,152,52\nv_mul_f32 v146, s97, v146\nv_mul_f32 v147, s97, v147\nC 38,140,141\nMvaa 188,156,52\n" \
"C 39,142,143\nv_mul_f32 v148, s97, v148\nv_mul_f32 v149, s97, v149\nMvaa 188,124,36\nv_mul_f32 v150, s97, v150\nv_mul_f32 v151, s97, v151\nC 40,144,145\nMvaa 192,96,40\n" \
"C 41,146,147\nv_mul_f32 v152, s97, v152\nv_mul_f32 v153, s97, v153\nMvaa 192,128,56\nv_mul_f32 v154, s97, v154\nv_mul_f32 v155, s97, v155\nC 42,148,149\nMvaa 196,132,56\n" \
"C 43,150,151\nv_mul_f32 v156, s97, v156\nv_mul_f32 v157, s97, v157\nMvaa 196,100,40\nv_mul_f32 v158, s97, v158\nv_mul_f32 v159, s97, v159\nC 44,152,153\nMvaa 200,104,40\n" \
"C 45,154,155\nC 46,156,157\nC 47,158,159\nMvaa 200,136,56\nv_mul_f32 v160, s97, v160\nv_mul_f32 v161, s97, v161\nv_mul_f32 v162, s97, v162\nMvaa 204,140,56\n" \
"v_mul_f32 v163, s97, v163\nv_mul_f32 v164, s97, v164\nv_mul_f32 v165, s97, v165\nMvaa 204,108,40\nv_mul_f32 v166, s97, v166\nv_mul_f32 v167, s97, v167\nC 48,160,161\nMvaa 208,112,40\n" \
"s_waitcnt vmcnt(18)\ns_barrier\nC 49,162,163\nMvaa 208,144,56\nRa 160,5,0\nv_mul_f32 v168, s97, v168\nv_mul_f32 v169, s97, v169\nMvaa 212,148,56\n" \
"v_mul_f32 v170, s97, v170\nv_mul_f32 v171, s97, v171\nC 50,164,165\nMvaa 212,116,40\nRa 164,5,1024\nC 51,166,167\nv_mul_f32 v172, s97, v172\nMvaa 216,120,40\n" \
"v_mul_f32 v173, s97, v173\nv_mul_f32 v174, s97, v174\nv_mul_f32 v175, s97, v175\nMvaa 216,152,56\nRa 168,5,2048\nC 52,168,169\nC 53,170,171\nMvaa 220,156,56\n" \
"v_mul_f32 v176, s97, v176\nv_mul_f32 v177, s97, v177\nv_mul_f32 v178, s97, v178\nMvaa 220,124,40\nRa 172,5,3072\nv_mul_f32 v179, s97, v179\nC 54,172,173\nMvaa 224,96,44\n" \
"C 55,174,175\nv_mul_f32 v180, s97, v180\nv_mul_f32 v181, s97, v181\nMvaa 224,128,60\nRa 176,5,4096\nv_mul_f32 v182, s97, v182\nv_mul_f32 v183, s97, v183\nMvaa 228,132,60\n" \
"C 56,176,177\nC 57,178,179\nv_mul_f32 v184, s97, v184\nMvaa 228,100,44\nRa 180,5,5120\nv_mul_f32 v185, s97, v185\nv_mul_f32 v186, s97, v186\nMvaa 232,104,44\n" \
"v_mul_f32 v187, s97, v187\nC 58,180,181\nC 59,182,183\nMvaa 232,136,60\nRa 184,5,6144\nv_mul_f32 v188, s97, v188\nv_mul_f32 v189, s97, v189\nMvaa 236,140,60\n" \
"v_mul_f32 v190, s97, v190\nv_mul_f32 v191, s97, v191\nC 60,184,185\nMvaa 236,108,44\nRa 188,5,7168\nC 61,186,187\nC 62,188,189\nMvaa 240,112,44\n" \
"C 63,190,191\nv_mul_f32 v192, s97, v192\nv_mul_f32 v193, s97, v193\nMvaa 240,144,60\nv_mul_f32 v194, s97, v194\nv_mul_f32 v195, s97, v195\nv_mul_f32 v196, s97, v196\nMvaa 244,148,60\n" \
"v_mul_f32 v197, s97, v197\nv_mul_f32 v198, s97, v198\nv_mul_f32 v199, s97, v199\nMvaa 244,116,44\nC 64,192,193\nC 65,194,195\nv_mul_f32 v200, s97, v200\nMvaa 248,120,44\n" \
"s_bitcmp1_b32 s33, 2\nv_mul_f32 v201, s97, v201\nv_mul_f32 v202, s97, v202\nMvaa 248,152,60\nv_mul_f32 v203, s97, v203\nC 66,196,197\nC 67,198,199\nMvaa 252,156,60\n" \
"Mvaa 252,124,44\ns_waitcnt lgkmcnt(0)\nQvav 128,160,32,12\nRa 128,5,8192\nv_mul_f32 v204, s97, v204\nv_mul_f32 v205, s97, v205\nMvav 128,164,36\nv_mul_f32 v206, s97, v206\n" \
"v_mul_f32 v207, s97, v207\nC 68,200,201\nMvav 128,168,40\nRa 132,5,9216\nC 69,202,203\nv_mul_f32 v208, s97, v208\nMvav 128,172,44\nv_mul_f32 v209, s97, v209\n" \
"v_mul_f32 v210, s97, v210\nv_mul_f32 v211, s97, v211\nQvav 132,176,32,12\nRa 136,5,10240\nC 70,204,205\nC 71,206,207\nMvav 132,180,36\nv_mul_f32 v212, s97, v212\n" \
"v_mul_f32 v213, s97, v213\nv_mul_f32 v214, s97, v214\nMvav 132,184,40\nRa 140,5,11264\nv_mul_f32 v215, s97, v215\nC 72,208,209\nMvav 132,188,44\nC 73,210,211\n" \
"v_mul_f32 v216, s97, v216\nv_mul_f32 v217, s97, v217\nQvav 136,160,48,12\nRa 144,5,12288\nv_mul_f32 v218, s97, v218\nv_mul_f32 v219, s97, v219\nMvav 136,164,52\nC 74,212,213\n" \
"C 75,214,215\nv_mul_f32 v220, s97, v220\nMvav 136,168,56\nRa 148,5,13312\nv_mul_f32 v221, s97, v221\nv_mul_f32 v222, s97, v222\nMvav 136,172,60\nv_mul_f32 v223, s97, v223\n" \
"C 76,216,217\nC 77,218,219\nQvav 140,176,48,12\nRa 152,5,14336\nC 78,220,221\nC 79,222,223\nMvav 140,180,52\nv_mul_f32 v224, s97, v224\n" \
"v_mul_f32 v225, s97, v225\nv_mul_f32 v226, s97, v226\nMvav 140,184,56\nRa 156,5,15360\nv_mul_f32 v227, s97, v227\nv_mul_f32 v228, s97, v228\nMvav 140,188,60\nv_mul_f32 v229, s97, v229\n" \
"v_mul_f32 v230, s97, v230\nv_mul_f32 v231, s97, v231\nQvav 144,160,64,12\nC 80,224,225\nC 81,226,227\nv_mul_f32 v232, s97, v232\nMvav 144,164,68\nv_mul_f32 v233, s97, v233\n" \
"v_mul_f32 v234, s97, v234\nv_mul_f32 v235, s97, v235\nMvav 144,168,72\nC 82,228,229\nC 83,230,231\nv_mul_f32 v236, s97, v236\nMvav 144,172,76\nv_mul_f32 v237, s97, v237\n" \
"v_mul_f32 v238, s97, v238\nv_mul_f32 v239, s97, v239\nQvav 148,176,64,12\nC 84,232,233\nC 85,234,235\nv_mul_f32 v240, s97, v240\nMvav 148,180,68\nv_mul_f32 v241, s97, v241\n" \
"v_mul_f32 v242, s97, v242\nv_mul_f32 v243, s97, v243\nC 86,236,237\nC 87,238,239\nv_mul_f32 v244, s97, v244\nv_mul_f32 v245, s97, v245\nv_mul_f32 v246, s97, v246\nv_mul_f32 v247, s97, v247\n" \
"C 88,240,241\nC 89,242,243\nv_mul_f32 v248, s97, v248\nv_mul_f32 v249, s97, v249\nv_mul_f32 v250, s97, v250\nv_mul_f32 v251, s97, v251\nC 90,244,245\nC 91,246,247\n" \
"v_mul_f32 v252, s97, v252\nv_mul_f32 v253, s97, v253\nv_mul_f32 v254, s97, v254\nv_mul_f32 v255, s97, v255\nC 92,248,249\nC 93,250,251\nC 94,252,253\nC 95,254,255\n" \
"Mvav 148,184,72\nE 240,128\nMvav 148,188,76\nE 241,129\nQvav 152,160,80,12\nC 192,240,241\nE 242,130\nMvav 152,164,84\n" \
"E 243,131\nMvav 152,168,88\nC 193,242,243\nMvav 152,172,92\nE 244,132\nQvav 156,176,80,12\nE 245,133\nMvav 156,180,84\n" \
"C 194,244,245\nE 246,134\nMvav 156,184,88\nE 247,135\nMvav 156,188,92\ns_waitcnt lgkmcnt(0)\nC 195,246,247\nQvav 160,128,32,12\n" \
"E 240,136\nMvav 160,132,36\nE 241,137\nMvav 160,136,40\nC 196,240,241\nE 242,138\nMvav 160,140,44\nQvav 168,128,48,12\n" \
"v_mov_b32 v6, v5\nv_add_u32 v5, s74, v9\nE 243,139\nMvav 168,132,52\nC 197,242,243\nE 244,140\nMvav 168,136,56\nE 245,141\n" \
"Mvav 168,140,60\nC 198,244,245\nE 246,142\nQvav 176,128,64,12\ns_waitcnt vmcnt(9)\ns_barrier\nMvav 176,132,68\nRa 160,5,0\n" \
"E 247,143\nMvav 176,136,72\ns_mov_b32 m0, s73\nC 199,246,247\nE 240,144\nMvav 176,140,76\nRa 164,5,1024\nDS 0,60,s77,0\n" \
"E 241,145\nQvav 184,128,80,12\nC 200,240,241\nMvav 184,132,84\nRa 168,5,2048\nE 242,146\nMvav 184,136,88\nE 243,147\n" \
"Mvav 184,140,92\nRa 172,5,3072\nC 201,242,243\nE 244,148\nQvav 164,144,32,12\nDS 0,60,s77,1024\nE 245,149\nMvav 164,148,36\n" \
"Ra 176,5,4096\nC 202,244,245\nMvav 164,152,40\nE 246,150\nMvav 164,156,44\nRa 180,5,5120\nE 247,151\nQvav 172,144,48,12\n" \
"C 203,246,247\nE 240,152\nMvav 172,148,52\nRa 184,5,6144\nDS 0,60,s77,2048\nMvav 172,152,56\nE 241,153\nMvav 172,156,60\n" \
"Ra 188,5,7168\nC 204,240,241\nE 242,154\nQvav 180,144,64,12\nE 243,155\nMvav 180,148,68\nC 205,242,243\nE 244,156\n" \
"Mvav 180,152,72\nDS 0,60,s77,3072\nMvav 180,156,76\nE 245,157\nQvav 188,144,80,12\nC 206,244,245\nE 246,158\nMvav 188,148,84\n" \
"E 247,159\nMvav 188,152,88\nC 207,246,247\ns_mov_b32 s96, s39\nMvav 188,156,92\ns_waitcnt lgkmcnt(0)\nQvav 128,160,32,12\nE 240,160\n" \
"Mvav 128,164,36\nE 241,161\nMvav 128,168,40\nC 208,240,241\nRa 128,6,49152\nMvav 128,172,44\nE 242,162\ns_add_u32 m0, s75, 0xc000\n" \
"Qvav 136,160,48,12\nDS 8,60,s77,0\nMvav 136,164,52\nE 243,163\nMvav 136,168,56\nC 209,242,243\nRa 132,6,50176\nMvav 136,172,60\n" \
"E 244,164\nQvav 144,160,64,12\nDS 8,60,s77,1024\nMvav 144,164,68\nE 245,165\nMvav 144,168,72\nC 210,244,245\nRa 136,6,51200\n" \
"Mvav 144,172,76\nE 246,166\ns_add_u32 s95, s34, 16384\nQvav 152,160,80,12\nbuffer_store_dwordx4 a[192:195], v0, s[60:63], s95 offen offset:0 sc1\nMvav 152,164,84\nE 247,167\nMvav 152,168,88\n" \
"C 211,246,247\nRa 140,6,52224\nMvav 152,172,92\nE 240,168\nQvav 132,176,32,12\nDS 8,60,s77,2048\nMvav 132,180,36\nE 241,169\n" \
"Mvav 132,184,40\nC 212,240,241\nRa 144,6,53248\nMvav 132,188,44\nE 242,170\nQvav 140,176,48,12\nbuffer_store_dwordx4 a[196:199], v0, s[60:63], s95 offen offset:1024 sc1\nMvav 140,180,52\n" \
"E 243,171\nMvav 140,184,56\nC 213,242,243\nRa 148,6,54272\nMvav 140,188,60\nE 244,172\nQvav 148,176,64,12\nDS 8,60,s77,3072\n" \
"Mvav 148,180,68\nE 245,173\nMvav 148,184,72\nC 214,244,245\nRa 152,6,55296\nMvav 148,188,76\nE 246,174\nQvav 156,176,80,12\n" \
"Ra 156,6,56320\ns_mov_b32 s76, s73\nMvav 156,180,84\nE 247,175\nMvav 156,184,88\nC 215,246,247\ns_mov_b32 s73, s74\nMvav 156,188,92\n" \
"E 240,176\nv_smfmac_f32_16x16x64_bf16 v[96:99], v[104:107], v[192:199], v7\ns_mov_b32 s74, s75\nv_smfmac_f32_16x16x64_bf16 v[100:103], v[104:107], v[200:207], v7\ns_waitcnt lgkmcnt(0)\nZaav 96,128,204\nbuffer_store_dwordx4 a[200:203], v0, s[60:63], s95 offen offset:2048 sc1\nZaav 100,132,204\n" \
"E 241,177\nZaav 68,132,200\nC 216,240,241\nRa 160,5,8192\nZaav 64,128,200\nE 242,178\nZaav 32,128,196\ns_branch 99f\n" \
".p2align 12\nzp8:\ns_setpc_b64 s[98:99]\n99:\nRa 164,5,9216\ns_mov_b32 s75, s76\nZaav 36,132,196\nE 243,179\n" \
"Zaav 4,132,192\nC 217,242,243\ns_add_u32 s77, s77, 0x4000\nZaav 0,128,192\nE 244,180\nZaav 8,136,192\nRa 168,5,10240\nZaav 12,140,192\n" \
"E 245,181\nZaav 44,140,196\nC 218,244,245\nRa 172,5,11264\nZaav 40,136,196\nE 246,182\nZaav 72,136,200\nRa 176,5,12288\n" \
"Zaav 76,140,200\nE 247,183\nZaav 108,140,204\nC 219,246,247\nZaav 104,136,204\nE 240,184\nZaav 112,144,204\nbuffer_store_dwordx4 a[204:207], v0, s[60:63], s95 offen offset:3072 sc1\n" \
"Zaav 116,148,204\nE 241,185\nZaav 84,148,200\nC 220,240,241\nRa 180,5,13312\nZaav 80,144,200\nE 242,186\nZaav 48,144,196\n" \
"Ra 184,5,14336\nZaav 52,148,196\nE 243,187\nZaav 20,148,192\nC 221,242,243\nZaav 16,144,192\nE 244,188\nZaav 24,152,192\n" \
"Ra 188,5,15360\nZaav 28,156,192\nE 245,189\nZaav 60,156,196\nC 222,244,245\nZaav 56,152,196\nE 246,190\nZaav 88,152,200\n" \
"Zaav 92,156,200\nE 247,191\nZaav 124,156,204\nC 223,246,247\ns_waitcnt lgkmcnt(0)\nZaav 120,152,204\nQvav 160,160,32,12\nE 240,128\n" \
"Mvav 160,164,36\nE 241,129\nMvav 160,168,40\nC 192,240,241\nRa 128,6,57344\nMvav 160,172,44\nE 242,130\ns_add_u32 s95, s34, 20480\n" \
"Qvav 168,160,48,12\nbuffer_store_dwordx4 a[208:211], v0, s[60:63], s95 offen offset:0 sc1\nMvav 168,164,52\nE 243,131\nMvav 168,168,56\nC 193,242,243\nMvav 168,172,60\nE 244,132\n" \
"Qvav 176,160,64,12\nRa 132,6,58368\nMvav 176,164,68\nE 245,133\nMvav 176,168,72\nC 194,244,245\nRa 136,6,59392\nMvav 176,172,76\n" \
"E 246,134\nQvav 184,160,80,12\nRa 140,6,60416\nMvav 184,164,84\nE 247,135\nMvav 184,168,88\nC 195,246,247\nMvav 184,172,92\n" \
"E 240,136\nQvav 164,176,32,12\nRa 144,6,61440\nMvav 164,180,36\nE 241,137\nMvav 164,184,40\nC 196,240,241\nRa 148,6,62464\n" \
"Mvav 164,188,44\nE 242,138\nQvav 172,176,48,12\nbuffer_store_dwordx4 a[212:215], v0, s[60:63], s95 offen offset:1024 sc1\nMvav 172,180,52\nE 243,139\nMvav 172,184,56\nC 197,242,243\n" \
"Mvav 172,188,60\nE 244,140\nQvav 180,176,64,12\nRa 152,6,63488\nMvav 180,180,68\nE 245,141\nMvav 180,184,72\nC 198,244,245\n" \
"Ra 156,6,64512\nMvav 180,188,76\nE 246,142\nQvav 188,176,80,12\nv_mov_b32 v6, v5\nv_add_u32 v5, s74, v9\nMvav 188,180,84\nE 247,143\n" \
"Mvav 188,184,88\nC 199,246,247\nMvav 188,188,92\nE 240,144\nv_smfmac_f32_16x16x64_bf16 v[96:99], v[104:107], v[208:215], v7\nv_smfmac_f32_16x16x64_bf16 v[100:103], v[104:107], v[216:223], v7\ns_waitcnt vmcnt(15) lgkmcnt(0)\ns_barrier\n" \
"Maav 96,128,220\nbuffer_store_dwordx4 a[216:219], v0, s[60:63], s95 offen offset:2048 sc1\nMaav 100,132,220\nE 241,145\nMaav 68,132,216\nC 200,240,241\nRa 160,5,0\nMaav 64,128,216\n" \
"E 242,146\ns_mov_b32 m0, s73\nMaav 32,128,212\nDS 0,60,s77,0\nMaav 36,132,212\nE 243,147\nMaav 4,132,208\nC 201,242,243\n" \
"Ra 164,5,1024\nMaav 0,128,208\nE 244,148\nMaav 8,136,208\nDS 0,60,s77,1024\nMaav 12,140,208\nE 245,149\nMaav 44,140,212\n" \
"C 202,244,245\nRa 168,5,2048\nMaav 40,136,212\nE 246,150\nMaav 72,136,216\nRa 172,5,3072\nRa 176,5,4096\nMaav 76,140,216\n" \
"E 247,151\nMaav 108,140,220\nC 203,246,247\nRa 180,5,5120\nMaav 104,136,220\nE 240,152\nMaav 112,144,220\nDS 0,60,s77,2048\n" \
"Maav 116,148,220\nE 241,153\nMaav 84,148,216\nC 204,240,241\nRa 184,5,6144\nMaav 80,144,216\nE 242,154\nMaav 48,144,212\n" \
"buffer_store_dwordx4 a[220:223], v0, s[60:63], s95 offen offset:3072 sc1\nMaav 52,148,212\nE 243,155\nMaav 20,148,208\nC 205,242,243\nRa 188,5,7168\nMaav 16,144,208\nE 244,156\n" \
"Maav 24,152,208\nDS 0,60,s77,3072\nMaav 28,156,208\nE 245,157\nMaav 60,156,212\nC 206,244,245\nMaav 56,152,212\nE 246,158\n" \
"Maav 88,152,216\nMaav 92,156,216\nE 247,159\nMaav 124,156,220\nC 207,246,247\ns_waitcnt lgkmcnt(0)\nMaav 120,152,220\nQvav 128,160,32,12\n" \
"E 240,160\nMvav 128,164,36\nE 241,161\nMvav 128,168,40\nC 208,240,241\nRa 128,6,49152\nMvav 128,172,44\nE 242,162\n" \
"s_add_u32 m0, s75, 0xc000\nQvav 136,160,48,12\nDS 8,60,s77,0\nMvav 136,164,52\nE 243,163\nMvav 136,168,56\nC 209,242,243\nRa 132,6,50176\n" \
"Mvav 136,172,60\nE 244,164\nQvav 144,160,64,12\nDS 8,60,s77,1024\nMvav 144,164,68\nE 245,165\nMvav 144,168,72\nC 210,244,245\n" \
"Ra 136,6,51200\nMvav 144,172,76\nE 246,166\ns_add_u32 s95, s34, 24576\nQvav 152,160,80,12\nbuffer_store_dwordx4 a[224:227], v0, s[60:63], s95 offen offset:0 sc1\nMvav 152,164,84\nE 247,167\n" \
"Mvav 152,168,88\nC 211,246,247\nRa 140,6,52224\nMvav 152,172,92\nE 240,168\nQvav 132,176,32,12\nDS 8,60,s77,2048\nMvav 132,180,36\n" \
"E 241,169\nMvav 132,184,40\nC 212,240,241\nRa 144,6,53248\nMvav 132,188,44\nE 242,170\nQvav 140,176,48,12\nbuffer_store_dwordx4 a[228:231], v0, s[60:63], s95 offen offset:1024 sc1\n" \
"Mvav 140,180,52\nE 243,171\nMvav 140,184,56\nC 213,242,243\nRa 148,6,54272\nMvav 140,188,60\nE 244,172\nQvav 148,176,64,12\n" \
"DS 8,60,s77,3072\nMvav 148,180,68\nE 245,173\nMvav 148,184,72\nC 214,244,245\nRa 152,6,55296\nMvav 148,188,76\nE 246,174\n" \
"Qvav 156,176,80,12\nRa 156,6,56320\ns_mov_b32 s76, s73\nMvav 156,180,84\nE 247,175\nMvav 156,184,88\nC 215,246,247\ns_mov_b32 s73, s74\n" \
"Mvav 156,188,92\nE 240,176\nv_smfmac_f32_16x16x64_bf16 v[96:99], v[104:107], v[192:199], v7\ns_mov_b32 s74, s75\nv_smfmac_f32_16x16x64_bf16 v[100:103], v[104:107], v[200:207], v7\ns_waitcnt lgkmcnt(0)\nMaav 96,128,204\nbuffer_store_dwordx4 a[232:235], v0, s[60:63], s95 offen offset:2048 sc1\n" \
"Maav 100,132,204\nE 241,177\nMaav 68,132,200\nC 216,240,241\nRa 160,5,8192\nMaav 64,128,200\nE 242,178\nMaav 32,128,196\n" \
"Ra 164,5,9216\ns_mov_b32 s75, s76\nMaav 36,132,196\nE 243,179\nMaav 4,132,192\nC 217,242,243\ns_add_u32 s77, s77, 0x4000\nMaav 0,128,192\n" \
"E 244,180\nMaav 8,136,192\nRa 168,5,10240\nMaav 12,140,192\nE 245,181\nMaav 44,140,196\nC 218,244,245\nRa 172,5,11264\n" \
"Maav 40,136,196\nE 246,182\nMaav 72,136,200\nRa 176,5,12288\nMaav 76,140,200\nE 247,183\nMaav 108,140,204\nC 219,246,247\n" \
"Maav 104,136,204\nE 240,184\nMaav 112,144,204\nbuffer_store_dwordx4 a[236:239], v0, s[60:63], s95 offen offset:3072 sc1\nMaav 116,148,204\nE 241,185\nMaav 84,148,200\nC 220,240,241\n" \
"Ra 180,5,13312\nMaav 80,144,200\nE 242,186\nMaav 48,144,196\nRa 184,5,14336\nMaav 52,148,196\nE 243,187\nMaav 20,148,192\n" \
"C 221,242,243\nMaav 16,144,192\nE 244,188\nMaav 24,152,192\nRa 188,5,15360\nMaav 28,156,192\nE 245,189\nMaav 60,156,196\n" \
"C 222,244,245\nMaav 56,152,196\nE 246,190\nMaav 88,152,200\nMaav 92,156,200\nE 247,191\nMaav 124,156,204\nC 223,246,247\n" \
"s_waitcnt lgkmcnt(0)\nMaav 120,152,204\nQvav 160,160,32,12\nE 240,128\nMvav 160,164,36\nE 241,129\nMvav 160,168,40\nC 192,240,241\n" \
"Ra 128,6,57344\nMvav 160,172,44\nE 242,130\ns_add_u32 s95, s34, 28672\nQvav 168,160,48,12\nbuffer_store_dwordx4 a[240:243], v0, s[60:63], s95 offen offset:0 sc1\nMvav 168,164,52\nE 243,131\n" \
"Mvav 168,168,56\nC 193,242,243\nMvav 168,172,60\nE 244,132\nQvav 176,160,64,12\nRa 132,6,58368\nMvav 176,164,68\nE 245,133\n" \
"Mvav 176,168,72\nC 194,244,245\nRa 136,6,59392\nMvav 176,172,76\nE 246,134\nQvav 184,160,80,12\nRa 140,6,60416\nMvav 184,164,84\n" \
"E 247,135\nMvav 184,168,88\nC 195,246,247\nMvav 184,172,92\nE 240,136\nQvav 164,176,32,12\nRa 144,6,61440\nMvav 164,180,36\n" \
"E 241,137\nMvav 164,184,40\nC 196,240,241\nRa 148,6,62464\nMvav 164,188,44\nE 242,138\nQvav 172,176,48,12\nbuffer_store_dwordx4 a[244:247], v0, s[60:63], s95 offen offset:1024 sc1\n" \
"Mvav 172,180,52\nE 243,139\nMvav 172,184,56\nC 197,242,243\nMvav 172,188,60\nE 244,140\nQvav 180,176,64,12\nRa 152,6,63488\n" \
"Mvav 180,180,68\nE 245,141\nMvav 180,184,72\nC 198,244,245\nRa 156,6,64512\nMvav 180,188,76\nE 246,142\nQvav 188,176,80,12\n" \
"v_mov_b32 v6, v5\nv_add_u32 v5, s74, v9\nMvav 188,180,84\nE 247,143\nMvav 188,184,88\nC 199,246,247\nMvav 188,188,92\nE 240,144\n" \
"v_smfmac_f32_16x16x64_bf16 v[96:99], v[104:107], v[208:215], v7\nv_smfmac_f32_16x16x64_bf16 v[100:103], v[104:107], v[216:223], v7\ns_waitcnt vmcnt(20) lgkmcnt(0)\ns_barrier\nMaav 96,128,220\nbuffer_store_dwordx4 a[248:251], v0, s[60:63], s95 offen offset:2048 sc1\nMaav 100,132,220\nE 241,145\n" \
"Maav 68,132,216\nC 200,240,241\nRa 160,5,0\nMaav 64,128,216\nE 242,146\ns_mov_b32 m0, s73\nMaav 32,128,212\nDS 0,60,s77,0\n" \
"Maav 36,132,212\nE 243,147\nMaav 4,132,208\nC 201,242,243\nRa 164,5,1024\nMaav 0,128,208\nE 244,148\nMaav 8,136,208\n" \
"DS 0,60,s77,1024\nMaav 12,140,208\nE 245,149\nMaav 44,140,212\nC 202,244,245\nRa 168,5,2048\nMaav 40,136,212\nE 246,150\n" \
"Maav 72,136,216\nRa 172,5,3072\nRa 176,5,4096\nMaav 76,140,216\nE 247,151\nMaav 108,140,220\nC 203,246,247\nRa 180,5,5120\n" \
"Maav 104,136,220\nE 240,152\nMaav 112,144,220\nDS 0,60,s77,2048\nMaav 116,148,220\nE 241,153\nMaav 84,148,216\nC 204,240,241\n" \
"Ra 184,5,6144\nMaav 80,144,216\nE 242,154\nMaav 48,144,212\nbuffer_store_dwordx4 a[252:255], v0, s[60:63], s95 offen offset:3072 sc1\nMaav 52,148,212\nE 243,155\nMaav 20,148,208\n" \
"C 205,242,243\nRa 188,5,7168\nMaav 16,144,208\nE 244,156\nMaav 24,152,208\nDS 0,60,s77,3072\nMaav 28,156,208\nE 245,157\n" \
"Maav 60,156,212\nC 206,244,245\nMaav 56,152,212\nE 246,158\nMaav 88,152,216\nMaav 92,156,216\nE 247,159\nMaav 124,156,220\n" \
"C 207,246,247\ns_waitcnt lgkmcnt(0)\nMaav 120,152,220\nQvav 128,160,32,12\nE 240,160\nMvav 128,164,36\nE 241,161\nMvav 128,168,40\n" \
"C 208,240,241\nRa 128,6,49152\nMvav 128,172,44\nE 242,162\ns_add_u32 m0, s75, 0xc000\nQvav 136,160,48,12\nDS 8,60,s77,0\nMvav 136,164,52\n" \
"E 243,163\nMvav 136,168,56\nC 209,242,243\nRa 132,6,50176\nMvav 136,172,60\nE 244,164\nQvav 144,160,64,12\nDS 8,60,s77,1024\n" \
"Mvav 144,164,68\nE 245,165\nMvav 144,168,72\nC 210,244,245\nMvav 144,172,76\nE 246,166\nQvav 152,160,80,12\nRa 136,6,51200\n" \
"Ra 140,6,52224\nMvav 152,164,84\nE 247,167\nMvav 152,168,88\nC 211,246,247\nMvav 152,172,92\nE 240,168\nQvav 132,176,32,12\n" \
"DS 8,60,s77,2048\nMvav 132,180,36\nE 241,169\nMvav 132,184,40\nC 212,240,241\nRa 144,6,53248\nMvav 132,188,44\nE 242,170\n" \
"Qvav 140,176,48,12\nRa 148,6,54272\nMvav 140,180,52\nE 243,171\nMvav 140,184,56\nC 213,242,243\nRa 152,6,55296\nMvav 140,188,60\n" \
"E 244,172\nQvav 148,176,64,12\nDS 8,60,s77,3072\nMvav 148,180,68\nE 245,173\nMvav 148,184,72\nC 214,244,245\ns_mov_b32 s76, s73\n" \
"Mvav 148,188,76\nE 246,174\nQvav 156,176,80,12\nRa 156,6,56320\ns_mov_b32 s73, s74\nMvav 156,180,84\nE 247,175\nMvav 156,184,88\n" \
"C 215,246,247\ns_mov_b32 s74, s75\nMvav 156,188,92\nE 240,176\nv_smfmac_f32_16x16x64_bf16 v[96:99], v[104:107], v[192:199], v7\ns_mov_b32 s75, s76\nv_smfmac_f32_16x16x64_bf16 v[100:103], v[104:107], v[200:207], v7\ns_waitcnt lgkmcnt(0)\n" \
"Maav 96,128,204\nRa 160,5,8192\ns_add_u32 s77, s77, 0x4000\nMaav 100,132,204\nE 241,177\nMaav 68,132,200\nC 216,240,241\nRa 164,5,9216\n" \
"Maav 64,128,200\nE 242,178\nMaav 32,128,196\nMaav 36,132,196\nE 243,179\nMaav 4,132,192\nC 217,242,243\nRa 168,5,10240\n" \
"s_branch 99f\n.p2align 12\nzp9:\ns_setpc_b64 s[98:99]\n99:\nMaav 0,128,192\nE 244,180\nMaav 8,136,192\n" \
"Ra 172,5,11264\nMaav 12,140,192\nE 245,181\nMaav 44,140,196\nC 218,244,245\nMaav 40,136,196\nE 246,182\nMaav 72,136,200\n" \
"Ra 176,5,12288\nMaav 76,140,200\nE 247,183\nMaav 108,140,204\nC 219,246,247\nMaav 104,136,204\nE 240,184\nMaav 112,144,204\n" \
"Ra 180,5,13312\nMaav 116,148,204\nE 241,185\nMaav 84,148,200\nC 220,240,241\nMaav 80,144,200\nE 242,186\nMaav 48,144,196\n" \
"Ra 184,5,14336\nMaav 52,148,196\nE 243,187\nMaav 20,148,192\nC 221,242,243\nRa 188,5,15360\nMaav 16,144,192\nE 244,188\n" \
"Maav 24,152,192\nMaav 28,156,192\nE 245,189\nMaav 60,156,196\nC 222,244,245\nMaav 56,152,196\nE 246,190\nMaav 88,152,200\n" \
"Maav 92,156,200\nE 247,191\nMaav 124,156,204\nC 223,246,247\ns_waitcnt lgkmcnt(0)\nMaav 120,152,204\nQvav 160,160,32,12\nE 240,128\n" \
"Mvav 160,164,36\nE 241,129\nMvav 160,168,40\nC 192,240,241\nRa 128,6,57344\nMvav 160,172,44\nE 242,130\nQvav 168,160,48,12\n" \
"Ra 132,6,58368\nMvav 168,164,52\nE 243,131\nMvav 168,168,56\nC 193,242,243\nMvav 168,172,60\nE 244,132\nQvav 176,160,64,12\n" \
"Ra 136,6,59392\nMvav 176,164,68\nE 245,133\nMvav 176,168,72\nC 194,244,245\nMvav 176,172,76\nE 246,134\nQvav 184,160,80,12\n" \
"Ra 140,6,60416\nMvav 184,164,84\nE 247,135\nMvav 184,168,88\nC 195,246,247\nMvav 184,172,92\nE 240,136\nQvav 164,176,32,12\n" \
"Ra 144,6,61440\nMvav 164,180,36\nE 241,137\nMvav 164,184,40\nC 196,240,241\nMvav 164,188,44\nE 242,138\nQvav 172,176,48,12\n" \
"Ra 148,6,62464\nMvav 172,180,52\nE 243,139\nMvav 172,184,56\nC 197,242,243\nRa 152,6,63488\nMvav 172,188,60\nE 244,140\n" \
"Qvav 180,176,64,12\nMvav 180,180,68\nE 245,141\nMvav 180,184,72\nC 198,244,245\nRa 156,6,64512\nMvav 180,188,76\nE 246,142\n" \
"Qvav 188,176,80,12\nv_mov_b32 v6, v5\nv_add_u32 v5, s74, v9\nMvav 188,180,84\nE 247,143\nMvav 188,184,88\nC 199,246,247\nMvav 188,188,92\n" \
"E 240,144\nv_smfmac_f32_16x16x64_bf16 v[96:99], v[104:107], v[208:215], v7\nv_smfmac_f32_16x16x64_bf16 v[100:103], v[104:107], v[216:223], v7\ns_waitcnt vmcnt(5) lgkmcnt(0)\ns_barrier\nMaav 96,128,220\nbuffer_store_dwordx2 v[2:3], off, s[60:63], s36 sc1\nMaav 100,132,220\n" \
"E 241,145\nMaav 68,132,216\nC 200,240,241\nRa 160,5,0\nMaav 64,128,216\nE 242,146\ns_mov_b32 m0, s73\nMaav 32,128,212\n" \
"DS 0,60,s77,0\nMaav 36,132,212\nE 243,147\nMaav 4,132,208\nC 201,242,243\nRa 164,5,1024\nMaav 0,128,208\nE 244,148\n" \
"Maav 8,136,208\nDS 0,60,s77,1024\nMaav 12,140,208\nE 245,149\nMaav 44,140,212\nC 202,244,245\nRa 168,5,2048\nMaav 40,136,212\n" \
"E 246,150\nMaav 72,136,216\nRa 172,5,3072\nMaav 76,140,216\nE 247,151\nMaav 108,140,220\nC 203,246,247\nRa 176,5,4096\n" \
"Maav 104,136,220\nE 240,152\nMaav 112,144,220\nDS 0,60,s77,2048\nMaav 116,148,220\nE 241,153\nMaav 84,148,216\nC 204,240,241\n" \
"Ra 180,5,5120\nMaav 80,144,216\nE 242,154\nMaav 48,144,212\nRa 184,5,6144\nRa 188,5,7168\nMaav 52,148,212\nE 243,155\n" \
"Maav 20,148,208\nC 205,242,243\nMaav 16,144,208\nE 244,156\nMaav 24,152,208\nDS 0,60,s77,3072\nMaav 28,156,208\nE 245,157\n" \
"Maav 60,156,212\nC 206,244,245\nMaav 56,152,212\nE 246,158\nMaav 88,152,216\nMaav 92,156,216\nE 247,159\nMaav 124,156,220\n" \
"C 207,246,247\ns_waitcnt lgkmcnt(0)\nMaav 120,152,220\ns_branch 99f\n.p2align 12\nzgt:\nzp10:\ns_setpc_b64 s[98:99]\n" \
"99:\ns_nop 0\ns_nop 0\ns_nop 0\nzO:\nQvav 128,160,32,12\nE 240,160\nMvav 128,164,36\n" \
"E 241,161\nMvav 128,168,40\nC 208,240,241\nRa 128,6,49152\nMvav 128,172,44\nE 242,162\ns_add_u32 m0, s75, 0xc000\nQvav 136,160,48,12\n" \
"DS 8,60,s77,0\nMvav 136,164,52\nE 243,163\nMvav 136,168,56\nC 209,242,243\nRa 132,6,50176\nMvav 136,172,60\nE 244,164\n" \
"Qvav 144,160,64,12\nDS 8,60,s77,1024\nMvav 144,164,68\nE 245,165\nMvav 144,168,72\nC 210,244,245\nRa 136,6,51200\nMvav 144,172,76\n" \
"E 246,166\nQvav 152,160,80,12\nDS 8,60,s77,2048\nMvav 152,164,84\nE 247,167\nMvav 152,168,88\nC 211,246,247\nRa 140,6,52224\n" \
"Mvav 152,172,92\nE 240,168\nQvav 132,176,32,12\nDS 8,60,s77,3072\nMvav 132,180,36\nE 241,169\nMvav 132,184,40\nC 212,240,241\n" \
"Ra 144,6,53248\nMvav 132,188,44\nE 242,170\nQvav 140,176,48,12\nbuffer_load_dwordx2 v[10:11], v1, s[60:63], 0 offen offset:64 sc1\nMvav 140,180,52\nE 243,171\nMvav 140,184,56\n" \
"C 213,242,243\nRa 148,6,54272\nMvav 140,188,60\nE 244,172\nQvav 148,176,64,12\nbuffer_load_dword v30, v4, s[56:59], 0 offen sc1\nMvav 148,180,68\nE 245,173\n" \
"Mvav 148,184,72\nC 214,244,245\nRa 152,6,55296\nMvav 148,188,76\nE 246,174\nQvav 156,176,80,12\nRa 156,6,56320\ns_mov_b32 s76, s73\n" \
"Mvav 156,180,84\nE 247,175\nMvav 156,184,88\nC 215,246,247\ns_mov_b32 s73, s74\nMvav 156,188,92\nE 240,176\nv_smfmac_f32_16x16x64_bf16 v[96:99], v[104:107], v[192:199], v7\n" \
"s_mov_b32 s74, s75\nv_smfmac_f32_16x16x64_bf16 v[100:103], v[104:107], v[200:207], v7\ns_waitcnt lgkmcnt(0)\nMaav 96,128,204\nRa 160,5,8192\ns_mov_b32 s75, s76\nMaav 100,132,204\nE 241,177\n" \
"Maav 68,132,200\nC 216,240,241\nRa 164,5,9216\nMaav 64,128,200\nE 242,178\nMaav 32,128,196\ns_add_u32 s77, s77, 0x4000\nv_add_u32 v4, s45, v4\n" \
"Maav 36,132,196\nE 243,179\nMaav 4,132,192\nC 217,242,243\nRa 168,5,10240\nMaav 0,128,192\nE 244,180\nMaav 8,136,192\n" \
"Ra 172,5,11264\nMaav 12,140,192\nE 245,181\nMaav 44,140,196\nC 218,244,245\nMaav 40,136,196\nE 246,182\nMaav 72,136,200\n" \
"Ra 176,5,12288\nMaav 76,140,200\nE 247,183\nMaav 108,140,204\nC 219,246,247\nMaav 104,136,204\nE 240,184\nMaav 112,144,204\n" \
"Ra 180,5,13312\nMaav 116,148,204\nE 241,185\nMaav 84,148,200\nC 220,240,241\nMaav 80,144,200\nE 242,186\nMaav 48,144,196\n" \
"Ra 184,5,14336\nMaav 52,148,196\nE 243,187\nMaav 20,148,192\nC 221,242,243\nRa 188,5,15360\nMaav 16,144,192\nE 244,188\n" \
"Maav 24,152,192\nMaav 28,156,192\nE 245,189\nMaav 60,156,196\nC 222,244,245\nMaav 56,152,196\nE 246,190\nMaav 88,152,200\n" \
"Maav 92,156,200\nE 247,191\nMaav 124,156,204\nC 223,246,247\ns_waitcnt lgkmcnt(0)\nMaav 120,152,204\nzE:\nQvav 160,160,32,12\n" \
"E 240,128\nMvav 160,164,36\nE 241,129\nMvav 160,168,40\nC 192,240,241\nRa 128,6,57344\nMvav 160,172,44\nE 242,130\n" \
"Qvav 168,160,48,12\nRa 132,6,58368\nMvav 168,164,52\nE 243,131\nMvav 168,168,56\nC 193,242,243\nMvav 168,172,60\nE 244,132\n" \
"Qvav 176,160,64,12\nRa 136,6,59392\nMvav 176,164,68\nE 245,133\nMvav 176,168,72\nC 194,244,245\nMvav 176,172,76\nE 246,134\n" \
"Qvav 184,160,80,12\nRa 140,6,60416\nMvav 184,164,84\nE 247,135\nMvav 184,168,88\nC 195,246,247\nMvav 184,172,92\nE 240,136\n" \
"Qvav 164,176,32,12\nRa 144,6,61440\nMvav 164,180,36\nE 241,137\nMvav 164,184,40\nC 196,240,241\nMvav 164,188,44\nE 242,138\n" \
"Qvav 172,176,48,12\nRa 148,6,62464\nMvav 172,180,52\nE 243,139\nMvav 172,184,56\nC 197,242,243\nRa 152,6,63488\nMvav 172,188,60\n" \
"E 244,140\nQvav 180,176,64,12\nMvav 180,180,68\nE 245,141\nMvav 180,184,72\nC 198,244,245\nRa 156,6,64512\nMvav 180,188,76\n" \
"E 246,142\nQvav 188,176,80,12\nv_mov_b32 v6, v5\nv_add_u32 v5, s74, v9\nMvav 188,180,84\nE 247,143\nMvav 188,184,88\nC 199,246,247\n" \
"Mvav 188,188,92\nE 240,144\nv_smfmac_f32_16x16x64_bf16 v[96:99], v[104:107], v[208:215], v7\nv_smfmac_f32_16x16x64_bf16 v[100:103], v[104:107], v[216:223], v7\ns_waitcnt vmcnt(9) lgkmcnt(0)\ns_barrier\nMaav 96,128,220\nRa 160,5,0\n" \
"Ra 164,5,1024\nMaav 100,132,220\nE 241,145\nMaav 68,132,216\nC 200,240,241\nMaav 64,128,216\nE 242,146\ns_mov_b32 m0, s73\n" \
"Maav 32,128,212\nDS 0,60,s77,0\nMaav 36,132,212\nE 243,147\nMaav 4,132,208\nC 201,242,243\nRa 168,5,2048\nMaav 0,128,208\n" \
"E 244,148\nMaav 8,136,208\nDS 0,60,s77,1024\nMaav 12,140,208\nE 245,149\nMaav 44,140,212\nC 202,244,245\nRa 172,5,3072\n" \
"Maav 40,136,212\nE 246,150\nMaav 72,136,216\nRa 176,5,4096\nMaav 76,140,216\nE 247,151\nMaav 108,140,220\nC 203,246,247\n" \
"Ra 180,5,5120\nMaav 104,136,220\nE 240,152\nMaav 112,144,220\nDS 0,60,s77,2048\nMaav 116,148,220\nE 241,153\nMaav 84,148,216\n" \
"C 204,240,241\nMaav 80,144,216\nE 242,154\nMaav 48,144,212\nDS 0,60,s77,3072\nMaav 52,148,212\nE 243,155\nMaav 20,148,208\n" \
"C 205,242,243\nRa 184,5,6144\nMaav 16,144,208\nE 244,156\nMaav 24,152,208\nRa 188,5,7168\nMaav 28,156,208\nE 245,157\n" \
"Maav 60,156,212\nC 206,244,245\ns_sub_u32 s96, s96, 1\nMaav 56,152,212\nE 246,158\nMaav 88,152,216\ns_cmp_lg_u32 s96, 0\nMaav 92,156,216\n" \
"E 247,159\nMaav 124,156,220\nC 207,246,247\ns_waitcnt lgkmcnt(0)\nMaav 120,152,220\ns_cbranch_scc1 zO\ns_cmp_lg_u32 s40, 0\ns_cbranch_scc1 zL\n" \
"Qvav 128,160,32,112\nE 240,160\nMvav 128,164,36\nE 241,161\nMvav 128,168,40\nC 208,240,241\nRa 128,6,49152\nMvav 128,172,44\n" \
"E 242,162\ns_add_u32 m0, s75, 0xc000\nQvav 136,160,48,12\nDS 8,60,s77,0\nMvav 136,164,52\nE 243,163\nMvav 136,168,56\nC 209,242,243\n" \
"Ra 132,6,50176\nMvav 136,172,60\nE 244,164\nQvav 144,160,64,12\nDS 8,60,s77,1024\nMvav 144,164,68\nE 245,165\nMvav 144,168,72\n" \
"C 210,244,245\nMvav 144,172,76\nE 246,166\nQvav 152,160,80,12\nRa 136,6,51200\nRa 140,6,52224\nMvav 152,164,84\nE 247,167\n" \
"Mvav 152,168,88\nC 211,246,247\nMvav 152,172,92\nE 240,168\nQvav 132,176,32,116\nDS 8,60,s77,2048\nMvav 132,180,36\nE 241,169\n" \
"Mvav 132,184,40\nC 212,240,241\nRa 144,6,53248\nMvav 132,188,44\nE 242,170\nQvav 140,176,48,12\nRa 148,6,54272\nMvav 140,180,52\n" \
"E 243,171\nMvav 140,184,56\nC 213,242,243\nRa 152,6,55296\nMvav 140,188,60\nE 244,172\nQvav 148,176,64,12\nDS 8,60,s77,3072\n" \
"Mvav 148,180,68\nE 245,173\nMvav 148,184,72\nC 214,244,245\ns_mov_b32 s76, s73\nMvav 148,188,76\nE 246,174\nQvav 156,176,80,12\n" \
"Ra 156,6,56320\ns_mov_b32 s73, s74\nMvav 156,180,84\nE 247,175\nMvav 156,184,88\nC 215,246,247\ns_mov_b32 s74, s75\nMvav 156,188,92\n" \
"E 240,176\nv_smfmac_f32_16x16x64_bf16 v[96:99], v[104:107], v[192:199], v7\ns_mov_b32 s75, s76\nv_smfmac_f32_16x16x64_bf16 v[100:103], v[104:107], v[200:207], v7\ns_waitcnt lgkmcnt(0)\nMaav 96,128,204\nRa 160,5,8192\ns_add_u32 s77, s77, 0x4000\n" \
"Maav 100,132,204\nE 241,177\nMaav 68,132,200\nC 216,240,241\nRa 164,5,9216\nMaav 64,128,200\nE 242,178\nMaav 32,128,196\n" \
"Maav 36,132,196\nE 243,179\nMaav 4,132,192\nC 217,242,243\nRa 168,5,10240\nMaav 0,128,192\nE 244,180\nMaav 8,136,192\n" \
"Ra 172,5,11264\nMaav 12,140,192\nE 245,181\nMaav 44,140,196\nC 218,244,245\nMaav 40,136,196\nE 246,182\nMaav 72,136,200\n" \
"Ra 176,5,12288\nMaav 76,140,200\nE 247,183\nMaav 108,140,204\nC 219,246,247\nMaav 104,136,204\nE 240,184\nMaav 112,144,204\n" \
"Ra 180,5,13312\nMaav 116,148,204\nE 241,185\nMaav 84,148,200\nC 220,240,241\nMaav 80,144,200\nE 242,186\nMaav 48,144,196\n" \
"Ra 184,5,14336\nMaav 52,148,196\nE 243,187\nMaav 20,148,192\nC 221,242,243\nRa 188,5,15360\nMaav 16,144,192\nE 244,188\n" \
"Maav 24,152,192\nMaav 28,156,192\nE 245,189\nMaav 60,156,196\nC 222,244,245\nMaav 56,152,196\nE 246,190\nMaav 88,152,200\n" \
"Maav 92,156,200\nE 247,191\nMaav 124,156,204\nC 223,246,247\ns_waitcnt lgkmcnt(0)\nMaav 120,152,204\nQvav 160,160,32,120\nE 240,128\n" \
"Mvav 160,164,36\nE 241,129\nMvav 160,168,40\nC 192,240,241\nRa 128,6,57344\nMvav 160,172,44\nE 242,130\nQvav 168,160,48,12\n" \
"Ra 132,6,58368\nMvav 168,164,52\nE 243,131\nMvav 168,168,56\nC 193,242,243\nMvav 168,172,60\nE 244,132\nQvav 176,160,64,12\n" \
"Ra 136,6,59392\nMvav 176,164,68\nE 245,133\nMvav 176,168,72\nC 194,244,245\nMvav 176,172,76\nE 246,134\nQvav 184,160,80,12\n" \
"Ra 140,6,60416\nMvav 184,164,84\nE 247,135\nMvav 184,168,88\nC 195,246,247\nMvav 184,172,92\nE 240,136\nQvav 164,176,32,124\n" \
"Ra 144,6,61440\nMvav 164,180,36\nE 241,137\nMvav 164,184,40\nC 196,240,241\nMvav 164,188,44\nE 242,138\nQvav 172,176,48,12\n" \
"Ra 148,6,62464\nMvav 172,180,52\nE 243,139\nMvav 172,184,56\nC 197,242,243\nRa 152,6,63488\nMvav 172,188,60\nE 244,140\n" \
"Qvav 180,176,64,12\nMvav 180,180,68\nE 245,141\nMvav 180,184,72\nC 198,244,245\nRa 156,6,64512\nMvav 180,188,76\nE 246,142\n" \
"Qvav 188,176,80,12\nv_mov_b32 v6, v5\nv_add_u32 v5, s74, v9\nMvav 188,180,84\nE 247,143\nMvav 188,184,88\nC 199,246,247\nMvav 188,188,92\n" \
"E 240,144\nv_smfmac_f32_16x16x64_bf16 v[96:99], v[104:107], v[208:215], v7\nv_smfmac_f32_16x16x64_bf16 v[100:103], v[104:107], v[216:223], v7\ns_waitcnt vmcnt(8) lgkmcnt(0)\ns_barrier\nMaav 96,128,220\nRa 160,5,0\nRa 164,5,1024\n" \
"Maav 100,132,220\nE 241,145\nMaav 68,132,216\nC 200,240,241\nMaav 64,128,216\nE 242,146\ns_mov_b32 m0, s73\nMaav 32,128,212\n" \
"DS 0,60,s77,0\nMaav 36,132,212\nE 243,147\nMaav 4,132,208\nC 201,242,243\nRa 168,5,2048\nMaav 0,128,208\nE 244,148\n" \
"Maav 8,136,208\nDS 0,60,s77,1024\nMaav 12,140,208\nE 245,149\nMaav 44,140,212\nC 202,244,245\nRa 172,5,3072\nMaav 40,136,212\n" \
"E 246,150\nMaav 72,136,216\nRa 176,5,4096\nMaav 76,140,216\nE 247,151\nMaav 108,140,220\nC 203,246,247\nRa 180,5,5120\n" \
"Maav 104,136,220\nE 240,152\nMaav 112,144,220\nDS 0,60,s77,2048\nMaav 116,148,220\nE 241,153\nMaav 84,148,216\nC 204,240,241\n" \
"Maav 80,144,216\nE 242,154\nMaav 48,144,212\nRa 184,5,6144\nRa 188,5,7168\nMaav 52,148,212\nE 243,155\nMaav 20,148,208\n" \
"C 205,242,243\nMaav 16,144,208\nE 244,156\nMaav 24,152,208\nDS 0,60,s77,3072\nMaav 28,156,208\nE 245,157\nMaav 60,156,212\n" \
"C 206,244,245\nMaav 56,152,212\nE 246,158\nMaav 88,152,216\nMaav 92,156,216\nE 247,159\nMaav 124,156,220\nC 207,246,247\n" \
"s_waitcnt lgkmcnt(0)\nMaav 120,152,220\nQvav 136,160,48,112\nE 240,160\nRa 128,6,49152\nMvav 136,164,52\nE 241,161\nMvav 136,168,56\n" \
"C 208,240,241\nRa 132,6,50176\nMvav 136,172,60\nE 242,162\ns_add_u32 m0, s75, 0xc000\nQvav 144,160,64,12\nDS 8,60,s77,0\nMvav 144,164,68\n" \
"E 243,163\nRa 136,6,51200\nMvav 144,168,72\nC 209,242,243\nE 244,164\ns_branch 99f\n.p2align 12\nzp11:\n" \
"s_setpc_b64 s[98:99]\n99:\nMvav 144,172,76\nDS 8,60,s77,1024\nQvav 152,160,80,12\nE 245,165\nMvav 152,164,84\nC 210,244,245\n" \
"Ra 140,6,52224\nMvav 152,168,88\nE 246,166\nRa 144,6,53248\nMvav 152,172,92\nDS 8,60,s77,2048\nQvav 140,176,48,116\nE 247,167\n" \
"Mvav 140,180,52\nC 211,246,247\nE 240,168\nMvav 140,184,56\nDS 8,60,s77,3072\nMvav 140,188,60\nE 241,169\nRa 148,6,54272\n" \
"Qvav 148,176,64,12\nC 212,240,241\nRa 152,6,55296\nMvav 148,180,68\nE 242,170\nMvav 148,184,72\nE 243,171\nMvav 148,188,76\n" \
"C 213,242,243\nRa 156,6,56320\nQvav 156,176,80,12\nE 244,172\nMvav 156,180,84\ns_mov_b32 s76, s73\ns_mov_b32 s73, s74\nMvav 156,184,88\n" \
"E 245,173\nMvav 156,188,92\nC 214,244,245\ns_mov_b32 s74, s75\nv_smfmac_f32_16x16x64_bf16 v[96:99], v[104:107], v[192:199], v7\ns_mov_b32 s75, s76\nv_smfmac_f32_16x16x64_bf16 v[100:103], v[104:107], v[200:207], v7\ns_waitcnt lgkmcnt(0)\n" \
"Maav 96,128,204\nE 246,174\nMaav 100,132,204\nE 247,175\nMaav 68,132,200\nC 215,246,247\nRa 160,5,8192\nMaav 64,128,200\n" \
"E 240,176\nMaav 32,128,196\nRa 164,5,9216\ns_add_u32 s77, s77, 0x4000\nMaav 36,132,196\nE 241,177\nMaav 4,132,192\nC 216,240,241\n" \
"E 242,178\nMaav 0,128,192\nRa 168,5,10240\nMaav 8,136,192\nE 243,179\nMaav 12,140,192\nC 217,242,243\nRa 172,5,11264\n" \
"Maav 44,140,196\nE 244,180\nMaav 40,136,196\nRa 176,5,12288\nMaav 72,136,200\nE 245,181\nMaav 76,140,200\nC 218,244,245\n" \
"E 246,182\nMaav 108,140,204\nRa 180,5,13312\nMaav 104,136,204\nE 247,183\nMaav 112,144,204\nC 219,246,247\nMaav 116,148,204\n" \
"E 240,184\nMaav 84,148,200\nE 241,185\nMaav 80,144,200\nC 220,240,241\nRa 184,5,14336\nMaav 48,144,196\nE 242,186\n" \
"Maav 52,148,196\nRa 188,5,15360\nMaav 20,148,192\nE 243,187\nMaav 16,144,192\nC 221,242,243\nMaav 24,152,192\nE 244,188\n" \
"Maav 28,156,192\nE 245,189\nMaav 60,156,196\nC 222,244,245\nMaav 56,152,196\nE 246,190\nMaav 88,152,200\nMaav 92,156,200\n" \
"E 247,191\nMaav 124,156,204\nC 223,246,247\ns_waitcnt lgkmcnt(0)\nMaav 120,152,204\nzD4:\nQvav 168,160,48,120\nE 240,136\n" \
"s_add_u32 m0, s32, 98304\nMvav 168,164,52\nD 18,68,0,0\nMvav 168,168,56\nE 241,137\nMvav 168,172,60\nC 196,240,241\nRa 128,6,57344\n" \
"Qvav 176,160,64,12\nE 242,138\nMvav 176,164,68\nRa 132,6,58368\nRa 136,6,59392\nMvav 176,168,72\nE 243,139\nMvav 176,172,76\n" \
"C 197,242,243\nRa 140,6,60416\nQvav 184,160,80,12\nD 18,68,s89,1024\nMvav 184,164,84\nE 244,140\nMvav 184,168,88\nRa 144,6,61440\n" \
"Mvav 184,172,92\nE 245,141\nQvav 172,176,48,124\nC 198,244,245\nRa 148,6,62464\nMvav 172,180,52\nE 246,142\nMvav 172,184,56\n" \
"D 19,68,s90,2048\nMvav 172,188,60\nRa 152,6,63488\nRa 156,6,64512\nQvav 180,176,64,12\nE 247,143\nMvav 180,180,68\nC 199,246,247\n" \
"v_mov_b32 v6, v5\nMvav 180,184,72\nE 240,144\nMvav 180,188,76\nD 19,68,s91,3072\nQvav 188,176,80,12\nE 241,145\nMvav 188,180,84\n" \
"C 200,240,241\nv_add_u32 v18, 131072, v18\nMvav 188,184,88\nE 242,146\nMvav 188,188,92\nv_add_u32 v19, 131072, v19\nv_add_u32 v5, s74, v9\nv_smfmac_f32_16x16x64_bf16 v[96:99], v[104:107], v[208:215], v7\n" \
"v_smfmac_f32_16x16x64_bf16 v[100:103], v[104:107], v[216:223], v7\ns_waitcnt vmcnt(12) lgkmcnt(0)\ns_barrier\nMaav 96,128,220\nRa 160,5,0\nMaav 100,132,220\nE 243,147\nMaav 68,132,216\n" \
"C 201,242,243\nRa 164,5,1024\nMaav 64,128,216\nE 244,148\ns_mov_b32 m0, s73\nMaav 32,128,212\nDS 0,60,s77,0\ns_getpc_b64 s[100:101]\n" \
"7:\ns_add_u32 s100, s100, s2\ns_addc_u32 s101, s101, 0\nMaav 36,132,212\nE 245,149\ns_add_u32 s100, s100, zq0-7b\ns_addc_u32 s101, s101, 0\nMaav 4,132,208\n" \
"C 202,244,245\ns_swappc_b64 s[98:99], s[100:101]\nMaav 0,128,208\nRa 168,5,2048\nMaav 8,136,208\nE 246,150\nMaav 12,140,208\nDS 0,60,s77,1024\n" \
"Maav 44,140,212\nE 247,151\nMaav 40,136,212\nC 203,246,247\nRa 172,5,3072\nMaav 72,136,216\nE 240,152\nMaav 76,140,216\n" \
"Ra 176,5,4096\nMaav 108,140,220\nE 241,153\nMaav 104,136,220\nC 204,240,241\nMaav 112,144,220\nDS 0,60,s77,2048\nMaav 116,148,220\n" \
"E 242,154\nMaav 84,148,216\nRa 180,5,5120\nMaav 80,144,216\nE 243,155\nMaav 48,144,212\nC 205,242,243\nRa 184,5,6144\n" \
"Maav 52,148,212\nE 244,156\nMaav 20,148,208\nDS 0,60,s77,3072\nMaav 16,144,208\nRa 188,5,7168\nMaav 24,152,208\nE 245,157\n" \
"Maav 28,156,208\nC 206,244,245\nMaav 60,156,212\nE 246,158\nMaav 56,152,212\ns_mov_b32 s96, s41\ns_bitcmp1_b32 s33, 3\nMaav 88,152,216\n" \
"E 247,159\nMaav 92,156,216\nC 207,246,247\nMaav 124,156,220\ns_waitcnt lgkmcnt(0)\nMaav 120,152,220\ns_cbranch_scc1 zDa\nQvav 144,160,64,12\n" \
"E 240,168\nRa 128,6,49152\nMvav 144,164,68\nE 241,169\nMvav 144,168,72\nC 212,240,241\nRa 132,6,50176\nRa 136,6,51200\n" \
"Mvav 144,172,76\nE 242,170\ns_add_u32 m0, s75, 0xc000\nQvav 152,160,80,12\nDS 8,60,s77,0\nMvav 152,164,84\nE 243,171\nRa 140,6,52224\n" \
"Mvav 152,168,88\nC 213,242,243\nE 244,172\nMvav 152,172,92\nDS 8,60,s77,1024\nRa 144,6,53248\nQvav 148,176,64,12\nE 245,173\n" \
"Mvav 148,180,68\nC 214,244,245\nRa 148,6,54272\nMvav 148,184,72\nE 246,174\nRa 152,6,55296\nMvav 148,188,76\nE 247,175\n" \
"Qvav 156,176,80,12\nC 215,246,247\nRa 156,6,56320\nMvav 156,180,84\nE 240,176\nMvav 156,184,88\nMvav 156,188,92\nE 241,177\n" \
"Mvvv 96,108,196\nC 216,240,241\nE 242,178\nv_smfmac_f32_16x16x64_bf16 v[100:103], v[104:107], v[200:207], v7\ns_waitcnt lgkmcnt(0)\nMaav 96,128,204\nDS 8,60,s77,2048\nRa 160,5,8192\n" \
"Maav 100,132,204\nE 243,179\nMaav 68,132,200\nC 217,242,243\nRa 164,5,9216\nMaav 64,128,200\nE 244,180\nMaav 32,128,196\n" \
"E 245,181\nMaav 36,132,196\nC 218,244,245\nRa 168,5,10240\nMaav 40,136,196\nE 246,182\nRa 172,5,11264\nMaav 44,140,196\n" \
"DS 8,60,s77,3072\nMaav 76,140,200\nE 247,183\ns_add_u32 m0, s32, 114688\nMaav 72,136,200\nC 219,246,247\nE 240,184\nD 18,68,0,0\n" \
"Maav 104,136,204\nRa 176,5,12288\nRa 180,5,13312\nMaav 108,140,204\nE 241,185\nD 18,68,s89,1024\nMaav 112,144,204\nC 220,240,241\n" \
"Ra 184,5,14336\nRa 188,5,15360\nMaav 116,148,204\nE 242,186\nD 19,68,s90,2048\nMaav 84,148,200\nE 243,187\nMaav 80,144,200\n" \
"C 221,242,243\nD 19,68,s91,3072\nMaav 48,144,196\nE 244,188\nMaav 52,148,196\ns_ff1_i32_b64 s78, s[8:9]\ns_bitset0_b64 s[8:9], s78\nMaav 56,152,196\n" \
"E 245,189\nMaav 60,156,196\nC 222,244,245\nE 246,190\nMaav 92,156,200\ns_xor_b32 s82, s78, s10\ns_and_b32 s80, s82, 31\nMaav 88,152,200\n" \
"E 247,191\ns_lshl_b32 s80, s80, 18\ns_cmp_lt_u32 s78, 32\ns_cselect_b64 s[68:69], s[14:15], s[12:13]\ns_cselect_b32 s70, 0x800000, s92\nv_add_u32 v18, s80, v16\nv_xor_b32 v19, 128, v18\nMaav 120,152,204\n" \
"C 223,246,247\ns_waitcnt lgkmcnt(0)\nMaav 124,156,204\ns_mov_b32 s76, s73\ns_mov_b32 s73, s74\ns_mov_b32 s74, s75\ns_mov_b32 s75, s76\ns_add_u32 s77, s77, 0x4000\n" \
"Qvav 176,160,64,12\nE 240,144\ns_add_u32 m0, s32, 131072\nRa 128,6,57344\nMvav 176,164,68\nD 18,68,0,0\nMvav 176,168,72\nE 241,145\n" \
"Mvav 176,172,76\nC 200,240,241\nRa 132,6,58368\nRa 136,6,59392\nQvav 184,160,80,12\nD 18,68,s89,1024\nMvav 184,164,84\nE 242,146\n" \
"Mvav 184,168,88\nRa 140,6,60416\nRa 144,6,61440\nRa 148,6,62464\nMvav 184,172,92\nE 243,147\nQvav 180,176,64,12\nC 201,242,243\n" \
"Ra 152,6,63488\nMvav 180,180,68\nD 19,68,s90,2048\nRa 156,6,64512\nMvav 180,184,72\nE 244,148\nMvav 180,188,76\nD 19,68,s91,3072\n" \
"Qvav 188,176,80,12\nE 245,149\nMvav 188,180,84\nC 202,244,245\nv_add_u32 v18, 131072, v18\nMvav 188,184,88\nv_add_u32 v19, 131072, v19\nv_mov_b32 v6, v5\n" \
"Mvav 188,188,92\nE 246,150\nMvvv 96,108,212\nv_add_u32 v5, s74, v9\nv_smfmac_f32_16x16x64_bf16 v[100:103], v[104:107], v[216:223], v7\ns_waitcnt vmcnt(20) lgkmcnt(0)\ns_barrier\nMaav 96,128,220\n" \
"E 247,151\nMaav 100,132,220\nC 203,246,247\ns_mov_b32 m0, s73\nMaav 68,132,216\nDS 0,60,s77,0\nMaav 64,128,216\nE 240,152\n" \
"Maav 32,128,212\nRa 160,5,0\nRa 164,5,1024\nMaav 36,132,212\nE 241,153\nMaav 40,136,212\nC 204,240,241\nRa 168,5,2048\n" \
"Maav 44,140,212\nDS 0,60,s77,1024\nv_add_f32 v196, v96, v97\nMaav 76,140,216\nE 242,154\nv_add_f32 v197, v98, v99\nMaav 72,136,216\nRa 172,5,3072\n" \
"Ra 176,5,4096\nv_subrev_u32 v198, 0xf800000, v196\nMaav 104,136,220\nE 243,155\nv_subrev_u32 v199, 0xf800000, v197\nMaav 108,140,220\nC 205,242,243\nRa 180,5,5120\n" \
"v_max_u32 v198, v198, v199\nMaav 112,144,220\nDS 0,60,s77,2048\nv_cmp_lt_u32 vcc, 0x3c000000, v198\nMaav 116,148,220\nE 244,156\nMaav 84,148,216\nRa 184,5,6144\n" \
"Ra 188,5,7168\nMaav 80,144,216\nE 245,157\nMaav 48,144,212\nC 206,244,245\nMaav 52,148,212\nDS 0,60,s77,3072\nMaav 56,152,212\n" \
"E 246,158\nMaav 60,156,212\nMaav 92,156,216\nE 247,159\nMaav 88,152,216\nC 207,246,247\nMaav 120,152,220\ns_waitcnt lgkmcnt(0)\n" \
"Maav 124,156,220\ns_cbranch_vccnz zHs\nQvav 144,160,64,12\nE 240,176\nRa 128,6,49152\nMvav 144,164,68\nE 241,177\nMvav 144,168,72\n" \
"C 216,240,241\nRa 132,6,50176\nv_rcp_f32 v196, v196\nv_rcp_f32 v197, v197\nMvav 144,172,76\nE 242,178\ns_add_u32 m0, s75, 0xc000\ns_add_u32 s95, s83, 0\n" \
"AR 128,0\nQvav 152,160,80,12\nDS 8,60,s77,0\nRa 136,6,51200\nAR 129,1\nMvav 152,164,84\nE 243,179\nAR 130,2\n" \
"AR 131,3\nMvav 152,168,88\nC 217,242,243\nRa 140,6,52224\nAR 132,4\nAR 133,5\nMvav 152,172,92\nE 244,180\n" \
"AR 134,6\nQvav 148,176,64,12\nDS 8,60,s77,1024\nRa 144,6,53248\nAR 135,7\nv_mul_f32 v128, v128, v196\nMvav 148,180,68\nE 245,181\n" \
"v_mul_f32 v129, v129, v196\nMvav 148,184,72\nC 218,244,245\nRa 148,6,54272\nv_mul_f32 v130, v130, v196\nv_mul_f32 v131, v131, v196\nMvav 148,188,76\nE 246,182\n" \
"v_mul_f32 v132, v132, v196\nv_mul_f32 v133, v133, v196\nQvav 156,176,80,12\nRa 152,6,55296\nRa 156,6,56320\nv_mul_f32 v134, v134, v196\nMvav 156,180,84\nE 247,183\n" \
"v_mul_f32 v135, v135, v196\nC 32,128,129\nMvav 156,184,88\nC 219,246,247\nC 33,130,131\nMvav 156,188,92\nE 240,184\nC 34,132,133\n" \
"C 35,134,135\nv_smfmac_f32_16x16x64_bf16 v[100:103], v[104:107], v[200:207], v7\ns_waitcnt lgkmcnt(0)\nbuffer_store_dwordx4 v[32:35], v0, s[60:63], s95 offen offset:0 sc1\nAR 136,8\nMaav 96,128,204\nDS 8,60,s77,2048\nRa 160,5,8192\n" \
"AR 137,9\nMaav 100,132,204\nE 241,185\nAR 138,10\nAR 139,11\nMaav 68,132,200\nC 220,240,241\nRa 164,5,9216\n" \
"AR 140,12\nMaav 64,128,200\nE 242,186\nAR 141,13\nAR 142,14\nMaav 72,136,200\nDS 8,60,s77,3072\nRa 168,5,10240\n" \
"AR 143,15\nv_mul_f32 v136, v136, v196\nMaav 76,140,200\nE 243,187\nv_mul_f32 v137, v137, v196\nMaav 108,140,204\nC 221,242,243\nRa 172,5,11264\n" \
"v_mul_f32 v138, v138, v196\nv_mul_f32 v139, v139, v196\nMaav 104,136,204\nE 244,188\nv_mul_f32 v140, v140, v196\nMaav 112,144,204\nRa 176,5,12288\nRa 180,5,13312\n" \
"Ra 184,5,14336\nv_mul_f32 v141, v141, v196\nv_mul_f32 v142, v142, v196\nMaav 116,148,204\nE 245,189\nv_mul_f32 v143, v143, v196\nC 36,136,137\nMaav 84,148,200\n" \
"C 222,244,245\nRa 188,5,15360\nC 37,138,139\nMaav 80,144,200\nE 246,190\nC 38,140,141\nC 39,142,143\nMaav 88,152,200\n" \
"s_branch 99f\n.p2align 12\nzp12:\ns_setpc_b64 s[98:99]\n99:\ns_mov_b32 s76, s73\ns_mov_b32 s73, s74\nbuffer_store_dwordx4 v[36:39], v0, s[60:63], s95 offen offset:1024 sc1\n" \
"Maav 92,156,200\nE 247,191\nMaav 124,156,204\nC 223,246,247\ns_waitcnt lgkmcnt(0)\nMaav 120,152,204\ns_mov_b32 s74, s75\ns_mov_b32 s75, s76\n" \
"s_add_u32 s77, s77, 0x4000\nQvav 176,160,64,12\nE 240,144\nMvav 176,164,68\nE 241,145\nMvav 176,168,72\nC 200,240,241\nRa 128,6,57344\n" \
"s_add_u32 s95, s83, 32768\nAR 128,16\nMvav 176,172,76\nE 242,146\nAR 129,17\nAR 130,18\nQvav 184,160,80,12\nRa 132,6,58368\n" \
"Ra 136,6,59392\nAR 131,19\nMvav 184,164,84\nE 243,147\nAR 132,20\nAR 133,21\nMvav 184,168,88\nC 201,242,243\n" \
"Ra 140,6,60416\nAR 134,22\nMvav 184,172,92\nE 244,148\nAR 135,23\nv_mul_f32 v128, v128, v196\nQvav 180,176,64,12\nRa 144,6,61440\n" \
"Ra 148,6,62464\nv_mul_f32 v129, v129, v196\nMvav 180,180,68\nE 245,149\nv_mul_f32 v130, v130, v196\nv_mul_f32 v131, v131, v196\nMvav 180,184,72\nC 202,244,245\n" \
"Ra 152,6,63488\nv_mul_f32 v132, v132, v196\nMvav 180,188,76\nE 246,150\nv_mul_f32 v133, v133, v196\nv_mul_f32 v134, v134, v196\nQvav 188,176,80,12\nRa 156,6,64512\n" \
"v_mul_f32 v135, v135, v196\nMvav 188,180,84\nE 247,151\nC 40,128,129\nC 41,130,131\nMvav 188,184,88\nC 203,246,247\nv_mov_b32 v6, v5\n" \
"C 42,132,133\nMvav 188,188,92\nE 240,152\nC 43,134,135\nbuffer_store_dwordx4 v[40:43], v0, s[60:63], s95 offen offset:0 sc1\nv_smfmac_f32_16x16x64_bf16 v[100:103], v[104:107], v[216:223], v7\ns_waitcnt vmcnt(19) lgkmcnt(0)\ns_barrier\n" \
"v_add_u32 v5, s74, v9\nAR 136,24\nAR 137,25\nMaav 96,128,220\ns_mov_b32 m0, s73\nRa 160,5,0\nRa 164,5,1024\nAR 138,26\n" \
"Maav 100,132,220\nE 241,153\nDS 0,60,s77,0\nAR 139,27\nAR 140,28\nMaav 68,132,216\nC 204,240,241\nRa 168,5,2048\n" \
"AR 141,29\nMaav 64,128,216\nE 242,154\nAR 142,30\nAR 143,31\nMaav 72,136,216\nDS 0,60,s77,1024\nRa 172,5,3072\n" \
"v_mul_f32 v136, v136, v196\nMaav 76,140,216\nE 243,155\nv_mul_f32 v137, v137, v196\nv_mul_f32 v138, v138, v196\nMaav 108,140,220\nC 205,242,243\nRa 176,5,4096\n" \
"v_mul_f32 v139, v139, v196\nMaav 104,136,220\nE 244,156\nDS 0,60,s77,2048\nv_mul_f32 v140, v140, v196\nv_mul_f32 v141, v141, v196\nMaav 112,144,220\nRa 180,5,5120\n" \
"Ra 184,5,6144\nRa 188,5,7168\nv_mul_f32 v142, v142, v196\nMaav 116,148,220\nE 245,157\nv_mul_f32 v143, v143, v196\nC 44,136,137\nMaav 84,148,216\n" \
"C 206,244,245\nDS 0,60,s77,3072\nC 45,138,139\nMaav 80,144,216\nE 246,158\nC 46,140,141\nC 47,142,143\nMaav 88,152,216\n" \
"buffer_store_dwordx4 v[44:47], v0, s[60:63], s95 offen offset:1024 sc1\nMaav 92,156,216\nE 247,159\nMaav 124,156,220\nC 207,246,247\ns_waitcnt lgkmcnt(0)\nMaav 120,152,220\nQvav 144,160,64,12\n" \
"E 240,176\nRa 128,6,49152\nMvav 144,164,68\nE 241,177\nMvav 144,168,72\nC 216,240,241\nRa 132,6,50176\ns_add_u32 s95, s83, 8192\n" \
"AR 128,32\nMvav 144,172,76\nE 242,178\ns_add_u32 m0, s75, 0xc000\nAR 129,33\nAR 130,34\nQvav 152,160,80,12\nDS 8,60,s77,0\n" \
"Ra 136,6,51200\nAR 131,35\nMvav 152,164,84\nE 243,179\nAR 132,36\nAR 133,37\nMvav 152,168,88\nC 217,242,243\n" \
"Ra 140,6,52224\nAR 134,38\nMvav 152,172,92\nE 244,180\nAR 135,39\nv_mul_f32 v128, v128, v197\nQvav 148,176,64,12\nDS 8,60,s77,1024\n" \
"Ra 144,6,53248\nv_mul_f32 v129, v129, v197\nMvav 148,180,68\nE 245,181\nv_mul_f32 v130, v130, v197\nv_mul_f32 v131, v131, v197\nMvav 148,184,72\nC 218,244,245\n" \
"Ra 148,6,54272\nv_mul_f32 v132, v132, v197\nMvav 148,188,76\nE 246,182\nv_mul_f32 v133, v133, v197\nv_mul_f32 v134, v134, v197\nQvav 156,176,80,12\nRa 152,6,55296\n" \
"Ra 156,6,56320\nv_mul_f32 v135, v135, v197\nMvav 156,180,84\nE 247,183\nC 48,128,129\nC 49,130,131\nMvav 156,184,88\nC 219,246,247\n" \
"C 50,132,133\nMvav 156,188,92\nE 240,184\nC 51,134,135\nbuffer_store_dwordx4 v[48:51], v0, s[60:63], s95 offen offset:0 sc1\nv_smfmac_f32_16x16x64_bf16 v[100:103], v[104:107], v[200:207], v7\ns_waitcnt lgkmcnt(0)\nAR 136,40\n" \
"AR 137,41\nMaav 96,128,204\nDS 8,60,s77,2048\nRa 160,5,8192\nAR 138,42\nMaav 100,132,204\nE 241,185\nAR 139,43\n" \
"AR 140,44\nMaav 68,132,200\nC 220,240,241\nRa 164,5,9216\nAR 141,45\nMaav 64,128,200\nE 242,186\nAR 142,46\n" \
"AR 143,47\nMaav 72,136,200\nDS 8,60,s77,3072\nRa 168,5,10240\nv_mul_f32 v136, v136, v197\nMaav 76,140,200\nE 243,187\nv_mul_f32 v137, v137, v197\n" \
"v_mul_f32 v138, v138, v197\nMaav 108,140,204\nC 221,242,243\nRa 172,5,11264\nv_mul_f32 v139, v139, v197\nMaav 104,136,204\nE 244,188\nv_mul_f32 v140, v140, v197\n" \
"v_mul_f32 v141, v141, v197\nMaav 112,144,204\nRa 176,5,12288\nRa 180,5,13312\nRa 184,5,14336\nv_mul_f32 v142, v142, v197\nMaav 116,148,204\nE 245,189\n" \
"v_mul_f32 v143, v143, v197\nC 52,136,137\nMaav 84,148,200\nC 222,244,245\nRa 188,5,15360\nC 53,138,139\nMaav 80,144,200\nE 246,190\n" \
"C 54,140,141\nC 55,142,143\nMaav 88,152,200\ns_mov_b32 s76, s73\ns_mov_b32 s73, s74\nbuffer_store_dwordx4 v[52:55], v0, s[60:63], s95 offen offset:1024 sc1\nMaav 92,156,200\nE 247,191\n" \
"Maav 124,156,204\nC 223,246,247\ns_waitcnt lgkmcnt(0)\nMaav 120,152,204\ns_mov_b32 s74, s75\ns_mov_b32 s75, s76\ns_add_u32 s77, s77, 0x4000\nQvav 176,160,64,12\n" \
"E 240,144\nMvav 176,164,68\nE 241,145\nMvav 176,168,72\nC 200,240,241\nRa 128,6,57344\ns_add_u32 s95, s83, 40960\nAR 128,48\n" \
"Mvav 176,172,76\nE 242,146\nAR 129,49\nAR 130,50\nQvav 184,160,80,12\nRa 132,6,58368\nRa 136,6,59392\nAR 131,51\n" \
"Mvav 184,164,84\nE 243,147\nAR 132,52\nAR 133,53\nMvav 184,168,88\nC 201,242,243\nRa 140,6,60416\nAR 134,54\n" \
"Mvav 184,172,92\nE 244,148\nAR 135,55\nv_mul_f32 v128, v128, v197\nQvav 180,176,64,12\nRa 144,6,61440\nRa 148,6,62464\nv_mul_f32 v129, v129, v197\n" \
"Mvav 180,180,68\nE 245,149\nv_mul_f32 v130, v130, v197\nv_mul_f32 v131, v131, v197\nMvav 180,184,72\nC 202,244,245\nRa 152,6,63488\nv_mul_f32 v132, v132, v197\n" \
"Mvav 180,188,76\nE 246,150\nv_mul_f32 v133, v133, v197\nv_mul_f32 v134, v134, v197\nQvav 188,176,80,12\nRa 156,6,64512\nv_mul_f32 v135, v135, v197\nMvav 188,180,84\n" \
"E 247,151\nC 56,128,129\nC 57,130,131\nMvav 188,184,88\nC 203,246,247\nv_mov_b32 v6, v5\nC 58,132,133\nMvav 188,188,92\n" \
"E 240,152\nC 59,134,135\nbuffer_store_dwordx4 v[56:59], v0, s[60:63], s95 offen offset:0 sc1\nv_smfmac_f32_16x16x64_bf16 v[100:103], v[104:107], v[216:223], v7\ns_waitcnt vmcnt(14) lgkmcnt(0)\ns_barrier\nv_add_u32 v5, s74, v9\nAR 136,56\n" \
"AR 137,57\nMaav 96,128,220\ns_mov_b32 m0, s73\nRa 160,5,0\nRa 164,5,1024\nAR 138,58\nMaav 100,132,220\nE 241,153\n" \
"DS 0,60,s77,0\nAR 139,59\nAR 140,60\nMaav 68,132,216\nC 204,240,241\nRa 168,5,2048\nAR 141,61\nMaav 64,128,216\n" \
"E 242,154\nAR 142,62\nAR 143,63\nMaav 72,136,216\nDS 0,60,s77,1024\nRa 172,5,3072\nv_mul_f32 v136, v136, v197\nMaav 76,140,216\n" \
"E 243,155\nv_mul_f32 v137, v137, v197\nv_mul_f32 v138, v138, v197\nMaav 108,140,220\nC 205,242,243\nRa 176,5,4096\nv_mul_f32 v139, v139, v197\nMaav 104,136,220\n" \
"E 244,156\nDS 0,60,s77,2048\nv_mul_f32 v140, v140, v197\nv_mul_f32 v141, v141, v197\nMaav 112,144,220\nRa 180,5,5120\nRa 184,5,6144\nRa 188,5,7168\n" \
"v_mul_f32 v142, v142, v197\nMaav 116,148,220\nE 245,157\nv_mul_f32 v143, v143, v197\nC 60,136,137\nMaav 84,148,216\nC 206,244,245\nDS 0,60,s77,3072\n" \
"C 61,138,139\nMaav 80,144,216\nE 246,158\nC 62,140,141\nC 63,142,143\nMaav 88,152,216\nbuffer_store_dwordx4 v[60:63], v0, s[60:63], s95 offen offset:1024 sc1\nMaav 92,156,216\n" \
"E 247,159\nMaav 124,156,220\nC 207,246,247\ns_waitcnt lgkmcnt(0)\nMaav 120,152,220\ns_branch 99f\n.p2align 12\nzp13:\n" \
"s_setpc_b64 s[98:99]\n99:\nzH:\nQvav 144,160,64,12\nE 240,176\nRa 128,6,49152\nMvav 144,164,68\nE 241,177\n" \
"Mvav 144,168,72\nC 216,240,241\nRa 132,6,50176\nMvav 144,172,76\nE 242,178\ns_add_u32 m0, s75, 0xc000\nQvav 152,160,80,12\nDS 8,60,s77,0\n" \
"Ra 136,6,51200\nMvav 152,164,84\nE 243,179\nMvav 152,168,88\nC 217,242,243\nRa 140,6,52224\nMvav 152,172,92\nE 244,180\n" \
"Qvav 148,176,64,12\nDS 8,60,s77,1024\nRa 144,6,53248\nMvav 148,180,68\nE 245,181\nMvav 148,184,72\nC 218,244,245\nRa 148,6,54272\n" \
"Mvav 148,188,76\nE 246,182\nQvav 156,176,80,12\nRa 152,6,55296\nRa 156,6,56320\nMvav 156,180,84\nE 247,183\nMvav 156,184,88\n" \
"C 219,246,247\nMvav 156,188,92\nE 240,184\nv_smfmac_f32_16x16x64_bf16 v[100:103], v[104:107], v[200:207], v7\ns_waitcnt lgkmcnt(0)\nMaav 96,128,204\nDS 8,60,s77,2048\nRa 160,5,8192\n" \
"Maav 100,132,204\nE 241,185\nMaav 68,132,200\nC 220,240,241\nRa 164,5,9216\nMaav 64,128,200\nE 242,186\nMaav 72,136,200\n" \
"DS 8,60,s77,3072\nRa 168,5,10240\nMaav 76,140,200\nE 243,187\nMaav 108,140,204\nC 221,242,243\nRa 172,5,11264\nMaav 104,136,204\n" \
"E 244,188\nMaav 112,144,204\nRa 176,5,12288\nRa 180,5,13312\nRa 184,5,14336\nMaav 116,148,204\nE 245,189\nMaav 84,148,200\n" \
"C 222,244,245\nRa 188,5,15360\nMaav 80,144,200\nE 246,190\nMaav 88,152,200\ns_mov_b32 s76, s73\ns_mov_b32 s73, s74\nMaav 92,156,200\n" \
"E 247,191\nMaav 124,156,204\nC 223,246,247\ns_waitcnt lgkmcnt(0)\nMaav 120,152,204\ns_mov_b32 s74, s75\ns_mov_b32 s75, s76\ns_add_u32 s77, s77, 0x4000\n" \
"Qvav 176,160,64,12\nE 240,144\nMvav 176,164,68\nE 241,145\nMvav 176,168,72\nC 200,240,241\nRa 128,6,57344\nMvav 176,172,76\n" \
"E 242,146\nQvav 184,160,80,12\nRa 132,6,58368\nRa 136,6,59392\nMvav 184,164,84\nE 243,147\nMvav 184,168,88\nC 201,242,243\n" \
"Ra 140,6,60416\nMvav 184,172,92\nE 244,148\nQvav 180,176,64,12\nRa 144,6,61440\nRa 148,6,62464\nMvav 180,180,68\nE 245,149\n" \
"Mvav 180,184,72\nC 202,244,245\nRa 152,6,63488\nMvav 180,188,76\nE 246,150\nQvav 188,176,80,12\nRa 156,6,64512\nMvav 188,180,84\n" \
"E 247,151\nMvav 188,184,88\nC 203,246,247\nv_mov_b32 v6, v5\nMvav 188,188,92\nE 240,152\nv_smfmac_f32_16x16x64_bf16 v[100:103], v[104:107], v[216:223], v7\ns_waitcnt vmcnt(8) lgkmcnt(0)\n" \
"s_barrier\nv_add_u32 v5, s74, v9\nMaav 96,128,220\ns_mov_b32 m0, s73\nRa 160,5,0\nRa 164,5,1024\nMaav 100,132,220\nE 241,153\n" \
"DS 0,60,s77,0\nMaav 68,132,216\nC 204,240,241\nRa 168,5,2048\nMaav 64,128,216\nE 242,154\nMaav 72,136,216\nDS 0,60,s77,1024\n" \
"Ra 172,5,3072\nMaav 76,140,216\nE 243,155\nMaav 108,140,220\nC 205,242,243\nRa 176,5,4096\nMaav 104,136,220\nE 244,156\n" \
"DS 0,60,s77,2048\nMaav 112,144,220\nRa 180,5,5120\nRa 184,5,6144\nRa 188,5,7168\nMaav 116,148,220\nE 245,157\nMaav 84,148,216\n" \
"C 206,244,245\ns_sub_u32 s96, s96, 1\nDS 0,60,s77,3072\nMaav 80,144,216\nE 246,158\nMaav 88,152,216\ns_cmp_lg_u32 s96, 0\nMaav 92,156,216\n" \
"E 247,159\nMaav 124,156,220\nC 207,246,247\ns_waitcnt lgkmcnt(0)\nMaav 120,152,220\ns_cbranch_scc1 zH\nQvav 144,160,64,112\nE 240,176\n" \
"Ra 128,6,49152\nMvav 144,164,68\nE 241,177\nMvav 144,168,72\nC 216,240,241\nRa 132,6,50176\nMvav 144,172,76\nE 242,178\n" \
"s_add_u32 m0, s75, 0xc000\nQvav 152,160,80,12\nDS 8,60,s77,0\nRa 136,6,51200\nMvav 152,164,84\nE 243,179\nMvav 152,168,88\nC 217,242,243\n" \
"Ra 140,6,52224\nMvav 152,172,92\nE 244,180\nQvav 148,176,64,116\nDS 8,60,s77,1024\nRa 144,6,53248\nMvav 148,180,68\nE 245,181\n" \
"Mvav 148,184,72\nC 218,244,245\nRa 148,6,54272\nMvav 148,188,76\nE 246,182\nQvav 156,176,80,12\nRa 152,6,55296\nRa 156,6,56320\n" \
"Mvav 156,180,84\nE 247,183\nMvav 156,184,88\nC 219,246,247\nMvav 156,188,92\nE 240,184\nv_smfmac_f32_16x16x64_bf16 v[100:103], v[104:107], v[200:207], v7\ns_waitcnt lgkmcnt(0)\n" \
"Maav 96,128,204\nDS 8,60,s77,2048\nRa 160,5,8192\nMaav 100,132,204\nE 241,185\nMaav 68,132,200\nC 220,240,241\nRa 164,5,9216\n" \
"Maav 64,128,200\nE 242,186\nMaav 72,136,200\nDS 8,60,s77,3072\nRa 168,5,10240\nMaav 76,140,200\nE 243,187\nMaav 108,140,204\n" \
"C 221,242,243\nRa 172,5,11264\nMaav 104,136,204\nE 244,188\nMaav 112,144,204\nRa 176,5,12288\nRa 180,5,13312\nRa 184,5,14336\n" \
"Maav 116,148,204\nE 245,189\nMaav 84,148,200\nC 222,244,245\nRa 188,5,15360\nMaav 80,144,200\nE 246,190\nMaav 88,152,200\n" \
"s_mov_b32 s76, s73\ns_mov_b32 s73, s74\nMaav 92,156,200\nE 247,191\nMaav 124,156,204\nC 223,246,247\ns_waitcnt lgkmcnt(0)\nMaav 120,152,204\n" \
"s_mov_b32 s74, s75\ns_mov_b32 s75, s76\ns_add_u32 s77, s77, 0x4000\nQvav 176,160,64,120\nE 240,144\nMvav 176,164,68\nE 241,145\nMvav 176,168,72\n" \
"C 200,240,241\nRa 128,6,57344\nMvav 176,172,76\nE 242,146\nQvav 184,160,80,12\nRa 132,6,58368\nRa 136,6,59392\nMvav 184,164,84\n" \
"E 243,147\nMvav 184,168,88\nC 201,242,243\nRa 140,6,60416\nMvav 184,172,92\nE 244,148\nQvav 180,176,64,124\nRa 144,6,61440\n" \
"Ra 148,6,62464\nMvav 180,180,68\nE 245,149\nMvav 180,184,72\nC 202,244,245\nRa 152,6,63488\nMvav 180,188,76\nE 246,150\n" \
"Qvav 188,176,80,12\nRa 156,6,64512\nMvav 188,180,84\nE 247,151\nMvav 188,184,88\nC 203,246,247\nv_mov_b32 v6, v5\nMvav 188,188,92\n" \
"E 240,152\nv_smfmac_f32_16x16x64_bf16 v[100:103], v[104:107], v[216:223], v7\ns_waitcnt vmcnt(8) lgkmcnt(0)\ns_barrier\nv_add_u32 v5, s74, v9\nMaav 96,128,220\nRa 160,5,0\nRa 164,5,1024\n" \
"Maav 100,132,220\nE 241,153\nMaav 68,132,216\nC 204,240,241\nRa 168,5,2048\nMaav 64,128,216\nE 242,154\nMaav 72,136,216\n" \
"Ra 172,5,3072\nRa 176,5,4096\nMaav 76,140,216\nE 243,155\nMaav 108,140,220\nC 205,242,243\nRa 180,5,5120\nMaav 104,136,220\n" \
"E 244,156\nMaav 112,144,220\nRa 184,5,6144\nRa 188,5,7168\nMaav 116,148,220\nE 245,157\nMaav 84,148,216\nC 206,244,245\n" \
"Maav 80,144,216\nE 246,158\nMaav 88,152,216\nMaav 92,156,216\nE 247,159\nMaav 124,156,220\nC 207,246,247\ns_waitcnt lgkmcnt(0)\n" \
"Maav 120,152,220\nzE3:\nQvav 152,160,80,112\nE 240,176\nRa 128,6,49152\nRa 132,6,50176\nMvav 152,164,84\nE 241,177\n" \
"s_add_u32 m0, s32, 147456\nRa 136,6,51200\nMvav 152,168,88\nC 216,240,241\nE 242,178\nD 18,68,0,0\nRa 140,6,52224\nMvav 152,172,92\n" \
"Ra 144,6,53248\nRa 148,6,54272\nRa 152,6,55296\nQvav 156,176,80,116\nE 243,179\nRa 156,6,56320\nMvav 156,180,84\nC 217,242,243\n" \
"E 244,180\nMvav 156,184,88\nD 18,68,s89,1024\nMvav 156,188,92\nE 245,181\nv_smfmac_f32_16x16x64_bf16 v[100:103], v[104:107], v[200:207], v7\nC 218,244,245\ns_waitcnt lgkmcnt(0)\n" \
"Maav 96,128,204\nE 246,182\nRa 160,5,8192\nMaav 100,132,204\nD 19,68,s90,2048\nMaav 68,132,200\nE 247,183\nRa 164,5,9216\n" \
"Maav 64,128,200\nC 219,246,247\nE 240,184\nMaav 72,136,200\nE 241,185\nRa 168,5,10240\nMaav 76,140,200\nC 220,240,241\n" \
"D 19,68,s91,3072\nMaav 108,140,204\nE 242,186\nRa 172,5,11264\nMaav 104,136,204\nE 243,187\nMaav 112,144,204\nC 221,242,243\n" \
"Ra 176,5,12288\nRa 180,5,13312\nMaav 116,148,204\nE 244,188\nMaav 84,148,200\nE 245,189\nRa 184,5,14336\nMaav 80,144,200\n" \
"C 222,244,245\nRa 188,5,15360\nMaav 88,152,200\nE 246,190\nMaav 92,156,200\nE 247,191\nMaav 124,156,204\nC 223,246,247\n" \
"s_waitcnt lgkmcnt(0)\nMaav 120,152,204\nQvav 184,160,80,120\nRa 128,6,57344\nMvav 184,164,84\nRa 132,6,58368\nMvav 184,168,88\nRa 136,6,59392\n" \
"Mvav 184,172,92\nRa 140,6,60416\nQvav 188,176,80,124\nRa 144,6,61440\nMvav 188,180,84\nRa 148,6,62464\nMvav 188,184,88\nRa 152,6,63488\n" \
"Mvav 188,188,92\nRa 156,6,64512\nv_smfmac_f32_16x16x64_bf16 v[100:103], v[104:107], v[216:223], v7\ns_waitcnt vmcnt(4) lgkmcnt(0)\ns_barrier\nv_mov_b32 v6, v5\nMaav 96,128,220\nRa 160,6,49152\n" \
"Maav 100,132,220\nE 240,152\nE 241,153\nMaav 68,132,216\nRa 164,6,50176\nC 204,240,241\nMaav 64,128,216\nE 242,154\n" \
"Maav 72,136,216\nRa 168,6,51200\nMaav 76,140,216\nE 243,155\nMaav 108,140,220\nRa 172,6,52224\nC 205,242,243\nMaav 104,136,220\n" \
"E 244,156\nMaav 112,144,220\nRa 176,6,53248\nMaav 116,148,220\nE 245,157\nMaav 84,148,216\nRa 180,6,54272\nC 206,244,245\n" \
"Maav 80,144,216\nE 246,158\nMaav 88,152,216\nRa 184,6,55296\nMaav 92,156,216\nE 247,159\nMaav 124,156,220\nRa 188,6,56320\n" \
"C 207,246,247\nMaav 120,152,220\ns_waitcnt lgkmcnt(0)\nE 240,184\nMvvv 100,108,204\nRa 128,6,57344\nE 241,185\nMaav 96,160,204\n" \
"Ra 132,6,58368\nC 192,240,241\nE 242,186\nMaav 100,164,204\nRa 136,6,59392\nE 243,187\nMaav 104,168,204\nRa 140,6,60416\n" \
"C 193,242,243\nE 244,188\nMaav 108,172,204\nRa 144,6,61440\nE 245,189\nMaav 112,176,204\nRa 148,6,62464\nC 194,244,245\n" \
"E 246,190\nMaav 116,180,204\nRa 152,6,63488\nE 247,191\nMaav 120,184,204\nRa 156,6,64512\nC 195,246,247\nMaav 124,188,204\n" \
"s_nop 0\ns_waitcnt lgkmcnt(0)\nMvvv 100,108,192\nMaav 96,128,192\nMaav 100,132,192\nMaav 104,136,192\nMaav 108,140,192\nMaav 112,144,192\n" \
"Maav 116,148,192\nMaav 120,152,192\nv_add_f32 v104, v96, v97\nv_add_f32 v105, v98, v99\nv_add_f32 v106, v100, v101\nv_add_f32 v107, v102, v103\nMaav 124,156,192\nv_subrev_u32 v240, 0xf800000, v104\n" \
"v_subrev_u32 v241, 0xf800000, v105\nv_subrev_u32 v242, 0xf800000, v106\nv_subrev_u32 v243, 0xf800000, v107\nv_max3_u32 v244, v240, v241, v242\nv_max_u32 v244, v244, v243\nv_cmp_lt_u32 vcc, 0x3c000000, v244\ns_cbranch_vccnz zfb\nzend:\n" \
"s_nop 4\nv_rcp_f32 v110, v106\nv_rcp_f32 v111, v107\nv_xor_b32 v20, s21, v20\nv_xor_b32 v21, s21, v21\nv_xor_b32 v22, s21, v22\nv_xor_b32 v23, s21, v23\nv_xor_b32 v24, s21, v24\n" \
"v_xor_b32 v25, s21, v25\nv_xor_b32 v26, s21, v26\nv_xor_b32 v27, s21, v27\ns_add_u32 s95, s83, 16384\nAR 112,64\nAR 113,65\nAR 114,66\nAR 115,67\n" \
"AR 116,68\nAR 117,69\nAR 118,70\nAR 119,71\nv_pk_mul_f32 v[112:113], v[112:113], v[110:111] op_sel:[0,0] op_sel_hi:[1,0]\nv_pk_mul_f32 v[114:115], v[114:115], v[110:111] op_sel:[0,0] op_sel_hi:[1,0]\nv_pk_mul_f32 v[116:117], v[116:117], v[110:111] op_sel:[0,0] op_sel_hi:[1,0]\nv_pk_mul_f32 v[118:119], v[118:119], v[110:111] op_sel:[0,0] op_sel_hi:[1,0]\n" \
"C 64,112,113\nC 65,114,115\nC 66,116,117\nC 67,118,119\nTa 192,20,32768\nTa 194,20,33792\nTa 224,20,40960\nTa 226,20,41984\n" \
"buffer_store_dwordx4 v[64:67], v0, s[60:63], s95 offen offset:0 sc1\nAR 120,72\nAR 121,73\nAR 122,74\nAR 123,75\nAR 124,76\nAR 125,77\nAR 126,78\n" \
"AR 127,79\nv_pk_mul_f32 v[120:121], v[120:121], v[110:111] op_sel:[0,0] op_sel_hi:[1,0]\nv_pk_mul_f32 v[122:123], v[122:123], v[110:111] op_sel:[0,0] op_sel_hi:[1,0]\nv_pk_mul_f32 v[124:125], v[124:125], v[110:111] op_sel:[0,0] op_sel_hi:[1,0]\nv_pk_mul_f32 v[126:127], v[126:127], v[110:111] op_sel:[0,0] op_sel_hi:[1,0]\nC 68,120,121\nC 69,122,123\nC 70,124,125\n" \
"C 71,126,127\nTa 196,21,32768\nTa 198,21,33792\nTa 228,21,40960\nTa 230,21,41984\nbuffer_store_dwordx4 v[68:71], v0, s[60:63], s95 offen offset:1024 sc1\ns_add_u32 s95, s83, 24576\nAR 112,96\n" \
"AR 113,97\nAR 114,98\nAR 115,99\nAR 116,100\nAR 117,101\nAR 118,102\nAR 119,103\nv_pk_mul_f32 v[112:113], v[112:113], v[110:111] op_sel:[0,1] op_sel_hi:[1,1]\n" \
"v_pk_mul_f32 v[114:115], v[114:115], v[110:111] op_sel:[0,1] op_sel_hi:[1,1]\nv_pk_mul_f32 v[116:117], v[116:117], v[110:111] op_sel:[0,1] op_sel_hi:[1,1]\nv_pk_mul_f32 v[118:119], v[118:119], v[110:111] op_sel:[0,1] op_sel_hi:[1,1]\nC 80,112,113\nC 81,114,115\ns_branch 99f\n.p2align 12\nzq0:\n" \
"zp14:\ns_setpc_b64 s[98:99]\n99:\nC 82,116,117\nC 83,118,119\nTa 200,22,32768\nTa 202,22,33792\nTa 232,22,40960\n" \
"Ta 234,22,41984\nbuffer_store_dwordx4 v[80:83], v0, s[60:63], s95 offen offset:0 sc1\nAR 120,104\nAR 121,105\nAR 122,106\nAR 123,107\nAR 124,108\nAR 125,109\n" \
"AR 126,110\nAR 127,111\nv_pk_mul_f32 v[120:121], v[120:121], v[110:111] op_sel:[0,1] op_sel_hi:[1,1]\nv_pk_mul_f32 v[122:123], v[122:123], v[110:111] op_sel:[0,1] op_sel_hi:[1,1]\nv_pk_mul_f32 v[124:125], v[124:125], v[110:111] op_sel:[0,1] op_sel_hi:[1,1]\nv_pk_mul_f32 v[126:127], v[126:127], v[110:111] op_sel:[0,1] op_sel_hi:[1,1]\nC 84,120,121\nC 85,122,123\n" \
"C 86,124,125\nC 87,126,127\nTa 204,23,32768\nTa 206,23,33792\nTa 236,23,40960\nTa 238,23,41984\nbuffer_store_dwordx4 v[84:87], v0, s[60:63], s95 offen offset:1024 sc1\ns_add_u32 s95, s83, 49152\n" \
"AR 112,80\nAR 113,81\nAR 114,82\nAR 115,83\nAR 116,84\nAR 117,85\nAR 118,86\nAR 119,87\n" \
"v_pk_mul_f32 v[112:113], v[112:113], v[110:111] op_sel:[0,0] op_sel_hi:[1,0]\nv_pk_mul_f32 v[114:115], v[114:115], v[110:111] op_sel:[0,0] op_sel_hi:[1,0]\nv_pk_mul_f32 v[116:117], v[116:117], v[110:111] op_sel:[0,0] op_sel_hi:[1,0]\nv_pk_mul_f32 v[118:119], v[118:119], v[110:111] op_sel:[0,0] op_sel_hi:[1,0]\nC 72,112,113\nC 73,114,115\nC 74,116,117\nC 75,118,119\n" \
"Ta 208,24,32768\nTa 210,24,33792\nTa 240,24,40960\nTa 242,24,41984\nbuffer_store_dwordx4 v[72:75], v0, s[60:63], s95 offen offset:0 sc1\nAR 120,88\nAR 121,89\nAR 122,90\n" \
"AR 123,91\nAR 124,92\nAR 125,93\nAR 126,94\nAR 127,95\nv_pk_mul_f32 v[120:121], v[120:121], v[110:111] op_sel:[0,0] op_sel_hi:[1,0]\nv_pk_mul_f32 v[122:123], v[122:123], v[110:111] op_sel:[0,0] op_sel_hi:[1,0]\nv_pk_mul_f32 v[124:125], v[124:125], v[110:111] op_sel:[0,0] op_sel_hi:[1,0]\n" \
"v_pk_mul_f32 v[126:127], v[126:127], v[110:111] op_sel:[0,0] op_sel_hi:[1,0]\nC 76,120,121\nC 77,122,123\nC 78,124,125\nC 79,126,127\nTa 212,25,32768\nTa 214,25,33792\nTa 244,25,40960\n" \
"Ta 246,25,41984\nbuffer_store_dwordx4 v[76:79], v0, s[60:63], s95 offen offset:1024 sc1\ns_add_u32 s95, s83, 57344\nAR 112,112\nAR 113,113\nAR 114,114\nAR 115,115\nAR 116,116\n" \
"AR 117,117\nAR 118,118\nAR 119,119\nv_pk_mul_f32 v[112:113], v[112:113], v[110:111] op_sel:[0,1] op_sel_hi:[1,1]\nv_pk_mul_f32 v[114:115], v[114:115], v[110:111] op_sel:[0,1] op_sel_hi:[1,1]\nv_pk_mul_f32 v[116:117], v[116:117], v[110:111] op_sel:[0,1] op_sel_hi:[1,1]\nv_pk_mul_f32 v[118:119], v[118:119], v[110:111] op_sel:[0,1] op_sel_hi:[1,1]\nC 88,112,113\n" \
"C 89,114,115\nC 90,116,117\nC 91,118,119\nTa 216,26,32768\nTa 218,26,33792\nTa 248,26,40960\nTa 250,26,41984\nbuffer_store_dwordx4 v[88:91], v0, s[60:63], s95 offen offset:0 sc1\n" \
"AR 120,120\nAR 121,121\nAR 122,122\nAR 123,123\nAR 124,124\nAR 125,125\nAR 126,126\nAR 127,127\n" \
"v_pk_mul_f32 v[120:121], v[120:121], v[110:111] op_sel:[0,1] op_sel_hi:[1,1]\nv_pk_mul_f32 v[122:123], v[122:123], v[110:111] op_sel:[0,1] op_sel_hi:[1,1]\nv_pk_mul_f32 v[124:125], v[124:125], v[110:111] op_sel:[0,1] op_sel_hi:[1,1]\nv_pk_mul_f32 v[126:127], v[126:127], v[110:111] op_sel:[0,1] op_sel_hi:[1,1]\nC 92,120,121\nC 93,122,123\nC 94,124,125\nC 95,126,127\n" \
"Ta 220,27,32768\nTa 222,27,33792\nTa 252,27,40960\nTa 254,27,41984\nbuffer_store_dwordx4 v[92:95], v0, s[60:63], s95 offen offset:1024 sc1\ns_waitcnt lgkmcnt(0)\nZvav 128,192,32\nv_lshrrev_b32 v28, 1, v0\n" \
"s_lshl_b32 s82, s10, 3\nMvav 128,224,36\nv_xor_b32 v29, s82, v28\nv_and_b32 v28, 0x78, v28\nZvav 132,228,36\nv_add_u32 v29, s4, v29\nv_add_u32 v28, s5, v28\nMvav 132,196,32\n" \
"v_cmp_gt_u32 vcc, 512, v0\ns_nop 0\nZvav 136,200,32\nv_cndmask_b32 v1, v28, v29, vcc\ns_mov_b32 s79, s78\nMvav 136,232,36\ns_xor_b32 s82, s79, s10\ns_lshl_b32 s81, s82, 16\n" \
"Zvav 140,236,36\ns_cmp_lt_u32 s79, 32\ns_cselect_b32 s82, s18, s19\nMvav 140,204,32\ns_add_u32 s81, s81, s82\nZvav 144,208,32\nMvav 144,240,36\nZvav 148,244,36\n" \
"Mvav 148,212,32\nZvav 152,216,32\nMvav 152,248,36\nZvav 156,252,36\nMvav 156,220,32\nZvav 188,220,48\nMvav 188,252,52\nZvav 184,248,52\n" \
"Mvav 184,216,48\nZvav 180,212,48\ns_ff1_i32_b64 s78, s[8:9]\ns_bitset0_b64 s[8:9], s78\nMvav 180,244,52\ns_xor_b32 s82, s78, s10\ns_and_b32 s80, s82, 31\nZvav 176,240,52\n" \
"s_lshl_b32 s80, s80, 18\ns_cmp_lt_u32 s78, 32\nMvav 176,208,48\ns_cselect_b64 s[68:69], s[14:15], s[12:13]\ns_cselect_b32 s70, 0x800000, s92\nZvav 172,204,48\nv_add_u32 v18, s80, v16\nv_xor_b32 v19, 128, v18\n" \
"Mvav 172,236,52\nZvav 168,232,52\ns_waitcnt vmcnt(24)\ns_barrier\nMvav 168,200,48\nTa 128,20,49152\nTa 130,20,50176\nZvav 164,196,48\n" \
"Ta 160,20,57344\nMvav 164,228,52\nTa 162,20,58368\nZvav 160,224,52\nTa 132,21,49152\nMvav 160,192,48\nTa 134,21,50176\nZvav 192,192,64\n" \
"Ta 164,21,57344\nTa 166,21,58368\nMvav 192,224,68\nTa 136,22,49152\nZvav 196,228,68\nTa 138,22,50176\nMvav 196,196,64\nTa 168,22,57344\n" \
"Zvav 200,200,64\nTa 170,22,58368\nMvav 200,232,68\nTa 140,23,49152\nTa 142,23,50176\nZvav 204,236,68\nTa 172,23,57344\nMvav 204,204,64\n" \
"Ta 174,23,58368\nZvav 208,208,64\ns_add_u32 m0, s84, 0\nMvav 208,240,68\nD 18,68,0,0\nZvav 212,244,68\nTa 144,24,49152\nTa 146,24,50176\n" \
"Mvav 212,212,64\nTa 176,24,57344\nZvav 216,216,64\nTa 178,24,58368\nMvav 216,248,68\nD 18,68,s89,1024\nZvav 220,252,68\nTa 148,25,49152\n" \
"Mvav 220,220,64\nTa 150,25,50176\nZvav 252,220,80\nTa 180,25,57344\nTa 182,25,58368\nMvav 252,252,84\nTa 152,26,49152\nZvav 248,248,84\n" \
"Ta 154,26,50176\nMvav 248,216,80\nTa 184,26,57344\nZvav 244,212,80\nTa 186,26,58368\nMvav 244,244,84\nTa 156,27,49152\nTa 158,27,50176\n" \
"Zvav 240,240,84\ns_cmp_lg_u32 s32, 0\nMvav 240,208,80\nTa 188,27,57344\nZvav 236,204,80\ns_waitcnt vmcnt(2)\ns_barrier\ns_cbranch_scc1 zsn\n" \
"buffer_store_dwordx2 v[2:3], off, s[60:63], s31 sc1\nzsn:\nMvav 236,236,84\nD 19,68,s90,2048\nZvav 232,232,84\nTa 190,27,58368\nMvav 232,200,80\nZvav 228,196,80\n" \
"Mvav 228,228,84\nD 19,68,s91,3072\nZvav 224,224,84\nv_add_u32 v18, 131072, v18\nMvav 224,192,80\nv_add_u32 v19, 131072, v19\ns_waitcnt lgkmcnt(0)\nMvav 128,128,40\n" \
"Mvav 128,160,44\nMvav 132,164,44\nMvav 132,132,40\nMvav 136,136,40\nMvav 136,168,44\nMvav 140,172,44\nMvav 140,140,40\nMvav 144,144,40\n" \
"v_xor_b32 v20, 0x8000, v20\nv_xor_b32 v21, 0x8000, v21\nMvav 144,176,44\nv_xor_b32 v22, 0x8000, v22\nv_xor_b32 v23, 0x8000, v23\nMvav 148,180,44\nv_xor_b32 v24, 0x8000, v24\nv_xor_b32 v25, 0x8000, v25\n" \
"Mvav 148,148,40\nv_xor_b32 v26, 0x8000, v26\nv_xor_b32 v27, 0x8000, v27\nMvav 152,152,40\nMvav 152,184,44\nMvav 156,188,44\nMvav 156,156,40\nMvav 188,156,56\n" \
"buffer_load_dwordx2 v[10:11], v1, s[60:63], 0 offen sc1\nMvav 188,188,60\nMvav 184,184,60\nMvav 184,152,56\nMvav 180,148,56\nMvav 180,180,60\nMvav 176,176,60\nMvav 176,144,56\n" \
"Mvav 172,140,56\nMvav 172,172,60\nMvav 168,168,60\ns_waitcnt vmcnt(25)\ns_barrier\nMvav 168,136,56\nTa 192,20,32768\nTa 194,20,33792\n" \
"Ta 224,20,40960\nMvav 164,132,56\nTa 226,20,41984\nTa 196,21,32768\nMvav 164,164,60\nTa 198,21,33792\nTa 228,21,40960\nMvav 160,160,60\n" \
"Ta 230,21,41984\nTa 200,22,32768\nMvav 160,128,56\nTa 202,22,33792\nTa 232,22,40960\nMvav 192,128,72\nv_add_u32 v9, s81, v0\nTa 234,22,41984\n" \
"Mvav 192,160,76\ns_cmp_eq_u32 s32, 0\nTa 204,23,32768\nMvav 196,164,76\nTa 206,23,33792\nTa 236,23,40960\nMvav 196,132,72\nTa 238,23,41984\n" \
"Ta 208,24,32768\nMvav 200,136,72\ns_cbranch_scc1 zsk\ns_waitcnt vmcnt(0)\nv_cmp_eq_u64 vcc, v[10:11], v[2:3]\ns_andn2_b32 s82, s7, vcc_hi\ns_cselect_b64 vcc, 0, vcc\ns_bitcmp1_b64 vcc, s79\n" \
"s_cbranch_scc0 zs104\nzr104:\nzsk:\ns_barrier\ns_bitcmp1_b32 s33, 4\nMvav 200,168,76\ns_cbranch_scc1 zep\nzeq:\n" \
"Ta 210,24,33792\nMvav 204,172,76\nbuffer_load_dwordx4 a[0:3], v9, s[60:63], 0 offen offset:0 sc1\nMvav 204,140,72\nbuffer_load_dwordx4 a[4:7], v9, s[60:63], 0 offen offset:1024 sc1\nMvav 208,144,72\ns_add_u32 m0, s84, 16384\nTa 240,24,40960\n" \
"Mvav 208,176,76\nD 18,68,0,0\nbuffer_load_dwordx2 v[10:11], v1, s[60:63], 0 offen sc1\nMvav 212,180,76\ns_xor_b32 s84, s84, 0x38000\nbuffer_load_dwordx4 a[8:11], v9, s[60:63], s85 offen offset:0 sc1\nMvav 212,148,72\nTa 242,24,41984\n" \
"Ta 212,25,32768\nMvav 216,152,72\nbuffer_load_dwordx4 a[12:15], v9, s[60:63], s85 offen offset:1024 sc1\nMvav 216,184,76\nD 18,68,s89,1024\nbuffer_load_dwordx4 a[16:19], v9, s[60:63], s86 offen offset:0 sc1\nMvav 220,188,76\nTa 214,25,33792\n" \
"Ta 244,25,40960\nMvav 220,156,72\nbuffer_load_dwordx4 a[20:23], v9, s[60:63], s86 offen offset:1024 sc1\nMvav 252,156,88\nbuffer_load_dwordx4 a[24:27], v9, s[60:63], s87 offen offset:0 sc1\nMvav 252,188,92\nTa 246,25,41984\nTa 216,26,32768\n" \
"Mvav 248,184,92\nbuffer_load_dwordx4 a[28:31], v9, s[60:63], s87 offen offset:1024 sc1\nMvav 248,152,88\nv_add_u32 v9, 0x8000, v9\nTa 218,26,33792\nMvav 244,148,88\nTa 248,26,40960\nTa 250,26,41984\n" \
"Mvav 244,180,92\nbuffer_load_dwordx4 a[32:35], v9, s[60:63], 0 offen offset:0 sc1\nMvav 240,176,92\nbuffer_load_dwordx4 a[36:39], v9, s[60:63], 0 offen offset:1024 sc1\nMvav 240,144,88\nTa 220,27,32768\nTa 222,27,33792\nMvav 236,140,88\n" \
"buffer_load_dwordx4 a[40:43], v9, s[60:63], s85 offen offset:0 sc1\ns_mov_b32 s96, s22\nMvav 236,172,92\nD 19,68,s90,2048\nbuffer_load_dwordx4 a[44:47], v9, s[60:63], s85 offen offset:1024 sc1\ns_cmp_eq_u32 s96, 0\nMvav 232,168,92\nTa 252,27,40960\n" \
"Ta 254,27,41984\nMvav 232,136,88\nbuffer_load_dwordx4 a[48:51], v9, s[60:63], s86 offen offset:0 sc1\nMvav 228,132,88\nbuffer_load_dwordx4 a[52:55], v9, s[60:63], s86 offen offset:1024 sc1\nMvav 228,164,92\nD 19,68,s91,3072\nMvav 224,160,92\n" \
"Mvav 224,128,88\ns_waitcnt vmcnt(17) lgkmcnt(0)\ns_cbranch_scc1 zox\ns_branch 99f\n.p2align 12\nzp15:\ns_setpc_b64 s[98:99]\n99:\n" \
"zol:\nMvaa 128,192,0\ns_waitcnt vmcnt(10)\nv_cmp_eq_u64 vcc, v[10:11], v[2:3]\ns_mov_b32 s79, s78\nMvaa 128,224,4\nbuffer_load_dwordx4 a[56:59], v9, s[60:63], s87 offen offset:0 sc1\ns_xor_b32 s82, s79, s10\n" \
"Mvaa 132,228,4\ns_lshl_b32 s81, s82, 16\ns_cmp_lt_u32 s79, 32\ns_cselect_b32 s82, s18, s19\nMvaa 132,196,0\ns_add_u32 s81, s81, s82\ns_bitcmp1_b64 vcc, s79\nMvaa 136,200,0\n" \
"Mvaa 136,232,4\nbuffer_load_dwordx4 a[60:63], v9, s[60:63], s87 offen offset:1024 sc1\nMvaa 140,236,4\nMvaa 140,204,0\nv_add_u32 v9, s81, v0\nMvaa 144,208,0\nMvaa 144,240,4\nMvaa 148,244,4\n" \
"s_cbranch_scc0 zs107\nzr107:\nMvaa 148,212,0\ns_ff1_i32_b64 s78, s[8:9]\ns_bitset0_b64 s[8:9], s78\nMvaa 152,216,0\ns_xor_b32 s82, s78, s10\ns_and_b32 s80, s82, 31\n" \
"Mvaa 152,248,4\ns_lshl_b32 s80, s80, 18\ns_cmp_lt_u32 s78, 32\nMvaa 156,252,4\ns_cselect_b64 s[68:69], s[14:15], s[12:13]\ns_cselect_b32 s70, 0x800000, s92\nMvaa 156,220,0\nv_add_u32 v18, s80, v16\n" \
"v_xor_b32 v19, 128, v18\ns_waitcnt vmcnt(15)\nMvaa 188,220,8\nMvaa 188,252,12\nbuffer_load_dwordx4 a[0:3], v9, s[60:63], 0 offen offset:0 sc1\nMvaa 184,248,12\nMvaa 184,216,8\nMvaa 180,212,8\n" \
"Mvaa 180,244,12\nbuffer_load_dwordx4 a[4:7], v9, s[60:63], 0 offen offset:1024 sc1\nMvaa 176,240,12\nMvaa 176,208,8\nMvaa 172,204,8\nMvaa 172,236,12\nMvaa 168,232,12\ns_waitcnt vmcnt(29)\n" \
"s_barrier\nMvaa 168,200,8\nTa 128,20,49152\nTa 130,20,50176\nMvaa 164,196,8\nTa 160,20,57344\nMvaa 164,228,12\nTa 162,20,58368\n" \
"Ta 132,21,49152\nMvaa 160,224,12\nTa 134,21,50176\nMvaa 160,192,8\ns_waitcnt vmcnt(14)\nTa 164,21,57344\nMvaa 192,192,16\nTa 166,21,58368\n" \
"Mvaa 192,224,20\nbuffer_load_dwordx4 a[8:11], v9, s[60:63], s85 offen offset:0 sc1\nMvaa 196,228,20\nTa 136,22,49152\nMvaa 196,196,16\nTa 138,22,50176\nTa 168,22,57344\nMvaa 200,200,16\n" \
"Ta 170,22,58368\nMvaa 200,232,20\nbuffer_load_dwordx4 a[12:15], v9, s[60:63], s85 offen offset:1024 sc1\nMvaa 204,236,20\nTa 140,23,49152\nMvaa 204,204,16\nTa 142,23,50176\nTa 172,23,57344\n" \
"Mvaa 208,208,16\ns_add_u32 m0, s84, 0\nMvaa 208,240,20\nD 18,68,0,0\nMvaa 212,244,20\nTa 174,23,58368\nTa 144,24,49152\nMvaa 212,212,16\n" \
"Ta 146,24,50176\nMvaa 216,216,16\nTa 176,24,57344\nTa 178,24,58368\nMvaa 216,248,20\nD 18,68,s89,1024\nMvaa 220,252,20\nTa 148,25,49152\n" \
"Mvaa 220,220,16\ns_waitcnt vmcnt(16)\nTa 150,25,50176\nMvaa 252,220,24\nTa 180,25,57344\nMvaa 252,252,28\nbuffer_load_dwordx4 a[16:19], v9, s[60:63], s86 offen offset:0 sc1\nMvaa 248,248,28\n" \
"Ta 182,25,58368\nMvaa 248,216,24\nTa 152,26,49152\nTa 154,26,50176\nMvaa 244,212,24\nTa 184,26,57344\nMvaa 244,244,28\nbuffer_load_dwordx4 a[20:23], v9, s[60:63], s86 offen offset:1024 sc1\n" \
"Mvaa 240,240,28\nTa 186,26,58368\nMvaa 240,208,24\nTa 156,27,49152\nTa 158,27,50176\nMvaa 236,204,24\nTa 188,27,57344\nMvaa 236,236,28\n" \
"D 19,68,s90,2048\nMvaa 232,232,28\nTa 190,27,58368\nMvaa 232,200,24\nMvaa 228,196,24\nMvaa 228,228,28\nD 19,68,s91,3072\nMvaa 224,224,28\n" \
"v_add_u32 v18, 131072, v18\nMvaa 224,192,24\nv_add_u32 v19, 131072, v19\ns_waitcnt vmcnt(18) lgkmcnt(0)\nMvaa 128,128,32\nMvaa 128,160,36\nbuffer_load_dwordx4 a[24:27], v9, s[60:63], s87 offen offset:0 sc1\nMvaa 132,164,36\n" \
"Mvaa 132,132,32\nMvaa 136,136,32\nMvaa 136,168,36\nbuffer_load_dwordx4 a[28:31], v9, s[60:63], s87 offen offset:1024 sc1\nMvaa 140,172,36\nMvaa 140,140,32\nv_add_u32 v9, 0x8000, v9\nMvaa 144,144,32\n" \
"v_xor_b32 v20, 0x8000, v20\nv_xor_b32 v21, 0x8000, v21\nMvaa 144,176,36\nv_xor_b32 v22, 0x8000, v22\nv_xor_b32 v23, 0x8000, v23\nMvaa 148,180,36\nv_xor_b32 v24, 0x8000, v24\nv_xor_b32 v25, 0x8000, v25\n" \
"Mvaa 148,148,32\nbuffer_load_dwordx2 v[10:11], v1, s[60:63], 0 offen sc1\nMvaa 152,152,32\nv_xor_b32 v26, 0x8000, v26\nv_xor_b32 v27, 0x8000, v27\nMvaa 152,184,36\nMvaa 156,188,36\nMvaa 156,156,32\n" \
"s_waitcnt vmcnt(18)\nMvaa 188,156,40\nMvaa 188,188,44\nbuffer_load_dwordx4 a[32:35], v9, s[60:63], 0 offen offset:0 sc1\nMvaa 184,184,44\nMvaa 184,152,40\nMvaa 180,148,40\nMvaa 180,180,44\n" \
"buffer_load_dwordx4 a[36:39], v9, s[60:63], 0 offen offset:1024 sc1\nMvaa 176,176,44\nMvaa 176,144,40\nMvaa 172,140,40\nMvaa 172,172,44\nMvaa 168,168,44\ns_waitcnt vmcnt(30)\ns_barrier\n" \
"Mvaa 168,136,40\nTa 192,20,32768\nTa 194,20,33792\nMvaa 164,132,40\nTa 224,20,40960\nMvaa 164,164,44\nTa 226,20,41984\nTa 196,21,32768\n" \
"Mvaa 160,160,44\nTa 198,21,33792\nMvaa 160,128,40\ns_waitcnt vmcnt(18)\nTa 228,21,40960\nMvaa 192,128,48\nTa 230,21,41984\nMvaa 192,160,52\n" \
"buffer_load_dwordx4 a[40:43], v9, s[60:63], s85 offen offset:0 sc1\nMvaa 196,164,52\nTa 200,22,32768\nTa 202,22,33792\nMvaa 196,132,48\nTa 232,22,40960\nMvaa 200,136,48\nTa 234,22,41984\n" \
"Ta 204,23,32768\nMvaa 200,168,52\nbuffer_load_dwordx4 a[44:47], v9, s[60:63], s85 offen offset:1024 sc1\nMvaa 204,172,52\nTa 206,23,33792\nMvaa 204,140,48\nTa 236,23,40960\nTa 238,23,41984\n" \
"Mvaa 208,144,48\ns_add_u32 m0, s84, 16384\nMvaa 208,176,52\nD 18,68,0,0\nMvaa 212,180,52\ns_xor_b32 s84, s84, 0x38000\nTa 208,24,32768\nMvaa 212,148,48\n" \
"Ta 210,24,33792\nTa 240,24,40960\nMvaa 216,152,48\nTa 242,24,41984\nMvaa 216,184,52\nD 18,68,s89,1024\nMvaa 220,188,52\nTa 212,25,32768\n" \
"Ta 214,25,33792\nMvaa 220,156,48\ns_waitcnt vmcnt(19)\nMvaa 252,156,56\nTa 244,25,40960\nTa 246,25,41984\nMvaa 252,188,60\nbuffer_load_dwordx4 a[48:51], v9, s[60:63], s86 offen offset:0 sc1\n" \
"Mvaa 248,184,60\nTa 216,26,32768\nMvaa 248,152,56\nTa 218,26,33792\nTa 248,26,40960\nMvaa 244,148,56\nTa 250,26,41984\nMvaa 244,180,60\n" \
"buffer_load_dwordx4 a[52:55], v9, s[60:63], s86 offen offset:1024 sc1\nMvaa 240,176,60\nTa 220,27,32768\nTa 222,27,33792\nMvaa 240,144,56\nTa 252,27,40960\nMvaa 236,140,56\ns_sub_u32 s96, s96, 1\n" \
"Ta 254,27,41984\nMvaa 236,172,60\nD 19,68,s90,2048\nMvaa 232,168,60\ns_cmp_lg_u32 s96, 0\nMvaa 232,136,56\nMvaa 228,132,56\nMvaa 228,164,60\n" \
"D 19,68,s91,3072\nMvaa 224,160,60\nMvaa 224,128,56\ns_waitcnt vmcnt(21) lgkmcnt(0)\ns_cbranch_scc1 zol\nzox:\ns_cmp_eq_u32 s23, 0\ns_cbranch_scc1 zot\n" \
"s_cmp_eq_u32 s23, 1\ns_cbranch_scc1 zof\ns_cmp_eq_u32 s23, 3\ns_cbranch_scc1 zo1\ns_branch zos\ns_branch 99f\n.p2align 12\nzqR:\n" \
"zp16:\ns_setpc_b64 s[98:99]\n99:\nzof:\nMvaa 128,192,0\nMvaa 128,224,4\nbuffer_load_dwordx4 a[56:59], v9, s[60:63], s87 offen offset:0 sc1\nMvaa 132,228,4\n" \
"Mvaa 132,196,0\nMvaa 136,200,0\ns_waitcnt vmcnt(26)\ns_barrier\nMvaa 136,232,4\nbuffer_load_dwordx4 a[60:63], v9, s[60:63], s87 offen offset:1024 sc1\nMvaa 140,236,4\nTa 128,20,49152\n" \
"Ta 130,20,50176\nMvaa 140,204,0\nTa 160,20,57344\nTa 162,20,58368\nMvaa 144,208,0\nTa 132,21,49152\nMvaa 144,240,4\nTa 134,21,50176\n" \
"Ta 164,21,57344\nMvaa 148,244,4\nTa 166,21,58368\nMvaa 148,212,0\nTa 136,22,49152\nTa 138,22,50176\nMvaa 152,216,0\nTa 168,22,57344\n" \
"Mvaa 152,248,4\nTa 170,22,58368\nTa 140,23,49152\nMvaa 156,252,4\nTa 142,23,50176\nMvaa 156,220,0\nTa 172,23,57344\nTa 174,23,58368\n" \
"s_waitcnt vmcnt(15)\nMvaa 160,192,8\nTa 144,24,49152\nMvaa 160,224,12\nTa 146,24,50176\nTa 176,24,57344\nMvaa 164,228,12\nTa 178,24,58368\n" \
"Mvaa 164,196,8\nTa 148,25,49152\nTa 150,25,50176\nMvaa 168,200,8\nTa 180,25,57344\nMvaa 168,232,12\nTa 182,25,58368\nTa 152,26,49152\n" \
"Mvaa 172,236,12\nTa 154,26,50176\nMvaa 172,204,8\nTa 184,26,57344\nTa 186,26,58368\nMvaa 176,208,8\nTa 156,27,49152\nMvaa 176,240,12\n" \
"Ta 158,27,50176\nTa 188,27,57344\nMvaa 180,244,12\nTa 190,27,58368\nMvaa 180,212,8\nMvaa 184,216,8\nMvaa 184,248,12\nMvaa 188,252,12\n" \
"Mvaa 188,220,8\ns_waitcnt vmcnt(8) lgkmcnt(0)\nMvaa 128,128,32\nMvaa 128,160,36\nMvaa 132,164,36\nMvaa 132,132,32\nMvaa 136,136,32\nMvaa 136,168,36\n" \
"Mvaa 140,172,36\nMvaa 140,140,32\nMvaa 144,144,32\nMvaa 144,176,36\nMvaa 148,180,36\nMvaa 148,148,32\nMvaa 152,152,32\nMvaa 152,184,36\n" \
"Mvaa 156,188,36\nMvaa 156,156,32\ns_waitcnt vmcnt(5)\nMvaa 160,128,40\nMvaa 160,160,44\nMvaa 164,164,44\nMvaa 164,132,40\nC 32,128,129\n" \
"C 33,130,131\nMvaa 168,136,40\nC 34,132,133\nC 35,134,135\nMvaa 168,168,44\nC 36,136,137\nC 37,138,139\nMvaa 172,172,44\n" \
"C 38,140,141\nC 39,142,143\nMvaa 172,140,40\nC 40,144,145\nC 41,146,147\nMvaa 176,144,40\nC 42,148,149\nC 43,150,151\n" \
"Mvaa 176,176,44\nC 44,152,153\nC 45,154,155\nMvaa 180,180,44\nC 46,156,157\nC 47,158,159\nMvaa 180,148,40\ns_add_u32 s95, s24, 0\n" \
"Mvaa 184,152,40\nbuffer_store_dwordx4 v[32:35], v0, s[60:63], s95 offen offset:0 sc1\nMvaa 184,184,44\nMvaa 188,188,44\nbuffer_store_dwordx4 v[36:39], v0, s[60:63], s95 offen offset:1024 sc1\nMvaa 188,156,40\ns_waitcnt vmcnt(5)\nMvaa 192,192,16\n" \
"s_add_u32 s95, s25, 0\nMvaa 192,224,20\nbuffer_store_dwordx4 v[40:43], v0, s[60:63], s95 offen offset:0 sc1\nMvaa 192,128,48\nMvaa 192,160,52\nbuffer_store_dwordx4 v[44:47], v0, s[60:63], s95 offen offset:1024 sc1\nMvaa 196,228,20\nMvaa 196,196,16\n" \
"C 48,160,161\nC 49,162,163\nMvaa 196,164,52\nC 50,164,165\nC 51,166,167\nMvaa 196,132,48\nC 52,168,169\nC 53,170,171\n" \
"Mvaa 200,200,16\nC 54,172,173\nC 55,174,175\nMvaa 200,232,20\nC 56,176,177\nC 57,178,179\nMvaa 200,136,48\nC 58,180,181\n" \
"C 59,182,183\nMvaa 200,168,52\nC 60,184,185\nC 61,186,187\nMvaa 204,236,20\nC 62,188,189\nC 63,190,191\nMvaa 204,204,16\n" \
"s_add_u32 s95, s24, 2048\nMvaa 204,172,52\nbuffer_store_dwordx4 v[48:51], v0, s[60:63], s95 offen offset:0 sc1\nMvaa 204,140,48\nMvaa 208,208,16\nbuffer_store_dwordx4 v[52:55], v0, s[60:63], s95 offen offset:1024 sc1\nMvaa 208,240,20\nMvaa 208,144,48\n" \
"s_add_u32 s95, s25, 2048\nMvaa 208,176,52\nbuffer_store_dwordx4 v[56:59], v0, s[60:63], s95 offen offset:0 sc1\nMvaa 212,244,20\nMvaa 212,212,16\nbuffer_store_dwordx4 v[60:63], v0, s[60:63], s95 offen offset:1024 sc1\nMvaa 212,180,52\nMvaa 212,148,48\n" \
"Mvaa 216,216,16\nMvaa 216,248,20\nMvaa 216,152,48\nMvaa 216,184,52\nMvaa 220,252,20\nMvaa 220,220,16\nMvaa 220,188,52\nMvaa 220,156,48\n" \
"s_waitcnt vmcnt(8)\nMvaa 224,192,24\nMvaa 224,224,28\nMvaa 224,128,56\nMvaa 224,160,60\nC 64,192,193\nC 65,194,195\nMvaa 228,228,28\n" \
"C 66,196,197\nC 67,198,199\nMvaa 228,196,24\nC 68,200,201\nC 69,202,203\nMvaa 228,164,60\nC 70,204,205\nC 71,206,207\n" \
"Mvaa 228,132,56\nC 72,208,209\nC 73,210,211\nMvaa 232,200,24\nC 74,212,213\nC 75,214,215\nMvaa 232,232,28\nC 76,216,217\n" \
"C 77,218,219\nMvaa 232,136,56\nC 78,220,221\nC 79,222,223\nMvaa 232,168,60\ns_add_u32 s95, s24, 4096\nMvaa 236,236,28\nbuffer_store_dwordx4 v[64:67], v0, s[60:63], s95 offen offset:0 sc1\n" \
"Mvaa 236,204,24\nMvaa 236,172,60\nbuffer_store_dwordx4 v[68:71], v0, s[60:63], s95 offen offset:1024 sc1\nMvaa 236,140,56\nMvaa 240,208,24\ns_add_u32 s95, s25, 4096\nMvaa 240,240,28\nbuffer_store_dwordx4 v[72:75], v0, s[60:63], s95 offen offset:0 sc1\n" \
"Mvaa 240,144,56\nMvaa 240,176,60\nbuffer_store_dwordx4 v[76:79], v0, s[60:63], s95 offen offset:1024 sc1\nMvaa 244,244,28\nMvaa 244,212,24\nMvaa 244,180,60\nMvaa 244,148,56\nMvaa 248,216,24\n" \
"Mvaa 248,248,28\nMvaa 248,152,56\nMvaa 248,184,60\nMvaa 252,252,28\nMvaa 252,220,24\nMvaa 252,188,60\nMvaa 252,156,56\ns_nop 4\n" \
"C 80,224,225\nC 81,226,227\nC 82,228,229\nC 83,230,231\nC 84,232,233\nC 85,234,235\nC 86,236,237\nC 87,238,239\n" \
"C 88,240,241\nC 89,242,243\nC 90,244,245\nC 91,246,247\nC 92,248,249\nC 93,250,251\nC 94,252,253\nC 95,254,255\n" \
"s_add_u32 s95, s24, 6144\nbuffer_store_dwordx4 v[80:83], v0, s[60:63], s95 offen offset:0 sc1\nbuffer_store_dwordx4 v[84:87], v0, s[60:63], s95 offen offset:1024 sc1\ns_add_u32 s95, s25, 6144\nbuffer_store_dwordx4 v[88:91], v0, s[60:63], s95 offen offset:0 sc1\nbuffer_store_dwordx4 v[92:95], v0, s[60:63], s95 offen offset:1024 sc1\ns_waitcnt vmcnt(0)\nbuffer_store_dwordx2 v[2:3], off, s[60:63], s27 sc1\n" \
"s_branch zfin\ns_branch 99f\n.p2align 12\nzqS:\nzp17:\ns_setpc_b64 s[98:99]\n99:\nzos:\n" \
"C 32,128,129\nC 33,130,131\nC 34,132,133\nC 35,134,135\nC 36,136,137\nC 37,138,139\nC 38,140,141\nC 39,142,143\n" \
"C 40,144,145\nC 41,146,147\nC 42,148,149\nC 43,150,151\nC 44,152,153\nC 45,154,155\nC 46,156,157\nC 47,158,159\n" \
"Zvaa 128,192,0\ns_waitcnt vmcnt(10)\nv_cmp_eq_u64 vcc, v[10:11], v[2:3]\ns_mov_b32 s79, s78\nMvaa 128,224,4\nbuffer_load_dwordx4 a[56:59], v9, s[60:63], s87 offen offset:0 sc1\ns_xor_b32 s82, s79, s10\nZvaa 132,228,4\n" \
"s_lshl_b32 s81, s82, 16\ns_cmp_lt_u32 s79, 32\ns_cselect_b32 s82, s18, s19\nMvaa 132,196,0\ns_add_u32 s81, s81, s82\ns_bitcmp1_b64 vcc, s79\nC 48,160,161\nZvaa 136,200,0\n" \
"C 49,162,163\nC 50,164,165\nC 51,166,167\nMvaa 136,232,4\nbuffer_load_dwordx4 a[60:63], v9, s[60:63], s87 offen offset:1024 sc1\nC 52,168,169\nZvaa 140,236,4\nC 53,170,171\n" \
"C 54,172,173\nC 55,174,175\nMvaa 140,204,0\nv_add_u32 v9, s81, v0\nC 56,176,177\nC 57,178,179\nZvaa 144,208,0\nC 58,180,181\n" \
"C 59,182,183\nC 60,184,185\nMvaa 144,240,4\nC 61,186,187\nC 62,188,189\nC 63,190,191\nZvaa 148,244,4\ns_cbranch_scc0 zs108\n" \
"zr108:\ns_add_u32 s95, s24, 0\nbuffer_store_dwordx4 v[32:35], v0, s[60:63], s95 offen offset:0 sc1\nMvaa 148,212,0\ns_ff1_i32_b64 s78, s[8:9]\ns_bitset0_b64 s[8:9], s78\nZvaa 152,216,0\ns_xor_b32 s82, s78, s10\n" \
"s_and_b32 s80, s82, 31\nMvaa 152,248,4\ns_lshl_b32 s80, s80, 18\ns_cmp_lt_u32 s78, 32\nZvaa 156,252,4\ns_cselect_b64 s[68:69], s[14:15], s[12:13]\ns_cselect_b32 s70, 0x800000, s92\nMvaa 156,220,0\n" \
"v_add_u32 v18, s80, v16\nv_xor_b32 v19, 128, v18\ns_waitcnt vmcnt(16)\nZvaa 188,220,8\nC 64,192,193\nC 65,194,195\nC 66,196,197\nMvaa 188,252,12\n" \
"buffer_load_dwordx4 a[0:3], v9, s[60:63], 0 offen offset:0 sc1\nC 67,198,199\nZvaa 184,248,12\nC 68,200,201\nC 69,202,203\nC 70,204,205\nMvaa 184,216,8\nC 71,206,207\n" \
"C 72,208,209\nC 73,210,211\nZvaa 180,212,8\nC 74,212,213\nC 75,214,215\nC 76,216,217\nMvaa 180,244,12\nbuffer_load_dwordx4 a[4:7], v9, s[60:63], 0 offen offset:1024 sc1\n" \
"C 77,218,219\nZvaa 176,240,12\nC 78,220,221\nC 79,222,223\nMvaa 176,208,8\nZvaa 172,204,8\nbuffer_store_dwordx4 v[36:39], v0, s[60:63], s95 offen offset:1024 sc1\nMvaa 172,236,12\n" \
"Zvaa 168,232,12\ns_waitcnt vmcnt(31)\ns_barrier\nMvaa 168,200,8\nTa 128,20,49152\nTa 130,20,50176\nZvaa 164,196,8\nTa 160,20,57344\n" \
"s_add_u32 s95, s25, 0\nbuffer_store_dwordx4 v[40:43], v0, s[60:63], s95 offen offset:0 sc1\nMvaa 164,228,12\nTa 162,20,58368\nTa 132,21,49152\nZvaa 160,224,12\nTa 134,21,50176\nMvaa 160,192,8\n" \
"s_waitcnt vmcnt(17)\nTa 164,21,57344\nZvaa 192,192,16\nTa 166,21,58368\nC 80,224,225\nC 81,226,227\nMvaa 192,224,20\nbuffer_load_dwordx4 a[8:11], v9, s[60:63], s85 offen offset:0 sc1\n" \
"C 82,228,229\nZvaa 196,228,20\nTa 136,22,49152\nC 83,230,231\nC 84,232,233\nMvaa 196,196,16\nTa 138,22,50176\nTa 168,22,57344\n" \
"C 85,234,235\nZvaa 200,200,16\nTa 170,22,58368\nC 86,236,237\nC 87,238,239\nMvaa 200,232,20\nbuffer_load_dwordx4 a[12:15], v9, s[60:63], s85 offen offset:1024 sc1\nC 88,240,241\n" \
"Zvaa 204,236,20\nTa 140,23,49152\nC 89,242,243\nC 90,244,245\nMvaa 204,204,16\nTa 142,23,50176\nTa 172,23,57344\nC 91,246,247\n" \
"Zvaa 208,208,16\ns_add_u32 m0, s84, 0\nC 92,248,249\nC 93,250,251\nMvaa 208,240,20\nD 18,68,0,0\nC 94,252,253\nZvaa 212,244,20\n" \
"Ta 174,23,58368\nTa 144,24,49152\nC 95,254,255\nMvaa 212,212,16\nTa 146,24,50176\nZvaa 216,216,16\nTa 176,24,57344\nTa 178,24,58368\n" \
"Mvaa 216,248,20\nD 18,68,s89,1024\nbuffer_store_dwordx4 v[44:47], v0, s[60:63], s95 offen offset:1024 sc1\nZvaa 220,252,20\nTa 148,25,49152\nMvaa 220,220,16\ns_waitcnt vmcnt(20)\nTa 150,25,50176\n" \
"Zvaa 252,220,24\nTa 180,25,57344\nMvaa 252,252,28\nbuffer_load_dwordx4 a[16:19], v9, s[60:63], s86 offen offset:0 sc1\nZvaa 248,248,28\nTa 182,25,58368\nMvaa 248,216,24\nTa 152,26,49152\n" \
"Ta 154,26,50176\nZvaa 244,212,24\nTa 184,26,57344\nMvaa 244,244,28\nbuffer_load_dwordx4 a[20:23], v9, s[60:63], s86 offen offset:1024 sc1\ns_add_u32 s95, s24, 2048\nbuffer_store_dwordx4 v[48:51], v0, s[60:63], s95 offen offset:0 sc1\nZvaa 240,240,28\n" \
"Ta 186,26,58368\nMvaa 240,208,24\nTa 156,27,49152\nTa 158,27,50176\nZvaa 236,204,24\nTa 188,27,57344\nMvaa 236,236,28\nD 19,68,s90,2048\n" \
"Zvaa 232,232,28\nTa 190,27,58368\nMvaa 232,200,24\nZvaa 228,196,24\nMvaa 228,228,28\nD 19,68,s91,3072\nZvaa 224,224,28\nv_add_u32 v18, 131072, v18\n" \
"Mvaa 224,192,24\nv_add_u32 v19, 131072, v19\ns_waitcnt vmcnt(23) lgkmcnt(0)\nMvaa 128,128,32\nbuffer_store_dwordx4 v[52:55], v0, s[60:63], s95 offen offset:1024 sc1\nMvaa 128,160,36\nbuffer_load_dwordx4 a[24:27], v9, s[60:63], s87 offen offset:0 sc1\nMvaa 132,164,36\n" \
"Mvaa 132,132,32\nMvaa 136,136,32\nMvaa 136,168,36\nbuffer_load_dwordx4 a[28:31], v9, s[60:63], s87 offen offset:1024 sc1\nMvaa 140,172,36\nMvaa 140,140,32\nv_add_u32 v9, 0x8000, v9\nMvaa 144,144,32\n" \
"v_xor_b32 v20, 0x8000, v20\nv_xor_b32 v21, 0x8000, v21\nMvaa 144,176,36\nv_xor_b32 v22, 0x8000, v22\nv_xor_b32 v23, 0x8000, v23\nMvaa 148,180,36\nv_xor_b32 v24, 0x8000, v24\nv_xor_b32 v25, 0x8000, v25\n" \
"Mvaa 148,148,32\nbuffer_load_dwordx2 v[10:11], v1, s[60:63], 0 offen sc1\nMvaa 152,152,32\nv_xor_b32 v26, 0x8000, v26\nv_xor_b32 v27, 0x8000, v27\nMvaa 152,184,36\nMvaa 156,188,36\ns_add_u32 s95, s25, 2048\n" \
"buffer_store_dwordx4 v[56:59], v0, s[60:63], s95 offen offset:0 sc1\nMvaa 156,156,32\ns_waitcnt vmcnt(25)\nMvaa 188,156,40\nMvaa 188,188,44\nbuffer_load_dwordx4 a[32:35], v9, s[60:63], 0 offen offset:0 sc1\nMvaa 184,184,44\nMvaa 184,152,40\n" \
"Mvaa 180,148,40\nMvaa 180,180,44\nbuffer_load_dwordx4 a[36:39], v9, s[60:63], 0 offen offset:1024 sc1\nMvaa 176,176,44\nMvaa 176,144,40\nMvaa 172,140,40\nbuffer_store_dwordx4 v[60:63], v0, s[60:63], s95 offen offset:1024 sc1\nMvaa 172,172,44\n" \
"Mvaa 168,168,44\ns_waitcnt vmcnt(38)\ns_barrier\nMvaa 168,136,40\nTa 192,20,32768\nTa 194,20,33792\nMvaa 164,132,40\nTa 224,20,40960\n" \
"Mvaa 164,164,44\nTa 226,20,41984\nTa 196,21,32768\nMvaa 160,160,44\nTa 198,21,33792\nMvaa 160,128,40\ns_waitcnt vmcnt(26)\nTa 228,21,40960\n" \
"Mvaa 192,128,48\nTa 230,21,41984\nMvaa 192,160,52\nbuffer_load_dwordx4 a[40:43], v9, s[60:63], s85 offen offset:0 sc1\nMvaa 196,164,52\nTa 200,22,32768\nTa 202,22,33792\nMvaa 196,132,48\n" \
"Ta 232,22,40960\nMvaa 200,136,48\nTa 234,22,41984\nMvaa 200,168,52\nbuffer_load_dwordx4 a[44:47], v9, s[60:63], s85 offen offset:1024 sc1\ns_add_u32 s95, s24, 4096\nbuffer_store_dwordx4 v[64:67], v0, s[60:63], s95 offen offset:0 sc1\nMvaa 204,172,52\n" \
"Ta 204,23,32768\nTa 206,23,33792\nMvaa 204,140,48\nTa 236,23,40960\nMvaa 208,144,48\ns_add_u32 m0, s84, 16384\nTa 238,23,41984\nMvaa 208,176,52\n" \
"D 18,68,0,0\nMvaa 212,180,52\ns_xor_b32 s84, s84, 0x38000\nMvaa 212,148,48\nTa 208,24,32768\nTa 210,24,33792\nMvaa 216,152,48\nTa 240,24,40960\n" \
"Mvaa 216,184,52\nD 18,68,s89,1024\nMvaa 220,188,52\nTa 242,24,41984\nTa 212,25,32768\nMvaa 220,156,48\ns_waitcnt vmcnt(28)\nMvaa 252,156,56\n" \
"Ta 214,25,33792\nTa 244,25,40960\nMvaa 252,188,60\nbuffer_load_dwordx4 a[48:51], v9, s[60:63], s86 offen offset:0 sc1\nbuffer_store_dwordx4 v[68:71], v0, s[60:63], s95 offen offset:1024 sc1\nMvaa 248,184,60\nTa 246,25,41984\nMvaa 248,152,56\n" \
"Ta 216,26,32768\nTa 218,26,33792\nMvaa 244,148,56\nTa 248,26,40960\nMvaa 244,180,60\nbuffer_load_dwordx4 a[52:55], v9, s[60:63], s86 offen offset:1024 sc1\nMvaa 240,176,60\nTa 250,26,41984\n" \
"Mvaa 240,144,56\nTa 220,27,32768\nTa 222,27,33792\nMvaa 236,140,56\nTa 252,27,40960\nMvaa 236,172,60\nD 19,68,s90,2048\nMvaa 232,168,60\n" \
"Ta 254,27,41984\nMvaa 232,136,56\ns_add_u32 s95, s25, 4096\nbuffer_store_dwordx4 v[72:75], v0, s[60:63], s95 offen offset:0 sc1\nMvaa 228,132,56\nMvaa 228,164,60\nD 19,68,s91,3072\nMvaa 224,160,60\n" \
"Mvaa 224,128,56\ns_waitcnt vmcnt(31) lgkmcnt(0)\nMvaa 128,192,0\ns_waitcnt vmcnt(15)\nv_cmp_eq_u64 vcc, v[10:11], v[2:3]\ns_mov_b32 s79, s78\nMvaa 128,224,4\nbuffer_load_dwordx4 a[56:59], v9, s[60:63], s87 offen offset:0 sc1\n" \
"s_xor_b32 s82, s79, s10\nMvaa 132,228,4\ns_lshl_b32 s81, s82, 16\ns_cmp_lt_u32 s79, 32\ns_cselect_b32 s82, s18, s19\nMvaa 132,196,0\ns_add_u32 s81, s81, s82\ns_bitcmp1_b64 vcc, s79\n" \
"Mvaa 136,200,0\nMvaa 136,232,4\nbuffer_load_dwordx4 a[60:63], v9, s[60:63], s87 offen offset:1024 sc1\nMvaa 140,236,4\nMvaa 140,204,0\nv_add_u32 v9, s81, v0\nMvaa 144,208,0\nbuffer_store_dwordx4 v[76:79], v0, s[60:63], s95 offen offset:1024 sc1\n" \
"Mvaa 144,240,4\nMvaa 148,244,4\ns_cbranch_scc0 zs109\nzr109:\nMvaa 148,212,0\ns_ff1_i32_b64 s78, s[8:9]\ns_bitset0_b64 s[8:9], s78\nMvaa 152,216,0\n" \
"s_xor_b32 s82, s78, s10\ns_and_b32 s80, s82, 31\nMvaa 152,248,4\ns_lshl_b32 s80, s80, 18\ns_cmp_lt_u32 s78, 32\nMvaa 156,252,4\ns_cselect_b64 s[68:69], s[14:15], s[12:13]\ns_cselect_b32 s70, 0x800000, s92\n" \
"Mvaa 156,220,0\nv_add_u32 v18, s80, v16\nv_xor_b32 v19, 128, v18\ns_waitcnt vmcnt(30)\nMvaa 188,220,8\nMvaa 188,252,12\nbuffer_load_dwordx4 a[0:3], v9, s[60:63], 0 offen offset:0 sc1\nMvaa 184,248,12\n" \
"Mvaa 184,216,8\nMvaa 180,212,8\nMvaa 180,244,12\nbuffer_load_dwordx4 a[4:7], v9, s[60:63], 0 offen offset:1024 sc1\nMvaa 176,240,12\nMvaa 176,208,8\nMvaa 172,204,8\ns_add_u32 s95, s24, 6144\n" \
"buffer_store_dwordx4 v[80:83], v0, s[60:63], s95 offen offset:0 sc1\nMvaa 172,236,12\nMvaa 168,232,12\ns_waitcnt vmcnt(42)\ns_barrier\nMvaa 168,200,8\nTa 128,20,49152\nTa 130,20,50176\n" \
"Mvaa 164,196,8\nTa 160,20,57344\nMvaa 164,228,12\nTa 162,20,58368\nTa 132,21,49152\nMvaa 160,224,12\nTa 134,21,50176\nMvaa 160,192,8\n" \
"s_waitcnt vmcnt(28)\nTa 164,21,57344\nMvaa 192,192,16\nTa 166,21,58368\nMvaa 192,224,20\nbuffer_load_dwordx4 a[8:11], v9, s[60:63], s85 offen offset:0 sc1\nbuffer_store_dwordx4 v[84:87], v0, s[60:63], s95 offen offset:1024 sc1\nMvaa 196,228,20\n" \
"Ta 136,22,49152\nMvaa 196,196,16\nTa 138,22,50176\nTa 168,22,57344\nMvaa 200,200,16\nTa 170,22,58368\nMvaa 200,232,20\nbuffer_load_dwordx4 a[12:15], v9, s[60:63], s85 offen offset:1024 sc1\n" \
"Mvaa 204,236,20\nTa 140,23,49152\nMvaa 204,204,16\nTa 142,23,50176\nTa 172,23,57344\nMvaa 208,208,16\ns_add_u32 m0, s84, 0\nMvaa 208,240,20\n" \
"D 18,68,0,0\nMvaa 212,244,20\nTa 174,23,58368\nTa 144,24,49152\nMvaa 212,212,16\nTa 146,24,50176\nMvaa 216,216,16\nTa 176,24,57344\n" \
"Ta 178,24,58368\nMvaa 216,248,20\nD 18,68,s89,1024\ns_add_u32 s95, s25, 6144\nbuffer_store_dwordx4 v[88:91], v0, s[60:63], s95 offen offset:0 sc1\nMvaa 220,252,20\nTa 148,25,49152\nMvaa 220,220,16\n" \
"s_waitcnt vmcnt(28)\nTa 150,25,50176\nMvaa 252,220,24\nTa 180,25,57344\nMvaa 252,252,28\nbuffer_load_dwordx4 a[16:19], v9, s[60:63], s86 offen offset:0 sc1\nMvaa 248,248,28\nTa 182,25,58368\n" \
"Mvaa 248,216,24\nTa 152,26,49152\nTa 154,26,50176\nMvaa 244,212,24\nTa 184,26,57344\nMvaa 244,244,28\nbuffer_load_dwordx4 a[20:23], v9, s[60:63], s86 offen offset:1024 sc1\nMvaa 240,240,28\n" \
"Ta 186,26,58368\nMvaa 240,208,24\nTa 156,27,49152\nTa 158,27,50176\nMvaa 236,204,24\nTa 188,27,57344\nMvaa 236,236,28\nD 19,68,s90,2048\n" \
"buffer_store_dwordx4 v[92:95], v0, s[60:63], s95 offen offset:1024 sc1\nMvaa 232,232,28\nTa 190,27,58368\nMvaa 232,200,24\nMvaa 228,196,24\nMvaa 228,228,28\nD 19,68,s91,3072\nMvaa 224,224,28\n" \
"v_add_u32 v18, 131072, v18\nMvaa 224,192,24\nv_add_u32 v19, 131072, v19\ns_waitcnt vmcnt(29) lgkmcnt(0)\nMvaa 128,128,32\nMvaa 128,160,36\nbuffer_load_dwordx4 a[24:27], v9, s[60:63], s87 offen offset:0 sc1\nMvaa 132,164,36\n" \
"Mvaa 132,132,32\nMvaa 136,136,32\nMvaa 136,168,36\nbuffer_load_dwordx4 a[28:31], v9, s[60:63], s87 offen offset:1024 sc1\nMvaa 140,172,36\nMvaa 140,140,32\nv_add_u32 v9, 0x8000, v9\nMvaa 144,144,32\n" \
"v_xor_b32 v20, 0x8000, v20\nv_xor_b32 v21, 0x8000, v21\nMvaa 144,176,36\nv_xor_b32 v22, 0x8000, v22\nv_xor_b32 v23, 0x8000, v23\nMvaa 148,180,36\nv_xor_b32 v24, 0x8000, v24\nv_xor_b32 v25, 0x8000, v25\n" \
"Mvaa 148,148,32\nbuffer_load_dwordx2 v[10:11], v1, s[60:63], 0 offen sc1\nMvaa 152,152,32\nv_xor_b32 v26, 0x8000, v26\nv_xor_b32 v27, 0x8000, v27\nMvaa 152,184,36\nMvaa 156,188,36\nMvaa 156,156,32\n" \
"s_waitcnt vmcnt(29)\nMvaa 188,156,40\nMvaa 188,188,44\nbuffer_load_dwordx4 a[32:35], v9, s[60:63], 0 offen offset:0 sc1\nMvaa 184,184,44\nMvaa 184,152,40\nMvaa 180,148,40\nMvaa 180,180,44\n" \
"buffer_load_dwordx4 a[36:39], v9, s[60:63], 0 offen offset:1024 sc1\nMvaa 176,176,44\nMvaa 176,144,40\nMvaa 172,140,40\nMvaa 172,172,44\nMvaa 168,168,44\ns_waitcnt vmcnt(41)\ns_barrier\n" \
"Mvaa 168,136,40\nTa 192,20,32768\nTa 194,20,33792\nMvaa 164,132,40\nTa 224,20,40960\nMvaa 164,164,44\nTa 226,20,41984\nTa 196,21,32768\n" \
"s_branch 99f\n.p2align 12\nzp18:\ns_setpc_b64 s[98:99]\n99:\nMvaa 160,160,44\nTa 198,21,33792\nMvaa 160,128,40\n" \
"s_waitcnt vmcnt(25)\nTa 228,21,40960\nMvaa 192,128,48\nTa 230,21,41984\nMvaa 192,160,52\nbuffer_load_dwordx4 a[40:43], v9, s[60:63], s85 offen offset:0 sc1\nMvaa 196,164,52\nTa 200,22,32768\n" \
"Ta 202,22,33792\nMvaa 196,132,48\nTa 232,22,40960\nMvaa 200,136,48\nTa 234,22,41984\nMvaa 200,168,52\nbuffer_load_dwordx4 a[44:47], v9, s[60:63], s85 offen offset:1024 sc1\nMvaa 204,172,52\n" \
"Ta 204,23,32768\nTa 206,23,33792\nMvaa 204,140,48\nTa 236,23,40960\nMvaa 208,144,48\ns_add_u32 m0, s84, 16384\nTa 238,23,41984\nMvaa 208,176,52\n" \
"D 18,68,0,0\nMvaa 212,180,52\ns_xor_b32 s84, s84, 0x38000\nMvaa 212,148,48\nTa 208,24,32768\nTa 210,24,33792\nMvaa 216,152,48\nTa 240,24,40960\n" \
"Mvaa 216,184,52\nD 18,68,s89,1024\nMvaa 220,188,52\nTa 242,24,41984\nTa 212,25,32768\nMvaa 220,156,48\ns_waitcnt vmcnt(24)\nMvaa 252,156,56\n" \
"Ta 214,25,33792\nTa 244,25,40960\nMvaa 252,188,60\nbuffer_load_dwordx4 a[48:51], v9, s[60:63], s86 offen offset:0 sc1\nMvaa 248,184,60\nTa 246,25,41984\ns_mov_b32 s96, 5\ns_mov_b32 s23, 3\n" \
"Mvaa 248,152,56\nTa 216,26,32768\nTa 218,26,33792\nMvaa 244,148,56\nTa 248,26,40960\nMvaa 244,180,60\nbuffer_load_dwordx4 a[52:55], v9, s[60:63], s86 offen offset:1024 sc1\nMvaa 240,176,60\n" \
"Ta 250,26,41984\nMvaa 240,144,56\nTa 220,27,32768\nTa 222,27,33792\nMvaa 236,140,56\nTa 252,27,40960\nMvaa 236,172,60\nD 19,68,s90,2048\n" \
"Mvaa 232,168,60\nTa 254,27,41984\nMvaa 232,136,56\ns_waitcnt vmcnt(13)\nbuffer_store_dwordx2 v[2:3], off, s[60:63], s27 sc1\nMvaa 228,132,56\nMvaa 228,164,60\nD 19,68,s91,3072\n" \
"Mvaa 224,160,60\nMvaa 224,128,56\ns_waitcnt vmcnt(26) lgkmcnt(0)\ns_branch zol\nzo1:\nMvaa 128,192,0\nMvaa 128,224,4\nbuffer_load_dwordx4 a[56:59], v9, s[60:63], s87 offen offset:0 sc1\n" \
"Mvaa 132,228,4\nMvaa 132,196,0\nMvaa 136,200,0\ns_waitcnt vmcnt(26)\ns_barrier\nMvaa 136,232,4\nbuffer_load_dwordx4 a[60:63], v9, s[60:63], s87 offen offset:1024 sc1\nMvaa 140,236,4\n" \
"Ta 128,20,49152\nTa 130,20,50176\nMvaa 140,204,0\nTa 160,20,57344\nTa 162,20,58368\nMvaa 144,208,0\nTa 132,21,49152\nMvaa 144,240,4\n" \
"Ta 134,21,50176\nTa 164,21,57344\nMvaa 148,244,4\nTa 166,21,58368\nMvaa 148,212,0\nTa 136,22,49152\nTa 138,22,50176\nMvaa 152,216,0\n" \
"Ta 168,22,57344\nMvaa 152,248,4\nTa 170,22,58368\nTa 140,23,49152\nMvaa 156,252,4\nTa 142,23,50176\nMvaa 156,220,0\nTa 172,23,57344\n" \
"Ta 174,23,58368\ns_waitcnt vmcnt(21)\nMvaa 160,192,8\nTa 144,24,49152\nMvaa 160,224,12\nTa 146,24,50176\nTa 176,24,57344\nMvaa 164,228,12\n" \
"Ta 178,24,58368\nMvaa 164,196,8\nTa 148,25,49152\nTa 150,25,50176\nMvaa 168,200,8\nTa 180,25,57344\nMvaa 168,232,12\nTa 182,25,58368\n" \
"Ta 152,26,49152\nMvaa 172,236,12\nTa 154,26,50176\nMvaa 172,204,8\nTa 184,26,57344\nTa 186,26,58368\nMvaa 176,208,8\nTa 156,27,49152\n" \
"Mvaa 176,240,12\nTa 158,27,50176\nTa 188,27,57344\nMvaa 180,244,12\nTa 190,27,58368\nMvaa 180,212,8\nMvaa 184,216,8\ns_mov_b64 s[64:65], s[16:17]\n" \
"s_mov_b32 s66, 8388608\nMvaa 184,248,12\nv_bfe_u32 v28, v0, 4, 4\nv_lshrrev_b32 v29, 9, v0\nMvaa 188,252,12\nv_and_b32 v30, 7, v28\nv_xor_b32 v29, v29, v30\nMvaa 188,220,8\n" \
"s_waitcnt vmcnt(10) lgkmcnt(0)\ns_lshl_b32 s82, s32, 2\nMvaa 128,128,32\nv_lshrrev_b32 v30, 5, v0\nv_lshlrev_b32 v28, 7, v28\nMvaa 128,160,36\nv_and_b32 v30, 8, v30\nv_lshl_add_u32 v28, v29, 4, v28\n" \
"Mvaa 132,164,36\nv_add3_u32 v12, v28, v30, s82\nv_xor_b32 v13, 32, v12\nMvaa 132,132,32\nv_xor_b32 v14, 64, v12\nv_xor_b32 v15, 96, v12\nMvaa 136,136,32\nv_lshrrev_b32 v28, 7, v0\n" \
"v_lshlrev_b32 v29, 4, v28\nMvaa 136,168,36\nv_xor_b32 v29, v29, v0\nv_add_u32 v5, s82, v29\nMvaa 140,172,36\nv_and_b32 v29, 0x70, v0\nv_lshl_add_u32 v6, v28, 12, v29\nMvaa 140,140,32\n" \
"v_add_u32 v8, 0x8000, v6\nMvaa 144,144,32\nMvaa 144,176,36\nMvaa 148,180,36\nMvaa 148,148,32\nMvaa 152,152,32\nMvaa 152,184,36\nMvaa 156,188,36\n" \
"Mvaa 156,156,32\ns_waitcnt vmcnt(8)\nMvaa 160,128,40\nMvaa 160,160,44\nMvaa 164,164,44\nMvaa 164,132,40\nC 32,128,129\nC 33,130,131\n" \
"Mvaa 168,136,40\nC 34,132,133\nC 35,134,135\nMvaa 168,168,44\nds_write_b64 v12, v[32:33] offset:0\nC 36,136,137\nMvaa 172,172,44\nC 37,138,139\n" \
"ds_write_b64 v13, v[34:35] offset:0\nMvaa 172,140,40\nC 38,140,141\nC 39,142,143\nMvaa 176,144,40\nds_write_b64 v14, v[36:37] offset:0\nC 40,144,145\nMvaa 176,176,44\n" \
"C 41,146,147\nds_write_b64 v15, v[38:39] offset:0\nMvaa 180,180,44\nC 42,148,149\nC 43,150,151\nMvaa 180,148,40\nds_write_b64 v12, v[40:41] offset:2048\nC 44,152,153\n" \
"Mvaa 184,152,40\nC 45,154,155\nds_write_b64 v13, v[42:43] offset:2048\nMvaa 184,184,44\nC 46,156,157\nC 47,158,159\nMvaa 188,188,44\nds_write_b64 v14, v[44:45] offset:2048\n" \
"ds_write_b64 v15, v[46:47] offset:2048\nMvaa 188,156,40\ns_waitcnt vmcnt(4)\ns_add_u32 s95, s29, 0\nMvaa 192,192,16\nMvaa 192,224,20\ns_waitcnt lgkmcnt(0)\nRv 32,5,0\n" \
"Mvaa 192,128,48\nRv 36,5,1024\nRv 40,5,2048\nMvaa 192,160,52\nRv 44,5,3072\nMvaa 196,228,20\nMvaa 196,196,16\nMvaa 196,164,52\n" \
"Mvaa 196,132,48\nMvaa 200,200,16\ns_waitcnt lgkmcnt(0)\nMvaa 200,232,20\nbuffer_store_dwordx4 v[32:35], v6, s[64:67], s95 offen offset:0 sc1\nMvaa 200,136,48\nbuffer_store_dwordx4 v[36:39], v8, s[64:67], s95 offen offset:0 sc1\nMvaa 200,168,52\n" \
"buffer_store_dwordx4 v[40:43], v6, s[64:67], s95 offen offset:128 sc1\nMvaa 204,236,20\nbuffer_store_dwordx4 v[44:47], v8, s[64:67], s95 offen offset:128 sc1\nMvaa 204,204,16\nC 48,160,161\nC 49,162,163\nMvaa 204,172,52\nC 50,164,165\n" \
"C 51,166,167\nMvaa 204,140,48\nds_write_b64 v12, v[48:49] offset:4096\nC 52,168,169\nMvaa 208,208,16\nC 53,170,171\nds_write_b64 v13, v[50:51] offset:4096\nMvaa 208,240,20\n" \
"C 54,172,173\nC 55,174,175\nMvaa 208,144,48\nds_write_b64 v14, v[52:53] offset:4096\nC 56,176,177\nMvaa 208,176,52\nC 57,178,179\nds_write_b64 v15, v[54:55] offset:4096\n" \
"Mvaa 212,244,20\nC 58,180,181\nC 59,182,183\nMvaa 212,212,16\nds_write_b64 v12, v[56:57] offset:6144\nC 60,184,185\nMvaa 212,180,52\nC 61,186,187\n" \
"ds_write_b64 v13, v[58:59] offset:6144\nMvaa 212,148,48\nC 62,188,189\nC 63,190,191\nMvaa 216,216,16\nds_write_b64 v14, v[60:61] offset:6144\nds_write_b64 v15, v[62:63] offset:6144\nMvaa 216,248,20\n" \
"s_add_u32 s95, s29, 262144\nMvaa 216,152,48\nMvaa 216,184,52\ns_waitcnt lgkmcnt(0)\nRv 48,5,4096\nMvaa 220,252,20\nRv 52,5,5120\nRv 56,5,6144\n" \
"Mvaa 220,220,16\nRv 60,5,7168\nMvaa 220,188,52\nMvaa 220,156,48\ns_waitcnt vmcnt(4)\nMvaa 224,192,24\nMvaa 224,224,28\nMvaa 224,128,56\n" \
"s_waitcnt lgkmcnt(0)\nMvaa 224,160,60\nbuffer_store_dwordx4 v[48:51], v6, s[64:67], s95 offen offset:0 sc1\nMvaa 228,228,28\nbuffer_store_dwordx4 v[52:55], v8, s[64:67], s95 offen offset:0 sc1\nMvaa 228,196,24\nbuffer_store_dwordx4 v[56:59], v6, s[64:67], s95 offen offset:128 sc1\nMvaa 228,164,60\n" \
"buffer_store_dwordx4 v[60:63], v8, s[64:67], s95 offen offset:128 sc1\nMvaa 228,132,56\nC 64,192,193\nC 65,194,195\nMvaa 232,200,24\nC 66,196,197\nC 67,198,199\nMvaa 232,232,28\n" \
"ds_write_b64 v12, v[64:65] offset:8192\nC 68,200,201\nMvaa 232,136,56\nC 69,202,203\nds_write_b64 v13, v[66:67] offset:8192\nMvaa 232,168,60\nC 70,204,205\nC 71,206,207\n" \
"Mvaa 236,236,28\nds_write_b64 v14, v[68:69] offset:8192\nC 72,208,209\nMvaa 236,204,24\nC 73,210,211\nds_write_b64 v15, v[70:71] offset:8192\nMvaa 236,172,60\nC 74,212,213\n" \
"C 75,214,215\nMvaa 236,140,56\nds_write_b64 v12, v[72:73] offset:10240\nC 76,216,217\nMvaa 240,208,24\nC 77,218,219\nds_write_b64 v13, v[74:75] offset:10240\nMvaa 240,240,28\n" \
"C 78,220,221\nC 79,222,223\nMvaa 240,144,56\nds_write_b64 v14, v[76:77] offset:10240\nds_write_b64 v15, v[78:79] offset:10240\nMvaa 240,176,60\ns_add_u32 s95, s30, 0\nMvaa 244,244,28\n" \
"Mvaa 244,212,24\ns_waitcnt lgkmcnt(0)\nRv 64,5,8192\nMvaa 244,180,60\nRv 68,5,9216\nRv 72,5,10240\nMvaa 244,148,56\nRv 76,5,11264\n" \
"Mvaa 248,216,24\nMvaa 248,248,28\nMvaa 248,152,56\nMvaa 248,184,60\nMvaa 252,252,28\ns_waitcnt lgkmcnt(0)\nMvaa 252,220,24\nbuffer_store_dwordx4 v[64:67], v6, s[64:67], s95 offen offset:0 sc1\n" \
"Mvaa 252,188,60\nbuffer_store_dwordx4 v[68:71], v8, s[64:67], s95 offen offset:0 sc1\nMvaa 252,156,56\nbuffer_store_dwordx4 v[72:75], v6, s[64:67], s95 offen offset:128 sc1\nbuffer_store_dwordx4 v[76:79], v8, s[64:67], s95 offen offset:128 sc1\ns_nop 4\nC 80,224,225\nC 81,226,227\n" \
"C 82,228,229\nC 83,230,231\nds_write_b64 v12, v[80:81] offset:12288\nC 84,232,233\nC 85,234,235\nds_write_b64 v13, v[82:83] offset:12288\nC 86,236,237\nC 87,238,239\n" \
"ds_write_b64 v14, v[84:85] offset:12288\nC 88,240,241\nC 89,242,243\nds_write_b64 v15, v[86:87] offset:12288\nC 90,244,245\nC 91,246,247\nds_write_b64 v12, v[88:89] offset:14336\nC 92,248,249\n" \
"C 93,250,251\nds_write_b64 v13, v[90:91] offset:14336\nC 94,252,253\nC 95,254,255\nds_write_b64 v14, v[92:93] offset:14336\nds_write_b64 v15, v[94:95] offset:14336\ns_add_u32 s95, s30, 262144\ns_waitcnt lgkmcnt(0)\n" \
"Rv 80,5,12288\nRv 84,5,13312\nRv 88,5,14336\nRv 92,5,15360\ns_waitcnt lgkmcnt(0)\nbuffer_store_dwordx4 v[80:83], v6, s[64:67], s95 offen offset:0 sc1\nbuffer_store_dwordx4 v[84:87], v8, s[64:67], s95 offen offset:0 sc1\nbuffer_store_dwordx4 v[88:91], v6, s[64:67], s95 offen offset:128 sc1\n" \
"buffer_store_dwordx4 v[92:95], v8, s[64:67], s95 offen offset:128 sc1\ns_branch zfin\ns_branch 99f\n.p2align 12\nzqT:\nzp19:\ns_setpc_b64 s[98:99]\n99:\n" \
"zot:\nMvaa 128,192,0\ns_waitcnt vmcnt(10)\nv_cmp_eq_u64 vcc, v[10:11], v[2:3]\ns_mov_b32 s79, s78\nMvaa 128,224,4\nbuffer_load_dwordx4 a[56:59], v9, s[60:63], s87 offen offset:0 sc1\ns_xor_b32 s82, s79, s10\n" \
"Mvaa 132,228,4\ns_lshl_b32 s81, s82, 16\ns_cmp_lt_u32 s79, 32\ns_cselect_b32 s82, s18, s19\nMvaa 132,196,0\ns_add_u32 s81, s81, s82\ns_bitcmp1_b64 vcc, s79\nMvaa 136,200,0\n" \
"Mvaa 136,232,4\nbuffer_load_dwordx4 a[60:63], v9, s[60:63], s87 offen offset:1024 sc1\nMvaa 140,236,4\nMvaa 140,204,0\nv_add_u32 v9, s81, v0\ns_waitcnt vmcnt(15)\ns_waitcnt vmcnt(27)\ns_barrier\n" \
"Mvaa 172,204,8\nTa 128,20,49152\nTa 130,20,50176\nMvaa 172,236,12\nTa 160,20,57344\nMvaa 168,232,12\ns_cbranch_scc0 zs105\nzr105:\n" \
"Mvaa 168,200,8\nbuffer_load_dwordx4 a[64:67], v9, s[60:63], 0 offen offset:0 sc1\nMvaa 164,196,8\nTa 162,20,58368\nMvaa 164,228,12\nTa 132,21,49152\nMvaa 160,224,12\nTa 134,21,50176\n" \
"Ta 164,21,57344\nMvaa 160,192,8\nbuffer_load_dwordx4 a[68:71], v9, s[60:63], 0 offen offset:1024 sc1\ns_waitcnt vmcnt(14)\nMvaa 192,192,16\nTa 166,21,58368\nMvaa 192,224,20\nTa 136,22,49152\n" \
"Mvaa 196,228,20\nTa 138,22,50176\nMvaa 196,196,16\nbuffer_load_dwordx4 a[72:75], v9, s[60:63], s85 offen offset:0 sc1\nMvaa 200,200,16\nTa 168,22,57344\nMvaa 200,232,20\nTa 170,22,58368\n" \
"Mvaa 204,236,20\nTa 140,23,49152\nMvaa 204,204,16\nbuffer_load_dwordx4 a[76:79], v9, s[60:63], s85 offen offset:1024 sc1\ns_waitcnt vmcnt(14)\nMvaa 236,204,24\nTa 142,23,50176\nMvaa 236,236,28\n" \
"Ta 172,23,57344\nMvaa 232,232,28\nTa 174,23,58368\nMvaa 232,200,24\nbuffer_load_dwordx4 a[80:83], v9, s[60:63], s86 offen offset:0 sc1\nMvaa 228,196,24\nMvaa 228,228,28\nMvaa 224,224,28\n" \
"Mvaa 224,192,24\nbuffer_load_dwordx4 a[84:87], v9, s[60:63], s86 offen offset:1024 sc1\ns_waitcnt vmcnt(14) lgkmcnt(0)\nMvaa 128,128,32\nv_xor_b32 v20, 0x8000, v20\nv_xor_b32 v21, 0x8000, v21\nMvaa 128,160,36\nv_xor_b32 v22, 0x8000, v22\n" \
"v_xor_b32 v23, 0x8000, v23\nMvaa 132,164,36\nMvaa 132,132,32\nbuffer_load_dwordx4 a[88:91], v9, s[60:63], s87 offen offset:0 sc1\nMvaa 136,136,32\nMvaa 136,168,36\nMvaa 140,172,36\nMvaa 140,140,32\n" \
"buffer_load_dwordx4 a[92:95], v9, s[60:63], s87 offen offset:1024 sc1\ns_waitcnt vmcnt(13)\ns_waitcnt vmcnt(23)\ns_barrier\nMvaa 172,140,40\nTa 192,20,32768\nTa 194,20,33792\nMvaa 172,172,44\n" \
"Ta 224,20,40960\nMvaa 168,168,44\nTa 226,20,41984\nMvaa 168,136,40\nv_add_u32 v9, 0x8000, v9\nMvaa 164,132,40\nTa 196,21,32768\nMvaa 164,164,44\n" \
"Ta 198,21,33792\nMvaa 160,160,44\nTa 228,21,40960\nMvaa 160,128,40\nbuffer_load_dwordx4 a[96:99], v9, s[60:63], 0 offen offset:0 sc1\ns_waitcnt vmcnt(12)\nMvaa 192,128,48\nTa 230,21,41984\n" \
"Mvaa 192,160,52\nTa 200,22,32768\nMvaa 196,164,52\nTa 202,22,33792\nMvaa 196,132,48\nbuffer_load_dwordx4 a[100:103], v9, s[60:63], 0 offen offset:1024 sc1\nMvaa 200,136,48\nTa 232,22,40960\n" \
"Mvaa 200,168,52\nTa 234,22,41984\nMvaa 204,172,52\nTa 204,23,32768\nMvaa 204,140,48\nbuffer_load_dwordx4 a[104:107], v9, s[60:63], s85 offen offset:0 sc1\ns_waitcnt vmcnt(11)\nMvaa 236,140,56\n" \
"Ta 206,23,33792\nMvaa 236,172,60\nTa 236,23,40960\nMvaa 232,168,60\nTa 238,23,41984\nMvaa 232,136,56\nbuffer_load_dwordx4 a[108:111], v9, s[60:63], s85 offen offset:1024 sc1\nMvaa 228,132,56\n" \
"Mvaa 228,164,60\nMvaa 224,160,60\nMvaa 224,128,56\nbuffer_load_dwordx4 a[112:115], v9, s[60:63], s86 offen offset:0 sc1\ns_waitcnt vmcnt(11) lgkmcnt(0)\nMvaa 128,192,64\nv_bfe_u32 v28, v0, 4, 2\ns_and_b32 s82, s27, -32\n" \
"Mvaa 128,224,68\nv_lshl_add_u32 v7, v28, 3, s82\nv_bfe_u32 v28, v0, 6, 2\nMvaa 132,228,68\nv_lshrrev_b32 v29, 8, v0\nv_bfe_u32 v30, v0, 4, 2\nMvaa 132,196,64\nbuffer_load_dwordx4 a[116:119], v9, s[60:63], s86 offen offset:1024 sc1\n" \
"Mvaa 136,200,64\nv_cmp_eq_u32 vcc, v28, v29\nv_lshlrev_b32 v30, 4, v30\nMvaa 136,232,68\nv_mov_b32 v28, 0x3f80\nv_mov_b32 v29, 0\nMvaa 140,236,68\nv_cndmask_b32 v28, 0, v28, vcc\n" \
"v_lshlrev_b64 v[98:99], v30, v[28:29]\nMvaa 140,204,64\nbuffer_load_dwordx4 a[120:123], v9, s[60:63], s87 offen offset:0 sc1\ns_waitcnt vmcnt(11)\ns_waitcnt vmcnt(17)\ns_barrier\nMvaa 172,204,72\nTa 128,20,49152\n" \
"v_mov_b32 v96, 0\nMvaa 172,236,76\nTa 130,20,50176\nv_mov_b32 v97, 0\nMvaa 168,232,76\nTa 160,20,57344\nv_mov_b32 v100, 0\nMvaa 168,200,72\n" \
"buffer_load_dwordx4 a[124:127], v9, s[60:63], s87 offen offset:1024 sc1\nMvaa 164,196,72\nTa 162,20,58368\nv_mov_b32 v101, 0\nMvaa 164,228,76\nTa 132,21,49152\ns_mov_b64 s[64:65], s[16:17]\nMvaa 160,224,76\n" \
"Ta 134,21,50176\ns_mov_b32 s66, 8388608\nMvaa 160,192,72\ns_waitcnt vmcnt(10)\nTa 164,21,57344\nMvaa 192,192,80\nTa 166,21,58368\nv_bfe_u32 v28, v0, 4, 4\n" \
"Mvaa 192,224,84\nv_lshrrev_b32 v29, 9, v0\nv_and_b32 v30, 7, v28\nMvaa 196,228,84\nTa 136,22,49152\nv_xor_b32 v29, v29, v30\nMvaa 196,196,80\nTa 138,22,50176\n" \
"s_lshl_b32 s82, s32, 2\nMvaa 200,200,80\nTa 168,22,57344\nv_lshrrev_b32 v30, 5, v0\nMvaa 200,232,84\nTa 170,22,58368\nv_lshlrev_b32 v28, 7, v28\nMvaa 204,236,84\n" \
"Ta 140,23,49152\nv_and_b32 v30, 8, v30\nMvaa 204,204,80\ns_waitcnt vmcnt(8)\nTa 142,23,50176\nMvaa 236,204,88\nTa 172,23,57344\nv_lshl_add_u32 v28, v29, 4, v28\n" \
"Mvaa 236,236,92\nTa 174,23,58368\nv_add3_u32 v12, v28, v30, s82\nMvaa 232,232,92\nv_xor_b32 v13, 32, v12\nv_xor_b32 v14, 64, v12\nMvaa 232,200,88\nv_xor_b32 v15, 96, v12\n" \
"v_lshrrev_b32 v28, 7, v0\nMvaa 228,196,88\nv_lshlrev_b32 v29, 4, v28\nv_xor_b32 v29, v29, v0\nMvaa 228,228,92\nv_add_u32 v5, s82, v29\nv_and_b32 v29, 0x70, v0\nMvaa 224,224,92\n" \
"v_lshl_add_u32 v6, v28, 12, v29\nv_add_u32 v8, 0x8000, v6\nMvaa 224,192,88\ns_waitcnt vmcnt(6) lgkmcnt(0)\nMvaa 128,128,96\nMvaa 128,160,100\nMvaa 132,164,100\nMvaa 132,132,96\n" \
"Mvaa 136,136,96\nTa 144,24,49152\nMvaa 136,168,100\nTa 146,24,50176\nMvaa 140,172,100\nTa 176,24,57344\nbuffer_load_dwordx2 v[10:11], v7, s[60:63], 0 offen sc1\nMvaa 140,140,96\n" \
"s_waitcnt vmcnt(5)\nMvaa 172,140,104\nTa 178,24,58368\nMvaa 172,172,108\nTa 148,25,49152\nMvaa 168,168,108\nMvaa 168,136,104\nTa 150,25,50176\n" \
"Mvaa 164,132,104\nTa 180,25,57344\nC 104,128,129\nC 105,130,131\nMvaa 164,164,108\nTa 182,25,58368\nC 106,132,133\nC 107,134,135\n" \
"Mvaa 160,160,108\nC 108,136,137\nC 109,138,139\nC 110,140,141\nMvaa 160,128,104\ns_waitcnt vmcnt(3)\nTa 152,26,49152\nC 111,142,143\n" \
"Mvaa 192,128,112\ns_add_u32 s95, s24, 0\nbuffer_store_dwordx4 v[104:107], v0, s[60:63], s95 offen offset:0 sc1\nMvaa 192,160,116\nTa 154,26,50176\nbuffer_store_dwordx4 v[108:111], v0, s[60:63], s95 offen offset:1024 sc1\nMvaa 196,164,116\nTa 184,26,57344\n" \
"Mvaa 196,132,112\nTa 186,26,58368\nMvaa 200,136,112\nC 112,160,161\nC 113,162,163\nC 114,164,165\nMvaa 200,168,116\nTa 156,27,49152\n" \
"C 115,166,167\nC 116,168,169\nMvaa 204,172,116\nTa 158,27,50176\nC 117,170,171\nC 118,172,173\nMvaa 204,140,112\ns_waitcnt vmcnt(3)\n" \
"C 119,174,175\nMvaa 236,140,120\nTa 188,27,57344\nbuffer_store_dwordx4 v[112:115], v0, s[60:63], s95 offen offset:2048 sc1\nMvaa 236,172,124\nTa 190,27,58368\nbuffer_store_dwordx4 v[116:119], v0, s[60:63], s95 offen offset:3072 sc1\nMvaa 232,168,124\n" \
"Mvaa 232,136,120\nMvaa 228,132,120\nv_xor_b32 v24, 0x8000, v24\nv_xor_b32 v25, 0x8000, v25\nC 104,192,193\nMvaa 228,164,124\nv_xor_b32 v26, 0x8000, v26\nv_xor_b32 v27, 0x8000, v27\n" \
"C 105,194,195\nMvaa 224,160,124\nC 106,196,197\nC 107,198,199\nC 108,200,201\nMvaa 224,128,120\nC 109,202,203\nC 110,204,205\n" \
"C 111,206,207\nMvaa 144,208,0\ns_add_u32 s95, s24, 4096\nbuffer_store_dwordx4 v[104:107], v0, s[60:63], s95 offen offset:0 sc1\nMvaa 144,240,4\nbuffer_store_dwordx4 v[108:111], v0, s[60:63], s95 offen offset:1024 sc1\nMvaa 148,244,4\nMvaa 148,212,0\n" \
"Mvaa 152,216,0\nC 112,224,225\nC 113,226,227\nC 114,228,229\nMvaa 152,248,4\nC 115,230,231\nC 116,232,233\nC 117,234,235\n" \
"Mvaa 156,252,4\nC 118,236,237\nC 119,238,239\nMvaa 156,220,0\nbuffer_store_dwordx4 v[112:115], v0, s[60:63], s95 offen offset:2048 sc1\nMvaa 188,220,8\nbuffer_store_dwordx4 v[116:119], v0, s[60:63], s95 offen offset:3072 sc1\nMvaa 188,252,12\n" \
"s_waitcnt vmcnt(8)\nv_cmp_eq_u64 vcc, v[10:11], v[2:3]\ns_andn2_b32 s82, 10, vcc_lo\nMvaa 184,248,12\nMvaa 184,216,8\nMvaa 180,212,8\ns_cbranch_scc1 zs106\nzr106:\n" \
"Mvaa 180,244,12\ns_add_u32 s82, s26, 32768\ns_add_u32 s81, s82, 0\nMvaa 176,240,12\nbuffer_load_dwordx4 v[32:35], v0, s[60:63], s81 offen offset:0 sc1\nMvaa 176,208,8\nMvaa 208,208,16\nMvaa 208,240,20\n" \
"Mvaa 212,244,20\nbuffer_load_dwordx4 v[36:39], v0, s[60:63], s81 offen offset:1024 sc1\nMvaa 212,212,16\nMvaa 216,216,16\nMvaa 216,248,20\nMvaa 220,252,20\nbuffer_load_dwordx4 v[40:43], v0, s[60:63], s81 offen offset:2048 sc1\nMvaa 220,220,16\n" \
"Mvaa 252,220,24\nMvaa 252,252,28\nMvaa 248,248,28\nbuffer_load_dwordx4 v[44:47], v0, s[60:63], s81 offen offset:3072 sc1\nMvaa 248,216,24\nMvaa 244,212,24\nMvaa 244,244,28\nMvaa 240,240,28\n" \
"s_add_u32 s81, s82, 4096\nMvaa 240,208,24\ns_waitcnt lgkmcnt(0)\nMvaa 144,144,32\nbuffer_load_dwordx4 v[48:51], v0, s[60:63], s81 offen offset:0 sc1\nMvaa 144,176,36\nTa 208,24,32768\nMvaa 148,180,36\n" \
"Ta 210,24,33792\nMvaa 148,148,32\nMvaa 152,152,32\nTa 240,24,40960\nMvaa 152,184,36\nTa 242,24,41984\nMvaa 156,188,36\nbuffer_load_dwordx4 v[52:55], v0, s[60:63], s81 offen offset:1024 sc1\n" \
"Mvaa 156,156,32\nTa 212,25,32768\nMvaa 188,156,40\nTa 214,25,33792\nMvaa 188,188,44\nMvaa 184,184,44\nTa 244,25,40960\nMvaa 184,152,40\n" \
"Ta 246,25,41984\nMvaa 180,148,40\nbuffer_load_dwordx4 v[56:59], v0, s[60:63], s81 offen offset:2048 sc1\nMvaa 180,180,44\nTa 216,26,32768\nMvaa 176,176,44\nMvaa 176,144,40\nTa 218,26,33792\n" \
"Mvaa 208,144,48\nTa 248,26,40960\nMvaa 208,176,52\nbuffer_load_dwordx4 v[60:63], v0, s[60:63], s81 offen offset:3072 sc1\nMvaa 212,180,52\nTa 250,26,41984\nMvaa 212,148,48\nTa 220,27,32768\n" \
"Mvaa 216,152,48\nMvaa 216,184,52\nTa 222,27,33792\ns_add_u32 s82, s26, 98304\nMvaa 220,188,52\nTa 252,27,40960\ns_add_u32 s81, s82, 0\nMvaa 220,156,48\n" \
"buffer_load_dwordx4 v[64:67], v0, s[60:63], s81 offen offset:0 sc1\nMvaa 252,156,56\nTa 254,27,41984\nMvaa 252,188,60\nMvaa 248,184,60\nMvaa 248,152,56\nbuffer_load_dwordx4 v[68:71], v0, s[60:63], s81 offen offset:1024 sc1\nMvaa 244,148,56\n" \
"Mvaa 244,180,60\nMvaa 240,176,60\nMvaa 240,144,56\ns_waitcnt lgkmcnt(0)\nMvaa 144,208,64\nbuffer_load_dwordx4 v[72:75], v0, s[60:63], s81 offen offset:2048 sc1\nMvaa 144,240,68\nTa 144,24,49152\n" \
"Mvaa 148,244,68\nTa 146,24,50176\nMvaa 148,212,64\nMvaa 152,216,64\nTa 176,24,57344\ns_waitcnt vmcnt(11)\nbuffer_store_dwordx2 v[2:3], off, s[60:63], s27 sc1\nMvaa 152,248,68\n" \
"Ta 178,24,58368\nMvaa 156,252,68\nbuffer_load_dwordx4 v[76:79], v0, s[60:63], s81 offen offset:3072 sc1\nMvaa 156,220,64\nTa 148,25,49152\nMvaa 188,220,72\nTa 150,25,50176\nMvaa 188,252,76\n" \
"Mvaa 184,248,76\nTa 180,25,57344\ns_add_u32 s81, s82, 4096\nMvaa 184,216,72\nTa 182,25,58368\nMvaa 180,212,72\nbuffer_load_dwordx4 v[80:83], v0, s[60:63], s81 offen offset:0 sc1\nMvaa 180,244,76\n" \
"Ta 152,26,49152\nMvaa 176,240,76\nMvaa 176,208,72\nTa 154,26,50176\nMvaa 208,208,80\nTa 184,26,57344\nMvaa 208,240,84\nbuffer_load_dwordx4 v[84:87], v0, s[60:63], s81 offen offset:1024 sc1\n" \
"Mvaa 212,244,84\nTa 186,26,58368\nMvaa 212,212,80\nTa 156,27,49152\nMvaa 216,216,80\nMvaa 216,248,84\nTa 158,27,50176\nMvaa 220,252,84\n" \
"Ta 188,27,57344\nMvaa 220,220,80\nbuffer_load_dwordx4 v[88:91], v0, s[60:63], s81 offen offset:2048 sc1\nMvaa 252,220,88\nTa 190,27,58368\nMvaa 252,252,92\nMvaa 248,248,92\nMvaa 248,216,88\n" \
"buffer_load_dwordx4 v[92:95], v0, s[60:63], s81 offen offset:3072 sc1\nMvaa 244,212,88\nMvaa 244,244,92\nMvaa 240,240,92\nMvaa 240,208,88\ns_waitcnt lgkmcnt(0)\nMvaa 144,144,96\nbuffer_load_dwordx2 v[10:11], v7, s[60:63], 0 offen sc1\n" \
"Mvaa 144,176,100\nMvaa 148,180,100\nMvaa 148,148,96\nMvaa 152,152,96\nMvaa 152,184,100\nMvaa 156,188,100\nMvaa 156,156,96\nMvaa 188,156,104\n" \
"Mvaa 188,188,108\nMvaa 184,184,108\nMvaa 184,152,104\nMvaa 180,148,104\nMvaa 180,180,108\nMvaa 176,176,108\nMvaa 176,144,104\nMvaa 208,144,112\n" \
"buffer_load_dwordx2 v[102:103], v7, s[60:63], 0 offen sc1\nMvaa 208,176,116\nMvaa 212,180,116\nMvaa 212,148,112\nMvaa 216,152,112\nMvaa 216,184,116\nMvaa 220,188,116\nMvaa 220,156,112\n" \
"Mvaa 252,156,120\nMvaa 252,188,124\nMvaa 248,184,124\nMvaa 248,152,120\nMvaa 244,148,120\nMvaa 244,180,124\nMvaa 240,176,124\nMvaa 240,144,120\n" \
"buffer_load_dwordx2 v[120:121], v7, s[60:63], 0 offen sc1\ns_waitcnt vmcnt(2)\nv_cmp_eq_u64 vcc, v[10:11], v[2:3]\ns_andn2_b32 s82, s6, vcc_lo\ns_cbranch_scc1 zpl\nzpe:\ns_waitcnt vmcnt(18)\nMvvv 144,98,32\n" \
"s_add_u32 s82, s26, s28\ns_add_u32 s81, s82, 0\nbuffer_load_dwordx4 a[0:3], v0, s[60:63], s81 offen offset:0 sc1\nMvvv 148,96,32\nMvvv 152,98,36\nMvvv 156,96,36\nbuffer_load_dwordx4 a[4:7], v0, s[60:63], s81 offen offset:1024 sc1\ns_waitcnt vmcnt(18)\n" \
"Mvvv 176,98,40\nMvvv 180,96,40\nMvvv 184,98,44\nbuffer_load_dwordx4 a[8:11], v0, s[60:63], s81 offen offset:2048 sc1\nMvvv 188,96,44\ns_waitcnt vmcnt(17)\nMvvv 208,98,48\nMvvv 212,96,48\n" \
"s_branch 99f\n.p2align 12\nzp20:\ns_setpc_b64 s[98:99]\n99:\nbuffer_load_dwordx4 a[12:15], v0, s[60:63], s81 offen offset:3072 sc1\nMvvv 216,98,52\nMvvv 220,96,52\n" \
"s_waitcnt vmcnt(16)\nMvvv 240,98,56\ns_add_u32 s81, s82, 4096\nbuffer_load_dwordx4 a[16:19], v0, s[60:63], s81 offen offset:0 sc1\nMvvv 244,96,56\nMvvv 248,98,60\nMvvv 252,96,60\nbuffer_load_dwordx4 a[20:23], v0, s[60:63], s81 offen offset:1024 sc1\n" \
"s_waitcnt vmcnt(16)\nMvvv 144,98,64\nMvvv 148,96,64\nMvvv 152,98,68\nbuffer_load_dwordx4 a[24:27], v0, s[60:63], s81 offen offset:2048 sc1\nMvvv 156,96,68\ns_waitcnt vmcnt(14)\nMvvv 176,98,72\n" \
"Mvvv 180,96,72\nbuffer_load_dwordx4 a[28:31], v0, s[60:63], s81 offen offset:3072 sc1\nMvvv 184,98,76\nMvvv 188,96,76\ns_waitcnt vmcnt(13)\nMvvv 208,98,80\nMvvv 212,96,80\nMvvv 216,98,84\n" \
"Mvvv 220,96,84\ns_waitcnt vmcnt(11)\nMvvv 240,98,88\nMvvv 244,96,88\nMvvv 248,98,92\nMvvv 252,96,92\ns_waitcnt vmcnt(6)\nMvva 144,98,0\n" \
"Mvva 148,96,0\nMvva 152,98,4\nMvva 156,96,4\ns_waitcnt vmcnt(4)\nMvva 176,98,8\nMvva 180,96,8\nMvva 184,98,12\nMvva 188,96,12\n" \
"C 64,144,145\nC 65,146,147\nC 66,148,149\nC 67,150,151\nds_write_b64 v12, v[64:65] offset:0\nC 68,152,153\nC 69,154,155\nds_write_b64 v13, v[66:67] offset:0\n" \
"C 70,156,157\nC 71,158,159\nds_write_b64 v14, v[68:69] offset:0\nds_write_b64 v15, v[70:71] offset:0\ns_waitcnt vmcnt(2)\nMvva 208,98,16\nMvva 212,96,16\nMvva 216,98,20\n" \
"Mvva 220,96,20\ns_waitcnt lgkmcnt(0)\nRv 64,5,0\nRv 68,5,1024\nC 72,176,177\nC 73,178,179\nC 74,180,181\nC 75,182,183\n" \
"ds_write_b64 v12, v[72:73] offset:2048\nC 76,184,185\nC 77,186,187\nds_write_b64 v13, v[74:75] offset:2048\nC 78,188,189\nC 79,190,191\nds_write_b64 v14, v[76:77] offset:2048\nds_write_b64 v15, v[78:79] offset:2048\n" \
"s_waitcnt vmcnt(0)\nMvva 240,98,24\nMvva 244,96,24\nMvva 248,98,28\nMvva 252,96,28\ns_waitcnt lgkmcnt(0)\nRv 72,5,2048\nRv 76,5,3072\n" \
"C 80,208,209\nC 81,210,211\nC 82,212,213\nC 83,214,215\nds_write_b64 v12, v[80:81] offset:4096\nC 84,216,217\nC 85,218,219\nds_write_b64 v13, v[82:83] offset:4096\n" \
"C 86,220,221\nC 87,222,223\nds_write_b64 v14, v[84:85] offset:4096\nds_write_b64 v15, v[86:87] offset:4096\ns_add_u32 s95, s29, 0\ns_waitcnt lgkmcnt(10)\nbuffer_store_dwordx4 v[64:67], v6, s[64:67], s95 offen offset:0 sc1\nbuffer_store_dwordx4 v[68:71], v8, s[64:67], s95 offen offset:0 sc1\n" \
"s_nop 4\ns_waitcnt lgkmcnt(0)\nRv 80,5,4096\nRv 84,5,5120\nC 88,240,241\nC 89,242,243\nC 90,244,245\nC 91,246,247\n" \
"ds_write_b64 v12, v[88:89] offset:6144\nC 92,248,249\nC 93,250,251\nds_write_b64 v13, v[90:91] offset:6144\nC 94,252,253\nC 95,254,255\nds_write_b64 v14, v[92:93] offset:6144\nds_write_b64 v15, v[94:95] offset:6144\n" \
"s_add_u32 s95, s29, 262144\ns_waitcnt lgkmcnt(10)\nbuffer_store_dwordx4 v[72:75], v6, s[64:67], s95 offen offset:0 sc1\nbuffer_store_dwordx4 v[76:79], v8, s[64:67], s95 offen offset:0 sc1\ns_waitcnt lgkmcnt(0)\nRv 88,5,6144\nRv 92,5,7168\ns_add_u32 s95, s30, 0\n" \
"s_waitcnt lgkmcnt(6)\nbuffer_store_dwordx4 v[80:83], v6, s[64:67], s95 offen offset:0 sc1\nbuffer_store_dwordx4 v[84:87], v8, s[64:67], s95 offen offset:0 sc1\ns_add_u32 s95, s30, 262144\ns_waitcnt lgkmcnt(0)\nbuffer_store_dwordx4 v[88:91], v6, s[64:67], s95 offen offset:0 sc1\nbuffer_store_dwordx4 v[92:95], v8, s[64:67], s95 offen offset:0 sc1\nzfin:\n" \
".subsection 1\ns_branch 99f\n.p2align 12\nzp21:\ns_setpc_b64 s[98:99]\n99:\nzDa:\nQvav 144,160,64,112\n" \
"E 240,168\nRa 128,6,49152\nMvav 144,164,68\nE 241,169\nMvav 144,168,72\nC 212,240,241\nRa 132,6,50176\nRa 136,6,51200\n" \
"Mvav 144,172,76\nE 242,170\ns_add_u32 m0, s75, 0xc000\nQvav 152,160,80,12\nDS 8,60,s77,0\nMvav 152,164,84\nE 243,171\nRa 140,6,52224\n" \
"Mvav 152,168,88\nC 213,242,243\nE 244,172\nMvav 152,172,92\nDS 8,60,s77,1024\nRa 144,6,53248\nQvav 148,176,64,116\nE 245,173\n" \
"Mvav 148,180,68\nC 214,244,245\nRa 148,6,54272\nMvav 148,184,72\nE 246,174\nRa 152,6,55296\nMvav 148,188,76\nE 247,175\n" \
"Qvav 156,176,80,12\nC 215,246,247\nRa 156,6,56320\nMvav 156,180,84\nE 240,176\nMvav 156,184,88\nMvav 156,188,92\nE 241,177\n" \
"Mvvv 96,108,196\nC 216,240,241\nE 242,178\nv_smfmac_f32_16x16x64_bf16 v[100:103], v[104:107], v[200:207], v7\ns_waitcnt lgkmcnt(0)\nMaav 96,128,204\nDS 8,60,s77,2048\nRa 160,5,8192\n" \
"Maav 100,132,204\nE 243,179\nMaav 68,132,200\nC 217,242,243\nRa 164,5,9216\nMaav 64,128,200\nE 244,180\nMaav 32,128,196\n" \
"E 245,181\nMaav 36,132,196\nC 218,244,245\nRa 168,5,10240\nMaav 40,136,196\nE 246,182\nRa 172,5,11264\nMaav 44,140,196\n" \
"DS 8,60,s77,3072\nMaav 76,140,200\nE 247,183\ns_add_u32 m0, s32, 114688\nMaav 72,136,200\nC 219,246,247\nE 240,184\nD 18,68,0,0\n" \
"Maav 104,136,204\nRa 176,5,12288\nRa 180,5,13312\nMaav 108,140,204\nE 241,185\nD 18,68,s89,1024\nMaav 112,144,204\nC 220,240,241\n" \
"Ra 184,5,14336\nRa 188,5,15360\nMaav 116,148,204\nE 242,186\nD 19,68,s90,2048\nMaav 84,148,200\nE 243,187\nMaav 80,144,200\n" \
"C 221,242,243\nD 19,68,s91,3072\nMaav 48,144,196\nE 244,188\nMaav 52,148,196\ns_ff1_i32_b64 s78, s[8:9]\ns_bitset0_b64 s[8:9], s78\nMaav 56,152,196\n" \
"E 245,189\nMaav 60,156,196\nC 222,244,245\nE 246,190\nMaav 92,156,200\ns_xor_b32 s82, s78, s10\ns_and_b32 s80, s82, 31\nMaav 88,152,200\n" \
"E 247,191\ns_lshl_b32 s80, s80, 18\ns_cmp_lt_u32 s78, 32\ns_cselect_b64 s[68:69], s[14:15], s[12:13]\ns_cselect_b32 s70, 0x800000, s92\nv_add_u32 v18, s80, v16\nv_xor_b32 v19, 128, v18\nMaav 120,152,204\n" \
"C 223,246,247\ns_waitcnt lgkmcnt(0)\nMaav 124,156,204\ns_mov_b32 s76, s73\ns_mov_b32 s73, s74\ns_mov_b32 s74, s75\ns_mov_b32 s75, s76\ns_add_u32 s77, s77, 0x4000\n" \
"Qvav 176,160,64,120\nE 240,144\ns_add_u32 m0, s32, 131072\nRa 128,6,57344\nMvav 176,164,68\nD 18,68,0,0\nMvav 176,168,72\nE 241,145\n" \
"Mvav 176,172,76\nC 200,240,241\nRa 132,6,58368\nRa 136,6,59392\nQvav 184,160,80,12\nD 18,68,s89,1024\nMvav 184,164,84\nE 242,146\n" \
"Mvav 184,168,88\nRa 140,6,60416\nRa 144,6,61440\nRa 148,6,62464\nMvav 184,172,92\nE 243,147\nQvav 180,176,64,124\nC 201,242,243\n" \
"Ra 152,6,63488\nMvav 180,180,68\nD 19,68,s90,2048\nRa 156,6,64512\nMvav 180,184,72\nE 244,148\nMvav 180,188,76\nD 19,68,s91,3072\n" \
"Qvav 188,176,80,12\nE 245,149\nMvav 188,180,84\nC 202,244,245\nv_add_u32 v18, 131072, v18\nMvav 188,184,88\nv_add_u32 v19, 131072, v19\nv_mov_b32 v6, v5\n" \
"Mvav 188,188,92\nE 246,150\nMvvv 96,108,212\nv_add_u32 v5, s74, v9\nv_smfmac_f32_16x16x64_bf16 v[100:103], v[104:107], v[216:223], v7\ns_waitcnt vmcnt(20) lgkmcnt(0)\ns_barrier\nMaav 96,128,220\n" \
"E 247,151\nMaav 100,132,220\nC 203,246,247\nRa 160,5,0\nMaav 68,132,216\nRa 164,5,1024\nMaav 64,128,216\nE 240,152\n" \
"Maav 32,128,212\nRa 168,5,2048\nMaav 36,132,212\nE 241,153\nMaav 40,136,212\nC 204,240,241\nMaav 44,140,212\nRa 172,5,3072\n" \
"v_add_f32 v196, v96, v97\nv_add_f32 v197, v98, v99\nMaav 76,140,216\nE 242,154\nv_subrev_u32 v198, 0xf800000, v196\nMaav 72,136,216\nRa 176,5,4096\nv_subrev_u32 v199, 0xf800000, v197\n" \
"v_max_u32 v198, v198, v199\nMaav 104,136,220\nE 243,155\nv_cmp_lt_u32 vcc, 0x3c000000, v198\nMaav 108,140,220\nC 205,242,243\nMaav 112,144,220\nRa 180,5,5120\n" \
"Maav 116,148,220\nE 244,156\nMaav 84,148,216\nRa 184,5,6144\nMaav 80,144,216\nE 245,157\nMaav 48,144,212\nC 206,244,245\n" \
"Ra 188,5,7168\nMaav 52,148,212\nMaav 56,152,212\nE 246,158\nMaav 60,156,212\nMaav 92,156,216\nE 247,159\nMaav 88,152,216\n" \
"C 207,246,247\nMaav 120,152,220\ns_waitcnt lgkmcnt(0)\nMaav 124,156,220\ns_cbranch_vccnz zE3\ns_nop 7\nv_rcp_f32 v196, v196\nv_rcp_f32 v197, v197\n" \
"s_add_u32 s95, s83, 0\nAR 128,0\nAR 129,1\nAR 130,2\nAR 131,3\nAR 132,4\nAR 133,5\nAR 134,6\n" \
"AR 135,7\nv_pk_mul_f32 v[128:129], v[128:129], v[196:197] op_sel:[0,0] op_sel_hi:[1,0]\nv_pk_mul_f32 v[130:131], v[130:131], v[196:197] op_sel:[0,0] op_sel_hi:[1,0]\nv_pk_mul_f32 v[132:133], v[132:133], v[196:197] op_sel:[0,0] op_sel_hi:[1,0]\nv_pk_mul_f32 v[134:135], v[134:135], v[196:197] op_sel:[0,0] op_sel_hi:[1,0]\nC 32,128,129\nC 33,130,131\nC 34,132,133\n" \
"C 35,134,135\nbuffer_store_dwordx4 v[32:35], v0, s[60:63], s95 offen offset:0 sc1\nAR 136,8\nAR 137,9\nAR 138,10\nAR 139,11\nAR 140,12\nAR 141,13\n" \
"AR 142,14\nAR 143,15\nv_pk_mul_f32 v[136:137], v[136:137], v[196:197] op_sel:[0,0] op_sel_hi:[1,0]\nv_pk_mul_f32 v[138:139], v[138:139], v[196:197] op_sel:[0,0] op_sel_hi:[1,0]\nv_pk_mul_f32 v[140:141], v[140:141], v[196:197] op_sel:[0,0] op_sel_hi:[1,0]\nv_pk_mul_f32 v[142:143], v[142:143], v[196:197] op_sel:[0,0] op_sel_hi:[1,0]\nC 36,136,137\nC 37,138,139\n" \
"C 38,140,141\nC 39,142,143\nbuffer_store_dwordx4 v[36:39], v0, s[60:63], s95 offen offset:1024 sc1\ns_add_u32 s95, s83, 32768\nAR 128,16\nAR 129,17\nAR 130,18\nAR 131,19\n" \
"AR 132,20\nAR 133,21\nAR 134,22\nAR 135,23\nv_pk_mul_f32 v[128:129], v[128:129], v[196:197] op_sel:[0,0] op_sel_hi:[1,0]\nv_pk_mul_f32 v[130:131], v[130:131], v[196:197] op_sel:[0,0] op_sel_hi:[1,0]\nv_pk_mul_f32 v[132:133], v[132:133], v[196:197] op_sel:[0,0] op_sel_hi:[1,0]\nv_pk_mul_f32 v[134:135], v[134:135], v[196:197] op_sel:[0,0] op_sel_hi:[1,0]\n" \
"C 40,128,129\nC 41,130,131\nC 42,132,133\nC 43,134,135\nbuffer_store_dwordx4 v[40:43], v0, s[60:63], s95 offen offset:0 sc1\nAR 136,24\nAR 137,25\nAR 138,26\n" \
"AR 139,27\nAR 140,28\nAR 141,29\nAR 142,30\nAR 143,31\nv_pk_mul_f32 v[136:137], v[136:137], v[196:197] op_sel:[0,0] op_sel_hi:[1,0]\nv_pk_mul_f32 v[138:139], v[138:139], v[196:197] op_sel:[0,0] op_sel_hi:[1,0]\nv_pk_mul_f32 v[140:141], v[140:141], v[196:197] op_sel:[0,0] op_sel_hi:[1,0]\n" \
"v_pk_mul_f32 v[142:143], v[142:143], v[196:197] op_sel:[0,0] op_sel_hi:[1,0]\nC 44,136,137\nC 45,138,139\nC 46,140,141\nC 47,142,143\nbuffer_store_dwordx4 v[44:47], v0, s[60:63], s95 offen offset:1024 sc1\ns_add_u32 s95, s83, 8192\nAR 128,32\n" \
"AR 129,33\nAR 130,34\nAR 131,35\nAR 132,36\nAR 133,37\nAR 134,38\nAR 135,39\nv_pk_mul_f32 v[128:129], v[128:129], v[196:197] op_sel:[0,1] op_sel_hi:[1,1]\n" \
"v_pk_mul_f32 v[130:131], v[130:131], v[196:197] op_sel:[0,1] op_sel_hi:[1,1]\nv_pk_mul_f32 v[132:133], v[132:133], v[196:197] op_sel:[0,1] op_sel_hi:[1,1]\nv_pk_mul_f32 v[134:135], v[134:135], v[196:197] op_sel:[0,1] op_sel_hi:[1,1]\nC 48,128,129\nC 49,130,131\nC 50,132,133\nC 51,134,135\nbuffer_store_dwordx4 v[48:51], v0, s[60:63], s95 offen offset:0 sc1\n" \
"AR 136,40\nAR 137,41\nAR 138,42\nAR 139,43\nAR 140,44\nAR 141,45\nAR 142,46\nAR 143,47\n" \
"v_pk_mul_f32 v[136:137], v[136:137], v[196:197] op_sel:[0,1] op_sel_hi:[1,1]\nv_pk_mul_f32 v[138:139], v[138:139], v[196:197] op_sel:[0,1] op_sel_hi:[1,1]\nv_pk_mul_f32 v[140:141], v[140:141], v[196:197] op_sel:[0,1] op_sel_hi:[1,1]\nv_pk_mul_f32 v[142:143], v[142:143], v[196:197] op_sel:[0,1] op_sel_hi:[1,1]\nC 52,136,137\nC 53,138,139\nC 54,140,141\nC 55,142,143\n" \
"buffer_store_dwordx4 v[52:55], v0, s[60:63], s95 offen offset:1024 sc1\ns_add_u32 s95, s83, 40960\nAR 128,48\nAR 129,49\nAR 130,50\nAR 131,51\nAR 132,52\nAR 133,53\n" \
"AR 134,54\nAR 135,55\nv_pk_mul_f32 v[128:129], v[128:129], v[196:197] op_sel:[0,1] op_sel_hi:[1,1]\nv_pk_mul_f32 v[130:131], v[130:131], v[196:197] op_sel:[0,1] op_sel_hi:[1,1]\nv_pk_mul_f32 v[132:133], v[132:133], v[196:197] op_sel:[0,1] op_sel_hi:[1,1]\nv_pk_mul_f32 v[134:135], v[134:135], v[196:197] op_sel:[0,1] op_sel_hi:[1,1]\nC 56,128,129\nC 57,130,131\n" \
"C 58,132,133\nC 59,134,135\nbuffer_store_dwordx4 v[56:59], v0, s[60:63], s95 offen offset:0 sc1\nAR 136,56\nAR 137,57\nAR 138,58\nAR 139,59\nAR 140,60\n" \
"AR 141,61\nAR 142,62\nAR 143,63\nv_pk_mul_f32 v[136:137], v[136:137], v[196:197] op_sel:[0,1] op_sel_hi:[1,1]\nv_pk_mul_f32 v[138:139], v[138:139], v[196:197] op_sel:[0,1] op_sel_hi:[1,1]\nv_pk_mul_f32 v[140:141], v[140:141], v[196:197] op_sel:[0,1] op_sel_hi:[1,1]\nv_pk_mul_f32 v[142:143], v[142:143], v[196:197] op_sel:[0,1] op_sel_hi:[1,1]\nC 60,136,137\n" \
"C 61,138,139\nC 62,140,141\nC 63,142,143\nbuffer_store_dwordx4 v[60:63], v0, s[60:63], s95 offen offset:1024 sc1\ns_branch zE3\nzHs:\ns_add_u32 s96, s96, 2\ns_branch zH\n" \
"zL:\ns_waitcnt vmcnt(5)\ns_mov_b32 s96, s40\ns_mov_b32 s40, 0\nv_cmp_ne_u64 vcc, v[10:11], v[2:3]\ns_cbranch_vccz zO\ns_mov_b32 s94, 0x400000\nzt99:\n" \
"s_sleep 2\nbuffer_load_dwordx2 v[10:11], v1, s[60:63], 0 offen offset:64 sc1\ns_waitcnt vmcnt(0)\nv_cmp_ne_u64 vcc, v[10:11], v[2:3]\ns_cbranch_vccz zO\ns_sub_u32 s94, s94, 1\ns_cbranch_scc0 zt99\ns_branch zO\n" \
"zs101:\ns_mov_b32 s94, 0x400000\nzt101:\ns_sleep 2\nbuffer_load_dwordx2 v[10:11], v1, s[60:63], 0 offen offset:0 sc1\ns_waitcnt vmcnt(0)\nv_cmp_ne_u64 vcc, v[10:11], v[2:3]\ns_cbranch_vccz zr101\n" \
"s_sub_u32 s94, s94, 1\ns_cbranch_scc0 zt101\ns_branch zr101\nzs102:\ns_mov_b32 s94, 0x400000\nzt102:\ns_sleep 2\nbuffer_load_dwordx2 v[10:11], v1, s[60:63], 0 offen offset:0 sc1\n" \
"s_waitcnt vmcnt(0)\nv_cmp_ne_u64 vcc, v[10:11], v[2:3]\ns_cbranch_vccz zr102\ns_sub_u32 s94, s94, 1\ns_cbranch_scc0 zt102\ns_branch zr102\nzs103:\ns_mov_b32 s94, 0x400000\n" \
"zt103:\ns_sleep 2\nbuffer_load_dwordx2 v[10:11], off, s[60:63], s49 sc1\ns_waitcnt vmcnt(0)\nv_cmp_ne_u64 vcc, v[10:11], v[2:3]\ns_cbranch_vccz zr103\ns_sub_u32 s94, s94, 1\ns_cbranch_scc0 zt103\n" \
"s_branch zr103\nzs104:\ns_mov_b32 s94, 0x400000\nzt104:\nbuffer_load_dwordx2 v[10:11], v1, s[60:63], 0 offen sc1\ns_waitcnt vmcnt(0)\nv_cmp_eq_u64 vcc, v[10:11], v[2:3]\ns_andn2_b32 s82, s7, vcc_hi\n" \
"s_cselect_b64 vcc, 0, vcc\ns_bitcmp1_b64 vcc, s79\ns_cbranch_scc1 zr104\ns_sleep 1\ns_sub_u32 s94, s94, 1\ns_cbranch_scc0 zt104\ns_branch zr104\nzs105:\n" \
"s_mov_b32 s94, 0x400000\nzt105:\nbuffer_load_dwordx2 v[10:11], v1, s[60:63], 0 offen sc1\ns_waitcnt vmcnt(0)\nv_cmp_eq_u64 vcc, v[10:11], v[2:3]\ns_bitcmp1_b64 vcc, s79\ns_cbranch_scc1 zr105\ns_sleep 1\n" \
"s_sub_u32 s94, s94, 1\ns_cbranch_scc0 zt105\ns_branch zr105\nzs106:\ns_mov_b32 s94, 0x400000\nzt106:\nbuffer_load_dwordx2 v[10:11], v7, s[60:63], 0 offen sc1\ns_waitcnt vmcnt(0)\n" \
"v_cmp_eq_u64 vcc, v[10:11], v[2:3]\ns_andn2_b32 s82, 10, vcc_lo\ns_cbranch_scc0 zr106\ns_sleep 1\ns_sub_u32 s94, s94, 1\ns_cbranch_scc0 zt106\ns_branch zr106\nzpl:\n" \
"s_mov_b32 s94, 0x200000\nzpm:\nbuffer_load_dwordx2 v[10:11], v7, s[60:63], 0 offen sc1\ns_waitcnt vmcnt(2)\nv_cmp_eq_u64 vcc, v[102:103], v[2:3]\ns_andn2_b32 s82, s6, vcc_lo\ns_cbranch_scc0 zpe\nbuffer_load_dwordx2 v[102:103], v7, s[60:63], 0 offen sc1\n" \
"s_waitcnt vmcnt(2)\nv_cmp_eq_u64 vcc, v[120:121], v[2:3]\ns_andn2_b32 s82, s6, vcc_lo\ns_cbranch_scc0 zpe\nbuffer_load_dwordx2 v[120:121], v7, s[60:63], 0 offen sc1\ns_waitcnt vmcnt(2)\nv_cmp_eq_u64 vcc, v[10:11], v[2:3]\ns_andn2_b32 s82, s6, vcc_lo\n" \
"s_cbranch_scc0 zpe\ns_sub_u32 s94, s94, 1\ns_cbranch_scc0 zpm\ns_branch zpe\nzs107:\ns_mov_b32 s94, 0x400000\nzt107:\nbuffer_load_dwordx2 v[10:11], v1, s[60:63], 0 offen sc1\n" \
"s_waitcnt vmcnt(0)\nv_cmp_eq_u64 vcc, v[10:11], v[2:3]\ns_bitcmp1_b64 vcc, s79\ns_cbranch_scc1 zr107\ns_sleep 1\ns_sub_u32 s94, s94, 1\ns_cbranch_scc0 zt107\ns_branch zr107\n" \
"zs108:\ns_mov_b32 s94, 0x400000\nzt108:\nbuffer_load_dwordx2 v[10:11], v1, s[60:63], 0 offen sc1\ns_waitcnt vmcnt(0)\nv_cmp_eq_u64 vcc, v[10:11], v[2:3]\ns_bitcmp1_b64 vcc, s79\ns_cbranch_scc1 zr108\n" \
"s_sleep 1\ns_sub_u32 s94, s94, 1\ns_cbranch_scc0 zt108\ns_branch zr108\nzs109:\ns_mov_b32 s94, 0x400000\nzt109:\nbuffer_load_dwordx2 v[10:11], v1, s[60:63], 0 offen sc1\n" \
"s_waitcnt vmcnt(0)\nv_cmp_eq_u64 vcc, v[10:11], v[2:3]\ns_bitcmp1_b64 vcc, s79\ns_cbranch_scc1 zr109\ns_sleep 1\ns_sub_u32 s94, s94, 1\ns_cbranch_scc0 zt109\ns_branch zr109\n" \
"zep:\nv_lshrrev_b32 v28, 1, v0\ns_mov_b32 s94, 0x400000\nzepl:\nbuffer_load_dwordx2 v[96:97], v28, s[60:63], 0 offen offset:256 sc1\nbuffer_load_dwordx2 v[98:99], v28, s[60:63], 0 offen offset:768 sc1\nbuffer_load_dwordx2 v[100:101], v28, s[60:63], 0 offen offset:1280 sc1\nbuffer_load_dwordx2 v[102:103], v28, s[60:63], 0 offen offset:1792 sc1\n" \
"s_waitcnt vmcnt(0)\nv_cmp_ne_u64 s[98:99], v[96:97], v[2:3]\nv_cmp_ne_u64 s[100:101], v[98:99], v[2:3]\nv_cmp_ne_u64 vcc, v[100:101], v[2:3]\ns_or_b64 s[98:99], s[98:99], s[100:101]\ns_or_b64 s[98:99], s[98:99], vcc\nv_cmp_ne_u64 vcc, v[102:103], v[2:3]\ns_or_b64 s[98:99], s[98:99], vcc\n" \
"s_cbranch_scc0 zepd\ns_sub_u32 s94, s94, 1\ns_cbranch_scc0 zepl\nzepd:\nbuffer_store_dwordx2 v[2:3], off, s[60:63], 0 sc1\ns_branch zeq\ns_branch 99f\n.p2align 12\n" \
"zgv:\nzp22:\ns_setpc_b64 s[98:99]\n99:\nzV:\nZvaa 32,0,64\nTa 96,8,8192\ns_lshr_b32 s7, s3, 5\n" \
"Zvaa 36,4,64\nTa 98,8,9216\ns_lshr_b32 s5, 170, s7\nZvaa 44,4,68\nRa 32,27,0\ns_and_b32 s5, s5, 1\nZvaa 40,0,68\nTa 100,20,8192\n" \
"s_lshl_b32 s7, s7, 2\nZvaa 48,0,72\nTa 102,20,9216\ns_lshr_b32 s4, 0x12124444, s7\nZvaa 52,4,72\nRa 36,27,2048\ns_and_b32 s4, s4, 15\nZvaa 60,4,76\n" \
"Ta 104,21,8192\ns_xor_b32 s6, s5, s3\nZvaa 56,0,76\nTa 106,21,9216\ns_lshr_b32 s7, 0xff00, s4\nZvaa 64,0,80\nRa 40,27,4096\ns_and_b32 s7, s7, 0xff\n" \
"Zvaa 68,4,80\nTa 108,22,8192\ns_xor_b32 s10, s7, 0xff\nZvaa 76,4,84\nTa 110,22,9216\ns_lshl_b32 s7, s7, 8\nZvaa 72,0,84\nRa 44,27,6144\n" \
"s_or_b32 s7, s7, 0xff\nZvaa 80,0,88\nTa 112,23,8192\ns_add_u32 s8, s4, 5\nZvaa 84,4,88\nTa 114,23,9216\ns_sub_u32 s4, 262, s4\nZvaa 92,4,92\n" \
"Ra 48,27,8192\ns_add_u32 s9, s4, 0x101\nZvaa 88,0,92\nTa 116,24,8192\nZvaa 152,8,92\nTa 118,24,9216\nZvaa 156,12,92\nRa 52,27,10240\n" \
"Zvaa 148,12,88\nTa 120,25,8192\nZvaa 144,8,88\nTa 122,25,9216\nZvaa 136,8,84\nRa 56,27,12288\nZvaa 140,12,84\nTa 124,26,8192\n" \
"Zvaa 132,12,80\nTa 126,26,9216\nZvaa 128,8,80\nRa 60,27,14336\nZvaa 120,8,76\nAR 2,252\nAR 3,253\nZvaa 124,12,76\n" \
"v_readfirstlane_b32 s96, v2\nv_readfirstlane_b32 s97, v3\nZvaa 116,12,72\ns_mul_i32 s97, s97, 0x7f4a7c15\ns_mul_hi_u32 s98, s96, 0x7f4a7c15\nZvaa 112,8,72\ns_add_u32 s97, s97, s98\ns_mul_i32 s98, s96, 0x9e3779b9\n" \
"Zvaa 104,8,68\ns_add_u32 s97, s97, s98\ns_mul_i32 s96, s96, 0x7f4a7c15\nZvaa 108,12,68\ns_add_u32 s96, s96, 0xd9b4e019\ns_addc_u32 s97, s97, 0x632be59b\nZvaa 100,12,64\nv_mov_b32 v2, s96\n" \
"v_mov_b32 v3, s97\nZvaa 96,8,64\ns_mov_b32 s96, 3\nZvaa 160,16,64\ns_waitcnt lgkmcnt(0)\ns_barrier\nZvaa 164,20,64\ns_add_u32 m0, s88, 0\n" \
"Zvaa 172,20,68\nD 5,64,0,0\nZvaa 168,16,68\ns_cmp_lt_u32 s3, 128\ns_cselect_b32 s4, s9, s4\nZvaa 176,16,72\ns_bitcmp1_b32 s6, 0\ns_cselect_b32 s9, s4, s8\n" \
"s_cselect_b32 s7, s10, s7\nZvaa 180,20,72\ns_and_b32 s6, s6, 1\ns_lshr_b32 s10, s3, 1\nZvaa 188,20,76\nD 5,64,s73,1024\nZvaa 184,16,76\ns_and_b32 s10, s10, 2\n" \
"s_or_b32 s6, s6, s10\nZvaa 192,16,80\ns_lshl_b32 s6, s6, 3\ns_lshl_b32 s8, s7, s6\nZvaa 196,20,80\ns_bfe_u32 s10, s3, 0x20003\ns_add_u32 s6, s6, s10\nZvaa 204,20,84\n" \
"D 5,64,s74,2048\nZvaa 200,16,84\ns_bitset0_b32 s8, s6\ns_lshl_b32 s5, s5, 16\nZvaa 208,16,88\ns_or_b32 s9, s9, s5\nZvaa 212,20,88\nZvaa 220,20,92\n" \
"D 5,64,s75,3072\nZvaa 216,16,92\nZaaa 152,24,92\nZaaa 156,28,92\ns_add_u32 m0, s88, 4096\nZaaa 148,28,88\nD 5,64,s76,0\nZaaa 144,24,88\n" \
"Zaaa 136,24,84\nZaaa 140,28,84\nZaaa 132,28,80\nD 5,64,s77,1024\nZaaa 128,24,80\nZvaa 248,24,76\nZvaa 252,28,76\nZvaa 244,28,72\n" \
"D 5,64,s78,2048\nZvaa 240,24,72\nZvaa 232,24,68\nZvaa 236,28,68\nZvaa 228,28,64\nD 5,64,s79,3072\nZvaa 224,24,64\ns_branch 99f\n" \
".p2align 12\nzp23:\ns_setpc_b64 s[98:99]\n99:\ns_nop 0\nzgV:\nMvaa 32,32,96\nMvaa 36,36,96\n" \
"s_add_u32 m0, s32, 65536\nMvaa 44,36,100\nbuffer_load_dwordx4 v6, s[68:71], 0 offen offset:0 nt lds\nMvaa 40,32,100\nMvaa 48,32,104\nMvaa 52,36,104\nMvaa 60,36,108\nbuffer_load_dwordx4 v6, s[68:71], s89 offen offset:1024 nt lds\n" \
"Mvaa 56,32,108\nMvaa 64,32,112\nMvaa 68,36,112\nMvaa 76,36,116\nbuffer_load_dwordx4 v28, s[68:71], s90 offen offset:2048 nt lds\nMvaa 72,32,116\nMvaa 80,32,120\nMvaa 84,36,120\n" \
"Mvaa 92,36,124\nbuffer_load_dwordx4 v28, s[68:71], s91 offen offset:3072 nt lds\nMvaa 88,32,124\nMvaa 152,40,124\nMvaa 156,44,124\ns_add_u32 m0, s32, 81920\nMvaa 148,44,120\nbuffer_load_dwordx4 v6, s[68:71], s92 offen offset:0 nt lds\n" \
"Mvaa 144,40,120\nMvaa 136,40,116\nMvaa 140,44,116\nMvaa 132,44,112\nbuffer_load_dwordx4 v6, s[68:71], s93 offen offset:1024 nt lds\nMvaa 128,40,112\nMvaa 120,40,108\nMvaa 124,44,108\n" \
"Mvaa 116,44,104\nbuffer_load_dwordx4 v28, s[68:71], s94 offen offset:2048 nt lds\nMvaa 112,40,104\nMvaa 104,40,100\nMvaa 108,44,100\ns_swappc_b64 s[98:99], s[20:21]\nMvaa 100,44,96\nbuffer_load_dwordx4 v28, s[68:71], s95 offen offset:3072 nt lds\n" \
"Mvaa 96,40,96\ns_waitcnt vmcnt(17)\ns_barrier\nMvaa 160,48,96\nbuffer_load_dword v30, v4, s[52:55], 0 offen sc1\nTa 64,8,32768\nv_add_u32 v5, 128, v5\nMvaa 164,52,96\n" \
"Ta 66,8,33792\nv_add_u32 v6, s72, v6\nMvaa 172,52,100\nRa 0,7,32768\nv_add_u32 v28, s72, v28\nMvaa 168,48,100\nTa 68,20,32768\nMvaa 176,48,104\n" \
"Ta 70,20,33792\nMvaa 180,52,104\nRa 4,7,34816\nMvaa 188,52,108\nv_add_u32 v4, s44, v4\nTa 72,21,32768\nMvaa 184,48,108\nTa 74,21,33792\n" \
"Mvaa 192,48,112\nRa 8,7,36864\nMvaa 196,52,112\nTa 76,22,32768\nMvaa 204,52,116\nTa 78,22,33792\nMvaa 200,48,116\nRa 12,7,38912\n" \
"Mvaa 208,48,120\nTa 80,23,32768\nMvaa 212,52,120\nTa 82,23,33792\nMvaa 220,52,124\nRa 16,7,40960\nMvaa 216,48,124\nTa 84,24,32768\n" \
"Maaa 152,56,124\nTa 86,24,33792\nMaaa 156,60,124\nRa 20,7,43008\nMaaa 148,60,120\nTa 88,25,32768\nMaaa 144,56,120\nTa 90,25,33792\n" \
"Maaa 136,56,116\nRa 24,7,45056\nMaaa 140,60,116\nTa 92,26,32768\nMaaa 132,60,112\nTa 94,26,33792\nMaaa 128,56,112\nRa 28,7,47104\n" \
"Mvaa 248,56,108\nMvaa 252,60,108\nMvaa 244,60,104\nMvaa 240,56,104\nMvaa 232,56,100\nMvaa 236,60,100\nMvaa 228,60,96\nMvaa 224,56,96\n" \
"s_waitcnt lgkmcnt(0)\nMvaa 32,0,64\nTa 96,8,40960\ns_and_b32 s4, s3, 4\nMvaa 36,4,64\nTa 98,8,41984\ns_and_b32 s5, s3, 1\nMvaa 44,4,68\n" \
"Ra 32,27,32768\ns_lshl_b32 s4, s4, 2\nMvaa 40,0,68\nTa 100,20,40960\ns_lshl3_add_u32 s4, s5, s4\nMvaa 48,0,72\nTa 102,20,41984\ns_and_b32 s5, s3, 2\n" \
"Mvaa 52,4,72\nRa 36,27,34816\ns_bfe_u32 s6, s3, 0x20003\nMvaa 60,4,76\nTa 104,21,40960\ns_lshl1_add_u32 s4, s5, s4\nMvaa 56,0,76\nTa 106,21,41984\n" \
"s_add_u32 s7, s4, s6\nMvaa 64,0,80\nRa 40,27,36864\ns_lshr_b32 s10, s11, 3\nMvaa 68,4,80\nTa 108,22,40960\ns_and_b32 s22, s11, 7\nMvaa 76,4,84\n" \
"Ta 110,22,41984\ns_lshl_b32 s4, s7, 8\nMvaa 72,0,84\nRa 44,27,38912\ns_lshl_b32 s5, s10, 9\nMvaa 80,0,88\nTa 112,23,40960\ns_sub_u32 s4, s4, s5\n" \
"Mvaa 84,4,88\nTa 114,23,41984\nv_add_u32 v14, s4, v16\nMvaa 92,4,92\nRa 48,27,40960\ns_mul_i32 s38, s7, 525312\nMvaa 88,0,92\nTa 116,24,40960\n" \
"s_add_u32 s38, s38, 17039360\nMvaa 152,8,92\nTa 118,24,41984\ns_lshr_b32 s4, s7, 1\nMvaa 156,12,92\nRa 52,27,43008\ns_lshl_b32 s4, s4, 7\nMvaa 148,12,88\n" \
"Ta 120,25,40960\nv_lshrrev_b32 v18, 1, v0\nMvaa 144,8,88\nTa 122,25,41984\ns_add_u32 s4, s4, 256\nMvaa 136,8,84\nRa 56,27,45056\nv_and_b32 v18, 0x38, v18\n" \
"Mvaa 140,12,84\nTa 124,26,40960\nv_add_u32 v1, s4, v18\nMvaa 132,12,80\nTa 126,26,41984\ns_lshr_b32 s23, s3, 5\nMvaa 128,8,80\nRa 60,27,47104\n" \
"Mvaa 120,8,76\ns_cmp_lt_u32 s3, 128\ns_cselect_b32 s4, 0, 4\ns_cselect_b32 s5, 7, 23\nMvaa 124,12,76\ns_add_u32 s23, s23, s4\ns_sub_u32 s5, s5, s23\nMvaa 116,12,72\n" \
"s_lshl_b32 s42, s23, 2\ns_lshl_b32 s43, s5, 2\nMvaa 112,8,72\ns_sub_u32 s5, s5, s23\ns_lshl_b32 s37, s5, 18\nMvaa 104,8,68\ns_lshl1_add_u32 s41, s5, -5\ns_lshl1_add_u32 s4, s23, -1\n" \
"Mvaa 108,12,68\ns_lshl1_add_u32 s5, s23, -13\nMvaa 100,12,64\ns_cmp_lt_u32 s3, 128\ns_cselect_b32 s39, s4, 9\ns_cselect_b32 s40, 0, s5\nMvaa 96,8,64\ns_bfe_u32 s4, s32, 0x1000c\n" \
"s_lshr_b32 s5, s32, 13\nMvaa 160,16,64\ns_waitcnt lgkmcnt(0)\ns_barrier\nMvaa 164,20,64\ns_add_u32 m0, s88, 32768\ns_lshl1_add_u32 s4, s10, s4\nMvaa 172,20,68\n" \
"D 5,64,0,0\nMvaa 168,16,68\ns_lshl1_add_u32 s5, s22, s5\ns_mul_i32 s4, s4, 525312\nMvaa 176,16,72\ns_lshl_b32 s5, s5, 15\ns_add_u32 s4, s4, s5\nMvaa 180,20,72\n" \
"s_bitcmp1_b32 s33, 0\ns_cselect_b32 s5, 16810496, 0\ns_cselect_b32 s35, 8, 0\nMvaa 188,20,76\nD 5,64,s73,1024\nMvaa 184,16,76\ns_add_u32 s4, s4, s5\ns_add_u32 s34, s4, 17039360\n" \
"Mvaa 192,16,80\ns_lshl4_add_u32 s35, s11, s35\ns_add_u32 s35, s35, 256\nMvaa 196,20,80\ns_and_b32 s4, s3, 1\ns_lshl2_add_u32 s24, s4, s6\nMvaa 204,20,84\nD 5,64,s74,2048\n" \
"Mvaa 200,16,84\ns_bfe_u32 s4, s3, 0x20001\ns_bfe_u32 s5, s3, 0x20005\nMvaa 208,16,88\ns_lshl2_add_u32 s25, s4, s5\ns_lshl4_add_u32 s26, s24, s25\nMvaa 212,20,88\nv_and_b32 v18, 0x70, v17\n" \
"v_lshlrev_b32 v19, 1, v17\nMvaa 220,20,92\nD 5,64,s75,3072\nMvaa 216,16,92\ns_sub_u32 s4, s24, s22\nv_sub_u32 v19, v19, v18\nMaaa 152,24,92\ns_lshl_b32 s4, s4, 19\n" \
"v_add_u32 v9, s4, v19\nMaaa 156,28,92\ns_add_u32 m0, s88, 36864\ns_lshl_b32 s4, s23, 18\nMaaa 148,28,88\nD 5,64,s76,0\nMaaa 144,24,88\ns_lshl_b32 s5, s22, 19\n" \
"s_sub_u32 s4, s4, s5\nMaaa 136,24,84\ns_mul_i32 s5, s32, 24\ns_sub_u32 s4, s4, s5\nMaaa 140,28,84\nv_add_u32 v13, s4, v19\nv_lshrrev_b32 v18, 13, v16\nMaaa 132,28,80\n" \
"D 5,64,s77,1024\nMaaa 128,24,80\nv_and_b32 v19, 0x70, v16\nv_mul_u32_u24 v18, 6144, v18\nMvaa 248,24,76\ns_lshl_b32 s4, s25, 7\nv_bfe_u32 v12, v16, 7, 1\nMvaa 252,28,76\n" \
"v_add3_u32 v18, v18, v19, s4\nv_xor_b32 v19, 1, v12\nMvaa 244,28,72\nD 5,64,s78,2048\nMvaa 240,24,72\ns_bfe_u32 s4, s32, 0x1000c\nv_lshl_add_u32 v10, v12, 11, v18\nMvaa 232,24,68\n" \
"v_lshl_add_u32 v11, v19, 11, v18\nv_xor_b32 v12, s4, v12\nMvaa 236,28,68\ns_mul_i32 s5, s32, 12\nv_mul_u32_u24 v12, 196608, v12\nMvaa 228,28,64\nD 5,64,s79,3072\nMvaa 224,24,64\n" \
"s_sub_u32 s5, 4096, s5\nv_add3_u32 v12, v18, v12, s5\nMvaa 32,32,96\ns_lshl3_add_u32 s48, s26, 4096\ns_add_u32 s4, s48, 4096\nMvaa 36,36,96\ns_add_u32 m0, s32, 98304\ns_lshl_b32 s5, s26, 13\n" \
"Mvaa 44,36,100\nbuffer_load_dwordx4 v6, s[68:71], 0 offen offset:0 nt lds\nMvaa 40,32,100\ns_lshr_b32 s6, s32, 1\ns_add_u32 s5, s5, s6\nMvaa 48,32,104\ns_add_u32 s5, s5, 262144\ns_and_b32 s6, s3, 127\n" \
"Mvaa 52,36,104\ns_lshl_b32 s6, s6, 14\ns_add_u32 s6, s6, s32\nMvaa 60,36,108\nbuffer_load_dwordx4 v6, s[68:71], s89 offen offset:1024 nt lds\nMvaa 56,32,108\ns_cmp_lt_u32 s3, 128\ns_cselect_b32 s36, s4, s35\n" \
"s_cselect_b32 s46, s5, s6\nMvaa 64,32,112\ns_cmp_ge_u32 s32, 0x2000\ns_cselect_b32 s4, 0, 16\nMvaa 68,36,112\ns_sub_u32 s27, s26, s4\ns_xor_b32 s5, s32, 0x2000\nMvaa 76,36,116\n" \
"buffer_load_dwordx4 v28, s[68:71], s90 offen offset:2048 nt lds\nMvaa 72,32,116\ns_lshl_b32 s4, s27, 13\ns_lshr_b32 s5, s5, 1\nMvaa 80,32,120\ns_add_u32 s4, s4, s5\ns_add_u32 s4, s4, 262144\nMvaa 84,36,120\n" \
"s_lshr_b32 s5, s32, 13\ns_or_b32 s5, s5, s24\nMvaa 92,36,124\nbuffer_load_dwordx4 v28, s[68:71], s91 offen offset:3072 nt lds\nMvaa 88,32,124\ns_cmp_eq_u32 s5, 0\ns_cselect_b32 s51, 1, 0\ns_cselect_b32 s47, 0x7fff0000, s4\n" \
"s_cselect_b32 s27, s26, s27\nMvaa 152,40,124\ns_lshl3_add_u32 s49, s27, 4096\ns_lshl_b32 s4, s26, 15\nMvaa 156,44,124\ns_add_u32 m0, s32, 114688\ns_and_b32 s5, s32, 0x2000\nMvaa 148,44,120\n" \
"buffer_load_dwordx4 v6, s[68:71], s92 offen offset:0 nt lds\nMvaa 144,40,120\ns_lshl1_add_u32 s4, s5, s4\ns_bfe_u32 s5, s32, 0x1000c\nMvaa 136,40,116\ns_lshl_b32 s5, s5, 10\ns_add_u32 s4, s4, s5\nMvaa 140,44,116\n" \
"s_add_u32 s50, s4, 50660352\ns_lshr_b32 s6, s6, 12\nMvaa 132,44,112\nbuffer_load_dwordx4 v6, s[68:71], s93 offen offset:1024 nt lds\nMvaa 128,40,112\ns_mov_b32 s5, 182784\ns_mov_b32 s27, 2097152\nMvaa 120,40,108\n" \
"v_lshlrev_b32 v18, 3, v0\nv_cmp_gt_u32 vcc, 224, v0\nMvaa 124,44,108\ns_cmp_lt_u32 s6, 410\ns_cselect_b32 s4, 0, 410\ns_cselect_b32 s45, 734720, s5\ns_cselect_b64 s[56:57], s[14:15], s[12:13]\ns_cselect_b32 s27, 8388608, s27\n" \
"Mvaa 116,44,104\nbuffer_load_dwordx4 v28, s[68:71], s94 offen offset:2048 nt lds\nMvaa 112,40,104\ns_sub_u32 s6, s6, s4\ns_mul_i32 s6, s6, 1792\nMvaa 104,40,100\ns_cmp_lt_u32 s3, 128\ns_cselect_b32 s58, 0, s27\n" \
"Mvaa 108,44,100\ns_swappc_b64 s[98:99], s[18:19]\ns_add_u32 s18, s18, 0x4000\ns_addc_u32 s19, s19, 0\nMvaa 100,44,96\nbuffer_load_dwordx4 v28, s[68:71], s95 offen offset:3072 nt lds\nMvaa 96,40,96\ns_waitcnt vmcnt(17)\n" \
"s_barrier\nMvaa 160,48,96\nbuffer_load_dword v30, v4, s[52:55], 0 offen sc1\nTa 64,8,0\nv_add_u32 v5, 128, v5\nMvaa 164,52,96\nTa 66,8,1024\nv_add_u32 v6, s72, v6\n" \
"Mvaa 172,52,100\nRa 0,7,0\nv_add_u32 v28, s72, v28\nMvaa 168,48,100\nTa 68,20,0\nv_add_u32 v18, s6, v18\nMvaa 176,48,104\nTa 70,20,1024\n" \
"s_mov_b32 s59, 0x20000\nMvaa 180,52,104\nRa 4,7,2048\nv_cndmask_b32 v15, 2.0, v18, vcc\nMvaa 188,52,108\nv_add_u32 v4, s44, v4\nTa 72,21,0\nMvaa 184,48,108\n" \
"Ta 74,21,1024\nMvaa 192,48,112\nRa 8,7,4096\nMvaa 196,52,112\nTa 76,22,0\nMvaa 204,52,116\nTa 78,22,1024\nMvaa 200,48,116\n" \
"Ra 12,7,6144\nMvaa 208,48,120\nTa 80,23,0\nMvaa 212,52,120\nTa 82,23,1024\nMvaa 220,52,124\nRa 16,7,8192\nMvaa 216,48,124\n" \
"Ta 84,24,0\nMaaa 152,56,124\nTa 86,24,1024\nMaaa 156,60,124\nRa 20,7,10240\nMaaa 148,60,120\nTa 88,25,0\nMaaa 144,56,120\n" \
"Ta 90,25,1024\nMaaa 136,56,116\nRa 24,7,12288\nMaaa 140,60,116\nTa 92,26,0\nMaaa 132,60,112\nTa 94,26,1024\nMaaa 128,56,112\n" \
"Ra 28,7,14336\nMvaa 248,56,108\nMvaa 252,60,108\nMvaa 244,60,104\nMvaa 240,56,104\ns_sub_u32 s96, s96, 1\nMvaa 232,56,100\ns_cmp_lg_u32 s96, 0\n" \
"Mvaa 236,60,100\nMvaa 228,60,96\nMvaa 224,56,96\ns_waitcnt lgkmcnt(0)\ns_cbranch_scc0 zhV\nMvaa 32,0,64\nTa 96,8,8192\nMvaa 36,4,64\n" \
"Ta 98,8,9216\nMvaa 44,4,68\nRa 32,27,0\nMvaa 40,0,68\nTa 100,20,8192\nMvaa 48,0,72\nTa 102,20,9216\nMvaa 52,4,72\n" \
"Ra 36,27,2048\nMvaa 60,4,76\nTa 104,21,8192\nMvaa 56,0,76\nTa 106,21,9216\nMvaa 64,0,80\nRa 40,27,4096\nMvaa 68,4,80\n" \
"Ta 108,22,8192\nMvaa 76,4,84\nTa 110,22,9216\nMvaa 72,0,84\nRa 44,27,6144\nMvaa 80,0,88\nTa 112,23,8192\nMvaa 84,4,88\n" \
"Ta 114,23,9216\nMvaa 92,4,92\nRa 48,27,8192\nMvaa 88,0,92\nTa 116,24,8192\nMvaa 152,8,92\nTa 118,24,9216\nMvaa 156,12,92\n" \
"Ra 52,27,10240\nMvaa 148,12,88\nTa 120,25,8192\nMvaa 144,8,88\nTa 122,25,9216\nMvaa 136,8,84\nRa 56,27,12288\nMvaa 140,12,84\n" \
"Ta 124,26,8192\nMvaa 132,12,80\nTa 126,26,9216\nMvaa 128,8,80\nRa 60,27,14336\nMvaa 120,8,76\nMvaa 124,12,76\nMvaa 116,12,72\n" \
"Mvaa 112,8,72\nMvaa 104,8,68\nMvaa 108,12,68\nMvaa 100,12,64\nMvaa 96,8,64\nMvaa 160,16,64\ns_waitcnt lgkmcnt(0)\ns_barrier\n" \
"Mvaa 164,20,64\ns_add_u32 m0, s88, 0\nMvaa 172,20,68\nD 5,64,0,0\nMvaa 168,16,68\nMvaa 176,16,72\nMvaa 180,20,72\nMvaa 188,20,76\n" \
"D 5,64,s73,1024\nMvaa 184,16,76\nMvaa 192,16,80\nMvaa 196,20,80\nMvaa 204,20,84\nD 5,64,s74,2048\nMvaa 200,16,84\nMvaa 208,16,88\n" \
"Mvaa 212,20,88\nMvaa 220,20,92\nD 5,64,s75,3072\nMvaa 216,16,92\nMaaa 152,24,92\nMaaa 156,28,92\ns_add_u32 m0, s88, 4096\nMaaa 148,28,88\n" \
"D 5,64,s76,0\nMaaa 144,24,88\nMaaa 136,24,84\nMaaa 140,28,84\nMaaa 132,28,80\nD 5,64,s77,1024\nMaaa 128,24,80\nMvaa 248,24,76\n" \
"Mvaa 252,28,76\nMvaa 244,28,72\nD 5,64,s78,2048\nMvaa 240,24,72\nMvaa 232,24,68\nMvaa 236,28,68\nMvaa 228,28,64\nD 5,64,s79,3072\n" \
"Mvaa 224,24,64\ns_branch zgV\nzhV:\ns_bitcmp1_b32 s33, 1\ns_cbranch_scc1 zxV\nMvaa 32,0,64\nTa 96,8,8192\ns_mov_b64 s[64:65], s[84:85]\n" \
"Mvaa 36,4,64\nTa 98,8,9216\ns_mov_b32 s66, 4194304\nMvaa 44,4,68\nRa 32,27,0\ns_mov_b64 s[68:69], s[86:87]\nMvaa 40,0,68\nTa 100,20,8192\n" \
"s_branch 99f\n.p2align 12\nzp24:\ns_setpc_b64 s[98:99]\n99:\ns_mov_b32 s70, 8388608\nMvaa 48,0,72\nTa 102,20,9216\n" \
"s_mov_b32 s73, 15360\nMvaa 52,4,72\nRa 36,27,2048\ns_mov_b32 s74, 129024\nMvaa 60,4,76\nTa 104,21,8192\ns_mov_b32 s75, 144384\nMvaa 56,0,76\n" \
"Ta 106,21,9216\ns_add_u32 s76, s37, 0\nMvaa 64,0,80\nRa 40,27,4096\ns_add_u32 s77, s37, 15360\nMvaa 68,4,80\nTa 108,22,8192\ns_add_u32 s78, s37, 129024\n" \
"Mvaa 76,4,84\nTa 110,22,9216\ns_add_u32 s79, s37, 144384\nMvaa 72,0,84\nRa 44,27,6144\ns_mov_b32 s89, 31744\nMvaa 80,0,88\nTa 112,23,8192\n" \
"s_mov_b32 s90, 63488\nMvaa 84,4,88\nTa 114,23,9216\ns_mov_b32 s91, 95232\nMvaa 92,4,92\nRa 48,27,8192\ns_mov_b32 s72, 524288\nMvaa 88,0,92\n" \
"Ta 116,24,8192\nv_xor_b32 v28, 128, v14\nMvaa 152,8,92\nTa 118,24,9216\nMvaa 156,12,92\nRa 52,27,10240\nMvaa 148,12,88\nTa 120,25,8192\n" \
"Mvaa 144,8,88\nTa 122,25,9216\nMvaa 136,8,84\nRa 56,27,12288\nMvaa 140,12,84\nTa 124,26,8192\nMvaa 132,12,80\nTa 126,26,9216\n" \
"Mvaa 128,8,80\nRa 60,27,14336\nMvaa 120,8,76\nMvaa 124,12,76\nMvaa 116,12,72\nMvaa 112,8,72\nMvaa 104,8,68\nMvaa 108,12,68\n" \
"Mvaa 100,12,64\nMvaa 96,8,64\nMvaa 160,16,64\ns_waitcnt lgkmcnt(0)\ns_barrier\nMvaa 164,20,64\ns_add_u32 m0, s88, 131072\ns_and_b32 s10, s3, 2\n" \
"Mvaa 172,20,68\nD 13,64,0,0\nMvaa 168,16,68\ns_bfe_u32 s100, s3, 0x10002\ns_lshl_b32 s10, s10, 1\nMvaa 176,16,72\ns_bfe_u32 s2, s9, 0x10010\ns_xor_b32 s99, s2, s3\n" \
"Mvaa 180,20,72\ns_and_b32 s99, s99, 1\ns_lshl1_add_u32 s99, s100, s99\nMvaa 188,20,76\nD 13,64,s73,1024\nMvaa 184,16,76\ns_bfe_u32 s101, s3, 0x20003\ns_lshl3_add_u32 s11, s99, s101\n" \
"Mvaa 192,16,80\ns_add_u32 s101, s101, s10\ns_lshl3_add_u32 s10, s2, s10\nMvaa 196,20,80\ns_bfe_u32 s2, s3, 0x20005\ns_lshl1_add_u32 s2, s2, s100\nMvaa 204,20,84\nD 13,64,s74,2048\n" \
"Mvaa 200,16,84\ns_bfe_u32 s23, s9, 0x80008\ns_and_b32 s22, s9, 255\nMvaa 208,16,88\ns_lshr_b32 s6, 4, s99\ns_lshr_b32 s25, s3, 5\nMvaa 212,20,88\ns_lshl_b32 s4, s25, 8\n" \
"s_lshl_b32 s18, s25, 21\nMvaa 220,20,92\nD 13,64,s75,3072\nMvaa 216,16,92\ns_lshl3_add_u32 s25, s25, s101\ns_lshl_b32 s26, s25, 18\nMaaa 152,24,92\ns_lshl_b32 s27, s25, 7\n" \
"s_add_u32 s4, s4, 16384\nMaaa 156,28,92\ns_add_u32 m0, s88, 135168\ns_xor_b32 s25, s11, s10\nMaaa 148,28,88\nD 13,64,s76,0\nMaaa 144,24,88\ns_lshl3_add_u32 s31, s25, s4\n" \
"s_lshl_b32 s5, s2, 7\nMaaa 136,24,84\ns_add_u32 s5, s5, 8192\ns_lshr_b32 s25, s32, 1\nMaaa 140,28,84\ns_add_u32 s18, s18, s25\ns_add_u32 s18, s18, 54854656\nMaaa 132,28,80\n" \
"D 13,64,s77,1024\nMaaa 128,24,80\ns_lshl_b32 s19, s2, 19\ns_add_u32 s19, s19, s25\nMvaa 248,24,76\ns_add_u32 s19, s19, 48563200\ns_lshr_b32 s25, s32, 7\nMvaa 252,28,76\n" \
"s_add_u32 s27, s27, s25\ns_lshl3_add_u32 s27, s99, s27\nMvaa 244,28,72\nD 13,64,s78,2048\nMvaa 240,24,72\ns_add_u32 s27, s27, 32768\ns_lshl1_add_u32 s26, s32, s26\nMvaa 232,24,68\n" \
"s_add_u32 s26, s26, 71631872\ns_lshl_b32 s28, s99, 15\nMvaa 236,28,68\ns_add_u32 s24, s26, s28\ns_sub_u32 s28, 0x10000, s28\nMvaa 228,28,64\nD 13,64,s79,3072\nMvaa 224,24,64\n" \
"s_lshl_b32 s25, s100, 17\ns_add_u32 s26, s26, s25\nMvaa 32,32,96\ns_cmp_eq_u32 s99, 0\ns_cselect_b32 s21, 128, 0\nMvaa 36,36,96\ns_add_u32 m0, s32, 81920\ns_lshl_b32 s25, s21, 10\n" \
"Mvaa 44,36,100\nD 14,68,0,0\nMvaa 40,32,100\ns_add_u32 s24, s24, s25\ns_add_u32 s25, s24, 0x20000\nMvaa 48,32,104\ns_lshl_b32 s101, s101, 8\ns_and_b32 s20, s32, 0x2000\n" \
"Mvaa 52,36,104\ns_lshl3_add_u32 s20, s20, s101\ns_and_b32 s99, s32, 0x1000\nMvaa 60,36,108\nD 14,68,s89,1024\nMvaa 56,32,108\ns_lshl2_add_u32 s20, s99, s20\ns_lshl4_add_u32 s101, s32, s101\n" \
"Mvaa 64,32,112\ns_lshl_b32 s2, s2, 20\ns_add_u32 s2, s2, s101\nMvaa 68,36,112\ns_lshl_b32 s100, s100, 7\ns_add_u32 s101, s101, s100\nMvaa 76,36,116\nD 28,68,s90,2048\n" \
"Mvaa 72,32,116\ns_add_u32 s101, s101, 2048\ns_lshl_b32 s29, s42, 17\nMvaa 80,32,120\ns_lshl_b32 s30, s43, 17\ns_add_u32 s29, s29, s101\nMvaa 84,36,120\ns_add_u32 s30, s30, s101\n" \
"s_add_u32 s101, s2, 524288\nMvaa 92,36,124\nD 28,68,s91,3072\nMvaa 88,32,124\ns_cmp_eq_u32 s23, 2\ns_cselect_b32 s29, s2, s29\ns_cselect_b32 s30, s101, s30\ns_cselect_b32 s7, 0xffff, 0\n" \
"s_cselect_b32 s9, 0xff, 0\nMvaa 152,40,124\ns_and_b32 s101, s10, 8\ns_lshl_b32 s9, s9, s101\nMvaa 156,44,124\ns_add_u32 m0, s88, 0\nv_add_u32 v13, 128, v13\nMvaa 148,44,120\n" \
"D 13,64,0,0\nMvaa 144,40,120\ns_mov_b32 s2, zqS-zq0-8192\nMvaa 136,40,116\ns_cmp_eq_u32 s23, 1\ns_cselect_b32 s2, zqR-zq0-8192, s2\nMvaa 140,44,116\ns_cmp_eq_u32 s23, 0\n" \
"s_cselect_b32 s2, zqT-zq0-8192, s2\nMvaa 132,44,112\nD 13,64,s73,1024\nMvaa 128,40,112\ns_cmp_lt_u32 s32, 0x2000\ns_cselect_b32 s2, 0, s2\nMvaa 120,40,108\ns_add_u32 s2, s2, s32\n" \
"Mvaa 124,44,108\nMvaa 116,44,104\nD 13,64,s74,2048\nMvaa 112,40,104\nMvaa 104,40,100\nMvaa 108,44,100\nMvaa 100,44,96\nD 13,64,s75,3072\n" \
"Mvaa 96,40,96\ns_waitcnt vmcnt(17)\ns_barrier\nMvaa 160,48,96\nbuffer_load_dword v30, v4, s[52:55], 0 offen sc1\nTa 64,8,32768\nMvaa 164,52,96\nTa 66,8,33792\n" \
"Mvaa 172,52,100\nRa 0,7,32768\nMvaa 168,48,100\nTa 68,20,32768\nMvaa 176,48,104\nTa 70,20,33792\nMvaa 180,52,104\nRa 4,7,34816\n" \
"Mvaa 188,52,108\nv_add_u32 v4, s44, v4\nTa 72,21,32768\nMvaa 184,48,108\nTa 74,21,33792\nMvaa 192,48,112\nRa 8,7,36864\nMvaa 196,52,112\n" \
"Ta 76,22,32768\nMvaa 204,52,116\nTa 78,22,33792\nMvaa 200,48,116\nRa 12,7,38912\nMvaa 208,48,120\nTa 80,23,32768\nMvaa 212,52,120\n" \
"Ta 82,23,33792\nMvaa 220,52,124\nRa 16,7,40960\nMvaa 216,48,124\nTa 84,24,32768\nMaaa 152,56,124\nTa 86,24,33792\nMaaa 156,60,124\n" \
"Ra 20,7,43008\nMaaa 148,60,120\nTa 88,25,32768\nMaaa 144,56,120\nTa 90,25,33792\nMaaa 136,56,116\nRa 24,7,45056\nMaaa 140,60,116\n" \
"Ta 92,26,32768\nMaaa 132,60,112\nTa 94,26,33792\nMaaa 128,56,112\nRa 28,7,47104\nMvaa 248,56,108\nMvaa 252,60,108\nMvaa 244,60,104\n" \
"Mvaa 240,56,104\nMvaa 232,56,100\nMvaa 236,60,100\nMvaa 228,60,96\nMvaa 224,56,96\ns_waitcnt lgkmcnt(0)\nMvaa 32,0,64\nTa 96,8,40960\n" \
"Mvaa 36,4,64\nTa 98,8,41984\nMvaa 44,4,68\nRa 32,27,32768\nMvaa 40,0,68\nTa 100,20,40960\nMvaa 48,0,72\nTa 102,20,41984\n" \
"Mvaa 52,4,72\nRa 36,27,34816\nMvaa 60,4,76\nTa 104,21,40960\nMvaa 56,0,76\nTa 106,21,41984\nMvaa 64,0,80\nRa 40,27,36864\n" \
"Mvaa 68,4,80\nTa 108,22,40960\nMvaa 76,4,84\nTa 110,22,41984\nMvaa 72,0,84\nRa 44,27,38912\nMvaa 80,0,88\nTa 112,23,40960\n" \
"Mvaa 84,4,88\nTa 114,23,41984\nMvaa 92,4,92\nRa 48,27,40960\nMvaa 88,0,92\nTa 116,24,40960\nMvaa 152,8,92\nTa 118,24,41984\n" \
"Mvaa 156,12,92\nRa 52,27,43008\nMvaa 148,12,88\nTa 120,25,40960\nMvaa 144,8,88\nTa 122,25,41984\nMvaa 136,8,84\nRa 56,27,45056\n" \
"Mvaa 140,12,84\nTa 124,26,40960\nMvaa 132,12,80\nTa 126,26,41984\nMvaa 128,8,80\nRa 60,27,47104\nMvaa 120,8,76\ns_and_b32 s96, s32, 0x1000\n" \
"s_lshl_b32 s97, s96, 1\ns_lshl_b32 s96, s96, 2\nMvaa 124,12,76\nv_add_u32 v16, s97, v7\nv_subrev_u32 v20, s96, v8\nv_xor_b32 v17, 64, v16\nMvaa 116,12,72\nv_add_u32 v18, 0x20000, v16\n" \
"v_add_u32 v19, 0x20000, v17\nv_xor_b32 v21, 32, v20\nMvaa 112,8,72\nv_xor_b32 v22, 64, v20\nv_xor_b32 v23, 96, v20\nv_xor_b32 v24, 128, v20\nMvaa 104,8,68\nv_xor_b32 v25, 160, v20\n" \
"v_xor_b32 v26, 192, v20\nv_xor_b32 v27, 224, v20\nMvaa 108,12,68\nMvaa 100,12,64\nMvaa 96,8,64\nMvaa 160,16,64\ns_waitcnt vmcnt(5) lgkmcnt(0)\ns_barrier\n" \
"Mvaa 164,20,64\ns_add_u32 m0, s88, 4096\nMvaa 172,20,68\nD 13,64,s76,0\nMvaa 168,16,68\nTa 160,20,24576\nMvaa 176,16,72\nTa 162,20,25600\n" \
"Mvaa 180,20,72\nD 13,64,s77,1024\nMvaa 188,20,76\nTa 164,21,24576\nMvaa 184,16,76\nTa 166,21,25600\nMvaa 192,16,80\nD 13,64,s78,2048\n" \
"Mvaa 196,20,80\nTa 168,22,24576\nMvaa 204,20,84\nTa 170,22,25600\nMvaa 200,16,84\nD 13,64,s79,3072\nMvaa 208,16,88\nTa 172,23,24576\n" \
"Mvaa 212,20,88\ns_add_u32 m0, s32, 65536\nv_add_u32 v14, s72, v14\nv_add_u32 v28, s72, v28\nMvaa 220,20,92\nD 14,68,0,0\nMvaa 216,16,92\nTa 174,23,25600\n" \
"Maaa 152,24,92\nTa 176,24,24576\nMaaa 156,28,92\nD 14,68,s89,1024\nMaaa 148,28,88\nTa 178,24,25600\nMaaa 144,24,88\nTa 180,25,24576\n" \
"Maaa 136,24,84\nD 28,68,s90,2048\nMaaa 140,28,84\nTa 182,25,25600\nMaaa 132,28,80\nTa 184,26,24576\nMaaa 128,24,80\nD 28,68,s91,3072\n" \
"Mvaa 248,24,76\nTa 186,26,25600\nMvaa 252,28,76\ns_add_u32 m0, s88, 32768\nv_add_u32 v13, 128, v13\nMvaa 244,28,72\nD 13,64,0,0\nMvaa 240,24,72\n" \
"Ta 188,27,24576\nMvaa 232,24,68\nTa 190,27,25600\nMvaa 236,28,68\nD 13,64,s73,1024\nMvaa 228,28,64\nMvaa 224,24,64\nMvaa 32,32,96\n" \
"D 13,64,s74,2048\nMvaa 36,36,96\nTa 64,20,16384\nMvaa 40,32,100\nTa 66,20,17408\nMvaa 44,36,100\nD 13,64,s75,3072\nMvaa 48,32,104\n" \
"Mvaa 52,36,104\ns_add_u32 m0, s88, 36864\nMvaa 56,32,108\nD 13,64,s76,0\nMvaa 60,36,108\nRa 0,18,0\nMvaa 64,32,112\nMvaa 68,36,112\n" \
"D 13,64,s77,1024\nMvaa 72,32,116\nRa 16,19,0\nC 32,32,33\nC 33,34,35\nMvaa 76,36,116\nC 34,36,37\nC 35,38,39\n" \
"C 36,40,41\nMvaa 80,32,120\nD 13,64,s78,2048\nC 37,42,43\nMvaa 84,36,120\nTa 68,21,16384\nC 38,44,45\nC 39,46,47\n" \
"Mvaa 88,32,124\nC 40,48,49\nC 41,50,51\nC 42,52,53\nMvaa 92,36,124\nD 13,64,s79,3072\nC 43,54,55\nMvaa 96,40,96\n" \
"Ta 70,21,17408\nC 44,56,57\nC 45,58,59\nMvaa 100,44,96\ns_add_u32 m0, s32, 98304\nv_add_u32 v14, s72, v14\nv_add_u32 v28, s72, v28\nMvaa 104,40,100\n" \
"D 14,68,0,0\nC 46,60,61\nMvaa 108,44,100\nC 47,62,63\nC 48,64,65\nC 49,66,67\nMvaa 112,40,104\nTa 72,22,16384\n" \
"C 50,68,69\nC 51,70,71\nMvaa 116,44,104\nD 14,68,s89,1024\nC 52,72,73\nMvaa 120,40,108\nTa 74,22,17408\nC 53,74,75\n" \
"C 54,76,77\nMvaa 124,44,108\nC 55,78,79\nC 56,80,81\nC 57,82,83\nMvaa 128,40,112\nD 28,68,s90,2048\nC 58,84,85\n" \
"Mvaa 132,44,112\nTa 76,23,16384\nC 59,86,87\nC 60,88,89\nMvaa 136,40,116\nC 61,90,91\nC 62,92,93\nC 63,94,95\n" \
"Mvaa 140,44,116\nD 28,68,s91,3072\nC 64,96,97\nMvaa 144,40,120\nTa 78,23,17408\nC 65,98,99\nC 66,100,101\nMvaa 148,44,120\n" \
"C 67,102,103\nC 68,104,105\nC 69,106,107\nMvaa 152,40,124\nbuffer_load_dword v30, v4, s[52:55], 0 offen sc1\nC 70,108,109\nMvaa 156,44,124\nTa 80,24,16384\n" \
"C 71,110,111\nC 72,112,113\nMaaa 128,56,112\nC 73,114,115\nC 74,116,117\nC 75,118,119\nMaaa 132,60,112\nTa 82,24,17408\n" \
"C 76,120,121\nC 77,122,123\nMaaa 136,56,116\nC 78,124,125\nC 79,126,127\nC 80,128,129\nMaaa 140,60,116\nTa 84,25,16384\n" \
"C 81,130,131\nC 82,132,133\nMaaa 144,56,120\nv_add_u32 v4, s44, v4\nC 83,134,135\nC 84,136,137\nMaaa 148,60,120\nTa 86,25,17408\n" \
"C 85,138,139\nC 86,140,141\ns_branch 99f\n.p2align 12\nzp25:\ns_setpc_b64 s[98:99]\n99:\nMaaa 152,56,124\n" \
"C 87,142,143\nC 88,144,145\nC 89,146,147\nMaaa 156,60,124\nTa 88,26,16384\nC 90,148,149\nC 91,150,151\nMvaa 160,48,96\n" \
"C 92,152,153\nC 93,154,155\nC 94,156,157\nMvaa 164,52,96\nTa 90,26,17408\nC 95,158,159\nMvaa 168,48,100\nAR 96,128\n" \
"AR 97,129\nAR 98,130\nMvaa 172,52,100\nTa 92,27,16384\nAR 99,131\nAR 100,132\nMvaa 176,48,104\nAR 101,133\n" \
"AR 102,134\nAR 103,135\nMvaa 180,52,104\nTa 94,27,17408\nC 96,96,97\nC 97,98,99\nMvaa 184,48,108\nC 98,100,101\n" \
"C 99,102,103\nAW 240,96\nMvaa 188,52,108\nRa 4,18,2048\nAW 241,97\nAW 242,98\nMvaa 192,48,112\nAW 243,99\n" \
"AR 104,136\nAR 105,137\nMvaa 196,52,112\nRa 20,19,2048\nAR 106,138\nAR 107,139\nMvaa 200,48,116\nAR 108,140\n" \
"AR 109,141\nAR 110,142\nMvaa 204,52,116\nRa 8,18,4096\nAR 111,143\nC 104,104,105\nMvaa 208,48,120\nC 105,106,107\n" \
"C 106,108,109\nC 107,110,111\nMvaa 212,52,120\nRa 24,19,4096\nAW 244,104\nAW 245,105\nMvaa 216,48,124\nAW 246,106\n" \
"AW 247,107\nAR 112,144\nMvaa 220,52,124\nRa 12,18,6144\nAR 113,145\nAR 114,146\nMvaa 224,56,96\nAR 115,147\n" \
"AR 116,148\nAR 117,149\nMvaa 228,60,96\nRa 28,19,6144\nAR 118,150\nAR 119,151\nMvaa 232,56,100\nC 112,112,113\n" \
"C 113,114,115\nC 114,116,117\nMvaa 236,60,100\nC 115,118,119\nAW 248,112\nAW 249,113\nMvaa 240,56,104\nAW 250,114\n" \
"AW 251,115\nAR 120,152\nMvaa 244,60,104\nAR 121,153\nAR 122,154\nAR 123,155\nMvaa 248,56,108\nAR 124,156\n" \
"AR 125,157\nAR 126,158\nMvaa 252,60,108\nAR 127,159\nC 120,120,121\nC 121,122,123\ns_waitcnt lgkmcnt(6)\ns_branch zY\n" \
"zxV:\nMvaa 32,0,64\nTa 96,8,8192\ns_mov_b64 s[64:65], s[80:81]\nMvaa 36,4,64\nTa 98,8,9216\ns_mov_b32 s66, 4194304\nMvaa 44,4,68\n" \
"Ra 32,27,0\ns_mov_b64 s[68:69], s[82:83]\nMvaa 40,0,68\nTa 100,20,8192\ns_mov_b32 s70, 6291456\nMvaa 48,0,72\nTa 102,20,9216\ns_mov_b32 s73, 15360\n" \
"Mvaa 52,4,72\nRa 36,27,2048\ns_mov_b32 s74, 30720\nMvaa 60,4,76\nTa 104,21,8192\ns_mov_b32 s75, 46080\nMvaa 56,0,76\nTa 106,21,9216\n" \
"s_mov_b32 s76, 65536\nMvaa 64,0,80\nRa 40,27,4096\ns_mov_b32 s77, 80896\nMvaa 68,4,80\nTa 108,22,8192\ns_mov_b32 s78, 96256\nMvaa 76,4,84\n" \
"Ta 110,22,9216\ns_mov_b32 s79, 111616\nMvaa 72,0,84\nRa 44,27,6144\ns_mov_b32 s89, 23552\nMvaa 80,0,88\nTa 112,23,8192\ns_mov_b32 s90, 47104\n" \
"Mvaa 84,4,88\nTa 114,23,9216\ns_mov_b32 s91, 70656\nMvaa 92,4,92\nRa 48,27,8192\ns_mov_b32 s72, 393216\nMvaa 88,0,92\nTa 116,24,8192\n" \
"s_lshr_b32 s92, s32, 1\nMvaa 152,8,92\nTa 118,24,9216\nMvaa 156,12,92\nRa 52,27,10240\nMvaa 148,12,88\nTa 120,25,8192\nMvaa 144,8,88\n" \
"Ta 122,25,9216\nMvaa 136,8,84\nRa 56,27,12288\nMvaa 140,12,84\nTa 124,26,8192\nMvaa 132,12,80\nTa 126,26,9216\nMvaa 128,8,80\n" \
"Ra 60,27,14336\nMvaa 120,8,76\nMvaa 124,12,76\nMvaa 116,12,72\nMvaa 112,8,72\nMvaa 104,8,68\nMvaa 108,12,68\nMvaa 100,12,64\n" \
"Mvaa 96,8,64\nMvaa 160,16,64\ns_waitcnt lgkmcnt(0)\ns_barrier\nMvaa 164,20,64\ns_add_u32 m0, s88, 0\ns_and_b32 s10, s3, 2\nMvaa 172,20,68\n" \
"D 9,64,0,0\nMvaa 168,16,68\ns_bfe_u32 s100, s3, 0x10002\ns_lshl_b32 s10, s10, 1\nMvaa 176,16,72\ns_bfe_u32 s2, s9, 0x10010\ns_xor_b32 s99, s2, s3\nMvaa 180,20,72\n" \
"s_and_b32 s99, s99, 1\ns_lshl1_add_u32 s99, s100, s99\nMvaa 188,20,76\nD 9,64,s73,1024\nMvaa 184,16,76\ns_bfe_u32 s101, s3, 0x20003\ns_lshl3_add_u32 s11, s99, s101\nMvaa 192,16,80\n" \
"s_add_u32 s101, s101, s10\ns_lshl3_add_u32 s10, s2, s10\nMvaa 196,20,80\ns_bfe_u32 s2, s3, 0x20005\ns_lshl1_add_u32 s2, s2, s100\nMvaa 204,20,84\nD 9,64,s74,2048\nMvaa 200,16,84\n" \
"s_bfe_u32 s23, s9, 0x80008\ns_and_b32 s22, s9, 255\nMvaa 208,16,88\ns_lshr_b32 s6, 4, s99\ns_lshr_b32 s25, s3, 5\nMvaa 212,20,88\ns_lshl_b32 s4, s25, 8\ns_lshl_b32 s18, s25, 21\n" \
"Mvaa 220,20,92\nD 9,64,s75,3072\nMvaa 216,16,92\ns_lshl3_add_u32 s25, s25, s101\ns_lshl_b32 s26, s25, 18\nMaaa 152,24,92\ns_lshl_b32 s27, s25, 7\ns_add_u32 s4, s4, 16384\n" \
"Maaa 156,28,92\ns_add_u32 m0, s88, 4096\ns_xor_b32 s25, s11, s10\nMaaa 148,28,88\nD 9,64,s76,0\nMaaa 144,24,88\ns_lshl3_add_u32 s31, s25, s4\ns_lshl_b32 s5, s2, 7\n" \
"Maaa 136,24,84\ns_add_u32 s5, s5, 8192\ns_lshr_b32 s25, s32, 1\nMaaa 140,28,84\ns_add_u32 s18, s18, s25\ns_add_u32 s18, s18, 54854656\nMaaa 132,28,80\nD 9,64,s77,1024\n" \
"Maaa 128,24,80\ns_lshl_b32 s19, s2, 19\ns_add_u32 s19, s19, s25\nMvaa 248,24,76\ns_add_u32 s19, s19, 48563200\ns_lshr_b32 s25, s32, 7\nMvaa 252,28,76\ns_add_u32 s27, s27, s25\n" \
"s_lshl3_add_u32 s27, s99, s27\nMvaa 244,28,72\nD 9,64,s78,2048\nMvaa 240,24,72\ns_add_u32 s27, s27, 32768\ns_lshl1_add_u32 s26, s32, s26\nMvaa 232,24,68\ns_add_u32 s26, s26, 71631872\n" \
"s_lshl_b32 s28, s99, 15\nMvaa 236,28,68\ns_add_u32 s24, s26, s28\ns_sub_u32 s28, 0x10000, s28\nMvaa 228,28,64\nD 9,64,s79,3072\nMvaa 224,24,64\ns_lshl_b32 s25, s100, 17\n" \
"s_add_u32 s26, s26, s25\nMvaa 32,32,96\ns_cmp_eq_u32 s99, 0\ns_cselect_b32 s21, 128, 0\nMvaa 36,36,96\ns_add_u32 m0, s32, 65536\ns_lshl_b32 s25, s21, 10\nMvaa 44,36,100\n" \
"D 10,68,0,0\nMvaa 40,32,100\ns_add_u32 s24, s24, s25\ns_add_u32 s25, s24, 0x20000\nMvaa 48,32,104\ns_lshl_b32 s101, s101, 8\ns_and_b32 s20, s32, 0x2000\nMvaa 52,36,104\n" \
"s_lshl3_add_u32 s20, s20, s101\ns_and_b32 s99, s32, 0x1000\nMvaa 60,36,108\nD 10,68,s89,1024\nMvaa 56,32,108\ns_lshl2_add_u32 s20, s99, s20\ns_lshl4_add_u32 s101, s32, s101\nMvaa 64,32,112\n" \
"s_lshl_b32 s2, s2, 20\ns_add_u32 s2, s2, s101\nMvaa 68,36,112\ns_lshl_b32 s100, s100, 7\ns_add_u32 s101, s101, s100\nMvaa 76,36,116\nD 11,68,s90,2048\nMvaa 72,32,116\n" \
"s_add_u32 s101, s101, 2048\ns_lshl_b32 s29, s42, 17\nMvaa 80,32,120\ns_lshl_b32 s30, s43, 17\ns_add_u32 s29, s29, s101\nMvaa 84,36,120\ns_add_u32 s30, s30, s101\ns_add_u32 s101, s2, 524288\n" \
"Mvaa 92,36,124\nD 11,68,s91,3072\nMvaa 88,32,124\ns_cmp_eq_u32 s23, 2\ns_cselect_b32 s29, s2, s29\ns_cselect_b32 s30, s101, s30\ns_cselect_b32 s7, 0xffff, 0\ns_cselect_b32 s9, 0xff, 0\n" \
"Mvaa 152,40,124\ns_and_b32 s101, s10, 8\ns_lshl_b32 s9, s9, s101\nMvaa 156,44,124\ns_add_u32 m0, s92, 81920\ns_mov_b32 s2, zqS-zq0-8192\nMvaa 148,44,120\nD 12,68,0,0\n" \
"Mvaa 144,40,120\ns_cmp_eq_u32 s23, 1\ns_cselect_b32 s2, zqR-zq0-8192, s2\nMvaa 136,40,116\ns_cmp_eq_u32 s23, 0\ns_cselect_b32 s2, zqT-zq0-8192, s2\nMvaa 140,44,116\ns_cmp_lt_u32 s32, 0x2000\n" \
"s_cselect_b32 s2, 0, s2\nMvaa 132,44,112\nD 12,68,s89,1024\nMvaa 128,40,112\ns_add_u32 s2, s2, s32\nMvaa 120,40,108\nMvaa 124,44,108\nMvaa 116,44,104\n" \
"Mvaa 112,40,104\nMvaa 104,40,100\nMvaa 108,44,100\nMvaa 100,44,96\nbuffer_load_dword v30, v4, s[52:55], 0 offen sc1\nMvaa 96,40,96\ns_waitcnt vmcnt(16)\ns_barrier\n" \
"Mvaa 160,48,96\nTa 64,8,32768\nv_add_u32 v9, 128, v9\nMvaa 164,52,96\nTa 66,8,33792\nv_add_u32 v10, s72, v10\nMvaa 172,52,100\nRa 0,7,32768\n" \
"v_add_u32 v11, s72, v11\nMvaa 168,48,100\nTa 68,20,32768\nv_add_u32 v12, s72, v12\nMvaa 176,48,104\nv_add_u32 v4, s44, v4\nTa 70,20,33792\nMvaa 180,52,104\n" \
"Ra 4,7,34816\nMvaa 188,52,108\nTa 72,21,32768\nMvaa 184,48,108\nTa 74,21,33792\nMvaa 192,48,112\nRa 8,7,36864\nMvaa 196,52,112\n" \
"Ta 76,22,32768\nMvaa 204,52,116\nTa 78,22,33792\nMvaa 200,48,116\nRa 12,7,38912\nMvaa 208,48,120\nTa 80,23,32768\nMvaa 212,52,120\n" \
"Ta 82,23,33792\nMvaa 220,52,124\nRa 16,7,40960\nMvaa 216,48,124\nTa 84,24,32768\nMaaa 152,56,124\nTa 86,24,33792\nMaaa 156,60,124\n" \
"Ra 20,7,43008\nMaaa 148,60,120\nTa 88,25,32768\nMaaa 144,56,120\nTa 90,25,33792\nMaaa 136,56,116\nRa 24,7,45056\nMaaa 140,60,116\n" \
"Ta 92,26,32768\nMaaa 132,60,112\nTa 94,26,33792\nMaaa 128,56,112\nRa 28,7,47104\nMvaa 248,56,108\nMvaa 252,60,108\nMvaa 244,60,104\n" \
"Mvaa 240,56,104\nMvaa 232,56,100\nMvaa 236,60,100\nMvaa 228,60,96\nMvaa 224,56,96\ns_waitcnt lgkmcnt(0)\nMvaa 32,0,64\nTa 96,8,40960\n" \
"Mvaa 36,4,64\nTa 98,8,41984\nMvaa 44,4,68\nRa 32,27,32768\nMvaa 40,0,68\nTa 100,20,40960\nMvaa 48,0,72\nTa 102,20,41984\n" \
"Mvaa 52,4,72\nRa 36,27,34816\nMvaa 60,4,76\nTa 104,21,40960\nMvaa 56,0,76\nTa 106,21,41984\nMvaa 64,0,80\nRa 40,27,36864\n" \
"Mvaa 68,4,80\nTa 108,22,40960\nMvaa 76,4,84\nTa 110,22,41984\nMvaa 72,0,84\nRa 44,27,38912\nMvaa 80,0,88\nTa 112,23,40960\n" \
"Mvaa 84,4,88\nTa 114,23,41984\nMvaa 92,4,92\nRa 48,27,40960\nMvaa 88,0,92\nTa 116,24,40960\nMvaa 152,8,92\nTa 118,24,41984\n" \
"Mvaa 156,12,92\nRa 52,27,43008\nMvaa 148,12,88\nTa 120,25,40960\nMvaa 144,8,88\nTa 122,25,41984\nMvaa 136,8,84\nRa 56,27,45056\n" \
"Mvaa 140,12,84\nTa 124,26,40960\nMvaa 132,12,80\nTa 126,26,41984\nMvaa 128,8,80\nRa 60,27,47104\nMvaa 120,8,76\ns_and_b32 s96, s32, 0x1000\n" \
"s_lshr_b32 s97, s96, 6\nMvaa 124,12,76\ns_lshl_b32 s96, s96, 2\nv_subrev_u32 v20, s96, v8\nMvaa 116,12,72\nv_xor_b32 v20, s97, v20\nv_xor_b32 v21, 32, v20\nMvaa 112,8,72\n" \
"v_xor_b32 v22, 0x80, v20\nv_xor_b32 v23, 0xa0, v20\nMvaa 104,8,68\nMvaa 108,12,68\nMvaa 100,12,64\nMvaa 96,8,64\nMvaa 160,16,64\ns_waitcnt lgkmcnt(0)\n" \
"s_barrier\nMvaa 164,20,64\ns_add_u32 m0, s88, 32768\nMvaa 172,20,68\nD 9,64,0,0\nMvaa 168,16,68\nMvaa 176,16,72\nMvaa 180,20,72\n" \
"Mvaa 188,20,76\nD 9,64,s73,1024\nMvaa 184,16,76\nMvaa 192,16,80\nMvaa 196,20,80\nMvaa 204,20,84\nD 9,64,s74,2048\nMvaa 200,16,84\n" \
"Mvaa 208,16,88\nMvaa 212,20,88\nMvaa 220,20,92\nD 9,64,s75,3072\nMvaa 216,16,92\nMaaa 152,24,92\nMaaa 156,28,92\ns_add_u32 m0, s88, 36864\n" \
"Maaa 148,28,88\nD 9,64,s76,0\nMaaa 144,24,88\nMaaa 136,24,84\nMaaa 140,28,84\nMaaa 132,28,80\nD 9,64,s77,1024\nMaaa 128,24,80\n" \
"Mvaa 248,24,76\nMvaa 252,28,76\nMvaa 244,28,72\nD 9,64,s78,2048\nMvaa 240,24,72\nMvaa 232,24,68\nMvaa 236,28,68\nMvaa 228,28,64\n" \
"D 9,64,s79,3072\nMvaa 224,24,64\nMvaa 32,32,96\nMvaa 36,36,96\ns_add_u32 m0, s32, 98304\nMvaa 40,32,100\nD 10,68,0,0\nMvaa 44,36,100\n" \
"Mvaa 48,32,104\nMvaa 52,36,104\nMvaa 56,32,108\nD 10,68,s89,1024\nMvaa 60,36,108\nMvaa 64,32,112\nMvaa 68,36,112\nMvaa 72,32,116\n" \
"D 11,68,s90,2048\nC 32,32,33\nMvaa 76,36,116\nC 33,34,35\nC 34,36,37\nC 35,38,39\nMvaa 80,32,120\nC 36,40,41\n" \
"C 37,42,43\nC 38,44,45\nMvaa 84,36,120\nC 39,46,47\nC 40,48,49\nC 41,50,51\nMvaa 88,32,124\nD 11,68,s91,3072\n" \
"C 42,52,53\nMvaa 92,36,124\nC 43,54,55\nC 44,56,57\nC 45,58,59\ns_branch 99f\n.p2align 12\nzgx:\n" \
"zp26:\ns_setpc_b64 s[98:99]\n99:\nMvaa 96,40,96\nC 46,60,61\nC 47,62,63\nMvaa 100,44,96\ns_add_u32 m0, s92, 114688\n" \
"Mvaa 104,40,100\nD 12,68,0,0\nC 48,64,65\nMvaa 108,44,100\nC 49,66,67\nC 50,68,69\nC 51,70,71\nMvaa 112,40,104\n" \
"C 52,72,73\nC 53,74,75\nC 54,76,77\nMvaa 116,44,104\nC 55,78,79\nC 56,80,81\nC 57,82,83\nMvaa 120,40,108\n" \
"D 12,68,s89,1024\nC 58,84,85\nMvaa 124,44,108\nC 59,86,87\nC 60,88,89\nC 61,90,91\nMvaa 128,40,112\nC 62,92,93\n" \
"C 63,94,95\nMvaa 132,44,112\nMvaa 136,40,116\nC 96,96,97\nC 97,98,99\nC 98,100,101\nMvaa 140,44,116\nC 99,102,103\n" \
"AW 160,96\nAW 161,97\nMvaa 144,40,120\nAW 162,98\nAW 163,99\nC 104,104,105\nMvaa 148,44,120\nC 105,106,107\n" \
"C 106,108,109\nC 107,110,111\nMvaa 152,40,124\nbuffer_load_dword v30, v4, s[52:55], 0 offen sc1\nAW 164,104\nMvaa 156,44,124\ns_waitcnt vmcnt(16)\ns_barrier\n" \
"AW 165,105\nMvaa 160,48,96\nTa 64,20,0\nv_add_u32 v9, 128, v9\nAW 166,106\nMvaa 164,52,96\nTa 66,20,1024\nv_add_u32 v10, s72, v10\n" \
"AW 167,107\nMvaa 168,48,100\nRa 0,7,0\nv_add_u32 v11, s72, v11\nC 112,112,113\nMvaa 172,52,100\nTa 68,21,0\nv_add_u32 v12, s72, v12\n" \
"C 113,114,115\nMvaa 176,48,104\nv_add_u32 v4, s44, v4\nTa 70,21,1024\nC 114,116,117\nMvaa 180,52,104\nRa 4,7,2048\nC 115,118,119\n" \
"AW 168,112\nMvaa 184,48,108\nTa 72,22,0\nAW 169,113\nAW 170,114\nMvaa 188,52,108\nTa 74,22,1024\nAW 171,115\n" \
"C 120,120,121\nMvaa 192,48,112\nRa 8,7,4096\nC 121,122,123\nC 122,124,125\nMvaa 196,52,112\nTa 76,23,0\nC 123,126,127\n" \
"AW 172,120\nMvaa 200,48,116\nTa 78,23,1024\nAW 173,121\nAW 174,122\nMvaa 204,52,116\nRa 12,7,6144\nAW 175,123\n" \
"C 128,128,129\nMvaa 208,48,120\nTa 80,20,16384\nC 129,130,131\nC 130,132,133\nMvaa 212,52,120\nTa 82,20,17408\nC 131,134,135\n" \
"AW 176,128\nMvaa 216,48,124\nRa 16,7,8192\nAW 177,129\nAW 178,130\nMvaa 220,52,124\nTa 84,21,16384\nAW 179,131\n" \
"C 136,136,137\nMvaa 224,56,96\nTa 86,21,17408\nC 137,138,139\nC 138,140,141\nMvaa 228,60,96\nRa 20,7,10240\nC 139,142,143\n" \
"AW 180,136\nMvaa 232,56,100\nRa 24,7,12288\nAW 181,137\nAW 182,138\nMvaa 236,60,100\nRa 28,7,14336\nAW 183,139\n" \
"C 144,144,145\nMvaa 240,56,104\nC 145,146,147\nC 146,148,149\nC 147,150,151\nMvaa 244,60,104\nAW 184,144\nAW 185,145\n" \
"AW 186,146\nMvaa 248,56,108\nAW 187,147\nC 152,152,153\nC 153,154,155\nMvaa 252,60,108\nC 154,156,157\nC 155,158,159\n" \
"AW 188,152\nMaaa 128,56,112\nAW 189,153\nAW 190,154\nAW 191,155\nMaaa 132,60,112\nC 160,160,161\nC 161,162,163\n" \
"C 162,164,165\nMaaa 136,56,116\nC 163,166,167\nAW 192,160\nAW 193,161\nMaaa 140,60,116\nAW 194,162\nAW 195,163\n" \
"C 168,168,169\nMaaa 144,56,120\nC 169,170,171\nC 170,172,173\nC 171,174,175\nMaaa 148,60,120\nAW 196,168\nAW 197,169\n" \
"AW 198,170\nMaaa 152,56,124\nAW 199,171\nC 176,176,177\nC 177,178,179\nMaaa 156,60,124\nC 178,180,181\nC 179,182,183\n" \
"AW 200,176\ns_waitcnt lgkmcnt(0)\ns_branch zA\nzxK:\nMvaa 32,64,0\nTa 96,8,8192\ns_mov_b64 s[64:65], s[80:81]\nMvaa 64,64,4\n" \
"Ta 98,8,9216\ns_mov_b32 s66, 4194304\nMvaa 68,68,4\nRa 32,27,0\ns_mov_b64 s[68:69], s[82:83]\nMvaa 36,68,0\nTa 100,20,8192\ns_mov_b32 s70, 6291456\n" \
"Mvaa 40,72,0\nTa 102,20,9216\ns_mov_b32 s73, 15360\nMvaa 72,72,4\nRa 36,27,2048\ns_mov_b32 s74, 30720\nMvaa 76,76,4\nTa 104,21,8192\n" \
"s_mov_b32 s75, 46080\nMvaa 44,76,0\nTa 106,21,9216\ns_mov_b32 s76, 65536\nMvaa 48,80,0\nRa 40,27,4096\ns_mov_b32 s77, 80896\nMvaa 80,80,4\n" \
"Ta 108,22,8192\ns_mov_b32 s78, 96256\nMvaa 84,84,4\nTa 110,22,9216\ns_mov_b32 s79, 111616\nMvaa 52,84,0\nRa 44,27,6144\ns_mov_b32 s89, 23552\n" \
"Mvaa 56,88,0\nTa 112,23,8192\ns_mov_b32 s90, 47104\nMvaa 88,88,4\nTa 114,23,9216\ns_mov_b32 s91, 70656\nMvaa 92,92,4\nRa 48,27,8192\n" \
"s_mov_b32 s72, 393216\nMvaa 60,92,0\nTa 116,24,8192\ns_lshr_b32 s92, s32, 1\nMvaa 124,92,8\nTa 118,24,9216\nMvaa 156,92,12\nRa 52,27,10240\n" \
"Mvaa 152,88,12\nTa 120,25,8192\nMvaa 120,88,8\nTa 122,25,9216\nMvaa 116,84,8\nRa 56,27,12288\nMvaa 148,84,12\nTa 124,26,8192\n" \
"Mvaa 144,80,12\nTa 126,26,9216\nMvaa 112,80,8\nRa 60,27,14336\nMvaa 108,76,8\nMvaa 140,76,12\nMvaa 136,72,12\nMvaa 104,72,8\n" \
"Mvaa 100,68,8\nMvaa 132,68,12\nMvaa 128,64,12\nMvaa 96,64,8\nMvaa 160,64,16\ns_waitcnt lgkmcnt(0)\ns_barrier\nMvaa 192,64,20\n" \
"s_add_u32 m0, s88, 0\ns_and_b32 s10, s3, 2\nMvaa 196,68,20\nD 9,64,0,0\nMvaa 164,68,16\ns_bfe_u32 s100, s3, 0x10002\ns_lshl_b32 s10, s10, 1\nMvaa 168,72,16\n" \
"s_bfe_u32 s2, s9, 0x10010\ns_xor_b32 s99, s2, s3\nMvaa 200,72,20\ns_and_b32 s99, s99, 1\ns_lshl1_add_u32 s99, s100, s99\nMvaa 204,76,20\nD 9,64,s73,1024\nMvaa 172,76,16\n" \
"s_bfe_u32 s101, s3, 0x20003\ns_lshl3_add_u32 s11, s99, s101\nMvaa 176,80,16\ns_add_u32 s101, s101, s10\ns_lshl3_add_u32 s10, s2, s10\nMvaa 208,80,20\ns_bfe_u32 s2, s3, 0x20005\ns_lshl1_add_u32 s2, s2, s100\n" \
"Mvaa 212,84,20\nD 9,64,s74,2048\nMvaa 180,84,16\ns_bfe_u32 s23, s9, 0x80008\ns_and_b32 s22, s9, 255\nMvaa 184,88,16\ns_lshr_b32 s6, 4, s99\ns_lshr_b32 s25, s3, 5\n" \
"Mvaa 216,88,20\ns_lshl_b32 s4, s25, 8\ns_lshl_b32 s18, s25, 21\nMvaa 220,92,20\nD 9,64,s75,3072\nMvaa 188,92,16\ns_lshl3_add_u32 s25, s25, s101\ns_lshl_b32 s26, s25, 18\n" \
"Mvaa 252,92,24\ns_lshl_b32 s27, s25, 7\ns_add_u32 s4, s4, 16384\nMaaa 156,92,28\ns_add_u32 m0, s88, 4096\ns_xor_b32 s25, s11, s10\nMaaa 152,88,28\nD 9,64,s76,0\n" \
"Mvaa 248,88,24\ns_lshl3_add_u32 s31, s25, s4\ns_lshl_b32 s5, s2, 7\nMvaa 244,84,24\ns_add_u32 s5, s5, 8192\ns_lshr_b32 s25, s32, 1\nMaaa 148,84,28\ns_add_u32 s18, s18, s25\n" \
"s_add_u32 s18, s18, 54854656\nMaaa 144,80,28\nD 9,64,s77,1024\nMvaa 240,80,24\ns_lshl_b32 s19, s2, 19\ns_add_u32 s19, s19, s25\nMvaa 236,76,24\ns_add_u32 s19, s19, 48563200\n" \
"s_lshr_b32 s25, s32, 7\nMaaa 140,76,28\ns_add_u32 s27, s27, s25\ns_lshl3_add_u32 s27, s99, s27\nMaaa 136,72,28\nD 9,64,s78,2048\nMvaa 232,72,24\ns_add_u32 s27, s27, 32768\n" \
"s_lshl1_add_u32 s26, s32, s26\nMvaa 228,68,24\ns_add_u32 s26, s26, 71631872\ns_lshl_b32 s28, s99, 15\nMaaa 132,68,28\ns_add_u32 s24, s26, s28\ns_sub_u32 s28, 0x10000, s28\nMaaa 128,64,28\n" \
"D 9,64,s79,3072\nMvaa 224,64,24\ns_lshl_b32 s25, s100, 17\ns_add_u32 s26, s26, s25\nMvaa 32,96,32\ns_cmp_eq_u32 s99, 0\ns_cselect_b32 s21, 128, 0\nMvaa 64,96,36\n" \
"s_add_u32 m0, s32, 65536\ns_lshl_b32 s25, s21, 10\nMvaa 68,100,36\nD 10,68,0,0\nMvaa 36,100,32\ns_add_u32 s24, s24, s25\ns_add_u32 s25, s24, 0x20000\nMvaa 40,104,32\n" \
"s_lshl_b32 s101, s101, 8\ns_and_b32 s20, s32, 0x2000\nMvaa 72,104,36\ns_lshl3_add_u32 s20, s20, s101\ns_and_b32 s99, s32, 0x1000\nMvaa 76,108,36\nD 10,68,s89,1024\nMvaa 44,108,32\n" \
"s_lshl2_add_u32 s20, s99, s20\ns_lshl4_add_u32 s101, s32, s101\nMvaa 48,112,32\ns_lshl_b32 s2, s2, 20\ns_add_u32 s2, s2, s101\nMvaa 80,112,36\ns_lshl_b32 s100, s100, 7\ns_add_u32 s101, s101, s100\n" \
"Mvaa 84,116,36\nD 11,68,s90,2048\nMvaa 52,116,32\ns_add_u32 s101, s101, 2048\ns_lshl_b32 s29, s42, 17\nMvaa 56,120,32\ns_lshl_b32 s30, s43, 17\ns_add_u32 s29, s29, s101\n" \
"Mvaa 88,120,36\ns_add_u32 s30, s30, s101\ns_add_u32 s101, s2, 524288\nMvaa 92,124,36\nD 11,68,s91,3072\nMvaa 60,124,32\ns_cmp_eq_u32 s23, 2\ns_cselect_b32 s29, s2, s29\n" \
"s_cselect_b32 s30, s101, s30\ns_cselect_b32 s7, 0xffff, 0\ns_cselect_b32 s9, 0xff, 0\nMvaa 124,124,40\ns_and_b32 s101, s10, 8\ns_lshl_b32 s9, s9, s101\nMvaa 156,124,44\ns_add_u32 m0, s92, 81920\n" \
"s_mov_b32 s2, zqS-zq0-8192\nMvaa 152,120,44\nD 12,68,0,0\nMvaa 120,120,40\ns_cmp_eq_u32 s23, 1\ns_cselect_b32 s2, zqR-zq0-8192, s2\nMvaa 116,116,40\ns_cmp_eq_u32 s23, 0\n" \
"s_cselect_b32 s2, zqT-zq0-8192, s2\nMvaa 148,116,44\ns_cmp_lt_u32 s32, 0x2000\ns_cselect_b32 s2, 0, s2\nMvaa 144,112,44\nD 12,68,s89,1024\nMvaa 112,112,40\ns_add_u32 s2, s2, s32\n" \
"Mvaa 108,108,40\nMvaa 140,108,44\nMvaa 136,104,44\nMvaa 104,104,40\nMvaa 100,100,40\nMvaa 132,100,44\nMvaa 128,96,44\nbuffer_load_dword v30, v4, s[52:55], 0 offen sc1\n" \
"Mvaa 96,96,40\ns_waitcnt vmcnt(16)\ns_barrier\nMvaa 160,96,48\nTa 64,8,32768\nv_add_u32 v9, 128, v9\nMvaa 192,96,52\nTa 66,8,33792\n" \
"v_add_u32 v10, s72, v10\nMvaa 196,100,52\nRa 0,7,32768\nv_add_u32 v11, s72, v11\nMvaa 164,100,48\nTa 68,20,32768\nv_add_u32 v12, s72, v12\nMvaa 168,104,48\n" \
"v_add_u32 v4, s44, v4\nTa 70,20,33792\nMvaa 200,104,52\nRa 4,7,34816\nMvaa 204,108,52\nTa 72,21,32768\nMvaa 172,108,48\nTa 74,21,33792\n" \
"Mvaa 176,112,48\nRa 8,7,36864\nMvaa 208,112,52\nTa 76,22,32768\nMvaa 212,116,52\nTa 78,22,33792\nMvaa 180,116,48\nRa 12,7,38912\n" \
"Mvaa 184,120,48\nTa 80,23,32768\nMvaa 216,120,52\nTa 82,23,33792\nMvaa 220,124,52\nRa 16,7,40960\nMvaa 188,124,48\nTa 84,24,32768\n" \
"Mvaa 252,124,56\nTa 86,24,33792\nMaaa 156,124,60\nRa 20,7,43008\nMaaa 152,120,60\nTa 88,25,32768\nMvaa 248,120,56\nTa 90,25,33792\n" \
"Mvaa 244,116,56\nRa 24,7,45056\nMaaa 148,116,60\nTa 92,26,32768\nMaaa 144,112,60\nTa 94,26,33792\nMvaa 240,112,56\nRa 28,7,47104\n" \
"Mvaa 236,108,56\nMaaa 140,108,60\nMaaa 136,104,60\nMvaa 232,104,56\nMvaa 228,100,56\nMaaa 132,100,60\nMaaa 128,96,60\nMvaa 224,96,56\n" \
"s_waitcnt lgkmcnt(0)\nMvaa 32,64,0\nTa 96,8,40960\nMvaa 64,64,4\nTa 98,8,41984\nMvaa 68,68,4\nRa 32,27,32768\nMvaa 36,68,0\n" \
"Ta 100,20,40960\nMvaa 40,72,0\nTa 102,20,41984\nMvaa 72,72,4\nRa 36,27,34816\nMvaa 76,76,4\nTa 104,21,40960\nMvaa 44,76,0\n" \
"Ta 106,21,41984\nMvaa 48,80,0\nRa 40,27,36864\nMvaa 80,80,4\nTa 108,22,40960\nMvaa 84,84,4\nTa 110,22,41984\nMvaa 52,84,0\n" \
"Ra 44,27,38912\nMvaa 56,88,0\nTa 112,23,40960\nMvaa 88,88,4\nTa 114,23,41984\nMvaa 92,92,4\nRa 48,27,40960\nMvaa 60,92,0\n" \
"Ta 116,24,40960\nMvaa 124,92,8\nTa 118,24,41984\nMvaa 156,92,12\nRa 52,27,43008\nMvaa 152,88,12\nTa 120,25,40960\nMvaa 120,88,8\n" \
"Ta 122,25,41984\nMvaa 116,84,8\nRa 56,27,45056\nMvaa 148,84,12\nTa 124,26,40960\nMvaa 144,80,12\nTa 126,26,41984\nMvaa 112,80,8\n" \
"Ra 60,27,47104\nMvaa 108,76,8\ns_and_b32 s96, s32, 0x1000\ns_lshr_b32 s97, s96, 6\nMvaa 140,76,12\ns_lshl_b32 s96, s96, 2\nv_subrev_u32 v20, s96, v8\nMvaa 136,72,12\n" \
"v_xor_b32 v20, s97, v20\nv_xor_b32 v21, 32, v20\nMvaa 104,72,8\nv_xor_b32 v22, 0x80, v20\nv_xor_b32 v23, 0xa0, v20\nMvaa 100,68,8\nMvaa 132,68,12\nMvaa 128,64,12\n" \
"Mvaa 96,64,8\nMvaa 160,64,16\ns_waitcnt lgkmcnt(0)\ns_barrier\ns_branch 99f\n.p2align 12\nzp27:\ns_setpc_b64 s[98:99]\n" \
"99:\nMvaa 192,64,20\ns_add_u32 m0, s88, 32768\nMvaa 196,68,20\nD 9,64,0,0\nMvaa 164,68,16\nMvaa 168,72,16\nMvaa 200,72,20\n" \
"Mvaa 204,76,20\nD 9,64,s73,1024\nMvaa 172,76,16\nMvaa 176,80,16\nMvaa 208,80,20\nMvaa 212,84,20\nD 9,64,s74,2048\nMvaa 180,84,16\n" \
"Mvaa 184,88,16\nMvaa 216,88,20\nMvaa 220,92,20\nD 9,64,s75,3072\nMvaa 188,92,16\nMvaa 252,92,24\nMaaa 156,92,28\ns_add_u32 m0, s88, 36864\n" \
"Maaa 152,88,28\nD 9,64,s76,0\nMvaa 248,88,24\nMvaa 244,84,24\nMaaa 148,84,28\nMaaa 144,80,28\nD 9,64,s77,1024\nMvaa 240,80,24\n" \
"Mvaa 236,76,24\nMaaa 140,76,28\nMaaa 136,72,28\nD 9,64,s78,2048\nMvaa 232,72,24\nMvaa 228,68,24\nMaaa 132,68,28\nMaaa 128,64,28\n" \
"D 9,64,s79,3072\nMvaa 224,64,24\nMvaa 32,96,32\nMvaa 36,100,32\ns_add_u32 m0, s32, 98304\nMvaa 40,104,32\nD 10,68,0,0\nMvaa 44,108,32\n" \
"Mvaa 48,112,32\nMvaa 52,116,32\nMvaa 56,120,32\nD 10,68,s89,1024\nMvaa 60,124,32\nMvaa 64,96,36\nMvaa 68,100,36\nMvaa 72,104,36\n" \
"D 11,68,s90,2048\nC 32,32,33\nMvaa 76,108,36\nC 33,34,35\nC 34,36,37\nC 35,38,39\nMvaa 80,112,36\nC 36,40,41\n" \
"C 37,42,43\nC 38,44,45\nMvaa 84,116,36\nC 39,46,47\nC 40,48,49\nC 41,50,51\nMvaa 88,120,36\nD 11,68,s91,3072\n" \
"C 42,52,53\nMvaa 92,124,36\nC 43,54,55\nC 44,56,57\nC 45,58,59\nMvaa 96,96,40\nC 46,60,61\nC 47,62,63\n" \
"Mvaa 100,100,40\ns_add_u32 m0, s92, 114688\nMvaa 104,104,40\nD 12,68,0,0\nC 48,64,65\nMvaa 108,108,40\nC 49,66,67\nC 50,68,69\n" \
"C 51,70,71\nMvaa 112,112,40\nC 52,72,73\nC 53,74,75\nC 54,76,77\nMvaa 116,116,40\nC 55,78,79\nC 56,80,81\n" \
"C 57,82,83\nMvaa 120,120,40\nD 12,68,s89,1024\nC 58,84,85\nMvaa 124,124,40\nC 59,86,87\nC 60,88,89\nC 61,90,91\n" \
"Mvaa 128,96,44\nC 62,92,93\nC 63,94,95\nMvaa 132,100,44\nMvaa 136,104,44\nC 96,96,97\nC 97,98,99\nC 98,100,101\n" \
"Mvaa 140,108,44\nC 99,102,103\nAW 160,96\nAW 161,97\nMvaa 144,112,44\nAW 162,98\nAW 163,99\nC 104,104,105\n" \
"Mvaa 148,116,44\nC 105,106,107\nC 106,108,109\nC 107,110,111\nMvaa 152,120,44\nbuffer_load_dword v30, v4, s[52:55], 0 offen sc1\nAW 164,104\nMvaa 156,124,44\n" \
"s_waitcnt vmcnt(16)\ns_barrier\nAW 165,105\nMvaa 160,96,48\nTa 64,20,0\nv_add_u32 v9, 128, v9\nAW 166,106\nMvaa 164,100,48\n" \
"Ta 66,20,1024\nv_add_u32 v10, s72, v10\nAW 167,107\nMvaa 168,104,48\nRa 0,7,0\nv_add_u32 v11, s72, v11\nC 112,112,113\nMvaa 172,108,48\n" \
"Ta 68,21,0\nv_add_u32 v12, s72, v12\nC 113,114,115\nMvaa 176,112,48\nv_add_u32 v4, s44, v4\nTa 70,21,1024\nC 114,116,117\nMvaa 180,116,48\n" \
"Ra 4,7,2048\nC 115,118,119\nAW 168,112\nMvaa 184,120,48\nTa 72,22,0\nAW 169,113\nAW 170,114\nMvaa 188,124,48\n" \
"Ta 74,22,1024\nAW 171,115\nC 120,120,121\nMvaa 192,96,52\nRa 8,7,4096\nC 121,122,123\nC 122,124,125\nMvaa 196,100,52\n" \
"Ta 76,23,0\nC 123,126,127\nAW 172,120\nMvaa 200,104,52\nTa 78,23,1024\nAW 173,121\nAW 174,122\nMvaa 204,108,52\n" \
"Ra 12,7,6144\nAW 175,123\nC 128,128,129\nMvaa 208,112,52\nTa 80,20,16384\nC 129,130,131\nC 130,132,133\nMvaa 212,116,52\n" \
"Ta 82,20,17408\nC 131,134,135\nAW 176,128\nMvaa 216,120,52\nRa 16,7,8192\nAW 177,129\nAW 178,130\nMvaa 220,124,52\n" \
"Ta 84,21,16384\nAW 179,131\nC 136,136,137\nMvaa 224,96,56\nTa 86,21,17408\nC 137,138,139\nC 138,140,141\nMvaa 228,100,56\n" \
"Ra 20,7,10240\nC 139,142,143\nAW 180,136\nMvaa 232,104,56\nRa 24,7,12288\nAW 181,137\nAW 182,138\nMvaa 236,108,56\n" \
"Ra 28,7,14336\nAW 183,139\nC 144,144,145\nMvaa 240,112,56\nC 145,146,147\nC 146,148,149\nC 147,150,151\nMvaa 244,116,56\n" \
"AW 184,144\nAW 185,145\nAW 186,146\nMvaa 248,120,56\nAW 187,147\nC 152,152,153\nC 153,154,155\nMvaa 252,124,56\n" \
"C 154,156,157\nC 155,158,159\nAW 188,152\nMaaa 128,96,60\nAW 189,153\nAW 190,154\nAW 191,155\nMaaa 132,100,60\n" \
"C 160,160,161\nC 161,162,163\nC 162,164,165\nMaaa 136,104,60\nC 163,166,167\nAW 192,160\nAW 193,161\nMaaa 140,108,60\n" \
"AW 194,162\nAW 195,163\nC 168,168,169\nMaaa 144,112,60\nC 169,170,171\nC 170,172,173\nC 171,174,175\nMaaa 148,116,60\n" \
"AW 196,168\nAW 197,169\nAW 198,170\nMaaa 152,120,60\nAW 199,171\nC 176,176,177\nC 177,178,179\nMaaa 156,124,60\n" \
"C 178,180,181\nC 179,182,183\nAW 200,176\ns_waitcnt lgkmcnt(0)\nzA:\nZvaa 64,64,0\nTa 96,20,8192\nAW 201,177\n" \
"AW 202,178\nZvaa 88,64,4\nTa 98,20,9216\nAW 203,179\nC 184,184,185\nZvaa 92,68,4\nRa 32,27,0\nC 185,186,187\n" \
"C 186,188,189\nZvaa 68,68,0\nTa 100,21,8192\nC 187,190,191\nAW 204,184\nZvaa 72,72,0\nTa 102,21,9216\nAW 205,185\n" \
"AW 206,186\nZvaa 96,72,4\nRa 36,27,2048\nAW 207,187\nC 192,192,193\nZvaa 100,76,4\nTa 104,22,8192\nC 193,194,195\n" \
"C 194,196,197\nZvaa 76,76,0\nTa 106,22,9216\nC 195,198,199\nAW 208,192\nZvaa 80,0,80\nRa 40,27,4096\nAW 209,193\n" \
"AW 210,194\nZvaa 104,4,80\nTa 108,23,8192\nAW 211,195\nC 200,200,201\nZvaa 108,4,84\nTa 110,23,9216\nC 201,202,203\n" \
"C 202,204,205\nZvaa 84,0,84\nRa 44,27,6144\nC 203,206,207\nAW 212,200\nZvaa 132,8,84\nTa 112,22,16384\nAW 213,201\n" \
"AW 214,202\nZvaa 156,12,84\nTa 114,22,17408\nAW 215,203\nC 208,208,209\nZvaa 152,12,80\nRa 48,27,8192\nC 209,210,211\n" \
"C 210,212,213\nZvaa 128,8,80\nTa 116,23,16384\nC 211,214,215\nAW 216,208\nZvaa 124,76,8\nTa 118,23,17408\nAW 217,209\n" \
"AW 218,210\nZvaa 148,76,12\nRa 52,27,10240\nAW 219,211\nC 216,216,217\nZvaa 144,72,12\nRa 56,27,12288\nC 217,218,219\n" \
"C 218,220,221\nZvaa 120,72,8\nRa 60,27,14336\nC 219,222,223\nAW 220,216\nZvaa 116,68,8\nAW 221,217\nAW 222,218\n" \
"AW 223,219\nZvaa 140,68,12\nC 224,224,225\nC 225,226,227\nC 226,228,229\nZvaa 136,64,12\nC 227,230,231\nAW 224,224\n" \
"AW 225,225\nZvaa 112,64,8\nAW 226,226\nAW 227,227\nC 232,232,233\nZvaa 160,64,16\nC 233,234,235\nC 234,236,237\n" \
"C 235,238,239\nZvaa 184,64,20\nAW 228,232\nAW 229,233\nAW 230,234\nZvaa 188,68,20\ns_waitcnt lgkmcnt(0)\ns_barrier\n" \
"AW 231,235\nZvaa 164,68,16\ns_add_u32 m0, s88, 0\nC 240,240,241\nC 241,242,243\nZvaa 168,72,16\nD 9,64,0,0\nC 242,244,245\n" \
"Zvaa 192,72,20\nC 243,246,247\nAW 232,240\nAW 233,241\nZvaa 196,76,20\nAW 234,242\nAW 235,243\nC 248,248,249\n" \
"Zvaa 172,76,16\nC 249,250,251\nC 250,252,253\nC 251,254,255\nZvaa 176,16,80\nD 9,64,s73,1024\nAW 236,248\nZvaa 200,20,80\n" \
"AW 237,249\nAW 238,250\nAW 239,251\nZvaa 204,20,84\nZvaa 180,16,84\nZvaa 228,24,84\nD 9,64,s74,2048\nZvaa 252,28,84\n" \
"s_add_u32 s95, s34, 0\nZvaa 248,28,80\nbuffer_store_dwordx4 v[32:35], v0, s[60:63], s95 offen offset:0 sc1\nZvaa 224,24,80\nZvaa 220,76,24\nD 9,64,s75,3072\nZvaa 244,76,28\nZvaa 240,72,28\n" \
"Zvaa 216,72,24\ns_add_u32 m0, s88, 4096\nZvaa 212,68,24\nD 9,64,s76,0\nZvaa 236,68,28\nZvaa 232,64,28\nZvaa 208,64,24\nMvaa 64,96,32\n" \
"D 9,64,s77,1024\nMvaa 88,96,36\nMvaa 92,100,36\nMvaa 68,100,32\nMvaa 72,104,32\nD 9,64,s78,2048\nMvaa 96,104,36\nMvaa 100,108,36\n" \
"buffer_store_dwordx4 v[36:39], v0, s[60:63], s95 offen offset:1024 sc1\nMvaa 76,108,32\nMvaa 80,32,112\nD 9,64,s79,3072\nMvaa 104,36,112\nMvaa 108,36,116\nMvaa 84,32,116\ns_add_u32 m0, s32, 65536\n" \
"Mvaa 132,40,116\nD 10,68,0,0\nMvaa 156,44,116\nMvaa 152,44,112\nMvaa 128,40,112\nMvaa 124,108,40\nD 10,68,s89,1024\nMvaa 148,108,44\n" \
"Mvaa 144,104,44\nbuffer_store_dwordx4 v[40:43], v0, s[60:63], s95 offen offset:2048 sc1\nMvaa 120,104,40\nMvaa 116,100,40\nD 11,68,s90,2048\nMvaa 140,100,44\nMvaa 136,96,44\ns_waitcnt vmcnt(15)\n" \
"s_barrier\nMvaa 112,96,40\nTa 64,20,32768\nMvaa 160,96,48\nD 11,68,s91,3072\nTa 66,20,33792\nMvaa 184,96,52\nRa 0,7,32768\n" \
"Mvaa 188,100,52\nTa 68,21,32768\nMvaa 164,100,48\ns_add_u32 m0, s92, 81920\nTa 70,21,33792\nMvaa 168,104,48\nD 12,68,0,0\nRa 4,7,34816\n" \
"Mvaa 192,104,52\nTa 72,22,32768\nMvaa 196,108,52\nTa 74,22,33792\nMvaa 172,108,48\nRa 8,7,36864\nMvaa 176,48,112\nD 12,68,s89,1024\n" \
"Ta 76,23,32768\nMvaa 200,52,112\nTa 78,23,33792\nv_add_u32 v9, 128, v9\nMvaa 204,52,116\nRa 12,7,38912\nv_add_u32 v10, s72, v10\nMvaa 180,48,116\n" \
"Ta 80,20,49152\nv_add_u32 v11, s72, v11\nMvaa 228,56,116\nTa 82,20,50176\nv_add_u32 v12, s72, v12\nMvaa 252,60,116\nRa 16,7,40960\nMvaa 248,60,112\n" \
"Ta 84,21,49152\nMvaa 224,56,112\nTa 86,21,50176\nMvaa 220,108,56\nRa 20,7,43008\nMvaa 244,108,60\nRa 24,7,45056\nMvaa 240,104,60\n" \
"Ra 28,7,47104\nMvaa 216,104,56\nbuffer_store_dwordx4 v[44:47], v0, s[60:63], s95 offen offset:3072 sc1\nMvaa 212,100,56\nMvaa 236,100,60\nMvaa 232,96,60\nMvaa 208,96,56\ns_waitcnt lgkmcnt(0)\n" \
"Mvaa 64,64,0\nTa 96,20,40960\nMvaa 88,64,4\nTa 98,20,41984\nMvaa 92,68,4\nRa 32,27,32768\nMvaa 68,68,0\nTa 100,21,40960\n" \
"Mvaa 72,72,0\nTa 102,21,41984\nMvaa 96,72,4\nRa 36,27,34816\nMvaa 100,76,4\nTa 104,22,40960\nMvaa 76,76,0\nTa 106,22,41984\n" \
"Mvaa 80,0,80\ns_branch 99f\n.p2align 12\nzga:\nzp28:\ns_setpc_b64 s[98:99]\n99:\nRa 40,27,36864\n" \
"Mvaa 104,4,80\nTa 108,23,40960\nMvaa 108,4,84\nTa 110,23,41984\nMvaa 84,0,84\nRa 44,27,38912\nMvaa 132,8,84\nTa 112,22,49152\n" \
"Mvaa 156,12,84\nTa 114,22,50176\nMvaa 152,12,80\nRa 48,27,40960\nMvaa 128,8,80\nTa 116,23,49152\nMvaa 124,76,8\nTa 118,23,50176\n" \
"Mvaa 148,76,12\nRa 52,27,43008\nMvaa 144,72,12\nRa 56,27,45056\nMvaa 120,72,8\nRa 60,27,47104\ns_add_u32 s95, s34, 4096\nMvaa 116,68,8\n" \
"buffer_store_dwordx4 v[48:51], v0, s[60:63], s95 offen offset:0 sc1\nMvaa 140,68,12\nMvaa 136,64,12\nbuffer_store_dwordx4 v[52:55], v0, s[60:63], s95 offen offset:1024 sc1\nMvaa 112,64,8\nMvaa 160,64,16\nMvaa 184,64,20\nMvaa 188,68,20\n" \
"s_waitcnt lgkmcnt(0)\ns_barrier\nMvaa 164,68,16\ns_add_u32 m0, s88, 32768\nMvaa 168,72,16\nD 9,64,0,0\nMvaa 192,72,20\nMvaa 196,76,20\n" \
"Mvaa 172,76,16\nMvaa 176,16,80\nD 9,64,s73,1024\nMvaa 200,20,80\nMvaa 204,20,84\nMvaa 180,16,84\nMvaa 228,24,84\nD 9,64,s74,2048\n" \
"Mvaa 252,28,84\nMvaa 248,28,80\nbuffer_store_dwordx4 v[56:59], v0, s[60:63], s95 offen offset:2048 sc1\nMvaa 224,24,80\nMvaa 220,76,24\nD 9,64,s75,3072\nMvaa 244,76,28\nMvaa 240,72,28\n" \
"Mvaa 216,72,24\ns_add_u32 m0, s88, 36864\nMvaa 212,68,24\nD 9,64,s76,0\nMvaa 236,68,28\nMvaa 232,64,28\nMvaa 208,64,24\nMvaa 64,96,32\n" \
"D 9,64,s77,1024\nMvaa 88,96,36\nMvaa 92,100,36\nMvaa 68,100,32\nMvaa 72,104,32\nD 9,64,s78,2048\nMvaa 96,104,36\nMvaa 100,108,36\n" \
"Mvaa 76,108,32\nMvaa 80,32,112\nD 9,64,s79,3072\nMvaa 104,36,112\nMvaa 108,36,116\nbuffer_store_dwordx4 v[60:63], v0, s[60:63], s95 offen offset:3072 sc1\nMvaa 84,32,116\ns_add_u32 m0, s32, 98304\n" \
"Mvaa 132,40,116\nD 10,68,0,0\nMvaa 156,44,116\nMvaa 152,44,112\nMvaa 128,40,112\nMvaa 124,108,40\nD 10,68,s89,1024\nMvaa 148,108,44\n" \
"Mvaa 144,104,44\nMvaa 120,104,40\nMvaa 116,100,40\nD 11,68,s90,2048\nMvaa 140,100,44\nMvaa 136,96,44\ns_waitcnt vmcnt(16)\ns_barrier\n" \
"Mvaa 112,96,40\nTa 64,20,0\nMvaa 160,96,48\nD 11,68,s91,3072\nTa 66,20,1024\nMvaa 184,96,52\nRa 0,7,0\nMvaa 188,100,52\n" \
"Ta 68,21,0\nMvaa 164,100,48\ns_add_u32 m0, s92, 114688\nTa 70,21,1024\nMvaa 168,104,48\nD 12,68,0,0\nRa 4,7,2048\nMvaa 192,104,52\n" \
"Ta 72,22,0\nMvaa 196,108,52\nTa 74,22,1024\nMvaa 172,108,48\nRa 8,7,4096\nMvaa 176,48,112\nD 12,68,s89,1024\nTa 76,23,0\n" \
"Mvaa 200,52,112\nTa 78,23,1024\nv_add_u32 v9, 128, v9\nMvaa 204,52,116\nRa 12,7,6144\nv_add_u32 v10, s72, v10\nMvaa 180,48,116\nTa 80,20,16384\n" \
"v_add_u32 v11, s72, v11\nMvaa 228,56,116\nTa 82,20,17408\nv_add_u32 v12, s72, v12\nMvaa 252,60,116\nRa 16,7,8192\nMvaa 248,60,112\nTa 84,21,16384\n" \
"Mvaa 224,56,112\nTa 86,21,17408\nMvaa 220,108,56\nRa 20,7,10240\nMvaa 244,108,60\nRa 24,7,12288\nMvaa 240,104,60\nRa 28,7,14336\n" \
"s_add_u32 s95, s34, 8192\nMvaa 216,104,56\nbuffer_store_dwordx4 a[160:163], v0, s[60:63], s95 offen offset:0 sc1\nMvaa 212,100,56\nAR 48,128\nAR 49,129\nMvaa 236,100,60\nAR 50,130\n" \
"AR 51,131\nMvaa 232,96,60\nAR 52,132\nAR 53,133\nMvaa 208,96,56\ns_waitcnt lgkmcnt(0)\nAR 54,134\nMvaa 64,64,0\n" \
"Ta 96,20,8192\nAR 55,135\nMvaa 88,64,4\nTa 98,20,9216\nC 32,48,49\nMvaa 92,68,4\nRa 32,27,0\nC 33,50,51\n" \
"Mvaa 68,68,0\nTa 100,21,8192\nC 34,52,53\nMvaa 72,72,0\nTa 102,21,9216\nC 35,54,55\nMvaa 96,72,4\nRa 36,27,2048\n" \
"AR 56,136\nMvaa 100,76,4\nTa 104,22,8192\nAR 57,137\nMvaa 76,76,0\nTa 106,22,9216\nAR 58,138\nMvaa 80,0,80\n" \
"Ra 40,27,4096\nAR 59,139\nMvaa 104,4,80\nTa 108,23,8192\nAR 60,140\nMvaa 108,4,84\nTa 110,23,9216\nAR 61,141\n" \
"Mvaa 84,0,84\nRa 44,27,6144\nAR 62,142\nMvaa 132,8,84\nTa 112,22,16384\nAR 63,143\nMvaa 156,12,84\nTa 114,22,17408\n" \
"C 36,56,57\nMvaa 152,12,80\nRa 48,27,8192\nC 37,58,59\nMvaa 128,8,80\nTa 116,23,16384\nC 38,60,61\nMvaa 124,76,8\n" \
"Ta 118,23,17408\nC 39,62,63\nMvaa 148,76,12\nRa 52,27,10240\nAR 48,144\nMvaa 144,72,12\nRa 56,27,12288\nAR 49,145\n" \
"Mvaa 120,72,8\nRa 60,27,14336\nAR 50,146\nMvaa 116,68,8\nbuffer_store_dwordx4 a[164:167], v0, s[60:63], s95 offen offset:1024 sc1\nMvaa 140,68,12\nAR 51,147\nAR 52,148\n" \
"Mvaa 136,64,12\nbuffer_store_dwordx4 a[168:171], v0, s[60:63], s95 offen offset:2048 sc1\nMvaa 112,64,8\nAR 53,149\nAR 54,150\nMvaa 160,64,16\nAR 55,151\nC 40,48,49\n" \
"Mvaa 184,64,20\nC 41,50,51\nC 42,52,53\nMvaa 188,68,20\ns_waitcnt lgkmcnt(0)\ns_barrier\nMvaa 164,68,16\ns_add_u32 m0, s88, 0\n" \
"C 43,54,55\nMvaa 168,72,16\nD 9,64,0,0\nMvaa 192,72,20\nAR 56,152\nAR 57,153\nMvaa 196,76,20\nAR 58,154\n" \
"AR 59,155\nMvaa 172,76,16\nAR 60,156\nAR 61,157\nMvaa 176,16,80\nD 9,64,s73,1024\nMvaa 200,20,80\nAR 62,158\n" \
"AR 63,159\nMvaa 204,20,84\nC 44,56,57\nC 45,58,59\nMvaa 180,16,84\nC 46,60,61\nC 47,62,63\nMvaa 228,24,84\n" \
"D 9,64,s74,2048\nMvaa 252,28,84\nMvaa 248,28,80\nbuffer_store_dwordx4 a[172:175], v0, s[60:63], s95 offen offset:3072 sc1\nMvaa 224,24,80\nMvaa 220,76,24\nD 9,64,s75,3072\nMvaa 244,76,28\n" \
"Mvaa 240,72,28\nMvaa 216,72,24\ns_add_u32 m0, s88, 4096\nMvaa 212,68,24\nD 9,64,s76,0\nMvaa 236,68,28\nMvaa 232,64,28\nMvaa 208,64,24\n" \
"Mvaa 64,96,32\nD 9,64,s77,1024\nMvaa 88,96,36\nMvaa 92,100,36\nMvaa 68,100,32\nMvaa 72,104,32\nD 9,64,s78,2048\nMvaa 96,104,36\n" \
"Mvaa 100,108,36\nMvaa 76,108,32\nMvaa 80,32,112\nD 9,64,s79,3072\nMvaa 104,36,112\ns_add_u32 s95, s34, 12288\nMvaa 108,36,116\nbuffer_store_dwordx4 a[176:179], v0, s[60:63], s95 offen offset:0 sc1\n" \
"Mvaa 84,32,116\ns_add_u32 m0, s32, 65536\nMvaa 132,40,116\nD 10,68,0,0\nMvaa 156,44,116\nMvaa 152,44,112\nMvaa 128,40,112\nMvaa 124,108,40\n" \
"D 10,68,s89,1024\nMvaa 148,108,44\nMvaa 144,104,44\nMvaa 120,104,40\nMvaa 116,100,40\nD 11,68,s90,2048\nMvaa 140,100,44\nMvaa 136,96,44\n" \
"s_waitcnt vmcnt(16)\ns_barrier\nMvaa 112,96,40\nTa 64,20,32768\nMvaa 160,96,48\nD 11,68,s91,3072\nTa 66,20,33792\nMvaa 184,96,52\n" \
"Ra 0,7,32768\nMvaa 188,100,52\nTa 68,21,32768\nMvaa 164,100,48\ns_add_u32 m0, s92, 81920\nTa 70,21,33792\nMvaa 168,104,48\nD 12,68,0,0\n" \
"Ra 4,7,34816\nMvaa 192,104,52\nTa 72,22,32768\nMvaa 196,108,52\nTa 74,22,33792\nMvaa 172,108,48\nRa 8,7,36864\nMvaa 176,48,112\n" \
"D 12,68,s89,1024\nTa 76,23,32768\nMvaa 200,52,112\nTa 78,23,33792\nv_add_u32 v9, 128, v9\nMvaa 204,52,116\nRa 12,7,38912\nv_add_u32 v10, s72, v10\n" \
"Mvaa 180,48,116\nTa 80,20,49152\nv_add_u32 v11, s72, v11\nMvaa 228,56,116\nTa 82,20,50176\nv_add_u32 v12, s72, v12\nMvaa 252,60,116\nRa 16,7,40960\n" \
"Mvaa 248,60,112\nTa 84,21,49152\nMvaa 224,56,112\nTa 86,21,50176\nMvaa 220,108,56\nRa 20,7,43008\nMvaa 244,108,60\nRa 24,7,45056\n" \
"Mvaa 240,104,60\nRa 28,7,47104\nMvaa 216,104,56\nbuffer_store_dwordx4 a[180:183], v0, s[60:63], s95 offen offset:1024 sc1\nMvaa 212,100,56\nMvaa 236,100,60\nMvaa 232,96,60\nMvaa 208,96,56\n" \
"s_waitcnt lgkmcnt(0)\nMvaa 64,64,0\nTa 96,20,40960\nMvaa 88,64,4\nTa 98,20,41984\nMvaa 92,68,4\nRa 32,27,32768\nMvaa 68,68,0\n" \
"Ta 100,21,40960\nMvaa 72,72,0\nTa 102,21,41984\nMvaa 96,72,4\nRa 36,27,34816\nMvaa 100,76,4\nTa 104,22,40960\nMvaa 76,76,0\n" \
"Ta 106,22,41984\nMvaa 80,0,80\nRa 40,27,36864\nMvaa 104,4,80\nTa 108,23,40960\nMvaa 108,4,84\nTa 110,23,41984\nMvaa 84,0,84\n" \
"Ra 44,27,38912\nMvaa 132,8,84\nTa 112,22,49152\nMvaa 156,12,84\nTa 114,22,50176\nMvaa 152,12,80\nRa 48,27,40960\nMvaa 128,8,80\n" \
"Ta 116,23,49152\nMvaa 124,76,8\nTa 118,23,50176\nMvaa 148,76,12\nRa 52,27,43008\nMvaa 144,72,12\nRa 56,27,45056\nMvaa 120,72,8\n" \
"Ra 60,27,47104\nMvaa 116,68,8\nbuffer_store_dwordx4 a[184:187], v0, s[60:63], s95 offen offset:2048 sc1\nMvaa 140,68,12\nMvaa 136,64,12\nbuffer_store_dwordx4 a[188:191], v0, s[60:63], s95 offen offset:3072 sc1\nMvaa 112,64,8\nMvaa 160,64,16\n" \
"Mvaa 184,64,20\nMvaa 188,68,20\ns_waitcnt lgkmcnt(0)\ns_barrier\nMvaa 164,68,16\ns_add_u32 m0, s88, 32768\nMvaa 168,72,16\nD 9,64,0,0\n" \
"Mvaa 192,72,20\nMvaa 196,76,20\nMvaa 172,76,16\nMvaa 176,16,80\nD 9,64,s73,1024\nMvaa 200,20,80\nMvaa 204,20,84\nMvaa 180,16,84\n" \
"Mvaa 228,24,84\nD 9,64,s74,2048\nMvaa 252,28,84\ns_add_u32 s95, s34, 16384\nMvaa 248,28,80\nbuffer_store_dwordx4 a[192:195], v0, s[60:63], s95 offen offset:0 sc1\nMvaa 224,24,80\nMvaa 220,76,24\n" \
"D 9,64,s75,3072\nMvaa 244,76,28\nMvaa 240,72,28\nMvaa 216,72,24\ns_add_u32 m0, s88, 36864\nMvaa 212,68,24\nD 9,64,s76,0\nMvaa 236,68,28\n" \
"Mvaa 232,64,28\nMvaa 208,64,24\nMvaa 64,96,32\nD 9,64,s77,1024\nMvaa 88,96,36\nMvaa 92,100,36\nMvaa 68,100,32\nMvaa 72,104,32\n" \
"D 9,64,s78,2048\nMvaa 96,104,36\nMvaa 100,108,36\nMvaa 76,108,32\nMvaa 80,32,112\nD 9,64,s79,3072\nMvaa 104,36,112\nMvaa 108,36,116\n" \
"buffer_store_dwordx4 a[196:199], v0, s[60:63], s95 offen offset:1024 sc1\nMvaa 84,32,116\ns_add_u32 m0, s32, 98304\nMvaa 132,40,116\nD 10,68,0,0\nMvaa 156,44,116\nMvaa 152,44,112\nMvaa 128,40,112\n" \
"Mvaa 124,108,40\nD 10,68,s89,1024\nMvaa 148,108,44\nMvaa 144,104,44\nMvaa 120,104,40\nMvaa 116,100,40\nD 11,68,s90,2048\nMvaa 140,100,44\n" \
"Mvaa 136,96,44\ns_waitcnt vmcnt(16)\ns_barrier\nMvaa 112,96,40\nTa 64,20,0\nMvaa 160,96,48\nD 11,68,s91,3072\nTa 66,20,1024\n" \
"Mvaa 184,96,52\nRa 0,7,0\nMvaa 188,100,52\nTa 68,21,0\nMvaa 164,100,48\ns_add_u32 m0, s92, 114688\nTa 70,21,1024\nMvaa 168,104,48\n" \
"D 12,68,0,0\nRa 4,7,2048\nMvaa 192,104,52\nTa 72,22,0\nMvaa 196,108,52\nTa 74,22,1024\nMvaa 172,108,48\nRa 8,7,4096\n" \
"Mvaa 176,48,112\nD 12,68,s89,1024\nTa 76,23,0\nMvaa 200,52,112\nTa 78,23,1024\nv_add_u32 v9, 128, v9\nMvaa 204,52,116\nRa 12,7,6144\n" \
"v_add_u32 v10, s72, v10\nMvaa 180,48,116\nTa 80,20,16384\nv_add_u32 v11, s72, v11\nMvaa 228,56,116\nTa 82,20,17408\nv_add_u32 v12, s72, v12\nMvaa 252,60,116\n" \
"Ra 16,7,8192\nMvaa 248,60,112\ns_branch 99f\n.p2align 12\nzp29:\ns_setpc_b64 s[98:99]\n99:\nTa 84,21,16384\n" \
"Mvaa 224,56,112\nTa 86,21,17408\nMvaa 220,108,56\nRa 20,7,10240\nMvaa 244,108,60\nRa 24,7,12288\nMvaa 240,104,60\nRa 28,7,14336\n" \
"Mvaa 216,104,56\nbuffer_store_dwordx4 a[200:203], v0, s[60:63], s95 offen offset:2048 sc1\nMvaa 212,100,56\nMvaa 236,100,60\nMvaa 232,96,60\nMvaa 208,96,56\ns_waitcnt lgkmcnt(0)\nMvaa 64,64,0\n" \
"Ta 96,20,8192\nMvaa 88,64,4\nTa 98,20,9216\nMvaa 92,68,4\nRa 32,27,0\nMvaa 68,68,0\nTa 100,21,8192\nMvaa 72,72,0\n" \
"Ta 102,21,9216\nMvaa 96,72,4\nRa 36,27,2048\nMvaa 100,76,4\nTa 104,22,8192\nMvaa 76,76,0\nTa 106,22,9216\nMvaa 80,0,80\n" \
"Ra 40,27,4096\nMvaa 104,4,80\nTa 108,23,8192\nMvaa 108,4,84\nTa 110,23,9216\nMvaa 84,0,84\nRa 44,27,6144\nMvaa 132,8,84\n" \
"Ta 112,22,16384\nMvaa 156,12,84\nTa 114,22,17408\nMvaa 152,12,80\nRa 48,27,8192\nMvaa 128,8,80\nTa 116,23,16384\nMvaa 124,76,8\n" \
"Ta 118,23,17408\nMvaa 148,76,12\nRa 52,27,10240\nMvaa 144,72,12\nRa 56,27,12288\nMvaa 120,72,8\nRa 60,27,14336\nMvaa 116,68,8\n" \
"buffer_store_dwordx4 a[204:207], v0, s[60:63], s95 offen offset:3072 sc1\nMvaa 140,68,12\ns_add_u32 s95, s34, 20480\nMvaa 136,64,12\nbuffer_store_dwordx4 a[208:211], v0, s[60:63], s95 offen offset:0 sc1\nMvaa 112,64,8\nMvaa 160,64,16\nMvaa 184,64,20\n" \
"Mvaa 188,68,20\ns_waitcnt lgkmcnt(0)\ns_barrier\nMvaa 164,68,16\ns_add_u32 m0, s88, 0\nMvaa 168,72,16\nD 9,64,0,0\nMvaa 192,72,20\n" \
"Mvaa 196,76,20\nMvaa 172,76,16\nMvaa 176,16,80\nD 9,64,s73,1024\nMvaa 200,20,80\nMvaa 204,20,84\nMvaa 180,16,84\nMvaa 228,24,84\n" \
"D 9,64,s74,2048\nMvaa 252,28,84\nMvaa 248,28,80\nbuffer_store_dwordx4 a[212:215], v0, s[60:63], s95 offen offset:1024 sc1\nMvaa 224,24,80\nMvaa 220,76,24\nD 9,64,s75,3072\nMvaa 244,76,28\n" \
"Mvaa 240,72,28\nMvaa 216,72,24\ns_add_u32 m0, s88, 4096\nMvaa 212,68,24\nD 9,64,s76,0\nMvaa 236,68,28\nMvaa 232,64,28\nMvaa 208,64,24\n" \
"Mvaa 64,96,32\nD 9,64,s77,1024\nMvaa 88,96,36\nMvaa 92,100,36\nMvaa 68,100,32\nMvaa 72,104,32\nD 9,64,s78,2048\nMvaa 96,104,36\n" \
"Mvaa 100,108,36\nMvaa 76,108,32\nMvaa 80,32,112\nD 9,64,s79,3072\nMvaa 104,36,112\nMvaa 108,36,116\nbuffer_store_dwordx4 a[216:219], v0, s[60:63], s95 offen offset:2048 sc1\nMvaa 84,32,116\n" \
"s_add_u32 m0, s32, 65536\nMvaa 132,40,116\nD 10,68,0,0\nMvaa 156,44,116\nMvaa 152,44,112\nMvaa 128,40,112\nMvaa 124,108,40\nD 10,68,s89,1024\n" \
"Mvaa 148,108,44\nMvaa 144,104,44\nMvaa 120,104,40\nMvaa 116,100,40\nD 11,68,s90,2048\nMvaa 140,100,44\nMvaa 136,96,44\ns_waitcnt vmcnt(16)\n" \
"s_barrier\nMvaa 112,96,40\nTa 64,20,32768\nMvaa 160,96,48\nD 11,68,s91,3072\nTa 66,20,33792\nMvaa 184,96,52\nRa 0,7,32768\n" \
"Mvaa 188,100,52\nTa 68,21,32768\nMvaa 164,100,48\ns_add_u32 m0, s92, 81920\nTa 70,21,33792\nMvaa 168,104,48\nD 12,68,0,0\nRa 4,7,34816\n" \
"Mvaa 192,104,52\nTa 72,22,32768\nMvaa 196,108,52\nTa 74,22,33792\nMvaa 172,108,48\nRa 8,7,36864\nMvaa 176,48,112\nD 12,68,s89,1024\n" \
"Ta 76,23,32768\nMvaa 200,52,112\nTa 78,23,33792\nv_add_u32 v9, 128, v9\nMvaa 204,52,116\nRa 12,7,38912\nv_add_u32 v10, s72, v10\nMvaa 180,48,116\n" \
"Ta 80,20,49152\nv_add_u32 v11, s72, v11\nMvaa 228,56,116\nTa 82,20,50176\nv_add_u32 v12, s72, v12\nMvaa 252,60,116\nRa 16,7,40960\nMvaa 248,60,112\n" \
"Ta 84,21,49152\nMvaa 224,56,112\nTa 86,21,50176\nMvaa 220,108,56\nRa 20,7,43008\nMvaa 244,108,60\nRa 24,7,45056\nMvaa 240,104,60\n" \
"Ra 28,7,47104\nMvaa 216,104,56\nbuffer_store_dwordx4 a[220:223], v0, s[60:63], s95 offen offset:3072 sc1\nMvaa 212,100,56\nMvaa 236,100,60\nMvaa 232,96,60\nMvaa 208,96,56\ns_waitcnt lgkmcnt(0)\n" \
"Mvaa 64,64,0\nTa 96,20,40960\nMvaa 88,64,4\nTa 98,20,41984\nMvaa 92,68,4\nRa 32,27,32768\nMvaa 68,68,0\nTa 100,21,40960\n" \
"Mvaa 72,72,0\nTa 102,21,41984\nMvaa 96,72,4\nRa 36,27,34816\nMvaa 100,76,4\nTa 104,22,40960\nMvaa 76,76,0\nTa 106,22,41984\n" \
"Mvaa 80,0,80\nRa 40,27,36864\nMvaa 104,4,80\nTa 108,23,40960\nMvaa 108,4,84\nTa 110,23,41984\nMvaa 84,0,84\nRa 44,27,38912\n" \
"Mvaa 132,8,84\nTa 112,22,49152\nMvaa 156,12,84\nTa 114,22,50176\nMvaa 152,12,80\nRa 48,27,40960\nMvaa 128,8,80\nTa 116,23,49152\n" \
"Mvaa 124,76,8\nTa 118,23,50176\nMvaa 148,76,12\nRa 52,27,43008\nMvaa 144,72,12\nRa 56,27,45056\nMvaa 120,72,8\nRa 60,27,47104\n" \
"s_add_u32 s95, s34, 24576\nMvaa 116,68,8\nbuffer_store_dwordx4 a[224:227], v0, s[60:63], s95 offen offset:0 sc1\nMvaa 140,68,12\nMvaa 136,64,12\nbuffer_store_dwordx4 a[228:231], v0, s[60:63], s95 offen offset:1024 sc1\nMvaa 112,64,8\nMvaa 160,64,16\n" \
"Mvaa 184,64,20\nMvaa 188,68,20\ns_waitcnt lgkmcnt(0)\ns_barrier\nMvaa 164,68,16\ns_add_u32 m0, s88, 32768\nMvaa 168,72,16\nD 9,64,0,0\n" \
"Mvaa 192,72,20\nMvaa 196,76,20\nMvaa 172,76,16\nMvaa 176,16,80\nD 9,64,s73,1024\nMvaa 200,20,80\nMvaa 204,20,84\nMvaa 180,16,84\n" \
"Mvaa 228,24,84\nD 9,64,s74,2048\nMvaa 252,28,84\nMvaa 248,28,80\nbuffer_store_dwordx4 a[232:235], v0, s[60:63], s95 offen offset:2048 sc1\nMvaa 224,24,80\nMvaa 220,76,24\nD 9,64,s75,3072\n" \
"Mvaa 244,76,28\nMvaa 240,72,28\nMvaa 216,72,24\ns_add_u32 m0, s88, 36864\nMvaa 212,68,24\nD 9,64,s76,0\nMvaa 236,68,28\nMvaa 232,64,28\n" \
"Mvaa 208,64,24\nMvaa 64,96,32\nD 9,64,s77,1024\nMvaa 88,96,36\nMvaa 92,100,36\nMvaa 68,100,32\nMvaa 72,104,32\nD 9,64,s78,2048\n" \
"Mvaa 96,104,36\nMvaa 100,108,36\nMvaa 76,108,32\nMvaa 80,32,112\nD 9,64,s79,3072\nMvaa 104,36,112\nMvaa 108,36,116\nbuffer_store_dwordx4 a[236:239], v0, s[60:63], s95 offen offset:3072 sc1\n" \
"Mvaa 84,32,116\ns_add_u32 m0, s32, 98304\nMvaa 132,40,116\nD 10,68,0,0\nMvaa 156,44,116\nMvaa 152,44,112\nMvaa 128,40,112\nMvaa 124,108,40\n" \
"D 10,68,s89,1024\nMvaa 148,108,44\nMvaa 144,104,44\nMvaa 120,104,40\nMvaa 116,100,40\nD 11,68,s90,2048\nMvaa 140,100,44\nMvaa 136,96,44\n" \
"s_waitcnt vmcnt(16)\ns_barrier\nMvaa 112,96,40\nTa 64,20,0\nMvaa 160,96,48\nD 11,68,s91,3072\nTa 66,20,1024\nMvaa 184,96,52\n" \
"Ra 0,7,0\nMvaa 188,100,52\nTa 68,21,0\nMvaa 164,100,48\ns_add_u32 m0, s92, 114688\nTa 70,21,1024\nMvaa 168,104,48\nD 12,68,0,0\n" \
"Ra 4,7,2048\nMvaa 192,104,52\nTa 72,22,0\nMvaa 196,108,52\nTa 74,22,1024\nMvaa 172,108,48\nRa 8,7,4096\nMvaa 176,48,112\n" \
"D 12,68,s89,1024\nTa 76,23,0\nMvaa 200,52,112\nTa 78,23,1024\nv_add_u32 v9, 128, v9\nMvaa 204,52,116\nRa 12,7,6144\nv_add_u32 v10, s72, v10\n" \
"Mvaa 180,48,116\nTa 80,20,16384\nv_add_u32 v11, s72, v11\nMvaa 228,56,116\nTa 82,20,17408\nv_add_u32 v12, s72, v12\nMvaa 252,60,116\nRa 16,7,8192\n" \
"Mvaa 248,60,112\nTa 84,21,16384\nMvaa 224,56,112\nTa 86,21,17408\nMvaa 220,108,56\nRa 20,7,10240\nMvaa 244,108,60\nRa 24,7,12288\n" \
"Mvaa 240,104,60\nRa 28,7,14336\ns_add_u32 s95, s34, 28672\nMvaa 216,104,56\nbuffer_store_dwordx4 v[32:35], v0, s[60:63], s95 offen offset:0 sc1\nMvaa 212,100,56\nMvaa 236,100,60\nMvaa 232,96,60\n" \
"Mvaa 208,96,56\ns_waitcnt lgkmcnt(0)\nMvaa 64,64,0\nTa 96,20,8192\nMvaa 88,64,4\nTa 98,20,9216\nMvaa 92,68,4\nRa 32,27,0\n" \
"Mvaa 68,68,0\nTa 100,21,8192\nMvaa 72,72,0\nTa 102,21,9216\nMvaa 96,72,4\nRa 36,27,2048\nMvaa 100,76,4\nTa 104,22,8192\n" \
"Mvaa 76,76,0\nTa 106,22,9216\nMvaa 80,0,80\nRa 40,27,4096\nMvaa 104,4,80\nTa 108,23,8192\nMvaa 108,4,84\nTa 110,23,9216\n" \
"Mvaa 84,0,84\nRa 44,27,6144\nMvaa 132,8,84\nTa 112,22,16384\nMvaa 156,12,84\nTa 114,22,17408\nMvaa 152,12,80\nRa 48,27,8192\n" \
"Mvaa 128,8,80\nTa 116,23,16384\nMvaa 124,76,8\nTa 118,23,17408\nMvaa 148,76,12\nRa 52,27,10240\nMvaa 144,72,12\nRa 56,27,12288\n" \
"Mvaa 120,72,8\nRa 60,27,14336\nMvaa 116,68,8\nbuffer_store_dwordx4 v[36:39], v0, s[60:63], s95 offen offset:1024 sc1\nMvaa 140,68,12\nMvaa 136,64,12\nbuffer_store_dwordx4 v[40:43], v0, s[60:63], s95 offen offset:2048 sc1\nMvaa 112,64,8\n" \
"Mvaa 160,64,16\nMvaa 184,64,20\nMvaa 188,68,20\ns_waitcnt lgkmcnt(0)\ns_barrier\nMvaa 164,68,16\ns_add_u32 m0, s88, 0\nMvaa 168,72,16\n" \
"D 9,64,0,0\nMvaa 192,72,20\nMvaa 196,76,20\nbuffer_store_dwordx4 v[44:47], v0, s[60:63], s95 offen offset:3072 sc1\nMvaa 172,76,16\nMvaa 176,16,80\nD 9,64,s73,1024\nMvaa 200,20,80\n" \
"Mvaa 204,20,84\nMvaa 180,16,84\nMvaa 228,24,84\nD 9,64,s74,2048\nMvaa 252,28,84\nMvaa 248,28,80\nMvaa 224,24,80\nMvaa 220,76,24\n" \
"D 9,64,s75,3072\nMvaa 244,76,28\nMvaa 240,72,28\nMvaa 216,72,24\ns_add_u32 m0, s88, 4096\nMvaa 212,68,24\nD 9,64,s76,0\nMvaa 236,68,28\n" \
"Mvaa 232,64,28\nMvaa 208,64,24\nMvaa 64,96,32\nD 9,64,s77,1024\nMvaa 88,96,36\nMvaa 92,100,36\nMvaa 68,100,32\nMvaa 72,104,32\n" \
"D 9,64,s78,2048\nMvaa 96,104,36\nMvaa 100,108,36\nMvaa 76,108,32\nMvaa 80,32,112\nD 9,64,s79,3072\nMvaa 104,36,112\nMvaa 108,36,116\n" \
"Mvaa 84,32,116\ns_add_u32 m0, s32, 65536\nMvaa 132,40,116\nD 10,68,0,0\nMvaa 156,44,116\nMvaa 152,44,112\nMvaa 128,40,112\nMvaa 124,108,40\n" \
"D 10,68,s89,1024\nMvaa 148,108,44\nMvaa 144,104,44\nMvaa 120,104,40\nMvaa 116,100,40\nD 11,68,s90,2048\nMvaa 140,100,44\nMvaa 136,96,44\n" \
"s_waitcnt vmcnt(10)\ns_barrier\nMvaa 112,96,40\nTa 64,20,32768\nMvaa 160,96,48\nD 11,68,s91,3072\nTa 66,20,33792\nMvaa 184,96,52\n" \
"Ra 0,7,32768\nbuffer_store_dwordx2 v[2:3], off, s[60:63], s35 sc1\nMvaa 188,100,52\nTa 68,21,32768\nMvaa 164,100,48\ns_add_u32 m0, s92, 81920\nTa 70,21,33792\nMvaa 168,104,48\n" \
"D 12,68,0,0\nRa 4,7,34816\nMvaa 192,104,52\nTa 72,22,32768\nMvaa 196,108,52\nTa 74,22,33792\nMvaa 172,108,48\nRa 8,7,36864\n" \
"Mvaa 176,48,112\nD 12,68,s89,1024\nTa 76,23,32768\nMvaa 200,52,112\nTa 78,23,33792\nv_add_u32 v9, 128, v9\nMvaa 204,52,116\nRa 12,7,38912\n" \
"v_add_u32 v10, s72, v10\nMvaa 180,48,116\nTa 80,20,49152\nv_add_u32 v11, s72, v11\nMvaa 228,56,116\nTa 82,20,50176\nv_add_u32 v12, s72, v12\nMvaa 252,60,116\n" \
"Ra 16,7,40960\nMvaa 248,60,112\nTa 84,21,49152\nMvaa 224,56,112\nTa 86,21,50176\nMvaa 220,108,56\nRa 20,7,43008\nMvaa 244,108,60\n" \
"Ra 24,7,45056\nMvaa 240,104,60\nRa 28,7,47104\nMvaa 216,104,56\nMvaa 212,100,56\nMvaa 236,100,60\nMvaa 232,96,60\nMvaa 208,96,56\n" \
"s_waitcnt lgkmcnt(0)\nMvaa 64,64,0\nTa 96,20,40960\ns_branch 99f\n.p2align 12\nzp30:\ns_setpc_b64 s[98:99]\n99:\n" \
"Mvaa 88,64,4\nTa 98,20,41984\nMvaa 92,68,4\nRa 32,27,32768\nMvaa 68,68,0\nTa 100,21,40960\nMvaa 72,72,0\nTa 102,21,41984\n" \
"Mvaa 96,72,4\nRa 36,27,34816\nMvaa 100,76,4\nTa 104,22,40960\nMvaa 76,76,0\nTa 106,22,41984\nMvaa 80,0,80\nRa 40,27,36864\n" \
"Mvaa 104,4,80\nTa 108,23,40960\nMvaa 108,4,84\nTa 110,23,41984\nMvaa 84,0,84\nRa 44,27,38912\nMvaa 132,8,84\nTa 112,22,49152\n" \
"Mvaa 156,12,84\nTa 114,22,50176\nMvaa 152,12,80\nRa 48,27,40960\nMvaa 128,8,80\nTa 116,23,49152\nMvaa 124,76,8\nTa 118,23,50176\n" \
"Mvaa 148,76,12\nRa 52,27,43008\nMvaa 144,72,12\nRa 56,27,45056\nMvaa 120,72,8\nRa 60,27,47104\nMvaa 116,68,8\nMvaa 140,68,12\n" \
"Mvaa 136,64,12\nMvaa 112,64,8\nMvaa 160,64,16\nMvaa 184,64,20\nMvaa 188,68,20\ns_waitcnt lgkmcnt(0)\ns_barrier\nMvaa 164,68,16\n" \
"s_add_u32 m0, s88, 32768\nMvaa 168,72,16\nD 9,64,0,0\nMvaa 192,72,20\nMvaa 196,76,20\nMvaa 172,76,16\nMvaa 176,16,80\nD 9,64,s73,1024\n" \
"Mvaa 200,20,80\nMvaa 204,20,84\nMvaa 180,16,84\nMvaa 228,24,84\nD 9,64,s74,2048\nMvaa 252,28,84\nMvaa 248,28,80\nMvaa 224,24,80\n" \
"Mvaa 220,76,24\nD 9,64,s75,3072\nMvaa 244,76,28\nMvaa 240,72,28\nMvaa 216,72,24\ns_add_u32 m0, s88, 36864\nMvaa 212,68,24\nD 9,64,s76,0\n" \
"Mvaa 236,68,28\nMvaa 232,64,28\nMvaa 208,64,24\nMvaa 64,96,32\nD 9,64,s77,1024\nMvaa 88,96,36\nMvaa 92,100,36\ns_mov_b32 s96, 3\n" \
"Mvaa 68,100,32\nMvaa 72,104,32\nD 9,64,s78,2048\nMvaa 96,104,36\nMvaa 100,108,36\nMvaa 76,108,32\nMvaa 80,32,112\nD 9,64,s79,3072\n" \
"Mvaa 104,36,112\nMvaa 108,36,116\nMvaa 84,32,116\ns_add_u32 m0, s32, 98304\nMvaa 132,40,116\nD 10,68,0,0\nMvaa 156,44,116\nMvaa 152,44,112\n" \
"Mvaa 128,40,112\nMvaa 124,108,40\nD 10,68,s89,1024\nMvaa 148,108,44\nMvaa 144,104,44\nMvaa 120,104,40\nMvaa 116,100,40\nD 11,68,s90,2048\n" \
"Mvaa 140,100,44\nMvaa 136,96,44\ns_waitcnt vmcnt(11)\ns_barrier\nMvaa 112,96,40\nTa 64,20,0\nMvaa 160,96,48\nD 11,68,s91,3072\n" \
"Ta 66,20,1024\nMvaa 184,96,52\nRa 0,7,0\nMvaa 188,100,52\nTa 68,21,0\nMvaa 164,100,48\ns_add_u32 m0, s92, 114688\nTa 70,21,1024\n" \
"Mvaa 168,104,48\nD 12,68,0,0\nRa 4,7,2048\nMvaa 192,104,52\nTa 72,22,0\nMvaa 196,108,52\nTa 74,22,1024\nMvaa 172,108,48\n" \
"Ra 8,7,4096\nMvaa 176,48,112\nD 12,68,s89,1024\nTa 76,23,0\nMvaa 200,52,112\nTa 78,23,1024\nv_add_u32 v9, 128, v9\nMvaa 204,52,116\n" \
"Ra 12,7,6144\nv_add_u32 v10, s72, v10\nMvaa 180,48,116\nTa 80,20,16384\nv_add_u32 v11, s72, v11\nMvaa 228,56,116\nTa 82,20,17408\nv_add_u32 v12, s72, v12\n" \
"Mvaa 252,60,116\nRa 16,7,8192\nMvaa 248,60,112\nTa 84,21,16384\nMvaa 224,56,112\nTa 86,21,17408\nMvaa 220,108,56\nRa 20,7,10240\n" \
"Mvaa 244,108,60\nRa 24,7,12288\nMvaa 240,104,60\nRa 28,7,14336\nMvaa 216,104,56\nMvaa 212,100,56\nMvaa 236,100,60\nMvaa 232,96,60\n" \
"Mvaa 208,96,56\ns_waitcnt lgkmcnt(0)\ns_branch 99f\n.p2align 12\nzp31:\ns_setpc_b64 s[98:99]\n99:\ns_nop 0\n" \
"zal:\nMvaa 64,64,0\nTa 96,20,8192\nMvaa 88,64,4\nTa 98,20,9216\nMvaa 92,68,4\nRa 32,27,0\nMvaa 68,68,0\n" \
"Ta 100,21,8192\nMvaa 72,72,0\nTa 102,21,9216\nMvaa 96,72,4\nRa 36,27,2048\nMvaa 100,76,4\nTa 104,22,8192\nMvaa 76,76,0\n" \
"Ta 106,22,9216\nMvaa 80,0,80\nRa 40,27,4096\nMvaa 104,4,80\nTa 108,23,8192\nMvaa 108,4,84\nTa 110,23,9216\nMvaa 84,0,84\n" \
"Ra 44,27,6144\nMvaa 132,8,84\nTa 112,22,16384\nMvaa 156,12,84\nTa 114,22,17408\nMvaa 152,12,80\nRa 48,27,8192\nMvaa 128,8,80\n" \
"Ta 116,23,16384\nMvaa 124,76,8\nTa 118,23,17408\nMvaa 148,76,12\nRa 52,27,10240\nMvaa 144,72,12\nRa 56,27,12288\nMvaa 120,72,8\n" \
"Ra 60,27,14336\nMvaa 116,68,8\nMvaa 140,68,12\nMvaa 136,64,12\nMvaa 112,64,8\nMvaa 160,64,16\nMvaa 184,64,20\nMvaa 188,68,20\n" \
"s_waitcnt lgkmcnt(0)\ns_barrier\nMvaa 164,68,16\ns_add_u32 m0, s88, 0\nMvaa 168,72,16\nD 9,64,0,0\nMvaa 192,72,20\nMvaa 196,76,20\n" \
"Mvaa 172,76,16\nMvaa 176,16,80\nD 9,64,s73,1024\nMvaa 200,20,80\nMvaa 204,20,84\nMvaa 180,16,84\nMvaa 228,24,84\nD 9,64,s74,2048\n" \
"Mvaa 252,28,84\nMvaa 248,28,80\nMvaa 224,24,80\nMvaa 220,76,24\nD 9,64,s75,3072\nMvaa 244,76,28\nMvaa 240,72,28\nMvaa 216,72,24\n" \
"s_add_u32 m0, s88, 4096\nMvaa 212,68,24\nD 9,64,s76,0\nMvaa 236,68,28\nMvaa 232,64,28\nMvaa 208,64,24\nMvaa 64,96,32\nD 9,64,s77,1024\n" \
"Mvaa 88,96,36\nMvaa 92,100,36\nMvaa 68,100,32\nMvaa 72,104,32\nD 9,64,s78,2048\nMvaa 96,104,36\nMvaa 100,108,36\nMvaa 76,108,32\n" \
"Mvaa 80,32,112\nD 9,64,s79,3072\nMvaa 104,36,112\nMvaa 108,36,116\nMvaa 84,32,116\ns_add_u32 m0, s32, 65536\nMvaa 132,40,116\nD 10,68,0,0\n" \
"Mvaa 156,44,116\nMvaa 152,44,112\nMvaa 128,40,112\nMvaa 124,108,40\nD 10,68,s89,1024\nMvaa 148,108,44\nMvaa 144,104,44\nMvaa 120,104,40\n" \
"Mvaa 116,100,40\nD 11,68,s90,2048\nMvaa 140,100,44\nMvaa 136,96,44\ns_waitcnt vmcnt(11)\ns_barrier\nMvaa 112,96,40\nTa 64,20,32768\n" \
"Mvaa 160,96,48\nD 11,68,s91,3072\nTa 66,20,33792\nMvaa 184,96,52\nRa 0,7,32768\nMvaa 188,100,52\nTa 68,21,32768\nMvaa 164,100,48\n" \
"s_add_u32 m0, s92, 81920\nTa 70,21,33792\nMvaa 168,104,48\nD 12,68,0,0\nRa 4,7,34816\nMvaa 192,104,52\nTa 72,22,32768\nMvaa 196,108,52\n" \
"Ta 74,22,33792\nMvaa 172,108,48\nRa 8,7,36864\nMvaa 176,48,112\nD 12,68,s89,1024\nTa 76,23,32768\nMvaa 200,52,112\nTa 78,23,33792\n" \
"v_add_u32 v9, 128, v9\nMvaa 204,52,116\nRa 12,7,38912\nv_add_u32 v10, s72, v10\nMvaa 180,48,116\nTa 80,20,49152\nv_add_u32 v11, s72, v11\nMvaa 228,56,116\n" \
"Ta 82,20,50176\nv_add_u32 v12, s72, v12\nMvaa 252,60,116\nRa 16,7,40960\nMvaa 248,60,112\nTa 84,21,49152\nMvaa 224,56,112\nTa 86,21,50176\n" \
"Mvaa 220,108,56\nRa 20,7,43008\nMvaa 244,108,60\nRa 24,7,45056\nMvaa 240,104,60\nRa 28,7,47104\nMvaa 216,104,56\nMvaa 212,100,56\n" \
"Mvaa 236,100,60\nMvaa 232,96,60\nMvaa 208,96,56\ns_waitcnt lgkmcnt(0)\nMvaa 64,64,0\nTa 96,20,40960\nMvaa 88,64,4\nTa 98,20,41984\n" \
"Mvaa 92,68,4\nRa 32,27,32768\nMvaa 68,68,0\nTa 100,21,40960\nMvaa 72,72,0\nTa 102,21,41984\nMvaa 96,72,4\nRa 36,27,34816\n" \
"Mvaa 100,76,4\nTa 104,22,40960\nMvaa 76,76,0\nTa 106,22,41984\nMvaa 80,0,80\nRa 40,27,36864\nMvaa 104,4,80\nTa 108,23,40960\n" \
"Mvaa 108,4,84\nTa 110,23,41984\nMvaa 84,0,84\nRa 44,27,38912\nMvaa 132,8,84\nTa 112,22,49152\nMvaa 156,12,84\nTa 114,22,50176\n" \
"Mvaa 152,12,80\nRa 48,27,40960\nMvaa 128,8,80\nTa 116,23,49152\nMvaa 124,76,8\nTa 118,23,50176\nMvaa 148,76,12\nRa 52,27,43008\n" \
"Mvaa 144,72,12\nRa 56,27,45056\nMvaa 120,72,8\nRa 60,27,47104\nMvaa 116,68,8\nMvaa 140,68,12\nMvaa 136,64,12\nMvaa 112,64,8\n" \
"Mvaa 160,64,16\nMvaa 184,64,20\nMvaa 188,68,20\ns_waitcnt lgkmcnt(0)\ns_barrier\nMvaa 164,68,16\ns_add_u32 m0, s88, 32768\nMvaa 168,72,16\n" \
"D 9,64,0,0\nMvaa 192,72,20\nMvaa 196,76,20\nMvaa 172,76,16\nMvaa 176,16,80\nD 9,64,s73,1024\nMvaa 200,20,80\nMvaa 204,20,84\n" \
"Mvaa 180,16,84\nMvaa 228,24,84\nD 9,64,s74,2048\nMvaa 252,28,84\nMvaa 248,28,80\nMvaa 224,24,80\nMvaa 220,76,24\nD 9,64,s75,3072\n" \
"Mvaa 244,76,28\nMvaa 240,72,28\nMvaa 216,72,24\ns_add_u32 m0, s88, 36864\nMvaa 212,68,24\nD 9,64,s76,0\nMvaa 236,68,28\nMvaa 232,64,28\n" \
"Mvaa 208,64,24\nMvaa 64,96,32\nD 9,64,s77,1024\nMvaa 88,96,36\nMvaa 92,100,36\nMvaa 68,100,32\nMvaa 72,104,32\nD 9,64,s78,2048\n" \
"Mvaa 96,104,36\nMvaa 100,108,36\nMvaa 76,108,32\nMvaa 80,32,112\nD 9,64,s79,3072\nMvaa 104,36,112\nMvaa 108,36,116\nMvaa 84,32,116\n" \
"s_add_u32 m0, s32, 98304\nMvaa 132,40,116\nD 10,68,0,0\nMvaa 156,44,116\nMvaa 152,44,112\nMvaa 128,40,112\nMvaa 124,108,40\nD 10,68,s89,1024\n" \
"Mvaa 148,108,44\nMvaa 144,104,44\nMvaa 120,104,40\nMvaa 116,100,40\nD 11,68,s90,2048\nMvaa 140,100,44\nMvaa 136,96,44\ns_waitcnt vmcnt(11)\n" \
"s_barrier\nMvaa 112,96,40\nTa 64,20,0\nMvaa 160,96,48\nD 11,68,s91,3072\nTa 66,20,1024\nMvaa 184,96,52\nRa 0,7,0\n" \
"Mvaa 188,100,52\nTa 68,21,0\nMvaa 164,100,48\ns_add_u32 m0, s92, 114688\nTa 70,21,1024\nMvaa 168,104,48\nD 12,68,0,0\nRa 4,7,2048\n" \
"Mvaa 192,104,52\nTa 72,22,0\nMvaa 196,108,52\nTa 74,22,1024\nMvaa 172,108,48\nRa 8,7,4096\nMvaa 176,48,112\nD 12,68,s89,1024\n" \
"Ta 76,23,0\nMvaa 200,52,112\nTa 78,23,1024\nv_add_u32 v9, 128, v9\nMvaa 204,52,116\nRa 12,7,6144\nv_add_u32 v10, s72, v10\nMvaa 180,48,116\n" \
"Ta 80,20,16384\nv_add_u32 v11, s72, v11\nMvaa 228,56,116\nTa 82,20,17408\nv_add_u32 v12, s72, v12\nMvaa 252,60,116\nRa 16,7,8192\nMvaa 248,60,112\n" \
"Ta 84,21,16384\nMvaa 224,56,112\nTa 86,21,17408\nMvaa 220,108,56\nRa 20,7,10240\nMvaa 244,108,60\nRa 24,7,12288\nMvaa 240,104,60\n" \
"Ra 28,7,14336\nMvaa 216,104,56\ns_sub_u32 s96, s96, 1\nMvaa 212,100,56\ns_cmp_lg_u32 s96, 0\nMvaa 236,100,60\nMvaa 232,96,60\nMvaa 208,96,56\n" \
"s_waitcnt lgkmcnt(0)\ns_cbranch_scc1 zal\nMvaa 64,64,0\nTa 96,20,8192\ns_mov_b64 s[64:65], s[84:85]\ns_mov_b32 s66, 4194304\nMvaa 88,64,4\nTa 98,20,9216\n" \
"s_mov_b64 s[68:69], s[86:87]\ns_mov_b32 s70, 8388608\nMvaa 92,68,4\nRa 32,27,0\ns_mov_b32 s73, 15360\ns_mov_b32 s74, 129024\nMvaa 68,68,0\nTa 100,21,8192\n" \
"s_mov_b32 s75, 144384\ns_add_u32 s76, s37, 0\nMvaa 72,72,0\nTa 102,21,9216\ns_add_u32 s77, s37, 15360\ns_add_u32 s78, s37, 129024\nMvaa 96,72,4\nRa 36,27,2048\n" \
"s_add_u32 s79, s37, 144384\ns_mov_b32 s89, 31744\nMvaa 100,76,4\nTa 104,22,8192\ns_mov_b32 s90, 63488\ns_mov_b32 s91, 95232\nMvaa 76,76,0\nTa 106,22,9216\n" \
"s_mov_b32 s72, 524288\nv_xor_b32 v28, 128, v14\nMvaa 80,0,80\nRa 40,27,4096\nMvaa 104,4,80\nTa 108,23,8192\nMvaa 108,4,84\nTa 110,23,9216\n" \
"Mvaa 84,0,84\nRa 44,27,6144\nMvaa 132,8,84\nTa 112,22,16384\nMvaa 156,12,84\nTa 114,22,17408\nMvaa 152,12,80\nRa 48,27,8192\n" \
"Mvaa 128,8,80\nTa 116,23,16384\nMvaa 124,76,8\nTa 118,23,17408\nMvaa 148,76,12\nRa 52,27,10240\nMvaa 144,72,12\nRa 56,27,12288\n" \
"Mvaa 120,72,8\nRa 60,27,14336\nMvaa 116,68,8\nMvaa 140,68,12\nMvaa 136,64,12\nMvaa 112,64,8\nMvaa 160,64,16\nMvaa 184,64,20\n" \
"Mvaa 188,68,20\ns_waitcnt lgkmcnt(0)\ns_barrier\nMvaa 164,68,16\ns_add_u32 m0, s88, 131072\nMvaa 168,72,16\nD 13,64,0,0\nMvaa 192,72,20\n" \
"Mvaa 196,76,20\nMvaa 172,76,16\nMvaa 176,16,80\nD 13,64,s73,1024\nMvaa 200,20,80\nMvaa 204,20,84\nMvaa 180,16,84\nMvaa 228,24,84\n" \
"D 13,64,s74,2048\nMvaa 252,28,84\nMvaa 248,28,80\nMvaa 224,24,80\nMvaa 220,76,24\nD 13,64,s75,3072\nMvaa 244,76,28\nMvaa 240,72,28\n" \
"Mvaa 216,72,24\ns_add_u32 m0, s88, 135168\nMvaa 212,68,24\nD 13,64,s76,0\nMvaa 236,68,28\nMvaa 232,64,28\nMvaa 208,64,24\nMvaa 64,96,32\n" \
"D 13,64,s77,1024\nMvaa 88,96,36\nMvaa 92,100,36\nMvaa 68,100,32\nMvaa 72,104,32\nD 13,64,s78,2048\nMvaa 96,104,36\nMvaa 100,108,36\n" \
"Mvaa 76,108,32\nMvaa 80,32,112\nD 13,64,s79,3072\nMvaa 104,36,112\nMvaa 108,36,116\nMvaa 84,32,116\ns_add_u32 m0, s32, 81920\nMvaa 132,40,116\n" \
"D 14,68,0,0\nMvaa 156,44,116\nMvaa 152,44,112\nMvaa 128,40,112\nMvaa 124,108,40\nD 14,68,s89,1024\nMvaa 148,108,44\nMvaa 144,104,44\n" \
"Mvaa 120,104,40\nMvaa 116,100,40\nD 28,68,s90,2048\nMvaa 140,100,44\nMvaa 136,96,44\ns_waitcnt vmcnt(11)\ns_barrier\nMvaa 112,96,40\n" \
"Ta 64,20,32768\nMvaa 160,96,48\nD 28,68,s91,3072\nTa 66,20,33792\nMvaa 184,96,52\nRa 0,7,32768\nMvaa 188,100,52\nTa 68,21,32768\n" \
"Mvaa 164,100,48\ns_add_u32 m0, s88, 0\nv_add_u32 v13, 128, v13\nTa 70,21,33792\nMvaa 168,104,48\nD 13,64,0,0\nRa 4,7,34816\nMvaa 192,104,52\n" \
"Ta 72,22,32768\nMvaa 196,108,52\nTa 74,22,33792\nMvaa 172,108,48\nRa 8,7,36864\nMvaa 176,48,112\nD 13,64,s73,1024\nTa 76,23,32768\n" \
"Mvaa 200,52,112\nTa 78,23,33792\nMvaa 204,52,116\nRa 12,7,38912\nMvaa 180,48,116\nTa 80,20,49152\nMvaa 228,56,116\nD 13,64,s74,2048\n" \
"Ta 82,20,50176\nMvaa 252,60,116\nRa 16,7,40960\nMvaa 248,60,112\nTa 84,21,49152\nMvaa 224,56,112\nTa 86,21,50176\nMvaa 220,108,56\n" \
"D 13,64,s75,3072\nRa 20,7,43008\nMvaa 244,108,60\nRa 24,7,45056\nMvaa 240,104,60\nRa 28,7,47104\nMvaa 216,104,56\nMvaa 212,100,56\n" \
"Mvaa 236,100,60\nMvaa 232,96,60\nMvaa 208,96,56\ns_waitcnt lgkmcnt(0)\nMvaa 64,64,0\nTa 96,20,40960\nMvaa 88,64,4\nTa 98,20,41984\n" \
"Mvaa 92,68,4\nRa 32,27,32768\nMvaa 68,68,0\nTa 100,21,40960\nMvaa 72,72,0\nTa 102,21,41984\nMvaa 96,72,4\nRa 36,27,34816\n" \
"Mvaa 100,76,4\nTa 104,22,40960\nMvaa 76,76,0\nTa 106,22,41984\nMvaa 80,0,80\nRa 40,27,36864\ns_branch 99f\n.p2align 12\n" \
"zp32:\ns_setpc_b64 s[98:99]\n99:\nMvaa 104,4,80\nTa 108,23,40960\nMvaa 108,4,84\nTa 110,23,41984\nMvaa 84,0,84\n" \
"Ra 44,27,38912\nMvaa 132,8,84\nTa 112,22,49152\nMvaa 156,12,84\nTa 114,22,50176\nMvaa 152,12,80\nRa 48,27,40960\nMvaa 128,8,80\n" \
"Ta 116,23,49152\nMvaa 124,76,8\nTa 118,23,50176\nMvaa 148,76,12\nRa 52,27,43008\nMvaa 144,72,12\nRa 56,27,45056\ns_and_b32 s96, s32, 0x1000\n" \
"s_lshl_b32 s97, s96, 1\nMvaa 120,72,8\nRa 60,27,47104\ns_lshl_b32 s96, s96, 2\nv_add_u32 v16, s97, v7\nMvaa 116,68,8\nv_subrev_u32 v20, s96, v8\nv_xor_b32 v17, 64, v16\n" \
"v_add_u32 v18, 0x20000, v16\nMvaa 140,68,12\nv_add_u32 v19, 0x20000, v17\nv_xor_b32 v21, 32, v20\nv_xor_b32 v22, 64, v20\nMvaa 136,64,12\nv_xor_b32 v23, 96, v20\nv_xor_b32 v24, 128, v20\n" \
"v_xor_b32 v25, 160, v20\nMvaa 112,64,8\nv_xor_b32 v26, 192, v20\nv_xor_b32 v27, 224, v20\nMvaa 160,64,16\nMvaa 184,64,20\nMvaa 188,68,20\ns_waitcnt vmcnt(4) lgkmcnt(0)\n" \
"s_barrier\nMvaa 164,68,16\ns_add_u32 m0, s88, 4096\nMvaa 168,72,16\nD 13,64,s76,0\nMvaa 192,72,20\nTa 160,20,24576\nTa 162,20,25600\n" \
"Mvaa 196,76,20\nTa 164,21,24576\nMvaa 172,76,16\nD 13,64,s77,1024\nMvaa 176,16,80\nTa 166,21,25600\nTa 168,22,24576\nMvaa 200,20,80\n" \
"Ta 170,22,25600\nMvaa 204,20,84\nD 13,64,s78,2048\nMvaa 180,16,84\nTa 172,23,24576\nTa 174,23,25600\nMvaa 228,24,84\nTa 176,24,24576\n" \
"Mvaa 252,28,84\nD 13,64,s79,3072\nMvaa 248,28,80\nTa 178,24,25600\nTa 180,25,24576\nMvaa 224,24,80\ns_add_u32 m0, s32, 65536\nv_add_u32 v14, s72, v14\n" \
"v_add_u32 v28, s72, v28\nMvaa 220,76,24\nD 14,68,0,0\nMvaa 244,76,28\nTa 182,25,25600\nMvaa 240,72,28\nTa 184,26,24576\nTa 186,26,25600\n" \
"Mvaa 216,72,24\nD 14,68,s89,1024\nMvaa 212,68,24\nTa 188,27,24576\nMvaa 236,68,28\nTa 190,27,25600\nMvaa 232,64,28\nD 28,68,s90,2048\n" \
"Mvaa 208,64,24\nMvaa 112,96,40\nTa 64,20,16384\nMvaa 116,100,40\nD 28,68,s91,3072\nMvaa 120,104,40\nTa 66,20,17408\nMvaa 124,108,40\n" \
"s_add_u32 m0, s88, 32768\nv_add_u32 v13, 128, v13\nMvaa 128,40,112\nD 13,64,0,0\nMvaa 132,40,116\nRa 0,18,0\nMvaa 136,96,44\nRa 16,19,0\n" \
"Mvaa 140,100,44\nD 13,64,s73,1024\nMvaa 144,104,44\nTa 68,21,16384\nMvaa 148,108,44\nTa 70,21,17408\nC 112,112,113\nC 113,114,115\n" \
"Mvaa 152,44,112\nD 13,64,s74,2048\nC 114,116,117\nMvaa 156,44,116\nTa 72,22,16384\nC 115,118,119\nC 120,120,121\nMvaa 160,96,48\n" \
"C 121,122,123\nC 122,124,125\nC 123,126,127\nMvaa 164,100,48\nD 13,64,s75,3072\nC 38,128,129\nMvaa 168,104,48\nTa 74,22,17408\n" \
"C 39,130,131\nC 56,132,133\nMvaa 172,108,48\ns_add_u32 m0, s88, 36864\nTa 76,23,16384\nC 57,134,135\nMvaa 176,48,112\nD 13,64,s76,0\n" \
"AW 200,112\nMvaa 180,48,116\nTa 78,23,17408\nAW 201,113\nAW 202,114\nMvaa 184,96,52\nTa 80,24,16384\nAW 203,115\n" \
"AW 232,120\nMvaa 188,100,52\nD 13,64,s77,1024\nAW 233,121\nMvaa 192,104,52\nAW 234,122\nAW 235,123\nC 136,136,137\n" \
"Mvaa 196,108,52\nTa 82,24,17408\nC 137,138,139\nC 138,140,141\nMvaa 200,52,112\nD 13,64,s78,2048\nC 139,142,143\nMvaa 204,52,116\n" \
"Ta 84,25,16384\nC 144,144,145\nC 145,146,147\nMvaa 64,96,32\nTa 86,25,17408\nC 146,148,149\nC 147,150,151\nMvaa 68,100,32\n" \
"D 13,64,s79,3072\nC 40,152,153\nMvaa 72,104,32\nTa 88,26,16384\nC 41,154,155\nC 58,156,157\nMvaa 76,108,32\ns_add_u32 m0, s32, 98304\n" \
"v_add_u32 v14, s72, v14\nv_add_u32 v28, s72, v28\nMvaa 80,32,112\nD 14,68,0,0\nC 59,158,159\nMvaa 84,32,116\nTa 90,26,17408\nAW 204,136\n" \
"AW 205,137\nMvaa 208,96,56\nTa 92,27,16384\nAW 206,138\nAW 207,139\nMvaa 212,100,56\nD 14,68,s89,1024\nAW 236,144\n" \
"Mvaa 216,104,56\nTa 94,27,17408\nAW 237,145\nAW 238,146\nMvaa 220,108,56\nAW 239,147\nC 160,160,161\nC 161,162,163\n" \
"Mvaa 224,56,112\nD 28,68,s90,2048\nC 162,164,165\nMvaa 228,56,116\nRa 4,18,2048\nC 163,166,167\nC 168,168,169\nMvaa 232,96,60\n" \
"Ra 20,19,2048\nC 169,170,171\nC 170,172,173\nMvaa 236,100,60\nD 28,68,s91,3072\nC 171,174,175\nMvaa 240,104,60\nRa 8,18,4096\n" \
"C 42,176,177\nC 43,178,179\nMvaa 244,108,60\nRa 24,19,4096\nC 60,180,181\nC 61,182,183\nMvaa 248,60,112\nRa 12,18,6144\n" \
"AW 208,160\nAW 209,161\nMvaa 252,60,116\nRa 28,19,6144\nAW 210,162\nAW 211,163\nMvaa 88,96,36\nAW 240,168\n" \
"AW 241,169\nAW 242,170\nMvaa 92,100,36\nAW 243,171\nC 184,184,185\nC 185,186,187\nMvaa 96,104,36\nC 186,188,189\n" \
"C 187,190,191\nC 192,192,193\nMvaa 100,108,36\nC 193,194,195\nC 194,196,197\nC 195,198,199\nMvaa 104,36,112\nC 44,200,201\n" \
"C 45,202,203\nC 62,204,205\nMvaa 108,36,116\nC 63,206,207\nAW 212,184\nAW 213,185\ns_waitcnt lgkmcnt(6)\nZvaa 128,64,0\n" \
"v_add_u32 v13, 128, v13\nAW 214,186\nAW 215,187\nMvaa 128,160,16\ns_add_u32 m0, s88, 131072\nAW 244,192\nAW 245,193\nZvaa 132,164,16\n" \
"D 13,64,0,0\nAW 246,194\nMvaa 132,68,0\nv_add_u32 v14, s72, v14\nAW 247,195\nC 64,64,65\nZvaa 136,72,0\nv_add_u32 v28, s72, v28\n" \
"C 65,66,67\nC 66,68,69\nMvaa 136,168,16\nC 67,70,71\nC 72,72,73\nC 73,74,75\nZvaa 140,172,16\nC 74,76,77\n" \
"C 75,78,79\nC 34,80,81\nMvaa 140,76,0\ns_waitcnt lgkmcnt(0)\nD 13,64,s73,1024\nZvaa 144,80,0\nC 35,82,83\nC 52,84,85\n" \
"C 53,86,87\nMvaa 144,176,16\nAW 192,64\nAW 193,65\nAW 194,66\nZvaa 148,180,16\nAW 195,67\nAW 224,72\n" \
"AW 225,73\nMvaa 148,84,0\nAW 226,74\nAW 227,75\nC 208,208,209\nZvaa 152,88,0\nD 13,64,s74,2048\nC 209,210,211\n" \
"Mvaa 152,184,16\nC 210,212,213\nC 211,214,215\nC 216,216,217\nZvaa 156,188,16\nC 217,218,219\nC 218,220,221\nC 219,222,223\n" \
"Mvaa 156,92,0\nC 46,224,225\nC 47,226,227\nC 64,228,229\nZvaa 188,92,4\ns_waitcnt vmcnt(15)\ns_barrier\nC 65,230,231\n" \
"Mvaa 188,188,20\nTa 96,20,0\nTa 98,20,1024\nAW 216,208\nZvaa 184,184,20\nD 13,64,s75,3072\nAW 217,209\nMvaa 184,88,4\n" \
"Ta 128,20,8192\nTa 130,20,9216\nAW 218,210\nZvaa 180,84,4\nAW 219,211\nAW 248,216\nAW 249,217\nMvaa 180,180,20\n" \
"Ra 32,16,0\nRa 48,17,0\nAW 250,218\nZvaa 176,176,20\ns_add_u32 m0, s88, 135168\nTa 100,21,0\nAW 251,219\nMvaa 176,80,4\n" \
"D 13,64,s76,0\nC 232,232,233\nZvaa 172,76,4\nTa 102,21,1024\nTa 132,21,8192\nC 233,234,235\nMvaa 172,172,20\nC 234,236,237\n" \
"C 235,238,239\nC 240,240,241\nZvaa 168,168,20\nTa 134,21,9216\nTa 104,22,0\nC 241,242,243\nMvaa 168,72,4\nTa 106,22,1024\n" \
"Ta 136,22,8192\nC 242,244,245\nZvaa 164,68,4\nD 13,64,s77,1024\nC 243,246,247\nMvaa 164,164,20\nC 48,248,249\nC 49,250,251\n" \
"C 66,252,253\nZvaa 160,160,20\nTa 138,22,9216\nTa 108,23,0\nC 67,254,255\nMvaa 160,64,4\nAW 220,232\nAW 221,233\n" \
"AW 222,234\nZvaa 192,64,8\nTa 110,23,1024\nTa 140,23,8192\nAW 223,235\nMvaa 192,160,24\nD 13,64,s78,2048\nAW 252,240\n" \
"Zvaa 196,164,24\nTa 142,23,9216\nTa 112,24,0\nAW 253,241\nMvaa 196,68,8\nAW 254,242\nAW 255,243\nC 88,88,89\n" \
"Zvaa 200,72,8\nTa 114,24,1024\nTa 144,24,8192\nC 89,90,91\nMvaa 200,168,24\nTa 146,24,9216\nTa 116,25,0\nC 90,92,93\n" \
"Zvaa 204,172,24\nD 13,64,s79,3072\nC 91,94,95\nMvaa 204,76,8\nC 96,96,97\nC 97,98,99\nC 98,100,101\nZvaa 208,80,8\n" \
"Ta 118,25,1024\nTa 148,25,8192\nC 99,102,103\nMvaa 208,176,24\nv_add_u32 v13, 128, v13\nTa 150,25,9216\nC 36,104,105\nZvaa 212,180,24\n" \
"s_add_u32 m0, s32, 81920\nTa 120,26,0\nC 37,106,107\nMvaa 212,84,8\nD 14,68,0,0\nC 54,108,109\nZvaa 216,88,8\nTa 122,26,1024\n" \
"Ta 152,26,8192\nC 55,110,111\nMvaa 216,184,24\nAW 196,88\nAW 197,89\nAW 198,90\nZvaa 220,188,24\nTa 154,26,9216\n" \
"Ta 124,27,0\nAW 199,91\nMvaa 220,92,8\nAW 228,96\nAW 229,97\nAW 230,98\nZvaa 252,92,12\nD 14,68,s89,1024\n" \
"AW 231,99\nMvaa 252,188,28\nTa 126,27,1024\nTa 156,27,8192\nZvaa 248,184,28\nTa 158,27,9216\nRa 36,16,2048\nMvaa 248,88,12\n" \
"Zvaa 244,84,12\nRa 52,17,2048\nRa 40,16,4096\nMvaa 244,180,28\nD 28,68,s90,2048\nZvaa 240,176,28\nRa 56,17,4096\nRa 44,16,6144\n" \
"Mvaa 240,80,12\nZvaa 236,76,12\nRa 60,17,6144\nMvaa 236,172,28\nZvaa 232,168,28\nD 28,68,s91,3072\nMvaa 232,72,12\nZvaa 228,68,12\n" \
"Mvaa 228,164,28\nv_add_u32 v14, s72, v14\nZvaa 224,160,28\nv_add_u32 v28, s72, v28\nMvaa 224,64,12\ns_waitcnt lgkmcnt(6)\nMvaa 128,96,32\nbuffer_store_dwordx4 a[252:255], v0, s[60:63], s46 offen sc1\n" \
"Mvaa 128,128,48\ns_add_u32 m0, s88, 0\nMvaa 132,132,48\nD 13,64,0,0\nMvaa 132,100,32\nbuffer_store_dwordx2 v[48:49], v0, s[60:63], s46 offen offset:1024 sc1\nMvaa 136,104,32\nbuffer_store_dwordx2 v[66:67], v0, s[60:63], s46 offen offset:1032 sc1\n" \
"Mvaa 136,136,48\nv_mov_b32 v100, 0\nv_bfe_u32 v104, v0, 4, 4\nMvaa 140,140,48\nv_lshrrev_b32 v105, 6, v0\nv_and_b32 v105, 12, v105\nMvaa 140,108,32\ns_waitcnt lgkmcnt(0)\n" \
"D 13,64,s73,1024\nMvaa 144,112,32\nv_sub_u32 v104, v104, v105\nv_mov_b32 v107, 0xff800000\nMvaa 144,144,48\ns_mov_b32 s93, 0x3e8293ee\nv_subrev_u32 v105, 0, v104\nMvaa 148,148,48\n" \
"v_and_b32 v106, 0x80000001, v105\nv_cmp_eq_u32 vcc, 0, v106\nMvaa 148,116,32\nv_mov_b32 v84, 0x3f803f80\ns_nop 0\nMvaa 152,120,32\nD 13,64,s74,2048\nMvaa 152,152,48\n" \
"v_cndmask_b32 v76, v107, v100, vcc\nv_cmp_eq_u32 vcc, 0x80000000, v106\nMvaa 156,156,48\ns_nop 1\nv_cndmask_b32 v72, v107, v100, vcc\nMvaa 156,124,32\nv_subrev_u32 v105, 1, v104\nv_and_b32 v106, 0x80000001, v105\n" \
"Mvaa 188,124,36\ns_waitcnt vmcnt(18)\ns_barrier\nMvaa 188,156,52\nTa 64,20,32768\nTa 66,20,33792\nMvaa 184,152,52\nD 13,64,s75,3072\n" \
"Mvaa 184,120,36\nTa 160,20,40960\nTa 162,20,41984\nMvaa 180,116,36\nv_cmp_eq_u32 vcc, 0, v106\ns_branch 99f\n.p2align 12\nzp33:\n" \
"s_setpc_b64 s[98:99]\n99:\nv_mov_b32 v85, 0x3f803f80\nMvaa 180,148,52\nRa 0,16,32768\nRa 16,17,32768\nMvaa 176,144,52\ns_add_u32 m0, s88, 4096\n" \
"Ta 68,21,32768\nMvaa 176,112,36\nD 13,64,s76,0\nMvaa 172,108,36\nTa 70,21,33792\nTa 164,21,40960\nMvaa 172,140,52\ns_nop 0\n" \
"v_cndmask_b32 v77, v107, v100, vcc\nMvaa 168,136,52\nTa 166,21,41984\nTa 72,22,32768\nMvaa 168,104,36\nTa 74,22,33792\nTa 168,22,40960\nMvaa 164,100,36\n" \
"D 13,64,s77,1024\nMvaa 164,132,52\nv_cmp_eq_u32 vcc, 0x80000000, v106\ns_nop 1\nMvaa 160,128,52\nTa 170,22,41984\nTa 76,23,32768\nMvaa 160,96,36\n" \
"v_cndmask_b32 v73, v107, v100, vcc\nv_subrev_u32 v105, 2, v104\nMvaa 192,96,40\nTa 78,23,33792\nTa 172,23,40960\nMvaa 192,128,56\nD 13,64,s78,2048\nMvaa 196,132,56\n" \
"Ta 174,23,41984\nTa 80,24,32768\nMvaa 196,100,40\nv_and_b32 v106, 0x80000001, v105\nv_cmp_eq_u32 vcc, 0, v106\nMvaa 200,104,40\nTa 82,24,33792\nTa 176,24,40960\n" \
"Mvaa 200,136,56\nTa 178,24,41984\nTa 84,25,32768\nMvaa 204,140,56\nD 13,64,s79,3072\nMvaa 204,108,40\nv_mov_b32 v86, 0x3f803f80\ns_nop 0\n" \
"Mvaa 208,112,40\nTa 86,25,33792\nTa 180,25,40960\nMvaa 208,144,56\nv_add_u32 v13, 128, v13\nTa 182,25,41984\nMvaa 212,148,56\ns_add_u32 m0, s32, 65536\n" \
"Ta 88,26,32768\nMvaa 212,116,40\nD 14,68,0,0\nMvaa 216,120,40\nTa 90,26,33792\nTa 184,26,40960\nMvaa 216,152,56\nv_cndmask_b32 v78, v107, v100, vcc\n" \
"v_cmp_eq_u32 vcc, 0x80000000, v106\nMvaa 220,156,56\nTa 186,26,41984\nTa 92,27,32768\nMvaa 220,124,40\ns_nop 1\nv_cndmask_b32 v74, v107, v100, vcc\nMvaa 252,124,44\n" \
"D 14,68,s89,1024\nMvaa 252,156,60\nTa 94,27,33792\nTa 188,27,40960\nMvaa 248,152,60\nTa 190,27,41984\nRa 4,16,34816\nMvaa 248,120,44\n" \
"v_subrev_u32 v105, 3, v104\nv_and_b32 v106, 0x80000001, v105\nMvaa 244,116,44\nRa 20,17,34816\nRa 8,16,36864\nMvaa 244,148,60\nD 28,68,s90,2048\nMvaa 240,144,60\n" \
"Ra 24,17,36864\nRa 12,16,38912\nMvaa 240,112,44\nv_cmp_eq_u32 vcc, 0, v106\nv_mov_b32 v87, 0x3f803f80\nMvaa 236,108,44\nRa 28,17,38912\ns_nop 0\n" \
"Mvaa 236,140,60\nv_cndmask_b32 v79, v107, v100, vcc\nv_cmp_eq_u32 vcc, 0x80000000, v106\nMvaa 232,136,60\nD 28,68,s91,3072\nMvaa 232,104,44\ns_nop 1\nv_cndmask_b32 v75, v107, v100, vcc\n" \
"Mvaa 228,100,44\ns_cmp_eq_u32 s51, 0\ns_cselect_b64 vcc, -1, 0\nMvaa 228,132,60\nv_add_u32 v14, s72, v14\nv_cndmask_b32 v80, v107, v72, vcc\nMvaa 224,128,60\nv_add_u32 v28, s72, v28\n" \
"v_cndmask_b32 v81, v107, v73, vcc\nMvaa 224,96,44\ns_waitcnt lgkmcnt(6)\nv_cndmask_b32 v82, v107, v74, vcc\nMvaa 128,64,0\nv_cndmask_b32 v83, v107, v75, vcc\nQvaa 88,224,196,72\nQvaa 92,228,196,76\n" \
"Qvaa 108,228,200,72\nQvaa 112,232,200,76\nMvaa 128,160,16\ns_add_u32 m0, s88, 32768\nMvaa 132,164,16\nD 13,64,0,0\nMvaa 132,68,0\nMvaa 136,72,0\n" \
"v_max3_f32 v104, v88, v89, v90\nv_max3_f32 v105, v91, v92, v93\nMvaa 136,168,16\nv_max3_f32 v104, v104, v94, v95\nv_max_f32 v104, v104, v105\nMvaa 140,172,16\nv_mov_b32 v105, v104\nv_max3_f32 v124, v108, v109, v110\n" \
"Mvaa 140,76,0\ns_waitcnt lgkmcnt(0)\nD 13,64,s73,1024\nMvaa 144,80,0\nv_max3_f32 v125, v111, v112, v113\nv_max3_f32 v124, v124, v114, v115\nMvaa 144,176,16\nv_max_f32 v124, v124, v125\n" \
"v_mov_b32 v125, v124\nMvaa 148,180,16\nv_permlane16_swap_b32 v104, v105\nv_permlane16_swap_b32 v124, v125\nMvaa 148,84,0\nv_max_f32 v104, v104, v105\nv_mov_b32 v105, v104\nMvaa 152,88,0\n" \
"D 13,64,s74,2048\nMvaa 152,184,16\nv_max_f32 v124, v124, v125\nv_mov_b32 v125, v124\nMvaa 156,188,16\nv_permlane32_swap_b32 v104, v105\nv_permlane32_swap_b32 v124, v125\nMvaa 156,92,0\n" \
"v_max_f32 v104, v104, v105\nv_mul_f32 v104, s93, v104\nMvaa 188,92,4\ns_waitcnt vmcnt(18)\ns_barrier\nMvaa 188,188,20\nTa 96,20,16384\nTa 98,20,17408\n" \
"Mvaa 184,184,20\nD 13,64,s75,3072\nMvaa 184,88,4\nTa 128,20,24576\nTa 130,20,25600\nMvaa 180,84,4\nv_fma_f32 v88, v88, s93, -v104\nv_fma_f32 v89, v89, s93, -v104\n" \
"Mvaa 180,180,20\nRa 32,18,0\nRa 48,19,0\nMvaa 176,176,20\ns_add_u32 m0, s88, 36864\nTa 100,21,16384\nMvaa 176,80,4\nD 13,64,s76,0\n" \
"Mvaa 172,76,4\nTa 102,21,17408\nTa 132,21,24576\nMvaa 172,172,20\nv_fma_f32 v90, v90, s93, -v104\nv_fma_f32 v91, v91, s93, -v104\nMvaa 168,168,20\nTa 134,21,25600\n" \
"Ta 104,22,16384\nMvaa 168,72,4\nTa 106,22,17408\nTa 136,22,24576\nMvaa 164,68,4\nD 13,64,s77,1024\nMvaa 164,164,20\nv_fma_f32 v92, v92, s93, -v104\n" \
"v_fma_f32 v93, v93, s93, -v104\nMvaa 160,160,20\nTa 138,22,25600\nTa 108,23,16384\nMvaa 160,64,4\nv_fma_f32 v94, v94, s93, -v104\nv_fma_f32 v95, v95, s93, -v104\nMvaa 192,64,8\n" \
"Ta 110,23,17408\nTa 140,23,24576\nMvaa 192,160,24\nD 13,64,s78,2048\nMvaa 196,164,24\nTa 142,23,25600\nTa 112,24,16384\nMvaa 196,68,8\n" \
"E 88,88\nMvaa 200,72,8\nTa 114,24,17408\nTa 144,24,24576\nMvaa 200,168,24\nTa 146,24,25600\nTa 116,25,16384\nMvaa 204,172,24\n" \
"D 13,64,s79,3072\nMvaa 204,76,8\nE 89,89\nMvaa 208,80,8\nTa 118,25,17408\nTa 148,25,24576\nMvaa 208,176,24\nv_add_u32 v13, 128, v13\n" \
"Ta 150,25,25600\nMvaa 212,180,24\ns_add_u32 m0, s32, 98304\nTa 120,26,16384\nMvaa 212,84,8\nD 14,68,0,0\nMvaa 216,88,8\nTa 122,26,17408\n" \
"Ta 152,26,24576\nMvaa 216,184,24\nE 90,90\nMvaa 220,188,24\nTa 154,26,25600\nTa 124,27,16384\nMvaa 220,92,8\nC 96,88,89\n" \
"Mvaa 252,92,12\nD 14,68,s89,1024\nMvaa 252,188,28\nTa 126,27,17408\nTa 156,27,24576\nMvaa 248,184,28\nTa 158,27,25600\nRa 36,18,2048\n" \
"Mvaa 248,88,12\nE 91,91\nMvaa 244,84,12\nRa 52,19,2048\nRa 40,18,4096\nMvaa 244,180,28\nD 28,68,s90,2048\nMvaa 240,176,28\n" \
"Ra 56,19,4096\nRa 44,18,6144\nMvaa 240,80,12\nE 92,92\nMvaa 236,76,12\nRa 60,19,6144\nC 97,90,91\nMvaa 236,172,28\n" \
"E 93,93\nMvaa 232,168,28\nD 28,68,s91,3072\nMvaa 232,72,12\nE 94,94\nMvaa 228,68,12\nC 98,92,93\nMvaa 228,164,28\n" \
"v_add_u32 v14, s72, v14\nMvaa 224,160,28\nv_add_u32 v28, s72, v28\nMvaa 224,64,12\ns_waitcnt lgkmcnt(6)\nMvaa 128,96,32\nE 95,95\nMvaa 128,128,48\n" \
"s_add_u32 m0, s88, 131072\nv_max_f32 v124, v124, v125\nMvaa 132,132,48\nD 13,64,0,0\nMvaa 132,100,32\nv_mul_f32 v124, s93, v124\nv_fma_f32 v108, v108, s93, -v124\nMvaa 136,104,32\n" \
"v_fma_f32 v109, v109, s93, -v124\nv_fma_f32 v110, v110, s93, -v124\nMvaa 136,136,48\nv_fma_f32 v111, v111, s93, -v124\nv_fma_f32 v112, v112, s93, -v124\nMvaa 140,140,48\nv_fma_f32 v113, v113, s93, -v124\nv_fma_f32 v114, v114, s93, -v124\n" \
"Mvaa 140,108,32\ns_waitcnt lgkmcnt(0)\nD 13,64,s73,1024\nMvaa 144,112,32\nv_fma_f32 v115, v115, s93, -v124\nMvaa 144,144,48\nE 108,108\nMvaa 148,148,48\n" \
"E 109,109\nMvaa 148,116,32\nE 110,110\nMvaa 152,120,32\nD 13,64,s74,2048\nMvaa 152,152,48\nC 116,108,109\nMvaa 156,156,48\n" \
"E 111,111\nMvaa 156,124,32\nE 112,112\nMvaa 188,124,36\ns_waitcnt vmcnt(15)\ns_barrier\nMvaa 188,156,52\nTa 64,20,0\n" \
"Ta 66,20,1024\nMvaa 184,152,52\nD 13,64,s75,3072\nMvaa 184,120,36\nTa 160,20,8192\nTa 162,20,9216\nbuffer_store_dwordx2 v[2:3], off, s[60:63], s48 sc1\nMvaa 180,116,36\n" \
"C 117,110,111\nMvaa 180,148,52\nRa 0,16,0\nRa 16,17,0\nMvaa 176,144,52\ns_add_u32 m0, s88, 135168\nTa 68,21,0\nMvaa 176,112,36\n" \
"D 13,64,s76,0\nMvaa 172,108,36\nTa 70,21,1024\nTa 164,21,8192\nMvaa 172,140,52\nE 113,113\nMvaa 168,136,52\nTa 166,21,9216\n" \
"Ta 72,22,0\nMvaa 168,104,36\nTa 74,22,1024\nTa 168,22,8192\nMvaa 164,100,36\nD 13,64,s77,1024\nMvaa 164,132,52\nE 114,114\n" \
"Mvaa 160,128,52\nTa 170,22,9216\nTa 76,23,0\nMvaa 160,96,36\nC 118,112,113\nMvaa 192,96,40\nTa 78,23,1024\nTa 172,23,8192\n" \
"Mvaa 192,128,56\nD 13,64,s78,2048\nMvaa 196,132,56\nTa 174,23,9216\nTa 80,24,0\nMvaa 196,100,40\nE 115,115\nMvaa 200,104,40\n" \
"Ta 82,24,1024\nTa 176,24,8192\nMvaa 200,136,56\nTa 178,24,9216\nTa 84,25,0\nMvaa 204,140,56\nD 13,64,s79,3072\nMvaa 204,108,40\n" \
"C 99,94,95\nC 119,114,115\nMvaa 208,112,40\nTa 86,25,1024\nTa 180,25,8192\nZvvv 100,84,96\nZvvv 88,34,96\nZvvv 92,52,96\n" \
"Zvvv 120,84,116\nZvvv 108,36,116\nZvvv 112,54,116\nMvaa 208,144,56\nv_add_u32 v13, 128, v13\nTa 182,25,9216\nMvaa 212,148,56\ns_add_u32 m0, s32, 81920\n" \
"Ta 88,26,0\nMvaa 212,116,40\nD 14,68,0,0\nMvaa 216,120,40\nTa 90,26,1024\nTa 184,26,8192\nMvaa 216,152,56\nv_rcp_f32 v104, v100\n" \
"Mvaa 220,156,56\nTa 186,26,9216\nTa 92,27,0\nMvaa 220,124,40\nv_rcp_f32 v124, v120\nMvaa 252,124,44\nD 14,68,s89,1024\nMvaa 252,156,60\n" \
"Ta 94,27,1024\nTa 188,27,8192\nMvaa 248,152,60\nTa 190,27,9216\nRa 4,16,2048\nMvaa 248,120,44\nv_mul_f32 v88, v88, v104\nv_mul_f32 v89, v89, v104\n" \
"Mvaa 244,116,44\nRa 20,17,2048\nRa 8,16,4096\nMvaa 244,148,60\nD 28,68,s90,2048\nMvaa 240,144,60\nRa 24,17,4096\nRa 12,16,6144\n" \
"Mvaa 240,112,44\nv_mul_f32 v90, v90, v104\nv_mul_f32 v91, v91, v104\nMvaa 236,108,44\nRa 28,17,6144\nv_mul_f32 v92, v92, v104\nMvaa 236,140,60\nv_mul_f32 v93, v93, v104\n" \
"v_mul_f32 v94, v94, v104\nMvaa 232,136,60\nD 28,68,s91,3072\nMvaa 232,104,44\nv_mul_f32 v95, v95, v104\nC 96,88,89\nMvaa 228,100,44\nC 97,90,91\n" \
"C 98,92,93\nMvaa 228,132,60\nv_add_u32 v14, s72, v14\nC 99,94,95\nMvaa 224,128,60\nv_add_u32 v28, s72, v28\nMvaa 224,96,44\ns_waitcnt lgkmcnt(6)\n" \
"Mvaa 128,64,0\ns_add_u32 s95, s50, 2048\nbuffer_store_dwordx4 v[96:99], v0, s[60:63], s95 offen sc1\nMvaa 128,160,16\ns_add_u32 m0, s88, 0\nv_mul_f32 v108, v108, v124\nMvaa 132,164,16\nD 13,64,0,0\n" \
"Mvaa 132,68,0\nv_mul_f32 v109, v109, v124\nv_mul_f32 v110, v110, v124\nMvaa 136,72,0\nv_mul_f32 v111, v111, v124\nv_mul_f32 v112, v112, v124\nMvaa 136,168,16\nv_mul_f32 v113, v113, v124\n" \
"v_mul_f32 v114, v114, v124\nMvaa 140,172,16\nv_mul_f32 v115, v115, v124\nC 116,108,109\nMvaa 140,76,0\ns_waitcnt lgkmcnt(0)\nD 13,64,s73,1024\nMvaa 144,80,0\n" \
"C 117,110,111\nC 118,112,113\nMvaa 144,176,16\nC 119,114,115\nMvaa 148,180,16\ns_add_u32 s95, s50, 4096\nbuffer_store_dwordx4 v[116:119], v0, s[60:63], s95 offen sc1\nMvaa 148,84,0\n" \
"Qvaa 88,232,204,72\nQvaa 92,236,204,76\nQvaa 108,236,208,72\nQvaa 112,240,208,76\nMvaa 152,88,0\nD 13,64,s74,2048\nMvaa 152,184,16\nMvaa 156,188,16\n" \
"Mvaa 156,92,0\nMvaa 188,92,4\ns_waitcnt vmcnt(18)\ns_barrier\nMvaa 188,188,20\nTa 96,20,32768\nTa 98,20,33792\nMvaa 184,184,20\n" \
"D 13,64,s75,3072\nMvaa 184,88,4\nTa 128,20,40960\nTa 130,20,41984\nMvaa 180,84,4\nv_max3_f32 v104, v88, v89, v90\nv_max3_f32 v105, v91, v92, v93\nMvaa 180,180,20\n" \
"Ra 32,16,32768\nRa 48,17,32768\nMvaa 176,176,20\ns_add_u32 m0, s88, 4096\nTa 100,21,32768\nMvaa 176,80,4\nD 13,64,s76,0\nMvaa 172,76,4\n" \
"Ta 102,21,33792\nTa 132,21,40960\nMvaa 172,172,20\nv_max3_f32 v104, v104, v94, v95\nv_max_f32 v104, v104, v105\nMvaa 168,168,20\nTa 134,21,41984\nTa 104,22,32768\n" \
"Mvaa 168,72,4\nTa 106,22,33792\nTa 136,22,40960\nMvaa 164,68,4\nD 13,64,s77,1024\nMvaa 164,164,20\nv_mov_b32 v105, v104\nv_max3_f32 v124, v108, v109, v110\n" \
"Mvaa 160,160,20\nTa 138,22,41984\nTa 108,23,32768\nMvaa 160,64,4\nv_max3_f32 v125, v111, v112, v113\nv_max3_f32 v124, v124, v114, v115\nMvaa 192,64,8\nTa 110,23,33792\n" \
"Ta 140,23,40960\nMvaa 192,160,24\nD 13,64,s78,2048\nMvaa 196,164,24\nTa 142,23,41984\nTa 112,24,32768\nMvaa 196,68,8\nv_max_f32 v124, v124, v125\n" \
"s_branch 99f\n.p2align 12\nzp34:\ns_setpc_b64 s[98:99]\n99:\nv_mov_b32 v125, v124\nMvaa 200,72,8\nTa 114,24,33792\n" \
"Ta 144,24,40960\nMvaa 200,168,24\nTa 146,24,41984\nTa 116,25,32768\nMvaa 204,172,24\nD 13,64,s79,3072\nMvaa 204,76,8\nv_permlane16_swap_b32 v104, v105\n" \
"v_permlane16_swap_b32 v124, v125\nMvaa 208,80,8\nTa 118,25,33792\nTa 148,25,40960\nMvaa 208,176,24\nv_add_u32 v13, 128, v13\nTa 150,25,41984\nMvaa 212,180,24\n" \
"s_add_u32 m0, s32, 65536\nTa 120,26,32768\nMvaa 212,84,8\nD 14,68,0,0\nMvaa 216,88,8\nTa 122,26,33792\nTa 152,26,40960\nMvaa 216,184,24\n" \
"v_max_f32 v104, v104, v105\nv_mov_b32 v105, v104\nMvaa 220,188,24\nTa 154,26,41984\nTa 124,27,32768\nMvaa 220,92,8\nv_max_f32 v124, v124, v125\nv_mov_b32 v125, v124\n" \
"Mvaa 252,92,12\nD 14,68,s89,1024\nMvaa 252,188,28\nTa 126,27,33792\nTa 156,27,40960\nMvaa 248,184,28\nTa 158,27,41984\nRa 36,16,34816\n" \
"Mvaa 248,88,12\nv_permlane32_swap_b32 v104, v105\nv_permlane32_swap_b32 v124, v125\nMvaa 244,84,12\nRa 52,17,34816\nRa 40,16,36864\nMvaa 244,180,28\nD 28,68,s90,2048\n" \
"Mvaa 240,176,28\nRa 56,17,36864\nRa 44,16,38912\nMvaa 240,80,12\nv_max_f32 v104, v104, v105\nv_mul_f32 v104, s93, v104\nMvaa 236,76,12\nRa 60,17,38912\n" \
"v_fma_f32 v88, v88, s93, -v104\nMvaa 236,172,28\nv_fma_f32 v89, v89, s93, -v104\nv_fma_f32 v90, v90, s93, -v104\nMvaa 232,168,28\nD 28,68,s91,3072\nMvaa 232,72,12\nv_fma_f32 v91, v91, s93, -v104\n" \
"v_fma_f32 v92, v92, s93, -v104\nMvaa 228,68,12\nv_fma_f32 v93, v93, s93, -v104\nv_fma_f32 v94, v94, s93, -v104\nMvaa 228,164,28\nv_add_u32 v14, s72, v14\nv_fma_f32 v95, v95, s93, -v104\nMvaa 224,160,28\n" \
"v_add_u32 v28, s72, v28\nMvaa 224,64,12\ns_waitcnt lgkmcnt(6)\nMvaa 128,96,32\nE 88,88\nMvaa 128,128,48\ns_add_u32 m0, s88, 32768\nMvaa 132,132,48\n" \
"D 13,64,0,0\nMvaa 132,100,32\nE 89,89\nMvaa 136,104,32\nE 90,90\nMvaa 136,136,48\nC 96,88,89\nMvaa 140,140,48\n" \
"E 91,91\nMvaa 140,108,32\ns_waitcnt lgkmcnt(0)\nD 13,64,s73,1024\nMvaa 144,112,32\nE 92,92\nMvaa 144,144,48\nC 97,90,91\n" \
"Mvaa 148,148,48\nE 93,93\nMvaa 148,116,32\nE 94,94\nMvaa 152,120,32\nD 13,64,s74,2048\nMvaa 152,152,48\nC 98,92,93\n" \
"Mvaa 156,156,48\nE 95,95\nMvaa 156,124,32\nv_max_f32 v124, v124, v125\nv_mul_f32 v124, s93, v124\nMvaa 188,124,36\ns_waitcnt vmcnt(17)\ns_barrier\n" \
"Mvaa 188,156,52\nTa 64,20,16384\nTa 66,20,17408\nMvaa 184,152,52\nD 13,64,s75,3072\nMvaa 184,120,36\nTa 160,20,24576\nTa 162,20,25600\n" \
"Mvaa 180,116,36\nv_fma_f32 v108, v108, s93, -v124\nv_fma_f32 v109, v109, s93, -v124\nMvaa 180,148,52\nRa 0,18,0\nRa 16,19,0\nMvaa 176,144,52\ns_add_u32 m0, s88, 36864\n" \
"Ta 68,21,16384\nMvaa 176,112,36\nD 13,64,s76,0\nMvaa 172,108,36\nTa 70,21,17408\nTa 164,21,24576\nMvaa 172,140,52\nv_fma_f32 v110, v110, s93, -v124\n" \
"v_fma_f32 v111, v111, s93, -v124\nMvaa 168,136,52\nTa 166,21,25600\nTa 72,22,16384\nMvaa 168,104,36\nTa 74,22,17408\nTa 168,22,24576\nMvaa 164,100,36\n" \
"D 13,64,s77,1024\nMvaa 164,132,52\nv_fma_f32 v112, v112, s93, -v124\nv_fma_f32 v113, v113, s93, -v124\nMvaa 160,128,52\nTa 170,22,25600\nTa 76,23,16384\nMvaa 160,96,36\n" \
"v_fma_f32 v114, v114, s93, -v124\nv_fma_f32 v115, v115, s93, -v124\nMvaa 192,96,40\nTa 78,23,17408\nTa 172,23,24576\nMvaa 192,128,56\nD 13,64,s78,2048\nMvaa 196,132,56\n" \
"Ta 174,23,25600\nTa 80,24,16384\nMvaa 196,100,40\nE 108,108\nMvaa 200,104,40\nTa 82,24,17408\nTa 176,24,24576\nMvaa 200,136,56\n" \
"Ta 178,24,25600\nTa 84,25,16384\nMvaa 204,140,56\nD 13,64,s79,3072\nMvaa 204,108,40\nE 109,109\nMvaa 208,112,40\nTa 86,25,17408\n" \
"Ta 180,25,24576\nMvaa 208,144,56\nv_add_u32 v13, 128, v13\nTa 182,25,25600\nMvaa 212,148,56\ns_add_u32 m0, s32, 98304\nTa 88,26,16384\nMvaa 212,116,40\n" \
"D 14,68,0,0\nMvaa 216,120,40\nTa 90,26,17408\nTa 184,26,24576\nMvaa 216,152,56\nE 110,110\nMvaa 220,156,56\nTa 186,26,25600\n" \
"Ta 92,27,16384\nMvaa 220,124,40\nC 116,108,109\nMvaa 252,124,44\nD 14,68,s89,1024\nMvaa 252,156,60\nTa 94,27,17408\nTa 188,27,24576\n" \
"Mvaa 248,152,60\nTa 190,27,25600\nRa 4,18,2048\nMvaa 248,120,44\nE 111,111\nMvaa 244,116,44\nRa 20,19,2048\nRa 8,18,4096\n" \
"Mvaa 244,148,60\nD 28,68,s90,2048\nMvaa 240,144,60\nRa 24,19,4096\nRa 12,18,6144\nMvaa 240,112,44\nE 112,112\nMvaa 236,108,44\n" \
"Ra 28,19,6144\nC 117,110,111\nMvaa 236,140,60\nE 113,113\nMvaa 232,136,60\nD 28,68,s91,3072\nMvaa 232,104,44\nE 114,114\n" \
"Mvaa 228,100,44\nC 118,112,113\nMvaa 228,132,60\nv_add_u32 v14, s72, v14\nMvaa 224,128,60\nv_add_u32 v28, s72, v28\nMvaa 224,96,44\ns_waitcnt lgkmcnt(6)\n" \
"Mvaa 128,64,0\nE 115,115\nMvaa 128,160,16\ns_add_u32 m0, s88, 131072\nC 99,94,95\nMvaa 132,164,16\nD 13,64,0,0\nMvaa 132,68,0\n" \
"C 119,114,115\nZvvv 100,84,96\nZvvv 88,38,96\nZvvv 92,56,96\nZvvv 120,84,116\nZvvv 108,40,116\nZvvv 112,58,116\nMvaa 136,72,0\n" \
"Mvaa 136,168,16\nMvaa 140,172,16\nv_rcp_f32 v104, v100\nMvaa 140,76,0\ns_waitcnt lgkmcnt(0)\nD 13,64,s73,1024\nMvaa 144,80,0\nv_rcp_f32 v124, v120\n" \
"Mvaa 144,176,16\nv_mul_f32 v88, v88, v104\nv_mul_f32 v89, v89, v104\nMvaa 148,180,16\nv_mul_f32 v90, v90, v104\nv_mul_f32 v91, v91, v104\nMvaa 148,84,0\nv_mul_f32 v92, v92, v104\n" \
"v_mul_f32 v93, v93, v104\nMvaa 152,88,0\nD 13,64,s74,2048\nMvaa 152,184,16\nv_mul_f32 v94, v94, v104\nv_mul_f32 v95, v95, v104\nMvaa 156,188,16\nC 96,88,89\n" \
"C 97,90,91\nMvaa 156,92,0\nC 98,92,93\nC 99,94,95\nMvaa 188,92,4\ns_waitcnt vmcnt(15)\ns_barrier\nMvaa 188,188,20\n" \
"Ta 96,20,0\nTa 98,20,1024\nMvaa 184,184,20\nD 13,64,s75,3072\nMvaa 184,88,4\nTa 128,20,8192\nTa 130,20,9216\nMvaa 180,84,4\n" \
"s_add_u32 s95, s50, 6144\nbuffer_store_dwordx4 v[96:99], v0, s[60:63], s95 offen sc1\nMvaa 180,180,20\nRa 32,16,0\nRa 48,17,0\nMvaa 176,176,20\ns_add_u32 m0, s88, 135168\nTa 100,21,0\n" \
"Mvaa 176,80,4\nD 13,64,s76,0\nMvaa 172,76,4\nTa 102,21,1024\nTa 132,21,8192\nMvaa 172,172,20\nv_mul_f32 v108, v108, v124\nv_mul_f32 v109, v109, v124\n" \
"Mvaa 168,168,20\nTa 134,21,9216\nTa 104,22,0\nMvaa 168,72,4\nTa 106,22,1024\nTa 136,22,8192\nMvaa 164,68,4\nD 13,64,s77,1024\n" \
"Mvaa 164,164,20\nv_mul_f32 v110, v110, v124\nv_mul_f32 v111, v111, v124\nMvaa 160,160,20\nTa 138,22,9216\nTa 108,23,0\nMvaa 160,64,4\nv_mul_f32 v112, v112, v124\n" \
"v_mul_f32 v113, v113, v124\nMvaa 192,64,8\nTa 110,23,1024\nTa 140,23,8192\nMvaa 192,160,24\nD 13,64,s78,2048\nMvaa 196,164,24\nTa 142,23,9216\n" \
"Ta 112,24,0\nMvaa 196,68,8\nv_mul_f32 v114, v114, v124\nv_mul_f32 v115, v115, v124\nMvaa 200,72,8\nTa 114,24,1024\nTa 144,24,8192\nMvaa 200,168,24\n" \
"Ta 146,24,9216\nTa 116,25,0\nMvaa 204,172,24\nD 13,64,s79,3072\nMvaa 204,76,8\nC 116,108,109\nC 117,110,111\nMvaa 208,80,8\n" \
"Ta 118,25,1024\nTa 148,25,8192\nMvaa 208,176,24\nv_add_u32 v13, 128, v13\nTa 150,25,9216\nMvaa 212,180,24\ns_add_u32 m0, s32, 81920\nTa 120,26,0\n" \
"Mvaa 212,84,8\nD 14,68,0,0\nMvaa 216,88,8\nTa 122,26,1024\nTa 152,26,8192\nMvaa 216,184,24\nC 118,112,113\nC 119,114,115\n" \
"Mvaa 220,188,24\nTa 154,26,9216\nTa 124,27,0\nMvaa 220,92,8\ns_add_u32 s95, s50, 8192\nbuffer_store_dwordx4 v[116:119], v0, s[60:63], s95 offen sc1\nMvaa 252,92,12\nD 14,68,s89,1024\n" \
"Qvaa 88,240,212,72\nQvaa 92,244,212,76\nQvaa 108,244,216,72\nQvaa 112,248,216,76\nMvaa 252,188,28\nTa 126,27,1024\nTa 156,27,8192\nMvaa 248,184,28\n" \
"Ta 158,27,9216\nRa 36,16,2048\nMvaa 248,88,12\nv_max3_f32 v104, v88, v89, v90\nv_max3_f32 v105, v91, v92, v93\nMvaa 244,84,12\nRa 52,17,2048\nRa 40,16,4096\n" \
"Mvaa 244,180,28\nD 28,68,s90,2048\nMvaa 240,176,28\nRa 56,17,4096\nRa 44,16,6144\nMvaa 240,80,12\nv_max3_f32 v104, v104, v94, v95\nv_max_f32 v104, v104, v105\n" \
"Mvaa 236,76,12\nRa 60,17,6144\nv_mov_b32 v105, v104\nMvaa 236,172,28\nv_max3_f32 v124, v108, v109, v110\nv_max3_f32 v125, v111, v112, v113\nMvaa 232,168,28\nD 28,68,s91,3072\n" \
"Mvaa 232,72,12\nv_max3_f32 v124, v124, v114, v115\nv_max_f32 v124, v124, v125\nMvaa 228,68,12\nv_mov_b32 v125, v124\nv_permlane16_swap_b32 v104, v105\nMvaa 228,164,28\nv_add_u32 v14, s72, v14\n" \
"v_permlane16_swap_b32 v124, v125\nMvaa 224,160,28\nv_add_u32 v28, s72, v28\nv_max_f32 v104, v104, v105\nMvaa 224,64,12\ns_waitcnt lgkmcnt(6)\nv_mov_b32 v105, v104\nMvaa 128,96,32\n" \
"v_max_f32 v124, v124, v125\nv_mov_b32 v125, v124\nMvaa 128,128,48\ns_add_u32 m0, s88, 0\nv_permlane32_swap_b32 v104, v105\nMvaa 132,132,48\nD 13,64,0,0\nMvaa 132,100,32\n" \
"v_permlane32_swap_b32 v124, v125\nv_max_f32 v104, v104, v105\nMvaa 136,104,32\nbuffer_load_dwordx2 v[10:11], off, s[60:63], s49 sc1\nMvaa 136,136,48\nv_mul_f32 v104, s93, v104\nv_fma_f32 v88, v88, s93, -v104\nMvaa 140,140,48\n" \
"v_fma_f32 v89, v89, s93, -v104\nv_fma_f32 v90, v90, s93, -v104\nMvaa 140,108,32\ns_waitcnt lgkmcnt(0)\nD 13,64,s73,1024\nMvaa 144,112,32\nv_fma_f32 v91, v91, s93, -v104\nv_fma_f32 v92, v92, s93, -v104\n" \
"Mvaa 144,144,48\nv_fma_f32 v93, v93, s93, -v104\nv_fma_f32 v94, v94, s93, -v104\nMvaa 148,148,48\nv_fma_f32 v95, v95, s93, -v104\nMvaa 148,116,32\nE 88,88\nMvaa 152,120,32\n" \
"D 13,64,s74,2048\nMvaa 152,152,48\nE 89,89\nMvaa 156,156,48\nE 90,90\nMvaa 156,124,32\nC 96,88,89\nMvaa 188,124,36\n" \
"s_waitcnt vmcnt(18)\ns_barrier\nMvaa 188,156,52\nTa 64,20,32768\nTa 66,20,33792\nMvaa 184,152,52\nD 13,64,s75,3072\nMvaa 184,120,36\n" \
"Ta 160,20,40960\nTa 162,20,41984\nMvaa 180,116,36\nE 91,91\nMvaa 180,148,52\nRa 0,16,32768\nRa 16,17,32768\nMvaa 176,144,52\n" \
"s_add_u32 m0, s88, 4096\nTa 68,21,32768\nMvaa 176,112,36\nD 13,64,s76,0\nMvaa 172,108,36\nTa 70,21,33792\nTa 164,21,40960\nMvaa 172,140,52\n" \
"E 92,92\nMvaa 168,136,52\nTa 166,21,41984\nTa 72,22,32768\nMvaa 168,104,36\nTa 74,22,33792\nTa 168,22,40960\nMvaa 164,100,36\n" \
"D 13,64,s77,1024\nMvaa 164,132,52\nC 97,90,91\nMvaa 160,128,52\nTa 170,22,41984\nTa 76,23,32768\nMvaa 160,96,36\nE 93,93\n" \
"Mvaa 192,96,40\nTa 78,23,33792\nTa 172,23,40960\nMvaa 192,128,56\nD 13,64,s78,2048\nMvaa 196,132,56\nTa 174,23,41984\nTa 80,24,32768\n" \
"Mvaa 196,100,40\nE 94,94\nMvaa 200,104,40\nTa 82,24,33792\nTa 176,24,40960\nMvaa 200,136,56\nTa 178,24,41984\nTa 84,25,32768\n" \
"Mvaa 204,140,56\nD 13,64,s79,3072\nMvaa 204,108,40\nC 98,92,93\nMvaa 208,112,40\nTa 86,25,33792\nTa 180,25,40960\nMvaa 208,144,56\n" \
"v_add_u32 v13, 128, v13\nTa 182,25,41984\nMvaa 212,148,56\ns_add_u32 m0, s32, 65536\nTa 88,26,32768\nMvaa 212,116,40\nD 14,68,0,0\nMvaa 216,120,40\n" \
"Ta 90,26,33792\nTa 184,26,40960\nMvaa 216,152,56\nE 95,95\nMvaa 220,156,56\nTa 186,26,41984\nTa 92,27,32768\nMvaa 220,124,40\n" \
"v_max_f32 v124, v124, v125\nv_mul_f32 v124, s93, v124\nMvaa 252,124,44\nD 14,68,s89,1024\nMvaa 252,156,60\nTa 94,27,33792\nTa 188,27,40960\nMvaa 248,152,60\n" \
"Ta 190,27,41984\nRa 4,16,34816\nMvaa 248,120,44\nv_fma_f32 v108, v108, s93, -v124\nv_fma_f32 v109, v109, s93, -v124\nMvaa 244,116,44\nRa 20,17,34816\nRa 8,16,36864\n" \
"Mvaa 244,148,60\ns_branch 99f\n.p2align 12\nzp35:\ns_setpc_b64 s[98:99]\n99:\nD 28,68,s90,2048\nMvaa 240,144,60\n" \
"Ra 24,17,36864\nRa 12,16,38912\nMvaa 240,112,44\nv_fma_f32 v110, v110, s93, -v124\nv_fma_f32 v111, v111, s93, -v124\nMvaa 236,108,44\nRa 28,17,38912\nv_fma_f32 v112, v112, s93, -v124\n" \
"Mvaa 236,140,60\nv_fma_f32 v113, v113, s93, -v124\nv_fma_f32 v114, v114, s93, -v124\nMvaa 232,136,60\nD 28,68,s91,3072\nMvaa 232,104,44\nv_fma_f32 v115, v115, s93, -v124\nMvaa 228,100,44\n" \
"E 108,108\nMvaa 228,132,60\nv_add_u32 v14, s72, v14\nMvaa 224,128,60\nv_add_u32 v28, s72, v28\nMvaa 224,96,44\ns_waitcnt lgkmcnt(6)\nMvaa 128,64,0\n" \
"E 109,109\nMvaa 128,160,16\ns_add_u32 m0, s88, 32768\nMvaa 132,164,16\nD 13,64,0,0\nMvaa 132,68,0\nE 110,110\nMvaa 136,72,0\n" \
"s_waitcnt vmcnt(12)\nv_cmp_ne_u64 vcc, v[10:11], v[2:3]\ns_cbranch_vccnz zs103\nzr103:\nMvaa 136,168,16\nbuffer_load_dwordx4 v[68:71], v0, s[60:63], s47 offen sc1\nMvaa 140,172,16\nbuffer_load_dwordx2 v[32:33], v0, s[60:63], s47 offen offset:1024 sc1\n" \
"Mvaa 140,76,0\ns_waitcnt lgkmcnt(0)\nD 13,64,s73,1024\nbuffer_load_dwordx2 v[50:51], v0, s[60:63], s47 offen offset:1032 sc1\nMvaa 144,80,0\nC 116,108,109\nMvaa 144,176,16\nE 111,111\n" \
"Mvaa 148,180,16\nE 112,112\nMvaa 148,84,0\nC 117,110,111\nMvaa 152,88,0\nD 13,64,s74,2048\nMvaa 152,184,16\nE 113,113\n" \
"Mvaa 156,188,16\nE 114,114\nMvaa 156,92,0\nC 118,112,113\nMvaa 188,92,4\ns_waitcnt vmcnt(19)\ns_barrier\nMvaa 188,188,20\n" \
"Ta 96,20,16384\nTa 98,20,17408\nMvaa 184,184,20\nD 13,64,s75,3072\nMvaa 184,88,4\nTa 128,20,24576\nTa 130,20,25600\nMvaa 180,84,4\n" \
"E 115,115\nMvaa 180,180,20\nRa 32,18,0\nRa 48,19,0\nMvaa 176,176,20\ns_add_u32 m0, s88, 36864\nTa 100,21,16384\nMvaa 176,80,4\n" \
"D 13,64,s76,0\nMvaa 172,76,4\nTa 102,21,17408\nTa 132,21,24576\nMvaa 172,172,20\nC 99,94,95\nC 119,114,115\nMvaa 168,168,20\n" \
"Ta 134,21,25600\nTa 104,22,16384\nZvvv 100,84,96\nZvvv 88,42,96\nZvvv 92,60,96\nZvvv 120,84,116\nZvvv 108,44,116\nZvvv 112,62,116\n" \
"Mvaa 168,72,4\nTa 106,22,17408\nTa 136,22,24576\nMvaa 164,68,4\nD 13,64,s77,1024\nMvaa 164,164,20\nv_rcp_f32 v104, v100\nMvaa 160,160,20\n" \
"Ta 138,22,25600\nTa 108,23,16384\nMvaa 160,64,4\nv_rcp_f32 v124, v120\nMvaa 192,64,8\nTa 110,23,17408\nTa 140,23,24576\nMvaa 192,160,24\n" \
"D 13,64,s78,2048\nMvaa 196,164,24\nTa 142,23,25600\nTa 112,24,16384\nMvaa 196,68,8\nv_mul_f32 v88, v88, v104\nv_mul_f32 v89, v89, v104\nMvaa 200,72,8\n" \
"Ta 114,24,17408\nTa 144,24,24576\nMvaa 200,168,24\nTa 146,24,25600\nTa 116,25,16384\nMvaa 204,172,24\nD 13,64,s79,3072\nMvaa 204,76,8\n" \
"v_mul_f32 v90, v90, v104\nv_mul_f32 v91, v91, v104\nMvaa 208,80,8\nTa 118,25,17408\nTa 148,25,24576\nMvaa 208,176,24\nv_add_u32 v13, 128, v13\nTa 150,25,25600\n" \
"Mvaa 212,180,24\ns_add_u32 m0, s32, 98304\nTa 120,26,16384\nMvaa 212,84,8\nD 14,68,0,0\nMvaa 216,88,8\nTa 122,26,17408\nTa 152,26,24576\n" \
"Mvaa 216,184,24\nv_mul_f32 v92, v92, v104\nv_mul_f32 v93, v93, v104\nMvaa 220,188,24\nTa 154,26,25600\nTa 124,27,16384\nMvaa 220,92,8\nv_mul_f32 v94, v94, v104\n" \
"v_mul_f32 v95, v95, v104\nMvaa 252,92,12\nD 14,68,s89,1024\nMvaa 252,188,28\nTa 126,27,17408\nTa 156,27,24576\nMvaa 248,184,28\nTa 158,27,25600\n" \
"Ra 36,18,2048\nMvaa 248,88,12\nC 96,88,89\nC 97,90,91\nMvaa 244,84,12\nRa 52,19,2048\nRa 40,18,4096\nMvaa 244,180,28\n" \
"D 28,68,s90,2048\nMvaa 240,176,28\nRa 56,19,4096\nRa 44,18,6144\nMvaa 240,80,12\nC 98,92,93\nC 99,94,95\nMvaa 236,76,12\n" \
"Ra 60,19,6144\nMvaa 236,172,28\ns_add_u32 s95, s50, 10240\nbuffer_store_dwordx4 v[96:99], v0, s[60:63], s95 offen sc1\nMvaa 232,168,28\nD 28,68,s91,3072\nMvaa 232,72,12\nv_mul_f32 v108, v108, v124\n" \
"v_mul_f32 v109, v109, v124\nMvaa 228,68,12\nv_mul_f32 v110, v110, v124\nv_mul_f32 v111, v111, v124\nMvaa 228,164,28\nv_add_u32 v14, s72, v14\nv_mul_f32 v112, v112, v124\nMvaa 224,160,28\n" \
"v_add_u32 v28, s72, v28\nv_mul_f32 v113, v113, v124\nMvaa 224,64,12\ns_waitcnt lgkmcnt(6)\nv_mul_f32 v114, v114, v124\nMvaa 128,96,32\nv_mul_f32 v115, v115, v124\nC 116,108,109\n" \
"Mvaa 128,128,48\ns_add_u32 m0, s88, 131072\nC 117,110,111\nMvaa 132,132,48\nD 13,64,0,0\nMvaa 132,100,32\nC 118,112,113\nC 119,114,115\n" \
"Mvaa 136,104,32\ns_add_u32 s95, s50, 12288\nbuffer_store_dwordx4 v[116:119], v0, s[60:63], s95 offen sc1\nMvaa 136,136,48\nMvaa 140,140,48\nMvaa 140,108,32\ns_waitcnt lgkmcnt(0)\nD 13,64,s73,1024\n" \
"Mvaa 144,112,32\nMvaa 144,144,48\nMvaa 148,148,48\nMvaa 148,116,32\nMvaa 152,120,32\nD 13,64,s74,2048\nMvaa 152,152,48\nMvaa 156,156,48\n" \
"Mvaa 156,124,32\nMvaa 188,124,36\ns_waitcnt vmcnt(20)\ns_barrier\nMvaa 188,156,52\nTa 64,20,0\nTa 66,20,1024\nMvaa 184,152,52\n" \
"D 13,64,s75,3072\nMvaa 184,120,36\nTa 160,20,8192\nTa 162,20,9216\nMvaa 180,116,36\ns_waitcnt vmcnt(16)\nQvaa 88,248,220,72\nQvaa 92,252,220,76\n" \
"Qvva 108,68,192,80\nQvaa 112,224,192,76\nMvaa 180,148,52\nRa 0,16,0\nRa 16,17,0\nMvaa 176,144,52\ns_add_u32 m0, s88, 135168\nTa 68,21,0\n" \
"Mvaa 176,112,36\nD 13,64,s76,0\nMvaa 172,108,36\nTa 70,21,1024\nTa 164,21,8192\nMvaa 172,140,52\nv_max3_f32 v104, v88, v89, v90\nv_max3_f32 v105, v91, v92, v93\n" \
"Mvaa 168,136,52\nTa 166,21,9216\nTa 72,22,0\nMvaa 168,104,36\nTa 74,22,1024\nTa 168,22,8192\nMvaa 164,100,36\nD 13,64,s77,1024\n" \
"Mvaa 164,132,52\nv_max3_f32 v104, v104, v94, v95\nv_max_f32 v104, v104, v105\nMvaa 160,128,52\nTa 170,22,9216\nTa 76,23,0\nMvaa 160,96,36\nv_mov_b32 v105, v104\n" \
"v_max3_f32 v124, v108, v109, v110\nMvaa 192,96,40\nTa 78,23,1024\nTa 172,23,8192\nMvaa 192,128,56\nD 13,64,s78,2048\nMvaa 196,132,56\nTa 174,23,9216\n" \
"Ta 80,24,0\nMvaa 196,100,40\nv_max3_f32 v125, v111, v112, v113\nv_max3_f32 v124, v124, v114, v115\nMvaa 200,104,40\nTa 82,24,1024\nTa 176,24,8192\nMvaa 200,136,56\n" \
"Ta 178,24,9216\nTa 84,25,0\nMvaa 204,140,56\nD 13,64,s79,3072\nMvaa 204,108,40\nv_max_f32 v124, v124, v125\nv_mov_b32 v125, v124\nMvaa 208,112,40\n" \
"Ta 86,25,1024\nTa 180,25,8192\nMvaa 208,144,56\nv_add_u32 v13, 128, v13\nTa 182,25,9216\nMvaa 212,148,56\ns_add_u32 m0, s32, 81920\nTa 88,26,0\n" \
"Mvaa 212,116,40\nD 14,68,0,0\nMvaa 216,120,40\nTa 90,26,1024\nTa 184,26,8192\nMvaa 216,152,56\nv_permlane16_swap_b32 v104, v105\nv_permlane16_swap_b32 v124, v125\n" \
"Mvaa 220,156,56\nTa 186,26,9216\nTa 92,27,0\nMvaa 220,124,40\nv_max_f32 v104, v104, v105\nv_mov_b32 v105, v104\nMvaa 252,124,44\nD 14,68,s89,1024\n" \
"Mvaa 252,156,60\nTa 94,27,1024\nTa 188,27,8192\nMvaa 248,152,60\nTa 190,27,9216\nRa 4,16,2048\nMvaa 248,120,44\nv_max_f32 v124, v124, v125\n" \
"v_mov_b32 v125, v124\nMvaa 244,116,44\nRa 20,17,2048\nRa 8,16,4096\nMvaa 244,148,60\nD 28,68,s90,2048\nMvaa 240,144,60\nRa 24,17,4096\n" \
"Ra 12,16,6144\nMvaa 240,112,44\nv_permlane32_swap_b32 v104, v105\nv_permlane32_swap_b32 v124, v125\nMvaa 236,108,44\nRa 28,17,6144\nv_max_f32 v104, v104, v105\nMvaa 236,140,60\n" \
"v_mul_f32 v104, s93, v104\nv_fma_f32 v88, v88, s93, -v104\nMvaa 232,136,60\nD 28,68,s91,3072\nMvaa 232,104,44\nv_fma_f32 v89, v89, s93, -v104\nv_fma_f32 v90, v90, s93, -v104\nMvaa 228,100,44\n" \
"v_fma_f32 v91, v91, s93, -v104\nv_fma_f32 v92, v92, s93, -v104\nMvaa 228,132,60\nv_add_u32 v14, s72, v14\nv_fma_f32 v93, v93, s93, -v104\nMvaa 224,128,60\nv_add_u32 v28, s72, v28\nv_fma_f32 v94, v94, s93, -v104\n" \
"Mvaa 224,96,44\ns_waitcnt lgkmcnt(6)\nv_fma_f32 v95, v95, s93, -v104\nMvaa 128,64,0\nE 88,88\nMvaa 128,160,16\ns_add_u32 m0, s88, 0\nMvaa 132,164,16\n" \
"D 13,64,0,0\nMvaa 132,68,0\nE 89,89\nMvaa 136,72,0\nE 90,90\nMvaa 136,168,16\nC 96,88,89\nMvaa 140,172,16\n" \
"E 91,91\nMvaa 140,76,0\ns_waitcnt lgkmcnt(0)\nD 13,64,s73,1024\nMvaa 144,80,0\nE 92,92\nMvaa 144,176,16\nC 97,90,91\n" \
"Mvaa 148,180,16\nE 93,93\nMvaa 148,84,0\nE 94,94\nMvaa 152,88,0\nD 13,64,s74,2048\nMvaa 152,184,16\nC 98,92,93\n" \
"Mvaa 156,188,16\nE 95,95\nMvaa 156,92,0\nv_max_f32 v124, v124, v125\nv_mul_f32 v124, s93, v124\nMvaa 188,92,4\ns_waitcnt vmcnt(16)\ns_barrier\n" \
"Mvaa 188,188,20\nTa 96,20,32768\nTa 98,20,33792\nMvaa 184,184,20\nD 13,64,s75,3072\nMvaa 184,88,4\nTa 128,20,40960\nTa 130,20,41984\n" \
"Mvaa 180,84,4\nv_fma_f32 v108, v108, s93, -v124\nv_fma_f32 v109, v109, s93, -v124\nMvaa 180,180,20\nRa 32,16,32768\nRa 48,17,32768\nMvaa 176,176,20\ns_add_u32 m0, s88, 4096\n" \
"Ta 100,21,32768\nMvaa 176,80,4\nD 13,64,s76,0\nMvaa 172,76,4\nTa 102,21,33792\nTa 132,21,40960\nMvaa 172,172,20\nv_fma_f32 v110, v110, s93, -v124\n" \
"v_fma_f32 v111, v111, s93, -v124\nMvaa 168,168,20\nTa 134,21,41984\nTa 104,22,32768\nMvaa 168,72,4\nTa 106,22,33792\nTa 136,22,40960\nMvaa 164,68,4\n" \
"D 13,64,s77,1024\nMvaa 164,164,20\nv_fma_f32 v112, v112, s93, -v124\nv_fma_f32 v113, v113, s93, -v124\nMvaa 160,160,20\nTa 138,22,41984\nTa 108,23,32768\nMvaa 160,64,4\n" \
"v_fma_f32 v114, v114, s93, -v124\nv_fma_f32 v115, v115, s93, -v124\nMvaa 192,64,8\nTa 110,23,33792\nTa 140,23,40960\nMvaa 192,160,24\nD 13,64,s78,2048\nMvaa 196,164,24\n" \
"Ta 142,23,41984\nTa 112,24,32768\nMvaa 196,68,8\nE 108,108\nMvaa 200,72,8\nTa 114,24,33792\nTa 144,24,40960\nMvaa 200,168,24\n" \
"Ta 146,24,41984\nTa 116,25,32768\nMvaa 204,172,24\nD 13,64,s79,3072\nMvaa 204,76,8\nE 109,109\nMvaa 208,80,8\nTa 118,25,33792\n" \
"Ta 148,25,40960\nMvaa 208,176,24\nv_add_u32 v13, 128, v13\nTa 150,25,41984\nMvaa 212,180,24\ns_add_u32 m0, s32, 65536\nTa 120,26,32768\nMvaa 212,84,8\n" \
"D 14,68,0,0\nMvaa 216,88,8\nTa 122,26,33792\nTa 152,26,40960\nMvaa 216,184,24\nE 110,110\nMvaa 220,188,24\nTa 154,26,41984\n" \
"Ta 124,27,32768\nMvaa 220,92,8\nC 116,108,109\nMvaa 252,92,12\nD 14,68,s89,1024\nMvaa 252,188,28\nTa 126,27,33792\nTa 156,27,40960\n" \
"Mvaa 248,184,28\nTa 158,27,41984\nRa 36,16,34816\nMvaa 248,88,12\nE 111,111\nMvaa 244,84,12\nRa 52,17,34816\nRa 40,16,36864\n" \
"Mvaa 244,180,28\nD 28,68,s90,2048\nMvaa 240,176,28\nRa 56,17,36864\nRa 44,16,38912\nMvaa 240,80,12\nE 112,112\nMvaa 236,76,12\n" \
"Ra 60,17,38912\nC 117,110,111\nMvaa 236,172,28\nE 113,113\nMvaa 232,168,28\nD 28,68,s91,3072\nMvaa 232,72,12\nE 114,114\n" \
"Mvaa 228,68,12\nC 118,112,113\nMvaa 228,164,28\nv_add_u32 v14, s72, v14\nMvaa 224,160,28\nv_add_u32 v28, s72, v28\nMvaa 224,64,12\ns_waitcnt lgkmcnt(6)\n" \
"Mvaa 128,96,32\nE 115,115\nMvaa 128,128,48\ns_add_u32 m0, s88, 32768\nC 99,94,95\nMvaa 132,132,48\nD 13,64,0,0\nMvaa 132,100,32\n" \
"C 119,114,115\nZvvv 100,84,96\nZvvv 88,46,96\nZvvv 92,64,96\nZvvv 120,84,116\nZvvv 108,32,116\nZvvv 112,50,116\nMvaa 136,104,32\n" \
"Mvaa 136,136,48\nMvaa 140,140,48\nv_rcp_f32 v104, v100\nMvaa 140,108,32\ns_waitcnt lgkmcnt(0)\nD 13,64,s73,1024\nMvaa 144,112,32\nv_rcp_f32 v124, v120\n" \
"Mvaa 144,144,48\nv_mul_f32 v88, v88, v104\nv_mul_f32 v89, v89, v104\nMvaa 148,148,48\nv_mul_f32 v90, v90, v104\nv_mul_f32 v91, v91, v104\nMvaa 148,116,32\nv_mul_f32 v92, v92, v104\n" \
"v_mul_f32 v93, v93, v104\nMvaa 152,120,32\nD 13,64,s74,2048\nMvaa 152,152,48\nv_mul_f32 v94, v94, v104\ns_branch 99f\n.p2align 12\nzp36:\n" \
"s_setpc_b64 s[98:99]\n99:\nv_mul_f32 v95, v95, v104\nMvaa 156,156,48\nC 96,88,89\nC 97,90,91\nMvaa 156,124,32\nC 98,92,93\n" \
"C 99,94,95\nMvaa 188,124,36\ns_waitcnt vmcnt(15)\ns_barrier\nMvaa 188,156,52\nTa 64,20,16384\nTa 66,20,17408\nMvaa 184,152,52\n" \
"D 13,64,s75,3072\nMvaa 184,120,36\nTa 160,20,24576\nTa 162,20,25600\nMvaa 180,116,36\ns_add_u32 s95, s50, 14336\nbuffer_store_dwordx4 v[96:99], v0, s[60:63], s95 offen sc1\nMvaa 180,148,52\n" \
"Ra 0,18,0\nRa 16,19,0\nMvaa 176,144,52\ns_add_u32 m0, s88, 36864\nTa 68,21,16384\nMvaa 176,112,36\nD 13,64,s76,0\nMvaa 172,108,36\n" \
"Ta 70,21,17408\nTa 164,21,24576\nMvaa 172,140,52\nv_mul_f32 v108, v108, v124\nv_mul_f32 v109, v109, v124\nMvaa 168,136,52\nTa 166,21,25600\nTa 72,22,16384\n" \
"Mvaa 168,104,36\nTa 74,22,17408\nTa 168,22,24576\nMvaa 164,100,36\nD 13,64,s77,1024\nMvaa 164,132,52\nv_mul_f32 v110, v110, v124\nv_mul_f32 v111, v111, v124\n" \
"Mvaa 160,128,52\nTa 170,22,25600\nTa 76,23,16384\nMvaa 160,96,36\nv_mul_f32 v112, v112, v124\nv_mul_f32 v113, v113, v124\nMvaa 192,96,40\nTa 78,23,17408\n" \
"Ta 172,23,24576\nMvaa 192,128,56\nD 13,64,s78,2048\nMvaa 196,132,56\nTa 174,23,25600\nTa 80,24,16384\nMvaa 196,100,40\nv_mul_f32 v114, v114, v124\n" \
"v_mul_f32 v115, v115, v124\nMvaa 200,104,40\nTa 82,24,17408\nTa 176,24,24576\nMvaa 200,136,56\nTa 178,24,25600\nTa 84,25,16384\nMvaa 204,140,56\n" \
"D 13,64,s79,3072\nMvaa 204,108,40\nC 116,108,109\nC 117,110,111\nMvaa 208,112,40\nTa 86,25,17408\nTa 180,25,24576\nMvaa 208,144,56\n" \
"v_add_u32 v13, 128, v13\nTa 182,25,25600\nMvaa 212,148,56\ns_add_u32 m0, s32, 98304\nTa 88,26,16384\nMvaa 212,116,40\nD 14,68,0,0\nMvaa 216,120,40\n" \
"Ta 90,26,17408\nTa 184,26,24576\nMvaa 216,152,56\nC 118,112,113\nC 119,114,115\nMvaa 220,156,56\nTa 186,26,25600\nTa 92,27,16384\n" \
"Mvaa 220,124,40\ns_add_u32 s95, s50, 0\nbuffer_store_dwordx4 v[116:119], v0, s[60:63], s95 offen sc1\nMvaa 252,124,44\nD 14,68,s89,1024\nMvaa 252,156,60\nTa 94,27,17408\nTa 188,27,24576\n" \
"Mvaa 248,152,60\nTa 190,27,25600\nRa 4,18,2048\nMvaa 248,120,44\nMvaa 244,116,44\nRa 20,19,2048\nRa 8,18,4096\nMvaa 244,148,60\n" \
"D 28,68,s90,2048\nMvaa 240,144,60\nRa 24,19,4096\nRa 12,18,6144\nMvaa 240,112,44\nMvaa 236,108,44\nRa 28,19,6144\nMvaa 236,140,60\n" \
"Mvaa 232,136,60\nD 28,68,s91,3072\nMvaa 232,104,44\nMvaa 228,100,44\nMvaa 228,132,60\nv_add_u32 v14, s72, v14\nMvaa 224,128,60\nv_add_u32 v28, s72, v28\n" \
"Mvaa 224,96,44\ns_waitcnt lgkmcnt(6)\nMvaa 128,64,0\nMvaa 128,160,16\ns_add_u32 m0, s88, 131072\nMvaa 132,164,16\nD 13,64,0,0\nMvaa 132,68,0\n" \
"Mvaa 136,72,0\nMvaa 136,168,16\nMvaa 140,172,16\nMvaa 140,76,0\ns_waitcnt lgkmcnt(0)\nD 13,64,s73,1024\nMvaa 144,80,0\nMvaa 144,176,16\n" \
"Mvaa 148,180,16\nMvaa 148,84,0\nMvaa 152,88,0\nD 13,64,s74,2048\nMvaa 152,184,16\nMvaa 156,188,16\nMvaa 156,92,0\nMvaa 188,92,4\n" \
"s_waitcnt vmcnt(17)\ns_barrier\nMvaa 188,188,20\nTa 96,20,0\nTa 98,20,1024\nMvaa 184,184,20\nD 13,64,s75,3072\nMvaa 184,88,4\n" \
"Ta 128,20,8192\nTa 130,20,9216\nMvaa 180,84,4\nMvaa 180,180,20\nRa 32,16,0\nRa 48,17,0\nMvaa 176,176,20\ns_add_u32 m0, s88, 135168\n" \
"Ta 100,21,0\nMvaa 176,80,4\nD 13,64,s76,0\nMvaa 172,76,4\nTa 102,21,1024\nTa 132,21,8192\nMvaa 172,172,20\nMvaa 168,168,20\n" \
"Ta 134,21,9216\nTa 104,22,0\nMvaa 168,72,4\nTa 106,22,1024\nTa 136,22,8192\nMvaa 164,68,4\nD 13,64,s77,1024\nMvaa 164,164,20\n" \
"Mvaa 160,160,20\nTa 138,22,9216\nTa 108,23,0\nMvaa 160,64,4\nMvaa 192,64,8\nTa 110,23,1024\nTa 140,23,8192\nMvaa 192,160,24\n" \
"D 13,64,s78,2048\nMvaa 196,164,24\nTa 142,23,9216\nTa 112,24,0\nMvaa 196,68,8\nMvaa 200,72,8\nTa 114,24,1024\nTa 144,24,8192\n" \
"Mvaa 200,168,24\nTa 146,24,9216\nTa 116,25,0\nMvaa 204,172,24\nD 13,64,s79,3072\nMvaa 204,76,8\nMvaa 208,80,8\nTa 118,25,1024\n" \
"Ta 148,25,8192\nMvaa 208,176,24\nv_add_u32 v13, 128, v13\nTa 150,25,9216\nMvaa 212,180,24\ns_add_u32 m0, s32, 81920\nTa 120,26,0\nMvaa 212,84,8\n" \
"D 14,68,0,0\nMvaa 216,88,8\nTa 122,26,1024\nTa 152,26,8192\nMvaa 216,184,24\nMvaa 220,188,24\nTa 154,26,9216\nTa 124,27,0\n" \
"Mvaa 220,92,8\nMvaa 252,92,12\nD 14,68,s89,1024\nMvaa 252,188,28\nTa 126,27,1024\nTa 156,27,8192\nMvaa 248,184,28\nTa 158,27,9216\n" \
"Ra 36,16,2048\nMvaa 248,88,12\nMvaa 244,84,12\nRa 52,17,2048\nRa 40,16,4096\nMvaa 244,180,28\nD 28,68,s90,2048\nMvaa 240,176,28\n" \
"Ra 56,17,4096\nRa 44,16,6144\nMvaa 240,80,12\nMvaa 236,76,12\nRa 60,17,6144\nMvaa 236,172,28\nMvaa 232,168,28\nD 28,68,s91,3072\n" \
"Mvaa 232,72,12\nMvaa 228,68,12\nMvaa 228,164,28\nv_add_u32 v14, s72, v14\nMvaa 224,160,28\nv_add_u32 v28, s72, v28\nMvaa 224,64,12\ns_waitcnt lgkmcnt(6)\n" \
"Mvaa 128,96,32\nMvaa 128,128,48\nbuffer_load_dwordx2 v[10:11], v1, s[60:63], 0 offen offset:0 sc1\nMvaa 132,132,48\nv_subrev_u32 v9, s32, v0\nv_add_u32 v8, 0x1004200, v0\nMvaa 132,100,32\ns_add_u32 s77, s38, s32\n" \
"v_mov_b32 v12, 0xc2200000\nMvaa 136,104,32\nv_mov_b32 v13, 0xc2200000\nv_mov_b32 v14, 0xc2200000\nMvaa 136,136,48\nv_mov_b32 v15, 0xc2200000\nv_bfe_u32 v29, v0, 4, 4\nMvaa 140,140,48\n" \
"v_lshrrev_b32 v28, 8, v0\ns_lshr_b32 s96, s32, 8\nMvaa 140,108,32\ns_waitcnt lgkmcnt(0)\nv_lshlrev_b32 v6, 2, v28\nMvaa 144,112,32\nv_add_u32 v30, s96, v29\nv_sub_u32 v30, v30, v6\n" \
"Mvaa 144,144,48\nv_mov_b32 v6, 0xff800000\nv_cmp_gt_i32 s[90:91], 0, v30\nMvaa 148,148,48\nv_cmp_gt_i32 s[92:93], 1, v30\nv_cmp_gt_i32 s[74:75], 2, v30\nMvaa 148,116,32\nv_cmp_gt_i32 s[78:79], 3, v30\n" \
"v_cndmask_b32 v112, v12, v6, s[90:91]\nMvaa 152,120,32\nv_cndmask_b32 v113, v12, v6, s[92:93]\nv_cndmask_b32 v114, v12, v6, s[74:75]\nMvaa 152,152,48\nv_cndmask_b32 v115, v12, v6, s[78:79]\nv_cmp_gt_i32 s[90:91], 16, v30\nMvaa 156,156,48\n" \
"v_cmp_gt_i32 s[92:93], 17, v30\nv_cmp_gt_i32 s[74:75], 18, v30\nMvaa 156,124,32\nv_cmp_gt_i32 s[78:79], 19, v30\nv_cndmask_b32 v116, v12, v6, s[90:91]\nMvaa 188,124,36\ns_waitcnt vmcnt(13)\ns_barrier\n" \
"Mvaa 188,156,52\nTa 64,20,32768\nv_cndmask_b32 v117, v12, v6, s[92:93]\nMvaa 184,152,52\nTa 66,20,33792\nv_cndmask_b32 v118, v12, v6, s[74:75]\nMvaa 184,120,36\nTa 160,20,40960\n" \
"v_cndmask_b32 v119, v12, v6, s[78:79]\nMvaa 180,116,36\nTa 162,20,41984\nv_cmp_gt_i32 s[90:91], 32, v30\nMvaa 180,148,52\nRa 0,16,32768\nv_cmp_gt_i32 s[92:93], 33, v30\nMvaa 176,144,52\n" \
"Ra 16,17,32768\nv_cmp_gt_i32 s[74:75], 34, v30\nMvaa 176,112,36\nTa 68,21,32768\nv_cmp_gt_i32 s[78:79], 35, v30\nMvaa 172,108,36\nTa 70,21,33792\nv_cndmask_b32 v120, v12, v6, s[90:91]\n" \
"Mvaa 172,140,52\nTa 164,21,40960\nv_cndmask_b32 v121, v12, v6, s[92:93]\nMvaa 168,136,52\nTa 166,21,41984\nv_cndmask_b32 v122, v12, v6, s[74:75]\nMvaa 168,104,36\nTa 72,22,32768\n" \
"v_cndmask_b32 v123, v12, v6, s[78:79]\nMvaa 164,100,36\nTa 74,22,33792\nv_cmp_gt_i32 s[90:91], 48, v30\nMvaa 164,132,52\nTa 168,22,40960\nv_cmp_gt_i32 s[92:93], 49, v30\nMvaa 160,128,52\n" \
"Ta 170,22,41984\nv_cmp_gt_i32 s[74:75], 50, v30\nMvaa 160,96,36\nTa 76,23,32768\nv_cmp_gt_i32 s[78:79], 51, v30\nMvaa 192,96,40\nTa 78,23,33792\nv_cndmask_b32 v124, v12, v6, s[90:91]\n" \
"Mvaa 192,128,56\nTa 172,23,40960\nv_cndmask_b32 v125, v12, v6, s[92:93]\nMvaa 196,132,56\nTa 174,23,41984\nv_cndmask_b32 v126, v12, v6, s[74:75]\nMvaa 196,100,40\nTa 80,24,32768\n" \
"v_cndmask_b32 v127, v12, v6, s[78:79]\nMvaa 200,104,40\nTa 82,24,33792\nMvaa 200,136,56\nMvaa 204,140,56\nTa 176,24,40960\nMvaa 204,108,40\nTa 178,24,41984\n" \
"Mvaa 208,112,40\nTa 84,25,32768\nMvaa 208,144,56\nTa 86,25,33792\nMvaa 212,148,56\nTa 180,25,40960\nMvaa 212,116,40\nTa 182,25,41984\n" \
"Mvaa 216,120,40\nTa 88,26,32768\nMvaa 216,152,56\nTa 90,26,33792\nMvaa 220,156,56\nTa 184,26,40960\nMvaa 220,124,40\nTa 186,26,41984\n" \
"Mvaa 252,124,44\nTa 92,27,32768\nMvaa 252,156,60\nTa 94,27,33792\nMvaa 248,152,60\nTa 188,27,40960\nMvaa 248,120,44\nTa 190,27,41984\n" \
"Mvaa 244,116,44\nRa 4,16,34816\nMvaa 244,148,60\nRa 20,17,34816\nMvaa 240,144,60\nRa 8,16,36864\nMvaa 240,112,44\nRa 24,17,36864\n" \
"Mvaa 236,108,44\nRa 12,16,38912\nMvaa 236,140,60\nRa 28,17,38912\nMvaa 232,136,60\nMvaa 232,104,44\nMvaa 228,100,44\nMvaa 228,132,60\n" \
"Mvaa 224,128,60\nMvaa 224,96,44\ns_waitcnt lgkmcnt(6)\nMvaa 128,64,0\nv_and_b32 v30, 3, v29\nv_bfe_u32 v6, v29, 1, 1\nMvaa 128,160,16\nv_lshrrev_b32 v28, 1, v28\n" \
"v_cmp_eq_u32 s[90:91], 2, v30\nMvaa 132,164,16\nv_cmp_eq_u32 s[92:93], v6, v28\nv_and_b32 v30, 1, v29\nMvaa 132,68,0\nv_mov_b32 v6, 0x3f803f80\nv_cmp_eq_u32 s[78:79], 1, v30\nMvaa 136,72,0\n" \
"v_mov_b32 v7, 0x4444\nv_mov_b32 v30, 0xeeee\nMvaa 136,168,16\nv_cndmask_b32 v108, 0, v6, s[90:91]\nv_cndmask_b32 v109, 0, v6, s[90:91]\nMvaa 140,172,16\nv_cndmask_b32 v110, 0, v6, s[90:91]\nv_cndmask_b32 v111, 0, v6, s[90:91]\n" \
"Mvaa 140,76,0\ns_waitcnt lgkmcnt(0)\nv_cndmask_b32 v104, 0, v6, s[92:93]\nMvaa 144,80,0\nv_cndmask_b32 v105, 0, v6, s[92:93]\nv_cndmask_b32 v106, 0, v6, s[92:93]\nMvaa 144,176,16\nv_cndmask_b32 v107, 0, v6, s[92:93]\n" \
"v_cndmask_b32 v7, v7, v30, s[78:79]\nMvaa 148,180,16\nv_mov_b32 v96, 0\nv_mov_b32 v97, 0\nMvaa 148,84,0\nv_mov_b32 v98, 0\nv_mov_b32 v99, 0\nMvaa 152,88,0\n" \
"v_mov_b32 v100, 0\nv_mov_b32 v101, 0\nMvaa 152,184,16\nv_mov_b32 v102, 0\nv_mov_b32 v103, 0\nMvaa 156,188,16\nv_mov_b32 v5, v0\ns_mov_b32 s73, s32\n" \
"Mvaa 156,92,0\ns_add_u32 s74, s32, 0x4000\ns_add_u32 s75, s32, 0x8000\nMvaa 188,92,4\ns_waitcnt vmcnt(0)\ns_barrier\nv_cmp_ne_u64 vcc, v[10:11], v[2:3]\ns_cbranch_vccnz zs102\n" \
"zr102:\nMvaa 188,188,20\ns_mov_b32 m0, s32\nTa 96,20,16384\nMvaa 184,184,20\nDS 0,60,s77,0\nMvaa 184,88,4\nTa 98,20,17408\n" \
"Ta 128,20,24576\nMvaa 180,84,4\nTa 130,20,25600\ns_mov_b32 s97, 0x3e0293ee\nMvaa 180,180,20\nRa 32,18,0\ns_mov_b32 s89, 31744\nMvaa 176,176,20\n" \
"DS 0,60,s77,1024\nMvaa 176,80,4\nRa 48,19,0\nTa 100,21,16384\nMvaa 172,76,4\nTa 102,21,17408\ns_mov_b32 s90, 6144\nMvaa 172,172,20\n" \
"Ta 132,21,24576\ns_mov_b32 s91, 37888\nMvaa 168,168,20\nDS 0,60,s77,2048\nMvaa 168,72,4\nTa 134,21,25600\nTa 104,22,16384\nMvaa 164,68,4\n" \
"Ta 106,22,17408\ns_mov_b32 s92, 0x200000\nMvaa 164,164,20\nTa 136,22,24576\nTa 138,22,25600\nMvaa 160,160,20\nDS 0,60,s77,3072\nMvaa 160,64,4\n" \
"Ta 108,23,16384\ns_mov_b32 s78, s11\nMvaa 192,64,8\nTa 110,23,17408\ns_add_u32 s84, s32, 98304\nMvaa 192,160,24\ns_add_u32 m0, s32, 16384\ns_add_u32 s77, s77, 0x4000\n" \
"Mvaa 196,164,24\nDS 0,60,s77,0\nMvaa 196,68,8\nTa 140,23,24576\nTa 142,23,25600\nMvaa 200,72,8\nTa 112,24,16384\ns_mov_b32 s85, 8192\n" \
"Mvaa 200,168,24\nTa 114,24,17408\ns_mov_b32 s86, 16384\nMvaa 204,172,24\nDS 0,60,s77,1024\nMvaa 204,76,8\nTa 144,24,24576\nTa 146,24,25600\n" \
"Mvaa 208,80,8\nTa 116,25,16384\ns_mov_b32 s87, 24576\nMvaa 208,176,24\nTa 118,25,17408\nTa 148,25,24576\nMvaa 212,180,24\nDS 0,60,s77,2048\n" \
"Mvaa 212,84,8\nTa 150,25,25600\ns_xor_b32 s82, s11, s10\nMvaa 216,88,8\ns_branch 99f\n.p2align 12\nzp37:\ns_setpc_b64 s[98:99]\n" \
"99:\nTa 120,26,16384\ns_lshl_b32 s83, s82, 16\nMvaa 216,184,24\nTa 122,26,17408\nTa 152,26,24576\nMvaa 220,188,24\nDS 0,60,s77,3072\n" \
"Mvaa 220,92,8\nTa 154,26,25600\ns_add_u32 s83, s83, s18\nMvaa 252,92,12\nTa 124,27,16384\nTa 126,27,17408\nMvaa 252,188,28\ns_add_u32 m0, s32, 49152\n" \
"Mvaa 248,184,28\nDS 8,60,s77,0\nMvaa 248,88,12\nTa 156,27,24576\nTa 158,27,25600\nMvaa 244,84,12\nRa 36,18,2048\nMvaa 244,180,28\n" \
"Ra 52,19,2048\nMvaa 240,176,28\nDS 8,60,s77,1024\nMvaa 240,80,12\nRa 40,18,4096\nRa 56,19,4096\nMvaa 236,76,12\nRa 44,18,6144\n" \
"Mvaa 236,172,28\nRa 60,19,6144\nMvaa 232,168,28\nDS 8,60,s77,2048\nMvaa 232,72,12\nMvaa 228,68,12\nMvaa 228,164,28\nMvaa 224,160,28\n" \
"DS 8,60,s77,3072\nMvaa 224,64,12\ns_waitcnt lgkmcnt(6)\nMvaa 128,96,32\ns_add_u32 m0, s32, 32768\ns_add_u32 s77, s77, 0x4000\nMvaa 128,128,48\nDS 0,60,s77,0\n" \
"Mvaa 132,132,48\nv_lshrrev_b32 v17, 8, v0\nv_and_b32 v16, 0xf0, v0\nMvaa 132,100,32\nDS 0,60,s77,1024\nMvaa 136,104,32\nv_lshlrev_b32 v18, 5, v17\nv_xor_b32 v16, v16, v18\n" \
"Mvaa 136,136,48\nDS 0,60,s77,2048\nMvaa 140,140,48\nv_lshl_add_u32 v16, v17, 11, v16\nv_add_u32 v16, s20, v16\nMvaa 140,108,32\ns_waitcnt lgkmcnt(0)\nDS 0,60,s77,3072\n" \
"Mvaa 144,112,32\ns_add_u32 m0, s32, 65536\ns_xor_b32 s82, s78, s10\nMvaa 144,144,48\nDS 8,60,s77,0\nMvaa 148,148,48\ns_and_b32 s80, s82, 31\ns_lshl_b32 s80, s80, 18\n" \
"Mvaa 148,116,32\nDS 8,60,s77,1024\nMvaa 152,120,32\ns_cmp_lt_u32 s78, 32\ns_cselect_b64 s[68:69], s[14:15], s[12:13]\nMvaa 152,152,48\nDS 8,60,s77,2048\nMvaa 156,156,48\n" \
"s_cselect_b32 s70, 0x800000, s92\nv_add_u32 v18, s80, v16\nMvaa 156,124,32\nDS 8,60,s77,3072\nMvaa 160,96,36\ns_add_u32 s77, s77, 0x4000\nv_xor_b32 v19, 128, v18\nMvaa 160,128,52\n" \
"Mvaa 164,132,52\nMvaa 164,100,36\nMvaa 168,104,36\nMvaa 168,136,52\nv_mul_f32 v128, s97, v128\nv_mul_f32 v129, s97, v129\nv_mul_f32 v130, s97, v130\nMvaa 172,140,52\n" \
"v_mul_f32 v131, s97, v131\nv_mul_f32 v132, s97, v132\nv_mul_f32 v133, s97, v133\nMvaa 172,108,36\nv_mul_f32 v134, s97, v134\nv_mul_f32 v135, s97, v135\nC 32,128,129\nMvaa 176,112,36\n" \
"C 33,130,131\nv_mul_f32 v136, s97, v136\nv_mul_f32 v137, s97, v137\nMvaa 176,144,52\nv_mul_f32 v138, s97, v138\nv_mul_f32 v139, s97, v139\nC 34,132,133\nMvaa 180,148,52\n" \
"C 35,134,135\nv_mul_f32 v140, s97, v140\nv_mul_f32 v141, s97, v141\nMvaa 180,116,36\nv_mul_f32 v142, s97, v142\nv_mul_f32 v143, s97, v143\nC 36,136,137\nMvaa 184,120,36\n" \
"C 37,138,139\nv_mul_f32 v144, s97, v144\nv_mul_f32 v145, s97, v145\nMvaa 184,152,52\nv_mul_f32 v146, s97, v146\nv_mul_f32 v147, s97, v147\nC 38,140,141\nMvaa 188,156,52\n" \
"C 39,142,143\nv_mul_f32 v148, s97, v148\nv_mul_f32 v149, s97, v149\nMvaa 188,124,36\nv_mul_f32 v150, s97, v150\nv_mul_f32 v151, s97, v151\nC 40,144,145\nMvaa 192,96,40\n" \
"C 41,146,147\nv_mul_f32 v152, s97, v152\nv_mul_f32 v153, s97, v153\nMvaa 192,128,56\nv_mul_f32 v154, s97, v154\nv_mul_f32 v155, s97, v155\nC 42,148,149\nMvaa 196,132,56\n" \
"C 43,150,151\nv_mul_f32 v156, s97, v156\nv_mul_f32 v157, s97, v157\nMvaa 196,100,40\nv_mul_f32 v158, s97, v158\nv_mul_f32 v159, s97, v159\nC 44,152,153\nMvaa 200,104,40\n" \
"C 45,154,155\nC 46,156,157\nC 47,158,159\nMvaa 200,136,56\nv_mul_f32 v160, s97, v160\nv_mul_f32 v161, s97, v161\nv_mul_f32 v162, s97, v162\nMvaa 204,140,56\n" \
"v_mul_f32 v163, s97, v163\nv_mul_f32 v164, s97, v164\nv_mul_f32 v165, s97, v165\nMvaa 204,108,40\nv_mul_f32 v166, s97, v166\nv_mul_f32 v167, s97, v167\nC 48,160,161\nMvaa 208,112,40\n" \
"s_waitcnt vmcnt(16)\ns_barrier\nC 49,162,163\nMvaa 208,144,56\nRa 160,5,0\nv_mul_f32 v168, s97, v168\nv_mul_f32 v169, s97, v169\nMvaa 212,148,56\n" \
"v_mul_f32 v170, s97, v170\nv_mul_f32 v171, s97, v171\nC 50,164,165\nMvaa 212,116,40\nRa 164,5,1024\nC 51,166,167\nv_mul_f32 v172, s97, v172\nMvaa 216,120,40\n" \
"v_mul_f32 v173, s97, v173\nv_mul_f32 v174, s97, v174\nv_mul_f32 v175, s97, v175\nMvaa 216,152,56\nRa 168,5,2048\nC 52,168,169\nC 53,170,171\nMvaa 220,156,56\n" \
"v_mul_f32 v176, s97, v176\nv_mul_f32 v177, s97, v177\nv_mul_f32 v178, s97, v178\nMvaa 220,124,40\nRa 172,5,3072\nv_mul_f32 v179, s97, v179\nC 54,172,173\nMvaa 224,96,44\n" \
"C 55,174,175\nv_mul_f32 v180, s97, v180\nv_mul_f32 v181, s97, v181\nMvaa 224,128,60\nRa 176,5,4096\nv_mul_f32 v182, s97, v182\nv_mul_f32 v183, s97, v183\nMvaa 228,132,60\n" \
"C 56,176,177\nC 57,178,179\nv_mul_f32 v184, s97, v184\nMvaa 228,100,44\nRa 180,5,5120\nv_mul_f32 v185, s97, v185\nv_mul_f32 v186, s97, v186\nMvaa 232,104,44\n" \
"v_mul_f32 v187, s97, v187\nC 58,180,181\nC 59,182,183\nMvaa 232,136,60\nRa 184,5,6144\nv_mul_f32 v188, s97, v188\nv_mul_f32 v189, s97, v189\nMvaa 236,140,60\n" \
"v_mul_f32 v190, s97, v190\nv_mul_f32 v191, s97, v191\nC 60,184,185\nMvaa 236,108,44\nRa 188,5,7168\nC 61,186,187\nC 62,188,189\nMvaa 240,112,44\n" \
"C 63,190,191\nv_mul_f32 v192, s97, v192\nv_mul_f32 v193, s97, v193\nMvaa 240,144,60\nv_mul_f32 v194, s97, v194\nv_mul_f32 v195, s97, v195\nv_mul_f32 v196, s97, v196\nMvaa 244,148,60\n" \
"v_mul_f32 v197, s97, v197\nv_mul_f32 v198, s97, v198\nv_mul_f32 v199, s97, v199\nMvaa 244,116,44\nC 64,192,193\nC 65,194,195\nv_mul_f32 v200, s97, v200\nMvaa 248,120,44\n" \
"s_bitcmp1_b32 s33, 2\nv_mul_f32 v201, s97, v201\nv_mul_f32 v202, s97, v202\nMvaa 248,152,60\nv_mul_f32 v203, s97, v203\nC 66,196,197\nC 67,198,199\nMvaa 252,156,60\n" \
"Mvaa 252,124,44\ns_waitcnt lgkmcnt(0)\ns_cbranch_scc1 zZ\nQvav 128,160,32,12\nRa 128,5,8192\nv_mul_f32 v204, s97, v204\nv_mul_f32 v205, s97, v205\nMvav 128,164,36\n" \
"v_mul_f32 v206, s97, v206\nv_mul_f32 v207, s97, v207\nC 68,200,201\nMvav 128,168,40\nRa 132,5,9216\nC 69,202,203\nv_mul_f32 v208, s97, v208\nMvav 128,172,44\n" \
"v_mul_f32 v209, s97, v209\nv_mul_f32 v210, s97, v210\nv_mul_f32 v211, s97, v211\nQvav 132,176,32,12\nRa 136,5,10240\nC 70,204,205\nC 71,206,207\nMvav 132,180,36\n" \
"v_mul_f32 v212, s97, v212\nv_mul_f32 v213, s97, v213\nv_mul_f32 v214, s97, v214\nMvav 132,184,40\nRa 140,5,11264\nv_mul_f32 v215, s97, v215\nC 72,208,209\nMvav 132,188,44\n" \
"C 73,210,211\nv_mul_f32 v216, s97, v216\nv_mul_f32 v217, s97, v217\nQvav 136,160,48,12\nRa 144,5,12288\nv_mul_f32 v218, s97, v218\nv_mul_f32 v219, s97, v219\nMvav 136,164,52\n" \
"C 74,212,213\nC 75,214,215\nv_mul_f32 v220, s97, v220\nMvav 136,168,56\nRa 148,5,13312\nv_mul_f32 v221, s97, v221\nv_mul_f32 v222, s97, v222\nMvav 136,172,60\n" \
"v_mul_f32 v223, s97, v223\nC 76,216,217\nC 77,218,219\nQvav 140,176,48,12\nRa 152,5,14336\nC 78,220,221\nC 79,222,223\nMvav 140,180,52\n" \
"v_mul_f32 v224, s97, v224\nv_mul_f32 v225, s97, v225\nv_mul_f32 v226, s97, v226\nMvav 140,184,56\nRa 156,5,15360\nv_mul_f32 v227, s97, v227\nv_mul_f32 v228, s97, v228\nMvav 140,188,60\n" \
"v_mul_f32 v229, s97, v229\nv_mul_f32 v230, s97, v230\nv_mul_f32 v231, s97, v231\nQvav 144,160,64,12\nC 80,224,225\nC 81,226,227\nv_mul_f32 v232, s97, v232\nMvav 144,164,68\n" \
"v_mul_f32 v233, s97, v233\nv_mul_f32 v234, s97, v234\nv_mul_f32 v235, s97, v235\nMvav 144,168,72\nC 82,228,229\nC 83,230,231\nv_mul_f32 v236, s97, v236\nMvav 144,172,76\n" \
"v_mul_f32 v237, s97, v237\nv_mul_f32 v238, s97, v238\nv_mul_f32 v239, s97, v239\nQvav 148,176,64,12\nC 84,232,233\nC 85,234,235\nv_mul_f32 v240, s97, v240\nMvav 148,180,68\n" \
"v_mul_f32 v241, s97, v241\nv_mul_f32 v242, s97, v242\nv_mul_f32 v243, s97, v243\nC 86,236,237\nC 87,238,239\nv_mul_f32 v244, s97, v244\nv_mul_f32 v245, s97, v245\nv_mul_f32 v246, s97, v246\n" \
"v_mul_f32 v247, s97, v247\nC 88,240,241\nC 89,242,243\nv_mul_f32 v248, s97, v248\nv_mul_f32 v249, s97, v249\nv_mul_f32 v250, s97, v250\nv_mul_f32 v251, s97, v251\nC 90,244,245\n" \
"C 91,246,247\nv_mul_f32 v252, s97, v252\nv_mul_f32 v253, s97, v253\nv_mul_f32 v254, s97, v254\nv_mul_f32 v255, s97, v255\nC 92,248,249\nC 93,250,251\nC 94,252,253\n" \
"C 95,254,255\nMvav 148,184,72\nE 240,128\nMvav 148,188,76\nE 241,129\nQvav 152,160,80,12\nC 192,240,241\nE 242,130\n" \
"Mvav 152,164,84\nE 243,131\nMvav 152,168,88\nC 193,242,243\nMvav 152,172,92\nE 244,132\nQvav 156,176,80,12\nE 245,133\n" \
"Mvav 156,180,84\nC 194,244,245\nE 246,134\nMvav 156,184,88\nE 247,135\nMvav 156,188,92\ns_waitcnt lgkmcnt(0)\nC 195,246,247\n" \
"Qvav 160,128,32,12\nE 240,136\nMvav 160,132,36\nE 241,137\nMvav 160,136,40\nC 196,240,241\nE 242,138\nMvav 160,140,44\n" \
"Qvav 168,128,48,12\nv_mov_b32 v6, v5\nv_add_u32 v5, s74, v9\nE 243,139\nMvav 168,132,52\nC 197,242,243\nE 244,140\nMvav 168,136,56\n" \
"E 245,141\nMvav 168,140,60\nC 198,244,245\nE 246,142\nQvav 176,128,64,12\ns_waitcnt vmcnt(8)\ns_barrier\nbuffer_store_dwordx2 v[2:3], off, s[60:63], s36 sc1\n" \
"Mvav 176,132,68\nRa 160,5,0\nE 247,143\nMvav 176,136,72\ns_mov_b32 m0, s73\nC 199,246,247\nE 240,144\nMvav 176,140,76\n" \
"Ra 164,5,1024\nDS 0,60,s77,0\nE 241,145\nQvav 184,128,80,12\nC 200,240,241\nMvav 184,132,84\nRa 168,5,2048\nE 242,146\n" \
"Mvav 184,136,88\nE 243,147\nMvav 184,140,92\nRa 172,5,3072\nC 201,242,243\nE 244,148\nQvav 164,144,32,12\nDS 0,60,s77,1024\n" \
"E 245,149\nMvav 164,148,36\nRa 176,5,4096\nC 202,244,245\nMvav 164,152,40\nE 246,150\nMvav 164,156,44\nRa 180,5,5120\n" \
"E 247,151\nQvav 172,144,48,12\nC 203,246,247\nE 240,152\nMvav 172,148,52\nRa 184,5,6144\nDS 0,60,s77,2048\nMvav 172,152,56\n" \
"E 241,153\nMvav 172,156,60\nRa 188,5,7168\nC 204,240,241\nE 242,154\nQvav 180,144,64,12\nE 243,155\nMvav 180,148,68\n" \
"C 205,242,243\nE 244,156\nMvav 180,152,72\nDS 0,60,s77,3072\nMvav 180,156,76\nE 245,157\nQvav 188,144,80,12\nC 206,244,245\n" \
"E 246,158\nMvav 188,148,84\nE 247,159\nMvav 188,152,88\nC 207,246,247\ns_mov_b32 s96, s39\nMvav 188,156,92\ns_waitcnt lgkmcnt(0)\n" \
"Qvav 128,160,32,12\nE 240,160\nMvav 128,164,36\nE 241,161\nMvav 128,168,40\nC 208,240,241\nRa 128,6,49152\nMvav 128,172,44\n" \
"E 242,162\ns_add_u32 m0, s75, 0xc000\nQvav 136,160,48,12\nDS 8,60,s77,0\nMvav 136,164,52\nE 243,163\nMvav 136,168,56\nC 209,242,243\n" \
"Ra 132,6,50176\nMvav 136,172,60\nE 244,164\nQvav 144,160,64,12\nDS 8,60,s77,1024\nMvav 144,164,68\nE 245,165\nMvav 144,168,72\n" \
"C 210,244,245\nMvav 144,172,76\nE 246,166\nQvav 152,160,80,12\nRa 136,6,51200\nRa 140,6,52224\nMvav 152,164,84\nE 247,167\n" \
"Mvav 152,168,88\nC 211,246,247\nMvav 152,172,92\nE 240,168\nQvav 132,176,32,12\nDS 8,60,s77,2048\nMvav 132,180,36\nE 241,169\n" \
"Mvav 132,184,40\nC 212,240,241\nRa 144,6,53248\nMvav 132,188,44\nE 242,170\nQvav 140,176,48,12\nRa 148,6,54272\nMvav 140,180,52\n" \
"E 243,171\nMvav 140,184,56\nC 213,242,243\nRa 152,6,55296\nMvav 140,188,60\nE 244,172\nQvav 148,176,64,12\nDS 8,60,s77,3072\n" \
"Mvav 148,180,68\nE 245,173\nMvav 148,184,72\nC 214,244,245\ns_mov_b32 s76, s73\nMvav 148,188,76\nE 246,174\nQvav 156,176,80,12\n" \
"Ra 156,6,56320\ns_mov_b32 s73, s74\nMvav 156,180,84\nE 247,175\nMvav 156,184,88\nC 215,246,247\ns_mov_b32 s74, s75\nMvav 156,188,92\n" \
"E 240,176\nv_smfmac_f32_16x16x64_bf16 v[96:99], v[104:107], v[192:199], v7\ns_mov_b32 s75, s76\nv_smfmac_f32_16x16x64_bf16 v[100:103], v[104:107], v[200:207], v7\ns_waitcnt lgkmcnt(0)\nZaav 96,128,204\nRa 160,5,8192\ns_add_u32 s77, s77, 0x4000\n" \
"Zaav 100,132,204\nE 241,177\nZaav 68,132,200\nC 216,240,241\nRa 164,5,9216\nZaav 64,128,200\nE 242,178\nZaav 32,128,196\n" \
"Zaav 36,132,196\nE 243,179\nZaav 4,132,192\nC 217,242,243\nRa 168,5,10240\nZaav 0,128,192\nE 244,180\nZaav 8,136,192\n" \
"Ra 172,5,11264\nZaav 12,140,192\nE 245,181\nZaav 44,140,196\nC 218,244,245\nZaav 40,136,196\nE 246,182\nZaav 72,136,200\n" \
"Ra 176,5,12288\nZaav 76,140,200\nE 247,183\nZaav 108,140,204\nC 219,246,247\nZaav 104,136,204\nE 240,184\nZaav 112,144,204\n" \
"Ra 180,5,13312\nZaav 116,148,204\nE 241,185\nZaav 84,148,200\nC 220,240,241\nZaav 80,144,200\nE 242,186\nZaav 48,144,196\n" \
"Ra 184,5,14336\nZaav 52,148,196\nE 243,187\nZaav 20,148,192\nC 221,242,243\nRa 188,5,15360\nZaav 16,144,192\nE 244,188\n" \
"Zaav 24,152,192\nZaav 28,156,192\nE 245,189\nZaav 60,156,196\nC 222,244,245\nZaav 56,152,196\nE 246,190\nZaav 88,152,200\n" \
"Zaav 92,156,200\nE 247,191\nZaav 124,156,204\nC 223,246,247\ns_waitcnt lgkmcnt(0)\nZaav 120,152,204\ns_branch zE\nzZ:\n" \
"Qvav 128,160,32,112\nRa 128,5,8192\ns_branch 99f\n.p2align 12\nzp38:\ns_setpc_b64 s[98:99]\n99:\nv_mul_f32 v204, s97, v204\n" \
"v_mul_f32 v205, s97, v205\nMvav 128,164,36\nv_mul_f32 v206, s97, v206\nv_mul_f32 v207, s97, v207\nC 68,200,201\nMvav 128,168,40\nRa 132,5,9216\nC 69,202,203\n" \
"v_mul_f32 v208, s97, v208\nMvav 128,172,44\nv_mul_f32 v209, s97, v209\nv_mul_f32 v210, s97, v210\nv_mul_f32 v211, s97, v211\nQvav 132,176,32,116\nRa 136,5,10240\nC 70,204,205\n" \
"C 71,206,207\nMvav 132,180,36\nv_mul_f32 v212, s97, v212\nv_mul_f32 v213, s97, v213\nv_mul_f32 v214, s97, v214\nMvav 132,184,40\nRa 140,5,11264\nv_mul_f32 v215, s97, v215\n" \
"C 72,208,209\nMvav 132,188,44\nC 73,210,211\nv_mul_f32 v216, s97, v216\nv_mul_f32 v217, s97, v217\nQvav 136,160,48,12\nRa 144,5,12288\nv_mul_f32 v218, s97, v218\n" \
"v_mul_f32 v219, s97, v219\nMvav 136,164,52\nC 74,212,213\nC 75,214,215\nv_mul_f32 v220, s97, v220\nMvav 136,168,56\nRa 148,5,13312\nv_mul_f32 v221, s97, v221\n" \
"v_mul_f32 v222, s97, v222\nMvav 136,172,60\nv_mul_f32 v223, s97, v223\nC 76,216,217\nC 77,218,219\nQvav 140,176,48,12\nRa 152,5,14336\nC 78,220,221\n" \
"C 79,222,223\nMvav 140,180,52\nv_mul_f32 v224, s97, v224\nv_mul_f32 v225, s97, v225\nv_mul_f32 v226, s97, v226\nMvav 140,184,56\nRa 156,5,15360\nv_mul_f32 v227, s97, v227\n" \
"v_mul_f32 v228, s97, v228\nMvav 140,188,60\nv_mul_f32 v229, s97, v229\nv_mul_f32 v230, s97, v230\nv_mul_f32 v231, s97, v231\nQvav 144,160,64,12\nC 80,224,225\nC 81,226,227\n" \
"v_mul_f32 v232, s97, v232\nMvav 144,164,68\nv_mul_f32 v233, s97, v233\nv_mul_f32 v234, s97, v234\nv_mul_f32 v235, s97, v235\nMvav 144,168,72\nC 82,228,229\nC 83,230,231\n" \
"v_mul_f32 v236, s97, v236\nMvav 144,172,76\nv_mul_f32 v237, s97, v237\nv_mul_f32 v238, s97, v238\nv_mul_f32 v239, s97, v239\nQvav 148,176,64,12\nC 84,232,233\nC 85,234,235\n" \
"v_mul_f32 v240, s97, v240\nMvav 148,180,68\nv_mul_f32 v241, s97, v241\nv_mul_f32 v242, s97, v242\nv_mul_f32 v243, s97, v243\nC 86,236,237\nC 87,238,239\nv_mul_f32 v244, s97, v244\n" \
"v_mul_f32 v245, s97, v245\nv_mul_f32 v246, s97, v246\nv_mul_f32 v247, s97, v247\nC 88,240,241\nC 89,242,243\nv_mul_f32 v248, s97, v248\nv_mul_f32 v249, s97, v249\nv_mul_f32 v250, s97, v250\n" \
"v_mul_f32 v251, s97, v251\nC 90,244,245\nC 91,246,247\nv_mul_f32 v252, s97, v252\nv_mul_f32 v253, s97, v253\nv_mul_f32 v254, s97, v254\nv_mul_f32 v255, s97, v255\nC 92,248,249\n" \
"C 93,250,251\nC 94,252,253\nC 95,254,255\nMvav 148,184,72\nE 240,128\nMvav 148,188,76\nE 241,129\nQvav 152,160,80,12\n" \
"C 192,240,241\nE 242,130\nMvav 152,164,84\nE 243,131\nMvav 152,168,88\nC 193,242,243\nMvav 152,172,92\nE 244,132\n" \
"Qvav 156,176,80,12\nE 245,133\nMvav 156,180,84\nC 194,244,245\nE 246,134\nMvav 156,184,88\nE 247,135\nMvav 156,188,92\n" \
"s_waitcnt lgkmcnt(0)\nC 195,246,247\nQvav 160,128,32,120\nE 240,136\nMvav 160,132,36\nE 241,137\nMvav 160,136,40\nC 196,240,241\n" \
"E 242,138\nMvav 160,140,44\nQvav 168,128,48,12\nv_mov_b32 v6, v5\nv_add_u32 v5, s74, v9\nE 243,139\nMvav 168,132,52\nC 197,242,243\n" \
"E 244,140\nMvav 168,136,56\nE 245,141\nMvav 168,140,60\nC 198,244,245\nE 246,142\nQvav 176,128,64,12\ns_waitcnt vmcnt(8)\n" \
"s_barrier\nbuffer_store_dwordx2 v[2:3], off, s[60:63], s36 sc1\nMvav 176,132,68\nRa 160,5,0\nE 247,143\nMvav 176,136,72\ns_mov_b32 m0, s73\nC 199,246,247\n" \
"E 240,144\nMvav 176,140,76\nRa 164,5,1024\nDS 0,60,s77,0\nE 241,145\nQvav 184,128,80,12\nC 200,240,241\nMvav 184,132,84\n" \
"Ra 168,5,2048\nE 242,146\nMvav 184,136,88\nE 243,147\nMvav 184,140,92\nRa 172,5,3072\nC 201,242,243\nE 244,148\n" \
"Qvav 164,144,32,124\nDS 0,60,s77,1024\nE 245,149\nMvav 164,148,36\nRa 176,5,4096\nC 202,244,245\nMvav 164,152,40\nE 246,150\n" \
"Mvav 164,156,44\nRa 180,5,5120\nE 247,151\nQvav 172,144,48,12\nC 203,246,247\nE 240,152\nMvav 172,148,52\nRa 184,5,6144\n" \
"DS 0,60,s77,2048\nMvav 172,152,56\nE 241,153\nMvav 172,156,60\nRa 188,5,7168\nC 204,240,241\nE 242,154\nQvav 180,144,64,12\n" \
"E 243,155\nMvav 180,148,68\nC 205,242,243\nE 244,156\nMvav 180,152,72\nDS 0,60,s77,3072\nMvav 180,156,76\nE 245,157\n" \
"Qvav 188,144,80,12\nC 206,244,245\nE 246,158\nMvav 188,148,84\nE 247,159\nMvav 188,152,88\nC 207,246,247\ns_mov_b32 s96, s39\n" \
"Mvav 188,156,92\ns_waitcnt lgkmcnt(0)\nQvav 136,160,48,112\nE 240,160\nRa 128,6,49152\nMvav 136,164,52\nE 241,161\nMvav 136,168,56\n" \
"C 208,240,241\nRa 132,6,50176\nMvav 136,172,60\nE 242,162\ns_add_u32 m0, s75, 0xc000\nQvav 144,160,64,12\nDS 8,60,s77,0\nMvav 144,164,68\n" \
"E 243,163\nRa 136,6,51200\nMvav 144,168,72\nC 209,242,243\nE 244,164\nMvav 144,172,76\nDS 8,60,s77,1024\nQvav 152,160,80,12\n" \
"E 245,165\nMvav 152,164,84\nC 210,244,245\nRa 140,6,52224\nMvav 152,168,88\nE 246,166\nRa 144,6,53248\nMvav 152,172,92\n" \
"DS 8,60,s77,2048\nQvav 140,176,48,116\nE 247,167\nMvav 140,180,52\nC 211,246,247\nE 240,168\nMvav 140,184,56\nDS 8,60,s77,3072\n" \
"Mvav 140,188,60\nE 241,169\nRa 148,6,54272\nQvav 148,176,64,12\nC 212,240,241\nRa 152,6,55296\nMvav 148,180,68\nE 242,170\n" \
"Mvav 148,184,72\nE 243,171\nMvav 148,188,76\nC 213,242,243\nRa 156,6,56320\nQvav 156,176,80,12\nE 244,172\nMvav 156,180,84\n" \
"s_mov_b32 s76, s73\ns_mov_b32 s73, s74\nMvav 156,184,88\nE 245,173\nMvav 156,188,92\nC 214,244,245\ns_mov_b32 s74, s75\nv_smfmac_f32_16x16x64_bf16 v[96:99], v[104:107], v[192:199], v7\n" \
"s_mov_b32 s75, s76\nv_smfmac_f32_16x16x64_bf16 v[100:103], v[104:107], v[200:207], v7\ns_waitcnt lgkmcnt(0)\nZaav 96,128,204\nE 246,174\nZaav 100,132,204\nE 247,175\nZaav 68,132,200\n" \
"C 215,246,247\nRa 160,5,8192\nZaav 64,128,200\nE 240,176\nZaav 32,128,196\nRa 164,5,9216\ns_add_u32 s77, s77, 0x4000\nZaav 36,132,196\n" \
"E 241,177\nZaav 4,132,192\nC 216,240,241\nE 242,178\nZaav 0,128,192\nRa 168,5,10240\nZaav 8,136,192\nE 243,179\n" \
"Zaav 12,140,192\nC 217,242,243\nRa 172,5,11264\nZaav 44,140,196\nE 244,180\nZaav 40,136,196\nRa 176,5,12288\nZaav 72,136,200\n" \
"E 245,181\nZaav 76,140,200\nC 218,244,245\nE 246,182\nZaav 108,140,204\nRa 180,5,13312\nZaav 104,136,204\nE 247,183\n" \
"Zaav 112,144,204\nC 219,246,247\nZaav 116,148,204\nE 240,184\nZaav 84,148,200\nE 241,185\nZaav 80,144,200\nC 220,240,241\n" \
"Ra 184,5,14336\nZaav 48,144,196\nE 242,186\nZaav 52,148,196\nRa 188,5,15360\nZaav 20,148,192\nE 243,187\nZaav 16,144,192\n" \
"C 221,242,243\nZaav 24,152,192\nE 244,188\nZaav 28,156,192\nE 245,189\nZaav 60,156,196\nC 222,244,245\nZaav 56,152,196\n" \
"E 246,190\nZaav 88,152,200\nZaav 92,156,200\nE 247,191\nZaav 124,156,204\nC 223,246,247\ns_waitcnt lgkmcnt(0)\nZaav 120,152,204\n" \
"s_branch zD4\nzfb:\nv_cmp_lt_u32 vcc, 0x3c000000, v240\ns_cbranch_vccz zf0e\ns_add_u32 s96, s42, 2\ns_mov_b32 s77, s38\nv_mov_b32 v136, 0xff800000\nv_mov_b32 v138, 0\n" \
"v_add_u32 v140, 0x4000, v8\nv_accvgpr_write_b32 a0, 0\nv_accvgpr_write_b32 a1, 0\nv_accvgpr_write_b32 a2, 0\nv_accvgpr_write_b32 a3, 0\nv_accvgpr_write_b32 a4, 0\nv_accvgpr_write_b32 a5, 0\nv_accvgpr_write_b32 a6, 0\n" \
"v_accvgpr_write_b32 a7, 0\nv_accvgpr_write_b32 a8, 0\nv_accvgpr_write_b32 a9, 0\nv_accvgpr_write_b32 a10, 0\nv_accvgpr_write_b32 a11, 0\nv_accvgpr_write_b32 a12, 0\nv_accvgpr_write_b32 a13, 0\nv_accvgpr_write_b32 a14, 0\n" \
"v_accvgpr_write_b32 a15, 0\nv_accvgpr_write_b32 a16, 0\nv_accvgpr_write_b32 a17, 0\nv_accvgpr_write_b32 a18, 0\nv_accvgpr_write_b32 a19, 0\nv_accvgpr_write_b32 a20, 0\nv_accvgpr_write_b32 a21, 0\nv_accvgpr_write_b32 a22, 0\n" \
"v_accvgpr_write_b32 a23, 0\nv_accvgpr_write_b32 a24, 0\nv_accvgpr_write_b32 a25, 0\nv_accvgpr_write_b32 a26, 0\nv_accvgpr_write_b32 a27, 0\nv_accvgpr_write_b32 a28, 0\nv_accvgpr_write_b32 a29, 0\nv_accvgpr_write_b32 a30, 0\n" \
"v_accvgpr_write_b32 a31, 0\nzf0l:\ns_add_u32 s76, s77, 4096\nbuffer_load_dwordx4 a[160:163], v0, s[60:63], s77 offen offset:0 sc1\nbuffer_load_dwordx4 a[164:167], v0, s[60:63], s77 offen offset:1024 sc1\nbuffer_load_dwordx4 a[168:171], v0, s[60:63], s77 offen offset:2048 sc1\nbuffer_load_dwordx4 a[172:175], v0, s[60:63], s77 offen offset:3072 sc1\nbuffer_load_dwordx4 a[176:179], v0, s[60:63], s76 offen offset:0 sc1\n" \
"buffer_load_dwordx4 a[180:183], v0, s[60:63], s76 offen offset:1024 sc1\nbuffer_load_dwordx4 a[184:187], v0, s[60:63], s76 offen offset:2048 sc1\nbuffer_load_dwordx4 a[188:191], v0, s[60:63], s76 offen offset:3072 sc1\nbuffer_load_dwordx4 a[128:131], v140, s[60:63], s77 offen offset:0 sc1\nbuffer_load_dwordx4 a[132:135], v140, s[60:63], s77 offen offset:1024 sc1\nbuffer_load_dwordx4 a[136:139], v140, s[60:63], s77 offen offset:2048 sc1\nbuffer_load_dwordx4 a[140:143], v140, s[60:63], s77 offen offset:3072 sc1\nbuffer_load_dwordx4 a[144:147], v140, s[60:63], s76 offen offset:0 sc1\n" \
"buffer_load_dwordx4 a[148:151], v140, s[60:63], s76 offen offset:1024 sc1\nbuffer_load_dwordx4 a[152:155], v140, s[60:63], s76 offen offset:2048 sc1\nbuffer_load_dwordx4 a[156:159], v140, s[60:63], s76 offen offset:3072 sc1\nv_mov_b32 v144, v12\nv_mov_b32 v145, v12\nv_mov_b32 v146, v12\nv_mov_b32 v147, v12\nv_mov_b32 v148, v12\n" \
"v_mov_b32 v149, v12\nv_mov_b32 v150, v12\nv_mov_b32 v151, v12\ns_cmp_gt_u32 s96, 2\ns_cbranch_scc1 zf0m\nv_mov_b32 v144, v112\nv_mov_b32 v145, v113\nv_mov_b32 v146, v114\n" \
"v_mov_b32 v147, v115\nv_mov_b32 v148, v116\nv_mov_b32 v149, v117\nv_mov_b32 v150, v118\nv_mov_b32 v151, v119\ns_cmp_eq_u32 s96, 2\ns_cbranch_scc1 zf0m\nv_mov_b32 v144, v120\n" \
"v_mov_b32 v145, v121\nv_mov_b32 v146, v122\nv_mov_b32 v147, v123\nv_mov_b32 v148, v124\nv_mov_b32 v149, v125\nv_mov_b32 v150, v126\nv_mov_b32 v151, v127\nzf0m:\n" \
"s_waitcnt vmcnt(0)\nQvav 128,160,32,144\nMvav 128,164,36\nMvav 128,168,40\nMvav 128,172,44\nQvav 132,176,32,148\nMvav 132,180,36\nMvav 132,184,40\n" \
"Mvav 132,188,44\ns_nop 7\nv_max3_f32 v141, v128, v129, v130\nv_max3_f32 v142, v131, v132, v133\nv_max3_f32 v141, v141, v134, v135\nv_max3_f32 v141, v141, v142, v136\nv_mov_b32 v142, v141\ns_nop 1\n" \
"v_permlane16_swap_b32 v141, v142\ns_nop 0\nv_max_f32 v141, v141, v142\nv_mov_b32 v142, v141\ns_nop 1\nv_permlane32_swap_b32 v141, v142\ns_nop 0\nv_max_f32 v141, v141, v142\n" \
"v_sub_f32 v137, v136, v141\nE 137,137\nv_mov_b32 v136, v141\nv_sub_f32 v128, v128, v141\nv_sub_f32 v129, v129, v141\nv_sub_f32 v130, v130, v141\nv_sub_f32 v131, v131, v141\nv_sub_f32 v132, v132, v141\n" \
"v_sub_f32 v133, v133, v141\nv_sub_f32 v134, v134, v141\nv_sub_f32 v135, v135, v141\nE 128,128\nE 129,129\nE 130,130\nE 131,131\nE 132,132\n" \
"E 133,133\nE 134,134\nE 135,135\nv_mul_f32 v138, v138, v137\nC 192,128,129\nC 193,130,131\nC 194,132,133\nC 195,134,135\n" \
"v_add_f32 v138, v138, v128\nv_add_f32 v138, v138, v129\nv_add_f32 v138, v138, v130\nv_add_f32 v138, v138, v131\nv_add_f32 v138, v138, v132\nv_add_f32 v138, v138, v133\nv_add_f32 v138, v138, v134\nv_add_f32 v138, v138, v135\n" \
"AR 141,0\nv_mul_f32 v141, v141, v137\nAW 0,141\nAR 141,1\nv_mul_f32 v141, v141, v137\nAW 1,141\nAR 141,2\nv_mul_f32 v141, v141, v137\n" \
"AW 2,141\nAR 141,3\nv_mul_f32 v141, v141, v137\nAW 3,141\nAR 141,4\nv_mul_f32 v141, v141, v137\nAW 4,141\nAR 141,5\n" \
"v_mul_f32 v141, v141, v137\nAW 5,141\nAR 141,6\nv_mul_f32 v141, v141, v137\nAW 6,141\nAR 141,7\nv_mul_f32 v141, v141, v137\nAW 7,141\n" \
"AR 141,8\nv_mul_f32 v141, v141, v137\nAW 8,141\nAR 141,9\nv_mul_f32 v141, v141, v137\nAW 9,141\nAR 141,10\nv_mul_f32 v141, v141, v137\n" \
"AW 10,141\nAR 141,11\nv_mul_f32 v141, v141, v137\nAW 11,141\nAR 141,12\nv_mul_f32 v141, v141, v137\nAW 12,141\nAR 141,13\n" \
"v_mul_f32 v141, v141, v137\nAW 13,141\nAR 141,14\nv_mul_f32 v141, v141, v137\nAW 14,141\nAR 141,15\nv_mul_f32 v141, v141, v137\nAW 15,141\n" \
"AR 141,16\nv_mul_f32 v141, v141, v137\nAW 16,141\nAR 141,17\nv_mul_f32 v141, v141, v137\nAW 17,141\nAR 141,18\nv_mul_f32 v141, v141, v137\n" \
"AW 18,141\nAR 141,19\nv_mul_f32 v141, v141, v137\nAW 19,141\nAR 141,20\nv_mul_f32 v141, v141, v137\nAW 20,141\nAR 141,21\n" \
"v_mul_f32 v141, v141, v137\nAW 21,141\nAR 141,22\nv_mul_f32 v141, v141, v137\nAW 22,141\nAR 141,23\nv_mul_f32 v141, v141, v137\nAW 23,141\n" \
"AR 141,24\nv_mul_f32 v141, v141, v137\nAW 24,141\nAR 141,25\nv_mul_f32 v141, v141, v137\nAW 25,141\nAR 141,26\nv_mul_f32 v141, v141, v137\n" \
"AW 26,141\nAR 141,27\nv_mul_f32 v141, v141, v137\nAW 27,141\nAR 141,28\nv_mul_f32 v141, v141, v137\nAW 28,141\nAR 141,29\n" \
"v_mul_f32 v141, v141, v137\nAW 29,141\nAR 141,30\nv_mul_f32 v141, v141, v137\nAW 30,141\nAR 141,31\nv_mul_f32 v141, v141, v137\nAW 31,141\n" \
"s_nop 1\nMaav 0,128,192\nMaav 4,132,192\nMaav 8,136,192\nMaav 12,140,192\nMaav 16,144,192\nMaav 20,148,192\nMaav 24,152,192\n" \
"Maav 28,156,192\ns_add_u32 s77, s77, 8192\ns_sub_u32 s96, s96, 1\ns_cmp_lg_u32 s96, 0\ns_nop 7\ns_cbranch_scc1 zf0l\nv_mov_b32 v141, v138\ns_nop 1\n" \
"v_permlane16_swap_b32 v138, v141\ns_nop 0\nv_add_f32 v138, v138, v141\nv_mov_b32 v141, v138\ns_nop 1\nv_permlane32_swap_b32 v138, v141\ns_nop 0\nv_add_f32 v138, v138, v141\n" \
"v_mov_b32 v104, v138\nzf0e:\nv_cmp_lt_u32 vcc, 0x3c000000, v241\ns_cbranch_vccz zf1e\ns_add_u32 s96, s42, 4\ns_mov_b32 s77, s38\nv_mov_b32 v136, 0xff800000\nv_mov_b32 v138, 0\n" \
"v_add_u32 v140, 0x4000, v8\nv_accvgpr_write_b32 a32, 0\nv_accvgpr_write_b32 a33, 0\nv_accvgpr_write_b32 a34, 0\nv_accvgpr_write_b32 a35, 0\nv_accvgpr_write_b32 a36, 0\nv_accvgpr_write_b32 a37, 0\nv_accvgpr_write_b32 a38, 0\n" \
"v_accvgpr_write_b32 a39, 0\nv_accvgpr_write_b32 a40, 0\nv_accvgpr_write_b32 a41, 0\nv_accvgpr_write_b32 a42, 0\nv_accvgpr_write_b32 a43, 0\nv_accvgpr_write_b32 a44, 0\nv_accvgpr_write_b32 a45, 0\nv_accvgpr_write_b32 a46, 0\n" \
"v_accvgpr_write_b32 a47, 0\ns_branch 99f\n.p2align 12\nzp39:\ns_setpc_b64 s[98:99]\n99:\nv_accvgpr_write_b32 a48, 0\nv_accvgpr_write_b32 a49, 0\n" \
"v_accvgpr_write_b32 a50, 0\nv_accvgpr_write_b32 a51, 0\nv_accvgpr_write_b32 a52, 0\nv_accvgpr_write_b32 a53, 0\nv_accvgpr_write_b32 a54, 0\nv_accvgpr_write_b32 a55, 0\nv_accvgpr_write_b32 a56, 0\nv_accvgpr_write_b32 a57, 0\n" \
"v_accvgpr_write_b32 a58, 0\nv_accvgpr_write_b32 a59, 0\nv_accvgpr_write_b32 a60, 0\nv_accvgpr_write_b32 a61, 0\nv_accvgpr_write_b32 a62, 0\nv_accvgpr_write_b32 a63, 0\nzf1l:\ns_add_u32 s76, s77, 4096\n" \
"buffer_load_dwordx4 a[160:163], v0, s[60:63], s77 offen offset:0 sc1\nbuffer_load_dwordx4 a[164:167], v0, s[60:63], s77 offen offset:1024 sc1\nbuffer_load_dwordx4 a[168:171], v0, s[60:63], s77 offen offset:2048 sc1\nbuffer_load_dwordx4 a[172:175], v0, s[60:63], s77 offen offset:3072 sc1\nbuffer_load_dwordx4 a[176:179], v0, s[60:63], s76 offen offset:0 sc1\nbuffer_load_dwordx4 a[180:183], v0, s[60:63], s76 offen offset:1024 sc1\nbuffer_load_dwordx4 a[184:187], v0, s[60:63], s76 offen offset:2048 sc1\nbuffer_load_dwordx4 a[188:191], v0, s[60:63], s76 offen offset:3072 sc1\n" \
"buffer_load_dwordx4 a[128:131], v140, s[60:63], s77 offen offset:0 sc1\nbuffer_load_dwordx4 a[132:135], v140, s[60:63], s77 offen offset:1024 sc1\nbuffer_load_dwordx4 a[136:139], v140, s[60:63], s77 offen offset:2048 sc1\nbuffer_load_dwordx4 a[140:143], v140, s[60:63], s77 offen offset:3072 sc1\nbuffer_load_dwordx4 a[144:147], v140, s[60:63], s76 offen offset:0 sc1\nbuffer_load_dwordx4 a[148:151], v140, s[60:63], s76 offen offset:1024 sc1\nbuffer_load_dwordx4 a[152:155], v140, s[60:63], s76 offen offset:2048 sc1\nbuffer_load_dwordx4 a[156:159], v140, s[60:63], s76 offen offset:3072 sc1\n" \
"v_mov_b32 v144, v12\nv_mov_b32 v145, v12\nv_mov_b32 v146, v12\nv_mov_b32 v147, v12\nv_mov_b32 v148, v12\nv_mov_b32 v149, v12\nv_mov_b32 v150, v12\nv_mov_b32 v151, v12\n" \
"s_cmp_gt_u32 s96, 2\ns_cbranch_scc1 zf1m\nv_mov_b32 v144, v112\nv_mov_b32 v145, v113\nv_mov_b32 v146, v114\nv_mov_b32 v147, v115\nv_mov_b32 v148, v116\nv_mov_b32 v149, v117\n" \
"v_mov_b32 v150, v118\nv_mov_b32 v151, v119\ns_cmp_eq_u32 s96, 2\ns_cbranch_scc1 zf1m\nv_mov_b32 v144, v120\nv_mov_b32 v145, v121\nv_mov_b32 v146, v122\nv_mov_b32 v147, v123\n" \
"v_mov_b32 v148, v124\nv_mov_b32 v149, v125\nv_mov_b32 v150, v126\nv_mov_b32 v151, v127\nzf1m:\ns_waitcnt vmcnt(0)\nQvav 128,160,48,144\nMvav 128,164,52\n" \
"Mvav 128,168,56\nMvav 128,172,60\nQvav 132,176,48,148\nMvav 132,180,52\nMvav 132,184,56\nMvav 132,188,60\ns_nop 7\nv_max3_f32 v141, v128, v129, v130\n" \
"v_max3_f32 v142, v131, v132, v133\nv_max3_f32 v141, v141, v134, v135\nv_max3_f32 v141, v141, v142, v136\nv_mov_b32 v142, v141\ns_nop 1\nv_permlane16_swap_b32 v141, v142\ns_nop 0\nv_max_f32 v141, v141, v142\n" \
"v_mov_b32 v142, v141\ns_nop 1\nv_permlane32_swap_b32 v141, v142\ns_nop 0\nv_max_f32 v141, v141, v142\nv_sub_f32 v137, v136, v141\nE 137,137\nv_mov_b32 v136, v141\n" \
"v_sub_f32 v128, v128, v141\nv_sub_f32 v129, v129, v141\nv_sub_f32 v130, v130, v141\nv_sub_f32 v131, v131, v141\nv_sub_f32 v132, v132, v141\nv_sub_f32 v133, v133, v141\nv_sub_f32 v134, v134, v141\nv_sub_f32 v135, v135, v141\n" \
"E 128,128\nE 129,129\nE 130,130\nE 131,131\nE 132,132\nE 133,133\nE 134,134\nE 135,135\n" \
"v_mul_f32 v138, v138, v137\nC 192,128,129\nC 193,130,131\nC 194,132,133\nC 195,134,135\nv_add_f32 v138, v138, v128\nv_add_f32 v138, v138, v129\nv_add_f32 v138, v138, v130\n" \
"v_add_f32 v138, v138, v131\nv_add_f32 v138, v138, v132\nv_add_f32 v138, v138, v133\nv_add_f32 v138, v138, v134\nv_add_f32 v138, v138, v135\nAR 141,32\nv_mul_f32 v141, v141, v137\nAW 32,141\n" \
"AR 141,33\nv_mul_f32 v141, v141, v137\nAW 33,141\nAR 141,34\nv_mul_f32 v141, v141, v137\nAW 34,141\nAR 141,35\nv_mul_f32 v141, v141, v137\n" \
"AW 35,141\nAR 141,36\nv_mul_f32 v141, v141, v137\nAW 36,141\nAR 141,37\nv_mul_f32 v141, v141, v137\nAW 37,141\nAR 141,38\n" \
"v_mul_f32 v141, v141, v137\nAW 38,141\nAR 141,39\nv_mul_f32 v141, v141, v137\nAW 39,141\nAR 141,40\nv_mul_f32 v141, v141, v137\nAW 40,141\n" \
"AR 141,41\nv_mul_f32 v141, v141, v137\nAW 41,141\nAR 141,42\nv_mul_f32 v141, v141, v137\nAW 42,141\nAR 141,43\nv_mul_f32 v141, v141, v137\n" \
"AW 43,141\nAR 141,44\nv_mul_f32 v141, v141, v137\nAW 44,141\nAR 141,45\nv_mul_f32 v141, v141, v137\nAW 45,141\nAR 141,46\n" \
"v_mul_f32 v141, v141, v137\nAW 46,141\nAR 141,47\nv_mul_f32 v141, v141, v137\nAW 47,141\nAR 141,48\nv_mul_f32 v141, v141, v137\nAW 48,141\n" \
"AR 141,49\nv_mul_f32 v141, v141, v137\nAW 49,141\nAR 141,50\nv_mul_f32 v141, v141, v137\nAW 50,141\nAR 141,51\nv_mul_f32 v141, v141, v137\n" \
"AW 51,141\nAR 141,52\nv_mul_f32 v141, v141, v137\nAW 52,141\nAR 141,53\nv_mul_f32 v141, v141, v137\nAW 53,141\nAR 141,54\n" \
"v_mul_f32 v141, v141, v137\nAW 54,141\nAR 141,55\nv_mul_f32 v141, v141, v137\nAW 55,141\nAR 141,56\nv_mul_f32 v141, v141, v137\nAW 56,141\n" \
"AR 141,57\nv_mul_f32 v141, v141, v137\nAW 57,141\nAR 141,58\nv_mul_f32 v141, v141, v137\nAW 58,141\nAR 141,59\nv_mul_f32 v141, v141, v137\n" \
"AW 59,141\nAR 141,60\nv_mul_f32 v141, v141, v137\nAW 60,141\nAR 141,61\nv_mul_f32 v141, v141, v137\nAW 61,141\nAR 141,62\n" \
"v_mul_f32 v141, v141, v137\nAW 62,141\nAR 141,63\nv_mul_f32 v141, v141, v137\nAW 63,141\ns_nop 1\nMaav 32,128,192\nMaav 36,132,192\n" \
"Maav 40,136,192\nMaav 44,140,192\nMaav 48,144,192\nMaav 52,148,192\nMaav 56,152,192\nMaav 60,156,192\ns_add_u32 s77, s77, 8192\ns_sub_u32 s96, s96, 1\n" \
"s_cmp_lg_u32 s96, 0\ns_nop 7\ns_cbranch_scc1 zf1l\nv_mov_b32 v141, v138\ns_nop 1\nv_permlane16_swap_b32 v138, v141\ns_nop 0\nv_add_f32 v138, v138, v141\n" \
"v_mov_b32 v141, v138\ns_nop 1\nv_permlane32_swap_b32 v138, v141\ns_nop 0\nv_add_f32 v138, v138, v141\nv_mov_b32 v105, v138\nzf1e:\nv_cmp_lt_u32 vcc, 0x3c000000, v242\n" \
"s_cbranch_vccz zf2e\ns_add_u32 s96, s43, 2\ns_mov_b32 s77, s38\nv_mov_b32 v136, 0xff800000\nv_mov_b32 v138, 0\nv_add_u32 v140, 0x4000, v8\nv_accvgpr_write_b32 a64, 0\nv_accvgpr_write_b32 a65, 0\n" \
"v_accvgpr_write_b32 a66, 0\nv_accvgpr_write_b32 a67, 0\nv_accvgpr_write_b32 a68, 0\nv_accvgpr_write_b32 a69, 0\nv_accvgpr_write_b32 a70, 0\nv_accvgpr_write_b32 a71, 0\nv_accvgpr_write_b32 a72, 0\nv_accvgpr_write_b32 a73, 0\n" \
"v_accvgpr_write_b32 a74, 0\nv_accvgpr_write_b32 a75, 0\nv_accvgpr_write_b32 a76, 0\nv_accvgpr_write_b32 a77, 0\nv_accvgpr_write_b32 a78, 0\nv_accvgpr_write_b32 a79, 0\nv_accvgpr_write_b32 a80, 0\nv_accvgpr_write_b32 a81, 0\n" \
"v_accvgpr_write_b32 a82, 0\nv_accvgpr_write_b32 a83, 0\nv_accvgpr_write_b32 a84, 0\nv_accvgpr_write_b32 a85, 0\nv_accvgpr_write_b32 a86, 0\nv_accvgpr_write_b32 a87, 0\nv_accvgpr_write_b32 a88, 0\nv_accvgpr_write_b32 a89, 0\n" \
"v_accvgpr_write_b32 a90, 0\nv_accvgpr_write_b32 a91, 0\nv_accvgpr_write_b32 a92, 0\nv_accvgpr_write_b32 a93, 0\nv_accvgpr_write_b32 a94, 0\nv_accvgpr_write_b32 a95, 0\nzf2l:\ns_add_u32 s76, s77, 4096\n" \
"buffer_load_dwordx4 a[160:163], v0, s[60:63], s77 offen offset:0 sc1\nbuffer_load_dwordx4 a[164:167], v0, s[60:63], s77 offen offset:1024 sc1\nbuffer_load_dwordx4 a[168:171], v0, s[60:63], s77 offen offset:2048 sc1\nbuffer_load_dwordx4 a[172:175], v0, s[60:63], s77 offen offset:3072 sc1\nbuffer_load_dwordx4 a[176:179], v0, s[60:63], s76 offen offset:0 sc1\nbuffer_load_dwordx4 a[180:183], v0, s[60:63], s76 offen offset:1024 sc1\nbuffer_load_dwordx4 a[184:187], v0, s[60:63], s76 offen offset:2048 sc1\nbuffer_load_dwordx4 a[188:191], v0, s[60:63], s76 offen offset:3072 sc1\n" \
"buffer_load_dwordx4 a[128:131], v140, s[60:63], s77 offen offset:0 sc1\nbuffer_load_dwordx4 a[132:135], v140, s[60:63], s77 offen offset:1024 sc1\nbuffer_load_dwordx4 a[136:139], v140, s[60:63], s77 offen offset:2048 sc1\nbuffer_load_dwordx4 a[140:143], v140, s[60:63], s77 offen offset:3072 sc1\nbuffer_load_dwordx4 a[144:147], v140, s[60:63], s76 offen offset:0 sc1\nbuffer_load_dwordx4 a[148:151], v140, s[60:63], s76 offen offset:1024 sc1\nbuffer_load_dwordx4 a[152:155], v140, s[60:63], s76 offen offset:2048 sc1\nbuffer_load_dwordx4 a[156:159], v140, s[60:63], s76 offen offset:3072 sc1\n" \
"v_mov_b32 v144, v12\nv_mov_b32 v145, v12\nv_mov_b32 v146, v12\nv_mov_b32 v147, v12\nv_mov_b32 v148, v12\nv_mov_b32 v149, v12\nv_mov_b32 v150, v12\nv_mov_b32 v151, v12\n" \
"s_cmp_gt_u32 s96, 2\ns_cbranch_scc1 zf2m\nv_mov_b32 v144, v112\nv_mov_b32 v145, v113\nv_mov_b32 v146, v114\nv_mov_b32 v147, v115\nv_mov_b32 v148, v116\nv_mov_b32 v149, v117\n" \
"v_mov_b32 v150, v118\nv_mov_b32 v151, v119\ns_cmp_eq_u32 s96, 2\ns_cbranch_scc1 zf2m\nv_mov_b32 v144, v120\nv_mov_b32 v145, v121\nv_mov_b32 v146, v122\nv_mov_b32 v147, v123\n" \
"v_mov_b32 v148, v124\nv_mov_b32 v149, v125\nv_mov_b32 v150, v126\nv_mov_b32 v151, v127\nzf2m:\ns_waitcnt vmcnt(0)\nQvav 128,160,64,144\nMvav 128,164,68\n" \
"Mvav 128,168,72\nMvav 128,172,76\nQvav 132,176,64,148\nMvav 132,180,68\nMvav 132,184,72\nMvav 132,188,76\ns_nop 7\nv_max3_f32 v141, v128, v129, v130\n" \
"v_max3_f32 v142, v131, v132, v133\nv_max3_f32 v141, v141, v134, v135\nv_max3_f32 v141, v141, v142, v136\nv_mov_b32 v142, v141\ns_nop 1\nv_permlane16_swap_b32 v141, v142\ns_nop 0\nv_max_f32 v141, v141, v142\n" \
"v_mov_b32 v142, v141\ns_nop 1\nv_permlane32_swap_b32 v141, v142\ns_nop 0\nv_max_f32 v141, v141, v142\nv_sub_f32 v137, v136, v141\nE 137,137\nv_mov_b32 v136, v141\n" \
"v_sub_f32 v128, v128, v141\nv_sub_f32 v129, v129, v141\nv_sub_f32 v130, v130, v141\nv_sub_f32 v131, v131, v141\nv_sub_f32 v132, v132, v141\nv_sub_f32 v133, v133, v141\nv_sub_f32 v134, v134, v141\nv_sub_f32 v135, v135, v141\n" \
"E 128,128\nE 129,129\nE 130,130\nE 131,131\nE 132,132\nE 133,133\nE 134,134\nE 135,135\n" \
"v_mul_f32 v138, v138, v137\nC 192,128,129\nC 193,130,131\nC 194,132,133\nC 195,134,135\nv_add_f32 v138, v138, v128\nv_add_f32 v138, v138, v129\nv_add_f32 v138, v138, v130\n" \
"v_add_f32 v138, v138, v131\nv_add_f32 v138, v138, v132\nv_add_f32 v138, v138, v133\nv_add_f32 v138, v138, v134\nv_add_f32 v138, v138, v135\nAR 141,64\nv_mul_f32 v141, v141, v137\nAW 64,141\n" \
"AR 141,65\nv_mul_f32 v141, v141, v137\nAW 65,141\nAR 141,66\nv_mul_f32 v141, v141, v137\nAW 66,141\nAR 141,67\nv_mul_f32 v141, v141, v137\n" \
"AW 67,141\nAR 141,68\nv_mul_f32 v141, v141, v137\nAW 68,141\nAR 141,69\nv_mul_f32 v141, v141, v137\nAW 69,141\nAR 141,70\n" \
"v_mul_f32 v141, v141, v137\nAW 70,141\nAR 141,71\nv_mul_f32 v141, v141, v137\nAW 71,141\nAR 141,72\nv_mul_f32 v141, v141, v137\nAW 72,141\n" \
"AR 141,73\nv_mul_f32 v141, v141, v137\nAW 73,141\nAR 141,74\nv_mul_f32 v141, v141, v137\nAW 74,141\nAR 141,75\nv_mul_f32 v141, v141, v137\n" \
"AW 75,141\nAR 141,76\nv_mul_f32 v141, v141, v137\nAW 76,141\nAR 141,77\nv_mul_f32 v141, v141, v137\nAW 77,141\nAR 141,78\n" \
"v_mul_f32 v141, v141, v137\nAW 78,141\nAR 141,79\nv_mul_f32 v141, v141, v137\nAW 79,141\nAR 141,80\nv_mul_f32 v141, v141, v137\nAW 80,141\n" \
"AR 141,81\nv_mul_f32 v141, v141, v137\nAW 81,141\nAR 141,82\nv_mul_f32 v141, v141, v137\nAW 82,141\nAR 141,83\nv_mul_f32 v141, v141, v137\n" \
"AW 83,141\nAR 141,84\nv_mul_f32 v141, v141, v137\nAW 84,141\nAR 141,85\nv_mul_f32 v141, v141, v137\nAW 85,141\nAR 141,86\n" \
"v_mul_f32 v141, v141, v137\nAW 86,141\nAR 141,87\nv_mul_f32 v141, v141, v137\nAW 87,141\nAR 141,88\nv_mul_f32 v141, v141, v137\nAW 88,141\n" \
"AR 141,89\nv_mul_f32 v141, v141, v137\nAW 89,141\nAR 141,90\nv_mul_f32 v141, v141, v137\nAW 90,141\nAR 141,91\nv_mul_f32 v141, v141, v137\n" \
"AW 91,141\nAR 141,92\nv_mul_f32 v141, v141, v137\nAW 92,141\nAR 141,93\nv_mul_f32 v141, v141, v137\nAW 93,141\nAR 141,94\n" \
"v_mul_f32 v141, v141, v137\nAW 94,141\nAR 141,95\nv_mul_f32 v141, v141, v137\nAW 95,141\ns_nop 1\nMaav 64,128,192\nMaav 68,132,192\n" \
"Maav 72,136,192\nMaav 76,140,192\nMaav 80,144,192\nMaav 84,148,192\nMaav 88,152,192\nMaav 92,156,192\ns_add_u32 s77, s77, 8192\ns_sub_u32 s96, s96, 1\n" \
"s_cmp_lg_u32 s96, 0\ns_nop 7\ns_cbranch_scc1 zf2l\nv_mov_b32 v141, v138\ns_nop 1\nv_permlane16_swap_b32 v138, v141\ns_nop 0\nv_add_f32 v138, v138, v141\n" \
"v_mov_b32 v141, v138\ns_nop 1\nv_permlane32_swap_b32 v138, v141\ns_nop 0\nv_add_f32 v138, v138, v141\nv_mov_b32 v106, v138\nzf2e:\nv_cmp_lt_u32 vcc, 0x3c000000, v243\n" \
"s_cbranch_vccz zf3e\ns_add_u32 s96, s43, 4\ns_mov_b32 s77, s38\nv_mov_b32 v136, 0xff800000\nv_mov_b32 v138, 0\nv_add_u32 v140, 0x4000, v8\nv_accvgpr_write_b32 a96, 0\nv_accvgpr_write_b32 a97, 0\n" \
"v_accvgpr_write_b32 a98, 0\nv_accvgpr_write_b32 a99, 0\nv_accvgpr_write_b32 a100, 0\nv_accvgpr_write_b32 a101, 0\nv_accvgpr_write_b32 a102, 0\nv_accvgpr_write_b32 a103, 0\nv_accvgpr_write_b32 a104, 0\nv_accvgpr_write_b32 a105, 0\n" \
"v_accvgpr_write_b32 a106, 0\nv_accvgpr_write_b32 a107, 0\nv_accvgpr_write_b32 a108, 0\nv_accvgpr_write_b32 a109, 0\nv_accvgpr_write_b32 a110, 0\nv_accvgpr_write_b32 a111, 0\nv_accvgpr_write_b32 a112, 0\nv_accvgpr_write_b32 a113, 0\n" \
"v_accvgpr_write_b32 a114, 0\nv_accvgpr_write_b32 a115, 0\nv_accvgpr_write_b32 a116, 0\nv_accvgpr_write_b32 a117, 0\nv_accvgpr_write_b32 a118, 0\nv_accvgpr_write_b32 a119, 0\nv_accvgpr_write_b32 a120, 0\nv_accvgpr_write_b32 a121, 0\n" \
"v_accvgpr_write_b32 a122, 0\nv_accvgpr_write_b32 a123, 0\nv_accvgpr_write_b32 a124, 0\nv_accvgpr_write_b32 a125, 0\nv_accvgpr_write_b32 a126, 0\nv_accvgpr_write_b32 a127, 0\nzf3l:\ns_add_u32 s76, s77, 4096\n" \
"buffer_load_dwordx4 a[160:163], v0, s[60:63], s77 offen offset:0 sc1\nbuffer_load_dwordx4 a[164:167], v0, s[60:63], s77 offen offset:1024 sc1\nbuffer_load_dwordx4 a[168:171], v0, s[60:63], s77 offen offset:2048 sc1\nbuffer_load_dwordx4 a[172:175], v0, s[60:63], s77 offen offset:3072 sc1\nbuffer_load_dwordx4 a[176:179], v0, s[60:63], s76 offen offset:0 sc1\nbuffer_load_dwordx4 a[180:183], v0, s[60:63], s76 offen offset:1024 sc1\nbuffer_load_dwordx4 a[184:187], v0, s[60:63], s76 offen offset:2048 sc1\nbuffer_load_dwordx4 a[188:191], v0, s[60:63], s76 offen offset:3072 sc1\n" \
"buffer_load_dwordx4 a[128:131], v140, s[60:63], s77 offen offset:0 sc1\nbuffer_load_dwordx4 a[132:135], v140, s[60:63], s77 offen offset:1024 sc1\nbuffer_load_dwordx4 a[136:139], v140, s[60:63], s77 offen offset:2048 sc1\nbuffer_load_dwordx4 a[140:143], v140, s[60:63], s77 offen offset:3072 sc1\nbuffer_load_dwordx4 a[144:147], v140, s[60:63], s76 offen offset:0 sc1\nbuffer_load_dwordx4 a[148:151], v140, s[60:63], s76 offen offset:1024 sc1\nbuffer_load_dwordx4 a[152:155], v140, s[60:63], s76 offen offset:2048 sc1\nbuffer_load_dwordx4 a[156:159], v140, s[60:63], s76 offen offset:3072 sc1\n" \
"v_mov_b32 v144, v12\nv_mov_b32 v145, v12\nv_mov_b32 v146, v12\nv_mov_b32 v147, v12\nv_mov_b32 v148, v12\nv_mov_b32 v149, v12\nv_mov_b32 v150, v12\nv_mov_b32 v151, v12\n" \
"s_cmp_gt_u32 s96, 2\ns_cbranch_scc1 zf3m\nv_mov_b32 v144, v112\nv_mov_b32 v145, v113\nv_mov_b32 v146, v114\nv_mov_b32 v147, v115\nv_mov_b32 v148, v116\nv_mov_b32 v149, v117\n" \
"v_mov_b32 v150, v118\nv_mov_b32 v151, v119\ns_cmp_eq_u32 s96, 2\ns_cbranch_scc1 zf3m\nv_mov_b32 v144, v120\nv_mov_b32 v145, v121\nv_mov_b32 v146, v122\nv_mov_b32 v147, v123\n" \
"v_mov_b32 v148, v124\nv_mov_b32 v149, v125\nv_mov_b32 v150, v126\nv_mov_b32 v151, v127\nzf3m:\ns_waitcnt vmcnt(0)\nQvav 128,160,80,144\nMvav 128,164,84\n" \
"Mvav 128,168,88\nMvav 128,172,92\nQvav 132,176,80,148\nMvav 132,180,84\nMvav 132,184,88\nMvav 132,188,92\ns_nop 7\nv_max3_f32 v141, v128, v129, v130\n" \
"v_max3_f32 v142, v131, v132, v133\nv_max3_f32 v141, v141, v134, v135\nv_max3_f32 v141, v141, v142, v136\nv_mov_b32 v142, v141\ns_nop 1\nv_permlane16_swap_b32 v141, v142\ns_nop 0\nv_max_f32 v141, v141, v142\n" \
"v_mov_b32 v142, v141\ns_nop 1\nv_permlane32_swap_b32 v141, v142\ns_nop 0\nv_max_f32 v141, v141, v142\nv_sub_f32 v137, v136, v141\nE 137,137\nv_mov_b32 v136, v141\n" \
"v_sub_f32 v128, v128, v141\nv_sub_f32 v129, v129, v141\nv_sub_f32 v130, v130, v141\nv_sub_f32 v131, v131, v141\nv_sub_f32 v132, v132, v141\nv_sub_f32 v133, v133, v141\nv_sub_f32 v134, v134, v141\nv_sub_f32 v135, v135, v141\n" \
"E 128,128\nE 129,129\nE 130,130\nE 131,131\nE 132,132\nE 133,133\nE 134,134\nE 135,135\n" \
"v_mul_f32 v138, v138, v137\nC 192,128,129\nC 193,130,131\nC 194,132,133\nC 195,134,135\nv_add_f32 v138, v138, v128\nv_add_f32 v138, v138, v129\nv_add_f32 v138, v138, v130\n" \
"v_add_f32 v138, v138, v131\nv_add_f32 v138, v138, v132\nv_add_f32 v138, v138, v133\nv_add_f32 v138, v138, v134\nv_add_f32 v138, v138, v135\nAR 141,96\nv_mul_f32 v141, v141, v137\nAW 96,141\n" \
"AR 141,97\nv_mul_f32 v141, v141, v137\nAW 97,141\nAR 141,98\nv_mul_f32 v141, v141, v137\nAW 98,141\nAR 141,99\nv_mul_f32 v141, v141, v137\n" \
"AW 99,141\nAR 141,100\nv_mul_f32 v141, v141, v137\nAW 100,141\nAR 141,101\nv_mul_f32 v141, v141, v137\nAW 101,141\nAR 141,102\n" \
"v_mul_f32 v141, v141, v137\nAW 102,141\nAR 141,103\nv_mul_f32 v141, v141, v137\nAW 103,141\nAR 141,104\nv_mul_f32 v141, v141, v137\nAW 104,141\n" \
"AR 141,105\nv_mul_f32 v141, v141, v137\nAW 105,141\nAR 141,106\ns_branch 99f\n.p2align 12\nzp40:\ns_setpc_b64 s[98:99]\n" \
"99:\nv_mul_f32 v141, v141, v137\nAW 106,141\nAR 141,107\nv_mul_f32 v141, v141, v137\nAW 107,141\nAR 141,108\nv_mul_f32 v141, v141, v137\n" \
"AW 108,141\nAR 141,109\nv_mul_f32 v141, v141, v137\nAW 109,141\nAR 141,110\nv_mul_f32 v141, v141, v137\nAW 110,141\nAR 141,111\n" \
"v_mul_f32 v141, v141, v137\nAW 111,141\nAR 141,112\nv_mul_f32 v141, v141, v137\nAW 112,141\nAR 141,113\nv_mul_f32 v141, v141, v137\nAW 113,141\n" \
"AR 141,114\nv_mul_f32 v141, v141, v137\nAW 114,141\nAR 141,115\nv_mul_f32 v141, v141, v137\nAW 115,141\nAR 141,116\nv_mul_f32 v141, v141, v137\n" \
"AW 116,141\nAR 141,117\nv_mul_f32 v141, v141, v137\nAW 117,141\nAR 141,118\nv_mul_f32 v141, v141, v137\nAW 118,141\nAR 141,119\n" \
"v_mul_f32 v141, v141, v137\nAW 119,141\nAR 141,120\nv_mul_f32 v141, v141, v137\nAW 120,141\nAR 141,121\nv_mul_f32 v141, v141, v137\nAW 121,141\n" \
"AR 141,122\nv_mul_f32 v141, v141, v137\nAW 122,141\nAR 141,123\nv_mul_f32 v141, v141, v137\nAW 123,141\nAR 141,124\nv_mul_f32 v141, v141, v137\n" \
"AW 124,141\nAR 141,125\nv_mul_f32 v141, v141, v137\nAW 125,141\nAR 141,126\nv_mul_f32 v141, v141, v137\nAW 126,141\nAR 141,127\n" \
"v_mul_f32 v141, v141, v137\nAW 127,141\ns_nop 1\nMaav 96,128,192\nMaav 100,132,192\nMaav 104,136,192\nMaav 108,140,192\nMaav 112,144,192\n" \
"Maav 116,148,192\nMaav 120,152,192\nMaav 124,156,192\ns_add_u32 s77, s77, 8192\ns_sub_u32 s96, s96, 1\ns_cmp_lg_u32 s96, 0\ns_nop 7\ns_cbranch_scc1 zf3l\n" \
"v_mov_b32 v141, v138\ns_nop 1\nv_permlane16_swap_b32 v138, v141\ns_nop 0\nv_add_f32 v138, v138, v141\nv_mov_b32 v141, v138\ns_nop 1\nv_permlane32_swap_b32 v138, v141\n" \
"s_nop 0\nv_add_f32 v138, v138, v141\nv_mov_b32 v107, v138\nzf3e:\nv_rcp_f32 v196, v104\nv_rcp_f32 v197, v105\ns_nop 0\ns_add_u32 s95, s83, 0\n" \
"AR 128,0\nAR 129,1\nAR 130,2\nAR 131,3\nAR 132,4\nAR 133,5\nAR 134,6\nAR 135,7\n" \
"v_pk_mul_f32 v[128:129], v[128:129], v[196:197] op_sel:[0,0] op_sel_hi:[1,0]\nv_pk_mul_f32 v[130:131], v[130:131], v[196:197] op_sel:[0,0] op_sel_hi:[1,0]\nv_pk_mul_f32 v[132:133], v[132:133], v[196:197] op_sel:[0,0] op_sel_hi:[1,0]\nv_pk_mul_f32 v[134:135], v[134:135], v[196:197] op_sel:[0,0] op_sel_hi:[1,0]\nC 32,128,129\nC 33,130,131\nC 34,132,133\nC 35,134,135\n" \
"buffer_store_dwordx4 v[32:35], v0, s[60:63], s95 offen offset:0 sc1\nAR 136,8\nAR 137,9\nAR 138,10\nAR 139,11\nAR 140,12\nAR 141,13\nAR 142,14\n" \
"AR 143,15\nv_pk_mul_f32 v[136:137], v[136:137], v[196:197] op_sel:[0,0] op_sel_hi:[1,0]\nv_pk_mul_f32 v[138:139], v[138:139], v[196:197] op_sel:[0,0] op_sel_hi:[1,0]\nv_pk_mul_f32 v[140:141], v[140:141], v[196:197] op_sel:[0,0] op_sel_hi:[1,0]\nv_pk_mul_f32 v[142:143], v[142:143], v[196:197] op_sel:[0,0] op_sel_hi:[1,0]\nC 36,136,137\nC 37,138,139\nC 38,140,141\n" \
"C 39,142,143\nbuffer_store_dwordx4 v[36:39], v0, s[60:63], s95 offen offset:1024 sc1\ns_add_u32 s95, s83, 32768\nAR 128,16\nAR 129,17\nAR 130,18\nAR 131,19\nAR 132,20\n" \
"AR 133,21\nAR 134,22\nAR 135,23\nv_pk_mul_f32 v[128:129], v[128:129], v[196:197] op_sel:[0,0] op_sel_hi:[1,0]\nv_pk_mul_f32 v[130:131], v[130:131], v[196:197] op_sel:[0,0] op_sel_hi:[1,0]\nv_pk_mul_f32 v[132:133], v[132:133], v[196:197] op_sel:[0,0] op_sel_hi:[1,0]\nv_pk_mul_f32 v[134:135], v[134:135], v[196:197] op_sel:[0,0] op_sel_hi:[1,0]\nC 40,128,129\n" \
"C 41,130,131\nC 42,132,133\nC 43,134,135\nbuffer_store_dwordx4 v[40:43], v0, s[60:63], s95 offen offset:0 sc1\nAR 136,24\nAR 137,25\nAR 138,26\nAR 139,27\n" \
"AR 140,28\nAR 141,29\nAR 142,30\nAR 143,31\nv_pk_mul_f32 v[136:137], v[136:137], v[196:197] op_sel:[0,0] op_sel_hi:[1,0]\nv_pk_mul_f32 v[138:139], v[138:139], v[196:197] op_sel:[0,0] op_sel_hi:[1,0]\nv_pk_mul_f32 v[140:141], v[140:141], v[196:197] op_sel:[0,0] op_sel_hi:[1,0]\nv_pk_mul_f32 v[142:143], v[142:143], v[196:197] op_sel:[0,0] op_sel_hi:[1,0]\n" \
"C 44,136,137\nC 45,138,139\nC 46,140,141\nC 47,142,143\nbuffer_store_dwordx4 v[44:47], v0, s[60:63], s95 offen offset:1024 sc1\ns_add_u32 s95, s83, 8192\nAR 128,32\nAR 129,33\n" \
"AR 130,34\nAR 131,35\nAR 132,36\nAR 133,37\nAR 134,38\nAR 135,39\nv_pk_mul_f32 v[128:129], v[128:129], v[196:197] op_sel:[0,1] op_sel_hi:[1,1]\nv_pk_mul_f32 v[130:131], v[130:131], v[196:197] op_sel:[0,1] op_sel_hi:[1,1]\n" \
"v_pk_mul_f32 v[132:133], v[132:133], v[196:197] op_sel:[0,1] op_sel_hi:[1,1]\nv_pk_mul_f32 v[134:135], v[134:135], v[196:197] op_sel:[0,1] op_sel_hi:[1,1]\nC 48,128,129\nC 49,130,131\nC 50,132,133\nC 51,134,135\nbuffer_store_dwordx4 v[48:51], v0, s[60:63], s95 offen offset:0 sc1\nAR 136,40\n" \
"AR 137,41\nAR 138,42\nAR 139,43\nAR 140,44\nAR 141,45\nAR 142,46\nAR 143,47\nv_pk_mul_f32 v[136:137], v[136:137], v[196:197] op_sel:[0,1] op_sel_hi:[1,1]\n" \
"v_pk_mul_f32 v[138:139], v[138:139], v[196:197] op_sel:[0,1] op_sel_hi:[1,1]\nv_pk_mul_f32 v[140:141], v[140:141], v[196:197] op_sel:[0,1] op_sel_hi:[1,1]\nv_pk_mul_f32 v[142:143], v[142:143], v[196:197] op_sel:[0,1] op_sel_hi:[1,1]\nC 52,136,137\nC 53,138,139\nC 54,140,141\nC 55,142,143\nbuffer_store_dwordx4 v[52:55], v0, s[60:63], s95 offen offset:1024 sc1\n" \
"s_add_u32 s95, s83, 40960\nAR 128,48\nAR 129,49\nAR 130,50\nAR 131,51\nAR 132,52\nAR 133,53\nAR 134,54\n" \
"AR 135,55\nv_pk_mul_f32 v[128:129], v[128:129], v[196:197] op_sel:[0,1] op_sel_hi:[1,1]\nv_pk_mul_f32 v[130:131], v[130:131], v[196:197] op_sel:[0,1] op_sel_hi:[1,1]\nv_pk_mul_f32 v[132:133], v[132:133], v[196:197] op_sel:[0,1] op_sel_hi:[1,1]\nv_pk_mul_f32 v[134:135], v[134:135], v[196:197] op_sel:[0,1] op_sel_hi:[1,1]\nC 56,128,129\nC 57,130,131\nC 58,132,133\n" \
"C 59,134,135\nbuffer_store_dwordx4 v[56:59], v0, s[60:63], s95 offen offset:0 sc1\nAR 136,56\nAR 137,57\nAR 138,58\nAR 139,59\nAR 140,60\nAR 141,61\n" \
"AR 142,62\nAR 143,63\nv_pk_mul_f32 v[136:137], v[136:137], v[196:197] op_sel:[0,1] op_sel_hi:[1,1]\nv_pk_mul_f32 v[138:139], v[138:139], v[196:197] op_sel:[0,1] op_sel_hi:[1,1]\nv_pk_mul_f32 v[140:141], v[140:141], v[196:197] op_sel:[0,1] op_sel_hi:[1,1]\nv_pk_mul_f32 v[142:143], v[142:143], v[196:197] op_sel:[0,1] op_sel_hi:[1,1]\nC 60,136,137\nC 61,138,139\n" \
"C 62,140,141\nC 63,142,143\nbuffer_store_dwordx4 v[60:63], v0, s[60:63], s95 offen offset:1024 sc1\ns_branch zend\nzlast:\n.subsection 0\n.purgem D\n.purgem Ta\n" \
".purgem Ra\n.purgem Zvaa\n.purgem AR\n.purgem Zaaa\n.purgem Mvaa\n.purgem Maaa\n.purgem C\n.purgem AW\n" \
".purgem DS\n.purgem Qvav\n.purgem Mvav\n.purgem E\n.purgem Zaav\n.purgem Maav\n.purgem Mvvv\n.purgem Zvav\n" \
".purgem Rv\n.purgem Mvva\n.purgem Qvaa\n.purgem Zvvv\n.purgem Qvva\n" \
""
#define Z_STAMP 0
#define Z_PAD 0
#define Z_NTP 0
#define Z_NY 6
#define Z_NP 2
#define Z_PA 1
#define Z_CLOBA "v1", "v2", "v3", "v4", "v9", "v10", "v11", "v12", "v13", "v14", "v15", "v16", "v17", "v18", "v19", "v20", "v21", "v22", "v23", "v24", "v25", "v26", "v27", "v28", "v30", "v31", "v32", "v33", "v34", "v35", "v36", "v37", "v38", "v39", "v40", "v41", "v42", "v43", "v44", "v45", "v46", "v47", "v48", "v49", "v50", "v51", "v52", "v53", "v54", "v55", "v56", "v57", "v58", "v59", "v60", "v61", "v62", "v63", "v64", "v65", "v66", "v67", "v68", "v69", "v70", "v71", "v72", "v73", "v74", "v75", "v76", "v77", "v78", "v79", "v80", "v81", "v82", "v83", "v84", "v85", "v86", "v87", "v88", "v89", "v90", "v91", "v92", "v93", "v94", "v95", "v96", "v97", "v98", "v99", "v100", "v101", "v102", "v103", "v104", "v105", "v106", "v107", "v108", "v109", "v110", "v111", "v112", "v113", "v114", "v115", "v116", "v117", "v118", "v119", "v120", "v121", "v122", "v123", "v124", "v125", "v126", "v127", "v128", "v129", "v130", "v131", "v132", "v133", "v134", "v135", "v136", "v137", "v138", "v139", "v140", "v141", "v142", "v143", "v144", "v145", "v146", "v147", "v148", "v149", "v150", "v151", "v152", "v153", "v154", "v155", "v156", "v157", "v158", "v159", "v160", "v161", "v162", "v163", "v164", "v165", "v166", "v167", "v168", "v169", "v170", "v171", "v172", "v173", "v174", "v175", "v176", "v177", "v178", "v179", "v180", "v181", "v182", "v183", "v184", "v185", "v186", "v187", "v188", "v189", "v190", "v191", "v192", "v193", "v194", "v195", "v196", "v197", "v198", "v199", "v200", "v201", "v202", "v203", "v204", "v205", "v206", "v207", "v208", "v209", "v210", "v211", "v212", "v213", "v214", "v215", "v216", "v217", "v218", "v219", "v220", "v221", "v222", "v223", "v224", "v225", "v226", "v227", "v228", "v229", "v230", "v231", "v232", "v233", "v234", "v235", "v236", "v237", "v238", "v239", "v240", "v241", "v242", "v243", "v244", "v245", "v246", "v247", "v248", "v249", "v250", "v251", "v252", "v253", "v254", "v255", "a0", "a1", "a2", "a3", "a4", "a5", "a6", "a7", "a8", "a9", "a10", "a11", "a12", "a13", "a14", "a15", "a16", "a17", "a18", "a19", "a20", "a21", "a22", "a23", "a24", "a25", "a26", "a27", "a28", "a29", "a30", "a31", "a32", "a33", "a34", "a35", "a36", "a37", "a38", "a39", "a40", "a41", "a42", "a43", "a44", "a45", "a46", "a47", "a48", "a49", "a50", "a51", "a52", "a53", "a54", "a55", "a56", "a57", "a58", "a59", "a60", "a61", "a62", "a63", "a64", "a65", "a66", "a67", "a68", "a69", "a70", "a71", "a72", "a73", "a74", "a75", "a76", "a77", "a78", "a79", "a80", "a81", "a82", "a83", "a84", "a85", "a86", "a87", "a88", "a89", "a90", "a91", "a92", "a93", "a94", "a95", "a96", "a97", "a98", "a99", "a100", "a101", "a102", "a103", "a104", "a105", "a106", "a107", "a108", "a109", "a110", "a111", "a112", "a113", "a114", "a115", "a116", "a117", "a118", "a119", "a120", "a121", "a122", "a123", "a124", "a125", "a126", "a127", "a128", "a129", "a130", "a131", "a132", "a133", "a134", "a135", "a136", "a137", "a138", "a139", "a140", "a141", "a142", "a143", "a144", "a145", "a146", "a147", "a148", "a149", "a150", "a151", "a152", "a153", "a154", "a155", "a156", "a157", "a158", "a159", "a160", "a161", "a162", "a163", "a164", "a165", "a166", "a167", "a168", "a169", "a170", "a171", "a172", "a173", "a174", "a175", "a176", "a177", "a178", "a179", "a180", "a181", "a182", "a183", "a184", "a185", "a186", "a187", "a188", "a189", "a190", "a191", "a192", "a193", "a194", "a195", "a196", "a197", "a198", "a199", "a200", "a201", "a202", "a203", "a204", "a205", "a206", "a207", "a208", "a209", "a210", "a211", "a212", "a213", "a214", "a215", "a216", "a217", "a218", "a219", "a220", "a221", "a222", "a223", "a224", "a225", "a226", "a227", "a228", "a229", "a230", "a231", "a232", "a233", "a234", "a235", "a236", "a237", "a238", "a239", "a240", "a241", "a242", "a243", "a244", "a245", "a246", "a247", "a248", "a249", "a250", "a251", "a252", "a253", "a254", "a255", "s2", "s4", "s5", "s6", "s7", "s8", "s9", "s10", "s12", "s13", "s14", "s15", "s16", "s17", "s18", "s19", "s20", "s21", "s22", "s23", "s24", "s25", "s26", "s27", "s28", "s29", "s30", "s31", "s34", "s35", "s36", "s37", "s38", "s39", "s40", "s41", "s42", "s43", "s44", "s45", "s46", "s47", "s48", "s49", "s50", "s51", "s52", "s53", "s54", "s55", "s56", "s57", "s58", "s59", "s72", "s73", "s74", "s75", "s76", "s77", "s78", "s79", "s88", "s89", "s90", "s91", "s92", "s93", "s94", "s95", "s96", "s97", "s98", "s99", "s100", "s101", "m0", "scc", "vcc", "memory"
#define Z_CLOB "v16", "v17", "v18", "v19", "v20", "v21", "v22", "v23", "v24", "v25", "v26", "v27", "v28", "v29", "v30", "v31", "v32", "v33", "v34", "v35", "v36", "v37", "v38", "v39", "v40", "v41", "v42", "v43", "v44", "v45", "v46", "v47", "v48", "v49", "v50", "v51", "v52", "v53", "v54", "v55", "v56", "v57", "v58", "v59", "v60", "v61", "v62", "v63", "v64", "v65", "v66", "v67", "v68", "v69", "v70", "v71", "v72", "v73", "v74", "v75", "v76", "v77", "v78", "v79", "v80", "v81", "v82", "v83", "v84", "v85", "v86", "v87", "v88", "v89", "v90", "v91", "v92", "v93", "v94", "v95", "v96", "v97", "v98", "v99", "v100", "v101", "v102", "v103", "v104", "v105", "v106", "v107", "v108", "v109", "v110", "v111", "v112", "v113", "v114", "v115", "v116", "v117", "v118", "v119", "v120", "v121", "v122", "v123", "v124", "v125", "v126", "v127", "v128", "v129", "v130", "v131", "v132", "v133", "v134", "v135", "v136", "v137", "v138", "v139", "v140", "v141", "v142", "v143", "v144", "v145", "v146", "v147", "v148", "v149", "v150", "v151", "v152", "v153", "v154", "v155", "v156", "v157", "v158", "v159", "v160", "v161", "v162", "v163", "v164", "v165", "v166", "v167", "v168", "v169", "v170", "v171", "v172", "v173", "v174", "v175", "v176", "v177", "v178", "v179", "v180", "v181", "v182", "v183", "v184", "v185", "v186", "v187", "v188", "v189", "v190", "v191", "v192", "v193", "v194", "v195", "v196", "v197", "v198", "v199", "v200", "v201", "v202", "v203", "v204", "v205", "v206", "v207", "v208", "v209", "v210", "v211", "v212", "v213", "v214", "v215", "v216", "v217", "v218", "v219", "v220", "v221", "v222", "v223", "v224", "v225", "v226", "v227", "v228", "v229", "v230", "v231", "v232", "v233", "v234", "v235", "v236", "v237", "v238", "v239", "v240", "v241", "v242", "v243", "v244", "v245", "v246", "v247", "v248", "v249", "v250", "v251", "v252", "v253", "v254", "v255", "a0", "a1", "a2", "a3", "a4", "a5", "a6", "a7", "a8", "a9", "a10", "a11", "a12", "a13", "a14", "a15", "a16", "a17", "a18", "a19", "a20", "a21", "a22", "a23", "a24", "a25", "a26", "a27", "a28", "a29", "a30", "a31", "a32", "a33", "a34", "a35", "a36", "a37", "a38", "a39", "a40", "a41", "a42", "a43", "a44", "a45", "a46", "a47", "a48", "a49", "a50", "a51", "a52", "a53", "a54", "a55", "a56", "a57", "a58", "a59", "a60", "a61", "a62", "a63", "a64", "a65", "a66", "a67", "a68", "a69", "a70", "a71", "a72", "a73", "a74", "a75", "a76", "a77", "a78", "a79", "a80", "a81", "a82", "a83", "a84", "a85", "a86", "a87", "a88", "a89", "a90", "a91", "a92", "a93", "a94", "a95", "a96", "a97", "a98", "a99", "a100", "a101", "a102", "a103", "a104", "a105", "a106", "a107", "a108", "a109", "a110", "a111", "a112", "a113", "a114", "a115", "a116", "a117", "a118", "a119", "a120", "a121", "a122", "a123", "a124", "a125", "a126", "a127", "a128", "a129", "a130", "a131", "a132", "a133", "a134", "a135", "a136", "a137", "a138", "a139", "a140", "a141", "a142", "a143", "a144", "a145", "a146", "a147", "a148", "a149", "a150", "a151", "a152", "a153", "a154", "a155", "a156", "a157", "a158", "a159", "a160", "a161", "a162", "a163", "a164", "a165", "a166", "a167", "a168", "a169", "a170", "a171", "a172", "a173", "a174", "a175", "a176", "a177", "a178", "a179", "a180", "a181", "a182", "a183", "a184", "a185", "a186", "a187", "a188", "a189", "a190", "a191", "a192", "a193", "a194", "a195", "a196", "a197", "a198", "a199", "a200", "a201", "a202", "a203", "a204", "a205", "a206", "a207", "a208", "a209", "a210", "a211", "a212", "a213", "a214", "a215", "a216", "a217", "a218", "a219", "a220", "a221", "a222", "a223", "a224", "a225", "a226", "a227", "a228", "a229", "a230", "a231", "a232", "a233", "a234", "a235", "a236", "a237", "a238", "a239", "a240", "a241", "a242", "a243", "a244", "a245", "a246", "a247", "a248", "a249", "a250", "a251", "a252", "a253", "a254", "a255", "s2", "s4", "s5", "s6", "s7", "s8", "s9", "s10", "s11", "s12", "s13", "s14", "s15", "s16", "s17", "s18", "s19", "s20", "s21", "s22", "s23", "s24", "s25", "s26", "s27", "s28", "s29", "s30", "s31", "s72", "s73", "s74", "s75", "s76", "s77", "s78", "s79", "s88", "s89", "s90", "s91", "s92", "s93", "s94", "s95", "s96", "s97", "s98", "s99", "s100", "s101", "m0", "scc", "vcc", "memory"
#define Z_WS_CHECK static_assert(WS_OA == 50660352 && WS_CTX == 54854656 && WS_PART == 71631872 && FLG_LA == 8192 && FLG_C == 16384 && FLG_S == 32768 && WS_V - WS_K == 16810496, "layout")
#define Z_CLOB1 "v28", "a251", "a252", "a253", "s72", "s73", "s74", "s75", "s76", "s77", "s78", "s79", "s88", "s89", "s90", "s91", "s92", "s93", "s94", "s95", "m0", "scc", "memory"
typedef unsigned long long u64;
#define HPAD 1024
constexpr unsigned HS = 2048 * 256 + HPAD;
typedef float f32x32 __attribute__((ext_vector_type(32)));
typedef unsigned long long u64x4 __attribute__((ext_vector_type(4)));
typedef unsigned u32x16 __attribute__((ext_vector_type(16)));
typedef unsigned long long u64x2 __attribute__((ext_vector_type(2)));
#define XPAGE(name) asm volatile("s_branch 99f\n\t.p2align 12\n" name ":\n\ts_setpc_b64 s[68:69]\n99:")
#define TOUCH_CLOB "s64", "s65", "s66", "s67", "s68", "s69", "scc"
__device__ __forceinline__ unsigned lane_now() { unsigned l; asm volatile("v_mbcnt_lo_u32_b32 %0, -1, 0\n\tv_mbcnt_hi_u32_b32 %0, -1, %0" : "=v"(l)); return l; }
struct G1Desc { u64 p; unsigned n, w; };
struct G1Args { G1Desc ckv, wuk, wuv, ws; u64 Ax, Awqkv, Bx, wq, Awo, Bwo, out; };
constexpr unsigned PART_BYTES = 65536 + 8192 + 128;
constexpr unsigned WS_QKVA = 262144, WS_Q = WS_QKVA + 2048 * 6144, WS_K = WS_Q + 64 * 65536, WS_V = WS_K + 32 * HS + HPAD / 2, WS_OA = WS_V + 32 * HS + HPAD / 2,
                   WS_CTX = WS_OA + 2048 * 2048, WS_PART = WS_CTX + 2048 * 8192, WS_APART = WS_PART + 64 * 4 * 65536, WS_END = WS_APART + 64 * PART_BYTES, NOWHERE = 0x7fff0000u;
constexpr unsigned FLG_KV = 256, FLG_H = 4096, FLG_LA = 8192, FLG_C = 16384, FLG_S = 32768, WS_HALO = WS_QKVA;
Z_WS_CHECK;
typedef unsigned u32x4g __attribute__((ext_vector_type(4)));
#define STAMP(i)
#define RSTAMP(i)
#define STAMP_DECL
#define OARG
#define OPASS
#define STAMP_OUT()
namespace att {
typedef float    f32x4  __attribute__((ext_vector_type(4)));
typedef float    f32x16 __attribute__((ext_vector_type(16)));
typedef unsigned u32x4  __attribute__((ext_vector_type(4)));
#define VRING 0x8000u
#define QSTAGE 0x10000u
#define SRD_WORD3 0x00020000
#define DI __device__ __forceinline__
#define GAP() __builtin_amdgcn_sched_barrier(0)
DI unsigned rfl(unsigned x) { return __builtin_amdgcn_readfirstlane(x); }
DI unsigned lane_id() { return lane_now(); }
__global__ __launch_bounds__(256, 1)
void attn_kernel(G1Args kernel_args) {
  __shared__ __attribute__((aligned(1024))) char lds[163840];
  STAMP_DECL; STAMP(0); RSTAMP(14);
  u32x4 dA, dW, dG;
  u64x4 b4;
  unsigned b = blockIdx.x, tid = threadIdx.x;
  asm volatile("s_load_dwordx4 s[64:67], %6, 0x0\n\ts_lshr_b32 s56, %4, 2\n\ts_load_dwordx4 s[60:63], %6, 0x30\n\ts_and_b32 s56, s56, 16\n\ts_add_u32 s56, s56, 16\n\t"
               "s_load_dwordx4 s[68:71], %6, s56\n\ts_load_dwordx8 s[80:87], %6, 0x40"
               : "=&{s[64:67]}"(dA), "=&{s[68:71]}"(dW), "=&{s[60:63]}"(dG), "=&{s[80:87]}"(b4), "+{s3}"(b), "+v"(tid) : "s"(__builtin_amdgcn_kernarg_segment_ptr()) : "s56", "scc");
  asm volatile("s_getpc_b64 s[56:57]\n1:\n\ts_mov_b32 s58, zlast+256-1b\n\ts_mov_b32 s59, 0x20000\n\tbuffer_load_dword a254, %0, s[56:59], 0 offen"
               :: "v"(((b >> 3) * 64u + (tid >> 6) * 16u + (tid & 15u)) * 128u + ((tid & 48u) << 24)) : "s56", "s57", "s58", "s59", "a254");
  {
  const unsigned wid = rfl(tid >> 6), xcd = b & 7u, jj = b >> 3, wr = wid >> 1, wc = wid & 1u;
  unsigned wk = 4096u * wid;
  const unsigned rt1 = 2u * (xcd & 1u) + (jj & 1u) + ((b >> 5) & 4u), ct1 = 4u * (xcd >> 1) + ((jj >> 1) & 3u);
  const unsigned ln = tid & 63u;
  const unsigned l4 = ln >> 4, l15 = ln & 15u, swA = ((ln & 7u) ^ (ln >> 3)) << 4, cw = l15 ^ (l4 << 1);
  unsigned vA = (rt1 * 256u + 64u * wid + (ln >> 3)) * 1024u + swA, vW0 = (16u * wid + l4) * 8192u + ct1 * 512u + (cw << 4);
  unsigned vC = ((b * 4u + wid) * 16u + l15) * 128u + ((ln & 48u) << 24);
  const unsigned q4 = l15 >> 2, p4 = ln & 3u, f4 = q4 | ((l4 & 1u) << 2);
  unsigned rdA = (wr * 128u + l15) * 128u + ((l4 ^ (ln & 7u)) << 4), rdW = 65536u + wc * 16384u + (8u * l4 + q4) * 256u + (((p4 >> 1) ^ (f4 << 1)) << 4) + (p4 & 1u) * 8u;
  unsigned vL = 16u * ln, kvt = ct1 * 8u + rt1, role = (jj >> 3) & 1u;
  asm volatile("" :: "s"((__attribute__((address_space(3))) char*)lds));
#define Z_STV
  asm volatile(Z_MAIN : "+{v0}"(vL), "+{v5}"(vA), "+{v6}"(vW0), "+{v7}"(rdA), "+{v8}"(rdW), "+{v29}"(vC), "+{s11}"(kvt), "+{s33}"(role), "+{s[60:63]}"(dG), "+{s[64:67]}"(dA), "+{s[68:71]}"(dW), "+{s[80:87]}"(b4), "+{s3}"(b), "+{s32}"(wk) Z_STV
               : "{s[0:1]}"(__builtin_amdgcn_kernarg_segment_ptr()) : Z_CLOBA);
  }
  STAMP(13); RSTAMP(15); STAMP_OUT();
}
}

#define WSOFF 0
extern "C" void kernel_launch(void* const* d_in, const int* in_sizes, int n_in, void* d_out, int out_size,
                              void* d_ws, size_t ws_size, hipStream_t stream) {
  const bf16_t* A_x = (const bf16_t*)d_in[0];
  const bf16_t* A_wqkv = (const bf16_t*)d_in[1];
  const bf16_t* A_wo = (const bf16_t*)d_in[2];
  const bf16_t* B_x = (const bf16_t*)d_in[3];
  const bf16_t* B_ckv = (const bf16_t*)d_in[4];
  const bf16_t* B_wq = (const bf16_t*)d_in[5];
  const bf16_t* B_wuk = (const bf16_t*)d_in[6];
  const bf16_t* B_wuv = (const bf16_t*)d_in[7];
  const bf16_t* B_wo = (const bf16_t*)d_in[8];
    G1Args g1 = {{(u64)B_ckv, 2048u * 1024u, 0x20000u}, {(u64)B_wuk, 512u * 8192u, 0x20000u}, {(u64)B_wuv, 512u * 8192u, 0x20000u}, {(u64)d_ws + WSOFF, WS_END, 0x20000u}, (u64)A_x, (u64)A_wqkv, (u64)B_x, (u64)B_wq, (u64)A_wo, (u64)B_wo, (u64)d_out};
  att::attn_kernel<<<256, 256, 0, stream>>>(g1);
}
```
